```python
import torch

def _softmax(x, axis=-1):
    return torch.softmax(x, dim=axis)

def _gelu(x):
    return torch.nn.functional.gelu(x, approximate='tanh')

def _silu(x):
    return torch.nn.functional.silu(x)

def _sigmoid(x):
    return torch.sigmoid(x)

def _layernorm(x, axis=-1, eps=1e-05):
    m = x.mean(dim=axis, keepdim=True)
    v = x.var(dim=axis, keepdim=True, unbiased=False)
    return (x - m) / torch.sqrt(v + eps)

def _scatter_add(tgt, idx, src, axis=0):
    return tgt.clone().index_add_(axis, idx, src)

def _segment_sum(src, seg, n):
    out = torch.zeros((n,) + src.shape[1:], dtype=src.dtype)
    return out.index_add_(0, seg, src)

def _pad(x, pw):
    flat = tuple((v for pair in reversed(pw) for v in pair))
    return torch.nn.functional.pad(x, flat)
kernel_name = 'concat_heads__memory_aug_recurrent_mem_tr__ssm_conv_s4d__prompt'

def setup_inputs():

    _w = lambda a: (lambda t: t.to(torch.bfloat16) if t.is_floating_point() else t)(torch.as_tensor(a))

    def _A_setup():
        B, S, Dm = (1, 2048, 576)
        H, Dh = (32, 128)
        M, seg = (8, 32)
        seg = min(seg, S)
        S = S // seg * seg
        return {'x': _w((lambda *s: torch.randn(*s, dtype=torch.bfloat16) * 0.05)(B, S, Dm)), 'mem0': _w((lambda *s: torch.randn(*s, dtype=torch.bfloat16) * 0.4)(B, M, Dm)), 'Wq': _w((lambda *s: torch.randn(*s, dtype=torch.bfloat16) * 6.4)(Dm, H, Dh) * Dm ** (-0.5)), 'Wk': _w((lambda *s: torch.randn(*s, dtype=torch.bfloat16) * 6.4)(Dm, H, Dh) * Dm ** (-0.5)), 'Wv': _w((lambda *s: torch.randn(*s, dtype=torch.bfloat16) * 0.4)(Dm, H, Dh) * Dm ** (-0.5)), 'Wo': _w((lambda *s: torch.randn(*s, dtype=torch.bfloat16) * 0.4)(H, Dh, Dm) * (H * Dh) ** (-0.5)), 'seg_len': seg}
    _A_in = _A_setup()

    def _B_setup():
        Bsz, L, D, N = (1, 2048, 576, 128)
        return {'u': _w((lambda *s: torch.randn(*s, dtype=torch.bfloat16) * 0.05)(Bsz, L, D)), 'log_A_real': _w((lambda *s: torch.randn(*s, dtype=torch.bfloat16) * 3.2)(D, N) * 0.5), 'A_imag': _w((lambda *s: torch.randn(*s, dtype=torch.bfloat16) * 0.05)(D, N) * 3.14), 'C_real': _w((lambda *s: torch.randn(*s, dtype=torch.bfloat16) * 0.4)(D, N) / N ** 0.5), 'C_imag': _w((lambda *s: torch.randn(*s, dtype=torch.bfloat16) * 0.4)(D, N) / N ** 0.5), 'log_dt': _w((lambda *s: torch.randn(*s, dtype=torch.bfloat16) * 0.05)(D) * 0.5 - 4.0), 'D_skip': _w((lambda *s: torch.randn(*s, dtype=torch.bfloat16) * 0.05)(D))}
    _B_in = _B_setup()
    return {'A_x': _A_in['x'], 'A_mem0': _A_in['mem0'], 'A_Wq': _A_in['Wq'], 'A_Wk': _A_in['Wk'], 'A_Wv': _A_in['Wv'], 'A_Wo': _A_in['Wo'], 'A_seg_len': _A_in['seg_len'], 'B_u': _B_in['u'], 'B_log_A_real': _B_in['log_A_real'], 'B_A_imag': _B_in['A_imag'], 'B_C_real': _B_in['C_real'], 'B_C_imag': _B_in['C_imag'], 'B_log_dt': _B_in['log_dt'], 'B_D_skip': _B_in['D_skip']}

def reference(A_x, A_mem0, A_Wq, A_Wk, A_Wv, A_Wo, A_seg_len, B_u, B_log_A_real, B_A_imag, B_C_real, B_C_imag, B_log_dt, B_D_skip):

    def _A_ref(x, mem0, Wq, Wk, Wv, Wo, seg_len):
        B, S, Dm = x.shape
        H, Dh = (Wq.shape[1], Wq.shape[2])
        M = mem0.shape[1]
        scale = Dh ** (-0.5)
        n_seg = S // seg_len
        L = M + seg_len
        idx = torch.arange(L)
        mask = (idx[:, None] < M) | (idx[:, None] >= idx[None, :])
        mem = mem0
        outs = []
        for s in range(n_seg):
            xs = x[:, s * seg_len:(s + 1) * seg_len, :].clone()
            h = torch.concatenate([mem, xs], axis=1)
            q = torch.einsum('btd,dhe->bhte', h, Wq)
            k = torch.einsum('btd,dhe->bhte', h, Wk)
            v = torch.einsum('btd,dhe->bhte', h, Wv)
            scores = torch.einsum('bhqe,bhke->bhqk', q, k) * scale
            scores = torch.where(mask[None, None], scores, -1000000000.0)
            p = _softmax(scores, axis=-1)
            o = torch.einsum('bhqk,bhke->bhqe', p, v)
            o = torch.einsum('bhte,hed->btd', o, Wo)
            mem = o[:, :M, :].clone()
            outs.append(o[:, M:, :])
        return torch.concatenate(outs, axis=1)

    def _B_ref(u, log_A_real, A_imag, C_real, C_imag, log_dt, D_skip):
        L = u.shape[1]
        dt = torch.exp(log_dt)
        A = -torch.exp(log_A_real) + 1j * A_imag
        C = (C_real + 1j * C_imag) * dt[:, None]
        pos = (lambda x, dt: x.to(dt))(torch.arange(L), log_dt.dtype)
        P = torch.exp((A * dt[:, None])[:, :, None] * pos[None, None, :])
        K = 2.0 * torch.einsum('dn,dnl->dl', C, P).real
        L2 = 2 * L
        u_t = (lambda x, *d: x.permute(*d))(u, 0, 2, 1)
        Kf = (lambda x, *a, **k: torch.fft.rfft(x.float(), *a, **k))(K, L2)
        Uf = (lambda x, *a, **k: torch.fft.rfft(x.float(), *a, **k))(u_t, L2)
        y = (lambda x, *a, **k: torch.fft.irfft(x, *a, **k).to(torch.bfloat16))(Uf * Kf[None, :, :], L2)[:, :, :L]
        y = (lambda x, *d: x.permute(*d))(y, 0, 2, 1)
        return y + u * D_skip[None, None, :]
    a = _A_ref(A_x, A_mem0, A_Wq, A_Wk, A_Wv, A_Wo, A_seg_len)
    b = _B_ref(B_u, B_log_A_real, B_A_imag, B_C_real, B_C_imag, B_log_dt, B_D_skip)
    return torch.concatenate([a, b], axis=-1)
```

```cpp
#include <hip/hip_runtime.h>
#include <hip/hip_bf16.h>
#include <stdint.h>
#include <stddef.h>

typedef __attribute__((ext_vector_type(8))) __bf16 bf16x8;
typedef __attribute__((ext_vector_type(4))) short s16x4;
typedef __attribute__((ext_vector_type(4))) float f32x4;
typedef __attribute__((ext_vector_type(4))) unsigned u32x4;
typedef __attribute__((ext_vector_type(2))) unsigned u32x2;
typedef __attribute__((ext_vector_type(2))) float f32x2;
typedef unsigned short u16;
typedef unsigned long long u64;

#define GAS __attribute__((address_space(1)))
typedef GAS unsigned gu32;
#define RLX_AGENT __ATOMIC_RELAXED, __HIP_MEMORY_SCOPE_AGENT

static constexpr int DM = 576, NH = 32, DH = 128, MM = 8, SEG = 32, NSEG = 64, SL = 2048;
static constexpr int HD = NH * DH;
static constexpr int OUTW = 2 * DM;
static constexpr int NST = 128;

__device__ __forceinline__ float bf2f(u16 h) { return __uint_as_float(((unsigned)h) << 16); }
typedef __attribute__((ext_vector_type(2))) __bf16 bf16x2_t;
__device__ __forceinline__ unsigned pk2bf(float a, float b) {
    bf16x2_t v; v[0] = (__bf16)a; v[1] = (__bf16)b;
    return __builtin_bit_cast(unsigned, v);
}
__device__ __forceinline__ float bfr(float f) { return __uint_as_float(pk2bf(f, f) << 16); }
__device__ __forceinline__ float lo2f(unsigned u) { return __uint_as_float(u << 16); }
__device__ __forceinline__ float hi2f(unsigned u) { return __uint_as_float(u & 0xffff0000u); }
__device__ __forceinline__ float rowmax4(float x) {
    auto a = __builtin_amdgcn_permlane16_swap(__float_as_uint(x), __float_as_uint(x), false, false);
    x = fmaxf(__uint_as_float(a[0]), __uint_as_float(a[1]));
    auto b = __builtin_amdgcn_permlane32_swap(__float_as_uint(x), __float_as_uint(x), false, false);
    return fmaxf(__uint_as_float(b[0]), __uint_as_float(b[1]));
}
__device__ __forceinline__ float rowsum4(float x) {
    auto a = __builtin_amdgcn_permlane16_swap(__float_as_uint(x), __float_as_uint(x), false, false);
    x = __uint_as_float(a[0]) + __uint_as_float(a[1]);
    auto b = __builtin_amdgcn_permlane32_swap(__float_as_uint(x), __float_as_uint(x), false, false);
    return __uint_as_float(b[0]) + __uint_as_float(b[1]);
}

struct MainP {
    const u16* W[4];
    char* ws; const u16* X; const u16* mem0; const u16* U;
    int prm[8];
    const u16* logA; const u16* Aim; const u16* Cre; const u16* Cim; const u16* logdt; const u16* Dskip;
    u16* out;
};
static constexpr int LDS_TOTAL = 163840;
static constexpr unsigned kWaitCap = 1u << 18;
#define VMC0() asm volatile("s_waitcnt vmcnt(0)" ::: "memory")
#define PRM_DEFAULT 1170, 600, 96, 672, 928, 1621365619, 15728816, 0
#define ASM_EA \
    "s_load_dwordx8 s[40:47], %0, 0\n\t" \
    "s_load_dwordx4 s[36:39], %0, 0x20\n\t" \
    "s_load_dwordx8 s[84:91], %0, 0x40\n\t" \
    "s_getpc_b64 s[54:55]\n\t" \
    "ea_pc_%=:\n\t" \
    "s_bfe_u32 s69, %1, 0x20003\n\t" \
    "s_lshl_b32 s69, s69, 15\n\t" \
    "v_lshl_add_u32 v1, %2, 7, s69\n\t" \
    "v_min_u32 v1, code_end_marker-ea_pc_%=, v1\n\t" \
    "s_mov_b32 m0, 155648\n\t" \
    "v_readfirstlane_b32 s66, %2\n\t" \
    "global_load_lds_dword v1, s[54:55]\n\t" \
    "s_bitcmp1_b32 %1, 2\n\t" \
    "s_cselect_b32 s69, 24, 16\n\t" \
    "s_lshr_b32 s70, %1, 3\n\t" \
    "s_cmp_ge_u32 s70, s69\n\t" \
    "s_cbranch_scc1 ea_done_%=\n\t" \
    "s_lshr_b32 s66, s66, 6\n\t" \
    "s_and_b32 s69, %1, 0xc4\n\t" \
    "s_cmp_eq_u32 s69, 4\n\t" \
    "s_cselect_b32 s69, 72, 36\n\t" \
    "s_add_u32 s82, s66, s69\n\t" \
    "s_lshl_b32 s82, s82, 10\n\t" \
    "s_mov_b32 m0, s82\n\t" \
    "s_and_b32 s67, %1, 3\n\t" \
    "s_lshl_b32 s67, s67, 3\n\t" \
    "s_and_b32 s69, s70, 7\n\t" \
    "s_add_u32 s67, s67, s69\n\t" \
    "s_lshl_b32 s69, s67, 8\n\t" \
    "v_lshrrev_b32 v1, 4, %2\n\t" \
    "v_lshrrev_b32 v2, 3, %2\n\t" \
    "v_and_b32 v2, 6, v2\n\t" \
    "v_and_or_b32 v2, v1, 8, v2\n\t" \
    "v_xor_b32 v2, v2, %2\n\t" \
    "v_and_b32 v2, 15, v2\n\t" \
    "v_lshl_add_u32 v2, v2, 4, s69\n\t" \
    "v_lshl_add_u32 v200, v1, 13, v2\n\t" \
    "s_mov_b32 s71, 16\n\t" \
    "s_bitcmp1_b32 %1, 2\n\t" \
    "s_waitcnt lgkmcnt(0)\n\t" \
    "s_cselect_b64 s[48:49], s[42:43], s[40:41]\n\t" \
    "s_cselect_b64 s[40:41], s[40:41], s[42:43]\n\t" \
    "s_cmp_ge_u32 s70, 8\n\t" \
    "s_cselect_b64 s[42:43], s[44:45], s[48:49]\n\t" \
    "s_cselect_b64 s[44:45], s[46:47], s[40:41]\n\t" \
    "ea_lp1_%=:\n\t" \
    "global_load_lds_dwordx4 v200, s[42:43]\n\t" \
    "v_add_u32 v200, 131072, v200\n\t" \
    "s_add_u32 m0, m0, 4096\n\t" \
    "s_sub_u32 s71, s71, 1\n\t" \
    "s_cmp_lg_u32 s71, 0\n\t" \
    "s_cbranch_scc1 ea_lp1_%=\n\t" \
    "s_mov_b64 s[50:51], s[38:39]\n\t" \
    "s_cmp_lt_u32 s70, 8\n\t" \
    "s_cbranch_scc1 ea_w_%=\n\t" \
    "s_mul_i32 s69, s67, 147456\n\t" \
    "s_add_u32 s44, s44, s69\n\t" \
    "s_addc_u32 s45, s45, 0\n\t" \
    "s_and_b32 s69, s70, 16\n\t" \
    "s_lshl_b32 s69, s69, 1\n\t" \
    "s_add_u32 s67, s67, s69\n\t" \
    "s_bitcmp1_b32 %1, 2\n\t" \
    "s_cbranch_scc1 gv_begin_%=\n\t" \
    "s_branch vv_begin_%=\n\t" \
    "ea_w_%=:\n\t" \
    "s_bitcmp1_b32 %1, 2\n\t" \
    "s_cbranch_scc1 gx_begin_%=\n\t" \
    "qk_begin_%=:\n\t" \
    "v_mbcnt_lo_u32_b32 v223, -1, 0\n\t" \
    "v_mbcnt_hi_u32_b32 v223, -1, v223\n\t" \
    "s_mov_b32 s68, 262144\n\t" \
    "s_mov_b32 s72, 0\n\t" \
    "s_mov_b32 s83, -1\n\t" \
    "s_mov_b32 s65, 0\n\t" \
    "v_and_b32 v180, 3, v223\n\t" \
    "v_lshlrev_b32 v202, 3, v180\n\t" \
    "v_lshrrev_b32 v181, 5, v223\n\t" \
    "v_lshl_add_u32 v202, v181, 12, v202\n\t" \
    "v_bfe_u32 v182, v223, 2, 2\n\t" \
    "s_mov_b32 s70, 288\n\t" \
    "v_mad_u32_u24 v202, v182, s70, v202\n\t" \
    "v_bfe_u32 v183, v223, 4, 1\n\t" \
    "s_mov_b32 s70, 2176\n\t" \
    "v_mad_u32_u24 v202, v183, s70, v202\n\t" \
    "s_lshl_b32 s69, s66, 6\n\t" \
    "v_xor_b32 v202, s69, v202\n\t" \
    "v_xor_b32 v203, 32, v202\n\t" \
    "v_lshlrev_b32 v0, 4, v223\n\t" \
    "s_mul_i32 s69, s66, 2048\n\t" \
    "s_bfe_u32 s71, s67, 0x20003\n\t" \
    "s_mul_i32 s71, s71, 65536\n\t" \
    "s_add_u32 s69, s69, s71\n\t" \
    "s_add_u32 s69, s69, 32768\n\t" \
    "v_lshl_add_u32 v1, v223, 4, s69\n\t" \
    "s_bfe_u32 s71, s67, 0x20003\n\t" \
    "s_mul_i32 s69, s71, 65536\n\t" \
    "s_add_u32 s69, s69, 53248\n\t" \
    "v_lshl_add_u32 v2, v223, 4, s69\n\t" \
    "s_mov_b32 s69, 17476\n\t" \
    "v_bfe_u32 v184, v223, 1, 1\n\t" \
    "v_mov_b32 v10, s69\n\t" \
    "s_mov_b32 s70, 43690\n\t" \
    "v_mad_u32_u24 v10, v184, s70, v10\n\t" \
    "v_mov_b32 v11, 0\n\t" \
    "s_waitcnt vmcnt(12) lgkmcnt(0)\n\t" \
    "s_barrier\n\t" \
    "s_add_u32 m0, s82, 65536\n\t" \
    "s_nop 0\n\t" \
    "global_load_lds_dwordx4 v200, s[42:43]\n\t" \
    "v_add_u32 v200, 131072, v200\n\t" \
    "s_add_u32 m0, s82, 69632\n\t" \
    "s_nop 0\n\t" \
    "global_load_lds_dwordx4 v200, s[42:43]\n\t" \
    "v_add_u32 v200, 131072, v200\n\t" \
    "s_add_u32 m0, s82, 73728\n\t" \
    "s_nop 0\n\t" \
    "global_load_lds_dwordx4 v200, s[42:43]\n\t" \
    "v_add_u32 v200, 131072, v200\n\t" \
    "s_add_u32 m0, s82, 77824\n\t" \
    "s_nop 0\n\t" \
    "global_load_lds_dwordx4 v200, s[42:43]\n\t" \
    "v_add_u32 v200, 131072, v200\n\t" \
    "ds_read_b64_tr_b16 a[0:1], v202 offset:36864\n\t" \
    "ds_read_b64_tr_b16 a[2:3], v202 offset:37888\n\t" \
    "ds_read_b64_tr_b16 a[4:5], v202 offset:45056\n\t" \
    "ds_read_b64_tr_b16 a[6:7], v202 offset:46080\n\t" \
    "ds_read_b64_tr_b16 a[72:73], v203 offset:36864\n\t" \
    "ds_read_b64_tr_b16 a[74:75], v203 offset:37888\n\t" \
    "ds_read_b64_tr_b16 a[76:77], v203 offset:45056\n\t" \
    "ds_read_b64_tr_b16 a[78:79], v203 offset:46080\n\t" \
    "s_mul_i32 s69, s66, 512\n\t" \
    "s_add_u32 s69, s69, 16384\n\t" \
    "v_and_b32 v185, 13, v223\n\t" \
    "v_lshl_add_u32 v12, v185, 5, s69\n\t" \
    "v_lshl_add_u32 v12, v184, 2, v12\n\t" \
    "v_lshrrev_b32 v186, 4, v223\n\t" \
    "v_lshl_add_u32 v12, v186, 3, v12\n\t" \
    "s_mov_b32 s69, 16384\n\t" \
    "v_and_b32 v187, 15, v223\n\t" \
    "v_lshl_add_u32 v13, v187, 3, s69\n\t" \
    "v_lshl_add_u32 v13, v186, 8, v13\n\t" \
    "s_bfe_u32 s71, s67, 0x20003\n\t" \
    "s_mul_i32 s69, s71, 65536\n\t" \
    "s_add_u32 s69, s69, 266240\n\t" \
    "v_and_b32 v188, 31, v223\n\t" \
    "v_lshl_add_u32 v20, v188, 5, s69\n\t" \
    "v_lshl_add_u32 v20, v181, 2, v20\n\t" \
    "s_mul_i32 s69, s67, 32\n\t" \
    "s_add_u32 s69, s69, 262144\n\t" \
    "v_lshl_add_u32 v21, v180, 16, s69\n\t" \
    "v_bfe_u32 v189, v223, 2, 3\n\t" \
    "v_lshlrev_b32 v199, 10, v189\n\t" \
    "v_min_u32 v199, 4096, v199\n\t" \
    "v_add_u32 v21, v199, v21\n\t" \
    "s_bfe_u32 s71, s67, 0x20003\n\t" \
    "s_mul_i32 s69, s71, 65536\n\t" \
    "s_add_u32 s69, s69, 267264\n\t" \
    "v_min_u32 v35, 35, v223\n\t" \
    "v_lshl_add_u32 v35, v35, 5, s69\n\t" \
    "v_lshlrev_b32 v22, 2, v223\n\t" \
    "s_waitcnt vmcnt(12) lgkmcnt(0)\n\t" \
    "s_barrier\n\t" \
    "s_add_u32 m0, s82, 81920\n\t" \
    "s_nop 0\n\t" \
    "global_load_lds_dwordx4 v200, s[42:43]\n\t" \
    "v_add_u32 v200, 131072, v200\n\t" \
    "s_add_u32 m0, s82, 86016\n\t" \
    "s_nop 0\n\t" \
    "global_load_lds_dwordx4 v200, s[42:43]\n\t" \
    "v_add_u32 v200, 131072, v200\n\t" \
    "s_add_u32 m0, s82, 90112\n\t" \
    "s_nop 0\n\t" \
    "global_load_lds_dwordx4 v200, s[42:43]\n\t" \
    "v_add_u32 v200, 131072, v200\n\t" \
    "s_add_u32 m0, s82, 94208\n\t" \
    "s_nop 0\n\t" \
    "global_load_lds_dwordx4 v200, s[42:43]\n\t" \
    "v_add_u32 v200, 131072, v200\n\t" \
    "ds_read_b64_tr_b16 a[8:9], v202 offset:53248\n\t" \
    "ds_read_b64_tr_b16 a[10:11], v202 offset:54272\n\t" \
    "ds_read_b64_tr_b16 a[12:13], v202 offset:61440\n\t" \
    "ds_read_b64_tr_b16 a[14:15], v202 offset:62464\n\t" \
    "ds_read_b64_tr_b16 a[80:81], v203 offset:53248\n\t" \
    "ds_read_b64_tr_b16 a[82:83], v203 offset:54272\n\t" \
    "ds_read_b64_tr_b16 a[84:85], v203 offset:61440\n\t" \
    "ds_read_b64_tr_b16 a[86:87], v203 offset:62464\n\t" \
    "s_mul_i32 s69, s67, 128\n\t" \
    "s_add_u32 s69, s69, 6127616\n\t" \
    "v_mov_b32 v14, s69\n\t" \
    "v_and_b32 v190, 57, v223\n\t" \
    "v_lshlrev_b32 v23, 4, v190\n\t" \
    "v_lshl_add_u32 v23, v184, 6, v23\n\t" \
    "v_bfe_u32 v191, v223, 2, 1\n\t" \
    "v_lshl_add_u32 v23, v191, 5, v23\n\t" \
    "v_add_u32 v15, 4096, v23\n\t" \
    "s_mul_i32 s69, s66, 512\n\t" \
    "s_add_u32 s69, s69, 8192\n\t" \
    "v_lshl_add_u32 v16, v187, 4, s69\n\t" \
    "v_lshl_add_u32 v16, v186, 2, v16\n\t" \
    "s_mov_b32 s69, 8192\n\t" \
    "v_and_b32 v192, 1, v223\n\t" \
    "v_lshl_add_u32 v17, v192, 3, s69\n\t" \
    "v_lshrrev_b32 v193, 2, v223\n\t" \
    "v_lshl_add_u32 v17, v193, 4, v17\n\t" \
    "s_mul_i32 s69, s67, 128\n\t" \
    "s_add_u32 s69, s69, 6160384\n\t" \
    "v_mov_b32 v24, s69\n\t" \
    "s_waitcnt vmcnt(12) lgkmcnt(0)\n\t" \
    "s_barrier\n\t" \
    "s_add_u32 m0, s82, 98304\n\t" \
    "s_nop 0\n\t" \
    "global_load_lds_dwordx4 v200, s[42:43]\n\t" \
    "v_add_u32 v200, 131072, v200\n\t" \
    "s_add_u32 m0, s82, 102400\n\t" \
    "s_nop 0\n\t" \
    "global_load_lds_dwordx4 v200, s[42:43]\n\t" \
    "v_add_u32 v200, 131072, v200\n\t" \
    "s_add_u32 m0, s82, 106496\n\t" \
    "s_nop 0\n\t" \
    "global_load_lds_dwordx4 v200, s[42:43]\n\t" \
    "v_add_u32 v200, 131072, v200\n\t" \
    "s_add_u32 m0, s82, 110592\n\t" \
    "s_nop 0\n\t" \
    "global_load_lds_dwordx4 v200, s[42:43]\n\t" \
    "v_add_u32 v200, 131072, v200\n\t" \
    "v_add_u32 v202, 69632, v202\n\t" \
    "v_add_u32 v203, 69632, v203\n\t" \
    "ds_read_b64_tr_b16 a[16:17], v202 offset:0\n\t" \
    "ds_read_b64_tr_b16 a[18:19], v202 offset:1024\n\t" \
    "ds_read_b64_tr_b16 a[20:21], v202 offset:8192\n\t" \
    "ds_read_b64_tr_b16 a[22:23], v202 offset:9216\n\t" \
    "ds_read_b64_tr_b16 a[88:89], v203 offset:0\n\t" \
    "ds_read_b64_tr_b16 a[90:91], v203 offset:1024\n\t" \
    "ds_read_b64_tr_b16 a[92:93], v203 offset:8192\n\t" \
    "ds_read_b64_tr_b16 a[94:95], v203 offset:9216\n\t" \
    "s_bfe_u32 s71, s66, 0x10000\n\t" \
    "s_mul_i32 s69, s71, 128\n\t" \
    "s_bfe_u32 s71, s67, 0x40000\n\t" \
    "s_mul_i32 s71, s71, 512\n\t" \
    "s_add_u32 s69, s69, s71\n\t" \
    "s_bfe_u32 s71, s67, 0x10004\n\t" \
    "s_mul_i32 s71, s71, 16384\n\t" \
    "s_add_u32 s69, s69, s71\n\t" \
    "v_lshl_add_u32 v18, v180, 2, s69\n\t" \
    "v_lshl_add_u32 v18, v191, 5, v18\n\t" \
    "v_bfe_u32 v194, v223, 3, 1\n\t" \
    "v_lshl_add_u32 v18, v194, 8, v18\n\t" \
    "v_lshl_add_u32 v18, v183, 4, v18\n\t" \
    "v_lshl_add_u32 v18, v181, 6, v18\n\t" \
    "s_bfe_u32 s71, s66, 0x10000\n\t" \
    "s_mul_i32 s69, s71, 128\n\t" \
    "s_bfe_u32 s71, s67, 0x10000\n\t" \
    "s_mul_i32 s71, s71, 8\n\t" \
    "s_add_u32 s69, s69, s71\n\t" \
    "s_bfe_u32 s71, s67, 0x40001\n\t" \
    "s_mul_i32 s71, s71, 256\n\t" \
    "s_add_u32 s69, s69, s71\n\t" \
    "s_add_u32 s69, s69, 49152\n\t" \
    "v_lshl_add_u32 v19, v184, 5, s69\n\t" \
    "v_and_b32 v195, 5, v193\n\t" \
    "v_lshl_add_u32 v19, v195, 2, v19\n\t" \
    "v_lshl_add_u32 v19, v181, 6, v19\n\t" \
    "v_add_u32 v3, 65536, v18\n\t" \
    "v_add_u32 v4, 65536, v19\n\t" \
    "s_waitcnt vmcnt(12) lgkmcnt(0)\n\t" \
    "s_barrier\n\t" \
    "s_add_u32 m0, s82, 0\n\t" \
    "s_nop 0\n\t" \
    "global_load_lds_dwordx4 v200, s[42:43]\n\t" \
    "v_add_u32 v200, 131072, v200\n\t" \
    "s_add_u32 m0, s82, 4096\n\t" \
    "s_nop 0\n\t" \
    "global_load_lds_dwordx4 v200, s[42:43]\n\t" \
    "v_add_u32 v200, 131072, v200\n\t" \
    "s_add_u32 m0, s82, 8192\n\t" \
    "s_nop 0\n\t" \
    "global_load_lds_dwordx4 v200, s[42:43]\n\t" \
    "v_add_u32 v200, 131072, v200\n\t" \
    "s_add_u32 m0, s82, 12288\n\t" \
    "s_nop 0\n\t" \
    "global_load_lds_dwordx4 v200, s[42:43]\n\t" \
    "v_add_u32 v200, 131072, v200\n\t" \
    "ds_read_b64_tr_b16 a[24:25], v202 offset:16384\n\t" \
    "ds_read_b64_tr_b16 a[26:27], v202 offset:17408\n\t" \
    "ds_read_b64_tr_b16 a[28:29], v202 offset:24576\n\t" \
    "ds_read_b64_tr_b16 a[30:31], v202 offset:25600\n\t" \
    "ds_read_b64_tr_b16 a[96:97], v203 offset:16384\n\t" \
    "ds_read_b64_tr_b16 a[98:99], v203 offset:17408\n\t" \
    "ds_read_b64_tr_b16 a[100:101], v203 offset:24576\n\t" \
    "ds_read_b64_tr_b16 a[102:103], v203 offset:25600\n\t" \
    "s_mov_b32 s77, 3019898880\n\t" \
    "s_mov_b32 s78, 3054198966\n\t" \
    "s_mov_b32 s71, 3\n\t" \
    "qk_E1_%=:\n\t" \
    "s_memtime s[54:55]\n\t" \
    "s_nop 0\n\t" \
    "s_memrealtime s[4:5]\n\t" \
    "s_nop 0\n\t" \
    "s_memrealtime s[6:7]\n\t" \
    "s_nop 0\n\t" \
    "s_memrealtime s[8:9]\n\t" \
    "s_nop 0\n\t" \
    "s_memrealtime s[10:11]\n\t" \
    "s_nop 0\n\t" \
    "s_memrealtime s[12:13]\n\t" \
    "s_nop 0\n\t" \
    "s_memrealtime s[14:15]\n\t" \
    "s_nop 0\n\t" \
    "s_memrealtime s[16:17]\n\t" \
    "s_nop 0\n\t" \
    "s_memrealtime s[18:19]\n\t" \
    "s_nop 0\n\t" \
    "s_memrealtime s[20:21]\n\t" \
    "s_nop 0\n\t" \
    "s_memrealtime s[22:23]\n\t" \
    "s_nop 0\n\t" \
    "s_memrealtime s[24:25]\n\t" \
    "s_nop 0\n\t" \
    "s_memrealtime s[26:27]\n\t" \
    "s_nop 0\n\t" \
    "s_memrealtime s[28:29]\n\t" \
    "s_nop 0\n\t" \
    "s_memtime s[56:57]\n\t" \
    "s_mov_b32 s70, 1\n\t" \
    "s_sub_u32 s71, s71, 1\n\t" \
    "s_cselect_b32 s69, 0, -1\n\t" \
    "s_waitcnt lgkmcnt(0)\n\t" \
    "s_sub_u32 s54, s56, s54\n\t" \
    "s_and_b32 s54, s54, s69\n\t" \
    "s_cmp_gt_u32 s54, 124\n\t" \
    "s_cbranch_scc1 qk_E1_%=\n\t" \
    "s_cmp_eq_u32 s4, s28\n\t" \
    "s_addc_u32 s70, s70, 0\n\t" \
    "s_cmp_eq_u32 s6, s28\n\t" \
    "s_addc_u32 s70, s70, 0\n\t" \
    "s_cmp_eq_u32 s8, s28\n\t" \
    "s_addc_u32 s70, s70, 0\n\t" \
    "s_cmp_eq_u32 s10, s28\n\t" \
    "s_addc_u32 s70, s70, 0\n\t" \
    "s_cmp_eq_u32 s12, s28\n\t" \
    "s_addc_u32 s70, s70, 0\n\t" \
    "s_cmp_eq_u32 s14, s28\n\t" \
    "s_addc_u32 s70, s70, 0\n\t" \
    "s_cmp_eq_u32 s16, s28\n\t" \
    "s_addc_u32 s70, s70, 0\n\t" \
    "s_cmp_eq_u32 s18, s28\n\t" \
    "s_addc_u32 s70, s70, 0\n\t" \
    "s_cmp_eq_u32 s20, s28\n\t" \
    "s_addc_u32 s70, s70, 0\n\t" \
    "s_cmp_eq_u32 s22, s28\n\t" \
    "s_addc_u32 s70, s70, 0\n\t" \
    "s_cmp_eq_u32 s24, s28\n\t" \
    "s_addc_u32 s70, s70, 0\n\t" \
    "s_cmp_eq_u32 s26, s28\n\t" \
    "s_addc_u32 s70, s70, 0\n\t" \
    "s_mov_b32 s75, s73\n\t" \
    "s_mov_b32 s76, s74\n\t" \
    "s_lshl_b32 s70, s70, 3\n\t" \
    "s_sub_u32 s73, s56, s70\n\t" \
    "s_lshl_b32 s74, s28, 4\n\t" \
    "s_waitcnt vmcnt(12) lgkmcnt(0)\n\t" \
    "s_barrier\n\t" \
    "s_add_u32 m0, s82, 16384\n\t" \
    "s_nop 0\n\t" \
    "global_load_lds_dwordx4 v200, s[42:43]\n\t" \
    "v_add_u32 v200, 131072, v200\n\t" \
    "s_add_u32 m0, s82, 20480\n\t" \
    "s_nop 0\n\t" \
    "global_load_lds_dwordx4 v200, s[42:43]\n\t" \
    "v_add_u32 v200, 131072, v200\n\t" \
    "s_add_u32 m0, s82, 24576\n\t" \
    "s_nop 0\n\t" \
    "global_load_lds_dwordx4 v200, s[42:43]\n\t" \
    "v_add_u32 v200, 131072, v200\n\t" \
    "s_add_u32 m0, s82, 28672\n\t" \
    "s_nop 0\n\t" \
    "global_load_lds_dwordx4 v200, s[42:43]\n\t" \
    "v_add_u32 v200, 131072, v200\n\t" \
    "ds_read_b64_tr_b16 a[32:33], v202 offset:32768\n\t" \
    "ds_read_b64_tr_b16 a[34:35], v202 offset:33792\n\t" \
    "ds_read_b64_tr_b16 a[36:37], v202 offset:40960\n\t" \
    "ds_read_b64_tr_b16 a[38:39], v202 offset:41984\n\t" \
    "ds_read_b64_tr_b16 a[104:105], v203 offset:32768\n\t" \
    "ds_read_b64_tr_b16 a[106:107], v203 offset:33792\n\t" \
    "ds_read_b64_tr_b16 a[108:109], v203 offset:40960\n\t" \
    "ds_read_b64_tr_b16 a[110:111], v203 offset:41984\n\t" \
    "s_waitcnt vmcnt(12) lgkmcnt(0)\n\t" \
    "s_barrier\n\t" \
    "v_subrev_u32 v200, 4718592, v200\n\t" \
    "s_add_u32 m0, s82, 32768\n\t" \
    "s_nop 0\n\t" \
    "global_load_lds_dwordx4 v200, s[44:45]\n\t" \
    "v_add_u32 v200, 131072, v200\n\t" \
    "s_add_u32 m0, s82, 36864\n\t" \
    "s_nop 0\n\t" \
    "global_load_lds_dwordx4 v200, s[44:45]\n\t" \
    "v_add_u32 v200, 131072, v200\n\t" \
    "s_add_u32 m0, s82, 40960\n\t" \
    "s_nop 0\n\t" \
    "global_load_lds_dwordx4 v200, s[44:45]\n\t" \
    "v_add_u32 v200, 131072, v200\n\t" \
    "s_add_u32 m0, s82, 45056\n\t" \
    "s_nop 0\n\t" \
    "global_load_lds_dwordx4 v200, s[44:45]\n\t" \
    "v_add_u32 v200, 131072, v200\n\t" \
    "ds_read_b64_tr_b16 a[40:41], v202 offset:49152\n\t" \
    "ds_read_b64_tr_b16 a[42:43], v202 offset:50176\n\t" \
    "ds_read_b64_tr_b16 a[44:45], v202 offset:57344\n\t" \
    "ds_read_b64_tr_b16 a[46:47], v202 offset:58368\n\t" \
    "ds_read_b64_tr_b16 a[112:113], v203 offset:49152\n\t" \
    "ds_read_b64_tr_b16 a[114:115], v203 offset:50176\n\t" \
    "ds_read_b64_tr_b16 a[116:117], v203 offset:57344\n\t" \
    "ds_read_b64_tr_b16 a[118:119], v203 offset:58368\n\t" \
    "s_waitcnt vmcnt(12) lgkmcnt(0)\n\t" \
    "s_barrier\n\t" \
    "s_add_u32 m0, s82, 49152\n\t" \
    "s_nop 0\n\t" \
    "global_load_lds_dwordx4 v200, s[44:45]\n\t" \
    "v_add_u32 v200, 131072, v200\n\t" \
    "s_add_u32 m0, s82, 53248\n\t" \
    "s_nop 0\n\t" \
    "global_load_lds_dwordx4 v200, s[44:45]\n\t" \
    "v_add_u32 v200, 131072, v200\n\t" \
    "s_add_u32 m0, s82, 57344\n\t" \
    "s_nop 0\n\t" \
    "global_load_lds_dwordx4 v200, s[44:45]\n\t" \
    "v_add_u32 v200, 131072, v200\n\t" \
    "s_add_u32 m0, s82, 61440\n\t" \
    "s_nop 0\n\t" \
    "global_load_lds_dwordx4 v200, s[44:45]\n\t" \
    "v_add_u32 v200, 131072, v200\n\t" \
    "v_add_u32 v202, 65536, v202\n\t" \
    "v_add_u32 v203, 65536, v203\n\t" \
    "ds_read_b64_tr_b16 a[48:49], v202 offset:0\n\t" \
    "ds_read_b64_tr_b16 a[50:51], v202 offset:1024\n\t" \
    "ds_read_b64_tr_b16 a[52:53], v202 offset:8192\n\t" \
    "ds_read_b64_tr_b16 a[54:55], v202 offset:9216\n\t" \
    "ds_read_b64_tr_b16 a[120:121], v203 offset:0\n\t" \
    "ds_read_b64_tr_b16 a[122:123], v203 offset:1024\n\t" \
    "ds_read_b64_tr_b16 a[124:125], v203 offset:8192\n\t" \
    "ds_read_b64_tr_b16 a[126:127], v203 offset:9216\n\t" \
    "s_waitcnt vmcnt(12) lgkmcnt(0)\n\t" \
    "s_barrier\n\t" \
    "s_add_u32 m0, s82, 65536\n\t" \
    "s_nop 0\n\t" \
    "global_load_lds_dwordx4 v200, s[44:45]\n\t" \
    "v_add_u32 v200, 131072, v200\n\t" \
    "s_add_u32 m0, s82, 69632\n\t" \
    "s_nop 0\n\t" \
    "global_load_lds_dwordx4 v200, s[44:45]\n\t" \
    "v_add_u32 v200, 131072, v200\n\t" \
    "s_add_u32 m0, s82, 73728\n\t" \
    "s_nop 0\n\t" \
    "global_load_lds_dwordx4 v200, s[44:45]\n\t" \
    "v_add_u32 v200, 131072, v200\n\t" \
    "s_add_u32 m0, s82, 77824\n\t" \
    "s_nop 0\n\t" \
    "global_load_lds_dwordx4 v200, s[44:45]\n\t" \
    "v_add_u32 v200, 131072, v200\n\t" \
    "v_add_u32 v202, -98304, v202\n\t" \
    "v_add_u32 v203, -98304, v203\n\t" \
    "ds_read_b64_tr_b16 a[56:57], v202 offset:0\n\t" \
    "ds_read_b64_tr_b16 a[58:59], v202 offset:1024\n\t" \
    "ds_read_b64_tr_b16 a[60:61], v202 offset:8192\n\t" \
    "ds_read_b64_tr_b16 a[62:63], v202 offset:9216\n\t" \
    "ds_read_b64_tr_b16 a[128:129], v203 offset:0\n\t" \
    "ds_read_b64_tr_b16 a[130:131], v203 offset:1024\n\t" \
    "ds_read_b64_tr_b16 a[132:133], v203 offset:8192\n\t" \
    "ds_read_b64_tr_b16 a[134:135], v203 offset:9216\n\t" \
    "s_waitcnt vmcnt(12) lgkmcnt(0)\n\t" \
    "s_barrier\n\t" \
    "s_add_u32 m0, s82, 81920\n\t" \
    "s_nop 0\n\t" \
    "global_load_lds_dwordx4 v200, s[44:45]\n\t" \
    "v_add_u32 v200, 131072, v200\n\t" \
    "s_add_u32 m0, s82, 86016\n\t" \
    "s_nop 0\n\t" \
    "global_load_lds_dwordx4 v200, s[44:45]\n\t" \
    "v_add_u32 v200, 131072, v200\n\t" \
    "s_add_u32 m0, s82, 90112\n\t" \
    "s_nop 0\n\t" \
    "global_load_lds_dwordx4 v200, s[44:45]\n\t" \
    "v_add_u32 v200, 131072, v200\n\t" \
    "s_add_u32 m0, s82, 94208\n\t" \
    "s_nop 0\n\t" \
    "global_load_lds_dwordx4 v200, s[44:45]\n\t" \
    "v_add_u32 v200, 131072, v200\n\t" \
    "ds_read_b64_tr_b16 a[64:65], v202 offset:16384\n\t" \
    "ds_read_b64_tr_b16 a[66:67], v202 offset:17408\n\t" \
    "ds_read_b64_tr_b16 a[68:69], v202 offset:24576\n\t" \
    "ds_read_b64_tr_b16 a[70:71], v202 offset:25600\n\t" \
    "ds_read_b64_tr_b16 a[136:137], v203 offset:16384\n\t" \
    "ds_read_b64_tr_b16 a[138:139], v203 offset:17408\n\t" \
    "ds_read_b64_tr_b16 a[140:141], v203 offset:24576\n\t" \
    "ds_read_b64_tr_b16 a[142:143], v203 offset:25600\n\t" \
    "s_waitcnt vmcnt(12) lgkmcnt(0)\n\t" \
    "s_barrier\n\t" \
    "s_add_u32 m0, s82, 98304\n\t" \
    "s_nop 0\n\t" \
    "global_load_lds_dwordx4 v200, s[44:45]\n\t" \
    "v_add_u32 v200, 131072, v200\n\t" \
    "s_add_u32 m0, s82, 102400\n\t" \
    "s_nop 0\n\t" \
    "global_load_lds_dwordx4 v200, s[44:45]\n\t" \
    "v_add_u32 v200, 131072, v200\n\t" \
    "s_add_u32 m0, s82, 106496\n\t" \
    "s_nop 0\n\t" \
    "global_load_lds_dwordx4 v200, s[44:45]\n\t" \
    "v_add_u32 v200, 131072, v200\n\t" \
    "s_add_u32 m0, s82, 110592\n\t" \
    "s_nop 0\n\t" \
    "global_load_lds_dwordx4 v200, s[44:45]\n\t" \
    "v_add_u32 v200, 131072, v200\n\t" \
    "ds_read_b64_tr_b16 a[144:145], v202 offset:32768\n\t" \
    "ds_read_b64_tr_b16 a[146:147], v202 offset:33792\n\t" \
    "ds_read_b64_tr_b16 a[148:149], v202 offset:40960\n\t" \
    "ds_read_b64_tr_b16 a[150:151], v202 offset:41984\n\t" \
    "ds_read_b64_tr_b16 a[216:217], v203 offset:32768\n\t" \
    "ds_read_b64_tr_b16 a[218:219], v203 offset:33792\n\t" \
    "ds_read_b64_tr_b16 a[220:221], v203 offset:40960\n\t" \
    "ds_read_b64_tr_b16 a[222:223], v203 offset:41984\n\t" \
    "s_waitcnt vmcnt(12) lgkmcnt(0)\n\t" \
    "s_barrier\n\t" \
    "s_add_u32 m0, s82, 0\n\t" \
    "s_nop 0\n\t" \
    "global_load_lds_dwordx4 v200, s[44:45]\n\t" \
    "v_add_u32 v200, 131072, v200\n\t" \
    "s_add_u32 m0, s82, 4096\n\t" \
    "s_nop 0\n\t" \
    "global_load_lds_dwordx4 v200, s[44:45]\n\t" \
    "v_add_u32 v200, 131072, v200\n\t" \
    "s_add_u32 m0, s82, 8192\n\t" \
    "s_nop 0\n\t" \
    "global_load_lds_dwordx4 v200, s[44:45]\n\t" \
    "v_add_u32 v200, 131072, v200\n\t" \
    "s_add_u32 m0, s82, 12288\n\t" \
    "s_nop 0\n\t" \
    "global_load_lds_dwordx4 v200, s[44:45]\n\t" \
    "v_add_u32 v200, 131072, v200\n\t" \
    "ds_read_b64_tr_b16 a[152:153], v202 offset:49152\n\t" \
    "ds_read_b64_tr_b16 a[154:155], v202 offset:50176\n\t" \
    "ds_read_b64_tr_b16 a[156:157], v202 offset:57344\n\t" \
    "ds_read_b64_tr_b16 a[158:159], v202 offset:58368\n\t" \
    "ds_read_b64_tr_b16 a[224:225], v203 offset:49152\n\t" \
    "ds_read_b64_tr_b16 a[226:227], v203 offset:50176\n\t" \
    "ds_read_b64_tr_b16 a[228:229], v203 offset:57344\n\t" \
    "ds_read_b64_tr_b16 a[230:231], v203 offset:58368\n\t" \
    "s_waitcnt vmcnt(12) lgkmcnt(0)\n\t" \
    "s_barrier\n\t" \
    "s_add_u32 m0, s82, 16384\n\t" \
    "s_nop 0\n\t" \
    "global_load_lds_dwordx4 v200, s[44:45]\n\t" \
    "v_add_u32 v200, 131072, v200\n\t" \
    "s_add_u32 m0, s82, 20480\n\t" \
    "s_nop 0\n\t" \
    "global_load_lds_dwordx4 v200, s[44:45]\n\t" \
    "v_add_u32 v200, 131072, v200\n\t" \
    "s_add_u32 m0, s82, 24576\n\t" \
    "s_nop 0\n\t" \
    "global_load_lds_dwordx4 v200, s[44:45]\n\t" \
    "v_add_u32 v200, 131072, v200\n\t" \
    "s_add_u32 m0, s82, 28672\n\t" \
    "s_nop 0\n\t" \
    "global_load_lds_dwordx4 v200, s[44:45]\n\t" \
    "v_add_u32 v200, 131072, v200\n\t" \
    "v_add_u32 v202, 65536, v202\n\t" \
    "v_add_u32 v203, 65536, v203\n\t" \
    "ds_read_b64_tr_b16 a[160:161], v202 offset:0\n\t" \
    "ds_read_b64_tr_b16 a[162:163], v202 offset:1024\n\t" \
    "ds_read_b64_tr_b16 a[164:165], v202 offset:8192\n\t" \
    "ds_read_b64_tr_b16 a[166:167], v202 offset:9216\n\t" \
    "ds_read_b64_tr_b16 a[232:233], v203 offset:0\n\t" \
    "ds_read_b64_tr_b16 a[234:235], v203 offset:1024\n\t" \
    "ds_read_b64_tr_b16 a[236:237], v203 offset:8192\n\t" \
    "ds_read_b64_tr_b16 a[238:239], v203 offset:9216\n\t" \
    "s_waitcnt vmcnt(12) lgkmcnt(0)\n\t" \
    "s_barrier\n\t" \
    "s_add_u32 m0, s82, 32768\n\t" \
    "s_nop 0\n\t" \
    "global_load_lds_dwordx4 v200, s[44:45]\n\t" \
    "v_add_u32 v200, 131072, v200\n\t" \
    "s_add_u32 m0, s82, 36864\n\t" \
    "s_nop 0\n\t" \
    "global_load_lds_dwordx4 v200, s[44:45]\n\t" \
    "v_add_u32 v200, 131072, v200\n\t" \
    "s_add_u32 m0, s82, 40960\n\t" \
    "s_nop 0\n\t" \
    "global_load_lds_dwordx4 v200, s[44:45]\n\t" \
    "v_add_u32 v200, 131072, v200\n\t" \
    "s_add_u32 m0, s82, 45056\n\t" \
    "s_nop 0\n\t" \
    "global_load_lds_dwordx4 v200, s[44:45]\n\t" \
    "v_add_u32 v200, 131072, v200\n\t" \
    "ds_read_b64_tr_b16 a[168:169], v202 offset:16384\n\t" \
    "ds_read_b64_tr_b16 a[170:171], v202 offset:17408\n\t" \
    "ds_read_b64_tr_b16 a[172:173], v202 offset:24576\n\t" \
    "ds_read_b64_tr_b16 a[174:175], v202 offset:25600\n\t" \
    "ds_read_b64_tr_b16 a[240:241], v203 offset:16384\n\t" \
    "ds_read_b64_tr_b16 a[242:243], v203 offset:17408\n\t" \
    "ds_read_b64_tr_b16 a[244:245], v203 offset:24576\n\t" \
    "ds_read_b64_tr_b16 a[246:247], v203 offset:25600\n\t" \
    "s_waitcnt vmcnt(12) lgkmcnt(0)\n\t" \
    "s_barrier\n\t" \
    "s_add_u32 m0, s82, 49152\n\t" \
    "s_nop 0\n\t" \
    "global_load_lds_dwordx4 v200, s[44:45]\n\t" \
    "v_add_u32 v200, 131072, v200\n\t" \
    "s_add_u32 m0, s82, 53248\n\t" \
    "s_nop 0\n\t" \
    "global_load_lds_dwordx4 v200, s[44:45]\n\t" \
    "v_add_u32 v200, 131072, v200\n\t" \
    "s_add_u32 m0, s82, 57344\n\t" \
    "s_nop 0\n\t" \
    "global_load_lds_dwordx4 v200, s[44:45]\n\t" \
    "v_add_u32 v200, 131072, v200\n\t" \
    "s_add_u32 m0, s82, 61440\n\t" \
    "s_nop 0\n\t" \
    "global_load_lds_dwordx4 v200, s[44:45]\n\t" \
    "v_add_u32 v200, 131072, v200\n\t" \
    "ds_read_b64_tr_b16 a[176:177], v202 offset:32768\n\t" \
    "ds_read_b64_tr_b16 a[178:179], v202 offset:33792\n\t" \
    "ds_read_b64_tr_b16 a[180:181], v202 offset:40960\n\t" \
    "ds_read_b64_tr_b16 a[182:183], v202 offset:41984\n\t" \
    "ds_read_b64_tr_b16 a[248:249], v203 offset:32768\n\t" \
    "ds_read_b64_tr_b16 a[250:251], v203 offset:33792\n\t" \
    "ds_read_b64_tr_b16 a[252:253], v203 offset:40960\n\t" \
    "ds_read_b64_tr_b16 a[254:255], v203 offset:41984\n\t" \
    "s_waitcnt vmcnt(12) lgkmcnt(0)\n\t" \
    "s_barrier\n\t" \
    "v_mbcnt_lo_u32_b32 v160, -1, 0\n\t" \
    "v_mbcnt_hi_u32_b32 v160, -1, v160\n\t" \
    "v_min_u32 v160, 59, v160\n\t" \
    "v_lshlrev_b32 v160, 6, v160\n\t" \
    "v_add_u32 v160, 6225920, v160\n\t" \
    "v_mov_b32 v161, 6234112\n\t" \
    "global_load_dword v161, v161, s[36:37] sc1\n\t" \
    "global_load_dwordx2 v[162:163], v160, s[36:37] sc1\n\t" \
    "v_add_u32 v202, -65536, v202\n\t" \
    "v_add_u32 v203, -65536, v203\n\t" \
    "ds_read_b64_tr_b16 a[184:185], v202 offset:0\n\t" \
    "ds_read_b64_tr_b16 a[186:187], v202 offset:1024\n\t" \
    "ds_read_b64_tr_b16 a[188:189], v202 offset:8192\n\t" \
    "ds_read_b64_tr_b16 a[190:191], v202 offset:9216\n\t" \
    "ds_read_b64_tr_b16 v[224:225], v203 offset:0\n\t" \
    "ds_read_b64_tr_b16 v[226:227], v203 offset:1024\n\t" \
    "ds_read_b64_tr_b16 v[228:229], v203 offset:8192\n\t" \
    "ds_read_b64_tr_b16 v[230:231], v203 offset:9216\n\t" \
    "s_waitcnt vmcnt(10) lgkmcnt(0)\n\t" \
    "s_barrier\n\t" \
    "ds_read_b64_tr_b16 a[192:193], v202 offset:16384\n\t" \
    "ds_read_b64_tr_b16 a[194:195], v202 offset:17408\n\t" \
    "ds_read_b64_tr_b16 a[196:197], v202 offset:24576\n\t" \
    "ds_read_b64_tr_b16 a[198:199], v202 offset:25600\n\t" \
    "ds_read_b64_tr_b16 v[232:233], v203 offset:16384\n\t" \
    "ds_read_b64_tr_b16 v[234:235], v203 offset:17408\n\t" \
    "ds_read_b64_tr_b16 v[236:237], v203 offset:24576\n\t" \
    "ds_read_b64_tr_b16 v[238:239], v203 offset:25600\n\t" \
    "s_waitcnt vmcnt(6) lgkmcnt(0)\n\t" \
    "s_barrier\n\t" \
    "ds_read_b64_tr_b16 a[200:201], v202 offset:32768\n\t" \
    "ds_read_b64_tr_b16 a[202:203], v202 offset:33792\n\t" \
    "ds_read_b64_tr_b16 a[204:205], v202 offset:40960\n\t" \
    "ds_read_b64_tr_b16 a[206:207], v202 offset:41984\n\t" \
    "ds_read_b64_tr_b16 v[240:241], v203 offset:32768\n\t" \
    "ds_read_b64_tr_b16 v[242:243], v203 offset:33792\n\t" \
    "ds_read_b64_tr_b16 v[244:245], v203 offset:40960\n\t" \
    "ds_read_b64_tr_b16 v[246:247], v203 offset:41984\n\t" \
    "s_waitcnt vmcnt(2) lgkmcnt(0)\n\t" \
    "s_barrier\n\t" \
    "ds_read_b64_tr_b16 a[208:209], v202 offset:49152\n\t" \
    "ds_read_b64_tr_b16 a[210:211], v202 offset:50176\n\t" \
    "ds_read_b64_tr_b16 a[212:213], v202 offset:57344\n\t" \
    "ds_read_b64_tr_b16 a[214:215], v202 offset:58368\n\t" \
    "ds_read_b64_tr_b16 v[248:249], v203 offset:49152\n\t" \
    "ds_read_b64_tr_b16 v[250:251], v203 offset:50176\n\t" \
    "ds_read_b64_tr_b16 v[252:253], v203 offset:57344\n\t" \
    "ds_read_b64_tr_b16 v[254:255], v203 offset:58368\n\t" \
    "s_waitcnt vmcnt(0) lgkmcnt(0)\n\t" \
    "s_barrier\n\t" \
    "s_mul_i32 s70, s67, 7\n\t" \
    "s_add_u32 s69, s65, 0\n\t" \
    "s_xor_b32 s69, s69, s70\n\t" \
    "s_and_b32 s69, s69, 63\n\t" \
    "s_lshl_b32 s69, s69, 13\n\t" \
    "s_mul_i32 s70, s67, 524288\n\t" \
    "s_add_u32 s69, s69, s70\n\t" \
    "s_add_u32 s69, s69, 23068672\n\t" \
    "s_add_u32 s50, s36, s69\n\t" \
    "s_addc_u32 s51, s37, 0\n\t" \
    "v_sub_u32 v162, v162, v161\n\t" \
    "v_cmp_ne_u32 vcc, 1, v162\n\t" \
    "s_cbranch_vccz qk_Cok4_%=\n\t" \
    "qk_C3_%=:\n\t" \
    "global_load_dwordx2 v[162:163], v160, s[36:37] sc1\n\t" \
    "s_waitcnt vmcnt(0)\n\t" \
    "v_sub_u32 v162, v162, v161\n\t" \
    "v_cmp_ne_u32 vcc, 1, v162\n\t" \
    "s_cbranch_vccz qk_Cok4_%=\n\t" \
    "s_cmp_eq_u32 s68, 0\n\t" \
    "s_cbranch_scc1 qk_F5_%=\n\t" \
    "s_sub_u32 s68, s68, 1\n\t" \
    "s_sleep 4\n\t" \
    "s_branch qk_C3_%=\n\t" \
    "qk_F5_%=:\n\t" \
    "v_mov_b32 v164, 6095104\n\t" \
    "v_mov_b32 v165, 4\n\t" \
    "global_store_dword v164, v165, s[36:37] sc1\n\t" \
    "s_waitcnt vmcnt(0)\n\t" \
    "s_branch qk_Cok4_%=\n\t" \
    "qk_Cok4_%=:\n\t" \
    "s_cmp_lg_u32 s66, 0\n\t" \
    "s_cbranch_scc1 qk_Ca6_%=\n\t" \
    "v_mov_b32 v160, 6176768\n\t" \
    "s_mov_b64 exec, 1\n\t" \
    "global_atomic_add v160, v162, s[36:37] sc1\n\t" \
    "s_mov_b64 exec, -1\n\t" \
    "qk_Ca6_%=:\n\t" \
    "v_min_u32_dpp v163, v163, v163 row_shl:6 row_mask:0xf bank_mask:0xf\n\t" \
    "s_nop 0\n\t" \
    "v_readlane_b32 s69, v163, 0\n\t" \
    "v_readlane_b32 s70, v163, 1\n\t" \
    "s_nop 1\n\t" \
    "s_cmp_lt_u32 s70, s69\n\t" \
    "s_cselect_b32 s62, 8192, 0\n\t" \
    "s_cselect_b32 s69, s70, s69\n\t" \
    "s_mov_b32 s71, s69\n\t" \
    "v_readlane_b32 s69, v163, 2\n\t" \
    "v_readlane_b32 s70, v163, 3\n\t" \
    "s_nop 1\n\t" \
    "s_cmp_lt_u32 s70, s69\n\t" \
    "s_cselect_b32 s63, 8192, 0\n\t" \
    "s_cselect_b32 s69, s70, s69\n\t" \
    "s_max_u32 s71, s69, s71\n\t" \
    "v_readlane_b32 s69, v163, 4\n\t" \
    "v_readlane_b32 s70, v163, 5\n\t" \
    "s_nop 1\n\t" \
    "s_cmp_lt_u32 s70, s69\n\t" \
    "s_cselect_b32 s64, 8192, 0\n\t" \
    "s_cselect_b32 s69, s70, s69\n\t" \
    "s_max_u32 s71, s69, s71\n\t" \
    "s_add_u32 s63, s63, 0x200000\n\t" \
    "s_xor_b32 s82, s64, 8192\n\t" \
    "s_cmp_gt_u32 s71, 625\n\t" \
    "s_cselect_b32 s69, 1, 0\n\t" \
    "s_mul_i32 s70, s69, 160\n\t" \
    "s_add_u32 s86, s86, s70\n\t" \
    "s_mul_i32 s70, s69, 536\n\t" \
    "s_add_u32 s87, s87, s70\n\t" \
    "s_mul_i32 s70, s69, 256\n\t" \
    "s_add_u32 s88, s88, s70\n\t" \
    "v_add_u32 v14, s82, v14\n\t" \
    "v_add_u32 v24, s82, v24\n\t" \
    "v_mov_b32 v26, 0x3db504f3\n\t" \
    "v_mov_b32 v28, 0x3fb8aa3b\n\t" \
    "v_mov_b32 v27, 0x3db504f3\n\t" \
    "v_mov_b32 v29, 0x3fb8aa3b\n\t" \
    "s_mov_b32 s48, 0xaaaaaaaa\n\t" \
    "s_mov_b32 s49, 0xaaaaaaaa\n\t" \
    "s_mov_b32 s58, 0x00ff00ff\n\t" \
    "s_mov_b32 s59, 0x00ff00ff\n\t" \
    "s_lshl_b32 s69, s66, 2\n\t" \
    "v_mov_b32 v30, s69\n\t" \
    "v_mov_b32 v31, 0\n\t" \
    "ds_write_b32 v30, v31 offset:20496\n\t" \
    "s_waitcnt lgkmcnt(0)\n\t" \
    "global_load_dword v25, v24, s[36:37] sc1\n\t" \
    "s_mov_b32 s71, 3\n\t" \
    "qk_E8_%=:\n\t" \
    "s_memtime s[54:55]\n\t" \
    "s_nop 0\n\t" \
    "s_memrealtime s[4:5]\n\t" \
    "s_nop 0\n\t" \
    "s_memrealtime s[6:7]\n\t" \
    "s_nop 0\n\t" \
    "s_memrealtime s[8:9]\n\t" \
    "s_nop 0\n\t" \
    "s_memrealtime s[10:11]\n\t" \
    "s_nop 0\n\t" \
    "s_memrealtime s[12:13]\n\t" \
    "s_nop 0\n\t" \
    "s_memrealtime s[14:15]\n\t" \
    "s_nop 0\n\t" \
    "s_memrealtime s[16:17]\n\t" \
    "s_nop 0\n\t" \
    "s_memrealtime s[18:19]\n\t" \
    "s_nop 0\n\t" \
    "s_memrealtime s[20:21]\n\t" \
    "s_nop 0\n\t" \
    "s_memrealtime s[22:23]\n\t" \
    "s_nop 0\n\t" \
    "s_memrealtime s[24:25]\n\t" \
    "s_nop 0\n\t" \
    "s_memrealtime s[26:27]\n\t" \
    "s_nop 0\n\t" \
    "s_memrealtime s[28:29]\n\t" \
    "s_nop 0\n\t" \
    "s_memtime s[56:57]\n\t" \
    "s_mov_b32 s70, 1\n\t" \
    "s_sub_u32 s71, s71, 1\n\t" \
    "s_cselect_b32 s69, 0, -1\n\t" \
    "s_waitcnt lgkmcnt(0)\n\t" \
    "s_sub_u32 s54, s56, s54\n\t" \
    "s_and_b32 s54, s54, s69\n\t" \
    "s_cmp_gt_u32 s54, 124\n\t" \
    "s_cbranch_scc1 qk_E8_%=\n\t" \
    "s_cmp_eq_u32 s4, s28\n\t" \
    "s_addc_u32 s70, s70, 0\n\t" \
    "s_cmp_eq_u32 s6, s28\n\t" \
    "s_addc_u32 s70, s70, 0\n\t" \
    "s_cmp_eq_u32 s8, s28\n\t" \
    "s_addc_u32 s70, s70, 0\n\t" \
    "s_cmp_eq_u32 s10, s28\n\t" \
    "s_addc_u32 s70, s70, 0\n\t" \
    "s_cmp_eq_u32 s12, s28\n\t" \
    "s_addc_u32 s70, s70, 0\n\t" \
    "s_cmp_eq_u32 s14, s28\n\t" \
    "s_addc_u32 s70, s70, 0\n\t" \
    "s_cmp_eq_u32 s16, s28\n\t" \
    "s_addc_u32 s70, s70, 0\n\t" \
    "s_cmp_eq_u32 s18, s28\n\t" \
    "s_addc_u32 s70, s70, 0\n\t" \
    "s_cmp_eq_u32 s20, s28\n\t" \
    "s_addc_u32 s70, s70, 0\n\t" \
    "s_cmp_eq_u32 s22, s28\n\t" \
    "s_addc_u32 s70, s70, 0\n\t" \
    "s_cmp_eq_u32 s24, s28\n\t" \
    "s_addc_u32 s70, s70, 0\n\t" \
    "s_cmp_eq_u32 s26, s28\n\t" \
    "s_addc_u32 s70, s70, 0\n\t" \
    "s_mov_b32 s75, s73\n\t" \
    "s_mov_b32 s76, s74\n\t" \
    "s_lshl_b32 s70, s70, 3\n\t" \
    "s_sub_u32 s73, s56, s70\n\t" \
    "s_lshl_b32 s74, s28, 4\n\t" \
    "s_sub_u32 s69, s73, s75\n\t" \
    "s_sub_u32 s70, s74, s76\n\t" \
    "s_sub_u32 s71, s70, 1600\n\t" \
    "s_cmp_lt_u32 s71, 400000\n\t" \
    "s_cbranch_scc0 qk_K10_%=\n\t" \
    "v_cvt_f32_u32 v160, s69\n\t" \
    "v_cvt_f32_u32 v161, s70\n\t" \
    "v_rcp_f32 v162, v160\n\t" \
    "v_rcp_f32 v163, v161\n\t" \
    "v_mul_f32 v162, v162, v161\n\t" \
    "v_mul_f32 v163, v163, v160\n\t" \
    "v_mul_f32 v162, 0x4f800000, v162\n\t" \
    "v_mul_f32 v163, 0x4f000000, v163\n\t" \
    "v_cvt_u32_f32 v162, v162\n\t" \
    "v_cvt_u32_f32 v163, v163\n\t" \
    "v_readfirstlane_b32 s69, v162\n\t" \
    "v_readfirstlane_b32 s70, v163\n\t" \
    "s_nop 1\n\t" \
    "s_mov_b32 s78, s69\n\t" \
    "s_mov_b32 s77, s70\n\t" \
    "qk_K10_%=:\n\t" \
    "s_waitcnt vmcnt(0)\n\t" \
    "v_readfirstlane_b32 s83, v25\n\t" \
    "s_cmp_gt_u32 s66, 1\n\t" \
    "s_cbranch_scc1 qk_nk7_%=\n\t" \
    "s_cmp_lt_i32 s83, 0\n\t" \
    "s_cbranch_scc1 qk_nk7_%=\n\t" \
    "global_load_dwordx4 v[100:103], v23, s[50:51] offset:0 sc1\n\t" \
    "global_load_dwordx4 v[104:107], v23, s[50:51] offset:2048 sc1\n\t" \
    "global_load_dwordx4 v[108:111], v15, s[50:51] offset:0 sc1\n\t" \
    "global_load_dwordx4 v[112:115], v15, s[50:51] offset:2048 sc1\n\t" \
    "global_load_dwordx4 v[116:119], v23, s[50:51] offset:1024 sc1\n\t" \
    "global_load_dwordx4 v[120:123], v23, s[50:51] offset:3072 sc1\n\t" \
    "global_load_dwordx4 v[124:127], v15, s[50:51] offset:1024 sc1\n\t" \
    "global_load_dwordx4 v[128:131], v15, s[50:51] offset:3072 sc1\n\t" \
    "qk_nk7_%=:\n\t" \
    "s_barrier\n\t" \
    "s_mov_b32 s79, s74\n\t" \
    "s_add_u32 s80, s74, 2400\n\t" \
    "s_cmp_eq_u32 s66, 1\n\t" \
    "s_cbranch_scc1 qk_loop12_%=\n\t" \
    "s_cmp_eq_u32 s66, 2\n\t" \
    "s_cbranch_scc1 qk_loop13_%=\n\t" \
    "s_cmp_eq_u32 s66, 3\n\t" \
    "s_cbranch_scc1 qk_loop14_%=\n\t" \
    "qk_loop11_%=:\n\t" \
    "v_mov_b32 v72, 0\n\t" \
    "v_mov_b32 v73, 0\n\t" \
    "v_mov_b32 v74, 0\n\t" \
    "v_mov_b32 v75, 0\n\t" \
    "v_mov_b32 v76, 0\n\t" \
    "v_mov_b32 v77, 0\n\t" \
    "v_mov_b32 v78, 0\n\t" \
    "v_mov_b32 v79, 0\n\t" \
    "v_mov_b32 v80, 0\n\t" \
    "v_mov_b32 v81, 0\n\t" \
    "v_mov_b32 v82, 0\n\t" \
    "v_mov_b32 v83, 0\n\t" \
    "v_mov_b32 v84, 0\n\t" \
    "v_mov_b32 v85, 0\n\t" \
    "v_mov_b32 v86, 0\n\t" \
    "v_mov_b32 v87, 0\n\t" \
    "v_mov_b32 v88, 0\n\t" \
    "v_mov_b32 v89, 0\n\t" \
    "v_mov_b32 v90, 0\n\t" \
    "v_mov_b32 v91, 0\n\t" \
    "v_mov_b32 v92, 0\n\t" \
    "v_mov_b32 v93, 0\n\t" \
    "v_mov_b32 v94, 0\n\t" \
    "v_mov_b32 v95, 0\n\t" \
    "v_mov_b32 v96, 0\n\t" \
    "v_mov_b32 v97, 0\n\t" \
    "v_mov_b32 v98, 0\n\t" \
    "v_mov_b32 v99, 0\n\t" \
    "v_add_u32 v31, 1, v31\n\t" \
    "s_add_u32 s69, s65, 0\n\t" \
    "s_bitcmp1_b32 s69, 0\n\t" \
    "s_cselect_b32 s70, s63, s62\n\t" \
    "s_bfe_u32 s69, s69, 0x20001\n\t" \
    "s_lshl_b32 s69, s69, 19\n\t" \
    "s_add_u32 s69, s69, s70\n\t" \
    "s_add_u32 s46, s36, s69\n\t" \
    "s_addc_u32 s47, s37, 0\n\t" \
    "s_add_u32 s69, s65, 3\n\t" \
    "s_cmp_ge_i32 s83, s69\n\t" \
    "s_cselect_b64 vcc, -1, 0\n\t" \
    "v_mov_b32 v9, 6201216\n\t" \
    "v_cndmask_b32 v9, v24, v9, vcc\n\t" \
    "s_mul_i32 s70, s67, 7\n\t" \
    "s_add_u32 s69, s65, 1\n\t" \
    "s_xor_b32 s69, s69, s70\n\t" \
    "s_and_b32 s69, s69, 63\n\t" \
    "s_lshl_b32 s69, s69, 13\n\t" \
    "s_mul_i32 s70, s67, 524288\n\t" \
    "s_add_u32 s69, s69, s70\n\t" \
    "s_add_u32 s69, s69, 23068672\n\t" \
    "s_add_u32 s38, s36, s69\n\t" \
    "s_addc_u32 s39, s37, 0\n\t" \
    "qk_go20_%=:\n\t" \
    "s_sub_u32 s70, s79, s74\n\t" \
    "s_max_i32 s70, s70, 0\n\t" \
    "s_min_i32 s70, s70, 16000\n\t" \
    "s_lshl_b32 s70, s70, 1\n\t" \
    "s_mul_hi_u32 s70, s70, s77\n\t" \
    "s_add_u32 s70, s70, s73\n\t" \
    "s_sub_u32 s70, s70, 100\n\t" \
    "s_getpc_b64 s[30:31]\n\t" \
    "qk_Wp22_%=:\n\t" \
    "s_add_u32 s30, s30, qk_We23_%=-qk_Wp22_%=\n\t" \
    "s_addc_u32 s31, s31, 0\n\t" \
    "qk_W21_%=:\n\t" \
    "s_memtime s[56:57]\n\t" \
    "s_waitcnt lgkmcnt(0)\n\t" \
    "s_sub_u32 s69, s70, s56\n\t" \
    "s_cmp_gt_i32 s69, 111\n\t" \
    "s_cbranch_scc1 qk_W21_%=\n\t" \
    "s_max_i32 s69, s69, 0\n\t" \
    "s_lshr_b32 s69, s69, 1\n\t" \
    "s_and_b32 s69, s69, 0x3c\n\t" \
    "s_sub_u32 s30, s30, s69\n\t" \
    "s_subb_u32 s31, s31, 0\n\t" \
    "s_setpc_b64 s[30:31]\n\t" \
    ".p2align 6\n\t" \
    "s_nop 0\n\t" \
    "s_nop 0\n\t" \
    "s_nop 0\n\t" \
    "s_nop 1\n\t" \
    "s_nop 1\n\t" \
    "s_nop 1\n\t" \
    "s_nop 1\n\t" \
    "s_nop 1\n\t" \
    "s_nop 1\n\t" \
    "s_nop 1\n\t" \
    "s_nop 1\n\t" \
    "s_nop 1\n\t" \
    "s_nop 1\n\t" \
    "s_nop 1\n\t" \
    "s_nop 1\n\t" \
    "s_nop 1\n\t" \
    "qk_We23_%=:\n\t" \
    "qk_G24_%=:\n\t" \
    "global_load_dwordx4 v[36:39], v1, s[46:47] sc1\n\t" \
    "s_nop 9\n\t" \
    "global_load_dwordx4 v[40:43], v1, s[46:47] offset:1024 sc1\n\t" \
    "s_nop 9\n\t" \
    "global_load_dwordx4 v[44:47], v2, s[46:47] sc1\n\t" \
    "s_waitcnt vmcnt(2)\n\t" \
    "v_smfmac_f32_16x16x64_bf16 v[72:75], v[36:39], a[0:7], v10\n\t" \
    "ds_write_b128 v0, v[36:39] offset:0\n\t" \
    "v_smfmac_f32_16x16x64_bf16 v[76:79], v[36:39], a[72:79], v10\n\t" \
    "v_max3_u32 v160, v36, v37, v38\n\t" \
    "v_smfmac_f32_16x16x64_bf16 v[80:83], v[36:39], a[144:151], v10\n\t" \
    "v_smfmac_f32_16x16x64_bf16 v[84:87], v[36:39], a[216:223], v10\n\t" \
    "s_waitcnt vmcnt(1)\n\t" \
    "ds_write_b128 v0, v[40:43] offset:1024\n\t" \
    "v_smfmac_f32_16x16x64_bf16 v[72:75], v[40:43], a[8:15], v10\n\t" \
    "v_max3_u32 v160, v160, v39, v40\n\t" \
    "v_max3_u32 v160, v160, v41, v42\n\t" \
    "v_smfmac_f32_16x16x64_bf16 v[76:79], v[40:43], a[80:87], v10\n\t" \
    "v_max_u32 v160, v160, v43\n\t" \
    "v_cmp_eq_u32 vcc, -1, v160\n\t" \
    "v_smfmac_f32_16x16x64_bf16 v[80:83], v[40:43], a[152:159], v10\n\t" \
    "v_smfmac_f32_16x16x64_bf16 v[84:87], v[40:43], a[224:231], v10\n\t" \
    "s_cbranch_vccnz qk_Gbad25_%=\n\t" \
    "qk_Gc27_%=:\n\t" \
    "s_waitcnt lgkmcnt(0)\n\t" \
    "s_barrier\n\t" \
    "ds_read_b128 v[48:51], v0 offset:2048\n\t" \
    "ds_read_b128 v[52:55], v0 offset:3072\n\t" \
    "s_waitcnt vmcnt(0)\n\t" \
    "v_smfmac_f32_16x16x64_bf16 v[72:75], v[44:47], a[64:71], v10\n\t" \
    "ds_read_b128 v[56:59], v0 offset:4096\n\t" \
    "v_max3_u32 v160, v44, v45, v46\n\t" \
    "v_smfmac_f32_16x16x64_bf16 v[76:79], v[44:47], a[136:143], v10\n\t" \
    "ds_read_b128 v[60:63], v0 offset:5120\n\t" \
    "v_max_u32 v160, v160, v47\n\t" \
    "v_smfmac_f32_16x16x64_bf16 v[80:83], v[44:47], a[208:215], v10\n\t" \
    "ds_read_b128 v[64:67], v0 offset:6144\n\t" \
    "v_cmp_eq_u32 vcc, -1, v160\n\t" \
    "v_smfmac_f32_16x16x64_bf16 v[84:87], v[44:47], v[248:255], v10\n\t" \
    "ds_read_b128 v[68:71], v0 offset:7168\n\t" \
    "s_cbranch_vccnz qk_Gbad226_%=\n\t" \
    "qk_Gc228_%=:\n\t" \
    "s_waitcnt lgkmcnt(5)\n\t" \
    "v_smfmac_f32_16x16x64_bf16 v[72:75], v[48:51], a[16:23], v10\n\t" \
    "s_waitcnt lgkmcnt(4)\n\t" \
    "v_smfmac_f32_16x16x64_bf16 v[72:75], v[52:55], a[24:31], v10\n\t" \
    "s_waitcnt lgkmcnt(3)\n\t" \
    "v_smfmac_f32_16x16x64_bf16 v[72:75], v[56:59], a[32:39], v10\n\t" \
    "global_load_dword v25, v9, s[36:37] sc1\n\t" \
    "s_waitcnt lgkmcnt(2)\n\t" \
    "v_smfmac_f32_16x16x64_bf16 v[72:75], v[60:63], a[40:47], v10\n\t" \
    "s_waitcnt lgkmcnt(1)\n\t" \
    "v_smfmac_f32_16x16x64_bf16 v[72:75], v[64:67], a[48:55], v10\n\t" \
    "s_waitcnt lgkmcnt(0)\n\t" \
    "v_smfmac_f32_16x16x64_bf16 v[72:75], v[68:71], a[56:63], v10\n\t" \
    "v_smfmac_f32_16x16x64_bf16 v[76:79], v[48:51], a[88:95], v10\n\t" \
    "s_nop 1\n\t" \
    "s_nop 0\n\t" \
    "v_smfmac_f32_16x16x64_bf16 v[76:79], v[52:55], a[96:103], v10\n\t" \
    "s_nop 1\n\t" \
    "s_nop 0\n\t" \
    "v_smfmac_f32_16x16x64_bf16 v[76:79], v[56:59], a[104:111], v10\n\t" \
    "v_add_f32 v72, v72, v74\n\t" \
    "v_smfmac_f32_16x16x64_bf16 v[76:79], v[60:63], a[112:119], v10\n\t" \
    "v_add_f32 v73, v73, v75\n\t" \
    "v_smfmac_f32_16x16x64_bf16 v[76:79], v[64:67], a[120:127], v10\n\t" \
    "v_cvt_pk_bf16_f32 v156, v72, v73\n\t" \
    "v_smfmac_f32_16x16x64_bf16 v[76:79], v[68:71], a[128:135], v10\n\t" \
    "ds_write_b32 v12, v156 offset:0\n\t" \
    "v_smfmac_f32_16x16x64_bf16 v[80:83], v[48:51], a[160:167], v10\n\t" \
    "s_nop 1\n\t" \
    "s_nop 0\n\t" \
    "v_smfmac_f32_16x16x64_bf16 v[80:83], v[52:55], a[168:175], v10\n\t" \
    "s_nop 1\n\t" \
    "s_nop 0\n\t" \
    "v_smfmac_f32_16x16x64_bf16 v[80:83], v[56:59], a[176:183], v10\n\t" \
    "v_add_f32 v76, v76, v78\n\t" \
    "v_smfmac_f32_16x16x64_bf16 v[80:83], v[60:63], a[184:191], v10\n\t" \
    "v_add_f32 v77, v77, v79\n\t" \
    "v_smfmac_f32_16x16x64_bf16 v[80:83], v[64:67], a[192:199], v10\n\t" \
    "v_cvt_pk_bf16_f32 v157, v76, v77\n\t" \
    "v_smfmac_f32_16x16x64_bf16 v[80:83], v[68:71], a[200:207], v10\n\t" \
    "ds_write_b32 v12, v157 offset:64\n\t" \
    "v_smfmac_f32_16x16x64_bf16 v[84:87], v[48:51], a[232:239], v10\n\t" \
    "s_nop 1\n\t" \
    "s_nop 0\n\t" \
    "v_smfmac_f32_16x16x64_bf16 v[84:87], v[52:55], a[240:247], v10\n\t" \
    "s_nop 1\n\t" \
    "s_nop 0\n\t" \
    "v_smfmac_f32_16x16x64_bf16 v[84:87], v[56:59], a[248:255], v10\n\t" \
    "v_add_f32 v80, v80, v82\n\t" \
    "v_smfmac_f32_16x16x64_bf16 v[84:87], v[60:63], v[224:231], v10\n\t" \
    "v_add_f32 v81, v81, v83\n\t" \
    "v_smfmac_f32_16x16x64_bf16 v[84:87], v[64:67], v[232:239], v10\n\t" \
    "v_cvt_pk_bf16_f32 v158, v80, v81\n\t" \
    "v_smfmac_f32_16x16x64_bf16 v[84:87], v[68:71], v[240:247], v10\n\t" \
    "ds_write_b32 v16, v158 offset:0\n\t" \
    "s_nop 6\n\t" \
    "v_add_f32 v84, v84, v86\n\t" \
    "v_add_f32 v85, v85, v87\n\t" \
    "v_cvt_pk_bf16_f32 v159, v84, v85\n\t" \
    "ds_write_b32 v16, v159 offset:256\n\t" \
    "s_cmp_lt_i32 s83, 0\n\t" \
    "s_waitcnt lgkmcnt(0)\n\t" \
    "s_barrier\n\t" \
    "ds_read_b64_tr_b16 v[132:133], v13 offset:0\n\t" \
    "ds_read_b64_tr_b16 v[134:135], v13 offset:128\n\t" \
    "ds_read_b64_tr_b16 v[136:137], v13 offset:1024\n\t" \
    "ds_read_b64_tr_b16 v[138:139], v13 offset:1152\n\t" \
    "ds_read_b64_tr_b16 v[140:141], v17 offset:0\n\t" \
    "ds_read_b64_tr_b16 v[142:143], v17 offset:256\n\t" \
    "ds_read_b64_tr_b16 v[144:145], v17 offset:512\n\t" \
    "ds_read_b64_tr_b16 v[146:147], v17 offset:768\n\t" \
    "ds_read_b64_tr_b16 v[148:149], v17 offset:1024\n\t" \
    "ds_read_b64_tr_b16 v[150:151], v17 offset:1280\n\t" \
    "ds_read_b64_tr_b16 v[152:153], v17 offset:1536\n\t" \
    "ds_read_b64_tr_b16 v[154:155], v17 offset:1792\n\t" \
    "s_cbranch_scc1 qk_k018_%=\n\t" \
    "qk_k0b19_%=:\n\t" \
    "s_waitcnt lgkmcnt(10)\n\t" \
    "v_smfmac_f32_16x16x64_bf16 v[88:91], v[132:135], v[100:107], v10\n\t" \
    "v_smfmac_f32_16x16x64_bf16 v[92:95], v[132:135], v[116:123], v10\n\t" \
    "s_waitcnt lgkmcnt(8)\n\t" \
    "v_smfmac_f32_16x16x64_bf16 v[88:91], v[136:139], v[108:115], v10\n\t" \
    "v_smfmac_f32_16x16x64_bf16 v[92:95], v[136:139], v[124:131], v10\n\t" \
    "s_waitcnt lgkmcnt(4)\n\t" \
    "v_smfmac_f32_16x16x64_bf16 v[96:99], v[132:135], v[140:147], v10\n\t" \
    "s_waitcnt lgkmcnt(0)\n\t" \
    "s_nop 1\n\t" \
    "v_smfmac_f32_16x16x64_bf16 v[96:99], v[136:139], v[148:155], v10\n\t" \
    "s_nop 0\n\t" \
    "v_add_f32 v168, v88, v90\n\t" \
    "v_add_f32 v169, v92, v94\n\t" \
    "v_cvt_pk_bf16_f32 v168, 0, v168\n\t" \
    "v_cvt_pk_bf16_f32 v169, 0, v169\n\t" \
    "v_mul_f32 v168, v168, v26\n\t" \
    "v_mul_f32 v169, v169, v26\n\t" \
    "v_cvt_pk_bf16_f32 v168, 0, v168\n\t" \
    "v_add_f32 v172, v96, v98\n\t" \
    "v_cvt_pk_bf16_f32 v169, 0, v169\n\t" \
    "v_cvt_pk_bf16_f32 v172, 0, v172\n\t" \
    "v_max_f32 v178, v168, v169\n\t" \
    "v_mul_f32 v172, v172, v26\n\t" \
    "v_mul_f32 v168, v168, v28\n\t" \
    "v_cvt_pk_bf16_f32 v172, 0, v172\n\t" \
    "v_max_f32_dpp v178, v178, v178 row_mirror row_mask:0xf bank_mask:0xf\n\t" \
    "v_max_f32 v173, v178, v172\n\t" \
    "v_mul_f32 v172, v172, v28\n\t" \
    "v_mul_f32 v169, v169, v28\n\t" \
    "v_max_f32_dpp v173, v173, v173 quad_perm:[1,0,3,2] row_mask:0xf bank_mask:0xf\n\t" \
    "s_nop 1\n\t" \
    "v_max_f32_dpp v173, v173, v173 quad_perm:[2,3,0,1] row_mask:0xf bank_mask:0xf\n\t" \
    "s_nop 1\n\t" \
    "v_max_f32_dpp v173, v173, v173 row_half_mirror row_mask:0xf bank_mask:0xf\n\t" \
    "v_fma_f32 v172, -v173, v28, v172\n\t" \
    "v_fma_f32 v168, -v173, v28, v168\n\t" \
    "v_fma_f32 v169, -v173, v28, v169\n\t" \
    "v_exp_f32 v172, v172\n\t" \
    "v_exp_f32 v168, v168\n\t" \
    "v_exp_f32 v169, v169\n\t" \
    "v_fma_f32 v173, v172, 0.5, v168\n\t" \
    "v_add_f32 v173, v173, v169\n\t" \
    "v_mov_b32_dpp v174, v172 quad_perm:[0,0,2,2] row_mask:0xf bank_mask:0xf\n\t" \
    "v_mov_b32_dpp v175, v172 quad_perm:[1,1,3,3] row_mask:0xf bank_mask:0xf\n\t" \
    "v_add_f32_dpp v173, v173, v173 quad_perm:[1,0,3,2] row_mask:0xf bank_mask:0xf\n\t" \
    "v_mov_b32_dpp v176, v168 quad_perm:[1,0,3,2] row_mask:0xf bank_mask:0xf\n\t" \
    "v_mov_b32_dpp v177, v169 quad_perm:[1,0,3,2] row_mask:0xf bank_mask:0xf\n\t" \
    "v_add_f32_dpp v173, v173, v173 quad_perm:[2,3,0,1] row_mask:0xf bank_mask:0xf\n\t" \
    "v_cndmask_b32 v168, v168, v177, s[48:49]\n\t" \
    "v_cndmask_b32 v169, v176, v169, s[48:49]\n\t" \
    "v_add_f32_dpp v173, v173, v173 row_half_mirror row_mask:0xf bank_mask:0xf\n\t" \
    "s_nop 1\n\t" \
    "v_add_f32_dpp v173, v173, v173 row_mirror row_mask:0xf bank_mask:0xf\n\t" \
    "v_rcp_f32 v173, v173\n\t" \
    "s_nop 0\n\t" \
    "v_pk_mul_f32 v[168:169], v[168:169], v[172:173] op_sel:[0,1] op_sel_hi:[1,1]\n\t" \
    "v_pk_mul_f32 v[174:175], v[174:175], v[172:173] op_sel:[0,1] op_sel_hi:[1,1]\n\t" \
    "v_cvt_pk_bf16_f32 v176, v168, v169\n\t" \
    "v_cvt_pk_bf16_f32 v177, v174, v175\n\t" \
    "global_store_dword v18, v176, s[46:47] sc1\n\t" \
    "global_store_dword v3, v176, s[46:47] sc1\n\t" \
    "global_store_dword v19, v177, s[46:47] sc1\n\t" \
    "global_store_dword v4, v177, s[46:47] sc1\n\t" \
    "s_cmp_eq_u32 s65, 63\n\t" \
    "s_cbranch_scc1 qk_last15_%=\n\t" \
    "s_mov_b32 s71, 3\n\t" \
    "qk_E30_%=:\n\t" \
    "s_memtime s[54:55]\n\t" \
    "s_nop 0\n\t" \
    "s_memrealtime s[4:5]\n\t" \
    "s_nop 0\n\t" \
    "s_memrealtime s[6:7]\n\t" \
    "s_nop 0\n\t" \
    "s_memrealtime s[8:9]\n\t" \
    "s_nop 0\n\t" \
    "s_memrealtime s[10:11]\n\t" \
    "s_nop 0\n\t" \
    "s_memrealtime s[12:13]\n\t" \
    "s_nop 0\n\t" \
    "s_memrealtime s[14:15]\n\t" \
    "s_nop 0\n\t" \
    "s_memrealtime s[16:17]\n\t" \
    "s_nop 0\n\t" \
    "s_memrealtime s[18:19]\n\t" \
    "s_nop 0\n\t" \
    "s_memrealtime s[20:21]\n\t" \
    "s_nop 0\n\t" \
    "s_memrealtime s[22:23]\n\t" \
    "s_nop 0\n\t" \
    "s_memrealtime s[24:25]\n\t" \
    "s_nop 0\n\t" \
    "s_memrealtime s[26:27]\n\t" \
    "s_nop 0\n\t" \
    "s_memrealtime s[28:29]\n\t" \
    "s_nop 0\n\t" \
    "s_memtime s[56:57]\n\t" \
    "s_mov_b32 s70, 1\n\t" \
    "s_sub_u32 s71, s71, 1\n\t" \
    "s_cselect_b32 s69, 0, -1\n\t" \
    "s_waitcnt lgkmcnt(0)\n\t" \
    "s_sub_u32 s54, s56, s54\n\t" \
    "s_and_b32 s54, s54, s69\n\t" \
    "s_cmp_gt_u32 s54, 124\n\t" \
    "s_cbranch_scc1 qk_E30_%=\n\t" \
    "s_cmp_eq_u32 s4, s28\n\t" \
    "s_addc_u32 s70, s70, 0\n\t" \
    "s_cmp_eq_u32 s6, s28\n\t" \
    "s_addc_u32 s70, s70, 0\n\t" \
    "s_cmp_eq_u32 s8, s28\n\t" \
    "s_addc_u32 s70, s70, 0\n\t" \
    "s_cmp_eq_u32 s10, s28\n\t" \
    "s_addc_u32 s70, s70, 0\n\t" \
    "s_cmp_eq_u32 s12, s28\n\t" \
    "s_addc_u32 s70, s70, 0\n\t" \
    "s_cmp_eq_u32 s14, s28\n\t" \
    "s_addc_u32 s70, s70, 0\n\t" \
    "s_cmp_eq_u32 s16, s28\n\t" \
    "s_addc_u32 s70, s70, 0\n\t" \
    "s_cmp_eq_u32 s18, s28\n\t" \
    "s_addc_u32 s70, s70, 0\n\t" \
    "s_cmp_eq_u32 s20, s28\n\t" \
    "s_addc_u32 s70, s70, 0\n\t" \
    "s_cmp_eq_u32 s22, s28\n\t" \
    "s_addc_u32 s70, s70, 0\n\t" \
    "s_cmp_eq_u32 s24, s28\n\t" \
    "s_addc_u32 s70, s70, 0\n\t" \
    "s_cmp_eq_u32 s26, s28\n\t" \
    "s_addc_u32 s70, s70, 0\n\t" \
    "s_mov_b32 s75, s73\n\t" \
    "s_mov_b32 s76, s74\n\t" \
    "s_lshl_b32 s70, s70, 3\n\t" \
    "s_sub_u32 s73, s56, s70\n\t" \
    "s_lshl_b32 s74, s28, 4\n\t" \
    "s_sub_u32 s69, s73, s75\n\t" \
    "s_sub_u32 s70, s74, s76\n\t" \
    "s_sub_u32 s71, s70, 1600\n\t" \
    "s_cmp_lt_u32 s71, 400000\n\t" \
    "s_cbranch_scc0 qk_K32_%=\n\t" \
    "v_cvt_f32_u32 v160, s69\n\t" \
    "v_cvt_f32_u32 v161, s70\n\t" \
    "v_rcp_f32 v162, v160\n\t" \
    "v_rcp_f32 v163, v161\n\t" \
    "v_mul_f32 v162, v162, v161\n\t" \
    "v_mul_f32 v163, v163, v160\n\t" \
    "v_mul_f32 v162, 0x4f800000, v162\n\t" \
    "v_mul_f32 v163, 0x4f000000, v163\n\t" \
    "v_cvt_u32_f32 v162, v162\n\t" \
    "v_cvt_u32_f32 v163, v163\n\t" \
    "v_readfirstlane_b32 s69, v162\n\t" \
    "v_readfirstlane_b32 s70, v163\n\t" \
    "s_nop 1\n\t" \
    "s_sub_u32 s69, s69, s78\n\t" \
    "s_ashr_i32 s69, s69, 2\n\t" \
    "s_add_u32 s78, s78, s69\n\t" \
    "s_sub_u32 s70, s70, s77\n\t" \
    "s_ashr_i32 s70, s70, 2\n\t" \
    "s_add_u32 s77, s77, s70\n\t" \
    "qk_K32_%=:\n\t" \
    "s_waitcnt vmcnt(4)\n\t" \
    "v_readfirstlane_b32 s69, v25\n\t" \
    "s_add_u32 s71, s65, 1\n\t" \
    "s_max_i32 s83, s83, s69\n\t" \
    "qk_WF33_%=:\n\t" \
    "s_cmp_ge_i32 s83, s71\n\t" \
    "s_cbranch_scc1 qk_WFok34_%=\n\t" \
    "global_load_dword v160, v24, s[36:37] sc1\n\t" \
    "s_waitcnt vmcnt(0)\n\t" \
    "v_readfirstlane_b32 s83, v160\n\t" \
    "s_nop 1\n\t" \
    "s_cmp_ge_i32 s83, s71\n\t" \
    "s_cbranch_scc1 qk_WFok34_%=\n\t" \
    "s_cmp_eq_u32 s68, 0\n\t" \
    "s_cbranch_scc1 qk_F35_%=\n\t" \
    "s_sub_u32 s68, s68, 1\n\t" \
    "s_sleep 2\n\t" \
    "s_branch qk_WF33_%=\n\t" \
    "qk_F35_%=:\n\t" \
    "v_mov_b32 v160, 6095104\n\t" \
    "v_mov_b32 v161, 9\n\t" \
    "global_store_dword v160, v161, s[36:37] sc1\n\t" \
    "s_waitcnt vmcnt(0)\n\t" \
    "s_branch qk_WFok34_%=\n\t" \
    "qk_WFok34_%=:\n\t" \
    "v_mov_b32 v72, 0\n\t" \
    "v_mov_b32 v73, 0\n\t" \
    "v_mov_b32 v74, 0\n\t" \
    "v_mov_b32 v75, 0\n\t" \
    "v_mov_b32 v76, 0\n\t" \
    "v_mov_b32 v77, 0\n\t" \
    "v_mov_b32 v78, 0\n\t" \
    "v_mov_b32 v79, 0\n\t" \
    "v_mov_b32 v80, 0\n\t" \
    "v_mov_b32 v81, 0\n\t" \
    "v_mov_b32 v82, 0\n\t" \
    "v_mov_b32 v83, 0\n\t" \
    "v_mov_b32 v84, 0\n\t" \
    "v_mov_b32 v85, 0\n\t" \
    "v_mov_b32 v86, 0\n\t" \
    "v_mov_b32 v87, 0\n\t" \
    "v_mov_b32 v88, 0\n\t" \
    "v_mov_b32 v89, 0\n\t" \
    "v_mov_b32 v90, 0\n\t" \
    "v_mov_b32 v91, 0\n\t" \
    "v_mov_b32 v92, 0\n\t" \
    "v_mov_b32 v93, 0\n\t" \
    "v_mov_b32 v94, 0\n\t" \
    "v_mov_b32 v95, 0\n\t" \
    "v_mov_b32 v96, 0\n\t" \
    "v_mov_b32 v97, 0\n\t" \
    "v_mov_b32 v98, 0\n\t" \
    "v_mov_b32 v99, 0\n\t" \
    "s_add_u32 s79, s74, 416\n\t" \
    "s_sub_u32 s70, s79, s74\n\t" \
    "s_max_i32 s70, s70, 0\n\t" \
    "s_min_i32 s70, s70, 16000\n\t" \
    "s_lshl_b32 s70, s70, 1\n\t" \
    "s_mul_hi_u32 s70, s70, s77\n\t" \
    "s_add_u32 s70, s70, s73\n\t" \
    "qk_W36_%=:\n\t" \
    "s_memtime s[56:57]\n\t" \
    "s_waitcnt lgkmcnt(0)\n\t" \
    "s_sub_u32 s69, s70, s56\n\t" \
    "s_cmp_gt_i32 s69, 24\n\t" \
    "s_cbranch_scc1 qk_W36_%=\n\t" \
    "global_load_dwordx4 v[100:103], v23, s[38:39] offset:0 sc1\n\t" \
    "global_load_dwordx4 v[104:107], v23, s[38:39] offset:2048 sc1\n\t" \
    "global_load_dwordx4 v[108:111], v15, s[38:39] offset:0 sc1\n\t" \
    "global_load_dwordx4 v[112:115], v15, s[38:39] offset:2048 sc1\n\t" \
    "global_load_dwordx4 v[116:119], v23, s[38:39] offset:1024 sc1\n\t" \
    "global_load_dwordx4 v[120:123], v23, s[38:39] offset:3072 sc1\n\t" \
    "global_load_dwordx4 v[124:127], v15, s[38:39] offset:1024 sc1\n\t" \
    "global_load_dwordx4 v[128:131], v15, s[38:39] offset:3072 sc1\n\t" \
    "s_add_u32 s65, s65, 1\n\t" \
    "v_add_u32 v31, 1, v31\n\t" \
    "s_add_u32 s69, s65, 0\n\t" \
    "s_bitcmp1_b32 s69, 0\n\t" \
    "s_cselect_b32 s70, s63, s62\n\t" \
    "s_bfe_u32 s69, s69, 0x20001\n\t" \
    "s_lshl_b32 s69, s69, 19\n\t" \
    "s_add_u32 s69, s69, s70\n\t" \
    "s_add_u32 s46, s36, s69\n\t" \
    "s_addc_u32 s47, s37, 0\n\t" \
    "s_add_u32 s69, s65, 3\n\t" \
    "s_cmp_ge_i32 s83, s69\n\t" \
    "s_cselect_b64 vcc, -1, 0\n\t" \
    "v_mov_b32 v9, 6201216\n\t" \
    "v_cndmask_b32 v9, v24, v9, vcc\n\t" \
    "s_mul_i32 s70, s67, 7\n\t" \
    "s_add_u32 s69, s65, 1\n\t" \
    "s_xor_b32 s69, s69, s70\n\t" \
    "s_and_b32 s69, s69, 63\n\t" \
    "s_lshl_b32 s69, s69, 13\n\t" \
    "s_mul_i32 s70, s67, 524288\n\t" \
    "s_add_u32 s69, s69, s70\n\t" \
    "s_add_u32 s69, s69, 23068672\n\t" \
    "s_add_u32 s38, s36, s69\n\t" \
    "s_addc_u32 s39, s37, 0\n\t" \
    "s_waitcnt lgkmcnt(0)\n\t" \
    "s_barrier\n\t" \
    "ds_read_b32 v160, v11 offset:20480\n\t" \
    "s_waitcnt lgkmcnt(0)\n\t" \
    "v_readfirstlane_b32 s81, v160\n\t" \
    "s_nop 1\n\t" \
    "s_mul_hi_u32 s69, s85, s78\n\t" \
    "s_add_u32 s79, s81, s69\n\t" \
    "s_add_u32 s79, s79, s87\n\t" \
    "s_cmp_ge_u32 s65, 37\n\t" \
    "s_cselect_b32 s69, 28, 0\n\t" \
    "s_sub_u32 s79, s79, s69\n\t" \
    "s_add_u32 s80, s79, s88\n\t" \
    "s_branch qk_go20_%=\n\t" \
    "qk_Gbad25_%=:\n\t" \
    "s_waitcnt vmcnt(0) lgkmcnt(0)\n\t" \
    "s_nop 7\n\t" \
    "v_mov_b32 v72, 0\n\t" \
    "v_mov_b32 v73, 0\n\t" \
    "v_mov_b32 v74, 0\n\t" \
    "v_mov_b32 v75, 0\n\t" \
    "v_mov_b32 v76, 0\n\t" \
    "v_mov_b32 v77, 0\n\t" \
    "v_mov_b32 v78, 0\n\t" \
    "v_mov_b32 v79, 0\n\t" \
    "v_mov_b32 v80, 0\n\t" \
    "v_mov_b32 v81, 0\n\t" \
    "v_mov_b32 v82, 0\n\t" \
    "v_mov_b32 v83, 0\n\t" \
    "v_mov_b32 v84, 0\n\t" \
    "v_mov_b32 v85, 0\n\t" \
    "v_mov_b32 v86, 0\n\t" \
    "v_mov_b32 v87, 0\n\t" \
    "s_cmp_eq_u32 s68, 0\n\t" \
    "s_cbranch_scc1 qk_F37_%=\n\t" \
    "s_sub_u32 s68, s68, 1\n\t" \
    "s_branch qk_G24_%=\n\t" \
    "qk_F37_%=:\n\t" \
    "v_mov_b32 v160, 6095104\n\t" \
    "v_mov_b32 v161, 1\n\t" \
    "global_store_dword v160, v161, s[36:37] sc1\n\t" \
    "s_waitcnt vmcnt(0)\n\t" \
    "s_branch qk_Gc27_%=\n\t" \
    "qk_Gbad226_%=:\n\t" \
    "global_load_dwordx4 v[44:47], v2, s[46:47] sc1\n\t" \
    "s_waitcnt vmcnt(0)\n\t" \
    "v_max3_u32 v160, v44, v45, v46\n\t" \
    "v_max_u32 v160, v160, v47\n\t" \
    "v_cmp_eq_u32 vcc, -1, v160\n\t" \
    "s_cbranch_vccz qk_Gre229_%=\n\t" \
    "s_cmp_eq_u32 s68, 0\n\t" \
    "s_cbranch_scc1 qk_F38_%=\n\t" \
    "s_sub_u32 s68, s68, 1\n\t" \
    "s_sleep 1\n\t" \
    "s_branch qk_Gbad226_%=\n\t" \
    "qk_F38_%=:\n\t" \
    "v_mov_b32 v160, 6095104\n\t" \
    "v_mov_b32 v161, 2\n\t" \
    "global_store_dword v160, v161, s[36:37] sc1\n\t" \
    "s_waitcnt vmcnt(0)\n\t" \
    "s_branch qk_Gre229_%=\n\t" \
    "qk_Gre229_%=:\n\t" \
    "v_mov_b32 v72, 0\n\t" \
    "v_mov_b32 v73, 0\n\t" \
    "v_mov_b32 v74, 0\n\t" \
    "v_mov_b32 v75, 0\n\t" \
    "v_mov_b32 v76, 0\n\t" \
    "v_mov_b32 v77, 0\n\t" \
    "v_mov_b32 v78, 0\n\t" \
    "v_mov_b32 v79, 0\n\t" \
    "v_mov_b32 v80, 0\n\t" \
    "v_mov_b32 v81, 0\n\t" \
    "v_mov_b32 v82, 0\n\t" \
    "v_mov_b32 v83, 0\n\t" \
    "v_mov_b32 v84, 0\n\t" \
    "v_mov_b32 v85, 0\n\t" \
    "v_mov_b32 v86, 0\n\t" \
    "v_mov_b32 v87, 0\n\t" \
    "v_smfmac_f32_16x16x64_bf16 v[72:75], v[36:39], a[0:7], v10\n\t" \
    "v_smfmac_f32_16x16x64_bf16 v[76:79], v[36:39], a[72:79], v10\n\t" \
    "v_smfmac_f32_16x16x64_bf16 v[80:83], v[36:39], a[144:151], v10\n\t" \
    "v_smfmac_f32_16x16x64_bf16 v[84:87], v[36:39], a[216:223], v10\n\t" \
    "v_smfmac_f32_16x16x64_bf16 v[72:75], v[40:43], a[8:15], v10\n\t" \
    "v_smfmac_f32_16x16x64_bf16 v[76:79], v[40:43], a[80:87], v10\n\t" \
    "v_smfmac_f32_16x16x64_bf16 v[80:83], v[40:43], a[152:159], v10\n\t" \
    "v_smfmac_f32_16x16x64_bf16 v[84:87], v[40:43], a[224:231], v10\n\t" \
    "v_smfmac_f32_16x16x64_bf16 v[72:75], v[44:47], a[64:71], v10\n\t" \
    "v_smfmac_f32_16x16x64_bf16 v[76:79], v[44:47], a[136:143], v10\n\t" \
    "v_smfmac_f32_16x16x64_bf16 v[80:83], v[44:47], a[208:215], v10\n\t" \
    "v_smfmac_f32_16x16x64_bf16 v[84:87], v[44:47], v[248:255], v10\n\t" \
    "s_branch qk_Gc228_%=\n\t" \
    "qk_k018_%=:\n\t" \
    "qk_WF39_%=:\n\t" \
    "s_cmp_ge_i32 s83, s65\n\t" \
    "s_cbranch_scc1 qk_WFok40_%=\n\t" \
    "global_load_dword v160, v24, s[36:37] sc1\n\t" \
    "s_waitcnt vmcnt(0)\n\t" \
    "v_readfirstlane_b32 s83, v160\n\t" \
    "s_nop 1\n\t" \
    "s_cmp_ge_i32 s83, s65\n\t" \
    "s_cbranch_scc1 qk_WFok40_%=\n\t" \
    "s_cmp_eq_u32 s68, 0\n\t" \
    "s_cbranch_scc1 qk_F41_%=\n\t" \
    "s_sub_u32 s68, s68, 1\n\t" \
    "s_sleep 2\n\t" \
    "s_branch qk_WF39_%=\n\t" \
    "qk_F41_%=:\n\t" \
    "v_mov_b32 v160, 6095104\n\t" \
    "v_mov_b32 v161, 8\n\t" \
    "global_store_dword v160, v161, s[36:37] sc1\n\t" \
    "s_waitcnt vmcnt(0)\n\t" \
    "s_branch qk_WFok40_%=\n\t" \
    "qk_WFok40_%=:\n\t" \
    "global_load_dwordx4 v[100:103], v23, s[50:51] offset:0 sc1\n\t" \
    "global_load_dwordx4 v[104:107], v23, s[50:51] offset:2048 sc1\n\t" \
    "global_load_dwordx4 v[108:111], v15, s[50:51] offset:0 sc1\n\t" \
    "global_load_dwordx4 v[112:115], v15, s[50:51] offset:2048 sc1\n\t" \
    "global_load_dwordx4 v[116:119], v23, s[50:51] offset:1024 sc1\n\t" \
    "global_load_dwordx4 v[120:123], v23, s[50:51] offset:3072 sc1\n\t" \
    "global_load_dwordx4 v[124:127], v15, s[50:51] offset:1024 sc1\n\t" \
    "global_load_dwordx4 v[128:131], v15, s[50:51] offset:3072 sc1\n\t" \
    "s_waitcnt vmcnt(0)\n\t" \
    "s_branch qk_k0b19_%=\n\t" \
    "qk_last15_%=:\n\t" \
    "s_endpgm\n\t" \
    "qk_loop12_%=:\n\t" \
    "v_mov_b32 v72, 0\n\t" \
    "v_mov_b32 v73, 0\n\t" \
    "v_mov_b32 v74, 0\n\t" \
    "v_mov_b32 v75, 0\n\t" \
    "v_mov_b32 v76, 0\n\t" \
    "v_mov_b32 v77, 0\n\t" \
    "v_mov_b32 v78, 0\n\t" \
    "v_mov_b32 v79, 0\n\t" \
    "v_mov_b32 v80, 0\n\t" \
    "v_mov_b32 v81, 0\n\t" \
    "v_mov_b32 v82, 0\n\t" \
    "v_mov_b32 v83, 0\n\t" \
    "v_mov_b32 v84, 0\n\t" \
    "v_mov_b32 v85, 0\n\t" \
    "v_mov_b32 v86, 0\n\t" \
    "v_mov_b32 v87, 0\n\t" \
    "v_mov_b32 v88, 0\n\t" \
    "v_mov_b32 v89, 0\n\t" \
    "v_mov_b32 v90, 0\n\t" \
    "v_mov_b32 v91, 0\n\t" \
    "v_mov_b32 v92, 0\n\t" \
    "v_mov_b32 v93, 0\n\t" \
    "v_mov_b32 v94, 0\n\t" \
    "v_mov_b32 v95, 0\n\t" \
    "v_mov_b32 v96, 0\n\t" \
    "v_mov_b32 v97, 0\n\t" \
    "v_mov_b32 v98, 0\n\t" \
    "v_mov_b32 v99, 0\n\t" \
    "v_add_u32 v31, 1, v31\n\t" \
    "s_add_u32 s69, s65, 0\n\t" \
    "s_bitcmp1_b32 s69, 0\n\t" \
    "s_cselect_b32 s70, s63, s62\n\t" \
    "s_bfe_u32 s69, s69, 0x20001\n\t" \
    "s_lshl_b32 s69, s69, 19\n\t" \
    "s_add_u32 s69, s69, s70\n\t" \
    "s_add_u32 s46, s36, s69\n\t" \
    "s_addc_u32 s47, s37, 0\n\t" \
    "s_add_u32 s69, s65, 3\n\t" \
    "s_cmp_ge_i32 s83, s69\n\t" \
    "s_cselect_b64 vcc, -1, 0\n\t" \
    "v_mov_b32 v9, 6201216\n\t" \
    "v_cndmask_b32 v9, v24, v9, vcc\n\t" \
    "s_mul_i32 s70, s67, 7\n\t" \
    "s_add_u32 s69, s65, 1\n\t" \
    "s_xor_b32 s69, s69, s70\n\t" \
    "s_and_b32 s69, s69, 63\n\t" \
    "s_lshl_b32 s69, s69, 13\n\t" \
    "s_mul_i32 s70, s67, 524288\n\t" \
    "s_add_u32 s69, s69, s70\n\t" \
    "s_add_u32 s69, s69, 23068672\n\t" \
    "s_add_u32 s38, s36, s69\n\t" \
    "s_addc_u32 s39, s37, 0\n\t" \
    "qk_go47_%=:\n\t" \
    "s_sub_u32 s70, s79, s74\n\t" \
    "s_max_i32 s70, s70, 0\n\t" \
    "s_min_i32 s70, s70, 16000\n\t" \
    "s_lshl_b32 s70, s70, 1\n\t" \
    "s_mul_hi_u32 s70, s70, s77\n\t" \
    "s_add_u32 s70, s70, s73\n\t" \
    "s_sub_u32 s70, s70, 100\n\t" \
    "s_getpc_b64 s[30:31]\n\t" \
    "qk_Wp49_%=:\n\t" \
    "s_add_u32 s30, s30, qk_We50_%=-qk_Wp49_%=\n\t" \
    "s_addc_u32 s31, s31, 0\n\t" \
    "qk_W48_%=:\n\t" \
    "s_memtime s[56:57]\n\t" \
    "s_waitcnt lgkmcnt(0)\n\t" \
    "s_sub_u32 s69, s70, s56\n\t" \
    "s_cmp_gt_i32 s69, 111\n\t" \
    "s_cbranch_scc1 qk_W48_%=\n\t" \
    "s_max_i32 s69, s69, 0\n\t" \
    "s_lshr_b32 s69, s69, 1\n\t" \
    "s_and_b32 s69, s69, 0x3c\n\t" \
    "s_sub_u32 s30, s30, s69\n\t" \
    "s_subb_u32 s31, s31, 0\n\t" \
    "s_setpc_b64 s[30:31]\n\t" \
    ".p2align 6\n\t" \
    "s_nop 0\n\t" \
    "s_nop 0\n\t" \
    "s_nop 0\n\t" \
    "s_nop 1\n\t" \
    "s_nop 1\n\t" \
    "s_nop 1\n\t" \
    "s_nop 1\n\t" \
    "s_nop 1\n\t" \
    "s_nop 1\n\t" \
    "s_nop 1\n\t" \
    "s_nop 1\n\t" \
    "s_nop 1\n\t" \
    "s_nop 1\n\t" \
    "s_nop 1\n\t" \
    "s_nop 1\n\t" \
    "s_nop 1\n\t" \
    "qk_We50_%=:\n\t" \
    "qk_G51_%=:\n\t" \
    "global_load_dwordx4 v[36:39], v1, s[46:47] sc1\n\t" \
    "s_nop 9\n\t" \
    "global_load_dwordx4 v[40:43], v1, s[46:47] offset:1024 sc1\n\t" \
    "s_nop 9\n\t" \
    "global_load_dwordx4 v[44:47], v2, s[46:47] sc1\n\t" \
    "s_waitcnt vmcnt(2)\n\t" \
    "v_smfmac_f32_16x16x64_bf16 v[72:75], v[36:39], a[16:23], v10\n\t" \
    "ds_write_b128 v0, v[36:39] offset:2048\n\t" \
    "v_smfmac_f32_16x16x64_bf16 v[76:79], v[36:39], a[88:95], v10\n\t" \
    "v_max3_u32 v160, v36, v37, v38\n\t" \
    "v_smfmac_f32_16x16x64_bf16 v[80:83], v[36:39], a[160:167], v10\n\t" \
    "v_smfmac_f32_16x16x64_bf16 v[84:87], v[36:39], a[232:239], v10\n\t" \
    "s_waitcnt vmcnt(1)\n\t" \
    "ds_write_b128 v0, v[40:43] offset:3072\n\t" \
    "v_smfmac_f32_16x16x64_bf16 v[72:75], v[40:43], a[24:31], v10\n\t" \
    "v_max3_u32 v160, v160, v39, v40\n\t" \
    "v_max3_u32 v160, v160, v41, v42\n\t" \
    "v_smfmac_f32_16x16x64_bf16 v[76:79], v[40:43], a[96:103], v10\n\t" \
    "v_max_u32 v160, v160, v43\n\t" \
    "v_cmp_eq_u32 vcc, -1, v160\n\t" \
    "v_smfmac_f32_16x16x64_bf16 v[80:83], v[40:43], a[168:175], v10\n\t" \
    "v_smfmac_f32_16x16x64_bf16 v[84:87], v[40:43], a[240:247], v10\n\t" \
    "s_cbranch_vccnz qk_Gbad52_%=\n\t" \
    "qk_Gc54_%=:\n\t" \
    "s_waitcnt lgkmcnt(0)\n\t" \
    "s_barrier\n\t" \
    "ds_read_b128 v[48:51], v0 offset:4096\n\t" \
    "ds_read_b128 v[52:55], v0 offset:5120\n\t" \
    "s_waitcnt vmcnt(0)\n\t" \
    "v_smfmac_f32_16x16x64_bf16 v[72:75], v[44:47], a[64:71], v10\n\t" \
    "ds_read_b128 v[56:59], v0 offset:6144\n\t" \
    "v_max3_u32 v160, v44, v45, v46\n\t" \
    "v_smfmac_f32_16x16x64_bf16 v[76:79], v[44:47], a[136:143], v10\n\t" \
    "ds_read_b128 v[60:63], v0 offset:7168\n\t" \
    "v_max_u32 v160, v160, v47\n\t" \
    "v_smfmac_f32_16x16x64_bf16 v[80:83], v[44:47], a[208:215], v10\n\t" \
    "ds_read_b128 v[64:67], v0 offset:0\n\t" \
    "v_cmp_eq_u32 vcc, -1, v160\n\t" \
    "v_smfmac_f32_16x16x64_bf16 v[84:87], v[44:47], v[248:255], v10\n\t" \
    "ds_read_b128 v[68:71], v0 offset:1024\n\t" \
    "s_cbranch_vccnz qk_Gbad253_%=\n\t" \
    "qk_Gc255_%=:\n\t" \
    "s_waitcnt lgkmcnt(5)\n\t" \
    "v_smfmac_f32_16x16x64_bf16 v[72:75], v[48:51], a[32:39], v10\n\t" \
    "s_waitcnt lgkmcnt(4)\n\t" \
    "v_smfmac_f32_16x16x64_bf16 v[72:75], v[52:55], a[40:47], v10\n\t" \
    "s_waitcnt lgkmcnt(3)\n\t" \
    "v_smfmac_f32_16x16x64_bf16 v[72:75], v[56:59], a[48:55], v10\n\t" \
    "global_load_dword v25, v9, s[36:37] sc1\n\t" \
    "s_waitcnt lgkmcnt(2)\n\t" \
    "v_smfmac_f32_16x16x64_bf16 v[72:75], v[60:63], a[56:63], v10\n\t" \
    "s_waitcnt lgkmcnt(1)\n\t" \
    "v_smfmac_f32_16x16x64_bf16 v[72:75], v[64:67], a[0:7], v10\n\t" \
    "s_waitcnt lgkmcnt(0)\n\t" \
    "v_smfmac_f32_16x16x64_bf16 v[72:75], v[68:71], a[8:15], v10\n\t" \
    "v_smfmac_f32_16x16x64_bf16 v[76:79], v[48:51], a[104:111], v10\n\t" \
    "s_nop 1\n\t" \
    "s_nop 0\n\t" \
    "v_smfmac_f32_16x16x64_bf16 v[76:79], v[52:55], a[112:119], v10\n\t" \
    "s_nop 1\n\t" \
    "s_nop 0\n\t" \
    "v_smfmac_f32_16x16x64_bf16 v[76:79], v[56:59], a[120:127], v10\n\t" \
    "v_add_f32 v72, v72, v74\n\t" \
    "v_smfmac_f32_16x16x64_bf16 v[76:79], v[60:63], a[128:135], v10\n\t" \
    "v_add_f32 v73, v73, v75\n\t" \
    "v_smfmac_f32_16x16x64_bf16 v[76:79], v[64:67], a[72:79], v10\n\t" \
    "v_cvt_pk_bf16_f32 v156, v72, v73\n\t" \
    "v_smfmac_f32_16x16x64_bf16 v[76:79], v[68:71], a[80:87], v10\n\t" \
    "ds_write_b32 v12, v156 offset:0\n\t" \
    "v_smfmac_f32_16x16x64_bf16 v[80:83], v[48:51], a[176:183], v10\n\t" \
    "s_nop 1\n\t" \
    "s_nop 0\n\t" \
    "v_smfmac_f32_16x16x64_bf16 v[80:83], v[52:55], a[184:191], v10\n\t" \
    "s_nop 1\n\t" \
    "s_nop 0\n\t" \
    "v_smfmac_f32_16x16x64_bf16 v[80:83], v[56:59], a[192:199], v10\n\t" \
    "v_add_f32 v76, v76, v78\n\t" \
    "v_smfmac_f32_16x16x64_bf16 v[80:83], v[60:63], a[200:207], v10\n\t" \
    "v_add_f32 v77, v77, v79\n\t" \
    "v_smfmac_f32_16x16x64_bf16 v[80:83], v[64:67], a[144:151], v10\n\t" \
    "v_cvt_pk_bf16_f32 v157, v76, v77\n\t" \
    "v_smfmac_f32_16x16x64_bf16 v[80:83], v[68:71], a[152:159], v10\n\t" \
    "ds_write_b32 v12, v157 offset:64\n\t" \
    "v_smfmac_f32_16x16x64_bf16 v[84:87], v[48:51], a[248:255], v10\n\t" \
    "s_nop 1\n\t" \
    "s_nop 0\n\t" \
    "v_smfmac_f32_16x16x64_bf16 v[84:87], v[52:55], v[224:231], v10\n\t" \
    "s_nop 1\n\t" \
    "s_nop 0\n\t" \
    "v_smfmac_f32_16x16x64_bf16 v[84:87], v[56:59], v[232:239], v10\n\t" \
    "v_add_f32 v80, v80, v82\n\t" \
    "v_smfmac_f32_16x16x64_bf16 v[84:87], v[60:63], v[240:247], v10\n\t" \
    "v_add_f32 v81, v81, v83\n\t" \
    "v_smfmac_f32_16x16x64_bf16 v[84:87], v[64:67], a[216:223], v10\n\t" \
    "v_cvt_pk_bf16_f32 v158, v80, v81\n\t" \
    "v_smfmac_f32_16x16x64_bf16 v[84:87], v[68:71], a[224:231], v10\n\t" \
    "ds_write_b32 v16, v158 offset:0\n\t" \
    "s_nop 6\n\t" \
    "v_add_f32 v84, v84, v86\n\t" \
    "v_add_f32 v85, v85, v87\n\t" \
    "v_cvt_pk_bf16_f32 v159, v84, v85\n\t" \
    "ds_write_b32 v16, v159 offset:256\n\t" \
    "s_cmp_lt_i32 s83, 0\n\t" \
    "s_waitcnt lgkmcnt(0)\n\t" \
    "s_barrier\n\t" \
    "ds_read_b64_tr_b16 v[132:133], v13 offset:0\n\t" \
    "ds_read_b64_tr_b16 v[134:135], v13 offset:128\n\t" \
    "ds_read_b64_tr_b16 v[136:137], v13 offset:1024\n\t" \
    "ds_read_b64_tr_b16 v[138:139], v13 offset:1152\n\t" \
    "ds_read_b64_tr_b16 v[140:141], v17 offset:0\n\t" \
    "ds_read_b64_tr_b16 v[142:143], v17 offset:256\n\t" \
    "ds_read_b64_tr_b16 v[144:145], v17 offset:512\n\t" \
    "ds_read_b64_tr_b16 v[146:147], v17 offset:768\n\t" \
    "ds_read_b64_tr_b16 v[148:149], v17 offset:1024\n\t" \
    "ds_read_b64_tr_b16 v[150:151], v17 offset:1280\n\t" \
    "ds_read_b64_tr_b16 v[152:153], v17 offset:1536\n\t" \
    "ds_read_b64_tr_b16 v[154:155], v17 offset:1792\n\t" \
    "s_cbranch_scc1 qk_k045_%=\n\t" \
    "qk_k0b46_%=:\n\t" \
    "s_waitcnt lgkmcnt(10)\n\t" \
    "v_smfmac_f32_16x16x64_bf16 v[88:91], v[132:135], v[100:107], v10\n\t" \
    "v_smfmac_f32_16x16x64_bf16 v[92:95], v[132:135], v[116:123], v10\n\t" \
    "s_waitcnt lgkmcnt(8)\n\t" \
    "v_smfmac_f32_16x16x64_bf16 v[88:91], v[136:139], v[108:115], v10\n\t" \
    "v_smfmac_f32_16x16x64_bf16 v[92:95], v[136:139], v[124:131], v10\n\t" \
    "s_waitcnt lgkmcnt(4)\n\t" \
    "v_smfmac_f32_16x16x64_bf16 v[96:99], v[132:135], v[140:147], v10\n\t" \
    "s_waitcnt lgkmcnt(0)\n\t" \
    "s_nop 1\n\t" \
    "v_smfmac_f32_16x16x64_bf16 v[96:99], v[136:139], v[148:155], v10\n\t" \
    "s_nop 0\n\t" \
    "v_add_f32 v168, v89, v91\n\t" \
    "v_add_f32 v169, v93, v95\n\t" \
    "v_cvt_pk_bf16_f32 v168, 0, v168\n\t" \
    "v_cvt_pk_bf16_f32 v169, 0, v169\n\t" \
    "v_mul_f32 v168, v168, v26\n\t" \
    "v_mul_f32 v169, v169, v26\n\t" \
    "v_cvt_pk_bf16_f32 v168, 0, v168\n\t" \
    "v_add_f32 v172, v97, v99\n\t" \
    "v_cvt_pk_bf16_f32 v169, 0, v169\n\t" \
    "v_cvt_pk_bf16_f32 v172, 0, v172\n\t" \
    "v_max_f32 v178, v168, v169\n\t" \
    "v_mul_f32 v172, v172, v26\n\t" \
    "v_mul_f32 v168, v168, v28\n\t" \
    "v_cvt_pk_bf16_f32 v172, 0, v172\n\t" \
    "v_max_f32_dpp v178, v178, v178 row_mirror row_mask:0xf bank_mask:0xf\n\t" \
    "v_max_f32 v173, v178, v172\n\t" \
    "v_mul_f32 v172, v172, v28\n\t" \
    "v_mul_f32 v169, v169, v28\n\t" \
    "v_max_f32_dpp v173, v173, v173 quad_perm:[1,0,3,2] row_mask:0xf bank_mask:0xf\n\t" \
    "s_nop 1\n\t" \
    "v_max_f32_dpp v173, v173, v173 quad_perm:[2,3,0,1] row_mask:0xf bank_mask:0xf\n\t" \
    "s_nop 1\n\t" \
    "v_max_f32_dpp v173, v173, v173 row_half_mirror row_mask:0xf bank_mask:0xf\n\t" \
    "v_fma_f32 v172, -v173, v28, v172\n\t" \
    "v_fma_f32 v168, -v173, v28, v168\n\t" \
    "v_fma_f32 v169, -v173, v28, v169\n\t" \
    "v_exp_f32 v172, v172\n\t" \
    "v_exp_f32 v168, v168\n\t" \
    "v_exp_f32 v169, v169\n\t" \
    "v_fma_f32 v173, v172, 0.5, v168\n\t" \
    "v_add_f32 v173, v173, v169\n\t" \
    "v_mov_b32_dpp v174, v172 quad_perm:[0,0,2,2] row_mask:0xf bank_mask:0xf\n\t" \
    "v_mov_b32_dpp v175, v172 quad_perm:[1,1,3,3] row_mask:0xf bank_mask:0xf\n\t" \
    "v_add_f32_dpp v173, v173, v173 quad_perm:[1,0,3,2] row_mask:0xf bank_mask:0xf\n\t" \
    "v_mov_b32_dpp v176, v168 quad_perm:[1,0,3,2] row_mask:0xf bank_mask:0xf\n\t" \
    "v_mov_b32_dpp v177, v169 quad_perm:[1,0,3,2] row_mask:0xf bank_mask:0xf\n\t" \
    "v_add_f32_dpp v173, v173, v173 quad_perm:[2,3,0,1] row_mask:0xf bank_mask:0xf\n\t" \
    "v_cndmask_b32 v168, v168, v177, s[48:49]\n\t" \
    "v_cndmask_b32 v169, v176, v169, s[48:49]\n\t" \
    "v_add_f32_dpp v173, v173, v173 row_half_mirror row_mask:0xf bank_mask:0xf\n\t" \
    "s_nop 1\n\t" \
    "v_add_f32_dpp v173, v173, v173 row_mirror row_mask:0xf bank_mask:0xf\n\t" \
    "v_rcp_f32 v173, v173\n\t" \
    "s_nop 0\n\t" \
    "v_pk_mul_f32 v[168:169], v[168:169], v[172:173] op_sel:[0,1] op_sel_hi:[1,1]\n\t" \
    "v_pk_mul_f32 v[174:175], v[174:175], v[172:173] op_sel:[0,1] op_sel_hi:[1,1]\n\t" \
    "v_cvt_pk_bf16_f32 v176, v168, v169\n\t" \
    "v_cvt_pk_bf16_f32 v177, v174, v175\n\t" \
    "global_store_dword v18, v176, s[46:47] sc1\n\t" \
    "global_store_dword v3, v176, s[46:47] sc1\n\t" \
    "global_store_dword v19, v177, s[46:47] sc1\n\t" \
    "global_store_dword v4, v177, s[46:47] sc1\n\t" \
    "s_cmp_eq_u32 s65, 63\n\t" \
    "s_cbranch_scc1 qk_last42_%=\n\t" \
    "s_mov_b32 s71, 3\n\t" \
    "qk_E57_%=:\n\t" \
    "s_memtime s[54:55]\n\t" \
    "s_nop 0\n\t" \
    "s_memrealtime s[4:5]\n\t" \
    "s_nop 0\n\t" \
    "s_memrealtime s[6:7]\n\t" \
    "s_nop 0\n\t" \
    "s_memrealtime s[8:9]\n\t" \
    "s_nop 0\n\t" \
    "s_memrealtime s[10:11]\n\t" \
    "s_nop 0\n\t" \
    "s_memrealtime s[12:13]\n\t" \
    "s_nop 0\n\t" \
    "s_memrealtime s[14:15]\n\t" \
    "s_nop 0\n\t" \
    "s_memrealtime s[16:17]\n\t" \
    "s_nop 0\n\t" \
    "s_memrealtime s[18:19]\n\t" \
    "s_nop 0\n\t" \
    "s_memrealtime s[20:21]\n\t" \
    "s_nop 0\n\t" \
    "s_memrealtime s[22:23]\n\t" \
    "s_nop 0\n\t" \
    "s_memrealtime s[24:25]\n\t" \
    "s_nop 0\n\t" \
    "s_memrealtime s[26:27]\n\t" \
    "s_nop 0\n\t" \
    "s_memrealtime s[28:29]\n\t" \
    "s_nop 0\n\t" \
    "s_memtime s[56:57]\n\t" \
    "s_mov_b32 s70, 1\n\t" \
    "s_sub_u32 s71, s71, 1\n\t" \
    "s_cselect_b32 s69, 0, -1\n\t" \
    "s_waitcnt lgkmcnt(0)\n\t" \
    "s_sub_u32 s54, s56, s54\n\t" \
    "s_and_b32 s54, s54, s69\n\t" \
    "s_cmp_gt_u32 s54, 124\n\t" \
    "s_cbranch_scc1 qk_E57_%=\n\t" \
    "s_cmp_eq_u32 s4, s28\n\t" \
    "s_addc_u32 s70, s70, 0\n\t" \
    "s_cmp_eq_u32 s6, s28\n\t" \
    "s_addc_u32 s70, s70, 0\n\t" \
    "s_cmp_eq_u32 s8, s28\n\t" \
    "s_addc_u32 s70, s70, 0\n\t" \
    "s_cmp_eq_u32 s10, s28\n\t" \
    "s_addc_u32 s70, s70, 0\n\t" \
    "s_cmp_eq_u32 s12, s28\n\t" \
    "s_addc_u32 s70, s70, 0\n\t" \
    "s_cmp_eq_u32 s14, s28\n\t" \
    "s_addc_u32 s70, s70, 0\n\t" \
    "s_cmp_eq_u32 s16, s28\n\t" \
    "s_addc_u32 s70, s70, 0\n\t" \
    "s_cmp_eq_u32 s18, s28\n\t" \
    "s_addc_u32 s70, s70, 0\n\t" \
    "s_cmp_eq_u32 s20, s28\n\t" \
    "s_addc_u32 s70, s70, 0\n\t" \
    "s_cmp_eq_u32 s22, s28\n\t" \
    "s_addc_u32 s70, s70, 0\n\t" \
    "s_cmp_eq_u32 s24, s28\n\t" \
    "s_addc_u32 s70, s70, 0\n\t" \
    "s_cmp_eq_u32 s26, s28\n\t" \
    "s_addc_u32 s70, s70, 0\n\t" \
    "s_mov_b32 s75, s73\n\t" \
    "s_mov_b32 s76, s74\n\t" \
    "s_lshl_b32 s70, s70, 3\n\t" \
    "s_sub_u32 s73, s56, s70\n\t" \
    "s_lshl_b32 s74, s28, 4\n\t" \
    "s_sub_u32 s69, s73, s75\n\t" \
    "s_sub_u32 s70, s74, s76\n\t" \
    "s_sub_u32 s71, s70, 1600\n\t" \
    "s_cmp_lt_u32 s71, 400000\n\t" \
    "s_cbranch_scc0 qk_K59_%=\n\t" \
    "v_cvt_f32_u32 v160, s69\n\t" \
    "v_cvt_f32_u32 v161, s70\n\t" \
    "v_rcp_f32 v162, v160\n\t" \
    "v_rcp_f32 v163, v161\n\t" \
    "v_mul_f32 v162, v162, v161\n\t" \
    "v_mul_f32 v163, v163, v160\n\t" \
    "v_mul_f32 v162, 0x4f800000, v162\n\t" \
    "v_mul_f32 v163, 0x4f000000, v163\n\t" \
    "v_cvt_u32_f32 v162, v162\n\t" \
    "v_cvt_u32_f32 v163, v163\n\t" \
    "v_readfirstlane_b32 s69, v162\n\t" \
    "v_readfirstlane_b32 s70, v163\n\t" \
    "s_nop 1\n\t" \
    "s_sub_u32 s69, s69, s78\n\t" \
    "s_ashr_i32 s69, s69, 2\n\t" \
    "s_add_u32 s78, s78, s69\n\t" \
    "s_sub_u32 s70, s70, s77\n\t" \
    "s_ashr_i32 s70, s70, 2\n\t" \
    "s_add_u32 s77, s77, s70\n\t" \
    "qk_K59_%=:\n\t" \
    "s_waitcnt vmcnt(4)\n\t" \
    "v_readfirstlane_b32 s69, v25\n\t" \
    "s_add_u32 s71, s65, 1\n\t" \
    "s_max_i32 s83, s83, s69\n\t" \
    "qk_WF60_%=:\n\t" \
    "s_cmp_ge_i32 s83, s71\n\t" \
    "s_cbranch_scc1 qk_WFok61_%=\n\t" \
    "global_load_dword v160, v24, s[36:37] sc1\n\t" \
    "s_waitcnt vmcnt(0)\n\t" \
    "v_readfirstlane_b32 s83, v160\n\t" \
    "s_nop 1\n\t" \
    "s_cmp_ge_i32 s83, s71\n\t" \
    "s_cbranch_scc1 qk_WFok61_%=\n\t" \
    "s_cmp_eq_u32 s68, 0\n\t" \
    "s_cbranch_scc1 qk_F62_%=\n\t" \
    "s_sub_u32 s68, s68, 1\n\t" \
    "s_sleep 2\n\t" \
    "s_branch qk_WF60_%=\n\t" \
    "qk_F62_%=:\n\t" \
    "v_mov_b32 v160, 6095104\n\t" \
    "v_mov_b32 v161, 9\n\t" \
    "global_store_dword v160, v161, s[36:37] sc1\n\t" \
    "s_waitcnt vmcnt(0)\n\t" \
    "s_branch qk_WFok61_%=\n\t" \
    "qk_WFok61_%=:\n\t" \
    "v_mov_b32 v72, 0\n\t" \
    "v_mov_b32 v73, 0\n\t" \
    "v_mov_b32 v74, 0\n\t" \
    "v_mov_b32 v75, 0\n\t" \
    "v_mov_b32 v76, 0\n\t" \
    "v_mov_b32 v77, 0\n\t" \
    "v_mov_b32 v78, 0\n\t" \
    "v_mov_b32 v79, 0\n\t" \
    "v_mov_b32 v80, 0\n\t" \
    "v_mov_b32 v81, 0\n\t" \
    "v_mov_b32 v82, 0\n\t" \
    "v_mov_b32 v83, 0\n\t" \
    "v_mov_b32 v84, 0\n\t" \
    "v_mov_b32 v85, 0\n\t" \
    "v_mov_b32 v86, 0\n\t" \
    "v_mov_b32 v87, 0\n\t" \
    "v_mov_b32 v88, 0\n\t" \
    "v_mov_b32 v89, 0\n\t" \
    "v_mov_b32 v90, 0\n\t" \
    "v_mov_b32 v91, 0\n\t" \
    "v_mov_b32 v92, 0\n\t" \
    "v_mov_b32 v93, 0\n\t" \
    "v_mov_b32 v94, 0\n\t" \
    "v_mov_b32 v95, 0\n\t" \
    "v_mov_b32 v96, 0\n\t" \
    "v_mov_b32 v97, 0\n\t" \
    "v_mov_b32 v98, 0\n\t" \
    "v_mov_b32 v99, 0\n\t" \
    "s_add_u32 s79, s74, 416\n\t" \
    "s_sub_u32 s70, s79, s74\n\t" \
    "s_max_i32 s70, s70, 0\n\t" \
    "s_min_i32 s70, s70, 16000\n\t" \
    "s_lshl_b32 s70, s70, 1\n\t" \
    "s_mul_hi_u32 s70, s70, s77\n\t" \
    "s_add_u32 s70, s70, s73\n\t" \
    "qk_W63_%=:\n\t" \
    "s_memtime s[56:57]\n\t" \
    "s_waitcnt lgkmcnt(0)\n\t" \
    "s_sub_u32 s69, s70, s56\n\t" \
    "s_cmp_gt_i32 s69, 24\n\t" \
    "s_cbranch_scc1 qk_W63_%=\n\t" \
    "global_load_dwordx4 v[100:103], v23, s[38:39] offset:0 sc1\n\t" \
    "global_load_dwordx4 v[104:107], v23, s[38:39] offset:2048 sc1\n\t" \
    "global_load_dwordx4 v[108:111], v15, s[38:39] offset:0 sc1\n\t" \
    "global_load_dwordx4 v[112:115], v15, s[38:39] offset:2048 sc1\n\t" \
    "global_load_dwordx4 v[116:119], v23, s[38:39] offset:1024 sc1\n\t" \
    "global_load_dwordx4 v[120:123], v23, s[38:39] offset:3072 sc1\n\t" \
    "global_load_dwordx4 v[124:127], v15, s[38:39] offset:1024 sc1\n\t" \
    "global_load_dwordx4 v[128:131], v15, s[38:39] offset:3072 sc1\n\t" \
    "s_add_u32 s65, s65, 1\n\t" \
    "v_add_u32 v31, 1, v31\n\t" \
    "s_add_u32 s69, s65, 0\n\t" \
    "s_bitcmp1_b32 s69, 0\n\t" \
    "s_cselect_b32 s70, s63, s62\n\t" \
    "s_bfe_u32 s69, s69, 0x20001\n\t" \
    "s_lshl_b32 s69, s69, 19\n\t" \
    "s_add_u32 s69, s69, s70\n\t" \
    "s_add_u32 s46, s36, s69\n\t" \
    "s_addc_u32 s47, s37, 0\n\t" \
    "s_add_u32 s69, s65, 3\n\t" \
    "s_cmp_ge_i32 s83, s69\n\t" \
    "s_cselect_b64 vcc, -1, 0\n\t" \
    "v_mov_b32 v9, 6201216\n\t" \
    "v_cndmask_b32 v9, v24, v9, vcc\n\t" \
    "s_mul_i32 s70, s67, 7\n\t" \
    "s_add_u32 s69, s65, 1\n\t" \
    "s_xor_b32 s69, s69, s70\n\t" \
    "s_and_b32 s69, s69, 63\n\t" \
    "s_lshl_b32 s69, s69, 13\n\t" \
    "s_mul_i32 s70, s67, 524288\n\t" \
    "s_add_u32 s69, s69, s70\n\t" \
    "s_add_u32 s69, s69, 23068672\n\t" \
    "s_add_u32 s38, s36, s69\n\t" \
    "s_addc_u32 s39, s37, 0\n\t" \
    "s_waitcnt lgkmcnt(0)\n\t" \
    "s_barrier\n\t" \
    "ds_read_b32 v160, v11 offset:20480\n\t" \
    "s_waitcnt lgkmcnt(0)\n\t" \
    "v_readfirstlane_b32 s81, v160\n\t" \
    "s_nop 1\n\t" \
    "s_mul_hi_u32 s69, s85, s78\n\t" \
    "s_add_u32 s79, s81, s69\n\t" \
    "s_add_u32 s79, s79, s87\n\t" \
    "s_cmp_ge_u32 s65, 37\n\t" \
    "s_cselect_b32 s69, 28, 0\n\t" \
    "s_sub_u32 s79, s79, s69\n\t" \
    "s_add_u32 s80, s79, s88\n\t" \
    "s_branch qk_go47_%=\n\t" \
    "qk_Gbad52_%=:\n\t" \
    "s_waitcnt vmcnt(0) lgkmcnt(0)\n\t" \
    "s_nop 7\n\t" \
    "v_mov_b32 v72, 0\n\t" \
    "v_mov_b32 v73, 0\n\t" \
    "v_mov_b32 v74, 0\n\t" \
    "v_mov_b32 v75, 0\n\t" \
    "v_mov_b32 v76, 0\n\t" \
    "v_mov_b32 v77, 0\n\t" \
    "v_mov_b32 v78, 0\n\t" \
    "v_mov_b32 v79, 0\n\t" \
    "v_mov_b32 v80, 0\n\t" \
    "v_mov_b32 v81, 0\n\t" \
    "v_mov_b32 v82, 0\n\t" \
    "v_mov_b32 v83, 0\n\t" \
    "v_mov_b32 v84, 0\n\t" \
    "v_mov_b32 v85, 0\n\t" \
    "v_mov_b32 v86, 0\n\t" \
    "v_mov_b32 v87, 0\n\t" \
    "s_cmp_eq_u32 s68, 0\n\t" \
    "s_cbranch_scc1 qk_F64_%=\n\t" \
    "s_sub_u32 s68, s68, 1\n\t" \
    "s_branch qk_G51_%=\n\t" \
    "qk_F64_%=:\n\t" \
    "v_mov_b32 v160, 6095104\n\t" \
    "v_mov_b32 v161, 1\n\t" \
    "global_store_dword v160, v161, s[36:37] sc1\n\t" \
    "s_waitcnt vmcnt(0)\n\t" \
    "s_branch qk_Gc54_%=\n\t" \
    "qk_Gbad253_%=:\n\t" \
    "global_load_dwordx4 v[44:47], v2, s[46:47] sc1\n\t" \
    "s_waitcnt vmcnt(0)\n\t" \
    "v_max3_u32 v160, v44, v45, v46\n\t" \
    "v_max_u32 v160, v160, v47\n\t" \
    "v_cmp_eq_u32 vcc, -1, v160\n\t" \
    "s_cbranch_vccz qk_Gre256_%=\n\t" \
    "s_cmp_eq_u32 s68, 0\n\t" \
    "s_cbranch_scc1 qk_F65_%=\n\t" \
    "s_sub_u32 s68, s68, 1\n\t" \
    "s_sleep 1\n\t" \
    "s_branch qk_Gbad253_%=\n\t" \
    "qk_F65_%=:\n\t" \
    "v_mov_b32 v160, 6095104\n\t" \
    "v_mov_b32 v161, 2\n\t" \
    "global_store_dword v160, v161, s[36:37] sc1\n\t" \
    "s_waitcnt vmcnt(0)\n\t" \
    "s_branch qk_Gre256_%=\n\t" \
    "qk_Gre256_%=:\n\t" \
    "v_mov_b32 v72, 0\n\t" \
    "v_mov_b32 v73, 0\n\t" \
    "v_mov_b32 v74, 0\n\t" \
    "v_mov_b32 v75, 0\n\t" \
    "v_mov_b32 v76, 0\n\t" \
    "v_mov_b32 v77, 0\n\t" \
    "v_mov_b32 v78, 0\n\t" \
    "v_mov_b32 v79, 0\n\t" \
    "v_mov_b32 v80, 0\n\t" \
    "v_mov_b32 v81, 0\n\t" \
    "v_mov_b32 v82, 0\n\t" \
    "v_mov_b32 v83, 0\n\t" \
    "v_mov_b32 v84, 0\n\t" \
    "v_mov_b32 v85, 0\n\t" \
    "v_mov_b32 v86, 0\n\t" \
    "v_mov_b32 v87, 0\n\t" \
    "v_smfmac_f32_16x16x64_bf16 v[72:75], v[36:39], a[16:23], v10\n\t" \
    "v_smfmac_f32_16x16x64_bf16 v[76:79], v[36:39], a[88:95], v10\n\t" \
    "v_smfmac_f32_16x16x64_bf16 v[80:83], v[36:39], a[160:167], v10\n\t" \
    "v_smfmac_f32_16x16x64_bf16 v[84:87], v[36:39], a[232:239], v10\n\t" \
    "v_smfmac_f32_16x16x64_bf16 v[72:75], v[40:43], a[24:31], v10\n\t" \
    "v_smfmac_f32_16x16x64_bf16 v[76:79], v[40:43], a[96:103], v10\n\t" \
    "v_smfmac_f32_16x16x64_bf16 v[80:83], v[40:43], a[168:175], v10\n\t" \
    "v_smfmac_f32_16x16x64_bf16 v[84:87], v[40:43], a[240:247], v10\n\t" \
    "v_smfmac_f32_16x16x64_bf16 v[72:75], v[44:47], a[64:71], v10\n\t" \
    "v_smfmac_f32_16x16x64_bf16 v[76:79], v[44:47], a[136:143], v10\n\t" \
    "v_smfmac_f32_16x16x64_bf16 v[80:83], v[44:47], a[208:215], v10\n\t" \
    "v_smfmac_f32_16x16x64_bf16 v[84:87], v[44:47], v[248:255], v10\n\t" \
    "s_branch qk_Gc255_%=\n\t" \
    "qk_k045_%=:\n\t" \
    "qk_WF66_%=:\n\t" \
    "s_cmp_ge_i32 s83, s65\n\t" \
    "s_cbranch_scc1 qk_WFok67_%=\n\t" \
    "global_load_dword v160, v24, s[36:37] sc1\n\t" \
    "s_waitcnt vmcnt(0)\n\t" \
    "v_readfirstlane_b32 s83, v160\n\t" \
    "s_nop 1\n\t" \
    "s_cmp_ge_i32 s83, s65\n\t" \
    "s_cbranch_scc1 qk_WFok67_%=\n\t" \
    "s_cmp_eq_u32 s68, 0\n\t" \
    "s_cbranch_scc1 qk_F68_%=\n\t" \
    "s_sub_u32 s68, s68, 1\n\t" \
    "s_sleep 2\n\t" \
    "s_branch qk_WF66_%=\n\t" \
    "qk_F68_%=:\n\t" \
    "v_mov_b32 v160, 6095104\n\t" \
    "v_mov_b32 v161, 8\n\t" \
    "global_store_dword v160, v161, s[36:37] sc1\n\t" \
    "s_waitcnt vmcnt(0)\n\t" \
    "s_branch qk_WFok67_%=\n\t" \
    "qk_WFok67_%=:\n\t" \
    "global_load_dwordx4 v[100:103], v23, s[50:51] offset:0 sc1\n\t" \
    "global_load_dwordx4 v[104:107], v23, s[50:51] offset:2048 sc1\n\t" \
    "global_load_dwordx4 v[108:111], v15, s[50:51] offset:0 sc1\n\t" \
    "global_load_dwordx4 v[112:115], v15, s[50:51] offset:2048 sc1\n\t" \
    "global_load_dwordx4 v[116:119], v23, s[50:51] offset:1024 sc1\n\t" \
    "global_load_dwordx4 v[120:123], v23, s[50:51] offset:3072 sc1\n\t" \
    "global_load_dwordx4 v[124:127], v15, s[50:51] offset:1024 sc1\n\t" \
    "global_load_dwordx4 v[128:131], v15, s[50:51] offset:3072 sc1\n\t" \
    "s_waitcnt vmcnt(0)\n\t" \
    "s_branch qk_k0b46_%=\n\t" \
    "qk_last42_%=:\n\t" \
    "s_endpgm\n\t" \
    "qk_loop13_%=:\n\t" \
    "v_mov_b32 v72, 0\n\t" \
    "v_mov_b32 v73, 0\n\t" \
    "v_mov_b32 v74, 0\n\t" \
    "v_mov_b32 v75, 0\n\t" \
    "v_mov_b32 v76, 0\n\t" \
    "v_mov_b32 v77, 0\n\t" \
    "v_mov_b32 v78, 0\n\t" \
    "v_mov_b32 v79, 0\n\t" \
    "v_mov_b32 v80, 0\n\t" \
    "v_mov_b32 v81, 0\n\t" \
    "v_mov_b32 v82, 0\n\t" \
    "v_mov_b32 v83, 0\n\t" \
    "v_mov_b32 v84, 0\n\t" \
    "v_mov_b32 v85, 0\n\t" \
    "v_mov_b32 v86, 0\n\t" \
    "v_mov_b32 v87, 0\n\t" \
    "v_add_u32 v31, 1, v31\n\t" \
    "s_add_u32 s69, s65, 0\n\t" \
    "s_bitcmp1_b32 s69, 0\n\t" \
    "s_cselect_b32 s70, s63, s62\n\t" \
    "s_bfe_u32 s69, s69, 0x20001\n\t" \
    "s_lshl_b32 s69, s69, 19\n\t" \
    "s_add_u32 s69, s69, s70\n\t" \
    "s_add_u32 s46, s36, s69\n\t" \
    "s_addc_u32 s47, s37, 0\n\t" \
    "s_mul_i32 s70, s67, 7\n\t" \
    "s_add_u32 s69, s65, 0\n\t" \
    "s_xor_b32 s69, s69, s70\n\t" \
    "s_and_b32 s69, s69, 63\n\t" \
    "s_lshl_b32 s69, s69, 12\n\t" \
    "s_mul_i32 s70, s67, 262144\n\t" \
    "s_add_u32 s69, s69, s70\n\t" \
    "s_add_u32 s69, s69, 39845888\n\t" \
    "s_add_u32 s40, s36, s69\n\t" \
    "s_addc_u32 s41, s37, 0\n\t" \
    "s_sub_u32 s69, s65, 1\n\t" \
    "v_mov_b32 v25, s69\n\t" \
    "qk_go74_%=:\n\t" \
    "s_sub_u32 s70, s79, s74\n\t" \
    "s_max_i32 s70, s70, 0\n\t" \
    "s_min_i32 s70, s70, 16000\n\t" \
    "s_lshl_b32 s70, s70, 1\n\t" \
    "s_mul_hi_u32 s70, s70, s77\n\t" \
    "s_add_u32 s70, s70, s73\n\t" \
    "s_sub_u32 s70, s70, 100\n\t" \
    "s_getpc_b64 s[30:31]\n\t" \
    "qk_Wp76_%=:\n\t" \
    "s_add_u32 s30, s30, qk_We77_%=-qk_Wp76_%=\n\t" \
    "s_addc_u32 s31, s31, 0\n\t" \
    "qk_W75_%=:\n\t" \
    "s_memtime s[56:57]\n\t" \
    "s_waitcnt lgkmcnt(0)\n\t" \
    "s_sub_u32 s69, s70, s56\n\t" \
    "s_cmp_gt_i32 s69, 111\n\t" \
    "s_cbranch_scc1 qk_W75_%=\n\t" \
    "s_max_i32 s69, s69, 0\n\t" \
    "s_lshr_b32 s69, s69, 1\n\t" \
    "s_and_b32 s69, s69, 0x3c\n\t" \
    "s_sub_u32 s30, s30, s69\n\t" \
    "s_subb_u32 s31, s31, 0\n\t" \
    "s_setpc_b64 s[30:31]\n\t" \
    ".p2align 6\n\t" \
    "s_nop 0\n\t" \
    "s_nop 0\n\t" \
    "s_nop 0\n\t" \
    "s_nop 1\n\t" \
    "s_nop 1\n\t" \
    "s_nop 1\n\t" \
    "s_nop 1\n\t" \
    "s_nop 1\n\t" \
    "s_nop 1\n\t" \
    "s_nop 1\n\t" \
    "s_nop 1\n\t" \
    "s_nop 1\n\t" \
    "s_nop 1\n\t" \
    "s_nop 1\n\t" \
    "s_nop 1\n\t" \
    "s_nop 1\n\t" \
    "qk_We77_%=:\n\t" \
    "qk_G78_%=:\n\t" \
    "global_load_dwordx4 v[36:39], v1, s[46:47] sc1\n\t" \
    "s_nop 9\n\t" \
    "global_load_dwordx4 v[40:43], v1, s[46:47] offset:1024 sc1\n\t" \
    "s_nop 9\n\t" \
    "global_load_dwordx4 v[44:47], v2, s[46:47] sc1\n\t" \
    "s_waitcnt vmcnt(2)\n\t" \
    "v_smfmac_f32_16x16x64_bf16 v[72:75], v[36:39], a[32:39], v10\n\t" \
    "ds_write_b128 v0, v[36:39] offset:4096\n\t" \
    "v_smfmac_f32_16x16x64_bf16 v[76:79], v[36:39], a[104:111], v10\n\t" \
    "v_max3_u32 v160, v36, v37, v38\n\t" \
    "v_smfmac_f32_16x16x64_bf16 v[80:83], v[36:39], a[176:183], v10\n\t" \
    "v_smfmac_f32_16x16x64_bf16 v[84:87], v[36:39], a[248:255], v10\n\t" \
    "s_waitcnt vmcnt(1)\n\t" \
    "ds_write_b128 v0, v[40:43] offset:5120\n\t" \
    "v_smfmac_f32_16x16x64_bf16 v[72:75], v[40:43], a[40:47], v10\n\t" \
    "v_max3_u32 v160, v160, v39, v40\n\t" \
    "v_max3_u32 v160, v160, v41, v42\n\t" \
    "v_smfmac_f32_16x16x64_bf16 v[76:79], v[40:43], a[112:119], v10\n\t" \
    "v_max_u32 v160, v160, v43\n\t" \
    "v_cmp_eq_u32 vcc, -1, v160\n\t" \
    "v_smfmac_f32_16x16x64_bf16 v[80:83], v[40:43], a[184:191], v10\n\t" \
    "v_smfmac_f32_16x16x64_bf16 v[84:87], v[40:43], v[224:231], v10\n\t" \
    "s_cbranch_vccnz qk_Gbad79_%=\n\t" \
    "qk_Gc81_%=:\n\t" \
    "s_waitcnt lgkmcnt(0)\n\t" \
    "s_barrier\n\t" \
    "ds_read_b128 v[48:51], v0 offset:6144\n\t" \
    "ds_read_b128 v[52:55], v0 offset:7168\n\t" \
    "s_waitcnt vmcnt(0)\n\t" \
    "v_smfmac_f32_16x16x64_bf16 v[72:75], v[44:47], a[64:71], v10\n\t" \
    "ds_read_b128 v[56:59], v0 offset:0\n\t" \
    "v_max3_u32 v160, v44, v45, v46\n\t" \
    "v_smfmac_f32_16x16x64_bf16 v[76:79], v[44:47], a[136:143], v10\n\t" \
    "ds_read_b128 v[60:63], v0 offset:1024\n\t" \
    "v_max_u32 v160, v160, v47\n\t" \
    "v_smfmac_f32_16x16x64_bf16 v[80:83], v[44:47], a[208:215], v10\n\t" \
    "ds_read_b128 v[64:67], v0 offset:2048\n\t" \
    "v_cmp_eq_u32 vcc, -1, v160\n\t" \
    "v_smfmac_f32_16x16x64_bf16 v[84:87], v[44:47], v[248:255], v10\n\t" \
    "ds_read_b128 v[68:71], v0 offset:3072\n\t" \
    "s_cbranch_vccnz qk_Gbad280_%=\n\t" \
    "qk_Gc282_%=:\n\t" \
    "s_waitcnt lgkmcnt(5)\n\t" \
    "v_smfmac_f32_16x16x64_bf16 v[72:75], v[48:51], a[48:55], v10\n\t" \
    "global_store_dword v14, v25, s[36:37] sc1\n\t" \
    "s_waitcnt lgkmcnt(4)\n\t" \
    "v_smfmac_f32_16x16x64_bf16 v[72:75], v[52:55], a[56:63], v10\n\t" \
    "s_waitcnt lgkmcnt(3)\n\t" \
    "v_smfmac_f32_16x16x64_bf16 v[72:75], v[56:59], a[0:7], v10\n\t" \
    "s_waitcnt lgkmcnt(2)\n\t" \
    "v_smfmac_f32_16x16x64_bf16 v[72:75], v[60:63], a[8:15], v10\n\t" \
    "s_waitcnt lgkmcnt(1)\n\t" \
    "v_smfmac_f32_16x16x64_bf16 v[72:75], v[64:67], a[16:23], v10\n\t" \
    "s_waitcnt lgkmcnt(0)\n\t" \
    "v_smfmac_f32_16x16x64_bf16 v[72:75], v[68:71], a[24:31], v10\n\t" \
    "v_smfmac_f32_16x16x64_bf16 v[76:79], v[48:51], a[120:127], v10\n\t" \
    "s_nop 1\n\t" \
    "s_nop 0\n\t" \
    "v_smfmac_f32_16x16x64_bf16 v[76:79], v[52:55], a[128:135], v10\n\t" \
    "s_nop 1\n\t" \
    "s_nop 0\n\t" \
    "v_smfmac_f32_16x16x64_bf16 v[76:79], v[56:59], a[72:79], v10\n\t" \
    "v_add_f32 v72, v72, v74\n\t" \
    "v_smfmac_f32_16x16x64_bf16 v[76:79], v[60:63], a[80:87], v10\n\t" \
    "v_add_f32 v73, v73, v75\n\t" \
    "v_smfmac_f32_16x16x64_bf16 v[76:79], v[64:67], a[88:95], v10\n\t" \
    "v_cvt_pk_bf16_f32 v156, v72, v73\n\t" \
    "v_smfmac_f32_16x16x64_bf16 v[76:79], v[68:71], a[96:103], v10\n\t" \
    "ds_write_b32 v12, v156 offset:0\n\t" \
    "v_smfmac_f32_16x16x64_bf16 v[80:83], v[48:51], a[192:199], v10\n\t" \
    "s_nop 1\n\t" \
    "s_nop 0\n\t" \
    "v_smfmac_f32_16x16x64_bf16 v[80:83], v[52:55], a[200:207], v10\n\t" \
    "s_nop 1\n\t" \
    "s_nop 0\n\t" \
    "v_smfmac_f32_16x16x64_bf16 v[80:83], v[56:59], a[144:151], v10\n\t" \
    "v_add_f32 v76, v76, v78\n\t" \
    "v_smfmac_f32_16x16x64_bf16 v[80:83], v[60:63], a[152:159], v10\n\t" \
    "v_add_f32 v77, v77, v79\n\t" \
    "v_smfmac_f32_16x16x64_bf16 v[80:83], v[64:67], a[160:167], v10\n\t" \
    "v_cvt_pk_bf16_f32 v157, v76, v77\n\t" \
    "v_smfmac_f32_16x16x64_bf16 v[80:83], v[68:71], a[168:175], v10\n\t" \
    "ds_write_b32 v12, v157 offset:64\n\t" \
    "v_smfmac_f32_16x16x64_bf16 v[84:87], v[48:51], v[232:239], v10\n\t" \
    "s_nop 1\n\t" \
    "s_nop 0\n\t" \
    "v_smfmac_f32_16x16x64_bf16 v[84:87], v[52:55], v[240:247], v10\n\t" \
    "s_nop 1\n\t" \
    "s_nop 0\n\t" \
    "v_smfmac_f32_16x16x64_bf16 v[84:87], v[56:59], a[216:223], v10\n\t" \
    "v_add_f32 v80, v80, v82\n\t" \
    "v_smfmac_f32_16x16x64_bf16 v[84:87], v[60:63], a[224:231], v10\n\t" \
    "v_add_f32 v81, v81, v83\n\t" \
    "v_smfmac_f32_16x16x64_bf16 v[84:87], v[64:67], a[232:239], v10\n\t" \
    "v_cvt_pk_bf16_f32 v158, v80, v81\n\t" \
    "v_smfmac_f32_16x16x64_bf16 v[84:87], v[68:71], a[240:247], v10\n\t" \
    "ds_write_b32 v16, v158 offset:0\n\t" \
    "s_nop 6\n\t" \
    "v_add_f32 v84, v84, v86\n\t" \
    "v_add_f32 v85, v85, v87\n\t" \
    "v_cvt_pk_bf16_f32 v159, v84, v85\n\t" \
    "ds_write_b32 v16, v159 offset:256\n\t" \
    "s_waitcnt lgkmcnt(0)\n\t" \
    "s_barrier\n\t" \
    "s_sleep 1\n\t" \
    "ds_read_b64_tr_b16 v[140:141], v17 offset:0\n\t" \
    "ds_read_b64_tr_b16 v[142:143], v17 offset:256\n\t" \
    "ds_read_b64_tr_b16 v[144:145], v17 offset:512\n\t" \
    "ds_read_b64_tr_b16 v[146:147], v17 offset:768\n\t" \
    "ds_read_b64_tr_b16 v[148:149], v17 offset:1024\n\t" \
    "ds_read_b64_tr_b16 v[150:151], v17 offset:1280\n\t" \
    "ds_read_b64_tr_b16 v[152:153], v17 offset:1536\n\t" \
    "ds_read_b64_tr_b16 v[154:155], v17 offset:1792\n\t" \
    "s_waitcnt lgkmcnt(0)\n\t" \
    "s_cmp_eq_u32 s65, 63\n\t" \
    "s_cbranch_scc1 qk_last69_%=\n\t" \
    "s_mov_b32 s71, 3\n\t" \
    "qk_E84_%=:\n\t" \
    "s_memtime s[54:55]\n\t" \
    "s_nop 0\n\t" \
    "s_memrealtime s[4:5]\n\t" \
    "s_nop 0\n\t" \
    "s_memrealtime s[6:7]\n\t" \
    "s_nop 0\n\t" \
    "s_memrealtime s[8:9]\n\t" \
    "s_nop 0\n\t" \
    "s_memrealtime s[10:11]\n\t" \
    "s_nop 0\n\t" \
    "s_memrealtime s[12:13]\n\t" \
    "s_nop 0\n\t" \
    "s_memrealtime s[14:15]\n\t" \
    "s_nop 0\n\t" \
    "s_memrealtime s[16:17]\n\t" \
    "s_nop 0\n\t" \
    "s_memrealtime s[18:19]\n\t" \
    "s_nop 0\n\t" \
    "s_memrealtime s[20:21]\n\t" \
    "s_nop 0\n\t" \
    "s_memrealtime s[22:23]\n\t" \
    "s_nop 0\n\t" \
    "s_memrealtime s[24:25]\n\t" \
    "s_nop 0\n\t" \
    "s_memrealtime s[26:27]\n\t" \
    "s_nop 0\n\t" \
    "s_memrealtime s[28:29]\n\t" \
    "s_nop 0\n\t" \
    "s_memtime s[56:57]\n\t" \
    "s_mov_b32 s70, 1\n\t" \
    "s_sub_u32 s71, s71, 1\n\t" \
    "s_cselect_b32 s69, 0, -1\n\t" \
    "s_waitcnt lgkmcnt(0)\n\t" \
    "s_sub_u32 s54, s56, s54\n\t" \
    "s_and_b32 s54, s54, s69\n\t" \
    "s_cmp_gt_u32 s54, 124\n\t" \
    "s_cbranch_scc1 qk_E84_%=\n\t" \
    "s_cmp_eq_u32 s4, s28\n\t" \
    "s_addc_u32 s70, s70, 0\n\t" \
    "s_cmp_eq_u32 s6, s28\n\t" \
    "s_addc_u32 s70, s70, 0\n\t" \
    "s_cmp_eq_u32 s8, s28\n\t" \
    "s_addc_u32 s70, s70, 0\n\t" \
    "s_cmp_eq_u32 s10, s28\n\t" \
    "s_addc_u32 s70, s70, 0\n\t" \
    "s_cmp_eq_u32 s12, s28\n\t" \
    "s_addc_u32 s70, s70, 0\n\t" \
    "s_cmp_eq_u32 s14, s28\n\t" \
    "s_addc_u32 s70, s70, 0\n\t" \
    "s_cmp_eq_u32 s16, s28\n\t" \
    "s_addc_u32 s70, s70, 0\n\t" \
    "s_cmp_eq_u32 s18, s28\n\t" \
    "s_addc_u32 s70, s70, 0\n\t" \
    "s_cmp_eq_u32 s20, s28\n\t" \
    "s_addc_u32 s70, s70, 0\n\t" \
    "s_cmp_eq_u32 s22, s28\n\t" \
    "s_addc_u32 s70, s70, 0\n\t" \
    "s_cmp_eq_u32 s24, s28\n\t" \
    "s_addc_u32 s70, s70, 0\n\t" \
    "s_cmp_eq_u32 s26, s28\n\t" \
    "s_addc_u32 s70, s70, 0\n\t" \
    "s_mov_b32 s75, s73\n\t" \
    "s_mov_b32 s76, s74\n\t" \
    "s_lshl_b32 s70, s70, 3\n\t" \
    "s_sub_u32 s73, s56, s70\n\t" \
    "s_lshl_b32 s74, s28, 4\n\t" \
    "s_sub_u32 s69, s73, s75\n\t" \
    "s_sub_u32 s70, s74, s76\n\t" \
    "s_sub_u32 s71, s70, 1600\n\t" \
    "s_cmp_lt_u32 s71, 400000\n\t" \
    "s_cbranch_scc0 qk_K86_%=\n\t" \
    "v_cvt_f32_u32 v160, s69\n\t" \
    "v_cvt_f32_u32 v161, s70\n\t" \
    "v_rcp_f32 v162, v160\n\t" \
    "v_rcp_f32 v163, v161\n\t" \
    "v_mul_f32 v162, v162, v161\n\t" \
    "v_mul_f32 v163, v163, v160\n\t" \
    "v_mul_f32 v162, 0x4f800000, v162\n\t" \
    "v_mul_f32 v163, 0x4f000000, v163\n\t" \
    "v_cvt_u32_f32 v162, v162\n\t" \
    "v_cvt_u32_f32 v163, v163\n\t" \
    "v_readfirstlane_b32 s69, v162\n\t" \
    "v_readfirstlane_b32 s70, v163\n\t" \
    "s_nop 1\n\t" \
    "s_sub_u32 s69, s69, s78\n\t" \
    "s_ashr_i32 s69, s69, 2\n\t" \
    "s_add_u32 s78, s78, s69\n\t" \
    "s_sub_u32 s70, s70, s77\n\t" \
    "s_ashr_i32 s70, s70, 2\n\t" \
    "s_add_u32 s77, s77, s70\n\t" \
    "qk_K86_%=:\n\t" \
    "v_mov_b32 v72, 0\n\t" \
    "v_mov_b32 v73, 0\n\t" \
    "v_mov_b32 v74, 0\n\t" \
    "v_mov_b32 v75, 0\n\t" \
    "v_mov_b32 v76, 0\n\t" \
    "v_mov_b32 v77, 0\n\t" \
    "v_mov_b32 v78, 0\n\t" \
    "v_mov_b32 v79, 0\n\t" \
    "v_mov_b32 v80, 0\n\t" \
    "v_mov_b32 v81, 0\n\t" \
    "v_mov_b32 v82, 0\n\t" \
    "v_mov_b32 v83, 0\n\t" \
    "v_mov_b32 v84, 0\n\t" \
    "v_mov_b32 v85, 0\n\t" \
    "v_mov_b32 v86, 0\n\t" \
    "v_mov_b32 v87, 0\n\t" \
    "s_add_u32 s79, s74, 560\n\t" \
    "s_cmp_ge_u32 s65, 58\n\t" \
    "s_cselect_b32 s79, s74, s79\n\t" \
    "s_sub_u32 s70, s79, s74\n\t" \
    "s_max_i32 s70, s70, 0\n\t" \
    "s_min_i32 s70, s70, 16000\n\t" \
    "s_lshl_b32 s70, s70, 1\n\t" \
    "s_mul_hi_u32 s70, s70, s77\n\t" \
    "s_add_u32 s70, s70, s73\n\t" \
    "qk_W87_%=:\n\t" \
    "s_memtime s[56:57]\n\t" \
    "s_waitcnt lgkmcnt(0)\n\t" \
    "s_sub_u32 s69, s70, s56\n\t" \
    "s_cmp_gt_i32 s69, 24\n\t" \
    "s_cbranch_scc1 qk_W87_%=\n\t" \
    "s_mov_b64 exec, s[58:59]\n\t" \
    "global_store_dwordx4 v0, v[140:143], s[40:41] offset:0 sc1\n\t" \
    "global_store_dwordx4 v0, v[144:147], s[40:41] offset:1024 sc1\n\t" \
    "global_store_dwordx4 v0, v[148:151], s[40:41] offset:2048 sc1\n\t" \
    "global_store_dwordx4 v0, v[152:155], s[40:41] offset:3072 sc1\n\t" \
    "s_mov_b64 exec, -1\n\t" \
    "s_add_u32 s65, s65, 1\n\t" \
    "v_add_u32 v31, 1, v31\n\t" \
    "s_add_u32 s69, s65, 0\n\t" \
    "s_bitcmp1_b32 s69, 0\n\t" \
    "s_cselect_b32 s70, s63, s62\n\t" \
    "s_bfe_u32 s69, s69, 0x20001\n\t" \
    "s_lshl_b32 s69, s69, 19\n\t" \
    "s_add_u32 s69, s69, s70\n\t" \
    "s_add_u32 s46, s36, s69\n\t" \
    "s_addc_u32 s47, s37, 0\n\t" \
    "s_mul_i32 s70, s67, 7\n\t" \
    "s_add_u32 s69, s65, 0\n\t" \
    "s_xor_b32 s69, s69, s70\n\t" \
    "s_and_b32 s69, s69, 63\n\t" \
    "s_lshl_b32 s69, s69, 12\n\t" \
    "s_mul_i32 s70, s67, 262144\n\t" \
    "s_add_u32 s69, s69, s70\n\t" \
    "s_add_u32 s69, s69, 39845888\n\t" \
    "s_add_u32 s40, s36, s69\n\t" \
    "s_addc_u32 s41, s37, 0\n\t" \
    "s_sub_u32 s69, s65, 1\n\t" \
    "v_mov_b32 v25, s69\n\t" \
    "s_waitcnt lgkmcnt(0)\n\t" \
    "s_barrier\n\t" \
    "ds_read_b32 v160, v11 offset:20480\n\t" \
    "s_waitcnt lgkmcnt(0)\n\t" \
    "v_readfirstlane_b32 s81, v160\n\t" \
    "s_nop 1\n\t" \
    "s_mul_hi_u32 s69, s85, s78\n\t" \
    "s_add_u32 s79, s81, s69\n\t" \
    "s_add_u32 s79, s79, s87\n\t" \
    "s_cmp_ge_u32 s65, 37\n\t" \
    "s_cselect_b32 s69, 28, 0\n\t" \
    "s_sub_u32 s79, s79, s69\n\t" \
    "s_add_u32 s80, s79, s88\n\t" \
    "s_branch qk_go74_%=\n\t" \
    "qk_Gbad79_%=:\n\t" \
    "s_waitcnt vmcnt(0) lgkmcnt(0)\n\t" \
    "s_nop 7\n\t" \
    "v_mov_b32 v72, 0\n\t" \
    "v_mov_b32 v73, 0\n\t" \
    "v_mov_b32 v74, 0\n\t" \
    "v_mov_b32 v75, 0\n\t" \
    "v_mov_b32 v76, 0\n\t" \
    "v_mov_b32 v77, 0\n\t" \
    "v_mov_b32 v78, 0\n\t" \
    "v_mov_b32 v79, 0\n\t" \
    "v_mov_b32 v80, 0\n\t" \
    "v_mov_b32 v81, 0\n\t" \
    "v_mov_b32 v82, 0\n\t" \
    "v_mov_b32 v83, 0\n\t" \
    "v_mov_b32 v84, 0\n\t" \
    "v_mov_b32 v85, 0\n\t" \
    "v_mov_b32 v86, 0\n\t" \
    "v_mov_b32 v87, 0\n\t" \
    "s_cmp_eq_u32 s68, 0\n\t" \
    "s_cbranch_scc1 qk_F88_%=\n\t" \
    "s_sub_u32 s68, s68, 1\n\t" \
    "s_branch qk_G78_%=\n\t" \
    "qk_F88_%=:\n\t" \
    "v_mov_b32 v160, 6095104\n\t" \
    "v_mov_b32 v161, 1\n\t" \
    "global_store_dword v160, v161, s[36:37] sc1\n\t" \
    "s_waitcnt vmcnt(0)\n\t" \
    "s_branch qk_Gc81_%=\n\t" \
    "qk_Gbad280_%=:\n\t" \
    "global_load_dwordx4 v[44:47], v2, s[46:47] sc1\n\t" \
    "s_waitcnt vmcnt(0)\n\t" \
    "v_max3_u32 v160, v44, v45, v46\n\t" \
    "v_max_u32 v160, v160, v47\n\t" \
    "v_cmp_eq_u32 vcc, -1, v160\n\t" \
    "s_cbranch_vccz qk_Gre283_%=\n\t" \
    "s_cmp_eq_u32 s68, 0\n\t" \
    "s_cbranch_scc1 qk_F89_%=\n\t" \
    "s_sub_u32 s68, s68, 1\n\t" \
    "s_sleep 1\n\t" \
    "s_branch qk_Gbad280_%=\n\t" \
    "qk_F89_%=:\n\t" \
    "v_mov_b32 v160, 6095104\n\t" \
    "v_mov_b32 v161, 2\n\t" \
    "global_store_dword v160, v161, s[36:37] sc1\n\t" \
    "s_waitcnt vmcnt(0)\n\t" \
    "s_branch qk_Gre283_%=\n\t" \
    "qk_Gre283_%=:\n\t" \
    "v_mov_b32 v72, 0\n\t" \
    "v_mov_b32 v73, 0\n\t" \
    "v_mov_b32 v74, 0\n\t" \
    "v_mov_b32 v75, 0\n\t" \
    "v_mov_b32 v76, 0\n\t" \
    "v_mov_b32 v77, 0\n\t" \
    "v_mov_b32 v78, 0\n\t" \
    "v_mov_b32 v79, 0\n\t" \
    "v_mov_b32 v80, 0\n\t" \
    "v_mov_b32 v81, 0\n\t" \
    "v_mov_b32 v82, 0\n\t" \
    "v_mov_b32 v83, 0\n\t" \
    "v_mov_b32 v84, 0\n\t" \
    "v_mov_b32 v85, 0\n\t" \
    "v_mov_b32 v86, 0\n\t" \
    "v_mov_b32 v87, 0\n\t" \
    "v_smfmac_f32_16x16x64_bf16 v[72:75], v[36:39], a[32:39], v10\n\t" \
    "v_smfmac_f32_16x16x64_bf16 v[76:79], v[36:39], a[104:111], v10\n\t" \
    "v_smfmac_f32_16x16x64_bf16 v[80:83], v[36:39], a[176:183], v10\n\t" \
    "v_smfmac_f32_16x16x64_bf16 v[84:87], v[36:39], a[248:255], v10\n\t" \
    "v_smfmac_f32_16x16x64_bf16 v[72:75], v[40:43], a[40:47], v10\n\t" \
    "v_smfmac_f32_16x16x64_bf16 v[76:79], v[40:43], a[112:119], v10\n\t" \
    "v_smfmac_f32_16x16x64_bf16 v[80:83], v[40:43], a[184:191], v10\n\t" \
    "v_smfmac_f32_16x16x64_bf16 v[84:87], v[40:43], v[224:231], v10\n\t" \
    "v_smfmac_f32_16x16x64_bf16 v[72:75], v[44:47], a[64:71], v10\n\t" \
    "v_smfmac_f32_16x16x64_bf16 v[76:79], v[44:47], a[136:143], v10\n\t" \
    "v_smfmac_f32_16x16x64_bf16 v[80:83], v[44:47], a[208:215], v10\n\t" \
    "v_smfmac_f32_16x16x64_bf16 v[84:87], v[44:47], v[248:255], v10\n\t" \
    "s_branch qk_Gc282_%=\n\t" \
    "qk_last69_%=:\n\t" \
    "s_mov_b64 exec, s[58:59]\n\t" \
    "global_store_dwordx4 v0, v[140:143], s[40:41] offset:0 sc1\n\t" \
    "global_store_dwordx4 v0, v[144:147], s[40:41] offset:1024 sc1\n\t" \
    "global_store_dwordx4 v0, v[148:151], s[40:41] offset:2048 sc1\n\t" \
    "global_store_dwordx4 v0, v[152:155], s[40:41] offset:3072 sc1\n\t" \
    "s_mov_b64 exec, -1\n\t" \
    "s_endpgm\n\t" \
    "qk_loop14_%=:\n\t" \
    "v_mov_b32 v72, 0\n\t" \
    "v_mov_b32 v73, 0\n\t" \
    "v_mov_b32 v74, 0\n\t" \
    "v_mov_b32 v75, 0\n\t" \
    "v_mov_b32 v76, 0\n\t" \
    "v_mov_b32 v77, 0\n\t" \
    "v_mov_b32 v78, 0\n\t" \
    "v_mov_b32 v79, 0\n\t" \
    "v_mov_b32 v80, 0\n\t" \
    "v_mov_b32 v81, 0\n\t" \
    "v_mov_b32 v82, 0\n\t" \
    "v_mov_b32 v83, 0\n\t" \
    "v_mov_b32 v84, 0\n\t" \
    "v_mov_b32 v85, 0\n\t" \
    "v_mov_b32 v86, 0\n\t" \
    "v_mov_b32 v87, 0\n\t" \
    "v_add_u32 v31, 1, v31\n\t" \
    "s_add_u32 s69, s65, 0\n\t" \
    "s_bitcmp1_b32 s69, 0\n\t" \
    "s_cselect_b32 s70, s63, s62\n\t" \
    "s_bfe_u32 s69, s69, 0x20001\n\t" \
    "s_lshl_b32 s69, s69, 19\n\t" \
    "s_add_u32 s69, s69, s70\n\t" \
    "s_add_u32 s46, s36, s69\n\t" \
    "s_addc_u32 s47, s37, 0\n\t" \
    "qk_go95_%=:\n\t" \
    "s_sub_u32 s70, s79, s74\n\t" \
    "s_max_i32 s70, s70, 0\n\t" \
    "s_min_i32 s70, s70, 16000\n\t" \
    "s_lshl_b32 s70, s70, 1\n\t" \
    "s_mul_hi_u32 s70, s70, s77\n\t" \
    "s_add_u32 s70, s70, s73\n\t" \
    "s_sub_u32 s70, s70, 100\n\t" \
    "s_getpc_b64 s[30:31]\n\t" \
    "qk_Wp97_%=:\n\t" \
    "s_add_u32 s30, s30, qk_We98_%=-qk_Wp97_%=\n\t" \
    "s_addc_u32 s31, s31, 0\n\t" \
    "qk_W96_%=:\n\t" \
    "s_memtime s[56:57]\n\t" \
    "s_waitcnt lgkmcnt(0)\n\t" \
    "s_sub_u32 s69, s70, s56\n\t" \
    "s_cmp_gt_i32 s69, 111\n\t" \
    "s_cbranch_scc1 qk_W96_%=\n\t" \
    "s_max_i32 s69, s69, 0\n\t" \
    "s_lshr_b32 s69, s69, 1\n\t" \
    "s_and_b32 s69, s69, 0x3c\n\t" \
    "s_sub_u32 s30, s30, s69\n\t" \
    "s_subb_u32 s31, s31, 0\n\t" \
    "s_setpc_b64 s[30:31]\n\t" \
    ".p2align 6\n\t" \
    "s_nop 0\n\t" \
    "s_nop 0\n\t" \
    "s_nop 0\n\t" \
    "s_nop 1\n\t" \
    "s_nop 1\n\t" \
    "s_nop 1\n\t" \
    "s_nop 1\n\t" \
    "s_nop 1\n\t" \
    "s_nop 1\n\t" \
    "s_nop 1\n\t" \
    "s_nop 1\n\t" \
    "s_nop 1\n\t" \
    "s_nop 1\n\t" \
    "s_nop 1\n\t" \
    "s_nop 1\n\t" \
    "s_nop 1\n\t" \
    "qk_We98_%=:\n\t" \
    "qk_G99_%=:\n\t" \
    "global_load_dwordx4 v[36:39], v1, s[46:47] sc1\n\t" \
    "s_nop 9\n\t" \
    "global_load_dwordx4 v[40:43], v1, s[46:47] offset:1024 sc1\n\t" \
    "s_nop 9\n\t" \
    "global_load_dwordx4 v[44:47], v2, s[46:47] sc1\n\t" \
    "s_mul_hi_u32 s69, s84, s78\n\t" \
    "s_add_u32 s33, s74, s69\n\t" \
    "s_lshr_b32 s69, s90, 16\n\t" \
    "s_add_u32 s34, s33, s69\n\t" \
    "s_and_b32 s69, s90, 0xffff\n\t" \
    "s_sub_u32 s35, s80, s74\n\t" \
    "s_sub_u32 s35, s35, s69\n\t" \
    "s_add_u32 s99, s35, 1280\n\t" \
    "s_add_u32 s98, s34, 600\n\t" \
    "s_waitcnt vmcnt(2)\n\t" \
    "v_smfmac_f32_16x16x64_bf16 v[72:75], v[36:39], a[48:55], v10\n\t" \
    "ds_write_b128 v0, v[36:39] offset:6144\n\t" \
    "v_smfmac_f32_16x16x64_bf16 v[76:79], v[36:39], a[120:127], v10\n\t" \
    "v_max3_u32 v160, v36, v37, v38\n\t" \
    "v_smfmac_f32_16x16x64_bf16 v[80:83], v[36:39], a[192:199], v10\n\t" \
    "v_smfmac_f32_16x16x64_bf16 v[84:87], v[36:39], v[232:239], v10\n\t" \
    "s_waitcnt vmcnt(1)\n\t" \
    "ds_write_b128 v0, v[40:43] offset:7168\n\t" \
    "v_smfmac_f32_16x16x64_bf16 v[72:75], v[40:43], a[56:63], v10\n\t" \
    "v_max3_u32 v160, v160, v39, v40\n\t" \
    "v_max3_u32 v160, v160, v41, v42\n\t" \
    "v_smfmac_f32_16x16x64_bf16 v[76:79], v[40:43], a[128:135], v10\n\t" \
    "v_max_u32 v160, v160, v43\n\t" \
    "v_cmp_eq_u32 vcc, -1, v160\n\t" \
    "v_smfmac_f32_16x16x64_bf16 v[80:83], v[40:43], a[200:207], v10\n\t" \
    "v_smfmac_f32_16x16x64_bf16 v[84:87], v[40:43], v[240:247], v10\n\t" \
    "s_cbranch_vccnz qk_Gbad100_%=\n\t" \
    "qk_Gc102_%=:\n\t" \
    "s_waitcnt lgkmcnt(0)\n\t" \
    "s_barrier\n\t" \
    "s_memtime s[56:57]\n\t" \
    "ds_read_b128 v[48:51], v0 offset:0\n\t" \
    "ds_read_b128 v[52:55], v0 offset:1024\n\t" \
    "s_waitcnt vmcnt(0)\n\t" \
    "v_smfmac_f32_16x16x64_bf16 v[72:75], v[44:47], a[64:71], v10\n\t" \
    "ds_read_b128 v[56:59], v0 offset:2048\n\t" \
    "v_max3_u32 v160, v44, v45, v46\n\t" \
    "v_smfmac_f32_16x16x64_bf16 v[76:79], v[44:47], a[136:143], v10\n\t" \
    "ds_read_b128 v[60:63], v0 offset:3072\n\t" \
    "v_max_u32 v160, v160, v47\n\t" \
    "v_smfmac_f32_16x16x64_bf16 v[80:83], v[44:47], a[208:215], v10\n\t" \
    "ds_read_b128 v[64:67], v0 offset:4096\n\t" \
    "v_cmp_eq_u32 vcc, -1, v160\n\t" \
    "v_smfmac_f32_16x16x64_bf16 v[84:87], v[44:47], v[248:255], v10\n\t" \
    "ds_read_b128 v[68:71], v0 offset:5120\n\t" \
    "s_sub_u32 s69, s56, s73\n\t" \
    "s_mul_hi_u32 s69, s69, s78\n\t" \
    "s_cmp_gt_i32 s69, s35\n\t" \
    "s_cbranch_vccnz qk_Gbad2101_%=\n\t" \
    "qk_Gc2103_%=:\n\t" \
    "s_cselect_b32 s70, s34, s33\n\t" \
    "s_cmp_gt_i32 s69, s99\n\t" \
    "s_cselect_b32 s70, s98, s70\n\t" \
    "s_add_u32 s70, s69, s70\n\t" \
    "v_and_b32_e64 v162, -2, s70\n\t" \
    "global_store_dword v21, v162, s[46:47] sc1\n\t" \
    "s_waitcnt lgkmcnt(5)\n\t" \
    "v_smfmac_f32_16x16x64_bf16 v[72:75], v[48:51], a[0:7], v10\n\t" \
    "s_waitcnt lgkmcnt(4)\n\t" \
    "v_smfmac_f32_16x16x64_bf16 v[72:75], v[52:55], a[8:15], v10\n\t" \
    "s_waitcnt lgkmcnt(3)\n\t" \
    "v_smfmac_f32_16x16x64_bf16 v[72:75], v[56:59], a[16:23], v10\n\t" \
    "s_waitcnt lgkmcnt(2)\n\t" \
    "v_smfmac_f32_16x16x64_bf16 v[72:75], v[60:63], a[24:31], v10\n\t" \
    "s_waitcnt lgkmcnt(1)\n\t" \
    "v_smfmac_f32_16x16x64_bf16 v[72:75], v[64:67], a[32:39], v10\n\t" \
    "s_waitcnt lgkmcnt(0)\n\t" \
    "v_smfmac_f32_16x16x64_bf16 v[72:75], v[68:71], a[40:47], v10\n\t" \
    "v_smfmac_f32_16x16x64_bf16 v[76:79], v[48:51], a[72:79], v10\n\t" \
    "s_nop 1\n\t" \
    "s_nop 0\n\t" \
    "v_smfmac_f32_16x16x64_bf16 v[76:79], v[52:55], a[80:87], v10\n\t" \
    "s_nop 1\n\t" \
    "s_nop 0\n\t" \
    "v_smfmac_f32_16x16x64_bf16 v[76:79], v[56:59], a[88:95], v10\n\t" \
    "v_add_f32 v72, v72, v74\n\t" \
    "v_smfmac_f32_16x16x64_bf16 v[76:79], v[60:63], a[96:103], v10\n\t" \
    "v_add_f32 v73, v73, v75\n\t" \
    "v_smfmac_f32_16x16x64_bf16 v[76:79], v[64:67], a[104:111], v10\n\t" \
    "v_cvt_pk_bf16_f32 v156, v72, v73\n\t" \
    "v_smfmac_f32_16x16x64_bf16 v[76:79], v[68:71], a[112:119], v10\n\t" \
    "ds_write_b32 v12, v156 offset:0\n\t" \
    "v_smfmac_f32_16x16x64_bf16 v[80:83], v[48:51], a[144:151], v10\n\t" \
    "s_nop 1\n\t" \
    "s_nop 0\n\t" \
    "v_smfmac_f32_16x16x64_bf16 v[80:83], v[52:55], a[152:159], v10\n\t" \
    "s_nop 1\n\t" \
    "s_nop 0\n\t" \
    "v_smfmac_f32_16x16x64_bf16 v[80:83], v[56:59], a[160:167], v10\n\t" \
    "v_add_f32 v76, v76, v78\n\t" \
    "v_smfmac_f32_16x16x64_bf16 v[80:83], v[60:63], a[168:175], v10\n\t" \
    "v_add_f32 v77, v77, v79\n\t" \
    "v_smfmac_f32_16x16x64_bf16 v[80:83], v[64:67], a[176:183], v10\n\t" \
    "v_cvt_pk_bf16_f32 v157, v76, v77\n\t" \
    "v_smfmac_f32_16x16x64_bf16 v[80:83], v[68:71], a[184:191], v10\n\t" \
    "ds_write_b32 v12, v157 offset:64\n\t" \
    "v_smfmac_f32_16x16x64_bf16 v[84:87], v[48:51], a[216:223], v10\n\t" \
    "s_nop 1\n\t" \
    "s_nop 0\n\t" \
    "v_smfmac_f32_16x16x64_bf16 v[84:87], v[52:55], a[224:231], v10\n\t" \
    "s_nop 1\n\t" \
    "s_nop 0\n\t" \
    "v_smfmac_f32_16x16x64_bf16 v[84:87], v[56:59], a[232:239], v10\n\t" \
    "v_add_f32 v80, v80, v82\n\t" \
    "v_smfmac_f32_16x16x64_bf16 v[84:87], v[60:63], a[240:247], v10\n\t" \
    "v_add_f32 v81, v81, v83\n\t" \
    "v_smfmac_f32_16x16x64_bf16 v[84:87], v[64:67], a[248:255], v10\n\t" \
    "v_cvt_pk_bf16_f32 v158, v80, v81\n\t" \
    "v_smfmac_f32_16x16x64_bf16 v[84:87], v[68:71], v[224:231], v10\n\t" \
    "ds_write_b32 v16, v158 offset:0\n\t" \
    "s_nop 6\n\t" \
    "v_add_f32 v84, v84, v86\n\t" \
    "v_add_f32 v85, v85, v87\n\t" \
    "v_cvt_pk_bf16_f32 v159, v84, v85\n\t" \
    "ds_write_b32 v16, v159 offset:256\n\t" \
    "s_waitcnt lgkmcnt(0)\n\t" \
    "s_barrier\n\t" \
    "s_cmp_eq_u32 s65, 63\n\t" \
    "s_cbranch_scc1 qk_last90_%=\n\t" \
    "s_sub_u32 s70, s80, s74\n\t" \
    "s_max_i32 s70, s70, 0\n\t" \
    "s_min_i32 s70, s70, 16000\n\t" \
    "s_lshl_b32 s70, s70, 1\n\t" \
    "s_mul_hi_u32 s70, s70, s77\n\t" \
    "s_add_u32 s70, s70, s73\n\t" \
    "qk_W105_%=:\n\t" \
    "s_memtime s[56:57]\n\t" \
    "s_waitcnt lgkmcnt(0)\n\t" \
    "s_sub_u32 s69, s70, s56\n\t" \
    "s_cmp_gt_i32 s69, 24\n\t" \
    "s_cbranch_scc1 qk_W105_%=\n\t" \
    "global_load_dword v160, v20, s[46:47] sc1\n\t" \
    "s_mov_b32 s71, 3\n\t" \
    "qk_E109_%=:\n\t" \
    "s_memtime s[54:55]\n\t" \
    "s_nop 0\n\t" \
    "s_memrealtime s[4:5]\n\t" \
    "s_nop 0\n\t" \
    "s_memrealtime s[6:7]\n\t" \
    "s_nop 0\n\t" \
    "s_memrealtime s[8:9]\n\t" \
    "s_nop 0\n\t" \
    "s_memrealtime s[10:11]\n\t" \
    "s_nop 0\n\t" \
    "s_memrealtime s[12:13]\n\t" \
    "s_nop 0\n\t" \
    "s_memrealtime s[14:15]\n\t" \
    "s_nop 0\n\t" \
    "s_memrealtime s[16:17]\n\t" \
    "s_nop 0\n\t" \
    "s_memrealtime s[18:19]\n\t" \
    "s_nop 0\n\t" \
    "s_memrealtime s[20:21]\n\t" \
    "s_nop 0\n\t" \
    "s_memrealtime s[22:23]\n\t" \
    "s_nop 0\n\t" \
    "s_memrealtime s[24:25]\n\t" \
    "s_nop 0\n\t" \
    "s_memrealtime s[26:27]\n\t" \
    "s_nop 0\n\t" \
    "s_memrealtime s[28:29]\n\t" \
    "s_nop 0\n\t" \
    "s_memtime s[56:57]\n\t" \
    "s_mov_b32 s70, 1\n\t" \
    "s_sub_u32 s71, s71, 1\n\t" \
    "s_cselect_b32 s69, 0, -1\n\t" \
    "s_waitcnt lgkmcnt(0)\n\t" \
    "s_sub_u32 s54, s56, s54\n\t" \
    "s_and_b32 s54, s54, s69\n\t" \
    "s_cmp_gt_u32 s54, 124\n\t" \
    "s_cbranch_scc1 qk_E109_%=\n\t" \
    "s_cmp_eq_u32 s4, s28\n\t" \
    "s_addc_u32 s70, s70, 0\n\t" \
    "s_cmp_eq_u32 s6, s28\n\t" \
    "s_addc_u32 s70, s70, 0\n\t" \
    "s_cmp_eq_u32 s8, s28\n\t" \
    "s_addc_u32 s70, s70, 0\n\t" \
    "s_cmp_eq_u32 s10, s28\n\t" \
    "s_addc_u32 s70, s70, 0\n\t" \
    "s_cmp_eq_u32 s12, s28\n\t" \
    "s_addc_u32 s70, s70, 0\n\t" \
    "s_cmp_eq_u32 s14, s28\n\t" \
    "s_addc_u32 s70, s70, 0\n\t" \
    "s_cmp_eq_u32 s16, s28\n\t" \
    "s_addc_u32 s70, s70, 0\n\t" \
    "s_cmp_eq_u32 s18, s28\n\t" \
    "s_addc_u32 s70, s70, 0\n\t" \
    "s_cmp_eq_u32 s20, s28\n\t" \
    "s_addc_u32 s70, s70, 0\n\t" \
    "s_cmp_eq_u32 s22, s28\n\t" \
    "s_addc_u32 s70, s70, 0\n\t" \
    "s_cmp_eq_u32 s24, s28\n\t" \
    "s_addc_u32 s70, s70, 0\n\t" \
    "s_cmp_eq_u32 s26, s28\n\t" \
    "s_addc_u32 s70, s70, 0\n\t" \
    "s_mov_b32 s75, s73\n\t" \
    "s_mov_b32 s76, s74\n\t" \
    "s_lshl_b32 s70, s70, 3\n\t" \
    "s_sub_u32 s73, s56, s70\n\t" \
    "s_lshl_b32 s74, s28, 4\n\t" \
    "s_sub_u32 s69, s73, s75\n\t" \
    "s_sub_u32 s70, s74, s76\n\t" \
    "s_sub_u32 s71, s70, 1600\n\t" \
    "s_cmp_lt_u32 s71, 400000\n\t" \
    "s_cbranch_scc0 qk_K111_%=\n\t" \
    "v_cvt_f32_u32 v161, s69\n\t" \
    "v_cvt_f32_u32 v162, s70\n\t" \
    "v_rcp_f32 v163, v161\n\t" \
    "v_rcp_f32 v164, v162\n\t" \
    "v_mul_f32 v163, v163, v162\n\t" \
    "v_mul_f32 v164, v164, v161\n\t" \
    "v_mul_f32 v163, 0x4f800000, v163\n\t" \
    "v_mul_f32 v164, 0x4f000000, v164\n\t" \
    "v_cvt_u32_f32 v163, v163\n\t" \
    "v_cvt_u32_f32 v164, v164\n\t" \
    "v_readfirstlane_b32 s69, v163\n\t" \
    "v_readfirstlane_b32 s70, v164\n\t" \
    "s_nop 1\n\t" \
    "s_sub_u32 s69, s69, s78\n\t" \
    "s_ashr_i32 s69, s69, 2\n\t" \
    "s_add_u32 s78, s78, s69\n\t" \
    "s_sub_u32 s70, s70, s77\n\t" \
    "s_ashr_i32 s70, s70, 2\n\t" \
    "s_add_u32 s77, s77, s70\n\t" \
    "qk_K111_%=:\n\t" \
    "s_waitcnt vmcnt(0)\n\t" \
    "v_cmp_eq_u32 vcc, -1, v160\n\t" \
    "s_cbranch_vccz qk_Hok107_%=\n\t" \
    "qk_H106_%=:\n\t" \
    "global_load_dword v160, v20, s[46:47] sc1\n\t" \
    "s_waitcnt vmcnt(0)\n\t" \
    "v_cmp_eq_u32 vcc, -1, v160\n\t" \
    "s_cbranch_vccz qk_Hc108_%=\n\t" \
    "s_cmp_eq_u32 s68, 0\n\t" \
    "s_cbranch_scc1 qk_F112_%=\n\t" \
    "s_sub_u32 s68, s68, 1\n\t" \
    "s_sleep 1\n\t" \
    "s_branch qk_H106_%=\n\t" \
    "qk_F112_%=:\n\t" \
    "v_mov_b32 v161, 6095104\n\t" \
    "v_mov_b32 v162, 5\n\t" \
    "global_store_dword v161, v162, s[36:37] sc1\n\t" \
    "s_waitcnt vmcnt(0)\n\t" \
    "s_branch qk_Hc108_%=\n\t" \
    "qk_Hc108_%=:\n\t" \
    "qk_Hok107_%=:\n\t" \
    "v_subrev_u32 v160, s80, v160\n\t" \
    "v_and_b32 v20, -5, v20\n\t" \
    "s_nop 0\n\t" \
    "v_max_i32_dpp v160, v160, v160 row_ror:1 row_mask:0xf bank_mask:0xf\n\t" \
    "s_nop 1\n\t" \
    "v_max_i32_dpp v160, v160, v160 row_ror:2 row_mask:0xf bank_mask:0xf\n\t" \
    "s_nop 1\n\t" \
    "v_max_i32_dpp v160, v160, v160 row_ror:4 row_mask:0xf bank_mask:0xf\n\t" \
    "s_nop 1\n\t" \
    "v_max_i32_dpp v160, v160, v160 row_ror:8 row_mask:0xf bank_mask:0xf\n\t" \
    "s_nop 0\n\t" \
    "v_readlane_b32 s69, v160, 0\n\t" \
    "v_readlane_b32 s71, v160, 16\n\t" \
    "v_readlane_b32 s70, v160, 32\n\t" \
    "v_readlane_b32 s81, v160, 48\n\t" \
    "s_max_i32 s69, s69, s71\n\t" \
    "s_nop 0\n\t" \
    "s_max_i32 s71, s70, s81\n\t" \
    "s_max_i32 s69, s69, s71\n\t" \
    "s_add_u32 s81, s69, s80\n\t" \
    "v_mov_b32 v72, 0\n\t" \
    "v_mov_b32 v73, 0\n\t" \
    "v_mov_b32 v74, 0\n\t" \
    "v_mov_b32 v75, 0\n\t" \
    "v_mov_b32 v76, 0\n\t" \
    "v_mov_b32 v77, 0\n\t" \
    "v_mov_b32 v78, 0\n\t" \
    "v_mov_b32 v79, 0\n\t" \
    "v_mov_b32 v80, 0\n\t" \
    "v_mov_b32 v81, 0\n\t" \
    "v_mov_b32 v82, 0\n\t" \
    "v_mov_b32 v83, 0\n\t" \
    "v_mov_b32 v84, 0\n\t" \
    "v_mov_b32 v85, 0\n\t" \
    "v_mov_b32 v86, 0\n\t" \
    "v_mov_b32 v87, 0\n\t" \
    "s_cmp_lg_u32 s65, 0\n\t" \
    "s_cbranch_scc1 qk_Oc115_%=\n\t" \
    "s_add_u32 s79, s81, 240\n\t" \
    "s_sub_u32 s70, s79, s74\n\t" \
    "s_max_i32 s70, s70, 0\n\t" \
    "s_min_i32 s70, s70, 16000\n\t" \
    "s_lshl_b32 s70, s70, 1\n\t" \
    "s_mul_hi_u32 s70, s70, s77\n\t" \
    "s_add_u32 s70, s70, s73\n\t" \
    "qk_W116_%=:\n\t" \
    "s_memtime s[56:57]\n\t" \
    "s_waitcnt lgkmcnt(0)\n\t" \
    "s_sub_u32 s69, s70, s56\n\t" \
    "s_cmp_gt_i32 s69, 24\n\t" \
    "s_cbranch_scc1 qk_W116_%=\n\t" \
    "global_load_dword v160, v35, s[46:47] sc1\n\t" \
    "s_waitcnt vmcnt(0)\n\t" \
    "v_cmp_eq_u32 vcc, -1, v160\n\t" \
    "s_cbranch_vccz qk_Ook114_%=\n\t" \
    "qk_O113_%=:\n\t" \
    "global_load_dword v160, v35, s[46:47] offset:1536 sc1\n\t" \
    "s_waitcnt vmcnt(0)\n\t" \
    "v_cmp_eq_u32 vcc, -1, v160\n\t" \
    "s_cbranch_vccz qk_Ook114_%=\n\t" \
    "s_cmp_eq_u32 s68, 0\n\t" \
    "s_cbranch_scc1 qk_F117_%=\n\t" \
    "s_sub_u32 s68, s68, 1\n\t" \
    "s_sleep 3\n\t" \
    "s_branch qk_O113_%=\n\t" \
    "qk_F117_%=:\n\t" \
    "v_mov_b32 v161, 6095104\n\t" \
    "v_mov_b32 v162, 16\n\t" \
    "global_store_dword v161, v162, s[36:37] sc1\n\t" \
    "s_waitcnt vmcnt(0)\n\t" \
    "s_branch qk_Ook114_%=\n\t" \
    "qk_Ook114_%=:\n\t" \
    "v_subrev_u32 v160, s80, v160\n\t" \
    "s_nop 1\n\t" \
    "v_max_i32_dpp v160, v160, v160 row_ror:1 row_mask:0xf bank_mask:0xf\n\t" \
    "s_nop 1\n\t" \
    "v_max_i32_dpp v160, v160, v160 row_ror:2 row_mask:0xf bank_mask:0xf\n\t" \
    "s_nop 1\n\t" \
    "v_max_i32_dpp v160, v160, v160 row_ror:4 row_mask:0xf bank_mask:0xf\n\t" \
    "s_nop 1\n\t" \
    "v_max_i32_dpp v160, v160, v160 row_ror:8 row_mask:0xf bank_mask:0xf\n\t" \
    "s_nop 0\n\t" \
    "v_readlane_b32 s69, v160, 0\n\t" \
    "v_readlane_b32 s71, v160, 16\n\t" \
    "v_readlane_b32 s70, v160, 32\n\t" \
    "v_readlane_b32 s81, v160, 48\n\t" \
    "s_max_i32 s69, s69, s71\n\t" \
    "s_nop 0\n\t" \
    "s_max_i32 s71, s70, s81\n\t" \
    "s_max_i32 s69, s69, s71\n\t" \
    "s_add_u32 s81, s69, s80\n\t" \
    "qk_Oc115_%=:\n\t" \
    "v_mov_b32 v160, s81\n\t" \
    "ds_write_b32 v11, v160 offset:20480\n\t" \
    "s_add_u32 s65, s65, 1\n\t" \
    "v_add_u32 v31, 1, v31\n\t" \
    "s_add_u32 s69, s65, 0\n\t" \
    "s_bitcmp1_b32 s69, 0\n\t" \
    "s_cselect_b32 s70, s63, s62\n\t" \
    "s_bfe_u32 s69, s69, 0x20001\n\t" \
    "s_lshl_b32 s69, s69, 19\n\t" \
    "s_add_u32 s69, s69, s70\n\t" \
    "s_add_u32 s46, s36, s69\n\t" \
    "s_addc_u32 s47, s37, 0\n\t" \
    "s_waitcnt lgkmcnt(0)\n\t" \
    "s_barrier\n\t" \
    "ds_read_b32 v160, v11 offset:20480\n\t" \
    "s_waitcnt lgkmcnt(0)\n\t" \
    "v_readfirstlane_b32 s81, v160\n\t" \
    "s_nop 1\n\t" \
    "s_mul_hi_u32 s69, s85, s78\n\t" \
    "s_add_u32 s79, s81, s69\n\t" \
    "s_add_u32 s79, s79, s87\n\t" \
    "s_cmp_ge_u32 s65, 37\n\t" \
    "s_cselect_b32 s69, 28, 0\n\t" \
    "s_sub_u32 s79, s79, s69\n\t" \
    "s_add_u32 s80, s79, s88\n\t" \
    "s_branch qk_go95_%=\n\t" \
    "qk_Gbad100_%=:\n\t" \
    "s_waitcnt vmcnt(0) lgkmcnt(0)\n\t" \
    "s_nop 7\n\t" \
    "v_mov_b32 v72, 0\n\t" \
    "v_mov_b32 v73, 0\n\t" \
    "v_mov_b32 v74, 0\n\t" \
    "v_mov_b32 v75, 0\n\t" \
    "v_mov_b32 v76, 0\n\t" \
    "v_mov_b32 v77, 0\n\t" \
    "v_mov_b32 v78, 0\n\t" \
    "v_mov_b32 v79, 0\n\t" \
    "v_mov_b32 v80, 0\n\t" \
    "v_mov_b32 v81, 0\n\t" \
    "v_mov_b32 v82, 0\n\t" \
    "v_mov_b32 v83, 0\n\t" \
    "v_mov_b32 v84, 0\n\t" \
    "v_mov_b32 v85, 0\n\t" \
    "v_mov_b32 v86, 0\n\t" \
    "v_mov_b32 v87, 0\n\t" \
    "s_cmp_eq_u32 s68, 0\n\t" \
    "s_cbranch_scc1 qk_F118_%=\n\t" \
    "s_sub_u32 s68, s68, 1\n\t" \
    "s_branch qk_G99_%=\n\t" \
    "qk_F118_%=:\n\t" \
    "v_mov_b32 v160, 6095104\n\t" \
    "v_mov_b32 v161, 1\n\t" \
    "global_store_dword v160, v161, s[36:37] sc1\n\t" \
    "s_waitcnt vmcnt(0)\n\t" \
    "s_branch qk_Gc102_%=\n\t" \
    "qk_Gbad2101_%=:\n\t" \
    "global_load_dwordx4 v[44:47], v2, s[46:47] sc1\n\t" \
    "s_waitcnt vmcnt(0)\n\t" \
    "v_max3_u32 v160, v44, v45, v46\n\t" \
    "v_max_u32 v160, v160, v47\n\t" \
    "v_cmp_eq_u32 vcc, -1, v160\n\t" \
    "s_cbranch_vccz qk_Gre2104_%=\n\t" \
    "s_cmp_eq_u32 s68, 0\n\t" \
    "s_cbranch_scc1 qk_F119_%=\n\t" \
    "s_sub_u32 s68, s68, 1\n\t" \
    "s_sleep 1\n\t" \
    "s_branch qk_Gbad2101_%=\n\t" \
    "qk_F119_%=:\n\t" \
    "v_mov_b32 v160, 6095104\n\t" \
    "v_mov_b32 v161, 2\n\t" \
    "global_store_dword v160, v161, s[36:37] sc1\n\t" \
    "s_waitcnt vmcnt(0)\n\t" \
    "s_branch qk_Gre2104_%=\n\t" \
    "qk_Gre2104_%=:\n\t" \
    "v_mov_b32 v72, 0\n\t" \
    "v_mov_b32 v73, 0\n\t" \
    "v_mov_b32 v74, 0\n\t" \
    "v_mov_b32 v75, 0\n\t" \
    "v_mov_b32 v76, 0\n\t" \
    "v_mov_b32 v77, 0\n\t" \
    "v_mov_b32 v78, 0\n\t" \
    "v_mov_b32 v79, 0\n\t" \
    "v_mov_b32 v80, 0\n\t" \
    "v_mov_b32 v81, 0\n\t" \
    "v_mov_b32 v82, 0\n\t" \
    "v_mov_b32 v83, 0\n\t" \
    "v_mov_b32 v84, 0\n\t" \
    "v_mov_b32 v85, 0\n\t" \
    "v_mov_b32 v86, 0\n\t" \
    "v_mov_b32 v87, 0\n\t" \
    "v_smfmac_f32_16x16x64_bf16 v[72:75], v[36:39], a[48:55], v10\n\t" \
    "v_smfmac_f32_16x16x64_bf16 v[76:79], v[36:39], a[120:127], v10\n\t" \
    "v_smfmac_f32_16x16x64_bf16 v[80:83], v[36:39], a[192:199], v10\n\t" \
    "v_smfmac_f32_16x16x64_bf16 v[84:87], v[36:39], v[232:239], v10\n\t" \
    "v_smfmac_f32_16x16x64_bf16 v[72:75], v[40:43], a[56:63], v10\n\t" \
    "v_smfmac_f32_16x16x64_bf16 v[76:79], v[40:43], a[128:135], v10\n\t" \
    "v_smfmac_f32_16x16x64_bf16 v[80:83], v[40:43], a[200:207], v10\n\t" \
    "v_smfmac_f32_16x16x64_bf16 v[84:87], v[40:43], v[240:247], v10\n\t" \
    "v_smfmac_f32_16x16x64_bf16 v[72:75], v[44:47], a[64:71], v10\n\t" \
    "v_smfmac_f32_16x16x64_bf16 v[76:79], v[44:47], a[136:143], v10\n\t" \
    "v_smfmac_f32_16x16x64_bf16 v[80:83], v[44:47], a[208:215], v10\n\t" \
    "v_smfmac_f32_16x16x64_bf16 v[84:87], v[44:47], v[248:255], v10\n\t" \
    "s_waitcnt lgkmcnt(0)\n\t" \
    "s_memtime s[56:57]\n\t" \
    "s_waitcnt lgkmcnt(0)\n\t" \
    "s_sub_u32 s69, s56, s73\n\t" \
    "s_mul_hi_u32 s69, s69, s78\n\t" \
    "s_cmp_gt_i32 s69, s35\n\t" \
    "s_branch qk_Gc2103_%=\n\t" \
    "qk_last90_%=:\n\t" \
    "s_endpgm\n\t" \
    "vv_begin_%=:\n\t" \
    "v_mbcnt_lo_u32_b32 v223, -1, 0\n\t" \
    "v_mbcnt_hi_u32_b32 v223, -1, v223\n\t" \
    "s_mov_b32 s68, 262144\n\t" \
    "s_mov_b32 s72, 0\n\t" \
    "s_mov_b32 s83, -1\n\t" \
    "s_mov_b32 s65, 0\n\t" \
    "v_and_b32 v180, 3, v223\n\t" \
    "v_lshlrev_b32 v202, 3, v180\n\t" \
    "v_lshrrev_b32 v181, 5, v223\n\t" \
    "v_lshl_add_u32 v202, v181, 12, v202\n\t" \
    "v_bfe_u32 v182, v223, 2, 2\n\t" \
    "s_mov_b32 s70, 288\n\t" \
    "v_mad_u32_u24 v202, v182, s70, v202\n\t" \
    "v_bfe_u32 v183, v223, 4, 1\n\t" \
    "s_mov_b32 s70, 2176\n\t" \
    "v_mad_u32_u24 v202, v183, s70, v202\n\t" \
    "s_lshl_b32 s69, s66, 6\n\t" \
    "v_xor_b32 v202, s69, v202\n\t" \
    "v_xor_b32 v203, 32, v202\n\t" \
    "s_mul_i32 s69, s66, 9216\n\t" \
    "v_and_b32 v184, 1, v223\n\t" \
    "v_lshl_add_u32 v201, v184, 4, s69\n\t" \
    "v_lshrrev_b32 v185, 3, v223\n\t" \
    "s_mov_b32 s70, 1152\n\t" \
    "v_mad_u32_u24 v201, v185, s70, v201\n\t" \
    "v_bfe_u32 v186, v223, 1, 2\n\t" \
    "v_lshlrev_b32 v204, 5, v186\n\t" \
    "v_lshrrev_b32 v187, 4, v223\n\t" \
    "v_lshlrev_b32 v205, 5, v187\n\t" \
    "v_xor_b32 v204, v204, v205\n\t" \
    "v_add_u32 v201, v201, v204\n\t" \
    "s_mul_i32 s96, s66, 9216\n\t" \
    "v_lshlrev_b32 v207, 3, v180\n\t" \
    "v_bfe_u32 v188, v223, 2, 1\n\t" \
    "v_lshl_add_u32 v207, v188, 7, v207\n\t" \
    "v_bfe_u32 v189, v223, 3, 2\n\t" \
    "s_mov_b32 s70, 288\n\t" \
    "v_mad_u32_u24 v207, v189, s70, v207\n\t" \
    "s_mov_b32 s70, 9216\n\t" \
    "v_mad_u32_u24 v207, v181, s70, v207\n\t" \
    "s_mul_i32 s69, s66, 9\n\t" \
    "s_add_u32 s69, s69, 0\n\t" \
    "s_and_b32 s70, s69, 3\n\t" \
    "s_lshl_b32 s70, s70, 5\n\t" \
    "s_lshr_b32 s69, s69, 2\n\t" \
    "s_lshl_b32 s69, s69, 10\n\t" \
    "v_xor_b32 v204, s70, v207\n\t" \
    "v_add_u32 v204, s69, v204\n\t" \
    "s_mul_i32 s69, s66, 9\n\t" \
    "s_add_u32 s69, s69, 1\n\t" \
    "s_and_b32 s70, s69, 3\n\t" \
    "s_lshl_b32 s70, s70, 5\n\t" \
    "s_lshr_b32 s69, s69, 2\n\t" \
    "s_lshl_b32 s69, s69, 10\n\t" \
    "v_xor_b32 v205, s70, v207\n\t" \
    "v_add_u32 v205, s69, v205\n\t" \
    "s_mul_i32 s69, s66, 9\n\t" \
    "s_add_u32 s69, s69, 2\n\t" \
    "s_and_b32 s70, s69, 3\n\t" \
    "s_lshl_b32 s70, s70, 5\n\t" \
    "s_lshr_b32 s69, s69, 2\n\t" \
    "s_lshl_b32 s69, s69, 10\n\t" \
    "v_xor_b32 v206, s70, v207\n\t" \
    "v_add_u32 v206, s69, v206\n\t" \
    "s_mul_i32 s69, s66, 9\n\t" \
    "s_add_u32 s69, s69, 3\n\t" \
    "s_and_b32 s70, s69, 3\n\t" \
    "s_lshl_b32 s70, s70, 5\n\t" \
    "s_lshr_b32 s69, s69, 2\n\t" \
    "s_lshl_b32 s69, s69, 10\n\t" \
    "v_xor_b32 v207, s70, v207\n\t" \
    "v_add_u32 v207, s69, v207\n\t" \
    "s_waitcnt vmcnt(12) lgkmcnt(0)\n\t" \
    "s_barrier\n\t" \
    "s_add_u32 m0, s82, 65536\n\t" \
    "s_nop 0\n\t" \
    "global_load_lds_dwordx4 v200, s[42:43]\n\t" \
    "v_add_u32 v200, 131072, v200\n\t" \
    "s_add_u32 m0, s82, 69632\n\t" \
    "s_nop 0\n\t" \
    "global_load_lds_dwordx4 v200, s[42:43]\n\t" \
    "v_add_u32 v200, 131072, v200\n\t" \
    "s_add_u32 m0, s82, 73728\n\t" \
    "s_nop 0\n\t" \
    "global_load_lds_dwordx4 v200, s[42:43]\n\t" \
    "v_add_u32 v200, 131072, v200\n\t" \
    "s_add_u32 m0, s82, 77824\n\t" \
    "s_nop 0\n\t" \
    "global_load_lds_dwordx4 v200, s[42:43]\n\t" \
    "v_add_u32 v200, 131072, v200\n\t" \
    "ds_read_b64_tr_b16 a[0:1], v202 offset:36864\n\t" \
    "ds_read_b64_tr_b16 a[2:3], v202 offset:37888\n\t" \
    "ds_read_b64_tr_b16 a[4:5], v202 offset:45056\n\t" \
    "ds_read_b64_tr_b16 a[6:7], v202 offset:46080\n\t" \
    "ds_read_b64_tr_b16 a[72:73], v203 offset:36864\n\t" \
    "ds_read_b64_tr_b16 a[74:75], v203 offset:37888\n\t" \
    "ds_read_b64_tr_b16 a[76:77], v203 offset:45056\n\t" \
    "ds_read_b64_tr_b16 a[78:79], v203 offset:46080\n\t" \
    "v_lshlrev_b32 v0, 4, v223\n\t" \
    "s_mul_i32 s69, s66, 2048\n\t" \
    "s_bfe_u32 s71, s67, 0x20003\n\t" \
    "s_mul_i32 s71, s71, 65536\n\t" \
    "s_add_u32 s69, s69, s71\n\t" \
    "s_add_u32 s69, s69, 32768\n\t" \
    "v_lshl_add_u32 v1, v223, 4, s69\n\t" \
    "s_bfe_u32 s71, s67, 0x20003\n\t" \
    "s_mul_i32 s69, s71, 65536\n\t" \
    "s_add_u32 s69, s69, 53248\n\t" \
    "v_lshl_add_u32 v2, v223, 4, s69\n\t" \
    "s_mov_b32 s69, 17476\n\t" \
    "v_bfe_u32 v190, v223, 1, 1\n\t" \
    "v_mov_b32 v10, s69\n\t" \
    "s_mov_b32 s70, 43690\n\t" \
    "v_mad_u32_u24 v10, v190, s70, v10\n\t" \
    "v_mov_b32 v11, 0\n\t" \
    "s_waitcnt vmcnt(12) lgkmcnt(0)\n\t" \
    "s_barrier\n\t" \
    "s_add_u32 m0, s82, 81920\n\t" \
    "s_nop 0\n\t" \
    "global_load_lds_dwordx4 v200, s[42:43]\n\t" \
    "v_add_u32 v200, 131072, v200\n\t" \
    "s_add_u32 m0, s82, 86016\n\t" \
    "s_nop 0\n\t" \
    "global_load_lds_dwordx4 v200, s[42:43]\n\t" \
    "v_add_u32 v200, 131072, v200\n\t" \
    "s_add_u32 m0, s82, 90112\n\t" \
    "s_nop 0\n\t" \
    "global_load_lds_dwordx4 v200, s[42:43]\n\t" \
    "v_add_u32 v200, 131072, v200\n\t" \
    "s_add_u32 m0, s82, 94208\n\t" \
    "s_nop 0\n\t" \
    "global_load_lds_dwordx4 v200, s[42:43]\n\t" \
    "v_add_u32 v200, 131072, v200\n\t" \
    "ds_read_b64_tr_b16 a[8:9], v202 offset:53248\n\t" \
    "ds_read_b64_tr_b16 a[10:11], v202 offset:54272\n\t" \
    "ds_read_b64_tr_b16 a[12:13], v202 offset:61440\n\t" \
    "ds_read_b64_tr_b16 a[14:15], v202 offset:62464\n\t" \
    "ds_read_b64_tr_b16 a[80:81], v203 offset:53248\n\t" \
    "ds_read_b64_tr_b16 a[82:83], v203 offset:54272\n\t" \
    "ds_read_b64_tr_b16 a[84:85], v203 offset:61440\n\t" \
    "ds_read_b64_tr_b16 a[86:87], v203 offset:62464\n\t" \
    "s_mul_i32 s69, s66, 512\n\t" \
    "s_add_u32 s69, s69, 16384\n\t" \
    "v_and_b32 v191, 13, v223\n\t" \
    "v_lshl_add_u32 v12, v191, 5, s69\n\t" \
    "v_lshl_add_u32 v12, v190, 2, v12\n\t" \
    "v_lshl_add_u32 v12, v187, 3, v12\n\t" \
    "s_mov_b32 s69, 16384\n\t" \
    "v_and_b32 v192, 15, v223\n\t" \
    "v_lshl_add_u32 v13, v192, 3, s69\n\t" \
    "v_lshl_add_u32 v13, v187, 8, v13\n\t" \
    "s_bfe_u32 s71, s67, 0x20003\n\t" \
    "s_mul_i32 s69, s71, 65536\n\t" \
    "s_add_u32 s69, s69, 266240\n\t" \
    "v_and_b32 v193, 31, v223\n\t" \
    "v_lshl_add_u32 v20, v193, 5, s69\n\t" \
    "v_lshl_add_u32 v20, v181, 2, v20\n\t" \
    "s_mul_i32 s69, s67, 32\n\t" \
    "s_add_u32 s69, s69, 262144\n\t" \
    "v_lshl_add_u32 v21, v180, 16, s69\n\t" \
    "v_bfe_u32 v194, v223, 2, 3\n\t" \
    "v_lshlrev_b32 v199, 10, v194\n\t" \
    "v_min_u32 v199, 4096, v199\n\t" \
    "v_add_u32 v21, v199, v21\n\t" \
    "s_bfe_u32 s71, s67, 0x20003\n\t" \
    "s_mul_i32 s69, s71, 65536\n\t" \
    "s_add_u32 s69, s69, 267264\n\t" \
    "v_min_u32 v35, 35, v223\n\t" \
    "v_lshl_add_u32 v35, v35, 5, s69\n\t" \
    "v_lshlrev_b32 v22, 2, v223\n\t" \
    "s_waitcnt vmcnt(12) lgkmcnt(0)\n\t" \
    "s_barrier\n\t" \
    "s_add_u32 m0, s82, 98304\n\t" \
    "s_nop 0\n\t" \
    "global_load_lds_dwordx4 v200, s[42:43]\n\t" \
    "v_add_u32 v200, 131072, v200\n\t" \
    "s_add_u32 m0, s82, 102400\n\t" \
    "s_nop 0\n\t" \
    "global_load_lds_dwordx4 v200, s[42:43]\n\t" \
    "v_add_u32 v200, 131072, v200\n\t" \
    "s_add_u32 m0, s82, 106496\n\t" \
    "s_nop 0\n\t" \
    "global_load_lds_dwordx4 v200, s[42:43]\n\t" \
    "v_add_u32 v200, 131072, v200\n\t" \
    "s_add_u32 m0, s82, 110592\n\t" \
    "s_nop 0\n\t" \
    "global_load_lds_dwordx4 v200, s[42:43]\n\t" \
    "v_add_u32 v200, 131072, v200\n\t" \
    "v_add_u32 v202, 69632, v202\n\t" \
    "v_add_u32 v203, 69632, v203\n\t" \
    "ds_read_b64_tr_b16 a[16:17], v202 offset:0\n\t" \
    "ds_read_b64_tr_b16 a[18:19], v202 offset:1024\n\t" \
    "ds_read_b64_tr_b16 a[20:21], v202 offset:8192\n\t" \
    "ds_read_b64_tr_b16 a[22:23], v202 offset:9216\n\t" \
    "ds_read_b64_tr_b16 a[88:89], v203 offset:0\n\t" \
    "ds_read_b64_tr_b16 a[90:91], v203 offset:1024\n\t" \
    "ds_read_b64_tr_b16 a[92:93], v203 offset:8192\n\t" \
    "ds_read_b64_tr_b16 a[94:95], v203 offset:9216\n\t" \
    "s_mul_i32 s69, s66, 147456\n\t" \
    "s_mul_i32 s71, s67, 256\n\t" \
    "s_add_u32 s69, s69, s71\n\t" \
    "v_lshl_add_u32 v14, v192, 4, s69\n\t" \
    "v_lshl_add_u32 v14, v187, 14, v14\n\t" \
    "s_mul_i32 s69, s66, 147456\n\t" \
    "s_mul_i32 s71, s67, 256\n\t" \
    "s_add_u32 s69, s69, s71\n\t" \
    "s_add_u32 s69, s69, 65536\n\t" \
    "v_lshl_add_u32 v15, v192, 4, s69\n\t" \
    "v_lshl_add_u32 v15, v187, 14, v15\n\t" \
    "s_mul_i32 s69, s66, 147456\n\t" \
    "s_mul_i32 s71, s67, 256\n\t" \
    "s_add_u32 s69, s69, s71\n\t" \
    "s_add_u32 s69, s69, 131072\n\t" \
    "v_lshl_add_u32 v16, v192, 4, s69\n\t" \
    "v_lshl_add_u32 v16, v187, 2, v16\n\t" \
    "s_mul_i32 s97, s67, 171\n\t" \
    "s_lshr_b32 s97, s97, 9\n\t" \
    "s_mul_i32 s97, s97, 131072\n\t" \
    "s_mul_i32 s71, s66, 2304\n\t" \
    "s_add_u32 s69, s97, s71\n\t" \
    "s_mul_i32 s71, s67, 655360\n\t" \
    "s_add_u32 s69, s69, s71\n\t" \
    "v_lshl_add_u32 v17, v223, 4, s69\n\t" \
    "s_mul_i32 s71, s66, 2304\n\t" \
    "s_add_u32 s69, s97, s71\n\t" \
    "s_mul_i32 s71, s67, 655360\n\t" \
    "s_add_u32 s69, s69, s71\n\t" \
    "s_add_u32 s69, s69, 2048\n\t" \
    "v_lshl_add_u32 v18, v192, 4, s69\n\t" \
    "v_lshl_add_u32 v18, v187, 2, v18\n\t" \
    "s_waitcnt vmcnt(12) lgkmcnt(0)\n\t" \
    "s_barrier\n\t" \
    "s_add_u32 m0, s82, 0\n\t" \
    "s_nop 0\n\t" \
    "global_load_lds_dwordx4 v200, s[42:43]\n\t" \
    "v_add_u32 v200, 131072, v200\n\t" \
    "s_add_u32 m0, s82, 4096\n\t" \
    "s_nop 0\n\t" \
    "global_load_lds_dwordx4 v200, s[42:43]\n\t" \
    "v_add_u32 v200, 131072, v200\n\t" \
    "s_add_u32 m0, s82, 8192\n\t" \
    "s_nop 0\n\t" \
    "global_load_lds_dwordx4 v200, s[42:43]\n\t" \
    "v_add_u32 v200, 131072, v200\n\t" \
    "s_add_u32 m0, s82, 12288\n\t" \
    "s_nop 0\n\t" \
    "global_load_lds_dwordx4 v200, s[42:43]\n\t" \
    "v_add_u32 v200, 131072, v200\n\t" \
    "ds_read_b64_tr_b16 a[24:25], v202 offset:16384\n\t" \
    "ds_read_b64_tr_b16 a[26:27], v202 offset:17408\n\t" \
    "ds_read_b64_tr_b16 a[28:29], v202 offset:24576\n\t" \
    "ds_read_b64_tr_b16 a[30:31], v202 offset:25600\n\t" \
    "ds_read_b64_tr_b16 a[96:97], v203 offset:16384\n\t" \
    "ds_read_b64_tr_b16 a[98:99], v203 offset:17408\n\t" \
    "ds_read_b64_tr_b16 a[100:101], v203 offset:24576\n\t" \
    "ds_read_b64_tr_b16 a[102:103], v203 offset:25600\n\t" \
    "s_mov_b32 s77, 3019898880\n\t" \
    "s_mov_b32 s78, 3054198966\n\t" \
    "s_mov_b32 s71, 3\n\t" \
    "vv_E1_%=:\n\t" \
    "s_memtime s[54:55]\n\t" \
    "s_nop 0\n\t" \
    "s_memrealtime s[4:5]\n\t" \
    "s_nop 0\n\t" \
    "s_memrealtime s[6:7]\n\t" \
    "s_nop 0\n\t" \
    "s_memrealtime s[8:9]\n\t" \
    "s_nop 0\n\t" \
    "s_memrealtime s[10:11]\n\t" \
    "s_nop 0\n\t" \
    "s_memrealtime s[12:13]\n\t" \
    "s_nop 0\n\t" \
    "s_memrealtime s[14:15]\n\t" \
    "s_nop 0\n\t" \
    "s_memrealtime s[16:17]\n\t" \
    "s_nop 0\n\t" \
    "s_memrealtime s[18:19]\n\t" \
    "s_nop 0\n\t" \
    "s_memrealtime s[20:21]\n\t" \
    "s_nop 0\n\t" \
    "s_memrealtime s[22:23]\n\t" \
    "s_nop 0\n\t" \
    "s_memrealtime s[24:25]\n\t" \
    "s_nop 0\n\t" \
    "s_memrealtime s[26:27]\n\t" \
    "s_nop 0\n\t" \
    "s_memrealtime s[28:29]\n\t" \
    "s_nop 0\n\t" \
    "s_memtime s[56:57]\n\t" \
    "s_mov_b32 s70, 1\n\t" \
    "s_sub_u32 s71, s71, 1\n\t" \
    "s_cselect_b32 s69, 0, -1\n\t" \
    "s_waitcnt lgkmcnt(0)\n\t" \
    "s_sub_u32 s54, s56, s54\n\t" \
    "s_and_b32 s54, s54, s69\n\t" \
    "s_cmp_gt_u32 s54, 124\n\t" \
    "s_cbranch_scc1 vv_E1_%=\n\t" \
    "s_cmp_eq_u32 s4, s28\n\t" \
    "s_addc_u32 s70, s70, 0\n\t" \
    "s_cmp_eq_u32 s6, s28\n\t" \
    "s_addc_u32 s70, s70, 0\n\t" \
    "s_cmp_eq_u32 s8, s28\n\t" \
    "s_addc_u32 s70, s70, 0\n\t" \
    "s_cmp_eq_u32 s10, s28\n\t" \
    "s_addc_u32 s70, s70, 0\n\t" \
    "s_cmp_eq_u32 s12, s28\n\t" \
    "s_addc_u32 s70, s70, 0\n\t" \
    "s_cmp_eq_u32 s14, s28\n\t" \
    "s_addc_u32 s70, s70, 0\n\t" \
    "s_cmp_eq_u32 s16, s28\n\t" \
    "s_addc_u32 s70, s70, 0\n\t" \
    "s_cmp_eq_u32 s18, s28\n\t" \
    "s_addc_u32 s70, s70, 0\n\t" \
    "s_cmp_eq_u32 s20, s28\n\t" \
    "s_addc_u32 s70, s70, 0\n\t" \
    "s_cmp_eq_u32 s22, s28\n\t" \
    "s_addc_u32 s70, s70, 0\n\t" \
    "s_cmp_eq_u32 s24, s28\n\t" \
    "s_addc_u32 s70, s70, 0\n\t" \
    "s_cmp_eq_u32 s26, s28\n\t" \
    "s_addc_u32 s70, s70, 0\n\t" \
    "s_mov_b32 s75, s73\n\t" \
    "s_mov_b32 s76, s74\n\t" \
    "s_lshl_b32 s70, s70, 3\n\t" \
    "s_sub_u32 s73, s56, s70\n\t" \
    "s_lshl_b32 s74, s28, 4\n\t" \
    "s_waitcnt vmcnt(12) lgkmcnt(0)\n\t" \
    "s_barrier\n\t" \
    "s_add_u32 m0, s82, 16384\n\t" \
    "s_nop 0\n\t" \
    "global_load_lds_dwordx4 v200, s[42:43]\n\t" \
    "v_add_u32 v200, 131072, v200\n\t" \
    "s_add_u32 m0, s82, 20480\n\t" \
    "s_nop 0\n\t" \
    "global_load_lds_dwordx4 v200, s[42:43]\n\t" \
    "v_add_u32 v200, 131072, v200\n\t" \
    "s_add_u32 m0, s82, 24576\n\t" \
    "s_nop 0\n\t" \
    "global_load_lds_dwordx4 v200, s[42:43]\n\t" \
    "v_add_u32 v200, 131072, v200\n\t" \
    "s_add_u32 m0, s82, 28672\n\t" \
    "s_nop 0\n\t" \
    "global_load_lds_dwordx4 v200, s[42:43]\n\t" \
    "v_add_u32 v200, 131072, v200\n\t" \
    "ds_read_b64_tr_b16 a[32:33], v202 offset:32768\n\t" \
    "ds_read_b64_tr_b16 a[34:35], v202 offset:33792\n\t" \
    "ds_read_b64_tr_b16 a[36:37], v202 offset:40960\n\t" \
    "ds_read_b64_tr_b16 a[38:39], v202 offset:41984\n\t" \
    "ds_read_b64_tr_b16 a[104:105], v203 offset:32768\n\t" \
    "ds_read_b64_tr_b16 a[106:107], v203 offset:33792\n\t" \
    "ds_read_b64_tr_b16 a[108:109], v203 offset:40960\n\t" \
    "ds_read_b64_tr_b16 a[110:111], v203 offset:41984\n\t" \
    "s_waitcnt vmcnt(12) lgkmcnt(0)\n\t" \
    "s_barrier\n\t" \
    "s_add_u32 m0, s96, 0\n\t" \
    "s_nop 0\n\t" \
    "global_load_lds_dwordx4 v201, s[44:45] offset:0\n\t" \
    "s_add_u32 m0, s96, 896\n\t" \
    "s_nop 0\n\t" \
    "global_load_lds_dwordx4 v201, s[44:45] offset:128\n\t" \
    "s_add_u32 m0, s96, 1792\n\t" \
    "s_nop 0\n\t" \
    "global_load_lds_dwordx4 v201, s[44:45] offset:256\n\t" \
    "s_add_u32 m0, s96, 2688\n\t" \
    "s_nop 0\n\t" \
    "global_load_lds_dwordx4 v201, s[44:45] offset:384\n\t" \
    "s_add_u32 m0, s96, 3584\n\t" \
    "s_nop 0\n\t" \
    "global_load_lds_dwordx4 v201, s[44:45] offset:512\n\t" \
    "s_add_u32 m0, s96, 4480\n\t" \
    "s_nop 0\n\t" \
    "global_load_lds_dwordx4 v201, s[44:45] offset:640\n\t" \
    "s_add_u32 m0, s96, 5376\n\t" \
    "s_nop 0\n\t" \
    "global_load_lds_dwordx4 v201, s[44:45] offset:768\n\t" \
    "s_add_u32 m0, s96, 6272\n\t" \
    "s_nop 0\n\t" \
    "global_load_lds_dwordx4 v201, s[44:45] offset:896\n\t" \
    "s_add_u32 m0, s96, 7168\n\t" \
    "s_nop 0\n\t" \
    "global_load_lds_dwordx4 v201, s[44:45] offset:1024\n\t" \
    "v_add_u32 v201, 36864, v201\n\t" \
    "ds_read_b64_tr_b16 a[40:41], v202 offset:49152\n\t" \
    "ds_read_b64_tr_b16 a[42:43], v202 offset:50176\n\t" \
    "ds_read_b64_tr_b16 a[44:45], v202 offset:57344\n\t" \
    "ds_read_b64_tr_b16 a[46:47], v202 offset:58368\n\t" \
    "ds_read_b64_tr_b16 a[112:113], v203 offset:49152\n\t" \
    "ds_read_b64_tr_b16 a[114:115], v203 offset:50176\n\t" \
    "ds_read_b64_tr_b16 a[116:117], v203 offset:57344\n\t" \
    "ds_read_b64_tr_b16 a[118:119], v203 offset:58368\n\t" \
    "s_waitcnt vmcnt(17) lgkmcnt(0)\n\t" \
    "s_barrier\n\t" \
    "v_add_u32 v202, 65536, v202\n\t" \
    "v_add_u32 v203, 65536, v203\n\t" \
    "ds_read_b64_tr_b16 a[48:49], v202 offset:0\n\t" \
    "ds_read_b64_tr_b16 a[50:51], v202 offset:1024\n\t" \
    "ds_read_b64_tr_b16 a[52:53], v202 offset:8192\n\t" \
    "ds_read_b64_tr_b16 a[54:55], v202 offset:9216\n\t" \
    "ds_read_b64_tr_b16 a[120:121], v203 offset:0\n\t" \
    "ds_read_b64_tr_b16 a[122:123], v203 offset:1024\n\t" \
    "ds_read_b64_tr_b16 a[124:125], v203 offset:8192\n\t" \
    "ds_read_b64_tr_b16 a[126:127], v203 offset:9216\n\t" \
    "s_waitcnt vmcnt(13) lgkmcnt(0)\n\t" \
    "s_barrier\n\t" \
    "s_add_u32 m0, s96, 73728\n\t" \
    "s_nop 0\n\t" \
    "global_load_lds_dwordx4 v201, s[44:45] offset:0\n\t" \
    "s_add_u32 m0, s96, 74624\n\t" \
    "s_nop 0\n\t" \
    "global_load_lds_dwordx4 v201, s[44:45] offset:128\n\t" \
    "s_add_u32 m0, s96, 75520\n\t" \
    "s_nop 0\n\t" \
    "global_load_lds_dwordx4 v201, s[44:45] offset:256\n\t" \
    "s_add_u32 m0, s96, 76416\n\t" \
    "s_nop 0\n\t" \
    "global_load_lds_dwordx4 v201, s[44:45] offset:384\n\t" \
    "s_add_u32 m0, s96, 77312\n\t" \
    "s_nop 0\n\t" \
    "global_load_lds_dwordx4 v201, s[44:45] offset:512\n\t" \
    "s_add_u32 m0, s96, 78208\n\t" \
    "s_nop 0\n\t" \
    "global_load_lds_dwordx4 v201, s[44:45] offset:640\n\t" \
    "s_add_u32 m0, s96, 79104\n\t" \
    "s_nop 0\n\t" \
    "global_load_lds_dwordx4 v201, s[44:45] offset:768\n\t" \
    "s_add_u32 m0, s96, 80000\n\t" \
    "s_nop 0\n\t" \
    "global_load_lds_dwordx4 v201, s[44:45] offset:896\n\t" \
    "s_add_u32 m0, s96, 80896\n\t" \
    "s_nop 0\n\t" \
    "global_load_lds_dwordx4 v201, s[44:45] offset:1024\n\t" \
    "v_add_u32 v201, 36864, v201\n\t" \
    "v_add_u32 v202, -98304, v202\n\t" \
    "v_add_u32 v203, -98304, v203\n\t" \
    "ds_read_b64_tr_b16 a[56:57], v202 offset:0\n\t" \
    "ds_read_b64_tr_b16 a[58:59], v202 offset:1024\n\t" \
    "ds_read_b64_tr_b16 a[60:61], v202 offset:8192\n\t" \
    "ds_read_b64_tr_b16 a[62:63], v202 offset:9216\n\t" \
    "ds_read_b64_tr_b16 a[128:129], v203 offset:0\n\t" \
    "ds_read_b64_tr_b16 a[130:131], v203 offset:1024\n\t" \
    "ds_read_b64_tr_b16 a[132:133], v203 offset:8192\n\t" \
    "ds_read_b64_tr_b16 a[134:135], v203 offset:9216\n\t" \
    "s_waitcnt vmcnt(18) lgkmcnt(0)\n\t" \
    "s_barrier\n\t" \
    "s_add_u32 m0, s96, 110592\n\t" \
    "s_nop 0\n\t" \
    "global_load_lds_dwordx4 v201, s[44:45] offset:0\n\t" \
    "s_add_u32 m0, s96, 111488\n\t" \
    "s_nop 0\n\t" \
    "global_load_lds_dwordx4 v201, s[44:45] offset:128\n\t" \
    "s_add_u32 m0, s96, 112384\n\t" \
    "s_nop 0\n\t" \
    "global_load_lds_dwordx4 v201, s[44:45] offset:256\n\t" \
    "s_add_u32 m0, s96, 113280\n\t" \
    "s_nop 0\n\t" \
    "global_load_lds_dwordx4 v201, s[44:45] offset:384\n\t" \
    "s_add_u32 m0, s96, 114176\n\t" \
    "s_nop 0\n\t" \
    "global_load_lds_dwordx4 v201, s[44:45] offset:512\n\t" \
    "s_add_u32 m0, s96, 115072\n\t" \
    "s_nop 0\n\t" \
    "global_load_lds_dwordx4 v201, s[44:45] offset:640\n\t" \
    "s_add_u32 m0, s96, 115968\n\t" \
    "s_nop 0\n\t" \
    "global_load_lds_dwordx4 v201, s[44:45] offset:768\n\t" \
    "s_add_u32 m0, s96, 116864\n\t" \
    "s_nop 0\n\t" \
    "global_load_lds_dwordx4 v201, s[44:45] offset:896\n\t" \
    "s_add_u32 m0, s96, 117760\n\t" \
    "s_nop 0\n\t" \
    "global_load_lds_dwordx4 v201, s[44:45] offset:1024\n\t" \
    "v_add_u32 v201, 36864, v201\n\t" \
    "ds_read_b64_tr_b16 a[64:65], v202 offset:16384\n\t" \
    "ds_read_b64_tr_b16 a[66:67], v202 offset:17408\n\t" \
    "ds_read_b64_tr_b16 a[68:69], v202 offset:24576\n\t" \
    "ds_read_b64_tr_b16 a[70:71], v202 offset:25600\n\t" \
    "ds_read_b64_tr_b16 a[136:137], v203 offset:16384\n\t" \
    "ds_read_b64_tr_b16 a[138:139], v203 offset:17408\n\t" \
    "ds_read_b64_tr_b16 a[140:141], v203 offset:24576\n\t" \
    "ds_read_b64_tr_b16 a[142:143], v203 offset:25600\n\t" \
    "s_waitcnt vmcnt(18) lgkmcnt(0)\n\t" \
    "s_barrier\n\t" \
    "s_add_u32 m0, s96, 36864\n\t" \
    "s_nop 0\n\t" \
    "global_load_lds_dwordx4 v201, s[44:45] offset:0\n\t" \
    "s_add_u32 m0, s96, 37760\n\t" \
    "s_nop 0\n\t" \
    "global_load_lds_dwordx4 v201, s[44:45] offset:128\n\t" \
    "s_add_u32 m0, s96, 38656\n\t" \
    "s_nop 0\n\t" \
    "global_load_lds_dwordx4 v201, s[44:45] offset:256\n\t" \
    "s_add_u32 m0, s96, 39552\n\t" \
    "s_nop 0\n\t" \
    "global_load_lds_dwordx4 v201, s[44:45] offset:384\n\t" \
    "s_add_u32 m0, s96, 40448\n\t" \
    "s_nop 0\n\t" \
    "global_load_lds_dwordx4 v201, s[44:45] offset:512\n\t" \
    "s_add_u32 m0, s96, 41344\n\t" \
    "s_nop 0\n\t" \
    "global_load_lds_dwordx4 v201, s[44:45] offset:640\n\t" \
    "s_add_u32 m0, s96, 42240\n\t" \
    "s_nop 0\n\t" \
    "global_load_lds_dwordx4 v201, s[44:45] offset:768\n\t" \
    "s_add_u32 m0, s96, 43136\n\t" \
    "s_nop 0\n\t" \
    "global_load_lds_dwordx4 v201, s[44:45] offset:896\n\t" \
    "s_add_u32 m0, s96, 44032\n\t" \
    "s_nop 0\n\t" \
    "global_load_lds_dwordx4 v201, s[44:45] offset:1024\n\t" \
    "v_add_u32 v201, 36864, v201\n\t" \
    "ds_read_b64_tr_b16 a[144:145], v204 offset:0\n\t" \
    "ds_read_b64_tr_b16 a[146:147], v204 offset:18432\n\t" \
    "ds_read_b64_tr_b16 a[160:161], v205 offset:0\n\t" \
    "ds_read_b64_tr_b16 a[162:163], v205 offset:18432\n\t" \
    "ds_read_b64_tr_b16 a[176:177], v206 offset:0\n\t" \
    "ds_read_b64_tr_b16 a[178:179], v206 offset:18432\n\t" \
    "s_waitcnt lgkmcnt(6)\n\t" \
    "ds_read_b64_tr_b16 a[192:193], v207 offset:0\n\t" \
    "ds_read_b64_tr_b16 a[194:195], v207 offset:18432\n\t" \
    "ds_read_b64_tr_b16 a[208:209], v204 offset:1024\n\t" \
    "ds_read_b64_tr_b16 a[210:211], v204 offset:19456\n\t" \
    "ds_read_b64_tr_b16 a[224:225], v205 offset:1024\n\t" \
    "ds_read_b64_tr_b16 a[226:227], v205 offset:19456\n\t" \
    "s_waitcnt lgkmcnt(6)\n\t" \
    "ds_read_b64_tr_b16 a[240:241], v206 offset:1024\n\t" \
    "ds_read_b64_tr_b16 a[242:243], v206 offset:19456\n\t" \
    "ds_read_b64_tr_b16 v[224:225], v207 offset:1024\n\t" \
    "ds_read_b64_tr_b16 v[226:227], v207 offset:19456\n\t" \
    "ds_read_b64_tr_b16 v[240:241], v204 offset:2048\n\t" \
    "ds_read_b64_tr_b16 v[242:243], v204 offset:20480\n\t" \
    "s_waitcnt lgkmcnt(6)\n\t" \
    "s_waitcnt vmcnt(18) lgkmcnt(0)\n\t" \
    "s_barrier\n\t" \
    "v_mbcnt_lo_u32_b32 v140, -1, 0\n\t" \
    "v_mbcnt_hi_u32_b32 v140, -1, v140\n\t" \
    "v_min_u32 v140, 59, v140\n\t" \
    "v_lshlrev_b32 v140, 6, v140\n\t" \
    "v_add_u32 v140, 6225920, v140\n\t" \
    "v_mov_b32 v141, 6234112\n\t" \
    "global_load_dword v141, v141, s[36:37] sc1\n\t" \
    "global_load_dwordx2 v[142:143], v140, s[36:37] sc1\n\t" \
    "v_add_u32 v204, 73728, v204\n\t" \
    "v_add_u32 v205, 73728, v205\n\t" \
    "v_add_u32 v206, 73728, v206\n\t" \
    "v_add_u32 v207, 73728, v207\n\t" \
    "ds_read_b64_tr_b16 a[148:149], v204 offset:0\n\t" \
    "ds_read_b64_tr_b16 a[150:151], v204 offset:18432\n\t" \
    "ds_read_b64_tr_b16 a[164:165], v205 offset:0\n\t" \
    "ds_read_b64_tr_b16 a[166:167], v205 offset:18432\n\t" \
    "ds_read_b64_tr_b16 a[180:181], v206 offset:0\n\t" \
    "ds_read_b64_tr_b16 a[182:183], v206 offset:18432\n\t" \
    "s_waitcnt lgkmcnt(6)\n\t" \
    "ds_read_b64_tr_b16 a[196:197], v207 offset:0\n\t" \
    "ds_read_b64_tr_b16 a[198:199], v207 offset:18432\n\t" \
    "ds_read_b64_tr_b16 a[212:213], v204 offset:1024\n\t" \
    "ds_read_b64_tr_b16 a[214:215], v204 offset:19456\n\t" \
    "ds_read_b64_tr_b16 a[228:229], v205 offset:1024\n\t" \
    "ds_read_b64_tr_b16 a[230:231], v205 offset:19456\n\t" \
    "s_waitcnt lgkmcnt(6)\n\t" \
    "ds_read_b64_tr_b16 a[244:245], v206 offset:1024\n\t" \
    "ds_read_b64_tr_b16 a[246:247], v206 offset:19456\n\t" \
    "ds_read_b64_tr_b16 v[228:229], v207 offset:1024\n\t" \
    "ds_read_b64_tr_b16 v[230:231], v207 offset:19456\n\t" \
    "ds_read_b64_tr_b16 v[244:245], v204 offset:2048\n\t" \
    "ds_read_b64_tr_b16 v[246:247], v204 offset:20480\n\t" \
    "s_waitcnt lgkmcnt(6)\n\t" \
    "s_waitcnt vmcnt(11) lgkmcnt(0)\n\t" \
    "s_barrier\n\t" \
    "ds_read_b64_tr_b16 a[152:153], v204 offset:36864\n\t" \
    "ds_read_b64_tr_b16 a[154:155], v204 offset:55296\n\t" \
    "ds_read_b64_tr_b16 a[168:169], v205 offset:36864\n\t" \
    "ds_read_b64_tr_b16 a[170:171], v205 offset:55296\n\t" \
    "ds_read_b64_tr_b16 a[184:185], v206 offset:36864\n\t" \
    "ds_read_b64_tr_b16 a[186:187], v206 offset:55296\n\t" \
    "s_waitcnt lgkmcnt(6)\n\t" \
    "ds_read_b64_tr_b16 a[200:201], v207 offset:36864\n\t" \
    "ds_read_b64_tr_b16 a[202:203], v207 offset:55296\n\t" \
    "ds_read_b64_tr_b16 a[216:217], v204 offset:37888\n\t" \
    "ds_read_b64_tr_b16 a[218:219], v204 offset:56320\n\t" \
    "ds_read_b64_tr_b16 a[232:233], v205 offset:37888\n\t" \
    "ds_read_b64_tr_b16 a[234:235], v205 offset:56320\n\t" \
    "s_waitcnt lgkmcnt(6)\n\t" \
    "ds_read_b64_tr_b16 a[248:249], v206 offset:37888\n\t" \
    "ds_read_b64_tr_b16 a[250:251], v206 offset:56320\n\t" \
    "ds_read_b64_tr_b16 v[232:233], v207 offset:37888\n\t" \
    "ds_read_b64_tr_b16 v[234:235], v207 offset:56320\n\t" \
    "ds_read_b64_tr_b16 v[248:249], v204 offset:38912\n\t" \
    "ds_read_b64_tr_b16 v[250:251], v204 offset:57344\n\t" \
    "s_waitcnt lgkmcnt(6)\n\t" \
    "s_waitcnt vmcnt(2) lgkmcnt(0)\n\t" \
    "s_barrier\n\t" \
    "v_add_u32 v204, -36864, v204\n\t" \
    "v_add_u32 v205, -36864, v205\n\t" \
    "v_add_u32 v206, -36864, v206\n\t" \
    "v_add_u32 v207, -36864, v207\n\t" \
    "ds_read_b64_tr_b16 a[156:157], v204 offset:0\n\t" \
    "ds_read_b64_tr_b16 a[158:159], v204 offset:18432\n\t" \
    "ds_read_b64_tr_b16 a[172:173], v205 offset:0\n\t" \
    "ds_read_b64_tr_b16 a[174:175], v205 offset:18432\n\t" \
    "ds_read_b64_tr_b16 a[188:189], v206 offset:0\n\t" \
    "ds_read_b64_tr_b16 a[190:191], v206 offset:18432\n\t" \
    "s_waitcnt lgkmcnt(6)\n\t" \
    "ds_read_b64_tr_b16 a[204:205], v207 offset:0\n\t" \
    "ds_read_b64_tr_b16 a[206:207], v207 offset:18432\n\t" \
    "ds_read_b64_tr_b16 a[220:221], v204 offset:1024\n\t" \
    "ds_read_b64_tr_b16 a[222:223], v204 offset:19456\n\t" \
    "ds_read_b64_tr_b16 a[236:237], v205 offset:1024\n\t" \
    "ds_read_b64_tr_b16 a[238:239], v205 offset:19456\n\t" \
    "s_waitcnt lgkmcnt(6)\n\t" \
    "ds_read_b64_tr_b16 a[252:253], v206 offset:1024\n\t" \
    "ds_read_b64_tr_b16 a[254:255], v206 offset:19456\n\t" \
    "ds_read_b64_tr_b16 v[236:237], v207 offset:1024\n\t" \
    "ds_read_b64_tr_b16 v[238:239], v207 offset:19456\n\t" \
    "ds_read_b64_tr_b16 v[252:253], v204 offset:2048\n\t" \
    "ds_read_b64_tr_b16 v[254:255], v204 offset:20480\n\t" \
    "s_waitcnt lgkmcnt(6)\n\t" \
    "s_waitcnt vmcnt(0) lgkmcnt(0)\n\t" \
    "s_barrier\n\t" \
    "v_sub_u32 v142, v142, v141\n\t" \
    "v_cmp_ne_u32 vcc, 1, v142\n\t" \
    "s_cbranch_vccz vv_Cok4_%=\n\t" \
    "vv_C3_%=:\n\t" \
    "global_load_dwordx2 v[142:143], v140, s[36:37] sc1\n\t" \
    "s_waitcnt vmcnt(0)\n\t" \
    "v_sub_u32 v142, v142, v141\n\t" \
    "v_cmp_ne_u32 vcc, 1, v142\n\t" \
    "s_cbranch_vccz vv_Cok4_%=\n\t" \
    "s_cmp_eq_u32 s68, 0\n\t" \
    "s_cbranch_scc1 vv_F5_%=\n\t" \
    "s_sub_u32 s68, s68, 1\n\t" \
    "s_sleep 4\n\t" \
    "s_branch vv_C3_%=\n\t" \
    "vv_F5_%=:\n\t" \
    "v_mov_b32 v144, 6095104\n\t" \
    "v_mov_b32 v145, 4\n\t" \
    "global_store_dword v144, v145, s[36:37] sc1\n\t" \
    "s_waitcnt vmcnt(0)\n\t" \
    "s_branch vv_Cok4_%=\n\t" \
    "vv_Cok4_%=:\n\t" \
    "s_cmp_lg_u32 s66, 0\n\t" \
    "s_cbranch_scc1 vv_Ca6_%=\n\t" \
    "v_mov_b32 v140, 6176768\n\t" \
    "s_mov_b64 exec, 1\n\t" \
    "global_atomic_add v140, v142, s[36:37] sc1\n\t" \
    "s_mov_b64 exec, -1\n\t" \
    "vv_Ca6_%=:\n\t" \
    "v_min_u32_dpp v143, v143, v143 row_shl:6 row_mask:0xf bank_mask:0xf\n\t" \
    "s_nop 0\n\t" \
    "v_readlane_b32 s69, v143, 0\n\t" \
    "v_readlane_b32 s70, v143, 1\n\t" \
    "s_nop 1\n\t" \
    "s_cmp_lt_u32 s70, s69\n\t" \
    "s_cselect_b32 s62, 8192, 0\n\t" \
    "s_cselect_b32 s69, s70, s69\n\t" \
    "s_mov_b32 s71, s69\n\t" \
    "v_readlane_b32 s69, v143, 2\n\t" \
    "v_readlane_b32 s70, v143, 3\n\t" \
    "s_nop 1\n\t" \
    "s_cmp_lt_u32 s70, s69\n\t" \
    "s_cselect_b32 s63, 8192, 0\n\t" \
    "s_cselect_b32 s69, s70, s69\n\t" \
    "s_max_u32 s71, s69, s71\n\t" \
    "v_readlane_b32 s69, v143, 4\n\t" \
    "v_readlane_b32 s70, v143, 5\n\t" \
    "s_nop 1\n\t" \
    "s_cmp_lt_u32 s70, s69\n\t" \
    "s_cselect_b32 s64, 8192, 0\n\t" \
    "s_cselect_b32 s69, s70, s69\n\t" \
    "s_max_u32 s71, s69, s71\n\t" \
    "s_add_u32 s63, s63, 0x200000\n\t" \
    "s_xor_b32 s82, s64, 8192\n\t" \
    "s_cmp_gt_u32 s71, 625\n\t" \
    "s_cselect_b32 s69, 1, 0\n\t" \
    "s_mul_i32 s70, s69, 160\n\t" \
    "s_add_u32 s86, s86, s70\n\t" \
    "s_mul_i32 s70, s69, 536\n\t" \
    "s_add_u32 s87, s87, s70\n\t" \
    "s_mul_i32 s70, s69, 256\n\t" \
    "s_add_u32 s88, s88, s70\n\t" \
    "v_mov_b32 v148, -1\n\t" \
    "v_mov_b32 v149, -1\n\t" \
    "v_mov_b32 v150, -1\n\t" \
    "v_mov_b32 v151, -1\n\t" \
    "s_mov_b32 s71, 3\n\t" \
    "vv_E7_%=:\n\t" \
    "s_memtime s[54:55]\n\t" \
    "s_nop 0\n\t" \
    "s_memrealtime s[4:5]\n\t" \
    "s_nop 0\n\t" \
    "s_memrealtime s[6:7]\n\t" \
    "s_nop 0\n\t" \
    "s_memrealtime s[8:9]\n\t" \
    "s_nop 0\n\t" \
    "s_memrealtime s[10:11]\n\t" \
    "s_nop 0\n\t" \
    "s_memrealtime s[12:13]\n\t" \
    "s_nop 0\n\t" \
    "s_memrealtime s[14:15]\n\t" \
    "s_nop 0\n\t" \
    "s_memrealtime s[16:17]\n\t" \
    "s_nop 0\n\t" \
    "s_memrealtime s[18:19]\n\t" \
    "s_nop 0\n\t" \
    "s_memrealtime s[20:21]\n\t" \
    "s_nop 0\n\t" \
    "s_memrealtime s[22:23]\n\t" \
    "s_nop 0\n\t" \
    "s_memrealtime s[24:25]\n\t" \
    "s_nop 0\n\t" \
    "s_memrealtime s[26:27]\n\t" \
    "s_nop 0\n\t" \
    "s_memrealtime s[28:29]\n\t" \
    "s_nop 0\n\t" \
    "s_memtime s[56:57]\n\t" \
    "s_mov_b32 s70, 1\n\t" \
    "s_sub_u32 s71, s71, 1\n\t" \
    "s_cselect_b32 s69, 0, -1\n\t" \
    "s_waitcnt lgkmcnt(0)\n\t" \
    "s_sub_u32 s54, s56, s54\n\t" \
    "s_and_b32 s54, s54, s69\n\t" \
    "s_cmp_gt_u32 s54, 124\n\t" \
    "s_cbranch_scc1 vv_E7_%=\n\t" \
    "s_cmp_eq_u32 s4, s28\n\t" \
    "s_addc_u32 s70, s70, 0\n\t" \
    "s_cmp_eq_u32 s6, s28\n\t" \
    "s_addc_u32 s70, s70, 0\n\t" \
    "s_cmp_eq_u32 s8, s28\n\t" \
    "s_addc_u32 s70, s70, 0\n\t" \
    "s_cmp_eq_u32 s10, s28\n\t" \
    "s_addc_u32 s70, s70, 0\n\t" \
    "s_cmp_eq_u32 s12, s28\n\t" \
    "s_addc_u32 s70, s70, 0\n\t" \
    "s_cmp_eq_u32 s14, s28\n\t" \
    "s_addc_u32 s70, s70, 0\n\t" \
    "s_cmp_eq_u32 s16, s28\n\t" \
    "s_addc_u32 s70, s70, 0\n\t" \
    "s_cmp_eq_u32 s18, s28\n\t" \
    "s_addc_u32 s70, s70, 0\n\t" \
    "s_cmp_eq_u32 s20, s28\n\t" \
    "s_addc_u32 s70, s70, 0\n\t" \
    "s_cmp_eq_u32 s22, s28\n\t" \
    "s_addc_u32 s70, s70, 0\n\t" \
    "s_cmp_eq_u32 s24, s28\n\t" \
    "s_addc_u32 s70, s70, 0\n\t" \
    "s_cmp_eq_u32 s26, s28\n\t" \
    "s_addc_u32 s70, s70, 0\n\t" \
    "s_mov_b32 s75, s73\n\t" \
    "s_mov_b32 s76, s74\n\t" \
    "s_lshl_b32 s70, s70, 3\n\t" \
    "s_sub_u32 s73, s56, s70\n\t" \
    "s_lshl_b32 s74, s28, 4\n\t" \
    "s_sub_u32 s69, s73, s75\n\t" \
    "s_sub_u32 s70, s74, s76\n\t" \
    "s_sub_u32 s71, s70, 1600\n\t" \
    "s_cmp_lt_u32 s71, 400000\n\t" \
    "s_cbranch_scc0 vv_K9_%=\n\t" \
    "v_cvt_f32_u32 v140, s69\n\t" \
    "v_cvt_f32_u32 v141, s70\n\t" \
    "v_rcp_f32 v142, v140\n\t" \
    "v_rcp_f32 v143, v141\n\t" \
    "v_mul_f32 v142, v142, v141\n\t" \
    "v_mul_f32 v143, v143, v140\n\t" \
    "v_mul_f32 v142, 0x4f800000, v142\n\t" \
    "v_mul_f32 v143, 0x4f000000, v143\n\t" \
    "v_cvt_u32_f32 v142, v142\n\t" \
    "v_cvt_u32_f32 v143, v143\n\t" \
    "v_readfirstlane_b32 s69, v142\n\t" \
    "v_readfirstlane_b32 s70, v143\n\t" \
    "s_nop 1\n\t" \
    "s_mov_b32 s78, s69\n\t" \
    "s_mov_b32 s77, s70\n\t" \
    "vv_K9_%=:\n\t" \
    "s_mov_b32 s79, s74\n\t" \
    "s_sub_u32 s80, s74, 32000\n\t" \
    "s_cmp_eq_u32 s66, 1\n\t" \
    "s_cbranch_scc1 vv_loop11_%=\n\t" \
    "s_cmp_eq_u32 s66, 2\n\t" \
    "s_cbranch_scc1 vv_loop12_%=\n\t" \
    "s_cmp_eq_u32 s66, 3\n\t" \
    "s_cbranch_scc1 vv_loop13_%=\n\t" \
    "vv_loop10_%=:\n\t" \
    "v_mov_b32 v72, 0\n\t" \
    "v_mov_b32 v73, 0\n\t" \
    "v_mov_b32 v74, 0\n\t" \
    "v_mov_b32 v75, 0\n\t" \
    "v_mov_b32 v76, 0\n\t" \
    "v_mov_b32 v77, 0\n\t" \
    "v_mov_b32 v78, 0\n\t" \
    "v_mov_b32 v79, 0\n\t" \
    "v_mov_b32 v92, 0\n\t" \
    "v_mov_b32 v93, 0\n\t" \
    "v_mov_b32 v94, 0\n\t" \
    "v_mov_b32 v95, 0\n\t" \
    "v_mov_b32 v96, 0\n\t" \
    "v_mov_b32 v97, 0\n\t" \
    "v_mov_b32 v98, 0\n\t" \
    "v_mov_b32 v99, 0\n\t" \
    "v_mov_b32 v100, 0\n\t" \
    "v_mov_b32 v101, 0\n\t" \
    "v_mov_b32 v102, 0\n\t" \
    "v_mov_b32 v103, 0\n\t" \
    "v_mov_b32 v104, 0\n\t" \
    "v_mov_b32 v105, 0\n\t" \
    "v_mov_b32 v106, 0\n\t" \
    "v_mov_b32 v107, 0\n\t" \
    "v_mov_b32 v108, 0\n\t" \
    "v_mov_b32 v109, 0\n\t" \
    "v_mov_b32 v110, 0\n\t" \
    "v_mov_b32 v111, 0\n\t" \
    "v_mov_b32 v112, 0\n\t" \
    "v_mov_b32 v113, 0\n\t" \
    "v_mov_b32 v114, 0\n\t" \
    "v_mov_b32 v115, 0\n\t" \
    "v_mov_b32 v116, 0\n\t" \
    "v_mov_b32 v117, 0\n\t" \
    "v_mov_b32 v118, 0\n\t" \
    "v_mov_b32 v119, 0\n\t" \
    "v_mov_b32 v120, 0\n\t" \
    "v_mov_b32 v121, 0\n\t" \
    "v_mov_b32 v122, 0\n\t" \
    "v_mov_b32 v123, 0\n\t" \
    "v_mov_b32 v124, 0\n\t" \
    "v_mov_b32 v125, 0\n\t" \
    "v_mov_b32 v126, 0\n\t" \
    "v_mov_b32 v127, 0\n\t" \
    "vv_go15_%=:\n\t" \
    "s_add_u32 s69, s65, 0\n\t" \
    "s_bitcmp1_b32 s69, 0\n\t" \
    "s_cselect_b32 s70, s63, s62\n\t" \
    "s_bfe_u32 s69, s69, 0x20001\n\t" \
    "s_lshl_b32 s69, s69, 19\n\t" \
    "s_add_u32 s69, s69, s70\n\t" \
    "s_add_u32 s46, s36, s69\n\t" \
    "s_addc_u32 s47, s37, 0\n\t" \
    "s_add_u32 s69, s65, 0\n\t" \
    "s_mul_i32 s70, s69, 171\n\t" \
    "s_lshr_b32 s70, s70, 9\n\t" \
    "s_mul_i32 s70, s70, 3\n\t" \
    "s_sub_u32 s69, s69, s70\n\t" \
    "s_mul_i32 s69, s69, 36\n\t" \
    "s_lshl_b32 s69, s69, 14\n\t" \
    "s_add_u32 s69, s69, s64\n\t" \
    "s_add_u32 s69, s69, 4194304\n\t" \
    "s_add_u32 s38, s36, s69\n\t" \
    "s_addc_u32 s39, s37, 0\n\t" \
    "s_add_u32 s69, s65, 2\n\t" \
    "s_mul_i32 s70, s69, 171\n\t" \
    "s_lshr_b32 s70, s70, 9\n\t" \
    "s_mul_i32 s70, s70, 3\n\t" \
    "s_sub_u32 s69, s69, s70\n\t" \
    "s_mul_i32 s69, s69, 36\n\t" \
    "s_lshl_b32 s69, s69, 14\n\t" \
    "s_add_u32 s69, s69, s64\n\t" \
    "s_add_u32 s69, s69, 4194304\n\t" \
    "s_add_u32 s42, s36, s69\n\t" \
    "s_addc_u32 s43, s37, 0\n\t" \
    "s_mul_i32 s69, s65, 9216\n\t" \
    "s_add_u32 s69, s69, 123731968\n\t" \
    "s_add_u32 s40, s36, s69\n\t" \
    "s_addc_u32 s41, s37, 0\n\t" \
    "s_sub_u32 s70, s79, s74\n\t" \
    "s_max_i32 s70, s70, 0\n\t" \
    "s_min_i32 s70, s70, 16000\n\t" \
    "s_lshl_b32 s70, s70, 1\n\t" \
    "s_mul_hi_u32 s70, s70, s77\n\t" \
    "s_add_u32 s70, s70, s73\n\t" \
    "s_sub_u32 s70, s70, 100\n\t" \
    "s_getpc_b64 s[30:31]\n\t" \
    "vv_Wp17_%=:\n\t" \
    "s_add_u32 s30, s30, vv_We18_%=-vv_Wp17_%=\n\t" \
    "s_addc_u32 s31, s31, 0\n\t" \
    "vv_W16_%=:\n\t" \
    "s_memtime s[56:57]\n\t" \
    "s_waitcnt lgkmcnt(0)\n\t" \
    "s_sub_u32 s69, s70, s56\n\t" \
    "s_cmp_gt_i32 s69, 111\n\t" \
    "s_cbranch_scc1 vv_W16_%=\n\t" \
    "s_max_i32 s69, s69, 0\n\t" \
    "s_lshr_b32 s69, s69, 1\n\t" \
    "s_and_b32 s69, s69, 0x3c\n\t" \
    "s_sub_u32 s30, s30, s69\n\t" \
    "s_subb_u32 s31, s31, 0\n\t" \
    "s_setpc_b64 s[30:31]\n\t" \
    ".p2align 6\n\t" \
    "s_nop 0\n\t" \
    "s_nop 0\n\t" \
    "s_nop 0\n\t" \
    "s_nop 1\n\t" \
    "s_nop 1\n\t" \
    "s_nop 1\n\t" \
    "s_nop 1\n\t" \
    "s_nop 1\n\t" \
    "s_nop 1\n\t" \
    "s_nop 1\n\t" \
    "s_nop 1\n\t" \
    "s_nop 1\n\t" \
    "s_nop 1\n\t" \
    "s_nop 1\n\t" \
    "s_nop 1\n\t" \
    "s_nop 1\n\t" \
    "vv_We18_%=:\n\t" \
    "vv_G19_%=:\n\t" \
    "global_load_dwordx4 v[36:39], v1, s[46:47] sc1\n\t" \
    "s_nop 9\n\t" \
    "global_load_dwordx4 v[40:43], v1, s[46:47] offset:1024 sc1\n\t" \
    "s_nop 9\n\t" \
    "global_load_dwordx4 v[44:47], v2, s[46:47] sc1\n\t" \
    "s_waitcnt vmcnt(2)\n\t" \
    "v_smfmac_f32_16x16x64_bf16 v[72:75], v[36:39], a[0:7], v10\n\t" \
    "ds_write_b128 v0, v[36:39] offset:0\n\t" \
    "v_smfmac_f32_16x16x64_bf16 v[76:79], v[36:39], a[72:79], v10\n\t" \
    "v_max3_u32 v140, v36, v37, v38\n\t" \
    "s_waitcnt vmcnt(1)\n\t" \
    "ds_write_b128 v0, v[40:43] offset:1024\n\t" \
    "v_smfmac_f32_16x16x64_bf16 v[72:75], v[40:43], a[8:15], v10\n\t" \
    "v_max3_u32 v140, v140, v39, v40\n\t" \
    "v_max3_u32 v140, v140, v41, v42\n\t" \
    "v_max_u32 v140, v140, v43\n\t" \
    "v_cmp_eq_u32 vcc, -1, v140\n\t" \
    "v_smfmac_f32_16x16x64_bf16 v[76:79], v[40:43], a[80:87], v10\n\t" \
    "s_cbranch_vccnz vv_Gbad20_%=\n\t" \
    "vv_Gc22_%=:\n\t" \
    "s_waitcnt lgkmcnt(0)\n\t" \
    "s_barrier\n\t" \
    "ds_read_b128 v[48:51], v0 offset:2048\n\t" \
    "ds_read_b128 v[52:55], v0 offset:3072\n\t" \
    "s_waitcnt vmcnt(0)\n\t" \
    "v_smfmac_f32_16x16x64_bf16 v[72:75], v[44:47], a[64:71], v10\n\t" \
    "ds_read_b128 v[56:59], v0 offset:4096\n\t" \
    "ds_read_b128 v[60:63], v0 offset:5120\n\t" \
    "v_max3_u32 v140, v44, v45, v46\n\t" \
    "v_max_u32 v140, v140, v47\n\t" \
    "v_smfmac_f32_16x16x64_bf16 v[76:79], v[44:47], a[136:143], v10\n\t" \
    "ds_read_b128 v[64:67], v0 offset:6144\n\t" \
    "ds_read_b128 v[68:71], v0 offset:7168\n\t" \
    "v_cmp_eq_u32 vcc, -1, v140\n\t" \
    "s_cbranch_vccnz vv_Gbad221_%=\n\t" \
    "vv_Gc223_%=:\n\t" \
    "s_waitcnt lgkmcnt(5)\n\t" \
    "v_smfmac_f32_16x16x64_bf16 v[72:75], v[48:51], a[16:23], v10\n\t" \
    "s_waitcnt lgkmcnt(4)\n\t" \
    "v_smfmac_f32_16x16x64_bf16 v[72:75], v[52:55], a[24:31], v10\n\t" \
    "s_waitcnt lgkmcnt(3)\n\t" \
    "v_smfmac_f32_16x16x64_bf16 v[72:75], v[56:59], a[32:39], v10\n\t" \
    "s_waitcnt lgkmcnt(2)\n\t" \
    "v_smfmac_f32_16x16x64_bf16 v[72:75], v[60:63], a[40:47], v10\n\t" \
    "s_waitcnt lgkmcnt(1)\n\t" \
    "v_smfmac_f32_16x16x64_bf16 v[72:75], v[64:67], a[48:55], v10\n\t" \
    "s_waitcnt lgkmcnt(0)\n\t" \
    "v_smfmac_f32_16x16x64_bf16 v[72:75], v[68:71], a[56:63], v10\n\t" \
    "v_smfmac_f32_16x16x64_bf16 v[76:79], v[48:51], a[88:95], v10\n\t" \
    "s_nop 1\n\t" \
    "s_nop 0\n\t" \
    "v_smfmac_f32_16x16x64_bf16 v[76:79], v[52:55], a[96:103], v10\n\t" \
    "s_nop 1\n\t" \
    "s_nop 0\n\t" \
    "v_smfmac_f32_16x16x64_bf16 v[76:79], v[56:59], a[104:111], v10\n\t" \
    "v_add_f32 v72, v72, v74\n\t" \
    "v_smfmac_f32_16x16x64_bf16 v[76:79], v[60:63], a[112:119], v10\n\t" \
    "v_add_f32 v73, v73, v75\n\t" \
    "v_smfmac_f32_16x16x64_bf16 v[76:79], v[64:67], a[120:127], v10\n\t" \
    "v_cvt_pk_bf16_f32 v80, v72, v73\n\t" \
    "v_smfmac_f32_16x16x64_bf16 v[76:79], v[68:71], a[128:135], v10\n\t" \
    "ds_write_b32 v12, v80 offset:0\n\t" \
    "s_nop 6\n\t" \
    "v_add_f32 v76, v76, v78\n\t" \
    "v_add_f32 v77, v77, v79\n\t" \
    "v_cvt_pk_bf16_f32 v81, v76, v77\n\t" \
    "ds_write_b32 v12, v81 offset:64\n\t" \
    "s_waitcnt lgkmcnt(0)\n\t" \
    "s_barrier\n\t" \
    "ds_read_b64_tr_b16 v[84:85], v13 offset:0\n\t" \
    "ds_read_b64_tr_b16 v[86:87], v13 offset:128\n\t" \
    "ds_read_b64_tr_b16 v[88:89], v13 offset:1024\n\t" \
    "ds_read_b64_tr_b16 v[90:91], v13 offset:1152\n\t" \
    "s_waitcnt lgkmcnt(2)\n\t" \
    "v_smfmac_f32_16x16x64_bf16 v[92:95], v[84:87], a[144:151], v10\n\t" \
    "s_nop 2\n\t" \
    "s_waitcnt lgkmcnt(0)\n\t" \
    "v_smfmac_f32_16x16x64_bf16 v[92:95], v[88:91], a[152:159], v10\n\t" \
    "s_nop 2\n\t" \
    "v_smfmac_f32_16x16x64_bf16 v[96:99], v[84:87], a[160:167], v10\n\t" \
    "s_nop 2\n\t" \
    "v_smfmac_f32_16x16x64_bf16 v[96:99], v[88:91], a[168:175], v10\n\t" \
    "s_nop 2\n\t" \
    "v_smfmac_f32_16x16x64_bf16 v[100:103], v[84:87], a[176:183], v10\n\t" \
    "s_nop 0\n\t" \
    "v_add_f32 v92, v92, v94\n\t" \
    "v_add_f32 v93, v93, v95\n\t" \
    "v_smfmac_f32_16x16x64_bf16 v[100:103], v[88:91], a[184:191], v10\n\t" \
    "s_nop 1\n\t" \
    "v_cvt_pk_bf16_f32 v128, v92, v93\n\t" \
    "v_smfmac_f32_16x16x64_bf16 v[104:107], v[84:87], a[192:199], v10\n\t" \
    "s_nop 0\n\t" \
    "v_add_f32 v96, v96, v98\n\t" \
    "v_add_f32 v97, v97, v99\n\t" \
    "v_smfmac_f32_16x16x64_bf16 v[104:107], v[88:91], a[200:207], v10\n\t" \
    "s_nop 1\n\t" \
    "v_cvt_pk_bf16_f32 v129, v96, v97\n\t" \
    "v_smfmac_f32_16x16x64_bf16 v[124:127], v[84:87], v[240:247], v10\n\t" \
    "s_nop 0\n\t" \
    "v_add_f32 v100, v100, v102\n\t" \
    "v_add_f32 v101, v101, v103\n\t" \
    "v_smfmac_f32_16x16x64_bf16 v[124:127], v[88:91], v[248:255], v10\n\t" \
    "s_nop 1\n\t" \
    "global_load_dword v140, v20, s[46:47] sc1\n\t" \
    "v_cvt_pk_bf16_f32 v130, v100, v101\n\t" \
    "v_smfmac_f32_16x16x64_bf16 v[108:111], v[84:87], a[208:215], v10\n\t" \
    "s_nop 0\n\t" \
    "v_add_f32 v104, v104, v106\n\t" \
    "v_add_f32 v105, v105, v107\n\t" \
    "v_smfmac_f32_16x16x64_bf16 v[108:111], v[88:91], a[216:223], v10\n\t" \
    "s_nop 0\n\t" \
    "v_cvt_pk_bf16_f32 v131, v104, v105\n\t" \
    "v_permlane32_swap_b32 v128, v130\n\t" \
    "v_smfmac_f32_16x16x64_bf16 v[112:115], v[84:87], a[224:231], v10\n\t" \
    "s_nop 0\n\t" \
    "v_permlane32_swap_b32 v129, v131\n\t" \
    "s_nop 1\n\t" \
    "v_permlane16_swap_b32 v128, v129\n\t" \
    "v_smfmac_f32_16x16x64_bf16 v[112:115], v[88:91], a[232:239], v10\n\t" \
    "v_permlane16_swap_b32 v130, v131\n\t" \
    "global_store_dwordx4 v14, v[128:131], s[38:39] sc1\n\t" \
    "v_add_f32 v124, v124, v126\n\t" \
    "v_smfmac_f32_16x16x64_bf16 v[116:119], v[84:87], a[240:247], v10\n\t" \
    "s_nop 0\n\t" \
    "v_add_f32 v125, v125, v127\n\t" \
    "v_cvt_pk_bf16_f32 v136, v124, v125\n\t" \
    "v_smfmac_f32_16x16x64_bf16 v[116:119], v[88:91], a[248:255], v10\n\t" \
    "global_store_dword v16, v136, s[38:39] sc1\n\t" \
    "v_add_f32 v108, v108, v110\n\t" \
    "v_add_f32 v109, v109, v111\n\t" \
    "v_smfmac_f32_16x16x64_bf16 v[120:123], v[84:87], v[224:231], v10\n\t" \
    "s_nop 0\n\t" \
    "v_cvt_pk_bf16_f32 v132, v108, v109\n\t" \
    "v_add_f32 v112, v112, v114\n\t" \
    "v_smfmac_f32_16x16x64_bf16 v[120:123], v[88:91], v[232:239], v10\n\t" \
    "v_add_f32 v113, v113, v115\n\t" \
    "v_cvt_pk_bf16_f32 v133, v112, v113\n\t" \
    "v_add_f32 v116, v116, v118\n\t" \
    "v_add_f32 v117, v117, v119\n\t" \
    "v_cvt_pk_bf16_f32 v134, v116, v117\n\t" \
    "s_nop 2\n\t" \
    "v_pk_add_f32 v[120:121], v[120:121], v[122:123]\n\t" \
    "v_cvt_pk_bf16_f32 v135, v120, v121\n\t" \
    "v_permlane32_swap_b32 v132, v134\n\t" \
    "s_nop 0\n\t" \
    "v_permlane32_swap_b32 v133, v135\n\t" \
    "s_nop 1\n\t" \
    "v_permlane16_swap_b32 v132, v133\n\t" \
    "v_permlane16_swap_b32 v134, v135\n\t" \
    "global_store_dwordx4 v15, v[132:135], s[38:39] sc1\n\t" \
    "s_nop 1\n\t" \
    "global_store_dwordx4 v17, v[128:131], s[40:41] sc1\n\t" \
    "global_store_dwordx4 v17, v[132:135], s[40:41] offset:1024 sc1\n\t" \
    "global_store_dword v18, v136, s[40:41] sc1\n\t" \
    "s_cmp_eq_u32 s65, 63\n\t" \
    "s_cbranch_scc1 vv_end14_%=\n\t" \
    "s_mov_b32 s71, 3\n\t" \
    "vv_E25_%=:\n\t" \
    "s_memtime s[54:55]\n\t" \
    "s_nop 0\n\t" \
    "s_memrealtime s[4:5]\n\t" \
    "s_nop 0\n\t" \
    "s_memrealtime s[6:7]\n\t" \
    "s_nop 0\n\t" \
    "s_memrealtime s[8:9]\n\t" \
    "s_nop 0\n\t" \
    "s_memrealtime s[10:11]\n\t" \
    "s_nop 0\n\t" \
    "s_memrealtime s[12:13]\n\t" \
    "s_nop 0\n\t" \
    "s_memrealtime s[14:15]\n\t" \
    "s_nop 0\n\t" \
    "s_memrealtime s[16:17]\n\t" \
    "s_nop 0\n\t" \
    "s_memrealtime s[18:19]\n\t" \
    "s_nop 0\n\t" \
    "s_memrealtime s[20:21]\n\t" \
    "s_nop 0\n\t" \
    "s_memrealtime s[22:23]\n\t" \
    "s_nop 0\n\t" \
    "s_memrealtime s[24:25]\n\t" \
    "s_nop 0\n\t" \
    "s_memrealtime s[26:27]\n\t" \
    "s_nop 0\n\t" \
    "s_memrealtime s[28:29]\n\t" \
    "s_nop 0\n\t" \
    "s_memtime s[56:57]\n\t" \
    "s_mov_b32 s70, 1\n\t" \
    "s_sub_u32 s71, s71, 1\n\t" \
    "s_cselect_b32 s69, 0, -1\n\t" \
    "s_waitcnt lgkmcnt(0)\n\t" \
    "s_sub_u32 s54, s56, s54\n\t" \
    "s_and_b32 s54, s54, s69\n\t" \
    "s_cmp_gt_u32 s54, 124\n\t" \
    "s_cbranch_scc1 vv_E25_%=\n\t" \
    "s_cmp_eq_u32 s4, s28\n\t" \
    "s_addc_u32 s70, s70, 0\n\t" \
    "s_cmp_eq_u32 s6, s28\n\t" \
    "s_addc_u32 s70, s70, 0\n\t" \
    "s_cmp_eq_u32 s8, s28\n\t" \
    "s_addc_u32 s70, s70, 0\n\t" \
    "s_cmp_eq_u32 s10, s28\n\t" \
    "s_addc_u32 s70, s70, 0\n\t" \
    "s_cmp_eq_u32 s12, s28\n\t" \
    "s_addc_u32 s70, s70, 0\n\t" \
    "s_cmp_eq_u32 s14, s28\n\t" \
    "s_addc_u32 s70, s70, 0\n\t" \
    "s_cmp_eq_u32 s16, s28\n\t" \
    "s_addc_u32 s70, s70, 0\n\t" \
    "s_cmp_eq_u32 s18, s28\n\t" \
    "s_addc_u32 s70, s70, 0\n\t" \
    "s_cmp_eq_u32 s20, s28\n\t" \
    "s_addc_u32 s70, s70, 0\n\t" \
    "s_cmp_eq_u32 s22, s28\n\t" \
    "s_addc_u32 s70, s70, 0\n\t" \
    "s_cmp_eq_u32 s24, s28\n\t" \
    "s_addc_u32 s70, s70, 0\n\t" \
    "s_cmp_eq_u32 s26, s28\n\t" \
    "s_addc_u32 s70, s70, 0\n\t" \
    "s_mov_b32 s75, s73\n\t" \
    "s_mov_b32 s76, s74\n\t" \
    "s_lshl_b32 s70, s70, 3\n\t" \
    "s_sub_u32 s73, s56, s70\n\t" \
    "s_lshl_b32 s74, s28, 4\n\t" \
    "s_sub_u32 s69, s73, s75\n\t" \
    "s_sub_u32 s70, s74, s76\n\t" \
    "s_sub_u32 s71, s70, 1600\n\t" \
    "s_cmp_lt_u32 s71, 400000\n\t" \
    "s_cbranch_scc0 vv_K27_%=\n\t" \
    "v_cvt_f32_u32 v141, s69\n\t" \
    "v_cvt_f32_u32 v142, s70\n\t" \
    "v_rcp_f32 v143, v141\n\t" \
    "v_rcp_f32 v144, v142\n\t" \
    "v_mul_f32 v143, v143, v142\n\t" \
    "v_mul_f32 v144, v144, v141\n\t" \
    "v_mul_f32 v143, 0x4f800000, v143\n\t" \
    "v_mul_f32 v144, 0x4f000000, v144\n\t" \
    "v_cvt_u32_f32 v143, v143\n\t" \
    "v_cvt_u32_f32 v144, v144\n\t" \
    "v_readfirstlane_b32 s69, v143\n\t" \
    "v_readfirstlane_b32 s70, v144\n\t" \
    "s_nop 1\n\t" \
    "s_sub_u32 s69, s69, s78\n\t" \
    "s_ashr_i32 s69, s69, 2\n\t" \
    "s_add_u32 s78, s78, s69\n\t" \
    "s_sub_u32 s70, s70, s77\n\t" \
    "s_ashr_i32 s70, s70, 2\n\t" \
    "s_add_u32 s77, s77, s70\n\t" \
    "vv_K27_%=:\n\t" \
    "s_waitcnt vmcnt(6)\n\t" \
    "v_cmp_eq_u32 vcc, -1, v140\n\t" \
    "s_cbranch_vccz vv_Hok29_%=\n\t" \
    "vv_H28_%=:\n\t" \
    "global_load_dword v140, v20, s[46:47] sc1\n\t" \
    "s_waitcnt vmcnt(0)\n\t" \
    "v_cmp_eq_u32 vcc, -1, v140\n\t" \
    "s_cbranch_vccz vv_Hc30_%=\n\t" \
    "s_cmp_eq_u32 s68, 0\n\t" \
    "s_cbranch_scc1 vv_F31_%=\n\t" \
    "s_sub_u32 s68, s68, 1\n\t" \
    "s_sleep 5\n\t" \
    "s_branch vv_H28_%=\n\t" \
    "vv_F31_%=:\n\t" \
    "v_mov_b32 v141, 6095104\n\t" \
    "v_mov_b32 v142, 6\n\t" \
    "global_store_dword v141, v142, s[36:37] sc1\n\t" \
    "s_waitcnt vmcnt(0)\n\t" \
    "s_branch vv_Hc30_%=\n\t" \
    "vv_Hc30_%=:\n\t" \
    "vv_Hok29_%=:\n\t" \
    "v_subrev_u32 v140, s80, v140\n\t" \
    "v_and_b32 v20, -5, v20\n\t" \
    "s_nop 0\n\t" \
    "v_max_i32_dpp v140, v140, v140 row_ror:1 row_mask:0xf bank_mask:0xf\n\t" \
    "s_nop 1\n\t" \
    "v_max_i32_dpp v140, v140, v140 row_ror:2 row_mask:0xf bank_mask:0xf\n\t" \
    "s_nop 1\n\t" \
    "v_max_i32_dpp v140, v140, v140 row_ror:4 row_mask:0xf bank_mask:0xf\n\t" \
    "s_nop 1\n\t" \
    "v_max_i32_dpp v140, v140, v140 row_ror:8 row_mask:0xf bank_mask:0xf\n\t" \
    "s_nop 0\n\t" \
    "v_readlane_b32 s69, v140, 0\n\t" \
    "v_readlane_b32 s71, v140, 16\n\t" \
    "v_readlane_b32 s70, v140, 32\n\t" \
    "v_readlane_b32 s81, v140, 48\n\t" \
    "s_max_i32 s69, s69, s71\n\t" \
    "s_nop 0\n\t" \
    "s_max_i32 s71, s70, s81\n\t" \
    "s_max_i32 s69, s69, s71\n\t" \
    "s_add_u32 s81, s69, s80\n\t" \
    "s_cmp_lg_u32 s66, 0\n\t" \
    "s_cbranch_scc1 vv_SF32_%=\n\t" \
    "s_lshl_b32 s69, s67, 7\n\t" \
    "s_add_u32 s69, s69, 6131712\n\t" \
    "s_add_u32 s69, s69, s82\n\t" \
    "s_add_u32 s70, s65, -1\n\t" \
    "v_mov_b32 v140, s69\n\t" \
    "v_mov_b32 v141, s70\n\t" \
    "s_mov_b64 exec, 1\n\t" \
    "global_store_dword v140, v141, s[36:37] sc1\n\t" \
    "s_mov_b64 exec, -1\n\t" \
    "vv_SF32_%=:\n\t" \
    "v_mov_b32 v72, 0\n\t" \
    "v_mov_b32 v73, 0\n\t" \
    "v_mov_b32 v74, 0\n\t" \
    "v_mov_b32 v75, 0\n\t" \
    "v_mov_b32 v76, 0\n\t" \
    "v_mov_b32 v77, 0\n\t" \
    "v_mov_b32 v78, 0\n\t" \
    "v_mov_b32 v79, 0\n\t" \
    "v_mov_b32 v92, 0\n\t" \
    "v_mov_b32 v93, 0\n\t" \
    "v_mov_b32 v94, 0\n\t" \
    "v_mov_b32 v95, 0\n\t" \
    "v_mov_b32 v96, 0\n\t" \
    "v_mov_b32 v97, 0\n\t" \
    "v_mov_b32 v98, 0\n\t" \
    "v_mov_b32 v99, 0\n\t" \
    "v_mov_b32 v100, 0\n\t" \
    "v_mov_b32 v101, 0\n\t" \
    "v_mov_b32 v102, 0\n\t" \
    "v_mov_b32 v103, 0\n\t" \
    "v_mov_b32 v104, 0\n\t" \
    "v_mov_b32 v105, 0\n\t" \
    "v_mov_b32 v106, 0\n\t" \
    "v_mov_b32 v107, 0\n\t" \
    "v_mov_b32 v108, 0\n\t" \
    "v_mov_b32 v109, 0\n\t" \
    "v_mov_b32 v110, 0\n\t" \
    "v_mov_b32 v111, 0\n\t" \
    "v_mov_b32 v112, 0\n\t" \
    "v_mov_b32 v113, 0\n\t" \
    "v_mov_b32 v114, 0\n\t" \
    "v_mov_b32 v115, 0\n\t" \
    "v_mov_b32 v116, 0\n\t" \
    "v_mov_b32 v117, 0\n\t" \
    "v_mov_b32 v118, 0\n\t" \
    "v_mov_b32 v119, 0\n\t" \
    "v_mov_b32 v120, 0\n\t" \
    "v_mov_b32 v121, 0\n\t" \
    "v_mov_b32 v122, 0\n\t" \
    "v_mov_b32 v123, 0\n\t" \
    "v_mov_b32 v124, 0\n\t" \
    "v_mov_b32 v125, 0\n\t" \
    "v_mov_b32 v126, 0\n\t" \
    "v_mov_b32 v127, 0\n\t" \
    "s_add_u32 s79, s81, 400\n\t" \
    "s_sub_u32 s70, s79, s74\n\t" \
    "s_max_i32 s70, s70, 0\n\t" \
    "s_min_i32 s70, s70, 16000\n\t" \
    "s_lshl_b32 s70, s70, 1\n\t" \
    "s_mul_hi_u32 s70, s70, s77\n\t" \
    "s_add_u32 s70, s70, s73\n\t" \
    "vv_W36_%=:\n\t" \
    "s_memtime s[56:57]\n\t" \
    "s_waitcnt lgkmcnt(0)\n\t" \
    "s_sub_u32 s69, s70, s56\n\t" \
    "s_cmp_gt_i32 s69, 24\n\t" \
    "s_cbranch_scc1 vv_W36_%=\n\t" \
    "global_store_dwordx4 v14, v[148:151], s[42:43] sc1\n\t" \
    "global_store_dwordx4 v15, v[148:151], s[42:43] sc1\n\t" \
    "global_store_dword v16, v148, s[42:43] sc1\n\t" \
    "s_cmp_lg_u32 s65, 0\n\t" \
    "s_cbranch_scc1 vv_Oc35_%=\n\t" \
    "s_add_u32 s79, s81, 240\n\t" \
    "s_sub_u32 s70, s79, s74\n\t" \
    "s_max_i32 s70, s70, 0\n\t" \
    "s_min_i32 s70, s70, 16000\n\t" \
    "s_lshl_b32 s70, s70, 1\n\t" \
    "s_mul_hi_u32 s70, s70, s77\n\t" \
    "s_add_u32 s70, s70, s73\n\t" \
    "vv_W37_%=:\n\t" \
    "s_memtime s[56:57]\n\t" \
    "s_waitcnt lgkmcnt(0)\n\t" \
    "s_sub_u32 s69, s70, s56\n\t" \
    "s_cmp_gt_i32 s69, 24\n\t" \
    "s_cbranch_scc1 vv_W37_%=\n\t" \
    "global_load_dword v140, v35, s[46:47] sc1\n\t" \
    "s_waitcnt vmcnt(0)\n\t" \
    "v_cmp_eq_u32 vcc, -1, v140\n\t" \
    "s_cbranch_vccz vv_Ook34_%=\n\t" \
    "vv_O33_%=:\n\t" \
    "global_load_dword v140, v35, s[46:47] offset:1536 sc1\n\t" \
    "s_waitcnt vmcnt(0)\n\t" \
    "v_cmp_eq_u32 vcc, -1, v140\n\t" \
    "s_cbranch_vccz vv_Ook34_%=\n\t" \
    "s_cmp_eq_u32 s68, 0\n\t" \
    "s_cbranch_scc1 vv_F38_%=\n\t" \
    "s_sub_u32 s68, s68, 1\n\t" \
    "s_sleep 3\n\t" \
    "s_branch vv_O33_%=\n\t" \
    "vv_F38_%=:\n\t" \
    "v_mov_b32 v141, 6095104\n\t" \
    "v_mov_b32 v142, 17\n\t" \
    "global_store_dword v141, v142, s[36:37] sc1\n\t" \
    "s_waitcnt vmcnt(0)\n\t" \
    "s_branch vv_Ook34_%=\n\t" \
    "vv_Ook34_%=:\n\t" \
    "v_subrev_u32 v140, s80, v140\n\t" \
    "s_nop 1\n\t" \
    "v_max_i32_dpp v140, v140, v140 row_ror:1 row_mask:0xf bank_mask:0xf\n\t" \
    "s_nop 1\n\t" \
    "v_max_i32_dpp v140, v140, v140 row_ror:2 row_mask:0xf bank_mask:0xf\n\t" \
    "s_nop 1\n\t" \
    "v_max_i32_dpp v140, v140, v140 row_ror:4 row_mask:0xf bank_mask:0xf\n\t" \
    "s_nop 1\n\t" \
    "v_max_i32_dpp v140, v140, v140 row_ror:8 row_mask:0xf bank_mask:0xf\n\t" \
    "s_nop 0\n\t" \
    "v_readlane_b32 s69, v140, 0\n\t" \
    "v_readlane_b32 s71, v140, 16\n\t" \
    "v_readlane_b32 s70, v140, 32\n\t" \
    "v_readlane_b32 s81, v140, 48\n\t" \
    "s_max_i32 s69, s69, s71\n\t" \
    "s_nop 0\n\t" \
    "s_max_i32 s71, s70, s81\n\t" \
    "s_max_i32 s69, s69, s71\n\t" \
    "s_add_u32 s81, s69, s80\n\t" \
    "vv_Oc35_%=:\n\t" \
    "s_mul_hi_u32 s69, s85, s78\n\t" \
    "s_add_u32 s79, s81, s69\n\t" \
    "s_add_u32 s79, s79, s87\n\t" \
    "s_cmp_ge_u32 s65, 36\n\t" \
    "s_cselect_b32 s69, 28, 0\n\t" \
    "s_sub_u32 s79, s79, s69\n\t" \
    "s_add_u32 s80, s79, s88\n\t" \
    "s_add_u32 s65, s65, 1\n\t" \
    "s_branch vv_go15_%=\n\t" \
    "vv_Gbad20_%=:\n\t" \
    "s_waitcnt vmcnt(0) lgkmcnt(0)\n\t" \
    "s_nop 7\n\t" \
    "v_mov_b32 v72, 0\n\t" \
    "v_mov_b32 v73, 0\n\t" \
    "v_mov_b32 v74, 0\n\t" \
    "v_mov_b32 v75, 0\n\t" \
    "v_mov_b32 v76, 0\n\t" \
    "v_mov_b32 v77, 0\n\t" \
    "v_mov_b32 v78, 0\n\t" \
    "v_mov_b32 v79, 0\n\t" \
    "s_cmp_eq_u32 s68, 0\n\t" \
    "s_cbranch_scc1 vv_F39_%=\n\t" \
    "s_sub_u32 s68, s68, 1\n\t" \
    "s_branch vv_G19_%=\n\t" \
    "vv_F39_%=:\n\t" \
    "v_mov_b32 v140, 6095104\n\t" \
    "v_mov_b32 v141, 1\n\t" \
    "global_store_dword v140, v141, s[36:37] sc1\n\t" \
    "s_waitcnt vmcnt(0)\n\t" \
    "s_branch vv_Gc22_%=\n\t" \
    "vv_Gbad221_%=:\n\t" \
    "global_load_dwordx4 v[44:47], v2, s[46:47] sc1\n\t" \
    "s_waitcnt vmcnt(0)\n\t" \
    "v_max3_u32 v140, v44, v45, v46\n\t" \
    "v_max_u32 v140, v140, v47\n\t" \
    "v_cmp_eq_u32 vcc, -1, v140\n\t" \
    "s_cbranch_vccz vv_Gre224_%=\n\t" \
    "s_cmp_eq_u32 s68, 0\n\t" \
    "s_cbranch_scc1 vv_F40_%=\n\t" \
    "s_sub_u32 s68, s68, 1\n\t" \
    "s_sleep 1\n\t" \
    "s_branch vv_Gbad221_%=\n\t" \
    "vv_F40_%=:\n\t" \
    "v_mov_b32 v140, 6095104\n\t" \
    "v_mov_b32 v141, 2\n\t" \
    "global_store_dword v140, v141, s[36:37] sc1\n\t" \
    "s_waitcnt vmcnt(0)\n\t" \
    "s_branch vv_Gre224_%=\n\t" \
    "vv_Gre224_%=:\n\t" \
    "v_mov_b32 v72, 0\n\t" \
    "v_mov_b32 v73, 0\n\t" \
    "v_mov_b32 v74, 0\n\t" \
    "v_mov_b32 v75, 0\n\t" \
    "v_mov_b32 v76, 0\n\t" \
    "v_mov_b32 v77, 0\n\t" \
    "v_mov_b32 v78, 0\n\t" \
    "v_mov_b32 v79, 0\n\t" \
    "v_smfmac_f32_16x16x64_bf16 v[72:75], v[36:39], a[0:7], v10\n\t" \
    "s_nop 0\n\t" \
    "v_smfmac_f32_16x16x64_bf16 v[76:79], v[36:39], a[72:79], v10\n\t" \
    "v_smfmac_f32_16x16x64_bf16 v[72:75], v[40:43], a[8:15], v10\n\t" \
    "v_smfmac_f32_16x16x64_bf16 v[76:79], v[40:43], a[80:87], v10\n\t" \
    "v_smfmac_f32_16x16x64_bf16 v[72:75], v[44:47], a[64:71], v10\n\t" \
    "v_smfmac_f32_16x16x64_bf16 v[76:79], v[44:47], a[136:143], v10\n\t" \
    "s_branch vv_Gc223_%=\n\t" \
    "vv_end14_%=:\n\t" \
    "s_endpgm\n\t" \
    "vv_loop11_%=:\n\t" \
    "v_mov_b32 v72, 0\n\t" \
    "v_mov_b32 v73, 0\n\t" \
    "v_mov_b32 v74, 0\n\t" \
    "v_mov_b32 v75, 0\n\t" \
    "v_mov_b32 v76, 0\n\t" \
    "v_mov_b32 v77, 0\n\t" \
    "v_mov_b32 v78, 0\n\t" \
    "v_mov_b32 v79, 0\n\t" \
    "v_mov_b32 v92, 0\n\t" \
    "v_mov_b32 v93, 0\n\t" \
    "v_mov_b32 v94, 0\n\t" \
    "v_mov_b32 v95, 0\n\t" \
    "v_mov_b32 v96, 0\n\t" \
    "v_mov_b32 v97, 0\n\t" \
    "v_mov_b32 v98, 0\n\t" \
    "v_mov_b32 v99, 0\n\t" \
    "v_mov_b32 v100, 0\n\t" \
    "v_mov_b32 v101, 0\n\t" \
    "v_mov_b32 v102, 0\n\t" \
    "v_mov_b32 v103, 0\n\t" \
    "v_mov_b32 v104, 0\n\t" \
    "v_mov_b32 v105, 0\n\t" \
    "v_mov_b32 v106, 0\n\t" \
    "v_mov_b32 v107, 0\n\t" \
    "v_mov_b32 v108, 0\n\t" \
    "v_mov_b32 v109, 0\n\t" \
    "v_mov_b32 v110, 0\n\t" \
    "v_mov_b32 v111, 0\n\t" \
    "v_mov_b32 v112, 0\n\t" \
    "v_mov_b32 v113, 0\n\t" \
    "v_mov_b32 v114, 0\n\t" \
    "v_mov_b32 v115, 0\n\t" \
    "v_mov_b32 v116, 0\n\t" \
    "v_mov_b32 v117, 0\n\t" \
    "v_mov_b32 v118, 0\n\t" \
    "v_mov_b32 v119, 0\n\t" \
    "v_mov_b32 v120, 0\n\t" \
    "v_mov_b32 v121, 0\n\t" \
    "v_mov_b32 v122, 0\n\t" \
    "v_mov_b32 v123, 0\n\t" \
    "v_mov_b32 v124, 0\n\t" \
    "v_mov_b32 v125, 0\n\t" \
    "v_mov_b32 v126, 0\n\t" \
    "v_mov_b32 v127, 0\n\t" \
    "vv_go42_%=:\n\t" \
    "s_add_u32 s69, s65, 0\n\t" \
    "s_bitcmp1_b32 s69, 0\n\t" \
    "s_cselect_b32 s70, s63, s62\n\t" \
    "s_bfe_u32 s69, s69, 0x20001\n\t" \
    "s_lshl_b32 s69, s69, 19\n\t" \
    "s_add_u32 s69, s69, s70\n\t" \
    "s_add_u32 s46, s36, s69\n\t" \
    "s_addc_u32 s47, s37, 0\n\t" \
    "s_add_u32 s69, s65, 0\n\t" \
    "s_mul_i32 s70, s69, 171\n\t" \
    "s_lshr_b32 s70, s70, 9\n\t" \
    "s_mul_i32 s70, s70, 3\n\t" \
    "s_sub_u32 s69, s69, s70\n\t" \
    "s_mul_i32 s69, s69, 36\n\t" \
    "s_lshl_b32 s69, s69, 14\n\t" \
    "s_add_u32 s69, s69, s64\n\t" \
    "s_add_u32 s69, s69, 4194304\n\t" \
    "s_add_u32 s38, s36, s69\n\t" \
    "s_addc_u32 s39, s37, 0\n\t" \
    "s_add_u32 s69, s65, 2\n\t" \
    "s_mul_i32 s70, s69, 171\n\t" \
    "s_lshr_b32 s70, s70, 9\n\t" \
    "s_mul_i32 s70, s70, 3\n\t" \
    "s_sub_u32 s69, s69, s70\n\t" \
    "s_mul_i32 s69, s69, 36\n\t" \
    "s_lshl_b32 s69, s69, 14\n\t" \
    "s_add_u32 s69, s69, s64\n\t" \
    "s_add_u32 s69, s69, 4194304\n\t" \
    "s_add_u32 s42, s36, s69\n\t" \
    "s_addc_u32 s43, s37, 0\n\t" \
    "s_mul_i32 s69, s65, 9216\n\t" \
    "s_add_u32 s69, s69, 123731968\n\t" \
    "s_add_u32 s40, s36, s69\n\t" \
    "s_addc_u32 s41, s37, 0\n\t" \
    "s_sub_u32 s70, s79, s74\n\t" \
    "s_max_i32 s70, s70, 0\n\t" \
    "s_min_i32 s70, s70, 16000\n\t" \
    "s_lshl_b32 s70, s70, 1\n\t" \
    "s_mul_hi_u32 s70, s70, s77\n\t" \
    "s_add_u32 s70, s70, s73\n\t" \
    "s_sub_u32 s70, s70, 100\n\t" \
    "s_getpc_b64 s[30:31]\n\t" \
    "vv_Wp44_%=:\n\t" \
    "s_add_u32 s30, s30, vv_We45_%=-vv_Wp44_%=\n\t" \
    "s_addc_u32 s31, s31, 0\n\t" \
    "vv_W43_%=:\n\t" \
    "s_memtime s[56:57]\n\t" \
    "s_waitcnt lgkmcnt(0)\n\t" \
    "s_sub_u32 s69, s70, s56\n\t" \
    "s_cmp_gt_i32 s69, 111\n\t" \
    "s_cbranch_scc1 vv_W43_%=\n\t" \
    "s_max_i32 s69, s69, 0\n\t" \
    "s_lshr_b32 s69, s69, 1\n\t" \
    "s_and_b32 s69, s69, 0x3c\n\t" \
    "s_sub_u32 s30, s30, s69\n\t" \
    "s_subb_u32 s31, s31, 0\n\t" \
    "s_setpc_b64 s[30:31]\n\t" \
    ".p2align 6\n\t" \
    "s_nop 0\n\t" \
    "s_nop 0\n\t" \
    "s_nop 0\n\t" \
    "s_nop 1\n\t" \
    "s_nop 1\n\t" \
    "s_nop 1\n\t" \
    "s_nop 1\n\t" \
    "s_nop 1\n\t" \
    "s_nop 1\n\t" \
    "s_nop 1\n\t" \
    "s_nop 1\n\t" \
    "s_nop 1\n\t" \
    "s_nop 1\n\t" \
    "s_nop 1\n\t" \
    "s_nop 1\n\t" \
    "s_nop 1\n\t" \
    "vv_We45_%=:\n\t" \
    "vv_G46_%=:\n\t" \
    "global_load_dwordx4 v[36:39], v1, s[46:47] sc1\n\t" \
    "s_nop 9\n\t" \
    "global_load_dwordx4 v[40:43], v1, s[46:47] offset:1024 sc1\n\t" \
    "s_nop 9\n\t" \
    "global_load_dwordx4 v[44:47], v2, s[46:47] sc1\n\t" \
    "s_waitcnt vmcnt(2)\n\t" \
    "v_smfmac_f32_16x16x64_bf16 v[72:75], v[36:39], a[16:23], v10\n\t" \
    "ds_write_b128 v0, v[36:39] offset:2048\n\t" \
    "v_smfmac_f32_16x16x64_bf16 v[76:79], v[36:39], a[88:95], v10\n\t" \
    "v_max3_u32 v140, v36, v37, v38\n\t" \
    "s_waitcnt vmcnt(1)\n\t" \
    "ds_write_b128 v0, v[40:43] offset:3072\n\t" \
    "v_smfmac_f32_16x16x64_bf16 v[72:75], v[40:43], a[24:31], v10\n\t" \
    "v_max3_u32 v140, v140, v39, v40\n\t" \
    "v_max3_u32 v140, v140, v41, v42\n\t" \
    "v_max_u32 v140, v140, v43\n\t" \
    "v_cmp_eq_u32 vcc, -1, v140\n\t" \
    "v_smfmac_f32_16x16x64_bf16 v[76:79], v[40:43], a[96:103], v10\n\t" \
    "s_cbranch_vccnz vv_Gbad47_%=\n\t" \
    "vv_Gc49_%=:\n\t" \
    "s_waitcnt lgkmcnt(0)\n\t" \
    "s_barrier\n\t" \
    "ds_read_b128 v[48:51], v0 offset:4096\n\t" \
    "ds_read_b128 v[52:55], v0 offset:5120\n\t" \
    "s_waitcnt vmcnt(0)\n\t" \
    "v_smfmac_f32_16x16x64_bf16 v[72:75], v[44:47], a[64:71], v10\n\t" \
    "ds_read_b128 v[56:59], v0 offset:6144\n\t" \
    "ds_read_b128 v[60:63], v0 offset:7168\n\t" \
    "v_max3_u32 v140, v44, v45, v46\n\t" \
    "v_max_u32 v140, v140, v47\n\t" \
    "v_smfmac_f32_16x16x64_bf16 v[76:79], v[44:47], a[136:143], v10\n\t" \
    "ds_read_b128 v[64:67], v0 offset:0\n\t" \
    "ds_read_b128 v[68:71], v0 offset:1024\n\t" \
    "v_cmp_eq_u32 vcc, -1, v140\n\t" \
    "s_cbranch_vccnz vv_Gbad248_%=\n\t" \
    "vv_Gc250_%=:\n\t" \
    "s_waitcnt lgkmcnt(5)\n\t" \
    "v_smfmac_f32_16x16x64_bf16 v[72:75], v[48:51], a[32:39], v10\n\t" \
    "s_waitcnt lgkmcnt(4)\n\t" \
    "v_smfmac_f32_16x16x64_bf16 v[72:75], v[52:55], a[40:47], v10\n\t" \
    "s_waitcnt lgkmcnt(3)\n\t" \
    "v_smfmac_f32_16x16x64_bf16 v[72:75], v[56:59], a[48:55], v10\n\t" \
    "s_waitcnt lgkmcnt(2)\n\t" \
    "v_smfmac_f32_16x16x64_bf16 v[72:75], v[60:63], a[56:63], v10\n\t" \
    "s_waitcnt lgkmcnt(1)\n\t" \
    "v_smfmac_f32_16x16x64_bf16 v[72:75], v[64:67], a[0:7], v10\n\t" \
    "s_waitcnt lgkmcnt(0)\n\t" \
    "v_smfmac_f32_16x16x64_bf16 v[72:75], v[68:71], a[8:15], v10\n\t" \
    "v_smfmac_f32_16x16x64_bf16 v[76:79], v[48:51], a[104:111], v10\n\t" \
    "s_nop 1\n\t" \
    "s_nop 0\n\t" \
    "v_smfmac_f32_16x16x64_bf16 v[76:79], v[52:55], a[112:119], v10\n\t" \
    "s_nop 1\n\t" \
    "s_nop 0\n\t" \
    "v_smfmac_f32_16x16x64_bf16 v[76:79], v[56:59], a[120:127], v10\n\t" \
    "v_add_f32 v72, v72, v74\n\t" \
    "v_smfmac_f32_16x16x64_bf16 v[76:79], v[60:63], a[128:135], v10\n\t" \
    "v_add_f32 v73, v73, v75\n\t" \
    "v_smfmac_f32_16x16x64_bf16 v[76:79], v[64:67], a[72:79], v10\n\t" \
    "v_cvt_pk_bf16_f32 v80, v72, v73\n\t" \
    "v_smfmac_f32_16x16x64_bf16 v[76:79], v[68:71], a[80:87], v10\n\t" \
    "ds_write_b32 v12, v80 offset:0\n\t" \
    "s_nop 6\n\t" \
    "v_add_f32 v76, v76, v78\n\t" \
    "v_add_f32 v77, v77, v79\n\t" \
    "v_cvt_pk_bf16_f32 v81, v76, v77\n\t" \
    "ds_write_b32 v12, v81 offset:64\n\t" \
    "s_waitcnt lgkmcnt(0)\n\t" \
    "s_barrier\n\t" \
    "ds_read_b64_tr_b16 v[84:85], v13 offset:0\n\t" \
    "ds_read_b64_tr_b16 v[86:87], v13 offset:128\n\t" \
    "ds_read_b64_tr_b16 v[88:89], v13 offset:1024\n\t" \
    "ds_read_b64_tr_b16 v[90:91], v13 offset:1152\n\t" \
    "s_waitcnt lgkmcnt(2)\n\t" \
    "v_smfmac_f32_16x16x64_bf16 v[92:95], v[84:87], a[144:151], v10\n\t" \
    "s_nop 2\n\t" \
    "s_waitcnt lgkmcnt(0)\n\t" \
    "v_smfmac_f32_16x16x64_bf16 v[92:95], v[88:91], a[152:159], v10\n\t" \
    "s_nop 2\n\t" \
    "v_smfmac_f32_16x16x64_bf16 v[96:99], v[84:87], a[160:167], v10\n\t" \
    "s_nop 2\n\t" \
    "v_smfmac_f32_16x16x64_bf16 v[96:99], v[88:91], a[168:175], v10\n\t" \
    "s_nop 2\n\t" \
    "v_smfmac_f32_16x16x64_bf16 v[100:103], v[84:87], a[176:183], v10\n\t" \
    "s_nop 0\n\t" \
    "v_add_f32 v92, v92, v94\n\t" \
    "v_add_f32 v93, v93, v95\n\t" \
    "v_smfmac_f32_16x16x64_bf16 v[100:103], v[88:91], a[184:191], v10\n\t" \
    "s_nop 1\n\t" \
    "v_cvt_pk_bf16_f32 v128, v92, v93\n\t" \
    "v_smfmac_f32_16x16x64_bf16 v[104:107], v[84:87], a[192:199], v10\n\t" \
    "s_nop 0\n\t" \
    "v_add_f32 v96, v96, v98\n\t" \
    "v_add_f32 v97, v97, v99\n\t" \
    "v_smfmac_f32_16x16x64_bf16 v[104:107], v[88:91], a[200:207], v10\n\t" \
    "s_nop 1\n\t" \
    "v_cvt_pk_bf16_f32 v129, v96, v97\n\t" \
    "v_smfmac_f32_16x16x64_bf16 v[124:127], v[84:87], v[240:247], v10\n\t" \
    "s_nop 0\n\t" \
    "v_add_f32 v100, v100, v102\n\t" \
    "v_add_f32 v101, v101, v103\n\t" \
    "v_smfmac_f32_16x16x64_bf16 v[124:127], v[88:91], v[248:255], v10\n\t" \
    "s_nop 1\n\t" \
    "global_load_dword v140, v20, s[46:47] sc1\n\t" \
    "v_cvt_pk_bf16_f32 v130, v100, v101\n\t" \
    "v_smfmac_f32_16x16x64_bf16 v[108:111], v[84:87], a[208:215], v10\n\t" \
    "s_nop 0\n\t" \
    "v_add_f32 v104, v104, v106\n\t" \
    "v_add_f32 v105, v105, v107\n\t" \
    "v_smfmac_f32_16x16x64_bf16 v[108:111], v[88:91], a[216:223], v10\n\t" \
    "s_nop 0\n\t" \
    "v_cvt_pk_bf16_f32 v131, v104, v105\n\t" \
    "v_permlane32_swap_b32 v128, v130\n\t" \
    "v_smfmac_f32_16x16x64_bf16 v[112:115], v[84:87], a[224:231], v10\n\t" \
    "s_nop 0\n\t" \
    "v_permlane32_swap_b32 v129, v131\n\t" \
    "s_nop 1\n\t" \
    "v_permlane16_swap_b32 v128, v129\n\t" \
    "v_smfmac_f32_16x16x64_bf16 v[112:115], v[88:91], a[232:239], v10\n\t" \
    "v_permlane16_swap_b32 v130, v131\n\t" \
    "global_store_dwordx4 v14, v[128:131], s[38:39] sc1\n\t" \
    "v_add_f32 v124, v124, v126\n\t" \
    "v_smfmac_f32_16x16x64_bf16 v[116:119], v[84:87], a[240:247], v10\n\t" \
    "s_nop 0\n\t" \
    "v_add_f32 v125, v125, v127\n\t" \
    "v_cvt_pk_bf16_f32 v136, v124, v125\n\t" \
    "v_smfmac_f32_16x16x64_bf16 v[116:119], v[88:91], a[248:255], v10\n\t" \
    "global_store_dword v16, v136, s[38:39] sc1\n\t" \
    "v_add_f32 v108, v108, v110\n\t" \
    "v_add_f32 v109, v109, v111\n\t" \
    "v_smfmac_f32_16x16x64_bf16 v[120:123], v[84:87], v[224:231], v10\n\t" \
    "s_nop 0\n\t" \
    "v_cvt_pk_bf16_f32 v132, v108, v109\n\t" \
    "v_add_f32 v112, v112, v114\n\t" \
    "v_smfmac_f32_16x16x64_bf16 v[120:123], v[88:91], v[232:239], v10\n\t" \
    "v_add_f32 v113, v113, v115\n\t" \
    "v_cvt_pk_bf16_f32 v133, v112, v113\n\t" \
    "v_add_f32 v116, v116, v118\n\t" \
    "v_add_f32 v117, v117, v119\n\t" \
    "v_cvt_pk_bf16_f32 v134, v116, v117\n\t" \
    "s_nop 2\n\t" \
    "v_pk_add_f32 v[120:121], v[120:121], v[122:123]\n\t" \
    "v_cvt_pk_bf16_f32 v135, v120, v121\n\t" \
    "v_permlane32_swap_b32 v132, v134\n\t" \
    "s_nop 0\n\t" \
    "v_permlane32_swap_b32 v133, v135\n\t" \
    "s_nop 1\n\t" \
    "v_permlane16_swap_b32 v132, v133\n\t" \
    "v_permlane16_swap_b32 v134, v135\n\t" \
    "global_store_dwordx4 v15, v[132:135], s[38:39] sc1\n\t" \
    "s_nop 1\n\t" \
    "global_store_dwordx4 v17, v[128:131], s[40:41] sc1\n\t" \
    "global_store_dwordx4 v17, v[132:135], s[40:41] offset:1024 sc1\n\t" \
    "global_store_dword v18, v136, s[40:41] sc1\n\t" \
    "s_cmp_eq_u32 s65, 63\n\t" \
    "s_cbranch_scc1 vv_end41_%=\n\t" \
    "s_mov_b32 s71, 3\n\t" \
    "vv_E52_%=:\n\t" \
    "s_memtime s[54:55]\n\t" \
    "s_nop 0\n\t" \
    "s_memrealtime s[4:5]\n\t" \
    "s_nop 0\n\t" \
    "s_memrealtime s[6:7]\n\t" \
    "s_nop 0\n\t" \
    "s_memrealtime s[8:9]\n\t" \
    "s_nop 0\n\t" \
    "s_memrealtime s[10:11]\n\t" \
    "s_nop 0\n\t" \
    "s_memrealtime s[12:13]\n\t" \
    "s_nop 0\n\t" \
    "s_memrealtime s[14:15]\n\t" \
    "s_nop 0\n\t" \
    "s_memrealtime s[16:17]\n\t" \
    "s_nop 0\n\t" \
    "s_memrealtime s[18:19]\n\t" \
    "s_nop 0\n\t" \
    "s_memrealtime s[20:21]\n\t" \
    "s_nop 0\n\t" \
    "s_memrealtime s[22:23]\n\t" \
    "s_nop 0\n\t" \
    "s_memrealtime s[24:25]\n\t" \
    "s_nop 0\n\t" \
    "s_memrealtime s[26:27]\n\t" \
    "s_nop 0\n\t" \
    "s_memrealtime s[28:29]\n\t" \
    "s_nop 0\n\t" \
    "s_memtime s[56:57]\n\t" \
    "s_mov_b32 s70, 1\n\t" \
    "s_sub_u32 s71, s71, 1\n\t" \
    "s_cselect_b32 s69, 0, -1\n\t" \
    "s_waitcnt lgkmcnt(0)\n\t" \
    "s_sub_u32 s54, s56, s54\n\t" \
    "s_and_b32 s54, s54, s69\n\t" \
    "s_cmp_gt_u32 s54, 124\n\t" \
    "s_cbranch_scc1 vv_E52_%=\n\t" \
    "s_cmp_eq_u32 s4, s28\n\t" \
    "s_addc_u32 s70, s70, 0\n\t" \
    "s_cmp_eq_u32 s6, s28\n\t" \
    "s_addc_u32 s70, s70, 0\n\t" \
    "s_cmp_eq_u32 s8, s28\n\t" \
    "s_addc_u32 s70, s70, 0\n\t" \
    "s_cmp_eq_u32 s10, s28\n\t" \
    "s_addc_u32 s70, s70, 0\n\t" \
    "s_cmp_eq_u32 s12, s28\n\t" \
    "s_addc_u32 s70, s70, 0\n\t" \
    "s_cmp_eq_u32 s14, s28\n\t" \
    "s_addc_u32 s70, s70, 0\n\t" \
    "s_cmp_eq_u32 s16, s28\n\t" \
    "s_addc_u32 s70, s70, 0\n\t" \
    "s_cmp_eq_u32 s18, s28\n\t" \
    "s_addc_u32 s70, s70, 0\n\t" \
    "s_cmp_eq_u32 s20, s28\n\t" \
    "s_addc_u32 s70, s70, 0\n\t" \
    "s_cmp_eq_u32 s22, s28\n\t" \
    "s_addc_u32 s70, s70, 0\n\t" \
    "s_cmp_eq_u32 s24, s28\n\t" \
    "s_addc_u32 s70, s70, 0\n\t" \
    "s_cmp_eq_u32 s26, s28\n\t" \
    "s_addc_u32 s70, s70, 0\n\t" \
    "s_mov_b32 s75, s73\n\t" \
    "s_mov_b32 s76, s74\n\t" \
    "s_lshl_b32 s70, s70, 3\n\t" \
    "s_sub_u32 s73, s56, s70\n\t" \
    "s_lshl_b32 s74, s28, 4\n\t" \
    "s_sub_u32 s69, s73, s75\n\t" \
    "s_sub_u32 s70, s74, s76\n\t" \
    "s_sub_u32 s71, s70, 1600\n\t" \
    "s_cmp_lt_u32 s71, 400000\n\t" \
    "s_cbranch_scc0 vv_K54_%=\n\t" \
    "v_cvt_f32_u32 v141, s69\n\t" \
    "v_cvt_f32_u32 v142, s70\n\t" \
    "v_rcp_f32 v143, v141\n\t" \
    "v_rcp_f32 v144, v142\n\t" \
    "v_mul_f32 v143, v143, v142\n\t" \
    "v_mul_f32 v144, v144, v141\n\t" \
    "v_mul_f32 v143, 0x4f800000, v143\n\t" \
    "v_mul_f32 v144, 0x4f000000, v144\n\t" \
    "v_cvt_u32_f32 v143, v143\n\t" \
    "v_cvt_u32_f32 v144, v144\n\t" \
    "v_readfirstlane_b32 s69, v143\n\t" \
    "v_readfirstlane_b32 s70, v144\n\t" \
    "s_nop 1\n\t" \
    "s_sub_u32 s69, s69, s78\n\t" \
    "s_ashr_i32 s69, s69, 2\n\t" \
    "s_add_u32 s78, s78, s69\n\t" \
    "s_sub_u32 s70, s70, s77\n\t" \
    "s_ashr_i32 s70, s70, 2\n\t" \
    "s_add_u32 s77, s77, s70\n\t" \
    "vv_K54_%=:\n\t" \
    "s_waitcnt vmcnt(6)\n\t" \
    "v_cmp_eq_u32 vcc, -1, v140\n\t" \
    "s_cbranch_vccz vv_Hok56_%=\n\t" \
    "vv_H55_%=:\n\t" \
    "global_load_dword v140, v20, s[46:47] sc1\n\t" \
    "s_waitcnt vmcnt(0)\n\t" \
    "v_cmp_eq_u32 vcc, -1, v140\n\t" \
    "s_cbranch_vccz vv_Hc57_%=\n\t" \
    "s_cmp_eq_u32 s68, 0\n\t" \
    "s_cbranch_scc1 vv_F58_%=\n\t" \
    "s_sub_u32 s68, s68, 1\n\t" \
    "s_sleep 5\n\t" \
    "s_branch vv_H55_%=\n\t" \
    "vv_F58_%=:\n\t" \
    "v_mov_b32 v141, 6095104\n\t" \
    "v_mov_b32 v142, 6\n\t" \
    "global_store_dword v141, v142, s[36:37] sc1\n\t" \
    "s_waitcnt vmcnt(0)\n\t" \
    "s_branch vv_Hc57_%=\n\t" \
    "vv_Hc57_%=:\n\t" \
    "vv_Hok56_%=:\n\t" \
    "v_subrev_u32 v140, s80, v140\n\t" \
    "v_and_b32 v20, -5, v20\n\t" \
    "s_nop 0\n\t" \
    "v_max_i32_dpp v140, v140, v140 row_ror:1 row_mask:0xf bank_mask:0xf\n\t" \
    "s_nop 1\n\t" \
    "v_max_i32_dpp v140, v140, v140 row_ror:2 row_mask:0xf bank_mask:0xf\n\t" \
    "s_nop 1\n\t" \
    "v_max_i32_dpp v140, v140, v140 row_ror:4 row_mask:0xf bank_mask:0xf\n\t" \
    "s_nop 1\n\t" \
    "v_max_i32_dpp v140, v140, v140 row_ror:8 row_mask:0xf bank_mask:0xf\n\t" \
    "s_nop 0\n\t" \
    "v_readlane_b32 s69, v140, 0\n\t" \
    "v_readlane_b32 s71, v140, 16\n\t" \
    "v_readlane_b32 s70, v140, 32\n\t" \
    "v_readlane_b32 s81, v140, 48\n\t" \
    "s_max_i32 s69, s69, s71\n\t" \
    "s_nop 0\n\t" \
    "s_max_i32 s71, s70, s81\n\t" \
    "s_max_i32 s69, s69, s71\n\t" \
    "s_add_u32 s81, s69, s80\n\t" \
    "v_mov_b32 v72, 0\n\t" \
    "v_mov_b32 v73, 0\n\t" \
    "v_mov_b32 v74, 0\n\t" \
    "v_mov_b32 v75, 0\n\t" \
    "v_mov_b32 v76, 0\n\t" \
    "v_mov_b32 v77, 0\n\t" \
    "v_mov_b32 v78, 0\n\t" \
    "v_mov_b32 v79, 0\n\t" \
    "v_mov_b32 v92, 0\n\t" \
    "v_mov_b32 v93, 0\n\t" \
    "v_mov_b32 v94, 0\n\t" \
    "v_mov_b32 v95, 0\n\t" \
    "v_mov_b32 v96, 0\n\t" \
    "v_mov_b32 v97, 0\n\t" \
    "v_mov_b32 v98, 0\n\t" \
    "v_mov_b32 v99, 0\n\t" \
    "v_mov_b32 v100, 0\n\t" \
    "v_mov_b32 v101, 0\n\t" \
    "v_mov_b32 v102, 0\n\t" \
    "v_mov_b32 v103, 0\n\t" \
    "v_mov_b32 v104, 0\n\t" \
    "v_mov_b32 v105, 0\n\t" \
    "v_mov_b32 v106, 0\n\t" \
    "v_mov_b32 v107, 0\n\t" \
    "v_mov_b32 v108, 0\n\t" \
    "v_mov_b32 v109, 0\n\t" \
    "v_mov_b32 v110, 0\n\t" \
    "v_mov_b32 v111, 0\n\t" \
    "v_mov_b32 v112, 0\n\t" \
    "v_mov_b32 v113, 0\n\t" \
    "v_mov_b32 v114, 0\n\t" \
    "v_mov_b32 v115, 0\n\t" \
    "v_mov_b32 v116, 0\n\t" \
    "v_mov_b32 v117, 0\n\t" \
    "v_mov_b32 v118, 0\n\t" \
    "v_mov_b32 v119, 0\n\t" \
    "v_mov_b32 v120, 0\n\t" \
    "v_mov_b32 v121, 0\n\t" \
    "v_mov_b32 v122, 0\n\t" \
    "v_mov_b32 v123, 0\n\t" \
    "v_mov_b32 v124, 0\n\t" \
    "v_mov_b32 v125, 0\n\t" \
    "v_mov_b32 v126, 0\n\t" \
    "v_mov_b32 v127, 0\n\t" \
    "s_add_u32 s79, s81, 400\n\t" \
    "s_sub_u32 s70, s79, s74\n\t" \
    "s_max_i32 s70, s70, 0\n\t" \
    "s_min_i32 s70, s70, 16000\n\t" \
    "s_lshl_b32 s70, s70, 1\n\t" \
    "s_mul_hi_u32 s70, s70, s77\n\t" \
    "s_add_u32 s70, s70, s73\n\t" \
    "vv_W62_%=:\n\t" \
    "s_memtime s[56:57]\n\t" \
    "s_waitcnt lgkmcnt(0)\n\t" \
    "s_sub_u32 s69, s70, s56\n\t" \
    "s_cmp_gt_i32 s69, 24\n\t" \
    "s_cbranch_scc1 vv_W62_%=\n\t" \
    "global_store_dwordx4 v14, v[148:151], s[42:43] sc1\n\t" \
    "global_store_dwordx4 v15, v[148:151], s[42:43] sc1\n\t" \
    "global_store_dword v16, v148, s[42:43] sc1\n\t" \
    "s_cmp_lg_u32 s65, 0\n\t" \
    "s_cbranch_scc1 vv_Oc61_%=\n\t" \
    "s_add_u32 s79, s81, 240\n\t" \
    "s_sub_u32 s70, s79, s74\n\t" \
    "s_max_i32 s70, s70, 0\n\t" \
    "s_min_i32 s70, s70, 16000\n\t" \
    "s_lshl_b32 s70, s70, 1\n\t" \
    "s_mul_hi_u32 s70, s70, s77\n\t" \
    "s_add_u32 s70, s70, s73\n\t" \
    "vv_W63_%=:\n\t" \
    "s_memtime s[56:57]\n\t" \
    "s_waitcnt lgkmcnt(0)\n\t" \
    "s_sub_u32 s69, s70, s56\n\t" \
    "s_cmp_gt_i32 s69, 24\n\t" \
    "s_cbranch_scc1 vv_W63_%=\n\t" \
    "global_load_dword v140, v35, s[46:47] sc1\n\t" \
    "s_waitcnt vmcnt(0)\n\t" \
    "v_cmp_eq_u32 vcc, -1, v140\n\t" \
    "s_cbranch_vccz vv_Ook60_%=\n\t" \
    "vv_O59_%=:\n\t" \
    "global_load_dword v140, v35, s[46:47] offset:1536 sc1\n\t" \
    "s_waitcnt vmcnt(0)\n\t" \
    "v_cmp_eq_u32 vcc, -1, v140\n\t" \
    "s_cbranch_vccz vv_Ook60_%=\n\t" \
    "s_cmp_eq_u32 s68, 0\n\t" \
    "s_cbranch_scc1 vv_F64_%=\n\t" \
    "s_sub_u32 s68, s68, 1\n\t" \
    "s_sleep 3\n\t" \
    "s_branch vv_O59_%=\n\t" \
    "vv_F64_%=:\n\t" \
    "v_mov_b32 v141, 6095104\n\t" \
    "v_mov_b32 v142, 17\n\t" \
    "global_store_dword v141, v142, s[36:37] sc1\n\t" \
    "s_waitcnt vmcnt(0)\n\t" \
    "s_branch vv_Ook60_%=\n\t" \
    "vv_Ook60_%=:\n\t" \
    "v_subrev_u32 v140, s80, v140\n\t" \
    "s_nop 1\n\t" \
    "v_max_i32_dpp v140, v140, v140 row_ror:1 row_mask:0xf bank_mask:0xf\n\t" \
    "s_nop 1\n\t" \
    "v_max_i32_dpp v140, v140, v140 row_ror:2 row_mask:0xf bank_mask:0xf\n\t" \
    "s_nop 1\n\t" \
    "v_max_i32_dpp v140, v140, v140 row_ror:4 row_mask:0xf bank_mask:0xf\n\t" \
    "s_nop 1\n\t" \
    "v_max_i32_dpp v140, v140, v140 row_ror:8 row_mask:0xf bank_mask:0xf\n\t" \
    "s_nop 0\n\t" \
    "v_readlane_b32 s69, v140, 0\n\t" \
    "v_readlane_b32 s71, v140, 16\n\t" \
    "v_readlane_b32 s70, v140, 32\n\t" \
    "v_readlane_b32 s81, v140, 48\n\t" \
    "s_max_i32 s69, s69, s71\n\t" \
    "s_nop 0\n\t" \
    "s_max_i32 s71, s70, s81\n\t" \
    "s_max_i32 s69, s69, s71\n\t" \
    "s_add_u32 s81, s69, s80\n\t" \
    "vv_Oc61_%=:\n\t" \
    "s_mul_hi_u32 s69, s85, s78\n\t" \
    "s_add_u32 s79, s81, s69\n\t" \
    "s_add_u32 s79, s79, s87\n\t" \
    "s_cmp_ge_u32 s65, 36\n\t" \
    "s_cselect_b32 s69, 28, 0\n\t" \
    "s_sub_u32 s79, s79, s69\n\t" \
    "s_add_u32 s80, s79, s88\n\t" \
    "s_add_u32 s65, s65, 1\n\t" \
    "s_branch vv_go42_%=\n\t" \
    "vv_Gbad47_%=:\n\t" \
    "s_waitcnt vmcnt(0) lgkmcnt(0)\n\t" \
    "s_nop 7\n\t" \
    "v_mov_b32 v72, 0\n\t" \
    "v_mov_b32 v73, 0\n\t" \
    "v_mov_b32 v74, 0\n\t" \
    "v_mov_b32 v75, 0\n\t" \
    "v_mov_b32 v76, 0\n\t" \
    "v_mov_b32 v77, 0\n\t" \
    "v_mov_b32 v78, 0\n\t" \
    "v_mov_b32 v79, 0\n\t" \
    "s_cmp_eq_u32 s68, 0\n\t" \
    "s_cbranch_scc1 vv_F65_%=\n\t" \
    "s_sub_u32 s68, s68, 1\n\t" \
    "s_branch vv_G46_%=\n\t" \
    "vv_F65_%=:\n\t" \
    "v_mov_b32 v140, 6095104\n\t" \
    "v_mov_b32 v141, 1\n\t" \
    "global_store_dword v140, v141, s[36:37] sc1\n\t" \
    "s_waitcnt vmcnt(0)\n\t" \
    "s_branch vv_Gc49_%=\n\t" \
    "vv_Gbad248_%=:\n\t" \
    "global_load_dwordx4 v[44:47], v2, s[46:47] sc1\n\t" \
    "s_waitcnt vmcnt(0)\n\t" \
    "v_max3_u32 v140, v44, v45, v46\n\t" \
    "v_max_u32 v140, v140, v47\n\t" \
    "v_cmp_eq_u32 vcc, -1, v140\n\t" \
    "s_cbranch_vccz vv_Gre251_%=\n\t" \
    "s_cmp_eq_u32 s68, 0\n\t" \
    "s_cbranch_scc1 vv_F66_%=\n\t" \
    "s_sub_u32 s68, s68, 1\n\t" \
    "s_sleep 1\n\t" \
    "s_branch vv_Gbad248_%=\n\t" \
    "vv_F66_%=:\n\t" \
    "v_mov_b32 v140, 6095104\n\t" \
    "v_mov_b32 v141, 2\n\t" \
    "global_store_dword v140, v141, s[36:37] sc1\n\t" \
    "s_waitcnt vmcnt(0)\n\t" \
    "s_branch vv_Gre251_%=\n\t" \
    "vv_Gre251_%=:\n\t" \
    "v_mov_b32 v72, 0\n\t" \
    "v_mov_b32 v73, 0\n\t" \
    "v_mov_b32 v74, 0\n\t" \
    "v_mov_b32 v75, 0\n\t" \
    "v_mov_b32 v76, 0\n\t" \
    "v_mov_b32 v77, 0\n\t" \
    "v_mov_b32 v78, 0\n\t" \
    "v_mov_b32 v79, 0\n\t" \
    "v_smfmac_f32_16x16x64_bf16 v[72:75], v[36:39], a[16:23], v10\n\t" \
    "s_nop 0\n\t" \
    "v_smfmac_f32_16x16x64_bf16 v[76:79], v[36:39], a[88:95], v10\n\t" \
    "v_smfmac_f32_16x16x64_bf16 v[72:75], v[40:43], a[24:31], v10\n\t" \
    "v_smfmac_f32_16x16x64_bf16 v[76:79], v[40:43], a[96:103], v10\n\t" \
    "v_smfmac_f32_16x16x64_bf16 v[72:75], v[44:47], a[64:71], v10\n\t" \
    "v_smfmac_f32_16x16x64_bf16 v[76:79], v[44:47], a[136:143], v10\n\t" \
    "s_branch vv_Gc250_%=\n\t" \
    "vv_end41_%=:\n\t" \
    "s_endpgm\n\t" \
    "vv_loop12_%=:\n\t" \
    "v_mov_b32 v72, 0\n\t" \
    "v_mov_b32 v73, 0\n\t" \
    "v_mov_b32 v74, 0\n\t" \
    "v_mov_b32 v75, 0\n\t" \
    "v_mov_b32 v76, 0\n\t" \
    "v_mov_b32 v77, 0\n\t" \
    "v_mov_b32 v78, 0\n\t" \
    "v_mov_b32 v79, 0\n\t" \
    "v_mov_b32 v92, 0\n\t" \
    "v_mov_b32 v93, 0\n\t" \
    "v_mov_b32 v94, 0\n\t" \
    "v_mov_b32 v95, 0\n\t" \
    "v_mov_b32 v96, 0\n\t" \
    "v_mov_b32 v97, 0\n\t" \
    "v_mov_b32 v98, 0\n\t" \
    "v_mov_b32 v99, 0\n\t" \
    "v_mov_b32 v100, 0\n\t" \
    "v_mov_b32 v101, 0\n\t" \
    "v_mov_b32 v102, 0\n\t" \
    "v_mov_b32 v103, 0\n\t" \
    "v_mov_b32 v104, 0\n\t" \
    "v_mov_b32 v105, 0\n\t" \
    "v_mov_b32 v106, 0\n\t" \
    "v_mov_b32 v107, 0\n\t" \
    "v_mov_b32 v108, 0\n\t" \
    "v_mov_b32 v109, 0\n\t" \
    "v_mov_b32 v110, 0\n\t" \
    "v_mov_b32 v111, 0\n\t" \
    "v_mov_b32 v112, 0\n\t" \
    "v_mov_b32 v113, 0\n\t" \
    "v_mov_b32 v114, 0\n\t" \
    "v_mov_b32 v115, 0\n\t" \
    "v_mov_b32 v116, 0\n\t" \
    "v_mov_b32 v117, 0\n\t" \
    "v_mov_b32 v118, 0\n\t" \
    "v_mov_b32 v119, 0\n\t" \
    "v_mov_b32 v120, 0\n\t" \
    "v_mov_b32 v121, 0\n\t" \
    "v_mov_b32 v122, 0\n\t" \
    "v_mov_b32 v123, 0\n\t" \
    "v_mov_b32 v124, 0\n\t" \
    "v_mov_b32 v125, 0\n\t" \
    "v_mov_b32 v126, 0\n\t" \
    "v_mov_b32 v127, 0\n\t" \
    "vv_go68_%=:\n\t" \
    "s_add_u32 s69, s65, 0\n\t" \
    "s_bitcmp1_b32 s69, 0\n\t" \
    "s_cselect_b32 s70, s63, s62\n\t" \
    "s_bfe_u32 s69, s69, 0x20001\n\t" \
    "s_lshl_b32 s69, s69, 19\n\t" \
    "s_add_u32 s69, s69, s70\n\t" \
    "s_add_u32 s46, s36, s69\n\t" \
    "s_addc_u32 s47, s37, 0\n\t" \
    "s_add_u32 s69, s65, 0\n\t" \
    "s_mul_i32 s70, s69, 171\n\t" \
    "s_lshr_b32 s70, s70, 9\n\t" \
    "s_mul_i32 s70, s70, 3\n\t" \
    "s_sub_u32 s69, s69, s70\n\t" \
    "s_mul_i32 s69, s69, 36\n\t" \
    "s_lshl_b32 s69, s69, 14\n\t" \
    "s_add_u32 s69, s69, s64\n\t" \
    "s_add_u32 s69, s69, 4194304\n\t" \
    "s_add_u32 s38, s36, s69\n\t" \
    "s_addc_u32 s39, s37, 0\n\t" \
    "s_add_u32 s69, s65, 2\n\t" \
    "s_mul_i32 s70, s69, 171\n\t" \
    "s_lshr_b32 s70, s70, 9\n\t" \
    "s_mul_i32 s70, s70, 3\n\t" \
    "s_sub_u32 s69, s69, s70\n\t" \
    "s_mul_i32 s69, s69, 36\n\t" \
    "s_lshl_b32 s69, s69, 14\n\t" \
    "s_add_u32 s69, s69, s64\n\t" \
    "s_add_u32 s69, s69, 4194304\n\t" \
    "s_add_u32 s42, s36, s69\n\t" \
    "s_addc_u32 s43, s37, 0\n\t" \
    "s_mul_i32 s69, s65, 9216\n\t" \
    "s_add_u32 s69, s69, 123731968\n\t" \
    "s_add_u32 s40, s36, s69\n\t" \
    "s_addc_u32 s41, s37, 0\n\t" \
    "s_sub_u32 s70, s79, s74\n\t" \
    "s_max_i32 s70, s70, 0\n\t" \
    "s_min_i32 s70, s70, 16000\n\t" \
    "s_lshl_b32 s70, s70, 1\n\t" \
    "s_mul_hi_u32 s70, s70, s77\n\t" \
    "s_add_u32 s70, s70, s73\n\t" \
    "s_sub_u32 s70, s70, 100\n\t" \
    "s_getpc_b64 s[30:31]\n\t" \
    "vv_Wp70_%=:\n\t" \
    "s_add_u32 s30, s30, vv_We71_%=-vv_Wp70_%=\n\t" \
    "s_addc_u32 s31, s31, 0\n\t" \
    "vv_W69_%=:\n\t" \
    "s_memtime s[56:57]\n\t" \
    "s_waitcnt lgkmcnt(0)\n\t" \
    "s_sub_u32 s69, s70, s56\n\t" \
    "s_cmp_gt_i32 s69, 111\n\t" \
    "s_cbranch_scc1 vv_W69_%=\n\t" \
    "s_max_i32 s69, s69, 0\n\t" \
    "s_lshr_b32 s69, s69, 1\n\t" \
    "s_and_b32 s69, s69, 0x3c\n\t" \
    "s_sub_u32 s30, s30, s69\n\t" \
    "s_subb_u32 s31, s31, 0\n\t" \
    "s_setpc_b64 s[30:31]\n\t" \
    ".p2align 6\n\t" \
    "s_nop 0\n\t" \
    "s_nop 0\n\t" \
    "s_nop 0\n\t" \
    "s_nop 1\n\t" \
    "s_nop 1\n\t" \
    "s_nop 1\n\t" \
    "s_nop 1\n\t" \
    "s_nop 1\n\t" \
    "s_nop 1\n\t" \
    "s_nop 1\n\t" \
    "s_nop 1\n\t" \
    "s_nop 1\n\t" \
    "s_nop 1\n\t" \
    "s_nop 1\n\t" \
    "s_nop 1\n\t" \
    "s_nop 1\n\t" \
    "vv_We71_%=:\n\t" \
    "vv_G72_%=:\n\t" \
    "global_load_dwordx4 v[36:39], v1, s[46:47] sc1\n\t" \
    "s_nop 9\n\t" \
    "global_load_dwordx4 v[40:43], v1, s[46:47] offset:1024 sc1\n\t" \
    "s_nop 9\n\t" \
    "global_load_dwordx4 v[44:47], v2, s[46:47] sc1\n\t" \
    "s_waitcnt vmcnt(2)\n\t" \
    "v_smfmac_f32_16x16x64_bf16 v[72:75], v[36:39], a[32:39], v10\n\t" \
    "ds_write_b128 v0, v[36:39] offset:4096\n\t" \
    "v_smfmac_f32_16x16x64_bf16 v[76:79], v[36:39], a[104:111], v10\n\t" \
    "v_max3_u32 v140, v36, v37, v38\n\t" \
    "s_waitcnt vmcnt(1)\n\t" \
    "ds_write_b128 v0, v[40:43] offset:5120\n\t" \
    "v_smfmac_f32_16x16x64_bf16 v[72:75], v[40:43], a[40:47], v10\n\t" \
    "v_max3_u32 v140, v140, v39, v40\n\t" \
    "v_max3_u32 v140, v140, v41, v42\n\t" \
    "v_max_u32 v140, v140, v43\n\t" \
    "v_cmp_eq_u32 vcc, -1, v140\n\t" \
    "v_smfmac_f32_16x16x64_bf16 v[76:79], v[40:43], a[112:119], v10\n\t" \
    "s_cbranch_vccnz vv_Gbad73_%=\n\t" \
    "vv_Gc75_%=:\n\t" \
    "s_waitcnt lgkmcnt(0)\n\t" \
    "s_barrier\n\t" \
    "ds_read_b128 v[48:51], v0 offset:6144\n\t" \
    "ds_read_b128 v[52:55], v0 offset:7168\n\t" \
    "s_waitcnt vmcnt(0)\n\t" \
    "v_smfmac_f32_16x16x64_bf16 v[72:75], v[44:47], a[64:71], v10\n\t" \
    "ds_read_b128 v[56:59], v0 offset:0\n\t" \
    "ds_read_b128 v[60:63], v0 offset:1024\n\t" \
    "v_max3_u32 v140, v44, v45, v46\n\t" \
    "v_max_u32 v140, v140, v47\n\t" \
    "v_smfmac_f32_16x16x64_bf16 v[76:79], v[44:47], a[136:143], v10\n\t" \
    "ds_read_b128 v[64:67], v0 offset:2048\n\t" \
    "ds_read_b128 v[68:71], v0 offset:3072\n\t" \
    "v_cmp_eq_u32 vcc, -1, v140\n\t" \
    "s_cbranch_vccnz vv_Gbad274_%=\n\t" \
    "vv_Gc276_%=:\n\t" \
    "s_waitcnt lgkmcnt(5)\n\t" \
    "v_smfmac_f32_16x16x64_bf16 v[72:75], v[48:51], a[48:55], v10\n\t" \
    "s_waitcnt lgkmcnt(4)\n\t" \
    "v_smfmac_f32_16x16x64_bf16 v[72:75], v[52:55], a[56:63], v10\n\t" \
    "s_waitcnt lgkmcnt(3)\n\t" \
    "v_smfmac_f32_16x16x64_bf16 v[72:75], v[56:59], a[0:7], v10\n\t" \
    "s_waitcnt lgkmcnt(2)\n\t" \
    "v_smfmac_f32_16x16x64_bf16 v[72:75], v[60:63], a[8:15], v10\n\t" \
    "s_waitcnt lgkmcnt(1)\n\t" \
    "v_smfmac_f32_16x16x64_bf16 v[72:75], v[64:67], a[16:23], v10\n\t" \
    "s_waitcnt lgkmcnt(0)\n\t" \
    "v_smfmac_f32_16x16x64_bf16 v[72:75], v[68:71], a[24:31], v10\n\t" \
    "v_smfmac_f32_16x16x64_bf16 v[76:79], v[48:51], a[120:127], v10\n\t" \
    "s_nop 1\n\t" \
    "s_nop 0\n\t" \
    "v_smfmac_f32_16x16x64_bf16 v[76:79], v[52:55], a[128:135], v10\n\t" \
    "s_nop 1\n\t" \
    "s_nop 0\n\t" \
    "v_smfmac_f32_16x16x64_bf16 v[76:79], v[56:59], a[72:79], v10\n\t" \
    "v_add_f32 v72, v72, v74\n\t" \
    "v_smfmac_f32_16x16x64_bf16 v[76:79], v[60:63], a[80:87], v10\n\t" \
    "v_add_f32 v73, v73, v75\n\t" \
    "v_smfmac_f32_16x16x64_bf16 v[76:79], v[64:67], a[88:95], v10\n\t" \
    "v_cvt_pk_bf16_f32 v80, v72, v73\n\t" \
    "v_smfmac_f32_16x16x64_bf16 v[76:79], v[68:71], a[96:103], v10\n\t" \
    "ds_write_b32 v12, v80 offset:0\n\t" \
    "s_nop 6\n\t" \
    "v_add_f32 v76, v76, v78\n\t" \
    "v_add_f32 v77, v77, v79\n\t" \
    "v_cvt_pk_bf16_f32 v81, v76, v77\n\t" \
    "ds_write_b32 v12, v81 offset:64\n\t" \
    "s_waitcnt lgkmcnt(0)\n\t" \
    "s_barrier\n\t" \
    "ds_read_b64_tr_b16 v[84:85], v13 offset:0\n\t" \
    "ds_read_b64_tr_b16 v[86:87], v13 offset:128\n\t" \
    "ds_read_b64_tr_b16 v[88:89], v13 offset:1024\n\t" \
    "ds_read_b64_tr_b16 v[90:91], v13 offset:1152\n\t" \
    "s_waitcnt lgkmcnt(2)\n\t" \
    "v_smfmac_f32_16x16x64_bf16 v[92:95], v[84:87], a[144:151], v10\n\t" \
    "s_nop 2\n\t" \
    "s_waitcnt lgkmcnt(0)\n\t" \
    "v_smfmac_f32_16x16x64_bf16 v[92:95], v[88:91], a[152:159], v10\n\t" \
    "s_nop 2\n\t" \
    "v_smfmac_f32_16x16x64_bf16 v[96:99], v[84:87], a[160:167], v10\n\t" \
    "s_nop 2\n\t" \
    "v_smfmac_f32_16x16x64_bf16 v[96:99], v[88:91], a[168:175], v10\n\t" \
    "s_nop 2\n\t" \
    "v_smfmac_f32_16x16x64_bf16 v[100:103], v[84:87], a[176:183], v10\n\t" \
    "s_nop 0\n\t" \
    "v_add_f32 v92, v92, v94\n\t" \
    "v_add_f32 v93, v93, v95\n\t" \
    "v_smfmac_f32_16x16x64_bf16 v[100:103], v[88:91], a[184:191], v10\n\t" \
    "s_nop 1\n\t" \
    "v_cvt_pk_bf16_f32 v128, v92, v93\n\t" \
    "v_smfmac_f32_16x16x64_bf16 v[104:107], v[84:87], a[192:199], v10\n\t" \
    "s_nop 0\n\t" \
    "v_add_f32 v96, v96, v98\n\t" \
    "v_add_f32 v97, v97, v99\n\t" \
    "v_smfmac_f32_16x16x64_bf16 v[104:107], v[88:91], a[200:207], v10\n\t" \
    "s_nop 1\n\t" \
    "v_cvt_pk_bf16_f32 v129, v96, v97\n\t" \
    "v_smfmac_f32_16x16x64_bf16 v[124:127], v[84:87], v[240:247], v10\n\t" \
    "s_nop 0\n\t" \
    "v_add_f32 v100, v100, v102\n\t" \
    "v_add_f32 v101, v101, v103\n\t" \
    "v_smfmac_f32_16x16x64_bf16 v[124:127], v[88:91], v[248:255], v10\n\t" \
    "s_nop 1\n\t" \
    "global_load_dword v140, v20, s[46:47] sc1\n\t" \
    "v_cvt_pk_bf16_f32 v130, v100, v101\n\t" \
    "v_smfmac_f32_16x16x64_bf16 v[108:111], v[84:87], a[208:215], v10\n\t" \
    "s_nop 0\n\t" \
    "v_add_f32 v104, v104, v106\n\t" \
    "v_add_f32 v105, v105, v107\n\t" \
    "v_smfmac_f32_16x16x64_bf16 v[108:111], v[88:91], a[216:223], v10\n\t" \
    "s_nop 0\n\t" \
    "v_cvt_pk_bf16_f32 v131, v104, v105\n\t" \
    "v_permlane32_swap_b32 v128, v130\n\t" \
    "v_smfmac_f32_16x16x64_bf16 v[112:115], v[84:87], a[224:231], v10\n\t" \
    "s_nop 0\n\t" \
    "v_permlane32_swap_b32 v129, v131\n\t" \
    "s_nop 1\n\t" \
    "v_permlane16_swap_b32 v128, v129\n\t" \
    "v_smfmac_f32_16x16x64_bf16 v[112:115], v[88:91], a[232:239], v10\n\t" \
    "v_permlane16_swap_b32 v130, v131\n\t" \
    "global_store_dwordx4 v14, v[128:131], s[38:39] sc1\n\t" \
    "v_add_f32 v124, v124, v126\n\t" \
    "v_smfmac_f32_16x16x64_bf16 v[116:119], v[84:87], a[240:247], v10\n\t" \
    "s_nop 0\n\t" \
    "v_add_f32 v125, v125, v127\n\t" \
    "v_cvt_pk_bf16_f32 v136, v124, v125\n\t" \
    "v_smfmac_f32_16x16x64_bf16 v[116:119], v[88:91], a[248:255], v10\n\t" \
    "global_store_dword v16, v136, s[38:39] sc1\n\t" \
    "v_add_f32 v108, v108, v110\n\t" \
    "v_add_f32 v109, v109, v111\n\t" \
    "v_smfmac_f32_16x16x64_bf16 v[120:123], v[84:87], v[224:231], v10\n\t" \
    "s_nop 0\n\t" \
    "v_cvt_pk_bf16_f32 v132, v108, v109\n\t" \
    "v_add_f32 v112, v112, v114\n\t" \
    "v_smfmac_f32_16x16x64_bf16 v[120:123], v[88:91], v[232:239], v10\n\t" \
    "v_add_f32 v113, v113, v115\n\t" \
    "v_cvt_pk_bf16_f32 v133, v112, v113\n\t" \
    "v_add_f32 v116, v116, v118\n\t" \
    "v_add_f32 v117, v117, v119\n\t" \
    "v_cvt_pk_bf16_f32 v134, v116, v117\n\t" \
    "s_nop 2\n\t" \
    "v_pk_add_f32 v[120:121], v[120:121], v[122:123]\n\t" \
    "v_cvt_pk_bf16_f32 v135, v120, v121\n\t" \
    "v_permlane32_swap_b32 v132, v134\n\t" \
    "s_nop 0\n\t" \
    "v_permlane32_swap_b32 v133, v135\n\t" \
    "s_nop 1\n\t" \
    "v_permlane16_swap_b32 v132, v133\n\t" \
    "v_permlane16_swap_b32 v134, v135\n\t" \
    "global_store_dwordx4 v15, v[132:135], s[38:39] sc1\n\t" \
    "s_nop 1\n\t" \
    "global_store_dwordx4 v17, v[128:131], s[40:41] sc1\n\t" \
    "global_store_dwordx4 v17, v[132:135], s[40:41] offset:1024 sc1\n\t" \
    "global_store_dword v18, v136, s[40:41] sc1\n\t" \
    "s_cmp_eq_u32 s65, 63\n\t" \
    "s_cbranch_scc1 vv_end67_%=\n\t" \
    "s_mov_b32 s71, 3\n\t" \
    "vv_E78_%=:\n\t" \
    "s_memtime s[54:55]\n\t" \
    "s_nop 0\n\t" \
    "s_memrealtime s[4:5]\n\t" \
    "s_nop 0\n\t" \
    "s_memrealtime s[6:7]\n\t" \
    "s_nop 0\n\t" \
    "s_memrealtime s[8:9]\n\t" \
    "s_nop 0\n\t" \
    "s_memrealtime s[10:11]\n\t" \
    "s_nop 0\n\t" \
    "s_memrealtime s[12:13]\n\t" \
    "s_nop 0\n\t" \
    "s_memrealtime s[14:15]\n\t" \
    "s_nop 0\n\t" \
    "s_memrealtime s[16:17]\n\t" \
    "s_nop 0\n\t" \
    "s_memrealtime s[18:19]\n\t" \
    "s_nop 0\n\t" \
    "s_memrealtime s[20:21]\n\t" \
    "s_nop 0\n\t" \
    "s_memrealtime s[22:23]\n\t" \
    "s_nop 0\n\t" \
    "s_memrealtime s[24:25]\n\t" \
    "s_nop 0\n\t" \
    "s_memrealtime s[26:27]\n\t" \
    "s_nop 0\n\t" \
    "s_memrealtime s[28:29]\n\t" \
    "s_nop 0\n\t" \
    "s_memtime s[56:57]\n\t" \
    "s_mov_b32 s70, 1\n\t" \
    "s_sub_u32 s71, s71, 1\n\t" \
    "s_cselect_b32 s69, 0, -1\n\t" \
    "s_waitcnt lgkmcnt(0)\n\t" \
    "s_sub_u32 s54, s56, s54\n\t" \
    "s_and_b32 s54, s54, s69\n\t" \
    "s_cmp_gt_u32 s54, 124\n\t" \
    "s_cbranch_scc1 vv_E78_%=\n\t" \
    "s_cmp_eq_u32 s4, s28\n\t" \
    "s_addc_u32 s70, s70, 0\n\t" \
    "s_cmp_eq_u32 s6, s28\n\t" \
    "s_addc_u32 s70, s70, 0\n\t" \
    "s_cmp_eq_u32 s8, s28\n\t" \
    "s_addc_u32 s70, s70, 0\n\t" \
    "s_cmp_eq_u32 s10, s28\n\t" \
    "s_addc_u32 s70, s70, 0\n\t" \
    "s_cmp_eq_u32 s12, s28\n\t" \
    "s_addc_u32 s70, s70, 0\n\t" \
    "s_cmp_eq_u32 s14, s28\n\t" \
    "s_addc_u32 s70, s70, 0\n\t" \
    "s_cmp_eq_u32 s16, s28\n\t" \
    "s_addc_u32 s70, s70, 0\n\t" \
    "s_cmp_eq_u32 s18, s28\n\t" \
    "s_addc_u32 s70, s70, 0\n\t" \
    "s_cmp_eq_u32 s20, s28\n\t" \
    "s_addc_u32 s70, s70, 0\n\t" \
    "s_cmp_eq_u32 s22, s28\n\t" \
    "s_addc_u32 s70, s70, 0\n\t" \
    "s_cmp_eq_u32 s24, s28\n\t" \
    "s_addc_u32 s70, s70, 0\n\t" \
    "s_cmp_eq_u32 s26, s28\n\t" \
    "s_addc_u32 s70, s70, 0\n\t" \
    "s_mov_b32 s75, s73\n\t" \
    "s_mov_b32 s76, s74\n\t" \
    "s_lshl_b32 s70, s70, 3\n\t" \
    "s_sub_u32 s73, s56, s70\n\t" \
    "s_lshl_b32 s74, s28, 4\n\t" \
    "s_sub_u32 s69, s73, s75\n\t" \
    "s_sub_u32 s70, s74, s76\n\t" \
    "s_sub_u32 s71, s70, 1600\n\t" \
    "s_cmp_lt_u32 s71, 400000\n\t" \
    "s_cbranch_scc0 vv_K80_%=\n\t" \
    "v_cvt_f32_u32 v141, s69\n\t" \
    "v_cvt_f32_u32 v142, s70\n\t" \
    "v_rcp_f32 v143, v141\n\t" \
    "v_rcp_f32 v144, v142\n\t" \
    "v_mul_f32 v143, v143, v142\n\t" \
    "v_mul_f32 v144, v144, v141\n\t" \
    "v_mul_f32 v143, 0x4f800000, v143\n\t" \
    "v_mul_f32 v144, 0x4f000000, v144\n\t" \
    "v_cvt_u32_f32 v143, v143\n\t" \
    "v_cvt_u32_f32 v144, v144\n\t" \
    "v_readfirstlane_b32 s69, v143\n\t" \
    "v_readfirstlane_b32 s70, v144\n\t" \
    "s_nop 1\n\t" \
    "s_sub_u32 s69, s69, s78\n\t" \
    "s_ashr_i32 s69, s69, 2\n\t" \
    "s_add_u32 s78, s78, s69\n\t" \
    "s_sub_u32 s70, s70, s77\n\t" \
    "s_ashr_i32 s70, s70, 2\n\t" \
    "s_add_u32 s77, s77, s70\n\t" \
    "vv_K80_%=:\n\t" \
    "s_waitcnt vmcnt(6)\n\t" \
    "v_cmp_eq_u32 vcc, -1, v140\n\t" \
    "s_cbranch_vccz vv_Hok82_%=\n\t" \
    "vv_H81_%=:\n\t" \
    "global_load_dword v140, v20, s[46:47] sc1\n\t" \
    "s_waitcnt vmcnt(0)\n\t" \
    "v_cmp_eq_u32 vcc, -1, v140\n\t" \
    "s_cbranch_vccz vv_Hc83_%=\n\t" \
    "s_cmp_eq_u32 s68, 0\n\t" \
    "s_cbranch_scc1 vv_F84_%=\n\t" \
    "s_sub_u32 s68, s68, 1\n\t" \
    "s_sleep 5\n\t" \
    "s_branch vv_H81_%=\n\t" \
    "vv_F84_%=:\n\t" \
    "v_mov_b32 v141, 6095104\n\t" \
    "v_mov_b32 v142, 6\n\t" \
    "global_store_dword v141, v142, s[36:37] sc1\n\t" \
    "s_waitcnt vmcnt(0)\n\t" \
    "s_branch vv_Hc83_%=\n\t" \
    "vv_Hc83_%=:\n\t" \
    "vv_Hok82_%=:\n\t" \
    "v_subrev_u32 v140, s80, v140\n\t" \
    "v_and_b32 v20, -5, v20\n\t" \
    "s_nop 0\n\t" \
    "v_max_i32_dpp v140, v140, v140 row_ror:1 row_mask:0xf bank_mask:0xf\n\t" \
    "s_nop 1\n\t" \
    "v_max_i32_dpp v140, v140, v140 row_ror:2 row_mask:0xf bank_mask:0xf\n\t" \
    "s_nop 1\n\t" \
    "v_max_i32_dpp v140, v140, v140 row_ror:4 row_mask:0xf bank_mask:0xf\n\t" \
    "s_nop 1\n\t" \
    "v_max_i32_dpp v140, v140, v140 row_ror:8 row_mask:0xf bank_mask:0xf\n\t" \
    "s_nop 0\n\t" \
    "v_readlane_b32 s69, v140, 0\n\t" \
    "v_readlane_b32 s71, v140, 16\n\t" \
    "v_readlane_b32 s70, v140, 32\n\t" \
    "v_readlane_b32 s81, v140, 48\n\t" \
    "s_max_i32 s69, s69, s71\n\t" \
    "s_nop 0\n\t" \
    "s_max_i32 s71, s70, s81\n\t" \
    "s_max_i32 s69, s69, s71\n\t" \
    "s_add_u32 s81, s69, s80\n\t" \
    "v_mov_b32 v72, 0\n\t" \
    "v_mov_b32 v73, 0\n\t" \
    "v_mov_b32 v74, 0\n\t" \
    "v_mov_b32 v75, 0\n\t" \
    "v_mov_b32 v76, 0\n\t" \
    "v_mov_b32 v77, 0\n\t" \
    "v_mov_b32 v78, 0\n\t" \
    "v_mov_b32 v79, 0\n\t" \
    "v_mov_b32 v92, 0\n\t" \
    "v_mov_b32 v93, 0\n\t" \
    "v_mov_b32 v94, 0\n\t" \
    "v_mov_b32 v95, 0\n\t" \
    "v_mov_b32 v96, 0\n\t" \
    "v_mov_b32 v97, 0\n\t" \
    "v_mov_b32 v98, 0\n\t" \
    "v_mov_b32 v99, 0\n\t" \
    "v_mov_b32 v100, 0\n\t" \
    "v_mov_b32 v101, 0\n\t" \
    "v_mov_b32 v102, 0\n\t" \
    "v_mov_b32 v103, 0\n\t" \
    "v_mov_b32 v104, 0\n\t" \
    "v_mov_b32 v105, 0\n\t" \
    "v_mov_b32 v106, 0\n\t" \
    "v_mov_b32 v107, 0\n\t" \
    "v_mov_b32 v108, 0\n\t" \
    "v_mov_b32 v109, 0\n\t" \
    "v_mov_b32 v110, 0\n\t" \
    "v_mov_b32 v111, 0\n\t" \
    "v_mov_b32 v112, 0\n\t" \
    "v_mov_b32 v113, 0\n\t" \
    "v_mov_b32 v114, 0\n\t" \
    "v_mov_b32 v115, 0\n\t" \
    "v_mov_b32 v116, 0\n\t" \
    "v_mov_b32 v117, 0\n\t" \
    "v_mov_b32 v118, 0\n\t" \
    "v_mov_b32 v119, 0\n\t" \
    "v_mov_b32 v120, 0\n\t" \
    "v_mov_b32 v121, 0\n\t" \
    "v_mov_b32 v122, 0\n\t" \
    "v_mov_b32 v123, 0\n\t" \
    "v_mov_b32 v124, 0\n\t" \
    "v_mov_b32 v125, 0\n\t" \
    "v_mov_b32 v126, 0\n\t" \
    "v_mov_b32 v127, 0\n\t" \
    "s_add_u32 s79, s81, 400\n\t" \
    "s_sub_u32 s70, s79, s74\n\t" \
    "s_max_i32 s70, s70, 0\n\t" \
    "s_min_i32 s70, s70, 16000\n\t" \
    "s_lshl_b32 s70, s70, 1\n\t" \
    "s_mul_hi_u32 s70, s70, s77\n\t" \
    "s_add_u32 s70, s70, s73\n\t" \
    "vv_W88_%=:\n\t" \
    "s_memtime s[56:57]\n\t" \
    "s_waitcnt lgkmcnt(0)\n\t" \
    "s_sub_u32 s69, s70, s56\n\t" \
    "s_cmp_gt_i32 s69, 24\n\t" \
    "s_cbranch_scc1 vv_W88_%=\n\t" \
    "global_store_dwordx4 v14, v[148:151], s[42:43] sc1\n\t" \
    "global_store_dwordx4 v15, v[148:151], s[42:43] sc1\n\t" \
    "global_store_dword v16, v148, s[42:43] sc1\n\t" \
    "s_cmp_lg_u32 s65, 0\n\t" \
    "s_cbranch_scc1 vv_Oc87_%=\n\t" \
    "s_add_u32 s79, s81, 240\n\t" \
    "s_sub_u32 s70, s79, s74\n\t" \
    "s_max_i32 s70, s70, 0\n\t" \
    "s_min_i32 s70, s70, 16000\n\t" \
    "s_lshl_b32 s70, s70, 1\n\t" \
    "s_mul_hi_u32 s70, s70, s77\n\t" \
    "s_add_u32 s70, s70, s73\n\t" \
    "vv_W89_%=:\n\t" \
    "s_memtime s[56:57]\n\t" \
    "s_waitcnt lgkmcnt(0)\n\t" \
    "s_sub_u32 s69, s70, s56\n\t" \
    "s_cmp_gt_i32 s69, 24\n\t" \
    "s_cbranch_scc1 vv_W89_%=\n\t" \
    "global_load_dword v140, v35, s[46:47] sc1\n\t" \
    "s_waitcnt vmcnt(0)\n\t" \
    "v_cmp_eq_u32 vcc, -1, v140\n\t" \
    "s_cbranch_vccz vv_Ook86_%=\n\t" \
    "vv_O85_%=:\n\t" \
    "global_load_dword v140, v35, s[46:47] offset:1536 sc1\n\t" \
    "s_waitcnt vmcnt(0)\n\t" \
    "v_cmp_eq_u32 vcc, -1, v140\n\t" \
    "s_cbranch_vccz vv_Ook86_%=\n\t" \
    "s_cmp_eq_u32 s68, 0\n\t" \
    "s_cbranch_scc1 vv_F90_%=\n\t" \
    "s_sub_u32 s68, s68, 1\n\t" \
    "s_sleep 3\n\t" \
    "s_branch vv_O85_%=\n\t" \
    "vv_F90_%=:\n\t" \
    "v_mov_b32 v141, 6095104\n\t" \
    "v_mov_b32 v142, 17\n\t" \
    "global_store_dword v141, v142, s[36:37] sc1\n\t" \
    "s_waitcnt vmcnt(0)\n\t" \
    "s_branch vv_Ook86_%=\n\t" \
    "vv_Ook86_%=:\n\t" \
    "v_subrev_u32 v140, s80, v140\n\t" \
    "s_nop 1\n\t" \
    "v_max_i32_dpp v140, v140, v140 row_ror:1 row_mask:0xf bank_mask:0xf\n\t" \
    "s_nop 1\n\t" \
    "v_max_i32_dpp v140, v140, v140 row_ror:2 row_mask:0xf bank_mask:0xf\n\t" \
    "s_nop 1\n\t" \
    "v_max_i32_dpp v140, v140, v140 row_ror:4 row_mask:0xf bank_mask:0xf\n\t" \
    "s_nop 1\n\t" \
    "v_max_i32_dpp v140, v140, v140 row_ror:8 row_mask:0xf bank_mask:0xf\n\t" \
    "s_nop 0\n\t" \
    "v_readlane_b32 s69, v140, 0\n\t" \
    "v_readlane_b32 s71, v140, 16\n\t" \
    "v_readlane_b32 s70, v140, 32\n\t" \
    "v_readlane_b32 s81, v140, 48\n\t" \
    "s_max_i32 s69, s69, s71\n\t" \
    "s_nop 0\n\t" \
    "s_max_i32 s71, s70, s81\n\t" \
    "s_max_i32 s69, s69, s71\n\t" \
    "s_add_u32 s81, s69, s80\n\t" \
    "vv_Oc87_%=:\n\t" \
    "s_mul_hi_u32 s69, s85, s78\n\t" \
    "s_add_u32 s79, s81, s69\n\t" \
    "s_add_u32 s79, s79, s87\n\t" \
    "s_cmp_ge_u32 s65, 36\n\t" \
    "s_cselect_b32 s69, 28, 0\n\t" \
    "s_sub_u32 s79, s79, s69\n\t" \
    "s_add_u32 s80, s79, s88\n\t" \
    "s_add_u32 s65, s65, 1\n\t" \
    "s_branch vv_go68_%=\n\t" \
    "vv_Gbad73_%=:\n\t" \
    "s_waitcnt vmcnt(0) lgkmcnt(0)\n\t" \
    "s_nop 7\n\t" \
    "v_mov_b32 v72, 0\n\t" \
    "v_mov_b32 v73, 0\n\t" \
    "v_mov_b32 v74, 0\n\t" \
    "v_mov_b32 v75, 0\n\t" \
    "v_mov_b32 v76, 0\n\t" \
    "v_mov_b32 v77, 0\n\t" \
    "v_mov_b32 v78, 0\n\t" \
    "v_mov_b32 v79, 0\n\t" \
    "s_cmp_eq_u32 s68, 0\n\t" \
    "s_cbranch_scc1 vv_F91_%=\n\t" \
    "s_sub_u32 s68, s68, 1\n\t" \
    "s_branch vv_G72_%=\n\t" \
    "vv_F91_%=:\n\t" \
    "v_mov_b32 v140, 6095104\n\t" \
    "v_mov_b32 v141, 1\n\t" \
    "global_store_dword v140, v141, s[36:37] sc1\n\t" \
    "s_waitcnt vmcnt(0)\n\t" \
    "s_branch vv_Gc75_%=\n\t" \
    "vv_Gbad274_%=:\n\t" \
    "global_load_dwordx4 v[44:47], v2, s[46:47] sc1\n\t" \
    "s_waitcnt vmcnt(0)\n\t" \
    "v_max3_u32 v140, v44, v45, v46\n\t" \
    "v_max_u32 v140, v140, v47\n\t" \
    "v_cmp_eq_u32 vcc, -1, v140\n\t" \
    "s_cbranch_vccz vv_Gre277_%=\n\t" \
    "s_cmp_eq_u32 s68, 0\n\t" \
    "s_cbranch_scc1 vv_F92_%=\n\t" \
    "s_sub_u32 s68, s68, 1\n\t" \
    "s_sleep 1\n\t" \
    "s_branch vv_Gbad274_%=\n\t" \
    "vv_F92_%=:\n\t" \
    "v_mov_b32 v140, 6095104\n\t" \
    "v_mov_b32 v141, 2\n\t" \
    "global_store_dword v140, v141, s[36:37] sc1\n\t" \
    "s_waitcnt vmcnt(0)\n\t" \
    "s_branch vv_Gre277_%=\n\t" \
    "vv_Gre277_%=:\n\t" \
    "v_mov_b32 v72, 0\n\t" \
    "v_mov_b32 v73, 0\n\t" \
    "v_mov_b32 v74, 0\n\t" \
    "v_mov_b32 v75, 0\n\t" \
    "v_mov_b32 v76, 0\n\t" \
    "v_mov_b32 v77, 0\n\t" \
    "v_mov_b32 v78, 0\n\t" \
    "v_mov_b32 v79, 0\n\t" \
    "v_smfmac_f32_16x16x64_bf16 v[72:75], v[36:39], a[32:39], v10\n\t" \
    "s_nop 0\n\t" \
    "v_smfmac_f32_16x16x64_bf16 v[76:79], v[36:39], a[104:111], v10\n\t" \
    "v_smfmac_f32_16x16x64_bf16 v[72:75], v[40:43], a[40:47], v10\n\t" \
    "v_smfmac_f32_16x16x64_bf16 v[76:79], v[40:43], a[112:119], v10\n\t" \
    "v_smfmac_f32_16x16x64_bf16 v[72:75], v[44:47], a[64:71], v10\n\t" \
    "v_smfmac_f32_16x16x64_bf16 v[76:79], v[44:47], a[136:143], v10\n\t" \
    "s_branch vv_Gc276_%=\n\t" \
    "vv_end67_%=:\n\t" \
    "s_endpgm\n\t" \
    "vv_loop13_%=:\n\t" \
    "v_mov_b32 v72, 0\n\t" \
    "v_mov_b32 v73, 0\n\t" \
    "v_mov_b32 v74, 0\n\t" \
    "v_mov_b32 v75, 0\n\t" \
    "v_mov_b32 v76, 0\n\t" \
    "v_mov_b32 v77, 0\n\t" \
    "v_mov_b32 v78, 0\n\t" \
    "v_mov_b32 v79, 0\n\t" \
    "v_mov_b32 v92, 0\n\t" \
    "v_mov_b32 v93, 0\n\t" \
    "v_mov_b32 v94, 0\n\t" \
    "v_mov_b32 v95, 0\n\t" \
    "v_mov_b32 v96, 0\n\t" \
    "v_mov_b32 v97, 0\n\t" \
    "v_mov_b32 v98, 0\n\t" \
    "v_mov_b32 v99, 0\n\t" \
    "v_mov_b32 v100, 0\n\t" \
    "v_mov_b32 v101, 0\n\t" \
    "v_mov_b32 v102, 0\n\t" \
    "v_mov_b32 v103, 0\n\t" \
    "v_mov_b32 v104, 0\n\t" \
    "v_mov_b32 v105, 0\n\t" \
    "v_mov_b32 v106, 0\n\t" \
    "v_mov_b32 v107, 0\n\t" \
    "v_mov_b32 v108, 0\n\t" \
    "v_mov_b32 v109, 0\n\t" \
    "v_mov_b32 v110, 0\n\t" \
    "v_mov_b32 v111, 0\n\t" \
    "v_mov_b32 v112, 0\n\t" \
    "v_mov_b32 v113, 0\n\t" \
    "v_mov_b32 v114, 0\n\t" \
    "v_mov_b32 v115, 0\n\t" \
    "v_mov_b32 v116, 0\n\t" \
    "v_mov_b32 v117, 0\n\t" \
    "v_mov_b32 v118, 0\n\t" \
    "v_mov_b32 v119, 0\n\t" \
    "v_mov_b32 v120, 0\n\t" \
    "v_mov_b32 v121, 0\n\t" \
    "v_mov_b32 v122, 0\n\t" \
    "v_mov_b32 v123, 0\n\t" \
    "v_mov_b32 v124, 0\n\t" \
    "v_mov_b32 v125, 0\n\t" \
    "v_mov_b32 v126, 0\n\t" \
    "v_mov_b32 v127, 0\n\t" \
    "vv_go94_%=:\n\t" \
    "s_add_u32 s69, s65, 0\n\t" \
    "s_bitcmp1_b32 s69, 0\n\t" \
    "s_cselect_b32 s70, s63, s62\n\t" \
    "s_bfe_u32 s69, s69, 0x20001\n\t" \
    "s_lshl_b32 s69, s69, 19\n\t" \
    "s_add_u32 s69, s69, s70\n\t" \
    "s_add_u32 s46, s36, s69\n\t" \
    "s_addc_u32 s47, s37, 0\n\t" \
    "s_add_u32 s69, s65, 0\n\t" \
    "s_mul_i32 s70, s69, 171\n\t" \
    "s_lshr_b32 s70, s70, 9\n\t" \
    "s_mul_i32 s70, s70, 3\n\t" \
    "s_sub_u32 s69, s69, s70\n\t" \
    "s_mul_i32 s69, s69, 36\n\t" \
    "s_lshl_b32 s69, s69, 14\n\t" \
    "s_add_u32 s69, s69, s64\n\t" \
    "s_add_u32 s69, s69, 4194304\n\t" \
    "s_add_u32 s38, s36, s69\n\t" \
    "s_addc_u32 s39, s37, 0\n\t" \
    "s_add_u32 s69, s65, 2\n\t" \
    "s_mul_i32 s70, s69, 171\n\t" \
    "s_lshr_b32 s70, s70, 9\n\t" \
    "s_mul_i32 s70, s70, 3\n\t" \
    "s_sub_u32 s69, s69, s70\n\t" \
    "s_mul_i32 s69, s69, 36\n\t" \
    "s_lshl_b32 s69, s69, 14\n\t" \
    "s_add_u32 s69, s69, s64\n\t" \
    "s_add_u32 s69, s69, 4194304\n\t" \
    "s_add_u32 s42, s36, s69\n\t" \
    "s_addc_u32 s43, s37, 0\n\t" \
    "s_mul_i32 s69, s65, 9216\n\t" \
    "s_add_u32 s69, s69, 123731968\n\t" \
    "s_add_u32 s40, s36, s69\n\t" \
    "s_addc_u32 s41, s37, 0\n\t" \
    "s_sub_u32 s70, s79, s74\n\t" \
    "s_max_i32 s70, s70, 0\n\t" \
    "s_min_i32 s70, s70, 16000\n\t" \
    "s_lshl_b32 s70, s70, 1\n\t" \
    "s_mul_hi_u32 s70, s70, s77\n\t" \
    "s_add_u32 s70, s70, s73\n\t" \
    "s_sub_u32 s70, s70, 100\n\t" \
    "s_getpc_b64 s[30:31]\n\t" \
    "vv_Wp96_%=:\n\t" \
    "s_add_u32 s30, s30, vv_We97_%=-vv_Wp96_%=\n\t" \
    "s_addc_u32 s31, s31, 0\n\t" \
    "vv_W95_%=:\n\t" \
    "s_memtime s[56:57]\n\t" \
    "s_waitcnt lgkmcnt(0)\n\t" \
    "s_sub_u32 s69, s70, s56\n\t" \
    "s_cmp_gt_i32 s69, 111\n\t" \
    "s_cbranch_scc1 vv_W95_%=\n\t" \
    "s_max_i32 s69, s69, 0\n\t" \
    "s_lshr_b32 s69, s69, 1\n\t" \
    "s_and_b32 s69, s69, 0x3c\n\t" \
    "s_sub_u32 s30, s30, s69\n\t" \
    "s_subb_u32 s31, s31, 0\n\t" \
    "s_setpc_b64 s[30:31]\n\t" \
    ".p2align 6\n\t" \
    "s_nop 0\n\t" \
    "s_nop 0\n\t" \
    "s_nop 0\n\t" \
    "s_nop 1\n\t" \
    "s_nop 1\n\t" \
    "s_nop 1\n\t" \
    "s_nop 1\n\t" \
    "s_nop 1\n\t" \
    "s_nop 1\n\t" \
    "s_nop 1\n\t" \
    "s_nop 1\n\t" \
    "s_nop 1\n\t" \
    "s_nop 1\n\t" \
    "s_nop 1\n\t" \
    "s_nop 1\n\t" \
    "s_nop 1\n\t" \
    "vv_We97_%=:\n\t" \
    "vv_G98_%=:\n\t" \
    "global_load_dwordx4 v[36:39], v1, s[46:47] sc1\n\t" \
    "s_nop 9\n\t" \
    "global_load_dwordx4 v[40:43], v1, s[46:47] offset:1024 sc1\n\t" \
    "s_nop 9\n\t" \
    "global_load_dwordx4 v[44:47], v2, s[46:47] sc1\n\t" \
    "s_waitcnt vmcnt(2)\n\t" \
    "v_smfmac_f32_16x16x64_bf16 v[72:75], v[36:39], a[48:55], v10\n\t" \
    "ds_write_b128 v0, v[36:39] offset:6144\n\t" \
    "v_smfmac_f32_16x16x64_bf16 v[76:79], v[36:39], a[120:127], v10\n\t" \
    "v_max3_u32 v140, v36, v37, v38\n\t" \
    "s_waitcnt vmcnt(1)\n\t" \
    "ds_write_b128 v0, v[40:43] offset:7168\n\t" \
    "v_smfmac_f32_16x16x64_bf16 v[72:75], v[40:43], a[56:63], v10\n\t" \
    "v_max3_u32 v140, v140, v39, v40\n\t" \
    "v_max3_u32 v140, v140, v41, v42\n\t" \
    "v_max_u32 v140, v140, v43\n\t" \
    "v_cmp_eq_u32 vcc, -1, v140\n\t" \
    "v_smfmac_f32_16x16x64_bf16 v[76:79], v[40:43], a[128:135], v10\n\t" \
    "s_cbranch_vccnz vv_Gbad99_%=\n\t" \
    "vv_Gc101_%=:\n\t" \
    "s_waitcnt lgkmcnt(0)\n\t" \
    "s_barrier\n\t" \
    "ds_read_b128 v[48:51], v0 offset:0\n\t" \
    "ds_read_b128 v[52:55], v0 offset:1024\n\t" \
    "s_waitcnt vmcnt(0)\n\t" \
    "v_smfmac_f32_16x16x64_bf16 v[72:75], v[44:47], a[64:71], v10\n\t" \
    "ds_read_b128 v[56:59], v0 offset:2048\n\t" \
    "ds_read_b128 v[60:63], v0 offset:3072\n\t" \
    "v_max3_u32 v140, v44, v45, v46\n\t" \
    "v_max_u32 v140, v140, v47\n\t" \
    "v_smfmac_f32_16x16x64_bf16 v[76:79], v[44:47], a[136:143], v10\n\t" \
    "ds_read_b128 v[64:67], v0 offset:4096\n\t" \
    "ds_read_b128 v[68:71], v0 offset:5120\n\t" \
    "v_cmp_eq_u32 vcc, -1, v140\n\t" \
    "s_cbranch_vccnz vv_Gbad2100_%=\n\t" \
    "vv_Gc2102_%=:\n\t" \
    "s_waitcnt lgkmcnt(5)\n\t" \
    "v_smfmac_f32_16x16x64_bf16 v[72:75], v[48:51], a[0:7], v10\n\t" \
    "s_waitcnt lgkmcnt(4)\n\t" \
    "v_smfmac_f32_16x16x64_bf16 v[72:75], v[52:55], a[8:15], v10\n\t" \
    "s_waitcnt lgkmcnt(3)\n\t" \
    "v_smfmac_f32_16x16x64_bf16 v[72:75], v[56:59], a[16:23], v10\n\t" \
    "s_waitcnt lgkmcnt(2)\n\t" \
    "v_smfmac_f32_16x16x64_bf16 v[72:75], v[60:63], a[24:31], v10\n\t" \
    "s_waitcnt lgkmcnt(1)\n\t" \
    "v_smfmac_f32_16x16x64_bf16 v[72:75], v[64:67], a[32:39], v10\n\t" \
    "s_waitcnt lgkmcnt(0)\n\t" \
    "v_smfmac_f32_16x16x64_bf16 v[72:75], v[68:71], a[40:47], v10\n\t" \
    "v_smfmac_f32_16x16x64_bf16 v[76:79], v[48:51], a[72:79], v10\n\t" \
    "s_nop 1\n\t" \
    "s_nop 0\n\t" \
    "v_smfmac_f32_16x16x64_bf16 v[76:79], v[52:55], a[80:87], v10\n\t" \
    "s_nop 1\n\t" \
    "s_nop 0\n\t" \
    "v_smfmac_f32_16x16x64_bf16 v[76:79], v[56:59], a[88:95], v10\n\t" \
    "v_add_f32 v72, v72, v74\n\t" \
    "v_smfmac_f32_16x16x64_bf16 v[76:79], v[60:63], a[96:103], v10\n\t" \
    "v_add_f32 v73, v73, v75\n\t" \
    "v_smfmac_f32_16x16x64_bf16 v[76:79], v[64:67], a[104:111], v10\n\t" \
    "v_cvt_pk_bf16_f32 v80, v72, v73\n\t" \
    "v_smfmac_f32_16x16x64_bf16 v[76:79], v[68:71], a[112:119], v10\n\t" \
    "ds_write_b32 v12, v80 offset:0\n\t" \
    "s_nop 6\n\t" \
    "v_add_f32 v76, v76, v78\n\t" \
    "v_add_f32 v77, v77, v79\n\t" \
    "v_cvt_pk_bf16_f32 v81, v76, v77\n\t" \
    "ds_write_b32 v12, v81 offset:64\n\t" \
    "s_waitcnt lgkmcnt(0)\n\t" \
    "s_barrier\n\t" \
    "ds_read_b64_tr_b16 v[84:85], v13 offset:0\n\t" \
    "ds_read_b64_tr_b16 v[86:87], v13 offset:128\n\t" \
    "ds_read_b64_tr_b16 v[88:89], v13 offset:1024\n\t" \
    "ds_read_b64_tr_b16 v[90:91], v13 offset:1152\n\t" \
    "s_waitcnt lgkmcnt(2)\n\t" \
    "v_smfmac_f32_16x16x64_bf16 v[92:95], v[84:87], a[144:151], v10\n\t" \
    "s_nop 2\n\t" \
    "s_waitcnt lgkmcnt(0)\n\t" \
    "v_smfmac_f32_16x16x64_bf16 v[92:95], v[88:91], a[152:159], v10\n\t" \
    "s_nop 2\n\t" \
    "v_smfmac_f32_16x16x64_bf16 v[96:99], v[84:87], a[160:167], v10\n\t" \
    "s_nop 2\n\t" \
    "v_smfmac_f32_16x16x64_bf16 v[96:99], v[88:91], a[168:175], v10\n\t" \
    "s_nop 2\n\t" \
    "v_smfmac_f32_16x16x64_bf16 v[100:103], v[84:87], a[176:183], v10\n\t" \
    "s_nop 0\n\t" \
    "v_add_f32 v92, v92, v94\n\t" \
    "v_add_f32 v93, v93, v95\n\t" \
    "v_smfmac_f32_16x16x64_bf16 v[100:103], v[88:91], a[184:191], v10\n\t" \
    "s_nop 1\n\t" \
    "v_cvt_pk_bf16_f32 v128, v92, v93\n\t" \
    "v_smfmac_f32_16x16x64_bf16 v[104:107], v[84:87], a[192:199], v10\n\t" \
    "s_nop 0\n\t" \
    "v_add_f32 v96, v96, v98\n\t" \
    "v_add_f32 v97, v97, v99\n\t" \
    "v_smfmac_f32_16x16x64_bf16 v[104:107], v[88:91], a[200:207], v10\n\t" \
    "s_nop 1\n\t" \
    "v_cvt_pk_bf16_f32 v129, v96, v97\n\t" \
    "v_smfmac_f32_16x16x64_bf16 v[124:127], v[84:87], v[240:247], v10\n\t" \
    "s_nop 0\n\t" \
    "v_add_f32 v100, v100, v102\n\t" \
    "v_add_f32 v101, v101, v103\n\t" \
    "v_smfmac_f32_16x16x64_bf16 v[124:127], v[88:91], v[248:255], v10\n\t" \
    "s_nop 1\n\t" \
    "global_load_dword v140, v20, s[46:47] sc1\n\t" \
    "v_cvt_pk_bf16_f32 v130, v100, v101\n\t" \
    "v_smfmac_f32_16x16x64_bf16 v[108:111], v[84:87], a[208:215], v10\n\t" \
    "s_nop 0\n\t" \
    "v_add_f32 v104, v104, v106\n\t" \
    "v_add_f32 v105, v105, v107\n\t" \
    "v_smfmac_f32_16x16x64_bf16 v[108:111], v[88:91], a[216:223], v10\n\t" \
    "s_nop 0\n\t" \
    "v_cvt_pk_bf16_f32 v131, v104, v105\n\t" \
    "v_permlane32_swap_b32 v128, v130\n\t" \
    "v_smfmac_f32_16x16x64_bf16 v[112:115], v[84:87], a[224:231], v10\n\t" \
    "s_nop 0\n\t" \
    "v_permlane32_swap_b32 v129, v131\n\t" \
    "s_nop 1\n\t" \
    "v_permlane16_swap_b32 v128, v129\n\t" \
    "v_smfmac_f32_16x16x64_bf16 v[112:115], v[88:91], a[232:239], v10\n\t" \
    "v_permlane16_swap_b32 v130, v131\n\t" \
    "global_store_dwordx4 v14, v[128:131], s[38:39] sc1\n\t" \
    "v_add_f32 v124, v124, v126\n\t" \
    "v_smfmac_f32_16x16x64_bf16 v[116:119], v[84:87], a[240:247], v10\n\t" \
    "s_nop 0\n\t" \
    "v_add_f32 v125, v125, v127\n\t" \
    "v_cvt_pk_bf16_f32 v136, v124, v125\n\t" \
    "v_smfmac_f32_16x16x64_bf16 v[116:119], v[88:91], a[248:255], v10\n\t" \
    "global_store_dword v16, v136, s[38:39] sc1\n\t" \
    "v_add_f32 v108, v108, v110\n\t" \
    "v_add_f32 v109, v109, v111\n\t" \
    "v_smfmac_f32_16x16x64_bf16 v[120:123], v[84:87], v[224:231], v10\n\t" \
    "s_nop 0\n\t" \
    "v_cvt_pk_bf16_f32 v132, v108, v109\n\t" \
    "v_add_f32 v112, v112, v114\n\t" \
    "v_smfmac_f32_16x16x64_bf16 v[120:123], v[88:91], v[232:239], v10\n\t" \
    "v_add_f32 v113, v113, v115\n\t" \
    "v_cvt_pk_bf16_f32 v133, v112, v113\n\t" \
    "v_add_f32 v116, v116, v118\n\t" \
    "v_add_f32 v117, v117, v119\n\t" \
    "v_cvt_pk_bf16_f32 v134, v116, v117\n\t" \
    "s_nop 2\n\t" \
    "v_pk_add_f32 v[120:121], v[120:121], v[122:123]\n\t" \
    "v_cvt_pk_bf16_f32 v135, v120, v121\n\t" \
    "v_permlane32_swap_b32 v132, v134\n\t" \
    "s_nop 0\n\t" \
    "v_permlane32_swap_b32 v133, v135\n\t" \
    "s_nop 1\n\t" \
    "v_permlane16_swap_b32 v132, v133\n\t" \
    "v_permlane16_swap_b32 v134, v135\n\t" \
    "global_store_dwordx4 v15, v[132:135], s[38:39] sc1\n\t" \
    "s_nop 1\n\t" \
    "global_store_dwordx4 v17, v[128:131], s[40:41] sc1\n\t" \
    "global_store_dwordx4 v17, v[132:135], s[40:41] offset:1024 sc1\n\t" \
    "global_store_dword v18, v136, s[40:41] sc1\n\t" \
    "s_cmp_eq_u32 s65, 63\n\t" \
    "s_cbranch_scc1 vv_end93_%=\n\t" \
    "s_mov_b32 s71, 3\n\t" \
    "vv_E104_%=:\n\t" \
    "s_memtime s[54:55]\n\t" \
    "s_nop 0\n\t" \
    "s_memrealtime s[4:5]\n\t" \
    "s_nop 0\n\t" \
    "s_memrealtime s[6:7]\n\t" \
    "s_nop 0\n\t" \
    "s_memrealtime s[8:9]\n\t" \
    "s_nop 0\n\t" \
    "s_memrealtime s[10:11]\n\t" \
    "s_nop 0\n\t" \
    "s_memrealtime s[12:13]\n\t" \
    "s_nop 0\n\t" \
    "s_memrealtime s[14:15]\n\t" \
    "s_nop 0\n\t" \
    "s_memrealtime s[16:17]\n\t" \
    "s_nop 0\n\t" \
    "s_memrealtime s[18:19]\n\t" \
    "s_nop 0\n\t" \
    "s_memrealtime s[20:21]\n\t" \
    "s_nop 0\n\t" \
    "s_memrealtime s[22:23]\n\t" \
    "s_nop 0\n\t" \
    "s_memrealtime s[24:25]\n\t" \
    "s_nop 0\n\t" \
    "s_memrealtime s[26:27]\n\t" \
    "s_nop 0\n\t" \
    "s_memrealtime s[28:29]\n\t" \
    "s_nop 0\n\t" \
    "s_memtime s[56:57]\n\t" \
    "s_mov_b32 s70, 1\n\t" \
    "s_sub_u32 s71, s71, 1\n\t" \
    "s_cselect_b32 s69, 0, -1\n\t" \
    "s_waitcnt lgkmcnt(0)\n\t" \
    "s_sub_u32 s54, s56, s54\n\t" \
    "s_and_b32 s54, s54, s69\n\t" \
    "s_cmp_gt_u32 s54, 124\n\t" \
    "s_cbranch_scc1 vv_E104_%=\n\t" \
    "s_cmp_eq_u32 s4, s28\n\t" \
    "s_addc_u32 s70, s70, 0\n\t" \
    "s_cmp_eq_u32 s6, s28\n\t" \
    "s_addc_u32 s70, s70, 0\n\t" \
    "s_cmp_eq_u32 s8, s28\n\t" \
    "s_addc_u32 s70, s70, 0\n\t" \
    "s_cmp_eq_u32 s10, s28\n\t" \
    "s_addc_u32 s70, s70, 0\n\t" \
    "s_cmp_eq_u32 s12, s28\n\t" \
    "s_addc_u32 s70, s70, 0\n\t" \
    "s_cmp_eq_u32 s14, s28\n\t" \
    "s_addc_u32 s70, s70, 0\n\t" \
    "s_cmp_eq_u32 s16, s28\n\t" \
    "s_addc_u32 s70, s70, 0\n\t" \
    "s_cmp_eq_u32 s18, s28\n\t" \
    "s_addc_u32 s70, s70, 0\n\t" \
    "s_cmp_eq_u32 s20, s28\n\t" \
    "s_addc_u32 s70, s70, 0\n\t" \
    "s_cmp_eq_u32 s22, s28\n\t" \
    "s_addc_u32 s70, s70, 0\n\t" \
    "s_cmp_eq_u32 s24, s28\n\t" \
    "s_addc_u32 s70, s70, 0\n\t" \
    "s_cmp_eq_u32 s26, s28\n\t" \
    "s_addc_u32 s70, s70, 0\n\t" \
    "s_mov_b32 s75, s73\n\t" \
    "s_mov_b32 s76, s74\n\t" \
    "s_lshl_b32 s70, s70, 3\n\t" \
    "s_sub_u32 s73, s56, s70\n\t" \
    "s_lshl_b32 s74, s28, 4\n\t" \
    "s_sub_u32 s69, s73, s75\n\t" \
    "s_sub_u32 s70, s74, s76\n\t" \
    "s_sub_u32 s71, s70, 1600\n\t" \
    "s_cmp_lt_u32 s71, 400000\n\t" \
    "s_cbranch_scc0 vv_K106_%=\n\t" \
    "v_cvt_f32_u32 v141, s69\n\t" \
    "v_cvt_f32_u32 v142, s70\n\t" \
    "v_rcp_f32 v143, v141\n\t" \
    "v_rcp_f32 v144, v142\n\t" \
    "v_mul_f32 v143, v143, v142\n\t" \
    "v_mul_f32 v144, v144, v141\n\t" \
    "v_mul_f32 v143, 0x4f800000, v143\n\t" \
    "v_mul_f32 v144, 0x4f000000, v144\n\t" \
    "v_cvt_u32_f32 v143, v143\n\t" \
    "v_cvt_u32_f32 v144, v144\n\t" \
    "v_readfirstlane_b32 s69, v143\n\t" \
    "v_readfirstlane_b32 s70, v144\n\t" \
    "s_nop 1\n\t" \
    "s_sub_u32 s69, s69, s78\n\t" \
    "s_ashr_i32 s69, s69, 2\n\t" \
    "s_add_u32 s78, s78, s69\n\t" \
    "s_sub_u32 s70, s70, s77\n\t" \
    "s_ashr_i32 s70, s70, 2\n\t" \
    "s_add_u32 s77, s77, s70\n\t" \
    "vv_K106_%=:\n\t" \
    "s_waitcnt vmcnt(6)\n\t" \
    "v_cmp_eq_u32 vcc, -1, v140\n\t" \
    "s_cbranch_vccz vv_Hok108_%=\n\t" \
    "vv_H107_%=:\n\t" \
    "global_load_dword v140, v20, s[46:47] sc1\n\t" \
    "s_waitcnt vmcnt(0)\n\t" \
    "v_cmp_eq_u32 vcc, -1, v140\n\t" \
    "s_cbranch_vccz vv_Hc109_%=\n\t" \
    "s_cmp_eq_u32 s68, 0\n\t" \
    "s_cbranch_scc1 vv_F110_%=\n\t" \
    "s_sub_u32 s68, s68, 1\n\t" \
    "s_sleep 5\n\t" \
    "s_branch vv_H107_%=\n\t" \
    "vv_F110_%=:\n\t" \
    "v_mov_b32 v141, 6095104\n\t" \
    "v_mov_b32 v142, 6\n\t" \
    "global_store_dword v141, v142, s[36:37] sc1\n\t" \
    "s_waitcnt vmcnt(0)\n\t" \
    "s_branch vv_Hc109_%=\n\t" \
    "vv_Hc109_%=:\n\t" \
    "vv_Hok108_%=:\n\t" \
    "v_subrev_u32 v140, s80, v140\n\t" \
    "v_and_b32 v20, -5, v20\n\t" \
    "s_nop 0\n\t" \
    "v_max_i32_dpp v140, v140, v140 row_ror:1 row_mask:0xf bank_mask:0xf\n\t" \
    "s_nop 1\n\t" \
    "v_max_i32_dpp v140, v140, v140 row_ror:2 row_mask:0xf bank_mask:0xf\n\t" \
    "s_nop 1\n\t" \
    "v_max_i32_dpp v140, v140, v140 row_ror:4 row_mask:0xf bank_mask:0xf\n\t" \
    "s_nop 1\n\t" \
    "v_max_i32_dpp v140, v140, v140 row_ror:8 row_mask:0xf bank_mask:0xf\n\t" \
    "s_nop 0\n\t" \
    "v_readlane_b32 s69, v140, 0\n\t" \
    "v_readlane_b32 s71, v140, 16\n\t" \
    "v_readlane_b32 s70, v140, 32\n\t" \
    "v_readlane_b32 s81, v140, 48\n\t" \
    "s_max_i32 s69, s69, s71\n\t" \
    "s_nop 0\n\t" \
    "s_max_i32 s71, s70, s81\n\t" \
    "s_max_i32 s69, s69, s71\n\t" \
    "s_add_u32 s81, s69, s80\n\t" \
    "v_mov_b32 v72, 0\n\t" \
    "v_mov_b32 v73, 0\n\t" \
    "v_mov_b32 v74, 0\n\t" \
    "v_mov_b32 v75, 0\n\t" \
    "v_mov_b32 v76, 0\n\t" \
    "v_mov_b32 v77, 0\n\t" \
    "v_mov_b32 v78, 0\n\t" \
    "v_mov_b32 v79, 0\n\t" \
    "v_mov_b32 v92, 0\n\t" \
    "v_mov_b32 v93, 0\n\t" \
    "v_mov_b32 v94, 0\n\t" \
    "v_mov_b32 v95, 0\n\t" \
    "v_mov_b32 v96, 0\n\t" \
    "v_mov_b32 v97, 0\n\t" \
    "v_mov_b32 v98, 0\n\t" \
    "v_mov_b32 v99, 0\n\t" \
    "v_mov_b32 v100, 0\n\t" \
    "v_mov_b32 v101, 0\n\t" \
    "v_mov_b32 v102, 0\n\t" \
    "v_mov_b32 v103, 0\n\t" \
    "v_mov_b32 v104, 0\n\t" \
    "v_mov_b32 v105, 0\n\t" \
    "v_mov_b32 v106, 0\n\t" \
    "v_mov_b32 v107, 0\n\t" \
    "v_mov_b32 v108, 0\n\t" \
    "v_mov_b32 v109, 0\n\t" \
    "v_mov_b32 v110, 0\n\t" \
    "v_mov_b32 v111, 0\n\t" \
    "v_mov_b32 v112, 0\n\t" \
    "v_mov_b32 v113, 0\n\t" \
    "v_mov_b32 v114, 0\n\t" \
    "v_mov_b32 v115, 0\n\t" \
    "v_mov_b32 v116, 0\n\t" \
    "v_mov_b32 v117, 0\n\t" \
    "v_mov_b32 v118, 0\n\t" \
    "v_mov_b32 v119, 0\n\t" \
    "v_mov_b32 v120, 0\n\t" \
    "v_mov_b32 v121, 0\n\t" \
    "v_mov_b32 v122, 0\n\t" \
    "v_mov_b32 v123, 0\n\t" \
    "v_mov_b32 v124, 0\n\t" \
    "v_mov_b32 v125, 0\n\t" \
    "v_mov_b32 v126, 0\n\t" \
    "v_mov_b32 v127, 0\n\t" \
    "s_add_u32 s79, s81, 400\n\t" \
    "s_sub_u32 s70, s79, s74\n\t" \
    "s_max_i32 s70, s70, 0\n\t" \
    "s_min_i32 s70, s70, 16000\n\t" \
    "s_lshl_b32 s70, s70, 1\n\t" \
    "s_mul_hi_u32 s70, s70, s77\n\t" \
    "s_add_u32 s70, s70, s73\n\t" \
    "vv_W114_%=:\n\t" \
    "s_memtime s[56:57]\n\t" \
    "s_waitcnt lgkmcnt(0)\n\t" \
    "s_sub_u32 s69, s70, s56\n\t" \
    "s_cmp_gt_i32 s69, 24\n\t" \
    "s_cbranch_scc1 vv_W114_%=\n\t" \
    "global_store_dwordx4 v14, v[148:151], s[42:43] sc1\n\t" \
    "global_store_dwordx4 v15, v[148:151], s[42:43] sc1\n\t" \
    "global_store_dword v16, v148, s[42:43] sc1\n\t" \
    "s_cmp_lg_u32 s65, 0\n\t" \
    "s_cbranch_scc1 vv_Oc113_%=\n\t" \
    "s_add_u32 s79, s81, 240\n\t" \
    "s_sub_u32 s70, s79, s74\n\t" \
    "s_max_i32 s70, s70, 0\n\t" \
    "s_min_i32 s70, s70, 16000\n\t" \
    "s_lshl_b32 s70, s70, 1\n\t" \
    "s_mul_hi_u32 s70, s70, s77\n\t" \
    "s_add_u32 s70, s70, s73\n\t" \
    "vv_W115_%=:\n\t" \
    "s_memtime s[56:57]\n\t" \
    "s_waitcnt lgkmcnt(0)\n\t" \
    "s_sub_u32 s69, s70, s56\n\t" \
    "s_cmp_gt_i32 s69, 24\n\t" \
    "s_cbranch_scc1 vv_W115_%=\n\t" \
    "global_load_dword v140, v35, s[46:47] sc1\n\t" \
    "s_waitcnt vmcnt(0)\n\t" \
    "v_cmp_eq_u32 vcc, -1, v140\n\t" \
    "s_cbranch_vccz vv_Ook112_%=\n\t" \
    "vv_O111_%=:\n\t" \
    "global_load_dword v140, v35, s[46:47] offset:1536 sc1\n\t" \
    "s_waitcnt vmcnt(0)\n\t" \
    "v_cmp_eq_u32 vcc, -1, v140\n\t" \
    "s_cbranch_vccz vv_Ook112_%=\n\t" \
    "s_cmp_eq_u32 s68, 0\n\t" \
    "s_cbranch_scc1 vv_F116_%=\n\t" \
    "s_sub_u32 s68, s68, 1\n\t" \
    "s_sleep 3\n\t" \
    "s_branch vv_O111_%=\n\t" \
    "vv_F116_%=:\n\t" \
    "v_mov_b32 v141, 6095104\n\t" \
    "v_mov_b32 v142, 17\n\t" \
    "global_store_dword v141, v142, s[36:37] sc1\n\t" \
    "s_waitcnt vmcnt(0)\n\t" \
    "s_branch vv_Ook112_%=\n\t" \
    "vv_Ook112_%=:\n\t" \
    "v_subrev_u32 v140, s80, v140\n\t" \
    "s_nop 1\n\t" \
    "v_max_i32_dpp v140, v140, v140 row_ror:1 row_mask:0xf bank_mask:0xf\n\t" \
    "s_nop 1\n\t" \
    "v_max_i32_dpp v140, v140, v140 row_ror:2 row_mask:0xf bank_mask:0xf\n\t" \
    "s_nop 1\n\t" \
    "v_max_i32_dpp v140, v140, v140 row_ror:4 row_mask:0xf bank_mask:0xf\n\t" \
    "s_nop 1\n\t" \
    "v_max_i32_dpp v140, v140, v140 row_ror:8 row_mask:0xf bank_mask:0xf\n\t" \
    "s_nop 0\n\t" \
    "v_readlane_b32 s69, v140, 0\n\t" \
    "v_readlane_b32 s71, v140, 16\n\t" \
    "v_readlane_b32 s70, v140, 32\n\t" \
    "v_readlane_b32 s81, v140, 48\n\t" \
    "s_max_i32 s69, s69, s71\n\t" \
    "s_nop 0\n\t" \
    "s_max_i32 s71, s70, s81\n\t" \
    "s_max_i32 s69, s69, s71\n\t" \
    "s_add_u32 s81, s69, s80\n\t" \
    "vv_Oc113_%=:\n\t" \
    "s_mul_hi_u32 s69, s85, s78\n\t" \
    "s_add_u32 s79, s81, s69\n\t" \
    "s_add_u32 s79, s79, s87\n\t" \
    "s_cmp_ge_u32 s65, 36\n\t" \
    "s_cselect_b32 s69, 28, 0\n\t" \
    "s_sub_u32 s79, s79, s69\n\t" \
    "s_add_u32 s80, s79, s88\n\t" \
    "s_add_u32 s65, s65, 1\n\t" \
    "s_branch vv_go94_%=\n\t" \
    "vv_Gbad99_%=:\n\t" \
    "s_waitcnt vmcnt(0) lgkmcnt(0)\n\t" \
    "s_nop 7\n\t" \
    "v_mov_b32 v72, 0\n\t" \
    "v_mov_b32 v73, 0\n\t" \
    "v_mov_b32 v74, 0\n\t" \
    "v_mov_b32 v75, 0\n\t" \
    "v_mov_b32 v76, 0\n\t" \
    "v_mov_b32 v77, 0\n\t" \
    "v_mov_b32 v78, 0\n\t" \
    "v_mov_b32 v79, 0\n\t" \
    "s_cmp_eq_u32 s68, 0\n\t" \
    "s_cbranch_scc1 vv_F117_%=\n\t" \
    "s_sub_u32 s68, s68, 1\n\t" \
    "s_branch vv_G98_%=\n\t" \
    "vv_F117_%=:\n\t" \
    "v_mov_b32 v140, 6095104\n\t" \
    "v_mov_b32 v141, 1\n\t" \
    "global_store_dword v140, v141, s[36:37] sc1\n\t" \
    "s_waitcnt vmcnt(0)\n\t" \
    "s_branch vv_Gc101_%=\n\t" \
    "vv_Gbad2100_%=:\n\t" \
    "global_load_dwordx4 v[44:47], v2, s[46:47] sc1\n\t" \
    "s_waitcnt vmcnt(0)\n\t" \
    "v_max3_u32 v140, v44, v45, v46\n\t" \
    "v_max_u32 v140, v140, v47\n\t" \
    "v_cmp_eq_u32 vcc, -1, v140\n\t" \
    "s_cbranch_vccz vv_Gre2103_%=\n\t" \
    "s_cmp_eq_u32 s68, 0\n\t" \
    "s_cbranch_scc1 vv_F118_%=\n\t" \
    "s_sub_u32 s68, s68, 1\n\t" \
    "s_sleep 1\n\t" \
    "s_branch vv_Gbad2100_%=\n\t" \
    "vv_F118_%=:\n\t" \
    "v_mov_b32 v140, 6095104\n\t" \
    "v_mov_b32 v141, 2\n\t" \
    "global_store_dword v140, v141, s[36:37] sc1\n\t" \
    "s_waitcnt vmcnt(0)\n\t" \
    "s_branch vv_Gre2103_%=\n\t" \
    "vv_Gre2103_%=:\n\t" \
    "v_mov_b32 v72, 0\n\t" \
    "v_mov_b32 v73, 0\n\t" \
    "v_mov_b32 v74, 0\n\t" \
    "v_mov_b32 v75, 0\n\t" \
    "v_mov_b32 v76, 0\n\t" \
    "v_mov_b32 v77, 0\n\t" \
    "v_mov_b32 v78, 0\n\t" \
    "v_mov_b32 v79, 0\n\t" \
    "v_smfmac_f32_16x16x64_bf16 v[72:75], v[36:39], a[48:55], v10\n\t" \
    "s_nop 0\n\t" \
    "v_smfmac_f32_16x16x64_bf16 v[76:79], v[36:39], a[120:127], v10\n\t" \
    "v_smfmac_f32_16x16x64_bf16 v[72:75], v[40:43], a[56:63], v10\n\t" \
    "v_smfmac_f32_16x16x64_bf16 v[76:79], v[40:43], a[128:135], v10\n\t" \
    "v_smfmac_f32_16x16x64_bf16 v[72:75], v[44:47], a[64:71], v10\n\t" \
    "v_smfmac_f32_16x16x64_bf16 v[76:79], v[44:47], a[136:143], v10\n\t" \
    "s_branch vv_Gc2102_%=\n\t" \
    "vv_end93_%=:\n\t" \
    "s_endpgm\n\t" \
    "gx_begin_%=:\n\t" \
    "v_mbcnt_lo_u32_b32 v223, -1, 0\n\t" \
    "v_mbcnt_hi_u32_b32 v223, -1, v223\n\t" \
    "s_mov_b32 s68, 262144\n\t" \
    "s_mov_b32 s72, 0\n\t" \
    "s_mov_b32 s83, -1\n\t" \
    "s_mov_b32 s65, 0\n\t" \
    "v_and_b32 v180, 3, v223\n\t" \
    "v_lshlrev_b32 v202, 3, v180\n\t" \
    "v_lshrrev_b32 v181, 5, v223\n\t" \
    "v_lshl_add_u32 v202, v181, 12, v202\n\t" \
    "v_bfe_u32 v182, v223, 2, 2\n\t" \
    "s_mov_b32 s70, 288\n\t" \
    "v_mad_u32_u24 v202, v182, s70, v202\n\t" \
    "v_bfe_u32 v183, v223, 4, 1\n\t" \
    "s_mov_b32 s70, 2176\n\t" \
    "v_mad_u32_u24 v202, v183, s70, v202\n\t" \
    "s_lshl_b32 s69, s66, 6\n\t" \
    "v_xor_b32 v202, s69, v202\n\t" \
    "v_xor_b32 v203, 32, v202\n\t" \
    "v_lshlrev_b32 v3, 4, v180\n\t" \
    "v_lshlrev_b32 v2, 5, v181\n\t" \
    "v_xor_b32 v3, v3, v2\n\t" \
    "s_bfe_u32 s71, s66, 0x10000\n\t" \
    "s_mul_i32 s69, s71, 576\n\t" \
    "s_bfe_u32 s71, s66, 0x10001\n\t" \
    "s_mul_i32 s71, s71, 18432\n\t" \
    "s_add_u32 s69, s69, s71\n\t" \
    "v_lshrrev_b32 v184, 2, v223\n\t" \
    "v_mov_b32 v1, s69\n\t" \
    "s_mov_b32 s70, 1152\n\t" \
    "v_mad_u32_u24 v1, v184, s70, v1\n\t" \
    "v_add_u32 v1, v1, v3\n\t" \
    "v_and_b32 v185, 15, v223\n\t" \
    "v_lshlrev_b32 v3, 6, v185\n\t" \
    "v_lshrrev_b32 v186, 4, v223\n\t" \
    "v_lshl_add_u32 v3, v186, 4, v3\n\t" \
    "v_bfe_u32 v187, v223, 3, 1\n\t" \
    "v_lshlrev_b32 v2, 5, v187\n\t" \
    "v_xor_b32 v3, v3, v2\n\t" \
    "v_mov_b32 v2, v3\n\t" \
    "s_mul_i32 s89, s66, 9216\n\t" \
    "s_mov_b32 s90, 0\n\t" \
    "s_waitcnt vmcnt(12) lgkmcnt(0)\n\t" \
    "s_barrier\n\t" \
    "s_add_u32 m0, s82, 65536\n\t" \
    "s_nop 0\n\t" \
    "global_load_lds_dwordx4 v200, s[42:43]\n\t" \
    "v_add_u32 v200, 131072, v200\n\t" \
    "s_add_u32 m0, s82, 69632\n\t" \
    "s_nop 0\n\t" \
    "global_load_lds_dwordx4 v200, s[42:43]\n\t" \
    "v_add_u32 v200, 131072, v200\n\t" \
    "s_add_u32 m0, s82, 73728\n\t" \
    "s_nop 0\n\t" \
    "global_load_lds_dwordx4 v200, s[42:43]\n\t" \
    "v_add_u32 v200, 131072, v200\n\t" \
    "s_add_u32 m0, s82, 77824\n\t" \
    "s_nop 0\n\t" \
    "global_load_lds_dwordx4 v200, s[42:43]\n\t" \
    "v_add_u32 v200, 131072, v200\n\t" \
    "v_add_u32 v202, 73728, v202\n\t" \
    "v_add_u32 v203, 73728, v203\n\t" \
    "ds_read_b64_tr_b16 a[144:145], v202 offset:0\n\t" \
    "ds_read_b64_tr_b16 a[146:147], v202 offset:1024\n\t" \
    "ds_read_b64_tr_b16 a[148:149], v202 offset:8192\n\t" \
    "ds_read_b64_tr_b16 a[150:151], v202 offset:9216\n\t" \
    "ds_read_b64_tr_b16 a[216:217], v203 offset:0\n\t" \
    "ds_read_b64_tr_b16 a[218:219], v203 offset:1024\n\t" \
    "ds_read_b64_tr_b16 a[220:221], v203 offset:8192\n\t" \
    "ds_read_b64_tr_b16 a[222:223], v203 offset:9216\n\t" \
    "v_lshlrev_b32 v0, 4, v223\n\t" \
    "s_mul_i32 s69, s66, 2048\n\t" \
    "s_add_u32 s69, s69, 147456\n\t" \
    "v_lshl_add_u32 v4, v223, 4, s69\n\t" \
    "s_mov_b32 s69, 147456\n\t" \
    "v_lshl_add_u32 v5, v223, 4, s69\n\t" \
    "v_lshlrev_b32 v6, 2, v223\n\t" \
    "v_mov_b32 v7, 0\n\t" \
    "s_mul_i32 s69, s66, 2048\n\t" \
    "v_lshl_add_u32 v8, v223, 4, s69\n\t" \
    "s_waitcnt vmcnt(12) lgkmcnt(0)\n\t" \
    "s_barrier\n\t" \
    "s_add_u32 m0, s82, 0\n\t" \
    "s_nop 0\n\t" \
    "global_load_lds_dwordx4 v200, s[42:43]\n\t" \
    "v_add_u32 v200, 131072, v200\n\t" \
    "s_add_u32 m0, s82, 4096\n\t" \
    "s_nop 0\n\t" \
    "global_load_lds_dwordx4 v200, s[42:43]\n\t" \
    "v_add_u32 v200, 131072, v200\n\t" \
    "s_add_u32 m0, s82, 8192\n\t" \
    "s_nop 0\n\t" \
    "global_load_lds_dwordx4 v200, s[42:43]\n\t" \
    "v_add_u32 v200, 131072, v200\n\t" \
    "s_add_u32 m0, s82, 12288\n\t" \
    "s_nop 0\n\t" \
    "global_load_lds_dwordx4 v200, s[42:43]\n\t" \
    "v_add_u32 v200, 131072, v200\n\t" \
    "ds_read_b64_tr_b16 a[152:153], v202 offset:16384\n\t" \
    "ds_read_b64_tr_b16 a[154:155], v202 offset:17408\n\t" \
    "ds_read_b64_tr_b16 a[156:157], v202 offset:24576\n\t" \
    "ds_read_b64_tr_b16 a[158:159], v202 offset:25600\n\t" \
    "ds_read_b64_tr_b16 a[224:225], v203 offset:16384\n\t" \
    "ds_read_b64_tr_b16 a[226:227], v203 offset:17408\n\t" \
    "ds_read_b64_tr_b16 a[228:229], v203 offset:24576\n\t" \
    "ds_read_b64_tr_b16 a[230:231], v203 offset:25600\n\t" \
    "s_waitcnt vmcnt(12) lgkmcnt(0)\n\t" \
    "s_barrier\n\t" \
    "s_add_u32 m0, s82, 16384\n\t" \
    "s_nop 0\n\t" \
    "global_load_lds_dwordx4 v200, s[42:43]\n\t" \
    "v_add_u32 v200, 131072, v200\n\t" \
    "s_add_u32 m0, s82, 20480\n\t" \
    "s_nop 0\n\t" \
    "global_load_lds_dwordx4 v200, s[42:43]\n\t" \
    "v_add_u32 v200, 131072, v200\n\t" \
    "s_add_u32 m0, s82, 24576\n\t" \
    "s_nop 0\n\t" \
    "global_load_lds_dwordx4 v200, s[42:43]\n\t" \
    "v_add_u32 v200, 131072, v200\n\t" \
    "s_add_u32 m0, s82, 28672\n\t" \
    "s_nop 0\n\t" \
    "global_load_lds_dwordx4 v200, s[42:43]\n\t" \
    "v_add_u32 v200, 131072, v200\n\t" \
    "ds_read_b64_tr_b16 a[160:161], v202 offset:32768\n\t" \
    "ds_read_b64_tr_b16 a[162:163], v202 offset:33792\n\t" \
    "ds_read_b64_tr_b16 a[164:165], v202 offset:40960\n\t" \
    "ds_read_b64_tr_b16 a[166:167], v202 offset:41984\n\t" \
    "ds_read_b64_tr_b16 a[232:233], v203 offset:32768\n\t" \
    "ds_read_b64_tr_b16 a[234:235], v203 offset:33792\n\t" \
    "ds_read_b64_tr_b16 a[236:237], v203 offset:40960\n\t" \
    "ds_read_b64_tr_b16 a[238:239], v203 offset:41984\n\t" \
    "s_waitcnt vmcnt(12) lgkmcnt(0)\n\t" \
    "s_barrier\n\t" \
    "s_add_u32 m0, s82, 32768\n\t" \
    "s_nop 0\n\t" \
    "global_load_lds_dwordx4 v200, s[42:43]\n\t" \
    "v_add_u32 v200, 131072, v200\n\t" \
    "s_add_u32 m0, s82, 36864\n\t" \
    "s_nop 0\n\t" \
    "global_load_lds_dwordx4 v200, s[42:43]\n\t" \
    "v_add_u32 v200, 131072, v200\n\t" \
    "s_add_u32 m0, s82, 40960\n\t" \
    "s_nop 0\n\t" \
    "global_load_lds_dwordx4 v200, s[42:43]\n\t" \
    "v_add_u32 v200, 131072, v200\n\t" \
    "s_add_u32 m0, s82, 45056\n\t" \
    "s_nop 0\n\t" \
    "global_load_lds_dwordx4 v200, s[42:43]\n\t" \
    "v_add_u32 v200, 131072, v200\n\t" \
    "s_add_u32 m0, s89, 0\n\t" \
    "s_nop 0\n\t" \
    "global_load_lds_dwordx4 v1, s[50:51] offset:0\n\t" \
    "s_add_u32 m0, s89, 960\n\t" \
    "s_nop 0\n\t" \
    "global_load_lds_dwordx4 v1, s[50:51] offset:64\n\t" \
    "s_add_u32 m0, s89, 1920\n\t" \
    "s_nop 0\n\t" \
    "global_load_lds_dwordx4 v1, s[50:51] offset:128\n\t" \
    "s_add_u32 m0, s89, 2880\n\t" \
    "s_nop 0\n\t" \
    "global_load_lds_dwordx4 v1, s[50:51] offset:192\n\t" \
    "s_add_u32 m0, s89, 3840\n\t" \
    "s_nop 0\n\t" \
    "global_load_lds_dwordx4 v1, s[50:51] offset:256\n\t" \
    "s_add_u32 m0, s89, 4800\n\t" \
    "s_nop 0\n\t" \
    "global_load_lds_dwordx4 v1, s[50:51] offset:320\n\t" \
    "s_add_u32 m0, s89, 5760\n\t" \
    "s_nop 0\n\t" \
    "global_load_lds_dwordx4 v1, s[50:51] offset:384\n\t" \
    "s_add_u32 m0, s89, 6720\n\t" \
    "s_nop 0\n\t" \
    "global_load_lds_dwordx4 v1, s[50:51] offset:448\n\t" \
    "s_add_u32 m0, s89, 7680\n\t" \
    "s_nop 0\n\t" \
    "global_load_lds_dwordx4 v1, s[50:51] offset:512\n\t" \
    "ds_read_b64_tr_b16 a[168:169], v202 offset:49152\n\t" \
    "ds_read_b64_tr_b16 a[170:171], v202 offset:50176\n\t" \
    "ds_read_b64_tr_b16 a[172:173], v202 offset:57344\n\t" \
    "ds_read_b64_tr_b16 a[174:175], v202 offset:58368\n\t" \
    "ds_read_b64_tr_b16 a[240:241], v203 offset:49152\n\t" \
    "ds_read_b64_tr_b16 a[242:243], v203 offset:50176\n\t" \
    "ds_read_b64_tr_b16 a[244:245], v203 offset:57344\n\t" \
    "ds_read_b64_tr_b16 a[246:247], v203 offset:58368\n\t" \
    "s_waitcnt vmcnt(21) lgkmcnt(0)\n\t" \
    "s_barrier\n\t" \
    "s_add_u32 m0, s82, 49152\n\t" \
    "s_nop 0\n\t" \
    "global_load_lds_dwordx4 v200, s[42:43]\n\t" \
    "v_add_u32 v200, 131072, v200\n\t" \
    "s_add_u32 m0, s82, 53248\n\t" \
    "s_nop 0\n\t" \
    "global_load_lds_dwordx4 v200, s[42:43]\n\t" \
    "v_add_u32 v200, 131072, v200\n\t" \
    "s_add_u32 m0, s82, 57344\n\t" \
    "s_nop 0\n\t" \
    "global_load_lds_dwordx4 v200, s[42:43]\n\t" \
    "v_add_u32 v200, 131072, v200\n\t" \
    "s_add_u32 m0, s82, 61440\n\t" \
    "s_nop 0\n\t" \
    "global_load_lds_dwordx4 v200, s[42:43]\n\t" \
    "v_add_u32 v200, 131072, v200\n\t" \
    "s_add_u32 s50, s50, 36864\n\t" \
    "s_addc_u32 s51, s51, 0\n\t" \
    "s_add_u32 m0, s89, 36864\n\t" \
    "s_nop 0\n\t" \
    "global_load_lds_dwordx4 v1, s[50:51] offset:0\n\t" \
    "s_add_u32 m0, s89, 37824\n\t" \
    "s_nop 0\n\t" \
    "global_load_lds_dwordx4 v1, s[50:51] offset:64\n\t" \
    "s_add_u32 m0, s89, 38784\n\t" \
    "s_nop 0\n\t" \
    "global_load_lds_dwordx4 v1, s[50:51] offset:128\n\t" \
    "s_add_u32 m0, s89, 39744\n\t" \
    "s_nop 0\n\t" \
    "global_load_lds_dwordx4 v1, s[50:51] offset:192\n\t" \
    "s_add_u32 m0, s89, 40704\n\t" \
    "s_nop 0\n\t" \
    "global_load_lds_dwordx4 v1, s[50:51] offset:256\n\t" \
    "s_add_u32 m0, s89, 41664\n\t" \
    "s_nop 0\n\t" \
    "global_load_lds_dwordx4 v1, s[50:51] offset:320\n\t" \
    "s_add_u32 m0, s89, 42624\n\t" \
    "s_nop 0\n\t" \
    "global_load_lds_dwordx4 v1, s[50:51] offset:384\n\t" \
    "s_add_u32 m0, s89, 43584\n\t" \
    "s_nop 0\n\t" \
    "global_load_lds_dwordx4 v1, s[50:51] offset:448\n\t" \
    "s_add_u32 m0, s89, 44544\n\t" \
    "s_nop 0\n\t" \
    "global_load_lds_dwordx4 v1, s[50:51] offset:512\n\t" \
    "v_add_u32 v202, 65536, v202\n\t" \
    "v_add_u32 v203, 65536, v203\n\t" \
    "ds_read_b64_tr_b16 a[176:177], v202 offset:0\n\t" \
    "ds_read_b64_tr_b16 a[178:179], v202 offset:1024\n\t" \
    "ds_read_b64_tr_b16 a[180:181], v202 offset:8192\n\t" \
    "ds_read_b64_tr_b16 a[182:183], v202 offset:9216\n\t" \
    "ds_read_b64_tr_b16 a[248:249], v203 offset:0\n\t" \
    "ds_read_b64_tr_b16 a[250:251], v203 offset:1024\n\t" \
    "ds_read_b64_tr_b16 a[252:253], v203 offset:8192\n\t" \
    "ds_read_b64_tr_b16 a[254:255], v203 offset:9216\n\t" \
    "s_waitcnt vmcnt(30) lgkmcnt(0)\n\t" \
    "s_barrier\n\t" \
    "v_subrev_u32 v200, 4718592, v200\n\t" \
    "s_add_u32 m0, s82, 65536\n\t" \
    "s_nop 0\n\t" \
    "global_load_lds_dwordx4 v200, s[44:45]\n\t" \
    "v_add_u32 v200, 131072, v200\n\t" \
    "s_add_u32 m0, s82, 69632\n\t" \
    "s_nop 0\n\t" \
    "global_load_lds_dwordx4 v200, s[44:45]\n\t" \
    "v_add_u32 v200, 131072, v200\n\t" \
    "s_add_u32 m0, s82, 73728\n\t" \
    "s_nop 0\n\t" \
    "global_load_lds_dwordx4 v200, s[44:45]\n\t" \
    "v_add_u32 v200, 131072, v200\n\t" \
    "s_add_u32 m0, s82, 77824\n\t" \
    "s_nop 0\n\t" \
    "global_load_lds_dwordx4 v200, s[44:45]\n\t" \
    "v_add_u32 v200, 131072, v200\n\t" \
    "v_add_u32 v202, -65536, v202\n\t" \
    "v_add_u32 v203, -65536, v203\n\t" \
    "ds_read_b64_tr_b16 a[184:185], v202 offset:0\n\t" \
    "ds_read_b64_tr_b16 a[186:187], v202 offset:1024\n\t" \
    "ds_read_b64_tr_b16 a[188:189], v202 offset:8192\n\t" \
    "ds_read_b64_tr_b16 a[190:191], v202 offset:9216\n\t" \
    "ds_read_b64_tr_b16 v[224:225], v203 offset:0\n\t" \
    "ds_read_b64_tr_b16 v[226:227], v203 offset:1024\n\t" \
    "ds_read_b64_tr_b16 v[228:229], v203 offset:8192\n\t" \
    "ds_read_b64_tr_b16 v[230:231], v203 offset:9216\n\t" \
    "s_waitcnt vmcnt(30) lgkmcnt(0)\n\t" \
    "s_barrier\n\t" \
    "s_add_u32 m0, s82, 0\n\t" \
    "s_nop 0\n\t" \
    "global_load_lds_dwordx4 v200, s[44:45]\n\t" \
    "v_add_u32 v200, 131072, v200\n\t" \
    "s_add_u32 m0, s82, 4096\n\t" \
    "s_nop 0\n\t" \
    "global_load_lds_dwordx4 v200, s[44:45]\n\t" \
    "v_add_u32 v200, 131072, v200\n\t" \
    "s_add_u32 m0, s82, 8192\n\t" \
    "s_nop 0\n\t" \
    "global_load_lds_dwordx4 v200, s[44:45]\n\t" \
    "v_add_u32 v200, 131072, v200\n\t" \
    "s_add_u32 m0, s82, 12288\n\t" \
    "s_nop 0\n\t" \
    "global_load_lds_dwordx4 v200, s[44:45]\n\t" \
    "v_add_u32 v200, 131072, v200\n\t" \
    "ds_read_b64_tr_b16 a[192:193], v202 offset:16384\n\t" \
    "ds_read_b64_tr_b16 a[194:195], v202 offset:17408\n\t" \
    "ds_read_b64_tr_b16 a[196:197], v202 offset:24576\n\t" \
    "ds_read_b64_tr_b16 a[198:199], v202 offset:25600\n\t" \
    "ds_read_b64_tr_b16 v[232:233], v203 offset:16384\n\t" \
    "ds_read_b64_tr_b16 v[234:235], v203 offset:17408\n\t" \
    "ds_read_b64_tr_b16 v[236:237], v203 offset:24576\n\t" \
    "ds_read_b64_tr_b16 v[238:239], v203 offset:25600\n\t" \
    "s_waitcnt vmcnt(30) lgkmcnt(0)\n\t" \
    "s_barrier\n\t" \
    "s_add_u32 m0, s82, 16384\n\t" \
    "s_nop 0\n\t" \
    "global_load_lds_dwordx4 v200, s[44:45]\n\t" \
    "v_add_u32 v200, 131072, v200\n\t" \
    "s_add_u32 m0, s82, 20480\n\t" \
    "s_nop 0\n\t" \
    "global_load_lds_dwordx4 v200, s[44:45]\n\t" \
    "v_add_u32 v200, 131072, v200\n\t" \
    "s_add_u32 m0, s82, 24576\n\t" \
    "s_nop 0\n\t" \
    "global_load_lds_dwordx4 v200, s[44:45]\n\t" \
    "v_add_u32 v200, 131072, v200\n\t" \
    "s_add_u32 m0, s82, 28672\n\t" \
    "s_nop 0\n\t" \
    "global_load_lds_dwordx4 v200, s[44:45]\n\t" \
    "v_add_u32 v200, 131072, v200\n\t" \
    "ds_read_b64_tr_b16 a[200:201], v202 offset:32768\n\t" \
    "ds_read_b64_tr_b16 a[202:203], v202 offset:33792\n\t" \
    "ds_read_b64_tr_b16 a[204:205], v202 offset:40960\n\t" \
    "ds_read_b64_tr_b16 a[206:207], v202 offset:41984\n\t" \
    "ds_read_b64_tr_b16 v[240:241], v203 offset:32768\n\t" \
    "ds_read_b64_tr_b16 v[242:243], v203 offset:33792\n\t" \
    "ds_read_b64_tr_b16 v[244:245], v203 offset:40960\n\t" \
    "ds_read_b64_tr_b16 v[246:247], v203 offset:41984\n\t" \
    "s_waitcnt vmcnt(21) lgkmcnt(0)\n\t" \
    "s_barrier\n\t" \
    "s_add_u32 m0, s82, 32768\n\t" \
    "s_nop 0\n\t" \
    "global_load_lds_dwordx4 v200, s[44:45]\n\t" \
    "v_add_u32 v200, 131072, v200\n\t" \
    "s_add_u32 m0, s82, 36864\n\t" \
    "s_nop 0\n\t" \
    "global_load_lds_dwordx4 v200, s[44:45]\n\t" \
    "v_add_u32 v200, 131072, v200\n\t" \
    "s_add_u32 m0, s82, 40960\n\t" \
    "s_nop 0\n\t" \
    "global_load_lds_dwordx4 v200, s[44:45]\n\t" \
    "v_add_u32 v200, 131072, v200\n\t" \
    "s_add_u32 m0, s82, 45056\n\t" \
    "s_nop 0\n\t" \
    "global_load_lds_dwordx4 v200, s[44:45]\n\t" \
    "v_add_u32 v200, 131072, v200\n\t" \
    "v_mbcnt_lo_u32_b32 v164, -1, 0\n\t" \
    "v_mbcnt_hi_u32_b32 v164, -1, v164\n\t" \
    "s_cmp_eq_u32 s66, 0\n\t" \
    "s_cselect_b32 s69, 59, 11\n\t" \
    "v_min_u32 v164, s69, v164\n\t" \
    "v_lshlrev_b32 v164, 6, v164\n\t" \
    "v_add_u32 v164, 6225920, v164\n\t" \
    "v_mov_b32 v165, 6234112\n\t" \
    "global_load_dword v165, v165, s[36:37] sc1\n\t" \
    "global_load_dwordx2 v[166:167], v164, s[36:37] sc1\n\t" \
    "ds_read_b64_tr_b16 a[208:209], v202 offset:49152\n\t" \
    "ds_read_b64_tr_b16 a[210:211], v202 offset:50176\n\t" \
    "ds_read_b64_tr_b16 a[212:213], v202 offset:57344\n\t" \
    "ds_read_b64_tr_b16 a[214:215], v202 offset:58368\n\t" \
    "ds_read_b64_tr_b16 v[248:249], v203 offset:49152\n\t" \
    "ds_read_b64_tr_b16 v[250:251], v203 offset:50176\n\t" \
    "ds_read_b64_tr_b16 v[252:253], v203 offset:57344\n\t" \
    "ds_read_b64_tr_b16 v[254:255], v203 offset:58368\n\t" \
    "s_waitcnt vmcnt(14) lgkmcnt(0)\n\t" \
    "s_barrier\n\t" \
    "s_add_u32 m0, s82, 49152\n\t" \
    "s_nop 0\n\t" \
    "global_load_lds_dwordx4 v200, s[44:45]\n\t" \
    "v_add_u32 v200, 131072, v200\n\t" \
    "s_add_u32 m0, s82, 53248\n\t" \
    "s_nop 0\n\t" \
    "global_load_lds_dwordx4 v200, s[44:45]\n\t" \
    "v_add_u32 v200, 131072, v200\n\t" \
    "s_add_u32 m0, s82, 57344\n\t" \
    "s_nop 0\n\t" \
    "global_load_lds_dwordx4 v200, s[44:45]\n\t" \
    "v_add_u32 v200, 131072, v200\n\t" \
    "s_add_u32 m0, s82, 61440\n\t" \
    "s_nop 0\n\t" \
    "global_load_lds_dwordx4 v200, s[44:45]\n\t" \
    "v_add_u32 v200, 131072, v200\n\t" \
    "v_add_u32 v202, 65536, v202\n\t" \
    "v_add_u32 v203, 65536, v203\n\t" \
    "ds_read_b64_tr_b16 a[0:1], v202 offset:0\n\t" \
    "ds_read_b64_tr_b16 a[2:3], v202 offset:1024\n\t" \
    "ds_read_b64_tr_b16 a[4:5], v202 offset:8192\n\t" \
    "ds_read_b64_tr_b16 a[6:7], v202 offset:9216\n\t" \
    "ds_read_b64_tr_b16 a[72:73], v203 offset:0\n\t" \
    "ds_read_b64_tr_b16 a[74:75], v203 offset:1024\n\t" \
    "ds_read_b64_tr_b16 a[76:77], v203 offset:8192\n\t" \
    "ds_read_b64_tr_b16 a[78:79], v203 offset:9216\n\t" \
    "s_waitcnt lgkmcnt(6)\n\t" \
    "v_add_u32 v2, 36864, v3\n\t" \
    "ds_read_b128 v[24:27], v2 offset:0\n\t" \
    "ds_read_b128 v[28:31], v2 offset:18432\n\t" \
    "ds_read_b128 v[32:35], v2 offset:1024\n\t" \
    "ds_read_b128 v[36:39], v2 offset:19456\n\t" \
    "s_waitcnt lgkmcnt(3)\n\t" \
    "v_mfma_f32_16x16x32_bf16 v[88:91], a[144:147], v[24:27], 0\n\t" \
    "ds_read_b128 v[40:43], v2 offset:2048\n\t" \
    "s_waitcnt lgkmcnt(3)\n\t" \
    "v_mfma_f32_16x16x32_bf16 v[92:95], a[144:147], v[28:31], 0\n\t" \
    "ds_read_b128 v[44:47], v2 offset:20480\n\t" \
    "v_mfma_f32_16x16x32_bf16 v[96:99], a[216:219], v[24:27], 0\n\t" \
    "v_mfma_f32_16x16x32_bf16 v[100:103], a[216:219], v[28:31], 0\n\t" \
    "s_waitcnt lgkmcnt(3)\n\t" \
    "v_mfma_f32_16x16x32_bf16 v[88:91], a[148:151], v[32:35], v[88:91]\n\t" \
    "ds_read_b128 v[48:51], v2 offset:3072\n\t" \
    "s_waitcnt lgkmcnt(3)\n\t" \
    "v_mfma_f32_16x16x32_bf16 v[92:95], a[148:151], v[36:39], v[92:95]\n\t" \
    "ds_read_b128 v[52:55], v2 offset:21504\n\t" \
    "v_mfma_f32_16x16x32_bf16 v[96:99], a[220:223], v[32:35], v[96:99]\n\t" \
    "v_mfma_f32_16x16x32_bf16 v[100:103], a[220:223], v[36:39], v[100:103]\n\t" \
    "s_waitcnt lgkmcnt(3)\n\t" \
    "v_mfma_f32_16x16x32_bf16 v[88:91], a[152:155], v[40:43], v[88:91]\n\t" \
    "ds_read_b128 v[56:59], v2 offset:4096\n\t" \
    "s_waitcnt lgkmcnt(3)\n\t" \
    "v_mfma_f32_16x16x32_bf16 v[92:95], a[152:155], v[44:47], v[92:95]\n\t" \
    "ds_read_b128 v[60:63], v2 offset:22528\n\t" \
    "v_mfma_f32_16x16x32_bf16 v[96:99], a[224:227], v[40:43], v[96:99]\n\t" \
    "v_mfma_f32_16x16x32_bf16 v[100:103], a[224:227], v[44:47], v[100:103]\n\t" \
    "s_waitcnt lgkmcnt(3)\n\t" \
    "v_mfma_f32_16x16x32_bf16 v[88:91], a[156:159], v[48:51], v[88:91]\n\t" \
    "ds_read_b128 v[64:67], v2 offset:5120\n\t" \
    "s_waitcnt lgkmcnt(3)\n\t" \
    "v_mfma_f32_16x16x32_bf16 v[92:95], a[156:159], v[52:55], v[92:95]\n\t" \
    "ds_read_b128 v[68:71], v2 offset:23552\n\t" \
    "v_mfma_f32_16x16x32_bf16 v[96:99], a[228:231], v[48:51], v[96:99]\n\t" \
    "v_mfma_f32_16x16x32_bf16 v[100:103], a[228:231], v[52:55], v[100:103]\n\t" \
    "s_waitcnt lgkmcnt(3)\n\t" \
    "v_mfma_f32_16x16x32_bf16 v[88:91], a[160:163], v[56:59], v[88:91]\n\t" \
    "ds_read_b128 v[24:27], v2 offset:6144\n\t" \
    "s_waitcnt lgkmcnt(3)\n\t" \
    "v_mfma_f32_16x16x32_bf16 v[92:95], a[160:163], v[60:63], v[92:95]\n\t" \
    "ds_read_b128 v[28:31], v2 offset:24576\n\t" \
    "v_mfma_f32_16x16x32_bf16 v[96:99], a[232:235], v[56:59], v[96:99]\n\t" \
    "v_mfma_f32_16x16x32_bf16 v[100:103], a[232:235], v[60:63], v[100:103]\n\t" \
    "s_waitcnt lgkmcnt(3)\n\t" \
    "v_mfma_f32_16x16x32_bf16 v[88:91], a[164:167], v[64:67], v[88:91]\n\t" \
    "ds_read_b128 v[32:35], v2 offset:7168\n\t" \
    "s_waitcnt lgkmcnt(3)\n\t" \
    "v_mfma_f32_16x16x32_bf16 v[92:95], a[164:167], v[68:71], v[92:95]\n\t" \
    "ds_read_b128 v[36:39], v2 offset:25600\n\t" \
    "v_mfma_f32_16x16x32_bf16 v[96:99], a[236:239], v[64:67], v[96:99]\n\t" \
    "v_mfma_f32_16x16x32_bf16 v[100:103], a[236:239], v[68:71], v[100:103]\n\t" \
    "s_waitcnt lgkmcnt(3)\n\t" \
    "v_mfma_f32_16x16x32_bf16 v[88:91], a[168:171], v[24:27], v[88:91]\n\t" \
    "ds_read_b128 v[40:43], v2 offset:8192\n\t" \
    "s_waitcnt lgkmcnt(3)\n\t" \
    "v_mfma_f32_16x16x32_bf16 v[92:95], a[168:171], v[28:31], v[92:95]\n\t" \
    "ds_read_b128 v[44:47], v2 offset:26624\n\t" \
    "v_mfma_f32_16x16x32_bf16 v[96:99], a[240:243], v[24:27], v[96:99]\n\t" \
    "v_mfma_f32_16x16x32_bf16 v[100:103], a[240:243], v[28:31], v[100:103]\n\t" \
    "s_waitcnt lgkmcnt(3)\n\t" \
    "v_mfma_f32_16x16x32_bf16 v[88:91], a[172:175], v[32:35], v[88:91]\n\t" \
    "s_waitcnt lgkmcnt(2)\n\t" \
    "v_mfma_f32_16x16x32_bf16 v[92:95], a[172:175], v[36:39], v[92:95]\n\t" \
    "v_mfma_f32_16x16x32_bf16 v[96:99], a[244:247], v[32:35], v[96:99]\n\t" \
    "v_mfma_f32_16x16x32_bf16 v[100:103], a[244:247], v[36:39], v[100:103]\n\t" \
    "s_waitcnt lgkmcnt(1)\n\t" \
    "v_mfma_f32_16x16x32_bf16 v[88:91], a[176:179], v[40:43], v[88:91]\n\t" \
    "s_waitcnt lgkmcnt(0)\n\t" \
    "v_mfma_f32_16x16x32_bf16 v[92:95], a[176:179], v[44:47], v[92:95]\n\t" \
    "v_mfma_f32_16x16x32_bf16 v[96:99], a[248:251], v[40:43], v[96:99]\n\t" \
    "v_mfma_f32_16x16x32_bf16 v[100:103], a[248:251], v[44:47], v[100:103]\n\t" \
    "s_waitcnt vmcnt(14) lgkmcnt(0)\n\t" \
    "s_barrier\n\t" \
    "s_add_u32 m0, s82, 65536\n\t" \
    "s_nop 0\n\t" \
    "global_load_lds_dwordx4 v200, s[44:45]\n\t" \
    "v_add_u32 v200, 131072, v200\n\t" \
    "s_add_u32 m0, s82, 69632\n\t" \
    "s_nop 0\n\t" \
    "global_load_lds_dwordx4 v200, s[44:45]\n\t" \
    "v_add_u32 v200, 131072, v200\n\t" \
    "s_add_u32 m0, s82, 73728\n\t" \
    "s_nop 0\n\t" \
    "global_load_lds_dwordx4 v200, s[44:45]\n\t" \
    "v_add_u32 v200, 131072, v200\n\t" \
    "s_add_u32 m0, s82, 77824\n\t" \
    "s_nop 0\n\t" \
    "global_load_lds_dwordx4 v200, s[44:45]\n\t" \
    "v_add_u32 v200, 131072, v200\n\t" \
    "v_add_u32 v202, -65536, v202\n\t" \
    "v_add_u32 v203, -65536, v203\n\t" \
    "ds_read_b64_tr_b16 a[8:9], v202 offset:0\n\t" \
    "ds_read_b64_tr_b16 a[10:11], v202 offset:1024\n\t" \
    "ds_read_b64_tr_b16 a[12:13], v202 offset:8192\n\t" \
    "ds_read_b64_tr_b16 a[14:15], v202 offset:9216\n\t" \
    "ds_read_b64_tr_b16 a[80:81], v203 offset:0\n\t" \
    "ds_read_b64_tr_b16 a[82:83], v203 offset:1024\n\t" \
    "ds_read_b64_tr_b16 a[84:85], v203 offset:8192\n\t" \
    "ds_read_b64_tr_b16 a[86:87], v203 offset:9216\n\t" \
    "s_waitcnt lgkmcnt(6)\n\t" \
    "ds_read_b128 v[48:51], v2 offset:9216\n\t" \
    "ds_read_b128 v[52:55], v2 offset:27648\n\t" \
    "ds_read_b128 v[56:59], v2 offset:10240\n\t" \
    "ds_read_b128 v[60:63], v2 offset:28672\n\t" \
    "s_waitcnt lgkmcnt(3)\n\t" \
    "v_mfma_f32_16x16x32_bf16 v[88:91], a[180:183], v[48:51], v[88:91]\n\t" \
    "ds_read_b128 v[64:67], v2 offset:11264\n\t" \
    "s_waitcnt lgkmcnt(3)\n\t" \
    "v_mfma_f32_16x16x32_bf16 v[92:95], a[180:183], v[52:55], v[92:95]\n\t" \
    "ds_read_b128 v[68:71], v2 offset:29696\n\t" \
    "v_mfma_f32_16x16x32_bf16 v[96:99], a[252:255], v[48:51], v[96:99]\n\t" \
    "v_mfma_f32_16x16x32_bf16 v[100:103], a[252:255], v[52:55], v[100:103]\n\t" \
    "s_waitcnt lgkmcnt(3)\n\t" \
    "v_mfma_f32_16x16x32_bf16 v[88:91], a[184:187], v[56:59], v[88:91]\n\t" \
    "ds_read_b128 v[24:27], v2 offset:12288\n\t" \
    "s_waitcnt lgkmcnt(3)\n\t" \
    "v_mfma_f32_16x16x32_bf16 v[92:95], a[184:187], v[60:63], v[92:95]\n\t" \
    "ds_read_b128 v[28:31], v2 offset:30720\n\t" \
    "v_mfma_f32_16x16x32_bf16 v[96:99], v[224:227], v[56:59], v[96:99]\n\t" \
    "v_mfma_f32_16x16x32_bf16 v[100:103], v[224:227], v[60:63], v[100:103]\n\t" \
    "s_waitcnt lgkmcnt(3)\n\t" \
    "v_mfma_f32_16x16x32_bf16 v[88:91], a[188:191], v[64:67], v[88:91]\n\t" \
    "ds_read_b128 v[32:35], v2 offset:13312\n\t" \
    "s_waitcnt lgkmcnt(3)\n\t" \
    "v_mfma_f32_16x16x32_bf16 v[92:95], a[188:191], v[68:71], v[92:95]\n\t" \
    "ds_read_b128 v[36:39], v2 offset:31744\n\t" \
    "v_mfma_f32_16x16x32_bf16 v[96:99], v[228:231], v[64:67], v[96:99]\n\t" \
    "v_mfma_f32_16x16x32_bf16 v[100:103], v[228:231], v[68:71], v[100:103]\n\t" \
    "s_waitcnt lgkmcnt(3)\n\t" \
    "v_mfma_f32_16x16x32_bf16 v[88:91], a[192:195], v[24:27], v[88:91]\n\t" \
    "ds_read_b128 v[40:43], v2 offset:14336\n\t" \
    "s_waitcnt lgkmcnt(3)\n\t" \
    "v_mfma_f32_16x16x32_bf16 v[92:95], a[192:195], v[28:31], v[92:95]\n\t" \
    "ds_read_b128 v[44:47], v2 offset:32768\n\t" \
    "v_mfma_f32_16x16x32_bf16 v[96:99], v[232:235], v[24:27], v[96:99]\n\t" \
    "v_mfma_f32_16x16x32_bf16 v[100:103], v[232:235], v[28:31], v[100:103]\n\t" \
    "s_waitcnt lgkmcnt(3)\n\t" \
    "v_mfma_f32_16x16x32_bf16 v[88:91], a[196:199], v[32:35], v[88:91]\n\t" \
    "ds_read_b128 v[48:51], v2 offset:15360\n\t" \
    "s_waitcnt lgkmcnt(3)\n\t" \
    "v_mfma_f32_16x16x32_bf16 v[92:95], a[196:199], v[36:39], v[92:95]\n\t" \
    "ds_read_b128 v[52:55], v2 offset:33792\n\t" \
    "v_mfma_f32_16x16x32_bf16 v[96:99], v[236:239], v[32:35], v[96:99]\n\t" \
    "v_mfma_f32_16x16x32_bf16 v[100:103], v[236:239], v[36:39], v[100:103]\n\t" \
    "s_waitcnt lgkmcnt(3)\n\t" \
    "v_mfma_f32_16x16x32_bf16 v[88:91], a[200:203], v[40:43], v[88:91]\n\t" \
    "ds_read_b128 v[56:59], v2 offset:16384\n\t" \
    "s_waitcnt lgkmcnt(3)\n\t" \
    "v_mfma_f32_16x16x32_bf16 v[92:95], a[200:203], v[44:47], v[92:95]\n\t" \
    "ds_read_b128 v[60:63], v2 offset:34816\n\t" \
    "v_mfma_f32_16x16x32_bf16 v[96:99], v[240:243], v[40:43], v[96:99]\n\t" \
    "v_mfma_f32_16x16x32_bf16 v[100:103], v[240:243], v[44:47], v[100:103]\n\t" \
    "s_waitcnt lgkmcnt(3)\n\t" \
    "v_mfma_f32_16x16x32_bf16 v[88:91], a[204:207], v[48:51], v[88:91]\n\t" \
    "ds_read_b128 v[64:67], v2 offset:17408\n\t" \
    "s_waitcnt lgkmcnt(3)\n\t" \
    "v_mfma_f32_16x16x32_bf16 v[92:95], a[204:207], v[52:55], v[92:95]\n\t" \
    "ds_read_b128 v[68:71], v2 offset:35840\n\t" \
    "v_mfma_f32_16x16x32_bf16 v[96:99], v[244:247], v[48:51], v[96:99]\n\t" \
    "v_mfma_f32_16x16x32_bf16 v[100:103], v[244:247], v[52:55], v[100:103]\n\t" \
    "s_waitcnt lgkmcnt(3)\n\t" \
    "v_mfma_f32_16x16x32_bf16 v[88:91], a[208:211], v[56:59], v[88:91]\n\t" \
    "s_waitcnt lgkmcnt(2)\n\t" \
    "v_mfma_f32_16x16x32_bf16 v[92:95], a[208:211], v[60:63], v[92:95]\n\t" \
    "v_mfma_f32_16x16x32_bf16 v[96:99], v[248:251], v[56:59], v[96:99]\n\t" \
    "v_mfma_f32_16x16x32_bf16 v[100:103], v[248:251], v[60:63], v[100:103]\n\t" \
    "s_waitcnt lgkmcnt(1)\n\t" \
    "v_mfma_f32_16x16x32_bf16 v[88:91], a[212:215], v[64:67], v[88:91]\n\t" \
    "s_waitcnt lgkmcnt(0)\n\t" \
    "v_mfma_f32_16x16x32_bf16 v[92:95], a[212:215], v[68:71], v[92:95]\n\t" \
    "v_mfma_f32_16x16x32_bf16 v[96:99], v[252:255], v[64:67], v[96:99]\n\t" \
    "v_mfma_f32_16x16x32_bf16 v[100:103], v[252:255], v[68:71], v[100:103]\n\t" \
    "s_mul_i32 s70, s67, 7\n\t" \
    "s_add_u32 s69, s65, 1\n\t" \
    "s_xor_b32 s69, s69, s70\n\t" \
    "s_and_b32 s69, s69, 63\n\t" \
    "s_lshl_b32 s69, s69, 13\n\t" \
    "s_mul_i32 s70, s67, 524288\n\t" \
    "s_add_u32 s69, s69, s70\n\t" \
    "s_add_u32 s69, s69, 6291456\n\t" \
    "s_add_u32 s38, s36, s69\n\t" \
    "s_addc_u32 s39, s37, 0\n\t" \
    "s_add_u32 s40, s38, 16777216\n\t" \
    "s_addc_u32 s41, s39, 0\n\t" \
    "v_cvt_pk_bf16_f32 v112, v88, v89\n\t" \
    "v_cvt_pk_bf16_f32 v113, v90, v91\n\t" \
    "v_cvt_pk_bf16_f32 v114, v96, v97\n\t" \
    "v_cvt_pk_bf16_f32 v115, v98, v99\n\t" \
    "global_store_dwordx4 v8, v[112:115], s[40:41] offset:0 sc1\n\t" \
    "v_cvt_pk_bf16_f32 v116, v92, v93\n\t" \
    "v_cvt_pk_bf16_f32 v117, v94, v95\n\t" \
    "v_cvt_pk_bf16_f32 v118, v100, v101\n\t" \
    "v_cvt_pk_bf16_f32 v119, v102, v103\n\t" \
    "global_store_dwordx4 v8, v[116:119], s[40:41] offset:1024 sc1\n\t" \
    "s_waitcnt vmcnt(16) lgkmcnt(0)\n\t" \
    "s_barrier\n\t" \
    "s_add_u32 m0, s82, 0\n\t" \
    "s_nop 0\n\t" \
    "global_load_lds_dwordx4 v200, s[44:45]\n\t" \
    "v_add_u32 v200, 131072, v200\n\t" \
    "s_add_u32 m0, s82, 4096\n\t" \
    "s_nop 0\n\t" \
    "global_load_lds_dwordx4 v200, s[44:45]\n\t" \
    "v_add_u32 v200, 131072, v200\n\t" \
    "s_add_u32 m0, s82, 8192\n\t" \
    "s_nop 0\n\t" \
    "global_load_lds_dwordx4 v200, s[44:45]\n\t" \
    "v_add_u32 v200, 131072, v200\n\t" \
    "s_add_u32 m0, s82, 12288\n\t" \
    "s_nop 0\n\t" \
    "global_load_lds_dwordx4 v200, s[44:45]\n\t" \
    "v_add_u32 v200, 131072, v200\n\t" \
    "ds_read_b64_tr_b16 a[16:17], v202 offset:16384\n\t" \
    "ds_read_b64_tr_b16 a[18:19], v202 offset:17408\n\t" \
    "ds_read_b64_tr_b16 a[20:21], v202 offset:24576\n\t" \
    "ds_read_b64_tr_b16 a[22:23], v202 offset:25600\n\t" \
    "ds_read_b64_tr_b16 a[88:89], v203 offset:16384\n\t" \
    "ds_read_b64_tr_b16 a[90:91], v203 offset:17408\n\t" \
    "ds_read_b64_tr_b16 a[92:93], v203 offset:24576\n\t" \
    "ds_read_b64_tr_b16 a[94:95], v203 offset:25600\n\t" \
    "s_waitcnt lgkmcnt(6)\n\t" \
    "v_add_u32 v2, 0, v3\n\t" \
    "ds_read_b128 v[24:27], v2 offset:0\n\t" \
    "ds_read_b128 v[28:31], v2 offset:18432\n\t" \
    "ds_read_b128 v[32:35], v2 offset:1024\n\t" \
    "ds_read_b128 v[36:39], v2 offset:19456\n\t" \
    "s_waitcnt lgkmcnt(3)\n\t" \
    "v_mfma_f32_16x16x32_bf16 v[88:91], a[144:147], v[24:27], 0\n\t" \
    "ds_read_b128 v[40:43], v2 offset:2048\n\t" \
    "s_waitcnt lgkmcnt(3)\n\t" \
    "v_mfma_f32_16x16x32_bf16 v[92:95], a[144:147], v[28:31], 0\n\t" \
    "ds_read_b128 v[44:47], v2 offset:20480\n\t" \
    "v_mfma_f32_16x16x32_bf16 v[96:99], a[216:219], v[24:27], 0\n\t" \
    "v_mfma_f32_16x16x32_bf16 v[100:103], a[216:219], v[28:31], 0\n\t" \
    "s_waitcnt lgkmcnt(3)\n\t" \
    "v_mfma_f32_16x16x32_bf16 v[88:91], a[148:151], v[32:35], v[88:91]\n\t" \
    "ds_read_b128 v[48:51], v2 offset:3072\n\t" \
    "s_waitcnt lgkmcnt(3)\n\t" \
    "v_mfma_f32_16x16x32_bf16 v[92:95], a[148:151], v[36:39], v[92:95]\n\t" \
    "ds_read_b128 v[52:55], v2 offset:21504\n\t" \
    "v_mfma_f32_16x16x32_bf16 v[96:99], a[220:223], v[32:35], v[96:99]\n\t" \
    "v_mfma_f32_16x16x32_bf16 v[100:103], a[220:223], v[36:39], v[100:103]\n\t" \
    "s_waitcnt lgkmcnt(3)\n\t" \
    "v_mfma_f32_16x16x32_bf16 v[88:91], a[152:155], v[40:43], v[88:91]\n\t" \
    "ds_read_b128 v[56:59], v2 offset:4096\n\t" \
    "s_waitcnt lgkmcnt(3)\n\t" \
    "v_mfma_f32_16x16x32_bf16 v[92:95], a[152:155], v[44:47], v[92:95]\n\t" \
    "ds_read_b128 v[60:63], v2 offset:22528\n\t" \
    "v_mfma_f32_16x16x32_bf16 v[96:99], a[224:227], v[40:43], v[96:99]\n\t" \
    "v_mfma_f32_16x16x32_bf16 v[100:103], a[224:227], v[44:47], v[100:103]\n\t" \
    "s_waitcnt lgkmcnt(3)\n\t" \
    "v_mfma_f32_16x16x32_bf16 v[88:91], a[156:159], v[48:51], v[88:91]\n\t" \
    "ds_read_b128 v[64:67], v2 offset:5120\n\t" \
    "s_waitcnt lgkmcnt(3)\n\t" \
    "v_mfma_f32_16x16x32_bf16 v[92:95], a[156:159], v[52:55], v[92:95]\n\t" \
    "ds_read_b128 v[68:71], v2 offset:23552\n\t" \
    "v_mfma_f32_16x16x32_bf16 v[96:99], a[228:231], v[48:51], v[96:99]\n\t" \
    "v_mfma_f32_16x16x32_bf16 v[100:103], a[228:231], v[52:55], v[100:103]\n\t" \
    "s_waitcnt lgkmcnt(3)\n\t" \
    "v_mfma_f32_16x16x32_bf16 v[88:91], a[160:163], v[56:59], v[88:91]\n\t" \
    "ds_read_b128 v[24:27], v2 offset:6144\n\t" \
    "s_waitcnt lgkmcnt(3)\n\t" \
    "v_mfma_f32_16x16x32_bf16 v[92:95], a[160:163], v[60:63], v[92:95]\n\t" \
    "ds_read_b128 v[28:31], v2 offset:24576\n\t" \
    "v_mfma_f32_16x16x32_bf16 v[96:99], a[232:235], v[56:59], v[96:99]\n\t" \
    "v_mfma_f32_16x16x32_bf16 v[100:103], a[232:235], v[60:63], v[100:103]\n\t" \
    "s_waitcnt lgkmcnt(3)\n\t" \
    "v_mfma_f32_16x16x32_bf16 v[88:91], a[164:167], v[64:67], v[88:91]\n\t" \
    "ds_read_b128 v[32:35], v2 offset:7168\n\t" \
    "s_waitcnt lgkmcnt(3)\n\t" \
    "v_mfma_f32_16x16x32_bf16 v[92:95], a[164:167], v[68:71], v[92:95]\n\t" \
    "ds_read_b128 v[36:39], v2 offset:25600\n\t" \
    "v_mfma_f32_16x16x32_bf16 v[96:99], a[236:239], v[64:67], v[96:99]\n\t" \
    "v_mfma_f32_16x16x32_bf16 v[100:103], a[236:239], v[68:71], v[100:103]\n\t" \
    "s_waitcnt lgkmcnt(3)\n\t" \
    "v_mfma_f32_16x16x32_bf16 v[88:91], a[168:171], v[24:27], v[88:91]\n\t" \
    "ds_read_b128 v[40:43], v2 offset:8192\n\t" \
    "s_waitcnt lgkmcnt(3)\n\t" \
    "v_mfma_f32_16x16x32_bf16 v[92:95], a[168:171], v[28:31], v[92:95]\n\t" \
    "ds_read_b128 v[44:47], v2 offset:26624\n\t" \
    "v_mfma_f32_16x16x32_bf16 v[96:99], a[240:243], v[24:27], v[96:99]\n\t" \
    "v_mfma_f32_16x16x32_bf16 v[100:103], a[240:243], v[28:31], v[100:103]\n\t" \
    "s_waitcnt lgkmcnt(3)\n\t" \
    "v_mfma_f32_16x16x32_bf16 v[88:91], a[172:175], v[32:35], v[88:91]\n\t" \
    "s_waitcnt lgkmcnt(2)\n\t" \
    "v_mfma_f32_16x16x32_bf16 v[92:95], a[172:175], v[36:39], v[92:95]\n\t" \
    "v_mfma_f32_16x16x32_bf16 v[96:99], a[244:247], v[32:35], v[96:99]\n\t" \
    "v_mfma_f32_16x16x32_bf16 v[100:103], a[244:247], v[36:39], v[100:103]\n\t" \
    "s_waitcnt lgkmcnt(1)\n\t" \
    "v_mfma_f32_16x16x32_bf16 v[88:91], a[176:179], v[40:43], v[88:91]\n\t" \
    "s_waitcnt lgkmcnt(0)\n\t" \
    "v_mfma_f32_16x16x32_bf16 v[92:95], a[176:179], v[44:47], v[92:95]\n\t" \
    "v_mfma_f32_16x16x32_bf16 v[96:99], a[248:251], v[40:43], v[96:99]\n\t" \
    "v_mfma_f32_16x16x32_bf16 v[100:103], a[248:251], v[44:47], v[100:103]\n\t" \
    "s_waitcnt vmcnt(16) lgkmcnt(0)\n\t" \
    "s_barrier\n\t" \
    "s_add_u32 m0, s82, 16384\n\t" \
    "s_nop 0\n\t" \
    "global_load_lds_dwordx4 v200, s[44:45]\n\t" \
    "v_add_u32 v200, 131072, v200\n\t" \
    "s_add_u32 m0, s82, 20480\n\t" \
    "s_nop 0\n\t" \
    "global_load_lds_dwordx4 v200, s[44:45]\n\t" \
    "v_add_u32 v200, 131072, v200\n\t" \
    "s_add_u32 m0, s82, 24576\n\t" \
    "s_nop 0\n\t" \
    "global_load_lds_dwordx4 v200, s[44:45]\n\t" \
    "v_add_u32 v200, 131072, v200\n\t" \
    "s_add_u32 m0, s82, 28672\n\t" \
    "s_nop 0\n\t" \
    "global_load_lds_dwordx4 v200, s[44:45]\n\t" \
    "v_add_u32 v200, 131072, v200\n\t" \
    "ds_read_b64_tr_b16 a[24:25], v202 offset:32768\n\t" \
    "ds_read_b64_tr_b16 a[26:27], v202 offset:33792\n\t" \
    "ds_read_b64_tr_b16 a[28:29], v202 offset:40960\n\t" \
    "ds_read_b64_tr_b16 a[30:31], v202 offset:41984\n\t" \
    "ds_read_b64_tr_b16 a[96:97], v203 offset:32768\n\t" \
    "ds_read_b64_tr_b16 a[98:99], v203 offset:33792\n\t" \
    "ds_read_b64_tr_b16 a[100:101], v203 offset:40960\n\t" \
    "ds_read_b64_tr_b16 a[102:103], v203 offset:41984\n\t" \
    "s_waitcnt lgkmcnt(6)\n\t" \
    "ds_read_b128 v[48:51], v2 offset:9216\n\t" \
    "ds_read_b128 v[52:55], v2 offset:27648\n\t" \
    "ds_read_b128 v[56:59], v2 offset:10240\n\t" \
    "ds_read_b128 v[60:63], v2 offset:28672\n\t" \
    "s_waitcnt lgkmcnt(3)\n\t" \
    "v_mfma_f32_16x16x32_bf16 v[88:91], a[180:183], v[48:51], v[88:91]\n\t" \
    "ds_read_b128 v[64:67], v2 offset:11264\n\t" \
    "s_waitcnt lgkmcnt(3)\n\t" \
    "v_mfma_f32_16x16x32_bf16 v[92:95], a[180:183], v[52:55], v[92:95]\n\t" \
    "ds_read_b128 v[68:71], v2 offset:29696\n\t" \
    "v_mfma_f32_16x16x32_bf16 v[96:99], a[252:255], v[48:51], v[96:99]\n\t" \
    "v_mfma_f32_16x16x32_bf16 v[100:103], a[252:255], v[52:55], v[100:103]\n\t" \
    "s_waitcnt lgkmcnt(3)\n\t" \
    "v_mfma_f32_16x16x32_bf16 v[88:91], a[184:187], v[56:59], v[88:91]\n\t" \
    "ds_read_b128 v[24:27], v2 offset:12288\n\t" \
    "s_waitcnt lgkmcnt(3)\n\t" \
    "v_mfma_f32_16x16x32_bf16 v[92:95], a[184:187], v[60:63], v[92:95]\n\t" \
    "ds_read_b128 v[28:31], v2 offset:30720\n\t" \
    "v_mfma_f32_16x16x32_bf16 v[96:99], v[224:227], v[56:59], v[96:99]\n\t" \
    "v_mfma_f32_16x16x32_bf16 v[100:103], v[224:227], v[60:63], v[100:103]\n\t" \
    "s_waitcnt lgkmcnt(3)\n\t" \
    "v_mfma_f32_16x16x32_bf16 v[88:91], a[188:191], v[64:67], v[88:91]\n\t" \
    "ds_read_b128 v[32:35], v2 offset:13312\n\t" \
    "s_waitcnt lgkmcnt(3)\n\t" \
    "v_mfma_f32_16x16x32_bf16 v[92:95], a[188:191], v[68:71], v[92:95]\n\t" \
    "ds_read_b128 v[36:39], v2 offset:31744\n\t" \
    "v_mfma_f32_16x16x32_bf16 v[96:99], v[228:231], v[64:67], v[96:99]\n\t" \
    "v_mfma_f32_16x16x32_bf16 v[100:103], v[228:231], v[68:71], v[100:103]\n\t" \
    "s_waitcnt lgkmcnt(3)\n\t" \
    "v_mfma_f32_16x16x32_bf16 v[88:91], a[192:195], v[24:27], v[88:91]\n\t" \
    "ds_read_b128 v[40:43], v2 offset:14336\n\t" \
    "s_waitcnt lgkmcnt(3)\n\t" \
    "v_mfma_f32_16x16x32_bf16 v[92:95], a[192:195], v[28:31], v[92:95]\n\t" \
    "ds_read_b128 v[44:47], v2 offset:32768\n\t" \
    "v_mfma_f32_16x16x32_bf16 v[96:99], v[232:235], v[24:27], v[96:99]\n\t" \
    "v_mfma_f32_16x16x32_bf16 v[100:103], v[232:235], v[28:31], v[100:103]\n\t" \
    "s_waitcnt lgkmcnt(3)\n\t" \
    "v_mfma_f32_16x16x32_bf16 v[88:91], a[196:199], v[32:35], v[88:91]\n\t" \
    "ds_read_b128 v[48:51], v2 offset:15360\n\t" \
    "s_waitcnt lgkmcnt(3)\n\t" \
    "v_mfma_f32_16x16x32_bf16 v[92:95], a[196:199], v[36:39], v[92:95]\n\t" \
    "ds_read_b128 v[52:55], v2 offset:33792\n\t" \
    "v_mfma_f32_16x16x32_bf16 v[96:99], v[236:239], v[32:35], v[96:99]\n\t" \
    "v_mfma_f32_16x16x32_bf16 v[100:103], v[236:239], v[36:39], v[100:103]\n\t" \
    "s_waitcnt lgkmcnt(3)\n\t" \
    "v_mfma_f32_16x16x32_bf16 v[88:91], a[200:203], v[40:43], v[88:91]\n\t" \
    "ds_read_b128 v[56:59], v2 offset:16384\n\t" \
    "s_waitcnt lgkmcnt(3)\n\t" \
    "v_mfma_f32_16x16x32_bf16 v[92:95], a[200:203], v[44:47], v[92:95]\n\t" \
    "ds_read_b128 v[60:63], v2 offset:34816\n\t" \
    "v_mfma_f32_16x16x32_bf16 v[96:99], v[240:243], v[40:43], v[96:99]\n\t" \
    "v_mfma_f32_16x16x32_bf16 v[100:103], v[240:243], v[44:47], v[100:103]\n\t" \
    "s_waitcnt lgkmcnt(3)\n\t" \
    "v_mfma_f32_16x16x32_bf16 v[88:91], a[204:207], v[48:51], v[88:91]\n\t" \
    "ds_read_b128 v[64:67], v2 offset:17408\n\t" \
    "s_waitcnt lgkmcnt(3)\n\t" \
    "v_mfma_f32_16x16x32_bf16 v[92:95], a[204:207], v[52:55], v[92:95]\n\t" \
    "ds_read_b128 v[68:71], v2 offset:35840\n\t" \
    "v_mfma_f32_16x16x32_bf16 v[96:99], v[244:247], v[48:51], v[96:99]\n\t" \
    "v_mfma_f32_16x16x32_bf16 v[100:103], v[244:247], v[52:55], v[100:103]\n\t" \
    "s_waitcnt lgkmcnt(3)\n\t" \
    "v_mfma_f32_16x16x32_bf16 v[88:91], a[208:211], v[56:59], v[88:91]\n\t" \
    "s_waitcnt lgkmcnt(2)\n\t" \
    "v_mfma_f32_16x16x32_bf16 v[92:95], a[208:211], v[60:63], v[92:95]\n\t" \
    "v_mfma_f32_16x16x32_bf16 v[96:99], v[248:251], v[56:59], v[96:99]\n\t" \
    "v_mfma_f32_16x16x32_bf16 v[100:103], v[248:251], v[60:63], v[100:103]\n\t" \
    "s_waitcnt lgkmcnt(1)\n\t" \
    "v_mfma_f32_16x16x32_bf16 v[88:91], a[212:215], v[64:67], v[88:91]\n\t" \
    "s_waitcnt lgkmcnt(0)\n\t" \
    "v_mfma_f32_16x16x32_bf16 v[92:95], a[212:215], v[68:71], v[92:95]\n\t" \
    "v_mfma_f32_16x16x32_bf16 v[96:99], v[252:255], v[64:67], v[96:99]\n\t" \
    "v_mfma_f32_16x16x32_bf16 v[100:103], v[252:255], v[68:71], v[100:103]\n\t" \
    "s_mul_i32 s70, s67, 7\n\t" \
    "s_add_u32 s69, s65, 0\n\t" \
    "s_xor_b32 s69, s69, s70\n\t" \
    "s_and_b32 s69, s69, 63\n\t" \
    "s_lshl_b32 s69, s69, 13\n\t" \
    "s_mul_i32 s70, s67, 524288\n\t" \
    "s_add_u32 s69, s69, s70\n\t" \
    "s_add_u32 s69, s69, 6291456\n\t" \
    "s_add_u32 s38, s36, s69\n\t" \
    "s_addc_u32 s39, s37, 0\n\t" \
    "s_add_u32 s40, s38, 16777216\n\t" \
    "s_addc_u32 s41, s39, 0\n\t" \
    "v_cvt_pk_bf16_f32 v112, v88, v89\n\t" \
    "v_cvt_pk_bf16_f32 v113, v90, v91\n\t" \
    "v_cvt_pk_bf16_f32 v114, v96, v97\n\t" \
    "v_cvt_pk_bf16_f32 v115, v98, v99\n\t" \
    "global_store_dwordx4 v8, v[112:115], s[40:41] offset:0 sc1\n\t" \
    "v_cvt_pk_bf16_f32 v116, v92, v93\n\t" \
    "v_cvt_pk_bf16_f32 v117, v94, v95\n\t" \
    "v_cvt_pk_bf16_f32 v118, v100, v101\n\t" \
    "v_cvt_pk_bf16_f32 v119, v102, v103\n\t" \
    "global_store_dwordx4 v8, v[116:119], s[40:41] offset:1024 sc1\n\t" \
    "s_waitcnt vmcnt(16) lgkmcnt(0)\n\t" \
    "s_barrier\n\t" \
    "s_add_u32 m0, s82, 32768\n\t" \
    "s_nop 0\n\t" \
    "global_load_lds_dwordx4 v200, s[44:45]\n\t" \
    "v_add_u32 v200, 131072, v200\n\t" \
    "s_add_u32 m0, s82, 36864\n\t" \
    "s_nop 0\n\t" \
    "global_load_lds_dwordx4 v200, s[44:45]\n\t" \
    "v_add_u32 v200, 131072, v200\n\t" \
    "s_add_u32 m0, s82, 40960\n\t" \
    "s_nop 0\n\t" \
    "global_load_lds_dwordx4 v200, s[44:45]\n\t" \
    "v_add_u32 v200, 131072, v200\n\t" \
    "s_add_u32 m0, s82, 45056\n\t" \
    "s_nop 0\n\t" \
    "global_load_lds_dwordx4 v200, s[44:45]\n\t" \
    "v_add_u32 v200, 131072, v200\n\t" \
    "ds_read_b64_tr_b16 a[32:33], v202 offset:49152\n\t" \
    "ds_read_b64_tr_b16 a[34:35], v202 offset:50176\n\t" \
    "ds_read_b64_tr_b16 a[36:37], v202 offset:57344\n\t" \
    "ds_read_b64_tr_b16 a[38:39], v202 offset:58368\n\t" \
    "ds_read_b64_tr_b16 a[104:105], v203 offset:49152\n\t" \
    "ds_read_b64_tr_b16 a[106:107], v203 offset:50176\n\t" \
    "ds_read_b64_tr_b16 a[108:109], v203 offset:57344\n\t" \
    "ds_read_b64_tr_b16 a[110:111], v203 offset:58368\n\t" \
    "s_waitcnt vmcnt(16) lgkmcnt(0)\n\t" \
    "s_barrier\n\t" \
    "v_add_u32 v202, 65536, v202\n\t" \
    "v_add_u32 v203, 65536, v203\n\t" \
    "ds_read_b64_tr_b16 a[40:41], v202 offset:0\n\t" \
    "ds_read_b64_tr_b16 a[42:43], v202 offset:1024\n\t" \
    "ds_read_b64_tr_b16 a[44:45], v202 offset:8192\n\t" \
    "ds_read_b64_tr_b16 a[46:47], v202 offset:9216\n\t" \
    "ds_read_b64_tr_b16 a[112:113], v203 offset:0\n\t" \
    "ds_read_b64_tr_b16 a[114:115], v203 offset:1024\n\t" \
    "ds_read_b64_tr_b16 a[116:117], v203 offset:8192\n\t" \
    "ds_read_b64_tr_b16 a[118:119], v203 offset:9216\n\t" \
    "s_waitcnt vmcnt(10) lgkmcnt(0)\n\t" \
    "s_barrier\n\t" \
    "v_add_u32 v202, -65536, v202\n\t" \
    "v_add_u32 v203, -65536, v203\n\t" \
    "ds_read_b64_tr_b16 a[48:49], v202 offset:0\n\t" \
    "ds_read_b64_tr_b16 a[50:51], v202 offset:1024\n\t" \
    "ds_read_b64_tr_b16 a[52:53], v202 offset:8192\n\t" \
    "ds_read_b64_tr_b16 a[54:55], v202 offset:9216\n\t" \
    "ds_read_b64_tr_b16 a[120:121], v203 offset:0\n\t" \
    "ds_read_b64_tr_b16 a[122:123], v203 offset:1024\n\t" \
    "ds_read_b64_tr_b16 a[124:125], v203 offset:8192\n\t" \
    "ds_read_b64_tr_b16 a[126:127], v203 offset:9216\n\t" \
    "s_waitcnt vmcnt(6) lgkmcnt(0)\n\t" \
    "s_barrier\n\t" \
    "ds_read_b64_tr_b16 a[56:57], v202 offset:16384\n\t" \
    "ds_read_b64_tr_b16 a[58:59], v202 offset:17408\n\t" \
    "ds_read_b64_tr_b16 a[60:61], v202 offset:24576\n\t" \
    "ds_read_b64_tr_b16 a[62:63], v202 offset:25600\n\t" \
    "ds_read_b64_tr_b16 a[128:129], v203 offset:16384\n\t" \
    "ds_read_b64_tr_b16 a[130:131], v203 offset:17408\n\t" \
    "ds_read_b64_tr_b16 a[132:133], v203 offset:24576\n\t" \
    "ds_read_b64_tr_b16 a[134:135], v203 offset:25600\n\t" \
    "s_waitcnt vmcnt(0) lgkmcnt(0)\n\t" \
    "s_barrier\n\t" \
    "v_sub_u32 v166, v166, v165\n\t" \
    "v_cmp_ne_u32 vcc, 1, v166\n\t" \
    "s_cbranch_vccz gx_Cok2_%=\n\t" \
    "gx_C1_%=:\n\t" \
    "global_load_dwordx2 v[166:167], v164, s[36:37] sc1\n\t" \
    "s_waitcnt vmcnt(0)\n\t" \
    "v_sub_u32 v166, v166, v165\n\t" \
    "v_cmp_ne_u32 vcc, 1, v166\n\t" \
    "s_cbranch_vccz gx_Cok2_%=\n\t" \
    "s_cmp_eq_u32 s68, 0\n\t" \
    "s_cbranch_scc1 gx_F3_%=\n\t" \
    "s_sub_u32 s68, s68, 1\n\t" \
    "s_sleep 4\n\t" \
    "s_branch gx_C1_%=\n\t" \
    "gx_F3_%=:\n\t" \
    "v_mov_b32 v168, 6095104\n\t" \
    "v_mov_b32 v169, 4\n\t" \
    "global_store_dword v168, v169, s[36:37] sc1\n\t" \
    "s_waitcnt vmcnt(0)\n\t" \
    "s_branch gx_Cok2_%=\n\t" \
    "gx_Cok2_%=:\n\t" \
    "s_cmp_lg_u32 s66, 0\n\t" \
    "s_cbranch_scc1 gx_Ca4_%=\n\t" \
    "v_mov_b32 v164, 6176768\n\t" \
    "s_mov_b64 exec, 1\n\t" \
    "global_atomic_add v164, v166, s[36:37] sc1\n\t" \
    "s_mov_b64 exec, -1\n\t" \
    "gx_Ca4_%=:\n\t" \
    "v_min_u32_dpp v167, v167, v167 row_shl:6 row_mask:0xf bank_mask:0xf\n\t" \
    "s_nop 0\n\t" \
    "v_readlane_b32 s69, v167, 4\n\t" \
    "v_readlane_b32 s70, v167, 5\n\t" \
    "s_nop 1\n\t" \
    "s_cmp_lt_u32 s70, s69\n\t" \
    "s_cselect_b32 s82, 8192, 0\n\t" \
    "s_xor_b32 s82, s82, 8192\n\t" \
    "s_cmp_lg_u32 s66, 0\n\t" \
    "s_cbranch_scc1 gx_SF5_%=\n\t" \
    "s_lshl_b32 s69, s67, 7\n\t" \
    "s_add_u32 s69, s69, 6160384\n\t" \
    "s_add_u32 s69, s69, s82\n\t" \
    "s_add_u32 s70, s65, 1\n\t" \
    "v_mov_b32 v156, s69\n\t" \
    "v_mov_b32 v157, s70\n\t" \
    "s_mov_b64 exec, 1\n\t" \
    "global_store_dword v156, v157, s[36:37] sc1\n\t" \
    "s_mov_b64 exec, -1\n\t" \
    "gx_SF5_%=:\n\t" \
    "ds_read_b64_tr_b16 a[64:65], v202 offset:32768\n\t" \
    "ds_read_b64_tr_b16 a[66:67], v202 offset:33792\n\t" \
    "ds_read_b64_tr_b16 a[68:69], v202 offset:40960\n\t" \
    "ds_read_b64_tr_b16 a[70:71], v202 offset:41984\n\t" \
    "ds_read_b64_tr_b16 a[136:137], v203 offset:32768\n\t" \
    "ds_read_b64_tr_b16 a[138:139], v203 offset:33792\n\t" \
    "ds_read_b64_tr_b16 a[140:141], v203 offset:40960\n\t" \
    "ds_read_b64_tr_b16 a[142:143], v203 offset:41984\n\t" \
    "s_waitcnt lgkmcnt(0)\n\t" \
    "s_barrier\n\t" \
    "ds_read_b128 v[24:27], v2 offset:0\n\t" \
    "ds_read_b128 v[28:31], v2 offset:18432\n\t" \
    "ds_read_b128 v[32:35], v2 offset:1024\n\t" \
    "ds_read_b128 v[36:39], v2 offset:19456\n\t" \
    "ds_read_b128 v[40:43], v2 offset:2048\n\t" \
    "ds_read_b128 v[44:47], v2 offset:20480\n\t" \
    "s_cmp_lt_u32 s65, 62\n\t" \
    "s_cselect_b32 s69, 36864, 0\n\t" \
    "s_add_u32 s50, s50, s69\n\t" \
    "s_addc_u32 s51, s51, 0\n\t" \
    "s_add_u32 s90, s90, 36864\n\t" \
    "s_cmp_eq_u32 s90, 147456\n\t" \
    "s_cselect_b32 s90, 0, s90\n\t" \
    "s_add_u32 s91, s90, 36864\n\t" \
    "s_cmp_eq_u32 s91, 147456\n\t" \
    "s_cselect_b32 s91, 0, s91\n\t" \
    "s_add_u32 s91, s91, s89\n\t" \
    "v_add_u32 v17, s90, v3\n\t" \
    "s_branch gx_mid8_%=\n\t" \
    ".p2align 6\n\t" \
    "s_nop 0\n\t" \
    "s_nop 0\n\t" \
    "gx_loop6_%=:\n\t" \
    "s_cmp_lt_u32 s65, 62\n\t" \
    "s_cselect_b32 s69, 36864, 0\n\t" \
    "s_add_u32 s50, s50, s69\n\t" \
    "s_addc_u32 s51, s51, 0\n\t" \
    "s_add_u32 s90, s90, 36864\n\t" \
    "s_cmp_eq_u32 s90, 147456\n\t" \
    "s_cselect_b32 s90, 0, s90\n\t" \
    "s_add_u32 s91, s90, 36864\n\t" \
    "s_cmp_eq_u32 s91, 147456\n\t" \
    "s_cselect_b32 s91, 0, s91\n\t" \
    "s_add_u32 s91, s91, s89\n\t" \
    "v_add_u32 v17, s90, v3\n\t" \
    "s_waitcnt lgkmcnt(5)\n\t" \
    "v_mfma_f32_16x16x32_bf16 v[88:91], a[144:147], v[24:27], 0\n\t" \
    "ds_read_b128 v[48:51], v2 offset:3072\n\t" \
    "s_waitcnt lgkmcnt(5)\n\t" \
    "v_mfma_f32_16x16x32_bf16 v[92:95], a[144:147], v[28:31], 0\n\t" \
    "ds_read_b128 v[52:55], v2 offset:21504\n\t" \
    "v_mfma_f32_16x16x32_bf16 v[96:99], a[216:219], v[24:27], 0\n\t" \
    "v_mfma_f32_16x16x32_bf16 v[100:103], a[216:219], v[28:31], 0\n\t" \
    "s_waitcnt lgkmcnt(5)\n\t" \
    "v_mfma_f32_16x16x32_bf16 v[88:91], a[148:151], v[32:35], v[88:91]\n\t" \
    "ds_read_b128 v[56:59], v2 offset:4096\n\t" \
    "s_waitcnt lgkmcnt(5)\n\t" \
    "v_mfma_f32_16x16x32_bf16 v[92:95], a[148:151], v[36:39], v[92:95]\n\t" \
    "ds_read_b128 v[60:63], v2 offset:22528\n\t" \
    "v_mfma_f32_16x16x32_bf16 v[96:99], a[220:223], v[32:35], v[96:99]\n\t" \
    "v_mfma_f32_16x16x32_bf16 v[100:103], a[220:223], v[36:39], v[100:103]\n\t" \
    "s_waitcnt lgkmcnt(5)\n\t" \
    "v_mfma_f32_16x16x32_bf16 v[88:91], a[152:155], v[40:43], v[88:91]\n\t" \
    "ds_read_b128 v[64:67], v2 offset:5120\n\t" \
    "s_waitcnt lgkmcnt(5)\n\t" \
    "v_mfma_f32_16x16x32_bf16 v[92:95], a[152:155], v[44:47], v[92:95]\n\t" \
    "ds_read_b128 v[68:71], v2 offset:23552\n\t" \
    "v_mfma_f32_16x16x32_bf16 v[96:99], a[224:227], v[40:43], v[96:99]\n\t" \
    "v_mfma_f32_16x16x32_bf16 v[100:103], a[224:227], v[44:47], v[100:103]\n\t" \
    "s_waitcnt vmcnt(0)\n\t" \
    "s_barrier\n\t" \
    "s_cmp_lg_u32 s66, 0\n\t" \
    "s_cbranch_scc1 gx_SF9_%=\n\t" \
    "s_lshl_b32 s69, s67, 7\n\t" \
    "s_add_u32 s69, s69, 6098944\n\t" \
    "s_add_u32 s69, s69, s82\n\t" \
    "s_add_u32 s70, s65, -1\n\t" \
    "v_mov_b32 v156, s69\n\t" \
    "v_mov_b32 v157, s70\n\t" \
    "s_add_u32 s69, s69, 61440\n\t" \
    "s_max_i32 s70, s70, 1\n\t" \
    "v_writelane_b32 v156, s69, 1\n\t" \
    "v_writelane_b32 v157, s70, 1\n\t" \
    "s_mov_b64 exec, 3\n\t" \
    "global_store_dword v156, v157, s[36:37] sc1\n\t" \
    "s_mov_b64 exec, -1\n\t" \
    "gx_SF9_%=:\n\t" \
    "s_waitcnt lgkmcnt(5)\n\t" \
    "v_mfma_f32_16x16x32_bf16 v[88:91], a[156:159], v[48:51], v[88:91]\n\t" \
    "ds_read_b128 v[24:27], v2 offset:6144\n\t" \
    "s_waitcnt lgkmcnt(5)\n\t" \
    "v_mfma_f32_16x16x32_bf16 v[92:95], a[156:159], v[52:55], v[92:95]\n\t" \
    "ds_read_b128 v[28:31], v2 offset:24576\n\t" \
    "v_mfma_f32_16x16x32_bf16 v[96:99], a[228:231], v[48:51], v[96:99]\n\t" \
    "v_mfma_f32_16x16x32_bf16 v[100:103], a[228:231], v[52:55], v[100:103]\n\t" \
    "s_waitcnt lgkmcnt(5)\n\t" \
    "v_mfma_f32_16x16x32_bf16 v[88:91], a[160:163], v[56:59], v[88:91]\n\t" \
    "ds_read_b128 v[32:35], v2 offset:7168\n\t" \
    "s_waitcnt lgkmcnt(5)\n\t" \
    "v_mfma_f32_16x16x32_bf16 v[92:95], a[160:163], v[60:63], v[92:95]\n\t" \
    "ds_read_b128 v[36:39], v2 offset:25600\n\t" \
    "v_mfma_f32_16x16x32_bf16 v[96:99], a[232:235], v[56:59], v[96:99]\n\t" \
    "v_cvt_pk_bf16_f32 v104, v72, v73\n\t" \
    "v_mfma_f32_16x16x32_bf16 v[100:103], a[232:235], v[60:63], v[100:103]\n\t" \
    "v_cvt_pk_bf16_f32 v105, v74, v75\n\t" \
    "s_waitcnt lgkmcnt(5)\n\t" \
    "v_mfma_f32_16x16x32_bf16 v[88:91], a[164:167], v[64:67], v[88:91]\n\t" \
    "ds_read_b128 v[40:43], v2 offset:8192\n\t" \
    "s_waitcnt lgkmcnt(5)\n\t" \
    "v_mfma_f32_16x16x32_bf16 v[92:95], a[164:167], v[68:71], v[92:95]\n\t" \
    "ds_read_b128 v[44:47], v2 offset:26624\n\t" \
    "v_mfma_f32_16x16x32_bf16 v[96:99], a[236:239], v[64:67], v[96:99]\n\t" \
    "v_cvt_pk_bf16_f32 v106, v80, v81\n\t" \
    "v_mfma_f32_16x16x32_bf16 v[100:103], a[236:239], v[68:71], v[100:103]\n\t" \
    "v_cvt_pk_bf16_f32 v107, v82, v83\n\t" \
    "global_store_dwordx4 v8, v[104:107], s[38:39] offset:0 sc1\n\t" \
    "s_waitcnt lgkmcnt(5)\n\t" \
    "v_mfma_f32_16x16x32_bf16 v[88:91], a[168:171], v[24:27], v[88:91]\n\t" \
    "ds_read_b128 v[48:51], v2 offset:9216\n\t" \
    "s_waitcnt lgkmcnt(5)\n\t" \
    "v_mfma_f32_16x16x32_bf16 v[92:95], a[168:171], v[28:31], v[92:95]\n\t" \
    "ds_read_b128 v[52:55], v2 offset:27648\n\t" \
    "v_mfma_f32_16x16x32_bf16 v[96:99], a[240:243], v[24:27], v[96:99]\n\t" \
    "v_cvt_pk_bf16_f32 v108, v76, v77\n\t" \
    "v_mfma_f32_16x16x32_bf16 v[100:103], a[240:243], v[28:31], v[100:103]\n\t" \
    "v_cvt_pk_bf16_f32 v109, v78, v79\n\t" \
    "s_waitcnt lgkmcnt(5)\n\t" \
    "v_mfma_f32_16x16x32_bf16 v[88:91], a[172:175], v[32:35], v[88:91]\n\t" \
    "ds_read_b128 v[56:59], v2 offset:10240\n\t" \
    "s_waitcnt lgkmcnt(5)\n\t" \
    "v_mfma_f32_16x16x32_bf16 v[92:95], a[172:175], v[36:39], v[92:95]\n\t" \
    "ds_read_b128 v[60:63], v2 offset:28672\n\t" \
    "v_mfma_f32_16x16x32_bf16 v[96:99], a[244:247], v[32:35], v[96:99]\n\t" \
    "v_cvt_pk_bf16_f32 v110, v84, v85\n\t" \
    "v_mfma_f32_16x16x32_bf16 v[100:103], a[244:247], v[36:39], v[100:103]\n\t" \
    "v_cvt_pk_bf16_f32 v111, v86, v87\n\t" \
    "global_store_dwordx4 v8, v[108:111], s[38:39] offset:1024 sc1\n\t" \
    "s_waitcnt lgkmcnt(5)\n\t" \
    "v_mfma_f32_16x16x32_bf16 v[88:91], a[176:179], v[40:43], v[88:91]\n\t" \
    "ds_read_b128 v[64:67], v2 offset:11264\n\t" \
    "s_waitcnt lgkmcnt(5)\n\t" \
    "v_mfma_f32_16x16x32_bf16 v[92:95], a[176:179], v[44:47], v[92:95]\n\t" \
    "ds_read_b128 v[68:71], v2 offset:29696\n\t" \
    "v_mfma_f32_16x16x32_bf16 v[96:99], a[248:251], v[40:43], v[96:99]\n\t" \
    "v_mfma_f32_16x16x32_bf16 v[100:103], a[248:251], v[44:47], v[100:103]\n\t" \
    "s_waitcnt lgkmcnt(5)\n\t" \
    "v_mfma_f32_16x16x32_bf16 v[88:91], a[180:183], v[48:51], v[88:91]\n\t" \
    "ds_read_b128 v[24:27], v2 offset:12288\n\t" \
    "s_waitcnt lgkmcnt(5)\n\t" \
    "v_mfma_f32_16x16x32_bf16 v[92:95], a[180:183], v[52:55], v[92:95]\n\t" \
    "ds_read_b128 v[28:31], v2 offset:30720\n\t" \
    "v_mfma_f32_16x16x32_bf16 v[96:99], a[252:255], v[48:51], v[96:99]\n\t" \
    "s_add_u32 m0, s91, 0\n\t" \
    "v_mfma_f32_16x16x32_bf16 v[100:103], a[252:255], v[52:55], v[100:103]\n\t" \
    "global_load_lds_dwordx4 v1, s[50:51] offset:0\n\t" \
    "s_waitcnt lgkmcnt(5)\n\t" \
    "v_mfma_f32_16x16x32_bf16 v[88:91], a[184:187], v[56:59], v[88:91]\n\t" \
    "ds_read_b128 v[32:35], v2 offset:13312\n\t" \
    "s_waitcnt lgkmcnt(5)\n\t" \
    "v_mfma_f32_16x16x32_bf16 v[92:95], a[184:187], v[60:63], v[92:95]\n\t" \
    "ds_read_b128 v[36:39], v2 offset:31744\n\t" \
    "v_mfma_f32_16x16x32_bf16 v[96:99], v[224:227], v[56:59], v[96:99]\n\t" \
    "s_add_u32 m0, s91, 960\n\t" \
    "v_mfma_f32_16x16x32_bf16 v[100:103], v[224:227], v[60:63], v[100:103]\n\t" \
    "global_load_lds_dwordx4 v1, s[50:51] offset:64\n\t" \
    "s_waitcnt lgkmcnt(5)\n\t" \
    "v_mfma_f32_16x16x32_bf16 v[88:91], a[188:191], v[64:67], v[88:91]\n\t" \
    "ds_read_b128 v[40:43], v2 offset:14336\n\t" \
    "s_waitcnt lgkmcnt(5)\n\t" \
    "v_mfma_f32_16x16x32_bf16 v[92:95], a[188:191], v[68:71], v[92:95]\n\t" \
    "ds_read_b128 v[44:47], v2 offset:32768\n\t" \
    "v_mfma_f32_16x16x32_bf16 v[96:99], v[228:231], v[64:67], v[96:99]\n\t" \
    "s_add_u32 m0, s91, 1920\n\t" \
    "v_mfma_f32_16x16x32_bf16 v[100:103], v[228:231], v[68:71], v[100:103]\n\t" \
    "global_load_lds_dwordx4 v1, s[50:51] offset:128\n\t" \
    "s_waitcnt lgkmcnt(5)\n\t" \
    "v_mfma_f32_16x16x32_bf16 v[88:91], a[192:195], v[24:27], v[88:91]\n\t" \
    "ds_read_b128 v[48:51], v2 offset:15360\n\t" \
    "s_waitcnt lgkmcnt(5)\n\t" \
    "v_mfma_f32_16x16x32_bf16 v[92:95], a[192:195], v[28:31], v[92:95]\n\t" \
    "ds_read_b128 v[52:55], v2 offset:33792\n\t" \
    "v_mfma_f32_16x16x32_bf16 v[96:99], v[232:235], v[24:27], v[96:99]\n\t" \
    "s_add_u32 m0, s91, 2880\n\t" \
    "v_mfma_f32_16x16x32_bf16 v[100:103], v[232:235], v[28:31], v[100:103]\n\t" \
    "global_load_lds_dwordx4 v1, s[50:51] offset:192\n\t" \
    "s_waitcnt lgkmcnt(5)\n\t" \
    "v_mfma_f32_16x16x32_bf16 v[88:91], a[196:199], v[32:35], v[88:91]\n\t" \
    "ds_read_b128 v[56:59], v2 offset:16384\n\t" \
    "s_waitcnt lgkmcnt(5)\n\t" \
    "v_mfma_f32_16x16x32_bf16 v[92:95], a[196:199], v[36:39], v[92:95]\n\t" \
    "ds_read_b128 v[60:63], v2 offset:34816\n\t" \
    "v_mfma_f32_16x16x32_bf16 v[96:99], v[236:239], v[32:35], v[96:99]\n\t" \
    "s_add_u32 m0, s91, 3840\n\t" \
    "v_mfma_f32_16x16x32_bf16 v[100:103], v[236:239], v[36:39], v[100:103]\n\t" \
    "global_load_lds_dwordx4 v1, s[50:51] offset:256\n\t" \
    "s_waitcnt lgkmcnt(5)\n\t" \
    "v_mfma_f32_16x16x32_bf16 v[88:91], a[200:203], v[40:43], v[88:91]\n\t" \
    "ds_read_b128 v[64:67], v2 offset:17408\n\t" \
    "s_waitcnt lgkmcnt(5)\n\t" \
    "v_mfma_f32_16x16x32_bf16 v[92:95], a[200:203], v[44:47], v[92:95]\n\t" \
    "ds_read_b128 v[68:71], v2 offset:35840\n\t" \
    "v_mfma_f32_16x16x32_bf16 v[96:99], v[240:243], v[40:43], v[96:99]\n\t" \
    "s_add_u32 m0, s91, 4800\n\t" \
    "v_mfma_f32_16x16x32_bf16 v[100:103], v[240:243], v[44:47], v[100:103]\n\t" \
    "global_load_lds_dwordx4 v1, s[50:51] offset:320\n\t" \
    "s_waitcnt lgkmcnt(5)\n\t" \
    "v_mfma_f32_16x16x32_bf16 v[88:91], a[204:207], v[48:51], v[88:91]\n\t" \
    "ds_read_b128 v[24:27], v2 offset:0\n\t" \
    "s_waitcnt lgkmcnt(5)\n\t" \
    "v_mfma_f32_16x16x32_bf16 v[92:95], a[204:207], v[52:55], v[92:95]\n\t" \
    "ds_read_b128 v[28:31], v2 offset:18432\n\t" \
    "v_mfma_f32_16x16x32_bf16 v[96:99], v[244:247], v[48:51], v[96:99]\n\t" \
    "s_add_u32 m0, s91, 5760\n\t" \
    "v_mfma_f32_16x16x32_bf16 v[100:103], v[244:247], v[52:55], v[100:103]\n\t" \
    "global_load_lds_dwordx4 v1, s[50:51] offset:384\n\t" \
    "s_waitcnt lgkmcnt(5)\n\t" \
    "v_mfma_f32_16x16x32_bf16 v[88:91], a[208:211], v[56:59], v[88:91]\n\t" \
    "ds_read_b128 v[32:35], v2 offset:1024\n\t" \
    "s_waitcnt lgkmcnt(5)\n\t" \
    "v_mfma_f32_16x16x32_bf16 v[92:95], a[208:211], v[60:63], v[92:95]\n\t" \
    "ds_read_b128 v[36:39], v2 offset:19456\n\t" \
    "v_mfma_f32_16x16x32_bf16 v[96:99], v[248:251], v[56:59], v[96:99]\n\t" \
    "s_add_u32 m0, s91, 6720\n\t" \
    "v_mfma_f32_16x16x32_bf16 v[100:103], v[248:251], v[60:63], v[100:103]\n\t" \
    "global_load_lds_dwordx4 v1, s[50:51] offset:448\n\t" \
    "s_waitcnt lgkmcnt(5)\n\t" \
    "v_mfma_f32_16x16x32_bf16 v[88:91], a[212:215], v[64:67], v[88:91]\n\t" \
    "ds_read_b128 v[40:43], v2 offset:2048\n\t" \
    "s_waitcnt lgkmcnt(5)\n\t" \
    "v_mfma_f32_16x16x32_bf16 v[92:95], a[212:215], v[68:71], v[92:95]\n\t" \
    "ds_read_b128 v[44:47], v2 offset:20480\n\t" \
    "v_mfma_f32_16x16x32_bf16 v[96:99], v[252:255], v[64:67], v[96:99]\n\t" \
    "s_add_u32 m0, s91, 7680\n\t" \
    "v_mfma_f32_16x16x32_bf16 v[100:103], v[252:255], v[68:71], v[100:103]\n\t" \
    "global_load_lds_dwordx4 v1, s[50:51] offset:512\n\t" \
    "gx_mid8_%=:\n\t" \
    "s_waitcnt lgkmcnt(5)\n\t" \
    "v_mfma_f32_16x16x32_bf16 v[72:75], a[0:3], v[24:27], 0\n\t" \
    "ds_read_b128 v[48:51], v2 offset:3072\n\t" \
    "s_waitcnt lgkmcnt(5)\n\t" \
    "v_mfma_f32_16x16x32_bf16 v[76:79], a[0:3], v[28:31], 0\n\t" \
    "ds_read_b128 v[52:55], v2 offset:21504\n\t" \
    "v_mfma_f32_16x16x32_bf16 v[80:83], a[72:75], v[24:27], 0\n\t" \
    "s_mul_i32 s70, s67, 7\n\t" \
    "s_add_u32 s69, s65, 0\n\t" \
    "s_xor_b32 s69, s69, s70\n\t" \
    "s_and_b32 s69, s69, 63\n\t" \
    "s_lshl_b32 s69, s69, 13\n\t" \
    "s_mul_i32 s70, s67, 524288\n\t" \
    "s_add_u32 s69, s69, s70\n\t" \
    "s_add_u32 s69, s69, 6291456\n\t" \
    "s_add_u32 s38, s36, s69\n\t" \
    "s_addc_u32 s39, s37, 0\n\t" \
    "v_mfma_f32_16x16x32_bf16 v[84:87], a[72:75], v[28:31], 0\n\t" \
    "s_add_u32 s40, s38, 16777216\n\t" \
    "s_addc_u32 s41, s39, 0\n\t" \
    "s_waitcnt lgkmcnt(5)\n\t" \
    "v_mfma_f32_16x16x32_bf16 v[72:75], a[4:7], v[32:35], v[72:75]\n\t" \
    "ds_read_b128 v[56:59], v2 offset:4096\n\t" \
    "s_waitcnt lgkmcnt(5)\n\t" \
    "v_mfma_f32_16x16x32_bf16 v[76:79], a[4:7], v[36:39], v[76:79]\n\t" \
    "ds_read_b128 v[60:63], v2 offset:22528\n\t" \
    "v_mfma_f32_16x16x32_bf16 v[80:83], a[76:79], v[32:35], v[80:83]\n\t" \
    "v_mfma_f32_16x16x32_bf16 v[84:87], a[76:79], v[36:39], v[84:87]\n\t" \
    "s_waitcnt lgkmcnt(5)\n\t" \
    "v_mfma_f32_16x16x32_bf16 v[72:75], a[8:11], v[40:43], v[72:75]\n\t" \
    "ds_read_b128 v[64:67], v2 offset:5120\n\t" \
    "s_waitcnt lgkmcnt(5)\n\t" \
    "v_mfma_f32_16x16x32_bf16 v[76:79], a[8:11], v[44:47], v[76:79]\n\t" \
    "ds_read_b128 v[68:71], v2 offset:23552\n\t" \
    "v_mfma_f32_16x16x32_bf16 v[80:83], a[80:83], v[40:43], v[80:83]\n\t" \
    "v_cvt_pk_bf16_f32 v112, v88, v89\n\t" \
    "v_mfma_f32_16x16x32_bf16 v[84:87], a[80:83], v[44:47], v[84:87]\n\t" \
    "v_cvt_pk_bf16_f32 v113, v90, v91\n\t" \
    "s_waitcnt lgkmcnt(5)\n\t" \
    "v_mfma_f32_16x16x32_bf16 v[72:75], a[12:15], v[48:51], v[72:75]\n\t" \
    "ds_read_b128 v[24:27], v2 offset:6144\n\t" \
    "s_waitcnt lgkmcnt(5)\n\t" \
    "v_mfma_f32_16x16x32_bf16 v[76:79], a[12:15], v[52:55], v[76:79]\n\t" \
    "ds_read_b128 v[28:31], v2 offset:24576\n\t" \
    "v_mfma_f32_16x16x32_bf16 v[80:83], a[84:87], v[48:51], v[80:83]\n\t" \
    "v_cvt_pk_bf16_f32 v114, v96, v97\n\t" \
    "v_mfma_f32_16x16x32_bf16 v[84:87], a[84:87], v[52:55], v[84:87]\n\t" \
    "v_cvt_pk_bf16_f32 v115, v98, v99\n\t" \
    "global_store_dwordx4 v8, v[112:115], s[40:41] offset:0 sc1\n\t" \
    "s_waitcnt lgkmcnt(5)\n\t" \
    "v_mfma_f32_16x16x32_bf16 v[72:75], a[16:19], v[56:59], v[72:75]\n\t" \
    "ds_read_b128 v[32:35], v2 offset:7168\n\t" \
    "s_waitcnt lgkmcnt(5)\n\t" \
    "v_mfma_f32_16x16x32_bf16 v[76:79], a[16:19], v[60:63], v[76:79]\n\t" \
    "ds_read_b128 v[36:39], v2 offset:25600\n\t" \
    "v_mfma_f32_16x16x32_bf16 v[80:83], a[88:91], v[56:59], v[80:83]\n\t" \
    "v_cvt_pk_bf16_f32 v116, v92, v93\n\t" \
    "v_mfma_f32_16x16x32_bf16 v[84:87], a[88:91], v[60:63], v[84:87]\n\t" \
    "v_cvt_pk_bf16_f32 v117, v94, v95\n\t" \
    "s_waitcnt lgkmcnt(5)\n\t" \
    "v_mfma_f32_16x16x32_bf16 v[72:75], a[20:23], v[64:67], v[72:75]\n\t" \
    "ds_read_b128 v[40:43], v2 offset:8192\n\t" \
    "s_waitcnt lgkmcnt(5)\n\t" \
    "v_mfma_f32_16x16x32_bf16 v[76:79], a[20:23], v[68:71], v[76:79]\n\t" \
    "ds_read_b128 v[44:47], v2 offset:26624\n\t" \
    "v_mfma_f32_16x16x32_bf16 v[80:83], a[92:95], v[64:67], v[80:83]\n\t" \
    "v_cvt_pk_bf16_f32 v118, v100, v101\n\t" \
    "v_mfma_f32_16x16x32_bf16 v[84:87], a[92:95], v[68:71], v[84:87]\n\t" \
    "v_cvt_pk_bf16_f32 v119, v102, v103\n\t" \
    "global_store_dwordx4 v8, v[116:119], s[40:41] offset:1024 sc1\n\t" \
    "s_waitcnt lgkmcnt(5)\n\t" \
    "v_mfma_f32_16x16x32_bf16 v[72:75], a[24:27], v[24:27], v[72:75]\n\t" \
    "ds_read_b128 v[48:51], v2 offset:9216\n\t" \
    "s_waitcnt lgkmcnt(5)\n\t" \
    "v_mfma_f32_16x16x32_bf16 v[76:79], a[24:27], v[28:31], v[76:79]\n\t" \
    "ds_read_b128 v[52:55], v2 offset:27648\n\t" \
    "v_mfma_f32_16x16x32_bf16 v[80:83], a[96:99], v[24:27], v[80:83]\n\t" \
    "v_mfma_f32_16x16x32_bf16 v[84:87], a[96:99], v[28:31], v[84:87]\n\t" \
    "s_waitcnt lgkmcnt(5)\n\t" \
    "v_mfma_f32_16x16x32_bf16 v[72:75], a[28:31], v[32:35], v[72:75]\n\t" \
    "ds_read_b128 v[56:59], v2 offset:10240\n\t" \
    "s_waitcnt lgkmcnt(5)\n\t" \
    "v_mfma_f32_16x16x32_bf16 v[76:79], a[28:31], v[36:39], v[76:79]\n\t" \
    "ds_read_b128 v[60:63], v2 offset:28672\n\t" \
    "v_mfma_f32_16x16x32_bf16 v[80:83], a[100:103], v[32:35], v[80:83]\n\t" \
    "v_mfma_f32_16x16x32_bf16 v[84:87], a[100:103], v[36:39], v[84:87]\n\t" \
    "s_waitcnt lgkmcnt(5)\n\t" \
    "v_mfma_f32_16x16x32_bf16 v[72:75], a[32:35], v[40:43], v[72:75]\n\t" \
    "ds_read_b128 v[64:67], v2 offset:11264\n\t" \
    "s_waitcnt lgkmcnt(5)\n\t" \
    "v_mfma_f32_16x16x32_bf16 v[76:79], a[32:35], v[44:47], v[76:79]\n\t" \
    "ds_read_b128 v[68:71], v2 offset:29696\n\t" \
    "v_mfma_f32_16x16x32_bf16 v[80:83], a[104:107], v[40:43], v[80:83]\n\t" \
    "v_mfma_f32_16x16x32_bf16 v[84:87], a[104:107], v[44:47], v[84:87]\n\t" \
    "s_waitcnt lgkmcnt(5)\n\t" \
    "v_mfma_f32_16x16x32_bf16 v[72:75], a[36:39], v[48:51], v[72:75]\n\t" \
    "ds_read_b128 v[24:27], v2 offset:12288\n\t" \
    "s_waitcnt lgkmcnt(5)\n\t" \
    "v_mfma_f32_16x16x32_bf16 v[76:79], a[36:39], v[52:55], v[76:79]\n\t" \
    "ds_read_b128 v[28:31], v2 offset:30720\n\t" \
    "v_mfma_f32_16x16x32_bf16 v[80:83], a[108:111], v[48:51], v[80:83]\n\t" \
    "v_mfma_f32_16x16x32_bf16 v[84:87], a[108:111], v[52:55], v[84:87]\n\t" \
    "s_waitcnt lgkmcnt(5)\n\t" \
    "v_mfma_f32_16x16x32_bf16 v[72:75], a[40:43], v[56:59], v[72:75]\n\t" \
    "ds_read_b128 v[32:35], v2 offset:13312\n\t" \
    "s_waitcnt lgkmcnt(5)\n\t" \
    "v_mfma_f32_16x16x32_bf16 v[76:79], a[40:43], v[60:63], v[76:79]\n\t" \
    "ds_read_b128 v[36:39], v2 offset:31744\n\t" \
    "v_mfma_f32_16x16x32_bf16 v[80:83], a[112:115], v[56:59], v[80:83]\n\t" \
    "v_mfma_f32_16x16x32_bf16 v[84:87], a[112:115], v[60:63], v[84:87]\n\t" \
    "s_waitcnt lgkmcnt(5)\n\t" \
    "v_mfma_f32_16x16x32_bf16 v[72:75], a[44:47], v[64:67], v[72:75]\n\t" \
    "ds_read_b128 v[40:43], v2 offset:14336\n\t" \
    "s_waitcnt lgkmcnt(5)\n\t" \
    "v_mfma_f32_16x16x32_bf16 v[76:79], a[44:47], v[68:71], v[76:79]\n\t" \
    "ds_read_b128 v[44:47], v2 offset:32768\n\t" \
    "v_mfma_f32_16x16x32_bf16 v[80:83], a[116:119], v[64:67], v[80:83]\n\t" \
    "v_mfma_f32_16x16x32_bf16 v[84:87], a[116:119], v[68:71], v[84:87]\n\t" \
    "s_waitcnt lgkmcnt(5)\n\t" \
    "v_mfma_f32_16x16x32_bf16 v[72:75], a[48:51], v[24:27], v[72:75]\n\t" \
    "ds_read_b128 v[48:51], v2 offset:15360\n\t" \
    "s_waitcnt lgkmcnt(5)\n\t" \
    "v_mfma_f32_16x16x32_bf16 v[76:79], a[48:51], v[28:31], v[76:79]\n\t" \
    "ds_read_b128 v[52:55], v2 offset:33792\n\t" \
    "v_mfma_f32_16x16x32_bf16 v[80:83], a[120:123], v[24:27], v[80:83]\n\t" \
    "v_mfma_f32_16x16x32_bf16 v[84:87], a[120:123], v[28:31], v[84:87]\n\t" \
    "s_waitcnt lgkmcnt(5)\n\t" \
    "v_mfma_f32_16x16x32_bf16 v[72:75], a[52:55], v[32:35], v[72:75]\n\t" \
    "ds_read_b128 v[56:59], v2 offset:16384\n\t" \
    "s_waitcnt lgkmcnt(5)\n\t" \
    "v_mfma_f32_16x16x32_bf16 v[76:79], a[52:55], v[36:39], v[76:79]\n\t" \
    "ds_read_b128 v[60:63], v2 offset:34816\n\t" \
    "v_mfma_f32_16x16x32_bf16 v[80:83], a[124:127], v[32:35], v[80:83]\n\t" \
    "v_mfma_f32_16x16x32_bf16 v[84:87], a[124:127], v[36:39], v[84:87]\n\t" \
    "s_cmp_lg_u32 s65, 0\n\t" \
    "s_cbranch_scc1 gx_g110_%=\n\t" \
    "s_add_u32 m0, s91, 0\n\t" \
    "s_nop 0\n\t" \
    "global_load_lds_dwordx4 v1, s[50:51] offset:0\n\t" \
    "s_add_u32 m0, s91, 960\n\t" \
    "s_nop 0\n\t" \
    "global_load_lds_dwordx4 v1, s[50:51] offset:64\n\t" \
    "s_add_u32 m0, s91, 1920\n\t" \
    "s_nop 0\n\t" \
    "global_load_lds_dwordx4 v1, s[50:51] offset:128\n\t" \
    "s_add_u32 m0, s91, 2880\n\t" \
    "s_nop 0\n\t" \
    "global_load_lds_dwordx4 v1, s[50:51] offset:192\n\t" \
    "s_add_u32 m0, s91, 3840\n\t" \
    "s_nop 0\n\t" \
    "global_load_lds_dwordx4 v1, s[50:51] offset:256\n\t" \
    "s_add_u32 m0, s91, 4800\n\t" \
    "s_nop 0\n\t" \
    "global_load_lds_dwordx4 v1, s[50:51] offset:320\n\t" \
    "s_add_u32 m0, s91, 5760\n\t" \
    "s_nop 0\n\t" \
    "global_load_lds_dwordx4 v1, s[50:51] offset:384\n\t" \
    "s_add_u32 m0, s91, 6720\n\t" \
    "s_nop 0\n\t" \
    "global_load_lds_dwordx4 v1, s[50:51] offset:448\n\t" \
    "s_add_u32 m0, s91, 7680\n\t" \
    "s_nop 0\n\t" \
    "global_load_lds_dwordx4 v1, s[50:51] offset:512\n\t" \
    "gx_g110_%=:\n\t" \
    "s_waitcnt lgkmcnt(5)\n\t" \
    "v_mfma_f32_16x16x32_bf16 v[72:75], a[56:59], v[40:43], v[72:75]\n\t" \
    "ds_read_b128 v[64:67], v2 offset:17408\n\t" \
    "s_waitcnt lgkmcnt(5)\n\t" \
    "v_mfma_f32_16x16x32_bf16 v[76:79], a[56:59], v[44:47], v[76:79]\n\t" \
    "ds_read_b128 v[68:71], v2 offset:35840\n\t" \
    "v_mfma_f32_16x16x32_bf16 v[80:83], a[128:131], v[40:43], v[80:83]\n\t" \
    "v_mfma_f32_16x16x32_bf16 v[84:87], a[128:131], v[44:47], v[84:87]\n\t" \
    "s_waitcnt lgkmcnt(5)\n\t" \
    "v_mfma_f32_16x16x32_bf16 v[72:75], a[60:63], v[48:51], v[72:75]\n\t" \
    "ds_read_b128 v[24:27], v17 offset:0\n\t" \
    "s_waitcnt lgkmcnt(5)\n\t" \
    "v_mfma_f32_16x16x32_bf16 v[76:79], a[60:63], v[52:55], v[76:79]\n\t" \
    "ds_read_b128 v[28:31], v17 offset:18432\n\t" \
    "v_mfma_f32_16x16x32_bf16 v[80:83], a[132:135], v[48:51], v[80:83]\n\t" \
    "v_mfma_f32_16x16x32_bf16 v[84:87], a[132:135], v[52:55], v[84:87]\n\t" \
    "s_waitcnt lgkmcnt(5)\n\t" \
    "v_mfma_f32_16x16x32_bf16 v[72:75], a[64:67], v[56:59], v[72:75]\n\t" \
    "ds_read_b128 v[32:35], v17 offset:1024\n\t" \
    "s_waitcnt lgkmcnt(5)\n\t" \
    "v_mfma_f32_16x16x32_bf16 v[76:79], a[64:67], v[60:63], v[76:79]\n\t" \
    "ds_read_b128 v[36:39], v17 offset:19456\n\t" \
    "v_mfma_f32_16x16x32_bf16 v[80:83], a[136:139], v[56:59], v[80:83]\n\t" \
    "v_mfma_f32_16x16x32_bf16 v[84:87], a[136:139], v[60:63], v[84:87]\n\t" \
    "s_waitcnt lgkmcnt(5)\n\t" \
    "v_mfma_f32_16x16x32_bf16 v[72:75], a[68:71], v[64:67], v[72:75]\n\t" \
    "ds_read_b128 v[40:43], v17 offset:2048\n\t" \
    "s_waitcnt lgkmcnt(5)\n\t" \
    "v_mfma_f32_16x16x32_bf16 v[76:79], a[68:71], v[68:71], v[76:79]\n\t" \
    "ds_read_b128 v[44:47], v17 offset:20480\n\t" \
    "v_mfma_f32_16x16x32_bf16 v[80:83], a[140:143], v[64:67], v[80:83]\n\t" \
    "v_mfma_f32_16x16x32_bf16 v[84:87], a[140:143], v[68:71], v[84:87]\n\t" \
    "v_mov_b32 v2, v17\n\t" \
    "s_add_u32 s65, s65, 1\n\t" \
    "s_cmp_lt_u32 s65, 64\n\t" \
    "s_cbranch_scc1 gx_loop6_%=\n\t" \
    "s_nop 7\n\t" \
    "v_cvt_pk_bf16_f32 v104, v72, v73\n\t" \
    "v_cvt_pk_bf16_f32 v105, v74, v75\n\t" \
    "v_cvt_pk_bf16_f32 v106, v80, v81\n\t" \
    "v_cvt_pk_bf16_f32 v107, v82, v83\n\t" \
    "global_store_dwordx4 v8, v[104:107], s[38:39] offset:0 sc1\n\t" \
    "v_cvt_pk_bf16_f32 v108, v76, v77\n\t" \
    "v_cvt_pk_bf16_f32 v109, v78, v79\n\t" \
    "v_cvt_pk_bf16_f32 v110, v84, v85\n\t" \
    "v_cvt_pk_bf16_f32 v111, v86, v87\n\t" \
    "global_store_dwordx4 v8, v[108:111], s[38:39] offset:1024 sc1\n\t" \
    "s_waitcnt vmcnt(0) lgkmcnt(0)\n\t" \
    "s_barrier\n\t" \
    "s_cmp_lg_u32 s66, 0\n\t" \
    "s_cbranch_scc1 gx_SF11_%=\n\t" \
    "s_lshl_b32 s69, s67, 7\n\t" \
    "s_add_u32 s69, s69, 6098944\n\t" \
    "s_add_u32 s69, s69, s82\n\t" \
    "s_add_u32 s70, s65, 0\n\t" \
    "v_mov_b32 v156, s69\n\t" \
    "v_mov_b32 v157, s70\n\t" \
    "s_add_u32 s69, s69, 61440\n\t" \
    "s_max_i32 s70, s70, 1\n\t" \
    "v_writelane_b32 v156, s69, 1\n\t" \
    "v_writelane_b32 v157, s70, 1\n\t" \
    "s_mov_b64 exec, 3\n\t" \
    "global_store_dword v156, v157, s[36:37] sc1\n\t" \
    "s_mov_b64 exec, -1\n\t" \
    "gx_SF11_%=:\n\t" \
    "s_waitcnt vmcnt(0)\n\t" \
    "s_branch ea_done_%=\n\t" \
    "gv_begin_%=:\n\t" \
    "v_mbcnt_lo_u32_b32 v223, -1, 0\n\t" \
    "v_mbcnt_hi_u32_b32 v223, -1, v223\n\t" \
    "s_mov_b32 s68, 262144\n\t" \
    "s_mov_b32 s72, 0\n\t" \
    "s_mov_b32 s83, -1\n\t" \
    "s_mov_b32 s65, 0\n\t" \
    "v_and_b32 v180, 3, v223\n\t" \
    "v_lshlrev_b32 v202, 3, v180\n\t" \
    "v_lshrrev_b32 v181, 5, v223\n\t" \
    "v_lshl_add_u32 v202, v181, 12, v202\n\t" \
    "v_bfe_u32 v182, v223, 2, 2\n\t" \
    "s_mov_b32 s70, 288\n\t" \
    "v_mad_u32_u24 v202, v182, s70, v202\n\t" \
    "v_bfe_u32 v183, v223, 4, 1\n\t" \
    "s_mov_b32 s70, 2176\n\t" \
    "v_mad_u32_u24 v202, v183, s70, v202\n\t" \
    "s_lshl_b32 s69, s66, 6\n\t" \
    "v_xor_b32 v202, s69, v202\n\t" \
    "v_xor_b32 v203, 32, v202\n\t" \
    "s_mul_i32 s69, s66, 9216\n\t" \
    "v_and_b32 v184, 1, v223\n\t" \
    "v_lshl_add_u32 v201, v184, 4, s69\n\t" \
    "v_lshrrev_b32 v185, 3, v223\n\t" \
    "s_mov_b32 s70, 1152\n\t" \
    "v_mad_u32_u24 v201, v185, s70, v201\n\t" \
    "v_bfe_u32 v186, v223, 1, 2\n\t" \
    "v_lshlrev_b32 v204, 5, v186\n\t" \
    "v_lshrrev_b32 v187, 4, v223\n\t" \
    "v_lshlrev_b32 v205, 5, v187\n\t" \
    "v_xor_b32 v204, v204, v205\n\t" \
    "v_add_u32 v201, v201, v204\n\t" \
    "s_mul_i32 s96, s66, 9216\n\t" \
    "v_lshlrev_b32 v207, 3, v180\n\t" \
    "v_bfe_u32 v188, v223, 2, 1\n\t" \
    "v_lshl_add_u32 v207, v188, 7, v207\n\t" \
    "v_bfe_u32 v189, v223, 3, 2\n\t" \
    "s_mov_b32 s70, 288\n\t" \
    "v_mad_u32_u24 v207, v189, s70, v207\n\t" \
    "s_mov_b32 s70, 9216\n\t" \
    "v_mad_u32_u24 v207, v181, s70, v207\n\t" \
    "s_mul_i32 s69, s66, 9\n\t" \
    "s_add_u32 s69, s69, 0\n\t" \
    "s_and_b32 s70, s69, 3\n\t" \
    "s_lshl_b32 s70, s70, 5\n\t" \
    "s_lshr_b32 s69, s69, 2\n\t" \
    "s_lshl_b32 s69, s69, 10\n\t" \
    "v_xor_b32 v204, s70, v207\n\t" \
    "v_add_u32 v204, s69, v204\n\t" \
    "s_mul_i32 s69, s66, 9\n\t" \
    "s_add_u32 s69, s69, 1\n\t" \
    "s_and_b32 s70, s69, 3\n\t" \
    "s_lshl_b32 s70, s70, 5\n\t" \
    "s_lshr_b32 s69, s69, 2\n\t" \
    "s_lshl_b32 s69, s69, 10\n\t" \
    "v_xor_b32 v205, s70, v207\n\t" \
    "v_add_u32 v205, s69, v205\n\t" \
    "s_mul_i32 s69, s66, 9\n\t" \
    "s_add_u32 s69, s69, 2\n\t" \
    "s_and_b32 s70, s69, 3\n\t" \
    "s_lshl_b32 s70, s70, 5\n\t" \
    "s_lshr_b32 s69, s69, 2\n\t" \
    "s_lshl_b32 s69, s69, 10\n\t" \
    "v_xor_b32 v206, s70, v207\n\t" \
    "v_add_u32 v206, s69, v206\n\t" \
    "s_mul_i32 s69, s66, 9\n\t" \
    "s_add_u32 s69, s69, 3\n\t" \
    "s_and_b32 s70, s69, 3\n\t" \
    "s_lshl_b32 s70, s70, 5\n\t" \
    "s_lshr_b32 s69, s69, 2\n\t" \
    "s_lshl_b32 s69, s69, 10\n\t" \
    "v_xor_b32 v207, s70, v207\n\t" \
    "v_add_u32 v207, s69, v207\n\t" \
    "s_waitcnt vmcnt(12) lgkmcnt(0)\n\t" \
    "s_barrier\n\t" \
    "s_add_u32 m0, s82, 65536\n\t" \
    "s_nop 0\n\t" \
    "global_load_lds_dwordx4 v200, s[42:43]\n\t" \
    "v_add_u32 v200, 131072, v200\n\t" \
    "s_add_u32 m0, s82, 69632\n\t" \
    "s_nop 0\n\t" \
    "global_load_lds_dwordx4 v200, s[42:43]\n\t" \
    "v_add_u32 v200, 131072, v200\n\t" \
    "s_add_u32 m0, s82, 73728\n\t" \
    "s_nop 0\n\t" \
    "global_load_lds_dwordx4 v200, s[42:43]\n\t" \
    "v_add_u32 v200, 131072, v200\n\t" \
    "s_add_u32 m0, s82, 77824\n\t" \
    "s_nop 0\n\t" \
    "global_load_lds_dwordx4 v200, s[42:43]\n\t" \
    "v_add_u32 v200, 131072, v200\n\t" \
    "ds_read_b64_tr_b16 a[0:1], v202 offset:36864\n\t" \
    "ds_read_b64_tr_b16 a[2:3], v202 offset:37888\n\t" \
    "ds_read_b64_tr_b16 a[4:5], v202 offset:45056\n\t" \
    "ds_read_b64_tr_b16 a[6:7], v202 offset:46080\n\t" \
    "ds_read_b64_tr_b16 a[72:73], v203 offset:36864\n\t" \
    "ds_read_b64_tr_b16 a[74:75], v203 offset:37888\n\t" \
    "ds_read_b64_tr_b16 a[76:77], v203 offset:45056\n\t" \
    "ds_read_b64_tr_b16 a[78:79], v203 offset:46080\n\t" \
    "v_lshlrev_b32 v3, 4, v180\n\t" \
    "v_lshlrev_b32 v2, 5, v181\n\t" \
    "v_xor_b32 v3, v3, v2\n\t" \
    "s_bfe_u32 s71, s66, 0x10000\n\t" \
    "s_mul_i32 s69, s71, 576\n\t" \
    "s_bfe_u32 s71, s66, 0x10001\n\t" \
    "s_mul_i32 s71, s71, 18432\n\t" \
    "s_add_u32 s69, s69, s71\n\t" \
    "v_lshrrev_b32 v190, 2, v223\n\t" \
    "v_mov_b32 v1, s69\n\t" \
    "s_mov_b32 s70, 1152\n\t" \
    "v_mad_u32_u24 v1, v190, s70, v1\n\t" \
    "v_add_u32 v1, v1, v3\n\t" \
    "v_and_b32 v191, 15, v223\n\t" \
    "v_lshlrev_b32 v3, 6, v191\n\t" \
    "v_lshl_add_u32 v3, v187, 4, v3\n\t" \
    "v_bfe_u32 v192, v223, 3, 1\n\t" \
    "v_lshlrev_b32 v2, 5, v192\n\t" \
    "v_xor_b32 v3, v3, v2\n\t" \
    "v_mov_b32 v2, v3\n\t" \
    "s_lshr_b32 s65, s67, 5\n\t" \
    "s_mul_i32 s69, s65, 36864\n\t" \
    "s_add_u32 s50, s50, s69\n\t" \
    "s_addc_u32 s51, s51, 0\n\t" \
    "s_add_u32 s38, s36, 48234496\n\t" \
    "s_addc_u32 s39, s37, 0\n\t" \
    "s_mul_i32 s89, s66, 9216\n\t" \
    "s_mov_b32 s90, 0\n\t" \
    "s_waitcnt vmcnt(12) lgkmcnt(0)\n\t" \
    "s_barrier\n\t" \
    "s_add_u32 m0, s82, 81920\n\t" \
    "s_nop 0\n\t" \
    "global_load_lds_dwordx4 v200, s[42:43]\n\t" \
    "v_add_u32 v200, 131072, v200\n\t" \
    "s_add_u32 m0, s82, 86016\n\t" \
    "s_nop 0\n\t" \
    "global_load_lds_dwordx4 v200, s[42:43]\n\t" \
    "v_add_u32 v200, 131072, v200\n\t" \
    "s_add_u32 m0, s82, 90112\n\t" \
    "s_nop 0\n\t" \
    "global_load_lds_dwordx4 v200, s[42:43]\n\t" \
    "v_add_u32 v200, 131072, v200\n\t" \
    "s_add_u32 m0, s82, 94208\n\t" \
    "s_nop 0\n\t" \
    "global_load_lds_dwordx4 v200, s[42:43]\n\t" \
    "v_add_u32 v200, 131072, v200\n\t" \
    "ds_read_b64_tr_b16 a[8:9], v202 offset:53248\n\t" \
    "ds_read_b64_tr_b16 a[10:11], v202 offset:54272\n\t" \
    "ds_read_b64_tr_b16 a[12:13], v202 offset:61440\n\t" \
    "ds_read_b64_tr_b16 a[14:15], v202 offset:62464\n\t" \
    "ds_read_b64_tr_b16 a[80:81], v203 offset:53248\n\t" \
    "ds_read_b64_tr_b16 a[82:83], v203 offset:54272\n\t" \
    "ds_read_b64_tr_b16 a[84:85], v203 offset:61440\n\t" \
    "ds_read_b64_tr_b16 a[86:87], v203 offset:62464\n\t" \
    "v_lshlrev_b32 v0, 4, v223\n\t" \
    "s_mul_i32 s69, s66, 2048\n\t" \
    "s_add_u32 s69, s69, 147456\n\t" \
    "v_lshl_add_u32 v4, v223, 4, s69\n\t" \
    "s_mov_b32 s69, 147456\n\t" \
    "v_lshl_add_u32 v5, v223, 4, s69\n\t" \
    "v_lshlrev_b32 v6, 2, v223\n\t" \
    "v_mov_b32 v7, 0\n\t" \
    "s_waitcnt vmcnt(12) lgkmcnt(0)\n\t" \
    "s_barrier\n\t" \
    "s_add_u32 m0, s82, 98304\n\t" \
    "s_nop 0\n\t" \
    "global_load_lds_dwordx4 v200, s[42:43]\n\t" \
    "v_add_u32 v200, 131072, v200\n\t" \
    "s_add_u32 m0, s82, 102400\n\t" \
    "s_nop 0\n\t" \
    "global_load_lds_dwordx4 v200, s[42:43]\n\t" \
    "v_add_u32 v200, 131072, v200\n\t" \
    "s_add_u32 m0, s82, 106496\n\t" \
    "s_nop 0\n\t" \
    "global_load_lds_dwordx4 v200, s[42:43]\n\t" \
    "v_add_u32 v200, 131072, v200\n\t" \
    "s_add_u32 m0, s82, 110592\n\t" \
    "s_nop 0\n\t" \
    "global_load_lds_dwordx4 v200, s[42:43]\n\t" \
    "v_add_u32 v200, 131072, v200\n\t" \
    "v_add_u32 v202, 69632, v202\n\t" \
    "v_add_u32 v203, 69632, v203\n\t" \
    "ds_read_b64_tr_b16 a[16:17], v202 offset:0\n\t" \
    "ds_read_b64_tr_b16 a[18:19], v202 offset:1024\n\t" \
    "ds_read_b64_tr_b16 a[20:21], v202 offset:8192\n\t" \
    "ds_read_b64_tr_b16 a[22:23], v202 offset:9216\n\t" \
    "ds_read_b64_tr_b16 a[88:89], v203 offset:0\n\t" \
    "ds_read_b64_tr_b16 a[90:91], v203 offset:1024\n\t" \
    "ds_read_b64_tr_b16 a[92:93], v203 offset:8192\n\t" \
    "ds_read_b64_tr_b16 a[94:95], v203 offset:9216\n\t" \
    "s_bfe_u32 s71, s67, 0x50000\n\t" \
    "s_mul_i32 s69, s71, 32\n\t" \
    "s_add_u32 s69, s69, 262148\n\t" \
    "v_lshl_add_u32 v19, v180, 16, s69\n\t" \
    "v_lshl_add_u32 v19, v181, 13, v19\n\t" \
    "v_bfe_u32 v193, v223, 2, 3\n\t" \
    "v_lshlrev_b32 v199, 10, v193\n\t" \
    "v_min_u32 v199, 4096, v199\n\t" \
    "v_add_u32 v19, v199, v19\n\t" \
    "s_bfe_u32 s71, s67, 0x50000\n\t" \
    "s_mul_i32 s69, s71, 1024\n\t" \
    "v_lshl_add_u32 v17, v223, 4, s69\n\t" \
    "s_mul_i32 s69, s66, 9\n\t" \
    "s_add_u32 s69, s69, 0\n\t" \
    "s_mul_i32 s70, s69, 7\n\t" \
    "s_and_b32 s70, s70, 63\n\t" \
    "s_lshl_b32 s70, s70, 15\n\t" \
    "s_mul_i32 s69, s69, 2097152\n\t" \
    "s_add_u32 s69, s69, s70\n\t" \
    "v_add_u32 v8, s69, v17\n\t" \
    "s_mul_i32 s69, s66, 9\n\t" \
    "s_add_u32 s69, s69, 1\n\t" \
    "s_mul_i32 s70, s69, 7\n\t" \
    "s_and_b32 s70, s70, 63\n\t" \
    "s_lshl_b32 s70, s70, 15\n\t" \
    "s_mul_i32 s69, s69, 2097152\n\t" \
    "s_add_u32 s69, s69, s70\n\t" \
    "v_add_u32 v9, s69, v17\n\t" \
    "s_mul_i32 s69, s66, 9\n\t" \
    "s_add_u32 s69, s69, 2\n\t" \
    "s_mul_i32 s70, s69, 7\n\t" \
    "s_and_b32 s70, s70, 63\n\t" \
    "s_lshl_b32 s70, s70, 15\n\t" \
    "s_mul_i32 s69, s69, 2097152\n\t" \
    "s_add_u32 s69, s69, s70\n\t" \
    "v_add_u32 v10, s69, v17\n\t" \
    "s_mul_i32 s69, s66, 9\n\t" \
    "s_add_u32 s69, s69, 3\n\t" \
    "s_mul_i32 s70, s69, 7\n\t" \
    "s_and_b32 s70, s70, 63\n\t" \
    "s_lshl_b32 s70, s70, 15\n\t" \
    "s_mul_i32 s69, s69, 2097152\n\t" \
    "s_add_u32 s69, s69, s70\n\t" \
    "v_add_u32 v11, s69, v17\n\t" \
    "s_mul_i32 s69, s66, 9\n\t" \
    "s_add_u32 s69, s69, 4\n\t" \
    "s_mul_i32 s70, s69, 7\n\t" \
    "s_and_b32 s70, s70, 63\n\t" \
    "s_lshl_b32 s70, s70, 15\n\t" \
    "s_mul_i32 s69, s69, 2097152\n\t" \
    "s_add_u32 s69, s69, s70\n\t" \
    "v_add_u32 v12, s69, v17\n\t" \
    "s_mul_i32 s69, s66, 9\n\t" \
    "s_add_u32 s69, s69, 5\n\t" \
    "s_mul_i32 s70, s69, 7\n\t" \
    "s_and_b32 s70, s70, 63\n\t" \
    "s_lshl_b32 s70, s70, 15\n\t" \
    "s_mul_i32 s69, s69, 2097152\n\t" \
    "s_add_u32 s69, s69, s70\n\t" \
    "v_add_u32 v13, s69, v17\n\t" \
    "s_mul_i32 s69, s66, 9\n\t" \
    "s_add_u32 s69, s69, 6\n\t" \
    "s_mul_i32 s70, s69, 7\n\t" \
    "s_and_b32 s70, s70, 63\n\t" \
    "s_lshl_b32 s70, s70, 15\n\t" \
    "s_mul_i32 s69, s69, 2097152\n\t" \
    "s_add_u32 s69, s69, s70\n\t" \
    "v_add_u32 v14, s69, v17\n\t" \
    "s_mul_i32 s69, s66, 9\n\t" \
    "s_add_u32 s69, s69, 7\n\t" \
    "s_mul_i32 s70, s69, 7\n\t" \
    "s_and_b32 s70, s70, 63\n\t" \
    "s_lshl_b32 s70, s70, 15\n\t" \
    "s_mul_i32 s69, s69, 2097152\n\t" \
    "s_add_u32 s69, s69, s70\n\t" \
    "v_add_u32 v15, s69, v17\n\t" \
    "s_mul_i32 s69, s66, 9\n\t" \
    "s_add_u32 s69, s69, 8\n\t" \
    "s_mul_i32 s70, s69, 7\n\t" \
    "s_and_b32 s70, s70, 63\n\t" \
    "s_lshl_b32 s70, s70, 15\n\t" \
    "s_mul_i32 s69, s69, 2097152\n\t" \
    "s_add_u32 s69, s69, s70\n\t" \
    "v_add_u32 v16, s69, v17\n\t" \
    "s_waitcnt vmcnt(12) lgkmcnt(0)\n\t" \
    "s_barrier\n\t" \
    "s_add_u32 m0, s82, 0\n\t" \
    "s_nop 0\n\t" \
    "global_load_lds_dwordx4 v200, s[42:43]\n\t" \
    "v_add_u32 v200, 131072, v200\n\t" \
    "s_add_u32 m0, s82, 4096\n\t" \
    "s_nop 0\n\t" \
    "global_load_lds_dwordx4 v200, s[42:43]\n\t" \
    "v_add_u32 v200, 131072, v200\n\t" \
    "s_add_u32 m0, s82, 8192\n\t" \
    "s_nop 0\n\t" \
    "global_load_lds_dwordx4 v200, s[42:43]\n\t" \
    "v_add_u32 v200, 131072, v200\n\t" \
    "s_add_u32 m0, s82, 12288\n\t" \
    "s_nop 0\n\t" \
    "global_load_lds_dwordx4 v200, s[42:43]\n\t" \
    "v_add_u32 v200, 131072, v200\n\t" \
    "ds_read_b64_tr_b16 a[24:25], v202 offset:16384\n\t" \
    "ds_read_b64_tr_b16 a[26:27], v202 offset:17408\n\t" \
    "ds_read_b64_tr_b16 a[28:29], v202 offset:24576\n\t" \
    "ds_read_b64_tr_b16 a[30:31], v202 offset:25600\n\t" \
    "ds_read_b64_tr_b16 a[96:97], v203 offset:16384\n\t" \
    "ds_read_b64_tr_b16 a[98:99], v203 offset:17408\n\t" \
    "ds_read_b64_tr_b16 a[100:101], v203 offset:24576\n\t" \
    "ds_read_b64_tr_b16 a[102:103], v203 offset:25600\n\t" \
    "s_waitcnt vmcnt(12) lgkmcnt(0)\n\t" \
    "s_barrier\n\t" \
    "s_add_u32 m0, s82, 16384\n\t" \
    "s_nop 0\n\t" \
    "global_load_lds_dwordx4 v200, s[42:43]\n\t" \
    "v_add_u32 v200, 131072, v200\n\t" \
    "s_add_u32 m0, s82, 20480\n\t" \
    "s_nop 0\n\t" \
    "global_load_lds_dwordx4 v200, s[42:43]\n\t" \
    "v_add_u32 v200, 131072, v200\n\t" \
    "s_add_u32 m0, s82, 24576\n\t" \
    "s_nop 0\n\t" \
    "global_load_lds_dwordx4 v200, s[42:43]\n\t" \
    "v_add_u32 v200, 131072, v200\n\t" \
    "s_add_u32 m0, s82, 28672\n\t" \
    "s_nop 0\n\t" \
    "global_load_lds_dwordx4 v200, s[42:43]\n\t" \
    "v_add_u32 v200, 131072, v200\n\t" \
    "s_add_u32 m0, s89, 0\n\t" \
    "s_nop 0\n\t" \
    "global_load_lds_dwordx4 v1, s[50:51] offset:0\n\t" \
    "s_add_u32 m0, s89, 960\n\t" \
    "s_nop 0\n\t" \
    "global_load_lds_dwordx4 v1, s[50:51] offset:64\n\t" \
    "s_add_u32 m0, s89, 1920\n\t" \
    "s_nop 0\n\t" \
    "global_load_lds_dwordx4 v1, s[50:51] offset:128\n\t" \
    "s_add_u32 m0, s89, 2880\n\t" \
    "s_nop 0\n\t" \
    "global_load_lds_dwordx4 v1, s[50:51] offset:192\n\t" \
    "s_add_u32 m0, s89, 3840\n\t" \
    "s_nop 0\n\t" \
    "global_load_lds_dwordx4 v1, s[50:51] offset:256\n\t" \
    "s_add_u32 m0, s89, 4800\n\t" \
    "s_nop 0\n\t" \
    "global_load_lds_dwordx4 v1, s[50:51] offset:320\n\t" \
    "s_add_u32 m0, s89, 5760\n\t" \
    "s_nop 0\n\t" \
    "global_load_lds_dwordx4 v1, s[50:51] offset:384\n\t" \
    "s_add_u32 m0, s89, 6720\n\t" \
    "s_nop 0\n\t" \
    "global_load_lds_dwordx4 v1, s[50:51] offset:448\n\t" \
    "s_add_u32 m0, s89, 7680\n\t" \
    "s_nop 0\n\t" \
    "global_load_lds_dwordx4 v1, s[50:51] offset:512\n\t" \
    "ds_read_b64_tr_b16 a[32:33], v202 offset:32768\n\t" \
    "ds_read_b64_tr_b16 a[34:35], v202 offset:33792\n\t" \
    "ds_read_b64_tr_b16 a[36:37], v202 offset:40960\n\t" \
    "ds_read_b64_tr_b16 a[38:39], v202 offset:41984\n\t" \
    "ds_read_b64_tr_b16 a[104:105], v203 offset:32768\n\t" \
    "ds_read_b64_tr_b16 a[106:107], v203 offset:33792\n\t" \
    "ds_read_b64_tr_b16 a[108:109], v203 offset:40960\n\t" \
    "ds_read_b64_tr_b16 a[110:111], v203 offset:41984\n\t" \
    "s_waitcnt vmcnt(21) lgkmcnt(0)\n\t" \
    "s_barrier\n\t" \
    "s_add_u32 m0, s96, 73728\n\t" \
    "s_nop 0\n\t" \
    "global_load_lds_dwordx4 v201, s[44:45] offset:0\n\t" \
    "s_add_u32 m0, s96, 74624\n\t" \
    "s_nop 0\n\t" \
    "global_load_lds_dwordx4 v201, s[44:45] offset:128\n\t" \
    "s_add_u32 m0, s96, 75520\n\t" \
    "s_nop 0\n\t" \
    "global_load_lds_dwordx4 v201, s[44:45] offset:256\n\t" \
    "s_add_u32 m0, s96, 76416\n\t" \
    "s_nop 0\n\t" \
    "global_load_lds_dwordx4 v201, s[44:45] offset:384\n\t" \
    "s_add_u32 m0, s96, 77312\n\t" \
    "s_nop 0\n\t" \
    "global_load_lds_dwordx4 v201, s[44:45] offset:512\n\t" \
    "s_add_u32 m0, s96, 78208\n\t" \
    "s_nop 0\n\t" \
    "global_load_lds_dwordx4 v201, s[44:45] offset:640\n\t" \
    "s_add_u32 m0, s96, 79104\n\t" \
    "s_nop 0\n\t" \
    "global_load_lds_dwordx4 v201, s[44:45] offset:768\n\t" \
    "s_add_u32 m0, s96, 80000\n\t" \
    "s_nop 0\n\t" \
    "global_load_lds_dwordx4 v201, s[44:45] offset:896\n\t" \
    "s_add_u32 m0, s96, 80896\n\t" \
    "s_nop 0\n\t" \
    "global_load_lds_dwordx4 v201, s[44:45] offset:1024\n\t" \
    "v_add_u32 v201, 36864, v201\n\t" \
    "ds_read_b64_tr_b16 a[40:41], v202 offset:49152\n\t" \
    "ds_read_b64_tr_b16 a[42:43], v202 offset:50176\n\t" \
    "ds_read_b64_tr_b16 a[44:45], v202 offset:57344\n\t" \
    "ds_read_b64_tr_b16 a[46:47], v202 offset:58368\n\t" \
    "ds_read_b64_tr_b16 a[112:113], v203 offset:49152\n\t" \
    "ds_read_b64_tr_b16 a[114:115], v203 offset:50176\n\t" \
    "ds_read_b64_tr_b16 a[116:117], v203 offset:57344\n\t" \
    "ds_read_b64_tr_b16 a[118:119], v203 offset:58368\n\t" \
    "s_waitcnt vmcnt(26) lgkmcnt(0)\n\t" \
    "s_barrier\n\t" \
    "v_add_u32 v202, 65536, v202\n\t" \
    "v_add_u32 v203, 65536, v203\n\t" \
    "ds_read_b64_tr_b16 a[48:49], v202 offset:0\n\t" \
    "ds_read_b64_tr_b16 a[50:51], v202 offset:1024\n\t" \
    "ds_read_b64_tr_b16 a[52:53], v202 offset:8192\n\t" \
    "ds_read_b64_tr_b16 a[54:55], v202 offset:9216\n\t" \
    "ds_read_b64_tr_b16 a[120:121], v203 offset:0\n\t" \
    "ds_read_b64_tr_b16 a[122:123], v203 offset:1024\n\t" \
    "ds_read_b64_tr_b16 a[124:125], v203 offset:8192\n\t" \
    "ds_read_b64_tr_b16 a[126:127], v203 offset:9216\n\t" \
    "s_waitcnt vmcnt(22) lgkmcnt(0)\n\t" \
    "s_barrier\n\t" \
    "s_add_u32 m0, s96, 110592\n\t" \
    "s_nop 0\n\t" \
    "global_load_lds_dwordx4 v201, s[44:45] offset:0\n\t" \
    "s_add_u32 m0, s96, 111488\n\t" \
    "s_nop 0\n\t" \
    "global_load_lds_dwordx4 v201, s[44:45] offset:128\n\t" \
    "s_add_u32 m0, s96, 112384\n\t" \
    "s_nop 0\n\t" \
    "global_load_lds_dwordx4 v201, s[44:45] offset:256\n\t" \
    "s_add_u32 m0, s96, 113280\n\t" \
    "s_nop 0\n\t" \
    "global_load_lds_dwordx4 v201, s[44:45] offset:384\n\t" \
    "s_add_u32 m0, s96, 114176\n\t" \
    "s_nop 0\n\t" \
    "global_load_lds_dwordx4 v201, s[44:45] offset:512\n\t" \
    "s_add_u32 m0, s96, 115072\n\t" \
    "s_nop 0\n\t" \
    "global_load_lds_dwordx4 v201, s[44:45] offset:640\n\t" \
    "s_add_u32 m0, s96, 115968\n\t" \
    "s_nop 0\n\t" \
    "global_load_lds_dwordx4 v201, s[44:45] offset:768\n\t" \
    "s_add_u32 m0, s96, 116864\n\t" \
    "s_nop 0\n\t" \
    "global_load_lds_dwordx4 v201, s[44:45] offset:896\n\t" \
    "s_add_u32 m0, s96, 117760\n\t" \
    "s_nop 0\n\t" \
    "global_load_lds_dwordx4 v201, s[44:45] offset:1024\n\t" \
    "v_add_u32 v201, 36864, v201\n\t" \
    "v_mbcnt_lo_u32_b32 v164, -1, 0\n\t" \
    "v_mbcnt_hi_u32_b32 v164, -1, v164\n\t" \
    "s_cmp_eq_u32 s66, 0\n\t" \
    "s_cselect_b32 s69, 59, 11\n\t" \
    "v_min_u32 v164, s69, v164\n\t" \
    "v_lshlrev_b32 v164, 6, v164\n\t" \
    "v_add_u32 v164, 6225920, v164\n\t" \
    "v_mov_b32 v165, 6234112\n\t" \
    "global_load_dword v165, v165, s[36:37] sc1\n\t" \
    "global_load_dwordx2 v[166:167], v164, s[36:37] sc1\n\t" \
    "v_add_u32 v202, -98304, v202\n\t" \
    "v_add_u32 v203, -98304, v203\n\t" \
    "ds_read_b64_tr_b16 a[56:57], v202 offset:0\n\t" \
    "ds_read_b64_tr_b16 a[58:59], v202 offset:1024\n\t" \
    "ds_read_b64_tr_b16 a[60:61], v202 offset:8192\n\t" \
    "ds_read_b64_tr_b16 a[62:63], v202 offset:9216\n\t" \
    "ds_read_b64_tr_b16 a[128:129], v203 offset:0\n\t" \
    "ds_read_b64_tr_b16 a[130:131], v203 offset:1024\n\t" \
    "ds_read_b64_tr_b16 a[132:133], v203 offset:8192\n\t" \
    "ds_read_b64_tr_b16 a[134:135], v203 offset:9216\n\t" \
    "s_waitcnt vmcnt(29) lgkmcnt(0)\n\t" \
    "s_barrier\n\t" \
    "ds_read_b64_tr_b16 a[64:65], v202 offset:16384\n\t" \
    "ds_read_b64_tr_b16 a[66:67], v202 offset:17408\n\t" \
    "ds_read_b64_tr_b16 a[68:69], v202 offset:24576\n\t" \
    "ds_read_b64_tr_b16 a[70:71], v202 offset:25600\n\t" \
    "ds_read_b64_tr_b16 a[136:137], v203 offset:16384\n\t" \
    "ds_read_b64_tr_b16 a[138:139], v203 offset:17408\n\t" \
    "ds_read_b64_tr_b16 a[140:141], v203 offset:24576\n\t" \
    "ds_read_b64_tr_b16 a[142:143], v203 offset:25600\n\t" \
    "s_waitcnt vmcnt(11) lgkmcnt(0)\n\t" \
    "s_barrier\n\t" \
    "s_add_u32 m0, s96, 36864\n\t" \
    "s_nop 0\n\t" \
    "global_load_lds_dwordx4 v201, s[44:45] offset:0\n\t" \
    "s_add_u32 m0, s96, 37760\n\t" \
    "s_nop 0\n\t" \
    "global_load_lds_dwordx4 v201, s[44:45] offset:128\n\t" \
    "s_add_u32 m0, s96, 38656\n\t" \
    "s_nop 0\n\t" \
    "global_load_lds_dwordx4 v201, s[44:45] offset:256\n\t" \
    "s_add_u32 m0, s96, 39552\n\t" \
    "s_nop 0\n\t" \
    "global_load_lds_dwordx4 v201, s[44:45] offset:384\n\t" \
    "s_add_u32 m0, s96, 40448\n\t" \
    "s_nop 0\n\t" \
    "global_load_lds_dwordx4 v201, s[44:45] offset:512\n\t" \
    "s_add_u32 m0, s96, 41344\n\t" \
    "s_nop 0\n\t" \
    "global_load_lds_dwordx4 v201, s[44:45] offset:640\n\t" \
    "s_add_u32 m0, s96, 42240\n\t" \
    "s_nop 0\n\t" \
    "global_load_lds_dwordx4 v201, s[44:45] offset:768\n\t" \
    "s_add_u32 m0, s96, 43136\n\t" \
    "s_nop 0\n\t" \
    "global_load_lds_dwordx4 v201, s[44:45] offset:896\n\t" \
    "s_add_u32 m0, s96, 44032\n\t" \
    "s_nop 0\n\t" \
    "global_load_lds_dwordx4 v201, s[44:45] offset:1024\n\t" \
    "v_add_u32 v201, 36864, v201\n\t" \
    "v_add_u32 v204, 73728, v204\n\t" \
    "v_add_u32 v205, 73728, v205\n\t" \
    "v_add_u32 v206, 73728, v206\n\t" \
    "v_add_u32 v207, 73728, v207\n\t" \
    "ds_read_b64_tr_b16 a[144:145], v204 offset:0\n\t" \
    "ds_read_b64_tr_b16 a[146:147], v204 offset:18432\n\t" \
    "ds_read_b64_tr_b16 a[160:161], v205 offset:0\n\t" \
    "ds_read_b64_tr_b16 a[162:163], v205 offset:18432\n\t" \
    "ds_read_b64_tr_b16 a[176:177], v206 offset:0\n\t" \
    "ds_read_b64_tr_b16 a[178:179], v206 offset:18432\n\t" \
    "s_waitcnt lgkmcnt(6)\n\t" \
    "ds_read_b64_tr_b16 a[192:193], v207 offset:0\n\t" \
    "ds_read_b64_tr_b16 a[194:195], v207 offset:18432\n\t" \
    "ds_read_b64_tr_b16 a[208:209], v204 offset:1024\n\t" \
    "ds_read_b64_tr_b16 a[210:211], v204 offset:19456\n\t" \
    "ds_read_b64_tr_b16 a[224:225], v205 offset:1024\n\t" \
    "ds_read_b64_tr_b16 a[226:227], v205 offset:19456\n\t" \
    "s_waitcnt lgkmcnt(6)\n\t" \
    "ds_read_b64_tr_b16 a[240:241], v206 offset:1024\n\t" \
    "ds_read_b64_tr_b16 a[242:243], v206 offset:19456\n\t" \
    "ds_read_b64_tr_b16 v[224:225], v207 offset:1024\n\t" \
    "ds_read_b64_tr_b16 v[226:227], v207 offset:19456\n\t" \
    "ds_read_b64_tr_b16 v[240:241], v204 offset:2048\n\t" \
    "ds_read_b64_tr_b16 v[242:243], v204 offset:20480\n\t" \
    "s_waitcnt lgkmcnt(6)\n\t" \
    "s_waitcnt lgkmcnt(6)\n\t" \
    "ds_read_b128 v[24:27], v2 offset:0\n\t" \
    "ds_read_b128 v[28:31], v2 offset:18432\n\t" \
    "ds_read_b128 v[32:35], v2 offset:1024\n\t" \
    "ds_read_b128 v[36:39], v2 offset:19456\n\t" \
    "s_waitcnt lgkmcnt(3)\n\t" \
    "v_mfma_f32_16x16x32_bf16 v[72:75], a[0:3], v[24:27], 0\n\t" \
    "ds_read_b128 v[40:43], v2 offset:2048\n\t" \
    "s_waitcnt lgkmcnt(3)\n\t" \
    "v_mfma_f32_16x16x32_bf16 v[76:79], a[0:3], v[28:31], 0\n\t" \
    "ds_read_b128 v[44:47], v2 offset:20480\n\t" \
    "v_mfma_f32_16x16x32_bf16 v[80:83], a[72:75], v[24:27], 0\n\t" \
    "v_mfma_f32_16x16x32_bf16 v[84:87], a[72:75], v[28:31], 0\n\t" \
    "s_waitcnt lgkmcnt(3)\n\t" \
    "v_mfma_f32_16x16x32_bf16 v[72:75], a[4:7], v[32:35], v[72:75]\n\t" \
    "ds_read_b128 v[48:51], v2 offset:3072\n\t" \
    "s_waitcnt lgkmcnt(3)\n\t" \
    "v_mfma_f32_16x16x32_bf16 v[76:79], a[4:7], v[36:39], v[76:79]\n\t" \
    "ds_read_b128 v[52:55], v2 offset:21504\n\t" \
    "v_mfma_f32_16x16x32_bf16 v[80:83], a[76:79], v[32:35], v[80:83]\n\t" \
    "v_mfma_f32_16x16x32_bf16 v[84:87], a[76:79], v[36:39], v[84:87]\n\t" \
    "s_waitcnt lgkmcnt(3)\n\t" \
    "v_mfma_f32_16x16x32_bf16 v[72:75], a[8:11], v[40:43], v[72:75]\n\t" \
    "ds_read_b128 v[56:59], v2 offset:4096\n\t" \
    "s_waitcnt lgkmcnt(3)\n\t" \
    "v_mfma_f32_16x16x32_bf16 v[76:79], a[8:11], v[44:47], v[76:79]\n\t" \
    "ds_read_b128 v[60:63], v2 offset:22528\n\t" \
    "v_mfma_f32_16x16x32_bf16 v[80:83], a[80:83], v[40:43], v[80:83]\n\t" \
    "v_mfma_f32_16x16x32_bf16 v[84:87], a[80:83], v[44:47], v[84:87]\n\t" \
    "s_waitcnt lgkmcnt(3)\n\t" \
    "v_mfma_f32_16x16x32_bf16 v[72:75], a[12:15], v[48:51], v[72:75]\n\t" \
    "ds_read_b128 v[64:67], v2 offset:5120\n\t" \
    "s_waitcnt lgkmcnt(3)\n\t" \
    "v_mfma_f32_16x16x32_bf16 v[76:79], a[12:15], v[52:55], v[76:79]\n\t" \
    "ds_read_b128 v[68:71], v2 offset:23552\n\t" \
    "v_mfma_f32_16x16x32_bf16 v[80:83], a[84:87], v[48:51], v[80:83]\n\t" \
    "v_mfma_f32_16x16x32_bf16 v[84:87], a[84:87], v[52:55], v[84:87]\n\t" \
    "s_waitcnt lgkmcnt(3)\n\t" \
    "v_mfma_f32_16x16x32_bf16 v[72:75], a[16:19], v[56:59], v[72:75]\n\t" \
    "s_waitcnt lgkmcnt(2)\n\t" \
    "v_mfma_f32_16x16x32_bf16 v[76:79], a[16:19], v[60:63], v[76:79]\n\t" \
    "v_mfma_f32_16x16x32_bf16 v[80:83], a[88:91], v[56:59], v[80:83]\n\t" \
    "v_mfma_f32_16x16x32_bf16 v[84:87], a[88:91], v[60:63], v[84:87]\n\t" \
    "s_waitcnt lgkmcnt(1)\n\t" \
    "v_mfma_f32_16x16x32_bf16 v[72:75], a[20:23], v[64:67], v[72:75]\n\t" \
    "s_waitcnt lgkmcnt(0)\n\t" \
    "v_mfma_f32_16x16x32_bf16 v[76:79], a[20:23], v[68:71], v[76:79]\n\t" \
    "v_mfma_f32_16x16x32_bf16 v[80:83], a[92:95], v[64:67], v[80:83]\n\t" \
    "v_mfma_f32_16x16x32_bf16 v[84:87], a[92:95], v[68:71], v[84:87]\n\t" \
    "s_waitcnt vmcnt(11) lgkmcnt(0)\n\t" \
    "s_barrier\n\t" \
    "s_add_u32 m0, s96, 73728\n\t" \
    "s_nop 0\n\t" \
    "global_load_lds_dwordx4 v201, s[44:45] offset:0\n\t" \
    "s_add_u32 m0, s96, 74624\n\t" \
    "s_nop 0\n\t" \
    "global_load_lds_dwordx4 v201, s[44:45] offset:128\n\t" \
    "s_add_u32 m0, s96, 75520\n\t" \
    "s_nop 0\n\t" \
    "global_load_lds_dwordx4 v201, s[44:45] offset:256\n\t" \
    "s_add_u32 m0, s96, 76416\n\t" \
    "s_nop 0\n\t" \
    "global_load_lds_dwordx4 v201, s[44:45] offset:384\n\t" \
    "s_add_u32 m0, s96, 77312\n\t" \
    "s_nop 0\n\t" \
    "global_load_lds_dwordx4 v201, s[44:45] offset:512\n\t" \
    "s_add_u32 m0, s96, 78208\n\t" \
    "s_nop 0\n\t" \
    "global_load_lds_dwordx4 v201, s[44:45] offset:640\n\t" \
    "s_add_u32 m0, s96, 79104\n\t" \
    "s_nop 0\n\t" \
    "global_load_lds_dwordx4 v201, s[44:45] offset:768\n\t" \
    "s_add_u32 m0, s96, 80000\n\t" \
    "s_nop 0\n\t" \
    "global_load_lds_dwordx4 v201, s[44:45] offset:896\n\t" \
    "s_add_u32 m0, s96, 80896\n\t" \
    "s_nop 0\n\t" \
    "global_load_lds_dwordx4 v201, s[44:45] offset:1024\n\t" \
    "v_add_u32 v201, 36864, v201\n\t" \
    "ds_read_b64_tr_b16 a[148:149], v204 offset:36864\n\t" \
    "ds_read_b64_tr_b16 a[150:151], v204 offset:55296\n\t" \
    "ds_read_b64_tr_b16 a[164:165], v205 offset:36864\n\t" \
    "ds_read_b64_tr_b16 a[166:167], v205 offset:55296\n\t" \
    "ds_read_b64_tr_b16 a[180:181], v206 offset:36864\n\t" \
    "ds_read_b64_tr_b16 a[182:183], v206 offset:55296\n\t" \
    "s_waitcnt lgkmcnt(6)\n\t" \
    "ds_read_b64_tr_b16 a[196:197], v207 offset:36864\n\t" \
    "ds_read_b64_tr_b16 a[198:199], v207 offset:55296\n\t" \
    "ds_read_b64_tr_b16 a[212:213], v204 offset:37888\n\t" \
    "ds_read_b64_tr_b16 a[214:215], v204 offset:56320\n\t" \
    "ds_read_b64_tr_b16 a[228:229], v205 offset:37888\n\t" \
    "ds_read_b64_tr_b16 a[230:231], v205 offset:56320\n\t" \
    "s_waitcnt lgkmcnt(6)\n\t" \
    "ds_read_b64_tr_b16 a[244:245], v206 offset:37888\n\t" \
    "ds_read_b64_tr_b16 a[246:247], v206 offset:56320\n\t" \
    "ds_read_b64_tr_b16 v[228:229], v207 offset:37888\n\t" \
    "ds_read_b64_tr_b16 v[230:231], v207 offset:56320\n\t" \
    "ds_read_b64_tr_b16 v[244:245], v204 offset:38912\n\t" \
    "ds_read_b64_tr_b16 v[246:247], v204 offset:57344\n\t" \
    "s_waitcnt lgkmcnt(6)\n\t" \
    "s_waitcnt lgkmcnt(6)\n\t" \
    "ds_read_b128 v[24:27], v2 offset:6144\n\t" \
    "ds_read_b128 v[28:31], v2 offset:24576\n\t" \
    "ds_read_b128 v[32:35], v2 offset:7168\n\t" \
    "ds_read_b128 v[36:39], v2 offset:25600\n\t" \
    "s_waitcnt lgkmcnt(3)\n\t" \
    "v_mfma_f32_16x16x32_bf16 v[72:75], a[24:27], v[24:27], v[72:75]\n\t" \
    "ds_read_b128 v[40:43], v2 offset:8192\n\t" \
    "s_waitcnt lgkmcnt(3)\n\t" \
    "v_mfma_f32_16x16x32_bf16 v[76:79], a[24:27], v[28:31], v[76:79]\n\t" \
    "ds_read_b128 v[44:47], v2 offset:26624\n\t" \
    "v_mfma_f32_16x16x32_bf16 v[80:83], a[96:99], v[24:27], v[80:83]\n\t" \
    "v_mfma_f32_16x16x32_bf16 v[84:87], a[96:99], v[28:31], v[84:87]\n\t" \
    "s_waitcnt lgkmcnt(3)\n\t" \
    "v_mfma_f32_16x16x32_bf16 v[72:75], a[28:31], v[32:35], v[72:75]\n\t" \
    "ds_read_b128 v[48:51], v2 offset:9216\n\t" \
    "s_waitcnt lgkmcnt(3)\n\t" \
    "v_mfma_f32_16x16x32_bf16 v[76:79], a[28:31], v[36:39], v[76:79]\n\t" \
    "ds_read_b128 v[52:55], v2 offset:27648\n\t" \
    "v_mfma_f32_16x16x32_bf16 v[80:83], a[100:103], v[32:35], v[80:83]\n\t" \
    "v_mfma_f32_16x16x32_bf16 v[84:87], a[100:103], v[36:39], v[84:87]\n\t" \
    "s_waitcnt lgkmcnt(3)\n\t" \
    "v_mfma_f32_16x16x32_bf16 v[72:75], a[32:35], v[40:43], v[72:75]\n\t" \
    "ds_read_b128 v[56:59], v2 offset:10240\n\t" \
    "s_waitcnt lgkmcnt(3)\n\t" \
    "v_mfma_f32_16x16x32_bf16 v[76:79], a[32:35], v[44:47], v[76:79]\n\t" \
    "ds_read_b128 v[60:63], v2 offset:28672\n\t" \
    "v_mfma_f32_16x16x32_bf16 v[80:83], a[104:107], v[40:43], v[80:83]\n\t" \
    "v_mfma_f32_16x16x32_bf16 v[84:87], a[104:107], v[44:47], v[84:87]\n\t" \
    "s_waitcnt lgkmcnt(3)\n\t" \
    "v_mfma_f32_16x16x32_bf16 v[72:75], a[36:39], v[48:51], v[72:75]\n\t" \
    "ds_read_b128 v[64:67], v2 offset:11264\n\t" \
    "s_waitcnt lgkmcnt(3)\n\t" \
    "v_mfma_f32_16x16x32_bf16 v[76:79], a[36:39], v[52:55], v[76:79]\n\t" \
    "ds_read_b128 v[68:71], v2 offset:29696\n\t" \
    "v_mfma_f32_16x16x32_bf16 v[80:83], a[108:111], v[48:51], v[80:83]\n\t" \
    "v_mfma_f32_16x16x32_bf16 v[84:87], a[108:111], v[52:55], v[84:87]\n\t" \
    "s_waitcnt lgkmcnt(3)\n\t" \
    "v_mfma_f32_16x16x32_bf16 v[72:75], a[40:43], v[56:59], v[72:75]\n\t" \
    "s_waitcnt lgkmcnt(2)\n\t" \
    "v_mfma_f32_16x16x32_bf16 v[76:79], a[40:43], v[60:63], v[76:79]\n\t" \
    "v_mfma_f32_16x16x32_bf16 v[80:83], a[112:115], v[56:59], v[80:83]\n\t" \
    "v_mfma_f32_16x16x32_bf16 v[84:87], a[112:115], v[60:63], v[84:87]\n\t" \
    "s_waitcnt lgkmcnt(1)\n\t" \
    "v_mfma_f32_16x16x32_bf16 v[72:75], a[44:47], v[64:67], v[72:75]\n\t" \
    "s_waitcnt lgkmcnt(0)\n\t" \
    "v_mfma_f32_16x16x32_bf16 v[76:79], a[44:47], v[68:71], v[76:79]\n\t" \
    "v_mfma_f32_16x16x32_bf16 v[80:83], a[116:119], v[64:67], v[80:83]\n\t" \
    "v_mfma_f32_16x16x32_bf16 v[84:87], a[116:119], v[68:71], v[84:87]\n\t" \
    "s_waitcnt vmcnt(9) lgkmcnt(0)\n\t" \
    "s_barrier\n\t" \
    "s_add_u32 s50, s50, 73728\n\t" \
    "s_addc_u32 s51, s51, 0\n\t" \
    "s_add_u32 m0, s89, 110592\n\t" \
    "s_nop 0\n\t" \
    "global_load_lds_dwordx4 v1, s[50:51] offset:0\n\t" \
    "s_add_u32 m0, s89, 111552\n\t" \
    "s_nop 0\n\t" \
    "global_load_lds_dwordx4 v1, s[50:51] offset:64\n\t" \
    "s_add_u32 m0, s89, 112512\n\t" \
    "s_nop 0\n\t" \
    "global_load_lds_dwordx4 v1, s[50:51] offset:128\n\t" \
    "s_add_u32 m0, s89, 113472\n\t" \
    "s_nop 0\n\t" \
    "global_load_lds_dwordx4 v1, s[50:51] offset:192\n\t" \
    "s_add_u32 m0, s89, 114432\n\t" \
    "s_nop 0\n\t" \
    "global_load_lds_dwordx4 v1, s[50:51] offset:256\n\t" \
    "s_add_u32 m0, s89, 115392\n\t" \
    "s_nop 0\n\t" \
    "global_load_lds_dwordx4 v1, s[50:51] offset:320\n\t" \
    "s_add_u32 m0, s89, 116352\n\t" \
    "s_nop 0\n\t" \
    "global_load_lds_dwordx4 v1, s[50:51] offset:384\n\t" \
    "s_add_u32 m0, s89, 117312\n\t" \
    "s_nop 0\n\t" \
    "global_load_lds_dwordx4 v1, s[50:51] offset:448\n\t" \
    "s_add_u32 m0, s89, 118272\n\t" \
    "s_nop 0\n\t" \
    "global_load_lds_dwordx4 v1, s[50:51] offset:512\n\t" \
    "v_add_u32 v204, -36864, v204\n\t" \
    "v_add_u32 v205, -36864, v205\n\t" \
    "v_add_u32 v206, -36864, v206\n\t" \
    "v_add_u32 v207, -36864, v207\n\t" \
    "ds_read_b64_tr_b16 a[152:153], v204 offset:0\n\t" \
    "ds_read_b64_tr_b16 a[154:155], v204 offset:18432\n\t" \
    "ds_read_b64_tr_b16 a[168:169], v205 offset:0\n\t" \
    "ds_read_b64_tr_b16 a[170:171], v205 offset:18432\n\t" \
    "ds_read_b64_tr_b16 a[184:185], v206 offset:0\n\t" \
    "ds_read_b64_tr_b16 a[186:187], v206 offset:18432\n\t" \
    "s_waitcnt lgkmcnt(6)\n\t" \
    "ds_read_b64_tr_b16 a[200:201], v207 offset:0\n\t" \
    "ds_read_b64_tr_b16 a[202:203], v207 offset:18432\n\t" \
    "ds_read_b64_tr_b16 a[216:217], v204 offset:1024\n\t" \
    "ds_read_b64_tr_b16 a[218:219], v204 offset:19456\n\t" \
    "ds_read_b64_tr_b16 a[232:233], v205 offset:1024\n\t" \
    "ds_read_b64_tr_b16 a[234:235], v205 offset:19456\n\t" \
    "s_waitcnt lgkmcnt(6)\n\t" \
    "ds_read_b64_tr_b16 a[248:249], v206 offset:1024\n\t" \
    "ds_read_b64_tr_b16 a[250:251], v206 offset:19456\n\t" \
    "ds_read_b64_tr_b16 v[232:233], v207 offset:1024\n\t" \
    "ds_read_b64_tr_b16 v[234:235], v207 offset:19456\n\t" \
    "ds_read_b64_tr_b16 v[248:249], v204 offset:2048\n\t" \
    "ds_read_b64_tr_b16 v[250:251], v204 offset:20480\n\t" \
    "s_waitcnt lgkmcnt(6)\n\t" \
    "s_waitcnt lgkmcnt(6)\n\t" \
    "ds_read_b128 v[24:27], v2 offset:12288\n\t" \
    "ds_read_b128 v[28:31], v2 offset:30720\n\t" \
    "ds_read_b128 v[32:35], v2 offset:13312\n\t" \
    "ds_read_b128 v[36:39], v2 offset:31744\n\t" \
    "s_waitcnt lgkmcnt(3)\n\t" \
    "v_mfma_f32_16x16x32_bf16 v[72:75], a[48:51], v[24:27], v[72:75]\n\t" \
    "ds_read_b128 v[40:43], v2 offset:14336\n\t" \
    "s_waitcnt lgkmcnt(3)\n\t" \
    "v_mfma_f32_16x16x32_bf16 v[76:79], a[48:51], v[28:31], v[76:79]\n\t" \
    "ds_read_b128 v[44:47], v2 offset:32768\n\t" \
    "v_mfma_f32_16x16x32_bf16 v[80:83], a[120:123], v[24:27], v[80:83]\n\t" \
    "v_mfma_f32_16x16x32_bf16 v[84:87], a[120:123], v[28:31], v[84:87]\n\t" \
    "s_waitcnt lgkmcnt(3)\n\t" \
    "v_mfma_f32_16x16x32_bf16 v[72:75], a[52:55], v[32:35], v[72:75]\n\t" \
    "ds_read_b128 v[48:51], v2 offset:15360\n\t" \
    "s_waitcnt lgkmcnt(3)\n\t" \
    "v_mfma_f32_16x16x32_bf16 v[76:79], a[52:55], v[36:39], v[76:79]\n\t" \
    "ds_read_b128 v[52:55], v2 offset:33792\n\t" \
    "v_mfma_f32_16x16x32_bf16 v[80:83], a[124:127], v[32:35], v[80:83]\n\t" \
    "v_mfma_f32_16x16x32_bf16 v[84:87], a[124:127], v[36:39], v[84:87]\n\t" \
    "s_waitcnt lgkmcnt(3)\n\t" \
    "v_mfma_f32_16x16x32_bf16 v[72:75], a[56:59], v[40:43], v[72:75]\n\t" \
    "ds_read_b128 v[56:59], v2 offset:16384\n\t" \
    "s_waitcnt lgkmcnt(3)\n\t" \
    "v_mfma_f32_16x16x32_bf16 v[76:79], a[56:59], v[44:47], v[76:79]\n\t" \
    "ds_read_b128 v[60:63], v2 offset:34816\n\t" \
    "v_mfma_f32_16x16x32_bf16 v[80:83], a[128:131], v[40:43], v[80:83]\n\t" \
    "v_mfma_f32_16x16x32_bf16 v[84:87], a[128:131], v[44:47], v[84:87]\n\t" \
    "s_waitcnt lgkmcnt(3)\n\t" \
    "v_mfma_f32_16x16x32_bf16 v[72:75], a[60:63], v[48:51], v[72:75]\n\t" \
    "ds_read_b128 v[64:67], v2 offset:17408\n\t" \
    "s_waitcnt lgkmcnt(3)\n\t" \
    "v_mfma_f32_16x16x32_bf16 v[76:79], a[60:63], v[52:55], v[76:79]\n\t" \
    "ds_read_b128 v[68:71], v2 offset:35840\n\t" \
    "v_mfma_f32_16x16x32_bf16 v[80:83], a[132:135], v[48:51], v[80:83]\n\t" \
    "v_mfma_f32_16x16x32_bf16 v[84:87], a[132:135], v[52:55], v[84:87]\n\t" \
    "s_waitcnt lgkmcnt(3)\n\t" \
    "v_mfma_f32_16x16x32_bf16 v[72:75], a[64:67], v[56:59], v[72:75]\n\t" \
    "s_waitcnt lgkmcnt(2)\n\t" \
    "v_mfma_f32_16x16x32_bf16 v[76:79], a[64:67], v[60:63], v[76:79]\n\t" \
    "v_mfma_f32_16x16x32_bf16 v[80:83], a[136:139], v[56:59], v[80:83]\n\t" \
    "v_mfma_f32_16x16x32_bf16 v[84:87], a[136:139], v[60:63], v[84:87]\n\t" \
    "s_waitcnt lgkmcnt(1)\n\t" \
    "v_mfma_f32_16x16x32_bf16 v[72:75], a[68:71], v[64:67], v[72:75]\n\t" \
    "s_waitcnt lgkmcnt(0)\n\t" \
    "v_mfma_f32_16x16x32_bf16 v[76:79], a[68:71], v[68:71], v[76:79]\n\t" \
    "v_mfma_f32_16x16x32_bf16 v[80:83], a[140:143], v[64:67], v[80:83]\n\t" \
    "v_mfma_f32_16x16x32_bf16 v[84:87], a[140:143], v[68:71], v[84:87]\n\t" \
    "s_waitcnt vmcnt(9) lgkmcnt(0)\n\t" \
    "s_barrier\n\t" \
    "ds_read_b64_tr_b16 a[156:157], v204 offset:36864\n\t" \
    "ds_read_b64_tr_b16 a[158:159], v204 offset:55296\n\t" \
    "ds_read_b64_tr_b16 a[172:173], v205 offset:36864\n\t" \
    "ds_read_b64_tr_b16 a[174:175], v205 offset:55296\n\t" \
    "ds_read_b64_tr_b16 a[188:189], v206 offset:36864\n\t" \
    "ds_read_b64_tr_b16 a[190:191], v206 offset:55296\n\t" \
    "s_waitcnt lgkmcnt(6)\n\t" \
    "ds_read_b64_tr_b16 a[204:205], v207 offset:36864\n\t" \
    "ds_read_b64_tr_b16 a[206:207], v207 offset:55296\n\t" \
    "ds_read_b64_tr_b16 a[220:221], v204 offset:37888\n\t" \
    "ds_read_b64_tr_b16 a[222:223], v204 offset:56320\n\t" \
    "ds_read_b64_tr_b16 a[236:237], v205 offset:37888\n\t" \
    "ds_read_b64_tr_b16 a[238:239], v205 offset:56320\n\t" \
    "s_waitcnt lgkmcnt(6)\n\t" \
    "ds_read_b64_tr_b16 a[252:253], v206 offset:37888\n\t" \
    "ds_read_b64_tr_b16 a[254:255], v206 offset:56320\n\t" \
    "ds_read_b64_tr_b16 v[236:237], v207 offset:37888\n\t" \
    "ds_read_b64_tr_b16 v[238:239], v207 offset:56320\n\t" \
    "ds_read_b64_tr_b16 v[252:253], v204 offset:38912\n\t" \
    "ds_read_b64_tr_b16 v[254:255], v204 offset:57344\n\t" \
    "s_waitcnt lgkmcnt(6)\n\t" \
    "s_waitcnt lgkmcnt(0)\n\t" \
    "s_barrier\n\t" \
    "v_sub_u32 v166, v166, v165\n\t" \
    "v_cmp_ne_u32 vcc, 1, v166\n\t" \
    "s_cbranch_vccz gv_Cok2_%=\n\t" \
    "gv_C1_%=:\n\t" \
    "global_load_dwordx2 v[166:167], v164, s[36:37] sc1\n\t" \
    "s_waitcnt vmcnt(0)\n\t" \
    "v_sub_u32 v166, v166, v165\n\t" \
    "v_cmp_ne_u32 vcc, 1, v166\n\t" \
    "s_cbranch_vccz gv_Cok2_%=\n\t" \
    "s_cmp_eq_u32 s68, 0\n\t" \
    "s_cbranch_scc1 gv_F3_%=\n\t" \
    "s_sub_u32 s68, s68, 1\n\t" \
    "s_sleep 4\n\t" \
    "s_branch gv_C1_%=\n\t" \
    "gv_F3_%=:\n\t" \
    "v_mov_b32 v168, 6095104\n\t" \
    "v_mov_b32 v169, 4\n\t" \
    "global_store_dword v168, v169, s[36:37] sc1\n\t" \
    "s_waitcnt vmcnt(0)\n\t" \
    "s_branch gv_Cok2_%=\n\t" \
    "gv_Cok2_%=:\n\t" \
    "s_cmp_lg_u32 s66, 0\n\t" \
    "s_cbranch_scc1 gv_Ca4_%=\n\t" \
    "v_mov_b32 v164, 6176768\n\t" \
    "s_mov_b64 exec, 1\n\t" \
    "global_atomic_add v164, v166, s[36:37] sc1\n\t" \
    "s_mov_b64 exec, -1\n\t" \
    "gv_Ca4_%=:\n\t" \
    "v_min_u32_dpp v167, v167, v167 row_shl:6 row_mask:0xf bank_mask:0xf\n\t" \
    "s_nop 0\n\t" \
    "v_readlane_b32 s69, v167, 4\n\t" \
    "v_readlane_b32 s70, v167, 5\n\t" \
    "s_nop 1\n\t" \
    "s_cmp_lt_u32 s70, s69\n\t" \
    "s_cselect_b32 s82, 8192, 0\n\t" \
    "s_xor_b32 s82, s82, 8192\n\t" \
    "s_lshr_b32 s69, s67, 5\n\t" \
    "s_or_b32 s69, s69, s66\n\t" \
    "s_cbranch_scc1 gv_npr5_%=\n\t" \
    "s_memrealtime s[54:55]\n\t" \
    "s_waitcnt lgkmcnt(0)\n\t" \
    "s_lshl_b32 s70, s54, 4\n\t" \
    "s_add_u32 s70, s70, 4160\n\t" \
    "v_mov_b32 v157, s70\n\t" \
    "global_store_dword v19, v157, s[36:37] sc1\n\t" \
    "gv_npr5_%=:\n\t" \
    "s_cmp_lt_u32 s65, 60\n\t" \
    "s_cselect_b32 s69, 73728, 0\n\t" \
    "s_add_u32 s50, s50, s69\n\t" \
    "s_addc_u32 s51, s51, 0\n\t" \
    "s_sub_u32 s90, s90, 36864\n\t" \
    "s_cselect_b32 s90, 110592, s90\n\t" \
    "s_sub_u32 s91, s90, 36864\n\t" \
    "s_cselect_b32 s91, 110592, s91\n\t" \
    "s_add_u32 s91, s91, s89\n\t" \
    "v_add_u32 v17, s90, v3\n\t" \
    "s_lshl_b32 s40, s65, 15\n\t" \
    "s_branch gv_mid8_%=\n\t" \
    ".p2align 6\n\t" \
    "s_nop 0\n\t" \
    "s_nop 0\n\t" \
    "gv_loop6_%=:\n\t" \
    "s_cmp_lt_u32 s65, 60\n\t" \
    "s_cselect_b32 s69, 73728, 0\n\t" \
    "s_add_u32 s50, s50, s69\n\t" \
    "s_addc_u32 s51, s51, 0\n\t" \
    "s_sub_u32 s90, s90, 36864\n\t" \
    "s_cselect_b32 s90, 110592, s90\n\t" \
    "s_sub_u32 s91, s90, 36864\n\t" \
    "s_cselect_b32 s91, 110592, s91\n\t" \
    "s_add_u32 s91, s91, s89\n\t" \
    "v_add_u32 v17, s90, v3\n\t" \
    "s_lshl_b32 s40, s65, 15\n\t" \
    "s_waitcnt lgkmcnt(5)\n\t" \
    "v_mfma_f32_16x16x32_bf16 v[72:75], a[0:3], v[24:27], 0\n\t" \
    "ds_read_b128 v[48:51], v2 offset:3072\n\t" \
    "s_waitcnt lgkmcnt(5)\n\t" \
    "v_mfma_f32_16x16x32_bf16 v[76:79], a[0:3], v[28:31], 0\n\t" \
    "ds_read_b128 v[52:55], v2 offset:21504\n\t" \
    "v_mfma_f32_16x16x32_bf16 v[80:83], a[72:75], v[24:27], 0\n\t" \
    "s_add_u32 m0, s91, 0\n\t" \
    "v_mfma_f32_16x16x32_bf16 v[84:87], a[72:75], v[28:31], 0\n\t" \
    "global_load_lds_dwordx4 v1, s[50:51] offset:0\n\t" \
    "s_waitcnt lgkmcnt(5)\n\t" \
    "v_mfma_f32_16x16x32_bf16 v[72:75], a[4:7], v[32:35], v[72:75]\n\t" \
    "ds_read_b128 v[56:59], v2 offset:4096\n\t" \
    "s_waitcnt lgkmcnt(5)\n\t" \
    "v_mfma_f32_16x16x32_bf16 v[76:79], a[4:7], v[36:39], v[76:79]\n\t" \
    "ds_read_b128 v[60:63], v2 offset:22528\n\t" \
    "v_mfma_f32_16x16x32_bf16 v[80:83], a[76:79], v[32:35], v[80:83]\n\t" \
    "s_add_u32 m0, s91, 960\n\t" \
    "v_mfma_f32_16x16x32_bf16 v[84:87], a[76:79], v[36:39], v[84:87]\n\t" \
    "global_load_lds_dwordx4 v1, s[50:51] offset:64\n\t" \
    "s_waitcnt lgkmcnt(5)\n\t" \
    "v_mfma_f32_16x16x32_bf16 v[72:75], a[8:11], v[40:43], v[72:75]\n\t" \
    "ds_read_b128 v[64:67], v2 offset:5120\n\t" \
    "s_waitcnt lgkmcnt(5)\n\t" \
    "v_mfma_f32_16x16x32_bf16 v[76:79], a[8:11], v[44:47], v[76:79]\n\t" \
    "ds_read_b128 v[68:71], v2 offset:23552\n\t" \
    "v_mfma_f32_16x16x32_bf16 v[80:83], a[80:83], v[40:43], v[80:83]\n\t" \
    "s_add_u32 m0, s91, 1920\n\t" \
    "v_mfma_f32_16x16x32_bf16 v[84:87], a[80:83], v[44:47], v[84:87]\n\t" \
    "global_load_lds_dwordx4 v1, s[50:51] offset:128\n\t" \
    "s_waitcnt lgkmcnt(5)\n\t" \
    "v_mfma_f32_16x16x32_bf16 v[72:75], a[12:15], v[48:51], v[72:75]\n\t" \
    "ds_read_b128 v[24:27], v2 offset:6144\n\t" \
    "s_waitcnt lgkmcnt(5)\n\t" \
    "v_mfma_f32_16x16x32_bf16 v[76:79], a[12:15], v[52:55], v[76:79]\n\t" \
    "ds_read_b128 v[28:31], v2 offset:24576\n\t" \
    "v_mfma_f32_16x16x32_bf16 v[80:83], a[84:87], v[48:51], v[80:83]\n\t" \
    "s_add_u32 m0, s91, 2880\n\t" \
    "v_mfma_f32_16x16x32_bf16 v[84:87], a[84:87], v[52:55], v[84:87]\n\t" \
    "global_load_lds_dwordx4 v1, s[50:51] offset:192\n\t" \
    "s_waitcnt lgkmcnt(5)\n\t" \
    "v_mfma_f32_16x16x32_bf16 v[72:75], a[16:19], v[56:59], v[72:75]\n\t" \
    "ds_read_b128 v[32:35], v2 offset:7168\n\t" \
    "s_waitcnt lgkmcnt(5)\n\t" \
    "v_mfma_f32_16x16x32_bf16 v[76:79], a[16:19], v[60:63], v[76:79]\n\t" \
    "ds_read_b128 v[36:39], v2 offset:25600\n\t" \
    "v_mfma_f32_16x16x32_bf16 v[80:83], a[88:91], v[56:59], v[80:83]\n\t" \
    "s_add_u32 m0, s91, 3840\n\t" \
    "v_mfma_f32_16x16x32_bf16 v[84:87], a[88:91], v[60:63], v[84:87]\n\t" \
    "global_load_lds_dwordx4 v1, s[50:51] offset:256\n\t" \
    "s_waitcnt lgkmcnt(5)\n\t" \
    "v_mfma_f32_16x16x32_bf16 v[72:75], a[20:23], v[64:67], v[72:75]\n\t" \
    "ds_read_b128 v[40:43], v2 offset:8192\n\t" \
    "s_waitcnt lgkmcnt(5)\n\t" \
    "v_mfma_f32_16x16x32_bf16 v[76:79], a[20:23], v[68:71], v[76:79]\n\t" \
    "ds_read_b128 v[44:47], v2 offset:26624\n\t" \
    "v_mfma_f32_16x16x32_bf16 v[80:83], a[92:95], v[64:67], v[80:83]\n\t" \
    "s_add_u32 m0, s91, 4800\n\t" \
    "v_mfma_f32_16x16x32_bf16 v[84:87], a[92:95], v[68:71], v[84:87]\n\t" \
    "global_load_lds_dwordx4 v1, s[50:51] offset:320\n\t" \
    "s_waitcnt lgkmcnt(5)\n\t" \
    "v_mfma_f32_16x16x32_bf16 v[72:75], a[24:27], v[24:27], v[72:75]\n\t" \
    "ds_read_b128 v[48:51], v2 offset:9216\n\t" \
    "s_waitcnt lgkmcnt(5)\n\t" \
    "v_mfma_f32_16x16x32_bf16 v[76:79], a[24:27], v[28:31], v[76:79]\n\t" \
    "ds_read_b128 v[52:55], v2 offset:27648\n\t" \
    "v_mfma_f32_16x16x32_bf16 v[80:83], a[96:99], v[24:27], v[80:83]\n\t" \
    "s_add_u32 m0, s91, 5760\n\t" \
    "v_mfma_f32_16x16x32_bf16 v[84:87], a[96:99], v[28:31], v[84:87]\n\t" \
    "global_load_lds_dwordx4 v1, s[50:51] offset:384\n\t" \
    "s_waitcnt lgkmcnt(5)\n\t" \
    "v_mfma_f32_16x16x32_bf16 v[72:75], a[28:31], v[32:35], v[72:75]\n\t" \
    "ds_read_b128 v[56:59], v2 offset:10240\n\t" \
    "s_waitcnt lgkmcnt(5)\n\t" \
    "v_mfma_f32_16x16x32_bf16 v[76:79], a[28:31], v[36:39], v[76:79]\n\t" \
    "ds_read_b128 v[60:63], v2 offset:28672\n\t" \
    "v_mfma_f32_16x16x32_bf16 v[80:83], a[100:103], v[32:35], v[80:83]\n\t" \
    "s_add_u32 m0, s91, 6720\n\t" \
    "v_mfma_f32_16x16x32_bf16 v[84:87], a[100:103], v[36:39], v[84:87]\n\t" \
    "global_load_lds_dwordx4 v1, s[50:51] offset:448\n\t" \
    "s_waitcnt lgkmcnt(5)\n\t" \
    "v_mfma_f32_16x16x32_bf16 v[72:75], a[32:35], v[40:43], v[72:75]\n\t" \
    "ds_read_b128 v[64:67], v2 offset:11264\n\t" \
    "s_waitcnt lgkmcnt(5)\n\t" \
    "v_mfma_f32_16x16x32_bf16 v[76:79], a[32:35], v[44:47], v[76:79]\n\t" \
    "ds_read_b128 v[68:71], v2 offset:29696\n\t" \
    "v_mfma_f32_16x16x32_bf16 v[80:83], a[104:107], v[40:43], v[80:83]\n\t" \
    "s_add_u32 m0, s91, 7680\n\t" \
    "v_mfma_f32_16x16x32_bf16 v[84:87], a[104:107], v[44:47], v[84:87]\n\t" \
    "global_load_lds_dwordx4 v1, s[50:51] offset:512\n\t" \
    "s_waitcnt lgkmcnt(5)\n\t" \
    "v_mfma_f32_16x16x32_bf16 v[72:75], a[36:39], v[48:51], v[72:75]\n\t" \
    "ds_read_b128 v[24:27], v2 offset:12288\n\t" \
    "s_waitcnt lgkmcnt(5)\n\t" \
    "v_mfma_f32_16x16x32_bf16 v[76:79], a[36:39], v[52:55], v[76:79]\n\t" \
    "ds_read_b128 v[28:31], v2 offset:30720\n\t" \
    "v_mfma_f32_16x16x32_bf16 v[80:83], a[108:111], v[48:51], v[80:83]\n\t" \
    "v_mfma_f32_16x16x32_bf16 v[84:87], a[108:111], v[52:55], v[84:87]\n\t" \
    "s_waitcnt lgkmcnt(5)\n\t" \
    "v_mfma_f32_16x16x32_bf16 v[72:75], a[40:43], v[56:59], v[72:75]\n\t" \
    "ds_read_b128 v[32:35], v2 offset:13312\n\t" \
    "s_waitcnt lgkmcnt(5)\n\t" \
    "v_mfma_f32_16x16x32_bf16 v[76:79], a[40:43], v[60:63], v[76:79]\n\t" \
    "ds_read_b128 v[36:39], v2 offset:31744\n\t" \
    "v_mfma_f32_16x16x32_bf16 v[80:83], a[112:115], v[56:59], v[80:83]\n\t" \
    "v_mfma_f32_16x16x32_bf16 v[84:87], a[112:115], v[60:63], v[84:87]\n\t" \
    "s_waitcnt lgkmcnt(5)\n\t" \
    "v_mfma_f32_16x16x32_bf16 v[72:75], a[44:47], v[64:67], v[72:75]\n\t" \
    "ds_read_b128 v[40:43], v2 offset:14336\n\t" \
    "s_waitcnt lgkmcnt(5)\n\t" \
    "v_mfma_f32_16x16x32_bf16 v[76:79], a[44:47], v[68:71], v[76:79]\n\t" \
    "ds_read_b128 v[44:47], v2 offset:32768\n\t" \
    "v_mfma_f32_16x16x32_bf16 v[80:83], a[116:119], v[64:67], v[80:83]\n\t" \
    "v_mfma_f32_16x16x32_bf16 v[84:87], a[116:119], v[68:71], v[84:87]\n\t" \
    "s_waitcnt lgkmcnt(5)\n\t" \
    "v_mfma_f32_16x16x32_bf16 v[72:75], a[48:51], v[24:27], v[72:75]\n\t" \
    "ds_read_b128 v[48:51], v2 offset:15360\n\t" \
    "s_waitcnt lgkmcnt(5)\n\t" \
    "v_mfma_f32_16x16x32_bf16 v[76:79], a[48:51], v[28:31], v[76:79]\n\t" \
    "ds_read_b128 v[52:55], v2 offset:33792\n\t" \
    "v_mfma_f32_16x16x32_bf16 v[80:83], a[120:123], v[24:27], v[80:83]\n\t" \
    "v_mfma_f32_16x16x32_bf16 v[84:87], a[120:123], v[28:31], v[84:87]\n\t" \
    "s_waitcnt lgkmcnt(5)\n\t" \
    "v_mfma_f32_16x16x32_bf16 v[72:75], a[52:55], v[32:35], v[72:75]\n\t" \
    "ds_read_b128 v[56:59], v2 offset:16384\n\t" \
    "s_waitcnt lgkmcnt(5)\n\t" \
    "v_mfma_f32_16x16x32_bf16 v[76:79], a[52:55], v[36:39], v[76:79]\n\t" \
    "ds_read_b128 v[60:63], v2 offset:34816\n\t" \
    "v_mfma_f32_16x16x32_bf16 v[80:83], a[124:127], v[32:35], v[80:83]\n\t" \
    "v_mfma_f32_16x16x32_bf16 v[84:87], a[124:127], v[36:39], v[84:87]\n\t" \
    "s_waitcnt lgkmcnt(5)\n\t" \
    "v_mfma_f32_16x16x32_bf16 v[72:75], a[56:59], v[40:43], v[72:75]\n\t" \
    "ds_read_b128 v[64:67], v2 offset:17408\n\t" \
    "s_waitcnt lgkmcnt(5)\n\t" \
    "v_mfma_f32_16x16x32_bf16 v[76:79], a[56:59], v[44:47], v[76:79]\n\t" \
    "ds_read_b128 v[68:71], v2 offset:35840\n\t" \
    "v_mfma_f32_16x16x32_bf16 v[80:83], a[128:131], v[40:43], v[80:83]\n\t" \
    "v_mfma_f32_16x16x32_bf16 v[84:87], a[128:131], v[44:47], v[84:87]\n\t" \
    "s_waitcnt lgkmcnt(5)\n\t" \
    "v_mfma_f32_16x16x32_bf16 v[72:75], a[60:63], v[48:51], v[72:75]\n\t" \
    "s_waitcnt lgkmcnt(4)\n\t" \
    "v_mfma_f32_16x16x32_bf16 v[76:79], a[60:63], v[52:55], v[76:79]\n\t" \
    "v_mfma_f32_16x16x32_bf16 v[80:83], a[132:135], v[48:51], v[80:83]\n\t" \
    "v_mfma_f32_16x16x32_bf16 v[84:87], a[132:135], v[52:55], v[84:87]\n\t" \
    "s_waitcnt lgkmcnt(3)\n\t" \
    "v_mfma_f32_16x16x32_bf16 v[72:75], a[64:67], v[56:59], v[72:75]\n\t" \
    "s_waitcnt lgkmcnt(2)\n\t" \
    "v_mfma_f32_16x16x32_bf16 v[76:79], a[64:67], v[60:63], v[76:79]\n\t" \
    "v_mfma_f32_16x16x32_bf16 v[80:83], a[136:139], v[56:59], v[80:83]\n\t" \
    "v_mfma_f32_16x16x32_bf16 v[84:87], a[136:139], v[60:63], v[84:87]\n\t" \
    "s_waitcnt lgkmcnt(1)\n\t" \
    "v_mfma_f32_16x16x32_bf16 v[72:75], a[68:71], v[64:67], v[72:75]\n\t" \
    "s_waitcnt lgkmcnt(0)\n\t" \
    "v_mfma_f32_16x16x32_bf16 v[76:79], a[68:71], v[68:71], v[76:79]\n\t" \
    "v_mfma_f32_16x16x32_bf16 v[80:83], a[140:143], v[64:67], v[80:83]\n\t" \
    "v_mfma_f32_16x16x32_bf16 v[84:87], a[140:143], v[68:71], v[84:87]\n\t" \
    "gv_mid8_%=:\n\t" \
    "v_mov_b32 v2, v17\n\t" \
    "s_nop 2\n\t" \
    "v_cvt_pk_bf16_f32 v104, v72, v73\n\t" \
    "v_cvt_pk_bf16_f32 v105, v74, v75\n\t" \
    "s_nop 0\n\t" \
    "v_cvt_pk_bf16_f32 v106, v80, v81\n\t" \
    "v_cvt_pk_bf16_f32 v107, v82, v83\n\t" \
    "ds_write_b128 v4, v[104:107] offset:0\n\t" \
    "v_cvt_pk_bf16_f32 v108, v76, v77\n\t" \
    "v_cvt_pk_bf16_f32 v109, v78, v79\n\t" \
    "v_cvt_pk_bf16_f32 v110, v84, v85\n\t" \
    "v_cvt_pk_bf16_f32 v111, v86, v87\n\t" \
    "ds_write_b128 v4, v[108:111] offset:1024\n\t" \
    "s_waitcnt vmcnt(18) lgkmcnt(0)\n\t" \
    "s_barrier\n\t" \
    "ds_read_b128 v[164:167], v5 offset:0\n\t" \
    "ds_read_b128 v[168:171], v5 offset:1024\n\t" \
    "ds_read_b128 v[172:175], v5 offset:2048\n\t" \
    "ds_read_b128 v[176:179], v5 offset:3072\n\t" \
    "ds_read_b128 v[180:183], v5 offset:4096\n\t" \
    "ds_read_b128 v[184:187], v5 offset:5120\n\t" \
    "ds_read_b128 v[188:191], v5 offset:6144\n\t" \
    "ds_read_b128 v[192:195], v5 offset:7168\n\t" \
    "s_cmp_lt_u32 s65, 10\n\t" \
    "s_cbranch_scc1 gv_fl9_%=\n\t" \
    "s_cmp_lg_u32 s66, 0\n\t" \
    "s_cbranch_scc1 gv_SF10_%=\n\t" \
    "s_lshl_b32 s69, s67, 7\n\t" \
    "s_add_u32 s69, s69, 6111232\n\t" \
    "s_add_u32 s69, s69, s82\n\t" \
    "s_add_u32 s70, s65, -4\n\t" \
    "v_mov_b32 v156, s69\n\t" \
    "v_mov_b32 v157, s70\n\t" \
    "s_mov_b64 exec, 1\n\t" \
    "global_store_dword v156, v157, s[36:37] sc1\n\t" \
    "s_mov_b64 exec, -1\n\t" \
    "gv_SF10_%=:\n\t" \
    "gv_fl9_%=:\n\t" \
    "v_xor_b32 v4, 0x2000, v4\n\t" \
    "v_xor_b32 v5, 0x2000, v5\n\t" \
    "s_waitcnt lgkmcnt(7)\n\t" \
    "v_mfma_f32_16x16x32_bf16 v[72:75], v[164:167], a[144:147], 0\n\t" \
    "s_waitcnt lgkmcnt(6)\n\t" \
    "v_mfma_f32_16x16x32_bf16 v[76:79], v[168:171], a[144:147], 0\n\t" \
    "s_waitcnt lgkmcnt(5)\n\t" \
    "v_mfma_f32_16x16x32_bf16 v[72:75], v[172:175], a[148:151], v[72:75]\n\t" \
    "s_waitcnt lgkmcnt(4)\n\t" \
    "v_mfma_f32_16x16x32_bf16 v[76:79], v[176:179], a[148:151], v[76:79]\n\t" \
    "s_waitcnt lgkmcnt(3)\n\t" \
    "v_mfma_f32_16x16x32_bf16 v[72:75], v[180:183], a[152:155], v[72:75]\n\t" \
    "s_waitcnt lgkmcnt(2)\n\t" \
    "v_mfma_f32_16x16x32_bf16 v[76:79], v[184:187], a[152:155], v[76:79]\n\t" \
    "s_waitcnt lgkmcnt(1)\n\t" \
    "v_mfma_f32_16x16x32_bf16 v[72:75], v[188:191], a[156:159], v[72:75]\n\t" \
    "s_waitcnt lgkmcnt(0)\n\t" \
    "v_mfma_f32_16x16x32_bf16 v[76:79], v[192:195], a[156:159], v[76:79]\n\t" \
    "v_mfma_f32_16x16x32_bf16 v[80:83], v[164:167], a[160:163], 0\n\t" \
    "v_mfma_f32_16x16x32_bf16 v[84:87], v[168:171], a[160:163], 0\n\t" \
    "v_mfma_f32_16x16x32_bf16 v[80:83], v[172:175], a[164:167], v[80:83]\n\t" \
    "v_mfma_f32_16x16x32_bf16 v[84:87], v[176:179], a[164:167], v[84:87]\n\t" \
    "v_mfma_f32_16x16x32_bf16 v[80:83], v[180:183], a[168:171], v[80:83]\n\t" \
    "v_mfma_f32_16x16x32_bf16 v[84:87], v[184:187], a[168:171], v[84:87]\n\t" \
    "v_cvt_pk_bf16_f32 v104, v72, v73\n\t" \
    "v_mfma_f32_16x16x32_bf16 v[80:83], v[188:191], a[172:175], v[80:83]\n\t" \
    "v_cvt_pk_bf16_f32 v105, v74, v75\n\t" \
    "v_mfma_f32_16x16x32_bf16 v[84:87], v[192:195], a[172:175], v[84:87]\n\t" \
    "v_cvt_pk_bf16_f32 v106, v76, v77\n\t" \
    "v_mfma_f32_16x16x32_bf16 v[88:91], v[164:167], a[176:179], 0\n\t" \
    "v_cvt_pk_bf16_f32 v107, v78, v79\n\t" \
    "v_mfma_f32_16x16x32_bf16 v[92:95], v[168:171], a[176:179], 0\n\t" \
    "v_xor_b32 v18, s40, v8\n\t" \
    "v_mfma_f32_16x16x32_bf16 v[88:91], v[172:175], a[180:183], v[88:91]\n\t" \
    "global_store_dwordx4 v18, v[104:107], s[38:39] sc1\n\t" \
    "v_mfma_f32_16x16x32_bf16 v[92:95], v[176:179], a[180:183], v[92:95]\n\t" \
    "v_mfma_f32_16x16x32_bf16 v[88:91], v[180:183], a[184:187], v[88:91]\n\t" \
    "v_mfma_f32_16x16x32_bf16 v[92:95], v[184:187], a[184:187], v[92:95]\n\t" \
    "v_cvt_pk_bf16_f32 v108, v80, v81\n\t" \
    "v_mfma_f32_16x16x32_bf16 v[88:91], v[188:191], a[188:191], v[88:91]\n\t" \
    "v_cvt_pk_bf16_f32 v109, v82, v83\n\t" \
    "v_mfma_f32_16x16x32_bf16 v[92:95], v[192:195], a[188:191], v[92:95]\n\t" \
    "v_cvt_pk_bf16_f32 v110, v84, v85\n\t" \
    "v_mfma_f32_16x16x32_bf16 v[72:75], v[164:167], a[192:195], 0\n\t" \
    "v_cvt_pk_bf16_f32 v111, v86, v87\n\t" \
    "v_mfma_f32_16x16x32_bf16 v[76:79], v[168:171], a[192:195], 0\n\t" \
    "v_xor_b32 v18, s40, v9\n\t" \
    "v_mfma_f32_16x16x32_bf16 v[72:75], v[172:175], a[196:199], v[72:75]\n\t" \
    "global_store_dwordx4 v18, v[108:111], s[38:39] sc1\n\t" \
    "v_mfma_f32_16x16x32_bf16 v[76:79], v[176:179], a[196:199], v[76:79]\n\t" \
    "v_mfma_f32_16x16x32_bf16 v[72:75], v[180:183], a[200:203], v[72:75]\n\t" \
    "v_mfma_f32_16x16x32_bf16 v[76:79], v[184:187], a[200:203], v[76:79]\n\t" \
    "v_cvt_pk_bf16_f32 v104, v88, v89\n\t" \
    "v_mfma_f32_16x16x32_bf16 v[72:75], v[188:191], a[204:207], v[72:75]\n\t" \
    "v_cvt_pk_bf16_f32 v105, v90, v91\n\t" \
    "v_mfma_f32_16x16x32_bf16 v[76:79], v[192:195], a[204:207], v[76:79]\n\t" \
    "v_cvt_pk_bf16_f32 v106, v92, v93\n\t" \
    "v_mfma_f32_16x16x32_bf16 v[80:83], v[164:167], a[208:211], 0\n\t" \
    "v_cvt_pk_bf16_f32 v107, v94, v95\n\t" \
    "v_mfma_f32_16x16x32_bf16 v[84:87], v[168:171], a[208:211], 0\n\t" \
    "v_xor_b32 v18, s40, v10\n\t" \
    "v_mfma_f32_16x16x32_bf16 v[80:83], v[172:175], a[212:215], v[80:83]\n\t" \
    "global_store_dwordx4 v18, v[104:107], s[38:39] sc1\n\t" \
    "v_mfma_f32_16x16x32_bf16 v[84:87], v[176:179], a[212:215], v[84:87]\n\t" \
    "v_mfma_f32_16x16x32_bf16 v[80:83], v[180:183], a[216:219], v[80:83]\n\t" \
    "v_mfma_f32_16x16x32_bf16 v[84:87], v[184:187], a[216:219], v[84:87]\n\t" \
    "v_cvt_pk_bf16_f32 v108, v72, v73\n\t" \
    "v_mfma_f32_16x16x32_bf16 v[80:83], v[188:191], a[220:223], v[80:83]\n\t" \
    "v_cvt_pk_bf16_f32 v109, v74, v75\n\t" \
    "v_mfma_f32_16x16x32_bf16 v[84:87], v[192:195], a[220:223], v[84:87]\n\t" \
    "v_cvt_pk_bf16_f32 v110, v76, v77\n\t" \
    "v_mfma_f32_16x16x32_bf16 v[88:91], v[164:167], a[224:227], 0\n\t" \
    "v_cvt_pk_bf16_f32 v111, v78, v79\n\t" \
    "v_mfma_f32_16x16x32_bf16 v[92:95], v[168:171], a[224:227], 0\n\t" \
    "v_xor_b32 v18, s40, v11\n\t" \
    "v_mfma_f32_16x16x32_bf16 v[88:91], v[172:175], a[228:231], v[88:91]\n\t" \
    "global_store_dwordx4 v18, v[108:111], s[38:39] sc1\n\t" \
    "v_mfma_f32_16x16x32_bf16 v[92:95], v[176:179], a[228:231], v[92:95]\n\t" \
    "v_mfma_f32_16x16x32_bf16 v[88:91], v[180:183], a[232:235], v[88:91]\n\t" \
    "v_mfma_f32_16x16x32_bf16 v[92:95], v[184:187], a[232:235], v[92:95]\n\t" \
    "v_cvt_pk_bf16_f32 v104, v80, v81\n\t" \
    "v_mfma_f32_16x16x32_bf16 v[88:91], v[188:191], a[236:239], v[88:91]\n\t" \
    "v_cvt_pk_bf16_f32 v105, v82, v83\n\t" \
    "v_mfma_f32_16x16x32_bf16 v[92:95], v[192:195], a[236:239], v[92:95]\n\t" \
    "v_cvt_pk_bf16_f32 v106, v84, v85\n\t" \
    "v_mfma_f32_16x16x32_bf16 v[72:75], v[164:167], a[240:243], 0\n\t" \
    "v_cvt_pk_bf16_f32 v107, v86, v87\n\t" \
    "v_mfma_f32_16x16x32_bf16 v[76:79], v[168:171], a[240:243], 0\n\t" \
    "v_xor_b32 v18, s40, v12\n\t" \
    "v_mfma_f32_16x16x32_bf16 v[72:75], v[172:175], a[244:247], v[72:75]\n\t" \
    "global_store_dwordx4 v18, v[104:107], s[38:39] sc1\n\t" \
    "v_mfma_f32_16x16x32_bf16 v[76:79], v[176:179], a[244:247], v[76:79]\n\t" \
    "v_mfma_f32_16x16x32_bf16 v[72:75], v[180:183], a[248:251], v[72:75]\n\t" \
    "v_mfma_f32_16x16x32_bf16 v[76:79], v[184:187], a[248:251], v[76:79]\n\t" \
    "v_cvt_pk_bf16_f32 v108, v88, v89\n\t" \
    "v_mfma_f32_16x16x32_bf16 v[72:75], v[188:191], a[252:255], v[72:75]\n\t" \
    "v_cvt_pk_bf16_f32 v109, v90, v91\n\t" \
    "v_mfma_f32_16x16x32_bf16 v[76:79], v[192:195], a[252:255], v[76:79]\n\t" \
    "v_cvt_pk_bf16_f32 v110, v92, v93\n\t" \
    "v_mfma_f32_16x16x32_bf16 v[80:83], v[164:167], v[224:227], 0\n\t" \
    "v_cvt_pk_bf16_f32 v111, v94, v95\n\t" \
    "v_mfma_f32_16x16x32_bf16 v[84:87], v[168:171], v[224:227], 0\n\t" \
    "v_xor_b32 v18, s40, v13\n\t" \
    "v_mfma_f32_16x16x32_bf16 v[80:83], v[172:175], v[228:231], v[80:83]\n\t" \
    "global_store_dwordx4 v18, v[108:111], s[38:39] sc1\n\t" \
    "v_mfma_f32_16x16x32_bf16 v[84:87], v[176:179], v[228:231], v[84:87]\n\t" \
    "v_mfma_f32_16x16x32_bf16 v[80:83], v[180:183], v[232:235], v[80:83]\n\t" \
    "v_mfma_f32_16x16x32_bf16 v[84:87], v[184:187], v[232:235], v[84:87]\n\t" \
    "v_cvt_pk_bf16_f32 v104, v72, v73\n\t" \
    "v_mfma_f32_16x16x32_bf16 v[80:83], v[188:191], v[236:239], v[80:83]\n\t" \
    "v_cvt_pk_bf16_f32 v105, v74, v75\n\t" \
    "v_mfma_f32_16x16x32_bf16 v[84:87], v[192:195], v[236:239], v[84:87]\n\t" \
    "v_cvt_pk_bf16_f32 v106, v76, v77\n\t" \
    "s_cmp_ge_u32 s65, 4\n\t" \
    "s_cbranch_scc1 gv_f1st11_%=\n\t" \
    "s_waitcnt vmcnt(6)\n\t" \
    "s_barrier\n\t" \
    "gv_f1st11_%=:\n\t" \
    "v_mfma_f32_16x16x32_bf16 v[88:91], v[164:167], v[240:243], 0\n\t" \
    "v_cvt_pk_bf16_f32 v107, v78, v79\n\t" \
    "ds_read_b128 v[24:27], v17 offset:0\n\t" \
    "v_mfma_f32_16x16x32_bf16 v[92:95], v[168:171], v[240:243], 0\n\t" \
    "v_xor_b32 v18, s40, v14\n\t" \
    "ds_read_b128 v[28:31], v17 offset:18432\n\t" \
    "v_mfma_f32_16x16x32_bf16 v[88:91], v[172:175], v[244:247], v[88:91]\n\t" \
    "global_store_dwordx4 v18, v[104:107], s[38:39] sc1\n\t" \
    "ds_read_b128 v[32:35], v17 offset:1024\n\t" \
    "v_mfma_f32_16x16x32_bf16 v[92:95], v[176:179], v[244:247], v[92:95]\n\t" \
    "ds_read_b128 v[36:39], v17 offset:19456\n\t" \
    "v_mfma_f32_16x16x32_bf16 v[88:91], v[180:183], v[248:251], v[88:91]\n\t" \
    "ds_read_b128 v[40:43], v17 offset:2048\n\t" \
    "v_mfma_f32_16x16x32_bf16 v[92:95], v[184:187], v[248:251], v[92:95]\n\t" \
    "v_cvt_pk_bf16_f32 v108, v80, v81\n\t" \
    "ds_read_b128 v[44:47], v17 offset:20480\n\t" \
    "v_mfma_f32_16x16x32_bf16 v[88:91], v[188:191], v[252:255], v[88:91]\n\t" \
    "v_cvt_pk_bf16_f32 v109, v82, v83\n\t" \
    "v_mfma_f32_16x16x32_bf16 v[92:95], v[192:195], v[252:255], v[92:95]\n\t" \
    "v_cvt_pk_bf16_f32 v110, v84, v85\n\t" \
    "v_cvt_pk_bf16_f32 v111, v86, v87\n\t" \
    "v_xor_b32 v18, s40, v15\n\t" \
    "global_store_dwordx4 v18, v[108:111], s[38:39] sc1\n\t" \
    "s_nop 1\n\t" \
    "v_cvt_pk_bf16_f32 v104, v88, v89\n\t" \
    "v_cvt_pk_bf16_f32 v105, v90, v91\n\t" \
    "v_cvt_pk_bf16_f32 v106, v92, v93\n\t" \
    "v_cvt_pk_bf16_f32 v107, v94, v95\n\t" \
    "v_xor_b32 v18, s40, v16\n\t" \
    "global_store_dwordx4 v18, v[104:107], s[38:39] sc1\n\t" \
    "s_sub_u32 s69, s65, 6\n\t" \
    "s_cmp_lt_u32 s69, 56\n\t" \
    "s_cbranch_scc1 gv_nofast7_%=\n\t" \
    "s_waitcnt vmcnt(0)\n\t" \
    "s_barrier\n\t" \
    "s_cmp_lg_u32 s66, 0\n\t" \
    "s_cbranch_scc1 gv_SF12_%=\n\t" \
    "s_lshl_b32 s69, s67, 7\n\t" \
    "s_add_u32 s69, s69, 6111232\n\t" \
    "s_add_u32 s69, s69, s82\n\t" \
    "s_add_u32 s70, s65, 0\n\t" \
    "v_mov_b32 v156, s69\n\t" \
    "v_mov_b32 v157, s70\n\t" \
    "s_mov_b64 exec, 1\n\t" \
    "global_store_dword v156, v157, s[36:37] sc1\n\t" \
    "s_mov_b64 exec, -1\n\t" \
    "gv_SF12_%=:\n\t" \
    "s_cmp_ge_u32 s65, 2\n\t" \
    "s_cbranch_scc1 gv_nofast7_%=\n\t" \
    "s_add_u32 m0, s91, 0\n\t" \
    "s_nop 0\n\t" \
    "global_load_lds_dwordx4 v1, s[50:51] offset:0\n\t" \
    "s_add_u32 m0, s91, 960\n\t" \
    "s_nop 0\n\t" \
    "global_load_lds_dwordx4 v1, s[50:51] offset:64\n\t" \
    "s_add_u32 m0, s91, 1920\n\t" \
    "s_nop 0\n\t" \
    "global_load_lds_dwordx4 v1, s[50:51] offset:128\n\t" \
    "s_add_u32 m0, s91, 2880\n\t" \
    "s_nop 0\n\t" \
    "global_load_lds_dwordx4 v1, s[50:51] offset:192\n\t" \
    "s_add_u32 m0, s91, 3840\n\t" \
    "s_nop 0\n\t" \
    "global_load_lds_dwordx4 v1, s[50:51] offset:256\n\t" \
    "s_add_u32 m0, s91, 4800\n\t" \
    "s_nop 0\n\t" \
    "global_load_lds_dwordx4 v1, s[50:51] offset:320\n\t" \
    "s_add_u32 m0, s91, 5760\n\t" \
    "s_nop 0\n\t" \
    "global_load_lds_dwordx4 v1, s[50:51] offset:384\n\t" \
    "s_add_u32 m0, s91, 6720\n\t" \
    "s_nop 0\n\t" \
    "global_load_lds_dwordx4 v1, s[50:51] offset:448\n\t" \
    "s_add_u32 m0, s91, 7680\n\t" \
    "s_nop 0\n\t" \
    "global_load_lds_dwordx4 v1, s[50:51] offset:512\n\t" \
    "gv_nofast7_%=:\n\t" \
    "s_add_u32 s65, s65, 2\n\t" \
    "s_cmp_lt_u32 s65, 64\n\t" \
    "s_cbranch_scc1 gv_loop6_%=\n\t" \
    "s_waitcnt vmcnt(0) lgkmcnt(0)\n\t" \
    "s_barrier\n\t" \
    "ea_done_%=:\n\t" \
    "s_waitcnt lgkmcnt(0)\n\t" \
    "ea_last_%=:\n\t" \
    ""
#define ASM_R \
    "s_mov_b64 s[36:37], %0\n\t" \
    "s_mov_b64 s[42:43], %1\n\t" \
    "s_mov_b64 s[44:45], %2\n\t" \
    "s_mov_b32 s66, %3\n\t" \
    "s_mov_b32 s67, %4\n\t" \
    "s_mov_b64 s[50:51], %5\n\t" \
    "s_mov_b32 s82, %7\n\t" \
    "s_load_dwordx8 s[84:91], %6, 0\n\t" \
    "v_mbcnt_lo_u32_b32 v223, -1, 0\n\t" \
    "v_mbcnt_hi_u32_b32 v223, -1, v223\n\t" \
    "s_getpc_b64 s[46:47]\n\t" \
    "rr_pc_%=:\n\t" \
    "s_lshl_b32 s69, s66, 13\n\t" \
    "v_lshl_add_u32 v222, v223, 7, s69\n\t" \
    "v_min_u32 v222, rr_last_%=-rr_pc_%=-4, v222\n\t" \
    "global_load_dword v222, v222, s[46:47]\n\t" \
    "v_lshlrev_b32 v223, 2, v223\n\t" \
    "v_add_u32 v223, s82, v223\n\t" \
    "ds_read_b32 v0, v223 offset:0\n\t" \
    "ds_read_b32 v1, v223 offset:256\n\t" \
    "ds_read_b32 v2, v223 offset:512\n\t" \
    "ds_read_b32 v3, v223 offset:768\n\t" \
    "ds_read_b32 v4, v223 offset:1024\n\t" \
    "ds_read_b32 v5, v223 offset:1280\n\t" \
    "ds_read_b32 v6, v223 offset:1536\n\t" \
    "ds_read_b32 v7, v223 offset:1792\n\t" \
    "ds_read_b32 v8, v223 offset:2048\n\t" \
    "ds_read_b32 v9, v223 offset:2304\n\t" \
    "ds_read_b32 v10, v223 offset:2560\n\t" \
    "ds_read_b32 v11, v223 offset:2816\n\t" \
    "s_waitcnt lgkmcnt(0)\n\t" \
    "ds_read_b32 v12, v223 offset:3072\n\t" \
    "ds_read_b32 v13, v223 offset:3328\n\t" \
    "ds_read_b32 v14, v223 offset:3584\n\t" \
    "ds_read_b32 v15, v223 offset:3840\n\t" \
    "ds_read_b32 v16, v223 offset:4096\n\t" \
    "ds_read_b32 v17, v223 offset:4352\n\t" \
    "ds_read_b32 v18, v223 offset:4608\n\t" \
    "ds_read_b32 v19, v223 offset:4864\n\t" \
    "ds_read_b32 v20, v223 offset:5120\n\t" \
    "ds_read_b32 v21, v223 offset:5376\n\t" \
    "ds_read_b32 v22, v223 offset:5632\n\t" \
    "ds_read_b32 v23, v223 offset:5888\n\t" \
    "s_waitcnt lgkmcnt(0)\n\t" \
    "ds_read_b32 v24, v223 offset:6144\n\t" \
    "ds_read_b32 v25, v223 offset:6400\n\t" \
    "ds_read_b32 v26, v223 offset:6656\n\t" \
    "ds_read_b32 v27, v223 offset:6912\n\t" \
    "ds_read_b32 v28, v223 offset:7168\n\t" \
    "ds_read_b32 v29, v223 offset:7424\n\t" \
    "ds_read_b32 v30, v223 offset:7680\n\t" \
    "ds_read_b32 v31, v223 offset:7936\n\t" \
    "ds_read_b32 v32, v223 offset:8192\n\t" \
    "ds_read_b32 v33, v223 offset:8448\n\t" \
    "ds_read_b32 v34, v223 offset:8704\n\t" \
    "ds_read_b32 v35, v223 offset:8960\n\t" \
    "s_waitcnt lgkmcnt(0)\n\t" \
    "ds_read_b32 v36, v223 offset:9216\n\t" \
    "ds_read_b32 v37, v223 offset:9472\n\t" \
    "ds_read_b32 v38, v223 offset:9728\n\t" \
    "ds_read_b32 v39, v223 offset:9984\n\t" \
    "ds_read_b32 v40, v223 offset:10240\n\t" \
    "ds_read_b32 v41, v223 offset:10496\n\t" \
    "ds_read_b32 v42, v223 offset:10752\n\t" \
    "ds_read_b32 v43, v223 offset:11008\n\t" \
    "ds_read_b32 v44, v223 offset:11264\n\t" \
    "ds_read_b32 v45, v223 offset:11520\n\t" \
    "ds_read_b32 v46, v223 offset:11776\n\t" \
    "ds_read_b32 v164, v223 offset:12032\n\t" \
    "s_waitcnt lgkmcnt(0)\n\t" \
    "ds_read_b32 v165, v223 offset:12288\n\t" \
    "ds_read_b32 v166, v223 offset:12544\n\t" \
    "s_waitcnt lgkmcnt(0)\n\t" \
    "s_mov_b32 s68, 262144\n\t" \
    "s_mov_b32 s72, 0\n\t" \
    "s_mov_b32 s83, -1\n\t" \
    "s_mov_b32 s65, 0\n\t" \
    "v_add_u32 v181, 65536, v2\n\t" \
    "v_add_u32 v182, 131072, v2\n\t" \
    "v_add_u32 v183, 196608, v2\n\t" \
    "s_mov_b32 s77, 3019898880\n\t" \
    "s_mov_b32 s78, 3054198966\n\t" \
    "s_mov_b32 s71, 3\n\t" \
    "rr_E1_%=:\n\t" \
    "s_memtime s[54:55]\n\t" \
    "s_nop 0\n\t" \
    "s_memrealtime s[4:5]\n\t" \
    "s_nop 0\n\t" \
    "s_memrealtime s[6:7]\n\t" \
    "s_nop 0\n\t" \
    "s_memrealtime s[8:9]\n\t" \
    "s_nop 0\n\t" \
    "s_memrealtime s[10:11]\n\t" \
    "s_nop 0\n\t" \
    "s_memrealtime s[12:13]\n\t" \
    "s_nop 0\n\t" \
    "s_memrealtime s[14:15]\n\t" \
    "s_nop 0\n\t" \
    "s_memrealtime s[16:17]\n\t" \
    "s_nop 0\n\t" \
    "s_memrealtime s[18:19]\n\t" \
    "s_nop 0\n\t" \
    "s_memrealtime s[20:21]\n\t" \
    "s_nop 0\n\t" \
    "s_memrealtime s[22:23]\n\t" \
    "s_nop 0\n\t" \
    "s_memrealtime s[24:25]\n\t" \
    "s_nop 0\n\t" \
    "s_memrealtime s[26:27]\n\t" \
    "s_nop 0\n\t" \
    "s_memrealtime s[28:29]\n\t" \
    "s_nop 0\n\t" \
    "s_memtime s[56:57]\n\t" \
    "s_mov_b32 s70, 1\n\t" \
    "s_sub_u32 s71, s71, 1\n\t" \
    "s_cselect_b32 s69, 0, -1\n\t" \
    "s_waitcnt lgkmcnt(0)\n\t" \
    "s_sub_u32 s54, s56, s54\n\t" \
    "s_and_b32 s54, s54, s69\n\t" \
    "s_cmp_gt_u32 s54, 124\n\t" \
    "s_cbranch_scc1 rr_E1_%=\n\t" \
    "s_cmp_eq_u32 s4, s28\n\t" \
    "s_addc_u32 s70, s70, 0\n\t" \
    "s_cmp_eq_u32 s6, s28\n\t" \
    "s_addc_u32 s70, s70, 0\n\t" \
    "s_cmp_eq_u32 s8, s28\n\t" \
    "s_addc_u32 s70, s70, 0\n\t" \
    "s_cmp_eq_u32 s10, s28\n\t" \
    "s_addc_u32 s70, s70, 0\n\t" \
    "s_cmp_eq_u32 s12, s28\n\t" \
    "s_addc_u32 s70, s70, 0\n\t" \
    "s_cmp_eq_u32 s14, s28\n\t" \
    "s_addc_u32 s70, s70, 0\n\t" \
    "s_cmp_eq_u32 s16, s28\n\t" \
    "s_addc_u32 s70, s70, 0\n\t" \
    "s_cmp_eq_u32 s18, s28\n\t" \
    "s_addc_u32 s70, s70, 0\n\t" \
    "s_cmp_eq_u32 s20, s28\n\t" \
    "s_addc_u32 s70, s70, 0\n\t" \
    "s_cmp_eq_u32 s22, s28\n\t" \
    "s_addc_u32 s70, s70, 0\n\t" \
    "s_cmp_eq_u32 s24, s28\n\t" \
    "s_addc_u32 s70, s70, 0\n\t" \
    "s_cmp_eq_u32 s26, s28\n\t" \
    "s_addc_u32 s70, s70, 0\n\t" \
    "s_mov_b32 s75, s73\n\t" \
    "s_mov_b32 s76, s74\n\t" \
    "s_lshl_b32 s70, s70, 3\n\t" \
    "s_sub_u32 s73, s56, s70\n\t" \
    "s_lshl_b32 s74, s28, 4\n\t" \
    "v_mov_b32 v172, 0x1f0000\n\t" \
    "v_mov_b32 v173, 0x3f0000\n\t" \
    "global_load_dword v172, v172, s[36:37]\n\t" \
    "global_load_dword v173, v173, s[36:37]\n\t" \
    "s_mov_b32 s65, 63\n\t" \
    "s_mul_i32 s70, s67, 7\n\t" \
    "s_add_u32 s69, s65, 0\n\t" \
    "s_xor_b32 s69, s69, s70\n\t" \
    "s_and_b32 s69, s69, 63\n\t" \
    "s_lshl_b32 s69, s69, 15\n\t" \
    "s_mul_i32 s70, s67, 2097152\n\t" \
    "s_add_u32 s69, s69, s70\n\t" \
    "s_add_u32 s69, s69, 48234496\n\t" \
    "s_add_u32 s38, s36, s69\n\t" \
    "s_addc_u32 s39, s37, 0\n\t" \
    "v_mov_b32 v174, 0\n\t" \
    "global_load_dword v174, v174, s[38:39]\n\t" \
    "v_mbcnt_lo_u32_b32 v184, -1, 0\n\t" \
    "v_mbcnt_hi_u32_b32 v184, -1, v184\n\t" \
    "v_min_u32 v184, 59, v184\n\t" \
    "v_lshlrev_b32 v184, 6, v184\n\t" \
    "v_add_u32 v184, 6225920, v184\n\t" \
    "v_mov_b32 v185, 6234112\n\t" \
    "global_load_dword v185, v185, s[36:37] sc1\n\t" \
    "global_load_dwordx2 v[186:187], v184, s[36:37] sc1\n\t" \
    "s_waitcnt vmcnt(0)\n\t" \
    "v_sub_u32 v186, v186, v185\n\t" \
    "v_cmp_ne_u32 vcc, 1, v186\n\t" \
    "s_cbranch_vccz rr_Cok4_%=\n\t" \
    "rr_C3_%=:\n\t" \
    "global_load_dwordx2 v[186:187], v184, s[36:37] sc1\n\t" \
    "s_waitcnt vmcnt(0)\n\t" \
    "v_sub_u32 v186, v186, v185\n\t" \
    "v_cmp_ne_u32 vcc, 1, v186\n\t" \
    "s_cbranch_vccz rr_Cok4_%=\n\t" \
    "s_cmp_eq_u32 s68, 0\n\t" \
    "s_cbranch_scc1 rr_F5_%=\n\t" \
    "s_sub_u32 s68, s68, 1\n\t" \
    "s_sleep 4\n\t" \
    "s_branch rr_C3_%=\n\t" \
    "rr_F5_%=:\n\t" \
    "v_mov_b32 v188, 6095104\n\t" \
    "v_mov_b32 v189, 4\n\t" \
    "global_store_dword v188, v189, s[36:37] sc1\n\t" \
    "s_waitcnt vmcnt(0)\n\t" \
    "s_branch rr_Cok4_%=\n\t" \
    "rr_Cok4_%=:\n\t" \
    "v_min_u32_dpp v187, v187, v187 row_shl:6 row_mask:0xf bank_mask:0xf\n\t" \
    "s_nop 0\n\t" \
    "v_readlane_b32 s69, v187, 0\n\t" \
    "v_readlane_b32 s70, v187, 1\n\t" \
    "s_nop 1\n\t" \
    "s_cmp_lt_u32 s70, s69\n\t" \
    "s_cselect_b32 s62, 8192, 0\n\t" \
    "s_cselect_b32 s69, s70, s69\n\t" \
    "s_mov_b32 s71, s69\n\t" \
    "v_readlane_b32 s69, v187, 2\n\t" \
    "v_readlane_b32 s70, v187, 3\n\t" \
    "s_nop 1\n\t" \
    "s_cmp_lt_u32 s70, s69\n\t" \
    "s_cselect_b32 s63, 8192, 0\n\t" \
    "s_cselect_b32 s69, s70, s69\n\t" \
    "s_max_u32 s71, s69, s71\n\t" \
    "v_readlane_b32 s69, v187, 4\n\t" \
    "v_readlane_b32 s70, v187, 5\n\t" \
    "s_nop 1\n\t" \
    "s_cmp_lt_u32 s70, s69\n\t" \
    "s_cselect_b32 s64, 8192, 0\n\t" \
    "s_cselect_b32 s69, s70, s69\n\t" \
    "s_max_u32 s71, s69, s71\n\t" \
    "s_add_u32 s63, s63, 0x200000\n\t" \
    "s_xor_b32 s82, s64, 8192\n\t" \
    "s_cmp_gt_u32 s71, 625\n\t" \
    "s_cselect_b32 s69, 1, 0\n\t" \
    "s_mul_i32 s70, s69, 160\n\t" \
    "s_add_u32 s86, s86, s70\n\t" \
    "s_mul_i32 s70, s69, 536\n\t" \
    "s_add_u32 s87, s87, s70\n\t" \
    "s_mul_i32 s70, s69, 256\n\t" \
    "s_add_u32 s88, s88, s70\n\t" \
    "v_add_u32 v6, s82, v6\n\t" \
    "v_mov_b32 v172, -1\n\t" \
    "v_mov_b32 v173, -1\n\t" \
    "v_mov_b32 v174, -1\n\t" \
    "v_mov_b32 v175, -1\n\t" \
    "v_mov_b32 v170, 0\n\t" \
    "s_mov_b32 s65, s66\n\t" \
    "s_sub_u32 s80, s74, 32000\n\t" \
    "v_mov_b32 v177, -1\n\t" \
    "s_lshl_b32 s69, s66, 2\n\t" \
    "v_add_u32 v178, s69, v9\n\t" \
    "v_xor_b32 v179, 4, v178\n\t" \
    "ds_write_b128 v9, v[172:175]\n\t" \
    "s_waitcnt lgkmcnt(0)\n\t" \
    "s_barrier\n\t" \
    "s_cmp_gt_u32 s65, 61\n\t" \
    "s_cbranch_scc1 rr_arm16_%=\n\t" \
    "s_add_u32 s69, s65, 2\n\t" \
    "s_bitcmp1_b32 s69, 0\n\t" \
    "s_cselect_b32 s70, s63, s62\n\t" \
    "s_bfe_u32 s69, s69, 0x20001\n\t" \
    "s_lshl_b32 s69, s69, 19\n\t" \
    "s_add_u32 s69, s69, s70\n\t" \
    "s_add_u32 s46, s36, s69\n\t" \
    "s_addc_u32 s47, s37, 0\n\t" \
    "global_store_dwordx4 v164, v[172:175], s[46:47] sc1\n\t" \
    "global_store_dwordx4 v165, v[172:175], s[46:47] sc1\n\t" \
    "global_store_dwordx4 v166, v[172:175], s[46:47] sc1\n\t" \
    "rr_arm16_%=:\n\t" \
    "s_waitcnt vmcnt(0)\n\t" \
    "v_mov_b32 v188, s65\n\t" \
    "ds_write_b32 v178, v188 offset:8\n\t" \
    "s_add_u32 s52, s36, s62\n\t" \
    "s_addc_u32 s53, s37, 0\n\t" \
    "v_lshrrev_b32 v187, 9, v0\n\t" \
    "v_lshl_add_u32 v187, v187, 2, v7\n\t" \
    "v_add_u32 v187, 3072, v187\n\t" \
    "v_mov_b32 v189, -1\n\t" \
    "v_mov_b32 v188, -1\n\t" \
    "s_sleep 40\n\t" \
    "s_mov_b32 s71, 3\n\t" \
    "rr_E17_%=:\n\t" \
    "s_memtime s[54:55]\n\t" \
    "s_nop 0\n\t" \
    "s_memrealtime s[4:5]\n\t" \
    "s_nop 0\n\t" \
    "s_memrealtime s[6:7]\n\t" \
    "s_nop 0\n\t" \
    "s_memrealtime s[8:9]\n\t" \
    "s_nop 0\n\t" \
    "s_memrealtime s[10:11]\n\t" \
    "s_nop 0\n\t" \
    "s_memrealtime s[12:13]\n\t" \
    "s_nop 0\n\t" \
    "s_memrealtime s[14:15]\n\t" \
    "s_nop 0\n\t" \
    "s_memrealtime s[16:17]\n\t" \
    "s_nop 0\n\t" \
    "s_memrealtime s[18:19]\n\t" \
    "s_nop 0\n\t" \
    "s_memrealtime s[20:21]\n\t" \
    "s_nop 0\n\t" \
    "s_memrealtime s[22:23]\n\t" \
    "s_nop 0\n\t" \
    "s_memrealtime s[24:25]\n\t" \
    "s_nop 0\n\t" \
    "s_memrealtime s[26:27]\n\t" \
    "s_nop 0\n\t" \
    "s_memrealtime s[28:29]\n\t" \
    "s_nop 0\n\t" \
    "s_memtime s[56:57]\n\t" \
    "s_mov_b32 s70, 1\n\t" \
    "s_sub_u32 s71, s71, 1\n\t" \
    "s_cselect_b32 s69, 0, -1\n\t" \
    "s_waitcnt lgkmcnt(0)\n\t" \
    "s_sub_u32 s54, s56, s54\n\t" \
    "s_and_b32 s54, s54, s69\n\t" \
    "s_cmp_gt_u32 s54, 124\n\t" \
    "s_cbranch_scc1 rr_E17_%=\n\t" \
    "s_cmp_eq_u32 s4, s28\n\t" \
    "s_addc_u32 s70, s70, 0\n\t" \
    "s_cmp_eq_u32 s6, s28\n\t" \
    "s_addc_u32 s70, s70, 0\n\t" \
    "s_cmp_eq_u32 s8, s28\n\t" \
    "s_addc_u32 s70, s70, 0\n\t" \
    "s_cmp_eq_u32 s10, s28\n\t" \
    "s_addc_u32 s70, s70, 0\n\t" \
    "s_cmp_eq_u32 s12, s28\n\t" \
    "s_addc_u32 s70, s70, 0\n\t" \
    "s_cmp_eq_u32 s14, s28\n\t" \
    "s_addc_u32 s70, s70, 0\n\t" \
    "s_cmp_eq_u32 s16, s28\n\t" \
    "s_addc_u32 s70, s70, 0\n\t" \
    "s_cmp_eq_u32 s18, s28\n\t" \
    "s_addc_u32 s70, s70, 0\n\t" \
    "s_cmp_eq_u32 s20, s28\n\t" \
    "s_addc_u32 s70, s70, 0\n\t" \
    "s_cmp_eq_u32 s22, s28\n\t" \
    "s_addc_u32 s70, s70, 0\n\t" \
    "s_cmp_eq_u32 s24, s28\n\t" \
    "s_addc_u32 s70, s70, 0\n\t" \
    "s_cmp_eq_u32 s26, s28\n\t" \
    "s_addc_u32 s70, s70, 0\n\t" \
    "s_mov_b32 s75, s73\n\t" \
    "s_mov_b32 s76, s74\n\t" \
    "s_lshl_b32 s70, s70, 3\n\t" \
    "s_sub_u32 s73, s56, s70\n\t" \
    "s_lshl_b32 s74, s28, 4\n\t" \
    "s_sub_u32 s69, s73, s75\n\t" \
    "s_sub_u32 s70, s74, s76\n\t" \
    "s_sub_u32 s71, s70, 1600\n\t" \
    "s_cmp_lt_u32 s71, 400000\n\t" \
    "s_cbranch_scc0 rr_K19_%=\n\t" \
    "v_cvt_f32_u32 v190, s69\n\t" \
    "v_cvt_f32_u32 v191, s70\n\t" \
    "v_rcp_f32 v192, v190\n\t" \
    "v_rcp_f32 v193, v191\n\t" \
    "v_mul_f32 v192, v192, v191\n\t" \
    "v_mul_f32 v193, v193, v190\n\t" \
    "v_mul_f32 v192, 0x4f800000, v192\n\t" \
    "v_mul_f32 v193, 0x4f000000, v193\n\t" \
    "v_cvt_u32_f32 v192, v192\n\t" \
    "v_cvt_u32_f32 v193, v193\n\t" \
    "v_readfirstlane_b32 s69, v192\n\t" \
    "v_readfirstlane_b32 s70, v193\n\t" \
    "s_nop 1\n\t" \
    "s_mov_b32 s78, s69\n\t" \
    "s_mov_b32 s77, s70\n\t" \
    "rr_K19_%=:\n\t" \
    "s_add_u32 s69, s65, 0\n\t" \
    "s_bitcmp1_b32 s69, 0\n\t" \
    "s_cselect_b32 s70, s63, s62\n\t" \
    "s_bfe_u32 s69, s69, 0x20001\n\t" \
    "s_lshl_b32 s69, s69, 19\n\t" \
    "s_add_u32 s69, s69, s70\n\t" \
    "s_add_u32 s46, s36, s69\n\t" \
    "s_addc_u32 s47, s37, 0\n\t" \
    "s_add_u32 s69, s65, 1\n\t" \
    "s_bitcmp1_b32 s69, 0\n\t" \
    "s_cselect_b32 s70, s63, s62\n\t" \
    "s_bfe_u32 s69, s69, 0x20001\n\t" \
    "s_lshl_b32 s69, s69, 19\n\t" \
    "s_add_u32 s69, s69, s70\n\t" \
    "s_add_u32 s48, s36, s69\n\t" \
    "s_addc_u32 s49, s37, 0\n\t" \
    "s_mul_i32 s70, s67, 7\n\t" \
    "s_add_u32 s69, s65, 0\n\t" \
    "s_xor_b32 s69, s69, s70\n\t" \
    "s_and_b32 s69, s69, 63\n\t" \
    "s_lshl_b32 s69, s69, 15\n\t" \
    "s_mul_i32 s70, s67, 2097152\n\t" \
    "s_add_u32 s69, s69, s70\n\t" \
    "s_add_u32 s69, s69, 48234496\n\t" \
    "s_add_u32 s38, s36, s69\n\t" \
    "s_addc_u32 s39, s37, 0\n\t" \
    "s_add_u32 s69, s65, 0\n\t" \
    "s_mul_i32 s70, s69, 171\n\t" \
    "s_lshr_b32 s70, s70, 9\n\t" \
    "s_mul_i32 s70, s70, 3\n\t" \
    "s_sub_u32 s69, s69, s70\n\t" \
    "s_mul_i32 s69, s69, 36\n\t" \
    "s_add_u32 s69, s69, s67\n\t" \
    "s_lshl_b32 s69, s69, 14\n\t" \
    "s_add_u32 s69, s69, s64\n\t" \
    "s_add_u32 s69, s69, 4194304\n\t" \
    "s_add_u32 s40, s36, s69\n\t" \
    "s_addc_u32 s41, s37, 0\n\t" \
    "s_mov_b32 s91, 0\n\t" \
    "s_mov_b32 s96, 0x55555555\n\t" \
    "s_mov_b32 s58, 0\n\t" \
    "s_mov_b32 s59, -1\n\t" \
    "v_mov_b32 v160, 0\n\t" \
    "v_mov_b32 v161, 0\n\t" \
    "v_mov_b32 v162, 0\n\t" \
    "v_mov_b32 v163, 0\n\t" \
    "rr_pg20_%=:\n\t" \
    "v_cmp_eq_u32 vcc, -1, v189\n\t" \
    "s_cbranch_vccz rr_ph24_%=\n\t" \
    "s_sleep 10\n\t" \
    "global_load_dword v189, v187, s[52:53] sc1\n\t" \
    "s_mov_b32 s71, 3\n\t" \
    "rr_E25_%=:\n\t" \
    "s_memtime s[54:55]\n\t" \
    "s_nop 0\n\t" \
    "s_memrealtime s[4:5]\n\t" \
    "s_nop 0\n\t" \
    "s_memrealtime s[6:7]\n\t" \
    "s_nop 0\n\t" \
    "s_memrealtime s[8:9]\n\t" \
    "s_nop 0\n\t" \
    "s_memrealtime s[10:11]\n\t" \
    "s_nop 0\n\t" \
    "s_memrealtime s[12:13]\n\t" \
    "s_nop 0\n\t" \
    "s_memrealtime s[14:15]\n\t" \
    "s_nop 0\n\t" \
    "s_memrealtime s[16:17]\n\t" \
    "s_nop 0\n\t" \
    "s_memrealtime s[18:19]\n\t" \
    "s_nop 0\n\t" \
    "s_memrealtime s[20:21]\n\t" \
    "s_nop 0\n\t" \
    "s_memrealtime s[22:23]\n\t" \
    "s_nop 0\n\t" \
    "s_memrealtime s[24:25]\n\t" \
    "s_nop 0\n\t" \
    "s_memrealtime s[26:27]\n\t" \
    "s_nop 0\n\t" \
    "s_memrealtime s[28:29]\n\t" \
    "s_nop 0\n\t" \
    "s_memtime s[56:57]\n\t" \
    "s_mov_b32 s70, 1\n\t" \
    "s_sub_u32 s71, s71, 1\n\t" \
    "s_cselect_b32 s69, 0, -1\n\t" \
    "s_waitcnt lgkmcnt(0)\n\t" \
    "s_sub_u32 s54, s56, s54\n\t" \
    "s_and_b32 s54, s54, s69\n\t" \
    "s_cmp_gt_u32 s54, 124\n\t" \
    "s_cbranch_scc1 rr_E25_%=\n\t" \
    "s_cmp_eq_u32 s4, s28\n\t" \
    "s_addc_u32 s70, s70, 0\n\t" \
    "s_cmp_eq_u32 s6, s28\n\t" \
    "s_addc_u32 s70, s70, 0\n\t" \
    "s_cmp_eq_u32 s8, s28\n\t" \
    "s_addc_u32 s70, s70, 0\n\t" \
    "s_cmp_eq_u32 s10, s28\n\t" \
    "s_addc_u32 s70, s70, 0\n\t" \
    "s_cmp_eq_u32 s12, s28\n\t" \
    "s_addc_u32 s70, s70, 0\n\t" \
    "s_cmp_eq_u32 s14, s28\n\t" \
    "s_addc_u32 s70, s70, 0\n\t" \
    "s_cmp_eq_u32 s16, s28\n\t" \
    "s_addc_u32 s70, s70, 0\n\t" \
    "s_cmp_eq_u32 s18, s28\n\t" \
    "s_addc_u32 s70, s70, 0\n\t" \
    "s_cmp_eq_u32 s20, s28\n\t" \
    "s_addc_u32 s70, s70, 0\n\t" \
    "s_cmp_eq_u32 s22, s28\n\t" \
    "s_addc_u32 s70, s70, 0\n\t" \
    "s_cmp_eq_u32 s24, s28\n\t" \
    "s_addc_u32 s70, s70, 0\n\t" \
    "s_cmp_eq_u32 s26, s28\n\t" \
    "s_addc_u32 s70, s70, 0\n\t" \
    "s_mov_b32 s75, s73\n\t" \
    "s_mov_b32 s76, s74\n\t" \
    "s_lshl_b32 s70, s70, 3\n\t" \
    "s_sub_u32 s73, s56, s70\n\t" \
    "s_lshl_b32 s74, s28, 4\n\t" \
    "s_waitcnt vmcnt(0)\n\t" \
    "rr_ph24_%=:\n\t" \
    "v_cmp_ne_u32 s[34:35], -1, v189\n\t" \
    "s_memtime s[56:57]\n\t" \
    "s_waitcnt lgkmcnt(0)\n\t" \
    "s_sub_u32 s69, s56, s73\n\t" \
    "s_mul_hi_u32 s69, s69, s78\n\t" \
    "s_add_u32 s69, s69, s74\n\t" \
    "s_add_u32 s69, s69, 1280\n\t" \
    "v_sub_u32 v184, s69, v189\n\t" \
    "v_cmp_le_i32 vcc, 0, v184\n\t" \
    "s_nop 1\n\t" \
    "s_and_b64 s[54:55], vcc, s[34:35]\n\t" \
    "s_and_b64 s[54:55], s[54:55], s[58:59]\n\t" \
    "s_cbranch_scc0 rr_pn21_%=\n\t" \
    "s_mov_b64 exec, s[54:55]\n\t" \
    "global_load_dword v177, v6, s[36:37] sc1\n\t" \
    "s_mov_b64 exec, -1\n\t" \
    "s_waitcnt vmcnt(0)\n\t" \
    "v_cmp_le_i32 vcc, s65, v177\n\t" \
    "s_nop 1\n\t" \
    "s_andn2_b32 s59, s59, vcc_hi\n\t" \
    "s_lshr_b32 s69, vcc_hi, 1\n\t" \
    "s_and_b32 s69, s69, vcc_hi\n\t" \
    "s_and_b32 s54, s69, s96\n\t" \
    "s_andn2_b32 s96, s96, s54\n\t" \
    "s_mov_b32 s55, s54\n\t" \
    "s_lshr_b64 s[54:55], s[54:55], s50\n\t" \
    "s_bitcmp1_b32 s54, 0\n\t" \
    "s_cbranch_scc0 rr_pk27_%=\n\t" \
    "global_load_dwordx4 a[0:3], v26, s[38:39] offset:0 sc1\n\t" \
    "global_load_dwordx4 a[4:7], v26, s[38:39] offset:1024 sc1\n\t" \
    "rr_pk27_%=:\n\t" \
    "s_bitcmp1_b32 s54, 2\n\t" \
    "s_cbranch_scc0 rr_pk28_%=\n\t" \
    "global_load_dwordx4 a[8:11], v27, s[38:39] offset:0 sc1\n\t" \
    "global_load_dwordx4 a[12:15], v27, s[38:39] offset:1024 sc1\n\t" \
    "rr_pk28_%=:\n\t" \
    "s_bitcmp1_b32 s54, 4\n\t" \
    "s_cbranch_scc0 rr_pk29_%=\n\t" \
    "global_load_dwordx4 a[16:19], v28, s[38:39] offset:0 sc1\n\t" \
    "global_load_dwordx4 a[20:23], v28, s[38:39] offset:1024 sc1\n\t" \
    "rr_pk29_%=:\n\t" \
    "s_bitcmp1_b32 s54, 6\n\t" \
    "s_cbranch_scc0 rr_pk30_%=\n\t" \
    "global_load_dwordx4 a[24:27], v29, s[38:39] offset:0 sc1\n\t" \
    "global_load_dwordx4 a[28:31], v29, s[38:39] offset:1024 sc1\n\t" \
    "rr_pk30_%=:\n\t" \
    "s_bitcmp1_b32 s54, 8\n\t" \
    "s_cbranch_scc0 rr_pk31_%=\n\t" \
    "global_load_dwordx4 a[32:35], v30, s[38:39] offset:0 sc1\n\t" \
    "global_load_dwordx4 a[36:39], v30, s[38:39] offset:1024 sc1\n\t" \
    "rr_pk31_%=:\n\t" \
    "s_bitcmp1_b32 s54, 10\n\t" \
    "s_cbranch_scc0 rr_pk32_%=\n\t" \
    "global_load_dwordx4 a[40:43], v31, s[38:39] offset:0 sc1\n\t" \
    "global_load_dwordx4 a[44:47], v31, s[38:39] offset:1024 sc1\n\t" \
    "rr_pk32_%=:\n\t" \
    "s_bitcmp1_b32 s54, 12\n\t" \
    "s_cbranch_scc0 rr_pk33_%=\n\t" \
    "global_load_dwordx4 a[48:51], v32, s[38:39] offset:0 sc1\n\t" \
    "global_load_dwordx4 a[52:55], v32, s[38:39] offset:1024 sc1\n\t" \
    "rr_pk33_%=:\n\t" \
    "s_bitcmp1_b32 s54, 14\n\t" \
    "s_cbranch_scc0 rr_pk34_%=\n\t" \
    "global_load_dwordx4 a[56:59], v33, s[38:39] offset:0 sc1\n\t" \
    "global_load_dwordx4 a[60:63], v33, s[38:39] offset:1024 sc1\n\t" \
    "rr_pk34_%=:\n\t" \
    "s_bitcmp1_b32 s54, 16\n\t" \
    "s_cbranch_scc0 rr_pk35_%=\n\t" \
    "global_load_dwordx4 a[64:67], v34, s[38:39] offset:0 sc1\n\t" \
    "global_load_dwordx4 a[68:71], v34, s[38:39] offset:1024 sc1\n\t" \
    "rr_pk35_%=:\n\t" \
    "s_bitcmp1_b32 s54, 18\n\t" \
    "s_cbranch_scc0 rr_pk36_%=\n\t" \
    "global_load_dwordx4 a[72:75], v35, s[38:39] offset:0 sc1\n\t" \
    "global_load_dwordx4 a[76:79], v35, s[38:39] offset:1024 sc1\n\t" \
    "rr_pk36_%=:\n\t" \
    "s_bitcmp1_b32 s54, 20\n\t" \
    "s_cbranch_scc0 rr_pk37_%=\n\t" \
    "global_load_dwordx4 a[80:83], v36, s[38:39] offset:0 sc1\n\t" \
    "global_load_dwordx4 a[84:87], v36, s[38:39] offset:1024 sc1\n\t" \
    "rr_pk37_%=:\n\t" \
    "s_bitcmp1_b32 s54, 22\n\t" \
    "s_cbranch_scc0 rr_pk38_%=\n\t" \
    "global_load_dwordx4 a[88:91], v37, s[38:39] offset:0 sc1\n\t" \
    "global_load_dwordx4 a[92:95], v37, s[38:39] offset:1024 sc1\n\t" \
    "rr_pk38_%=:\n\t" \
    "s_bitcmp1_b32 s54, 24\n\t" \
    "s_cbranch_scc0 rr_pk39_%=\n\t" \
    "global_load_dwordx4 a[96:99], v38, s[38:39] offset:0 sc1\n\t" \
    "global_load_dwordx4 a[100:103], v38, s[38:39] offset:1024 sc1\n\t" \
    "rr_pk39_%=:\n\t" \
    "s_bitcmp1_b32 s54, 26\n\t" \
    "s_cbranch_scc0 rr_pk40_%=\n\t" \
    "global_load_dwordx4 a[104:107], v39, s[38:39] offset:0 sc1\n\t" \
    "global_load_dwordx4 a[108:111], v39, s[38:39] offset:1024 sc1\n\t" \
    "rr_pk40_%=:\n\t" \
    "s_bitcmp1_b32 s54, 28\n\t" \
    "s_cbranch_scc0 rr_pk41_%=\n\t" \
    "global_load_dwordx4 a[112:115], v40, s[38:39] offset:0 sc1\n\t" \
    "global_load_dwordx4 a[116:119], v40, s[38:39] offset:1024 sc1\n\t" \
    "rr_pk41_%=:\n\t" \
    "s_bitcmp1_b32 s54, 30\n\t" \
    "s_cbranch_scc0 rr_pk42_%=\n\t" \
    "global_load_dwordx4 a[120:123], v41, s[38:39] offset:0 sc1\n\t" \
    "global_load_dwordx4 a[124:127], v41, s[38:39] offset:1024 sc1\n\t" \
    "rr_pk42_%=:\n\t" \
    "rr_pn21_%=:\n\t" \
    "s_orn2_b32 s69, s96, s34\n\t" \
    "s_cbranch_scc0 rr_pe22_%=\n\t" \
    "s_cmp_eq_u32 s68, 0\n\t" \
    "s_cbranch_scc1 rr_F43_%=\n\t" \
    "s_sub_u32 s68, s68, 1\n\t" \
    "s_branch rr_pg20_%=\n\t" \
    "rr_F43_%=:\n\t" \
    "v_mov_b32 v184, 6095104\n\t" \
    "v_mov_b32 v185, 10\n\t" \
    "global_store_dword v184, v185, s[36:37] sc1\n\t" \
    "s_waitcnt vmcnt(0)\n\t" \
    "s_branch rr_pe22_%=\n\t" \
    "rr_pe22_%=:\n\t" \
    "s_mov_b32 s83, s65\n\t" \
    "s_cmp_lg_u32 s65, 0\n\t" \
    "s_cbranch_scc1 rr_sl23_%=\n\t" \
    "s_waitcnt vmcnt(0)\n\t" \
    "v_mov_b32 v184, v189\n\t" \
    "v_subrev_u32 v184, s80, v184\n\t" \
    "s_nop 1\n\t" \
    "v_max_i32_dpp v184, v184, v184 row_ror:1 row_mask:0xf bank_mask:0xf\n\t" \
    "s_nop 1\n\t" \
    "v_max_i32_dpp v184, v184, v184 row_ror:2 row_mask:0xf bank_mask:0xf\n\t" \
    "s_nop 1\n\t" \
    "v_max_i32_dpp v184, v184, v184 row_ror:4 row_mask:0xf bank_mask:0xf\n\t" \
    "s_nop 1\n\t" \
    "v_max_i32_dpp v184, v184, v184 row_ror:8 row_mask:0xf bank_mask:0xf\n\t" \
    "s_nop 0\n\t" \
    "v_readlane_b32 s69, v184, 0\n\t" \
    "v_readlane_b32 s71, v184, 16\n\t" \
    "s_nop 1\n\t" \
    "s_max_i32 s69, s69, s71\n\t" \
    "s_add_u32 s81, s69, s80\n\t" \
    "s_memtime s[56:57]\n\t" \
    "s_waitcnt lgkmcnt(0)\n\t" \
    "s_sub_u32 s69, s56, s73\n\t" \
    "s_mul_hi_u32 s69, s69, s78\n\t" \
    "s_add_u32 s69, s69, s74\n\t" \
    "s_add_u32 s69, s69, 480\n\t" \
    "s_sub_u32 s69, s69, s81\n\t" \
    "s_max_i32 s69, s69, 0\n\t" \
    "s_add_u32 s69, s69, s81\n\t" \
    "v_and_b32_e64 v184, -2, s69\n\t" \
    "global_store_dword v3, v184, s[46:47] sc1\n\t" \
    "s_branch rr_h044_%=\n\t" \
    "rr_pf13_%=:\n\t" \
    "v_min_i32_dpp v177, v177, v177 row_ror:1 row_mask:0xf bank_mask:0xf\n\t" \
    "s_nop 1\n\t" \
    "v_min_i32_dpp v177, v177, v177 row_ror:2 row_mask:0xf bank_mask:0xf\n\t" \
    "s_nop 1\n\t" \
    "v_min_i32_dpp v177, v177, v177 row_ror:4 row_mask:0xf bank_mask:0xf\n\t" \
    "s_nop 1\n\t" \
    "v_min_i32_dpp v177, v177, v177 row_ror:8 row_mask:0xf bank_mask:0xf\n\t" \
    "s_nop 0\n\t" \
    "v_readlane_b32 s69, v177, 0\n\t" \
    "v_readlane_b32 s70, v177, 16\n\t" \
    "s_nop 1\n\t" \
    "s_min_i32 s69, s69, s70\n\t" \
    "s_max_i32 s83, s83, s69\n\t" \
    "s_mov_b32 s91, 0\n\t" \
    "s_cmp_gt_u32 s65, 62\n\t" \
    "s_cselect_b32 s83, -1, s83\n\t" \
    "s_cmp_ge_i32 s83, s65\n\t" \
    "s_cbranch_scc1 rr_npf14_%=\n\t" \
    "s_mul_hi_u32 s69, s85, s78\n\t" \
    "s_add_u32 s79, s81, s69\n\t" \
    "s_add_u32 s79, s79, s87\n\t" \
    "s_cmp_ge_u32 s65, 36\n\t" \
    "s_cselect_b32 s69, 28, 0\n\t" \
    "s_sub_u32 s79, s79, s69\n\t" \
    "s_add_u32 s80, s79, s88\n\t" \
    "s_add_u32 s79, s79, 320\n\t" \
    "s_sub_u32 s70, s79, s74\n\t" \
    "s_max_i32 s70, s70, 0\n\t" \
    "s_min_i32 s70, s70, 16000\n\t" \
    "s_lshl_b32 s70, s70, 1\n\t" \
    "s_mul_hi_u32 s70, s70, s77\n\t" \
    "s_add_u32 s70, s70, s73\n\t" \
    "rr_W46_%=:\n\t" \
    "s_memtime s[56:57]\n\t" \
    "s_waitcnt lgkmcnt(0)\n\t" \
    "s_sub_u32 s69, s70, s56\n\t" \
    "s_cmp_gt_i32 s69, 24\n\t" \
    "s_cbranch_scc1 rr_W46_%=\n\t" \
    "s_cmp_gt_u32 s65, 61\n\t" \
    "s_cbranch_scc1 rr_arm47_%=\n\t" \
    "s_add_u32 s69, s65, 2\n\t" \
    "s_bitcmp1_b32 s69, 0\n\t" \
    "s_cselect_b32 s70, s63, s62\n\t" \
    "s_bfe_u32 s69, s69, 0x20001\n\t" \
    "s_lshl_b32 s69, s69, 19\n\t" \
    "s_add_u32 s69, s69, s70\n\t" \
    "s_add_u32 s46, s36, s69\n\t" \
    "s_addc_u32 s47, s37, 0\n\t" \
    "global_store_dwordx4 v164, v[172:175], s[46:47] sc1\n\t" \
    "global_store_dwordx4 v165, v[172:175], s[46:47] sc1\n\t" \
    "global_store_dwordx4 v166, v[172:175], s[46:47] sc1\n\t" \
    "rr_arm47_%=:\n\t" \
    "s_waitcnt vmcnt(0)\n\t" \
    "v_mov_b32 v188, s65\n\t" \
    "ds_write_b32 v178, v188 offset:8\n\t" \
    "s_cmp_gt_u32 s65, 62\n\t" \
    "s_cbranch_scc1 rr_end8_%=\n\t" \
    "s_mov_b32 s91, 1\n\t" \
    "rr_WF48_%=:\n\t" \
    "s_cmp_ge_i32 s83, s65\n\t" \
    "s_cbranch_scc1 rr_WFok49_%=\n\t" \
    "global_load_dword v184, v6, s[36:37] sc1\n\t" \
    "s_waitcnt vmcnt(0)\n\t" \
    "v_min_i32_dpp v184, v184, v184 row_ror:1 row_mask:0xf bank_mask:0xf\n\t" \
    "s_nop 1\n\t" \
    "v_min_i32_dpp v184, v184, v184 row_ror:2 row_mask:0xf bank_mask:0xf\n\t" \
    "s_nop 1\n\t" \
    "v_min_i32_dpp v184, v184, v184 row_ror:4 row_mask:0xf bank_mask:0xf\n\t" \
    "s_nop 1\n\t" \
    "v_min_i32_dpp v184, v184, v184 row_ror:8 row_mask:0xf bank_mask:0xf\n\t" \
    "s_nop 0\n\t" \
    "v_readlane_b32 s69, v184, 0\n\t" \
    "v_readlane_b32 s70, v184, 16\n\t" \
    "s_nop 1\n\t" \
    "s_min_i32 s83, s69, s70\n\t" \
    "s_cmp_ge_i32 s83, s65\n\t" \
    "s_cbranch_scc1 rr_WFok49_%=\n\t" \
    "s_cmp_eq_u32 s68, 0\n\t" \
    "s_cbranch_scc1 rr_F50_%=\n\t" \
    "s_sub_u32 s68, s68, 1\n\t" \
    "s_sleep 2\n\t" \
    "s_branch rr_WF48_%=\n\t" \
    "rr_F50_%=:\n\t" \
    "v_mov_b32 v184, 6095104\n\t" \
    "v_mov_b32 v185, 10\n\t" \
    "global_store_dword v184, v185, s[36:37] sc1\n\t" \
    "s_waitcnt vmcnt(0)\n\t" \
    "s_branch rr_WFok49_%=\n\t" \
    "rr_WFok49_%=:\n\t" \
    "rr_npf14_%=:\n\t" \
    "s_mul_i32 s70, s67, 7\n\t" \
    "s_add_u32 s69, s65, 0\n\t" \
    "s_xor_b32 s69, s69, s70\n\t" \
    "s_and_b32 s69, s69, 63\n\t" \
    "s_lshl_b32 s69, s69, 15\n\t" \
    "s_mul_i32 s70, s67, 2097152\n\t" \
    "s_add_u32 s69, s69, s70\n\t" \
    "s_add_u32 s69, s69, 48234496\n\t" \
    "s_add_u32 s38, s36, s69\n\t" \
    "s_addc_u32 s39, s37, 0\n\t" \
    "s_mul_hi_u32 s69, s85, s78\n\t" \
    "s_add_u32 s79, s81, s69\n\t" \
    "s_add_u32 s79, s79, s87\n\t" \
    "s_cmp_ge_u32 s65, 36\n\t" \
    "s_cselect_b32 s69, 28, 0\n\t" \
    "s_sub_u32 s79, s79, s69\n\t" \
    "s_add_u32 s80, s79, s88\n\t" \
    "s_add_u32 s79, s79, -96\n\t" \
    "s_sub_u32 s70, s79, s74\n\t" \
    "s_max_i32 s70, s70, 0\n\t" \
    "s_min_i32 s70, s70, 16000\n\t" \
    "s_lshl_b32 s70, s70, 1\n\t" \
    "s_mul_hi_u32 s70, s70, s77\n\t" \
    "s_add_u32 s70, s70, s73\n\t" \
    "rr_W51_%=:\n\t" \
    "s_memtime s[56:57]\n\t" \
    "s_waitcnt lgkmcnt(0)\n\t" \
    "s_sub_u32 s69, s70, s56\n\t" \
    "s_cmp_gt_i32 s69, 24\n\t" \
    "s_cbranch_scc1 rr_W51_%=\n\t" \
    "global_load_dwordx4 a[0:3], v26, s[38:39] offset:0 sc1\n\t" \
    "global_load_dwordx4 a[4:7], v26, s[38:39] offset:1024 sc1\n\t" \
    "global_load_dwordx4 a[8:11], v27, s[38:39] offset:0 sc1\n\t" \
    "global_load_dwordx4 a[12:15], v27, s[38:39] offset:1024 sc1\n\t" \
    "global_load_dwordx4 a[16:19], v28, s[38:39] offset:0 sc1\n\t" \
    "global_load_dwordx4 a[20:23], v28, s[38:39] offset:1024 sc1\n\t" \
    "global_load_dwordx4 a[24:27], v29, s[38:39] offset:0 sc1\n\t" \
    "global_load_dwordx4 a[28:31], v29, s[38:39] offset:1024 sc1\n\t" \
    "global_load_dwordx4 a[32:35], v30, s[38:39] offset:0 sc1\n\t" \
    "global_load_dwordx4 a[36:39], v30, s[38:39] offset:1024 sc1\n\t" \
    "global_load_dwordx4 a[40:43], v31, s[38:39] offset:0 sc1\n\t" \
    "global_load_dwordx4 a[44:47], v31, s[38:39] offset:1024 sc1\n\t" \
    "global_load_dwordx4 a[48:51], v32, s[38:39] offset:0 sc1\n\t" \
    "global_load_dwordx4 a[52:55], v32, s[38:39] offset:1024 sc1\n\t" \
    "global_load_dwordx4 a[56:59], v33, s[38:39] offset:0 sc1\n\t" \
    "global_load_dwordx4 a[60:63], v33, s[38:39] offset:1024 sc1\n\t" \
    "global_load_dwordx4 a[64:67], v34, s[38:39] offset:0 sc1\n\t" \
    "global_load_dwordx4 a[68:71], v34, s[38:39] offset:1024 sc1\n\t" \
    "global_load_dwordx4 a[72:75], v35, s[38:39] offset:0 sc1\n\t" \
    "global_load_dwordx4 a[76:79], v35, s[38:39] offset:1024 sc1\n\t" \
    "global_load_dwordx4 a[80:83], v36, s[38:39] offset:0 sc1\n\t" \
    "global_load_dwordx4 a[84:87], v36, s[38:39] offset:1024 sc1\n\t" \
    "global_load_dwordx4 a[88:91], v37, s[38:39] offset:0 sc1\n\t" \
    "global_load_dwordx4 a[92:95], v37, s[38:39] offset:1024 sc1\n\t" \
    "global_load_dwordx4 a[96:99], v38, s[38:39] offset:0 sc1\n\t" \
    "global_load_dwordx4 a[100:103], v38, s[38:39] offset:1024 sc1\n\t" \
    "global_load_dwordx4 a[104:107], v39, s[38:39] offset:0 sc1\n\t" \
    "global_load_dwordx4 a[108:111], v39, s[38:39] offset:1024 sc1\n\t" \
    "global_load_dwordx4 a[112:115], v40, s[38:39] offset:0 sc1\n\t" \
    "global_load_dwordx4 a[116:119], v40, s[38:39] offset:1024 sc1\n\t" \
    "global_load_dwordx4 a[120:123], v41, s[38:39] offset:0 sc1\n\t" \
    "global_load_dwordx4 a[124:127], v41, s[38:39] offset:1024 sc1\n\t" \
    "s_add_u32 s69, s65, 4\n\t" \
    "s_min_i32 s69, s69, 62\n\t" \
    "s_cmp_ge_i32 s83, s69\n\t" \
    "s_cbranch_scc1 rr_nfl52_%=\n\t" \
    "global_load_dword v177, v6, s[36:37] sc1\n\t" \
    "rr_nfl52_%=:\n\t" \
    "s_cmp_lg_u32 s91, 0\n\t" \
    "s_cbranch_scc1 rr_la53_%=\n\t" \
    "s_mul_hi_u32 s69, s85, s78\n\t" \
    "s_add_u32 s79, s81, s69\n\t" \
    "s_add_u32 s79, s79, s87\n\t" \
    "s_cmp_ge_u32 s65, 36\n\t" \
    "s_cselect_b32 s69, 28, 0\n\t" \
    "s_sub_u32 s79, s79, s69\n\t" \
    "s_add_u32 s80, s79, s88\n\t" \
    "s_add_u32 s79, s79, 320\n\t" \
    "s_sub_u32 s70, s79, s74\n\t" \
    "s_max_i32 s70, s70, 0\n\t" \
    "s_min_i32 s70, s70, 16000\n\t" \
    "s_lshl_b32 s70, s70, 1\n\t" \
    "s_mul_hi_u32 s70, s70, s77\n\t" \
    "s_add_u32 s70, s70, s73\n\t" \
    "rr_W54_%=:\n\t" \
    "s_memtime s[56:57]\n\t" \
    "s_waitcnt lgkmcnt(0)\n\t" \
    "s_sub_u32 s69, s70, s56\n\t" \
    "s_cmp_gt_i32 s69, 24\n\t" \
    "s_cbranch_scc1 rr_W54_%=\n\t" \
    "s_cmp_gt_u32 s65, 61\n\t" \
    "s_cbranch_scc1 rr_arm55_%=\n\t" \
    "s_add_u32 s69, s65, 2\n\t" \
    "s_bitcmp1_b32 s69, 0\n\t" \
    "s_cselect_b32 s70, s63, s62\n\t" \
    "s_bfe_u32 s69, s69, 0x20001\n\t" \
    "s_lshl_b32 s69, s69, 19\n\t" \
    "s_add_u32 s69, s69, s70\n\t" \
    "s_add_u32 s46, s36, s69\n\t" \
    "s_addc_u32 s47, s37, 0\n\t" \
    "global_store_dwordx4 v164, v[172:175], s[46:47] sc1\n\t" \
    "global_store_dwordx4 v165, v[172:175], s[46:47] sc1\n\t" \
    "global_store_dwordx4 v166, v[172:175], s[46:47] sc1\n\t" \
    "rr_arm55_%=:\n\t" \
    "s_waitcnt vmcnt(0)\n\t" \
    "v_mov_b32 v188, s65\n\t" \
    "ds_write_b32 v178, v188 offset:8\n\t" \
    "rr_la53_%=:\n\t" \
    "s_waitcnt vmcnt(0)\n\t" \
    "v_min_i32_dpp v177, v177, v177 row_ror:1 row_mask:0xf bank_mask:0xf\n\t" \
    "s_nop 1\n\t" \
    "v_min_i32_dpp v177, v177, v177 row_ror:2 row_mask:0xf bank_mask:0xf\n\t" \
    "s_nop 1\n\t" \
    "v_min_i32_dpp v177, v177, v177 row_ror:4 row_mask:0xf bank_mask:0xf\n\t" \
    "s_nop 1\n\t" \
    "v_min_i32_dpp v177, v177, v177 row_ror:8 row_mask:0xf bank_mask:0xf\n\t" \
    "s_nop 0\n\t" \
    "v_readlane_b32 s69, v177, 0\n\t" \
    "v_readlane_b32 s70, v177, 16\n\t" \
    "s_nop 1\n\t" \
    "s_min_i32 s69, s69, s70\n\t" \
    "s_max_i32 s83, s83, s69\n\t" \
    "s_cmp_lg_u32 s65, 40\n\t" \
    "s_cbranch_scc1 rr_f256_%=\n\t" \
    "s_cmp_lg_u32 s67, 35\n\t" \
    "s_cbranch_scc1 rr_f256_%=\n\t" \
    "v_add_u32 v185, 36864, v6\n\t" \
    "rr_f2l57_%=:\n\t" \
    "global_load_dword v186, v185, s[36:37] sc1\n\t" \
    "s_waitcnt vmcnt(0)\n\t" \
    "v_cmp_ne_u32 vcc, 1, v186\n\t" \
    "s_cbranch_vccz rr_f256_%=\n\t" \
    "s_cmp_eq_u32 s68, 0\n\t" \
    "s_cbranch_scc1 rr_F58_%=\n\t" \
    "s_sub_u32 s68, s68, 1\n\t" \
    "s_sleep 2\n\t" \
    "s_branch rr_f2l57_%=\n\t" \
    "rr_F58_%=:\n\t" \
    "v_mov_b32 v187, 6095104\n\t" \
    "v_mov_b32 v188, 11\n\t" \
    "global_store_dword v187, v188, s[36:37] sc1\n\t" \
    "s_waitcnt vmcnt(0)\n\t" \
    "s_branch rr_f256_%=\n\t" \
    "rr_f256_%=:\n\t" \
    "rr_sl23_%=:\n\t" \
    "ds_read_b64 v[184:185], v9\n\t" \
    "s_sub_u32 s70, s65, 1\n\t" \
    "s_waitcnt lgkmcnt(0)\n\t" \
    "v_readfirstlane_b32 s69, v185\n\t" \
    "v_readfirstlane_b32 s81, v184\n\t" \
    "s_nop 0\n\t" \
    "s_cmp_eq_u32 s69, s70\n\t" \
    "s_cbranch_scc1 rr_u245_%=\n\t" \
    "s_cmp_eq_u32 s68, 0\n\t" \
    "s_cbranch_scc1 rr_F59_%=\n\t" \
    "s_sub_u32 s68, s68, 1\n\t" \
    "s_sleep 2\n\t" \
    "s_branch rr_sl23_%=\n\t" \
    "rr_F59_%=:\n\t" \
    "v_mov_b32 v184, 6095104\n\t" \
    "v_mov_b32 v185, 14\n\t" \
    "global_store_dword v184, v185, s[36:37] sc1\n\t" \
    "s_waitcnt vmcnt(0)\n\t" \
    "s_branch rr_u245_%=\n\t" \
    "rr_u245_%=:\n\t" \
    "s_add_u32 s69, s65, -1\n\t" \
    "s_bitcmp1_b32 s69, 0\n\t" \
    "s_cselect_b32 s70, s63, s62\n\t" \
    "s_bfe_u32 s69, s69, 0x20001\n\t" \
    "s_lshl_b32 s69, s69, 19\n\t" \
    "s_add_u32 s69, s69, s70\n\t" \
    "s_add_u32 s46, s36, s69\n\t" \
    "s_addc_u32 s47, s37, 0\n\t" \
    "s_cmp_lg_u32 s65, 1\n\t" \
    "s_cbranch_scc1 rr_Oc62_%=\n\t" \
    "s_add_u32 s79, s81, 480\n\t" \
    "s_sub_u32 s70, s79, s74\n\t" \
    "s_max_i32 s70, s70, 0\n\t" \
    "s_min_i32 s70, s70, 16000\n\t" \
    "s_lshl_b32 s70, s70, 1\n\t" \
    "s_mul_hi_u32 s70, s70, s77\n\t" \
    "s_add_u32 s70, s70, s73\n\t" \
    "rr_W63_%=:\n\t" \
    "s_memtime s[56:57]\n\t" \
    "s_waitcnt lgkmcnt(0)\n\t" \
    "s_sub_u32 s69, s70, s56\n\t" \
    "s_cmp_gt_i32 s69, 24\n\t" \
    "s_cbranch_scc1 rr_W63_%=\n\t" \
    "global_load_dword v184, v46, s[46:47] sc1\n\t" \
    "s_waitcnt vmcnt(0)\n\t" \
    "v_cmp_eq_u32 vcc, -1, v184\n\t" \
    "s_cbranch_vccz rr_Ook61_%=\n\t" \
    "rr_O60_%=:\n\t" \
    "global_load_dword v184, v46, s[46:47] offset:1536 sc1\n\t" \
    "s_waitcnt vmcnt(0)\n\t" \
    "v_cmp_eq_u32 vcc, -1, v184\n\t" \
    "s_cbranch_vccz rr_Ook61_%=\n\t" \
    "s_cmp_eq_u32 s68, 0\n\t" \
    "s_cbranch_scc1 rr_F64_%=\n\t" \
    "s_sub_u32 s68, s68, 1\n\t" \
    "s_sleep 3\n\t" \
    "s_branch rr_O60_%=\n\t" \
    "rr_F64_%=:\n\t" \
    "v_mov_b32 v185, 6095104\n\t" \
    "v_mov_b32 v186, 18\n\t" \
    "global_store_dword v185, v186, s[36:37] sc1\n\t" \
    "s_waitcnt vmcnt(0)\n\t" \
    "s_branch rr_Ook61_%=\n\t" \
    "rr_Ook61_%=:\n\t" \
    "v_subrev_u32 v184, s80, v184\n\t" \
    "s_nop 1\n\t" \
    "v_max_i32_dpp v184, v184, v184 row_ror:1 row_mask:0xf bank_mask:0xf\n\t" \
    "s_nop 1\n\t" \
    "v_max_i32_dpp v184, v184, v184 row_ror:2 row_mask:0xf bank_mask:0xf\n\t" \
    "s_nop 1\n\t" \
    "v_max_i32_dpp v184, v184, v184 row_ror:4 row_mask:0xf bank_mask:0xf\n\t" \
    "s_nop 1\n\t" \
    "v_max_i32_dpp v184, v184, v184 row_ror:8 row_mask:0xf bank_mask:0xf\n\t" \
    "s_nop 0\n\t" \
    "v_readlane_b32 s69, v184, 0\n\t" \
    "v_readlane_b32 s71, v184, 16\n\t" \
    "v_readlane_b32 s70, v184, 32\n\t" \
    "v_readlane_b32 s81, v184, 48\n\t" \
    "s_max_i32 s69, s69, s71\n\t" \
    "s_nop 0\n\t" \
    "s_max_i32 s71, s70, s81\n\t" \
    "s_max_i32 s69, s69, s71\n\t" \
    "s_add_u32 s81, s69, s80\n\t" \
    "rr_Oc62_%=:\n\t" \
    "s_mul_hi_u32 s69, s85, s78\n\t" \
    "s_add_u32 s79, s81, s69\n\t" \
    "s_add_u32 s79, s79, s87\n\t" \
    "s_cmp_ge_u32 s65, 36\n\t" \
    "s_cselect_b32 s69, 28, 0\n\t" \
    "s_sub_u32 s79, s79, s69\n\t" \
    "s_add_u32 s80, s79, s88\n\t" \
    "s_sub_u32 s70, s79, s74\n\t" \
    "s_max_i32 s70, s70, 0\n\t" \
    "s_min_i32 s70, s70, 16000\n\t" \
    "s_lshl_b32 s70, s70, 1\n\t" \
    "s_mul_hi_u32 s70, s70, s77\n\t" \
    "s_add_u32 s70, s70, s73\n\t" \
    "rr_W65_%=:\n\t" \
    "s_memtime s[56:57]\n\t" \
    "s_waitcnt lgkmcnt(0)\n\t" \
    "s_sub_u32 s69, s70, s56\n\t" \
    "s_cmp_gt_i32 s69, 24\n\t" \
    "s_cbranch_scc1 rr_W65_%=\n\t" \
    "rr_act7_%=:\n\t" \
    "s_add_u32 s69, s65, 0\n\t" \
    "s_bitcmp1_b32 s69, 0\n\t" \
    "s_cselect_b32 s70, s63, s62\n\t" \
    "s_bfe_u32 s69, s69, 0x20001\n\t" \
    "s_lshl_b32 s69, s69, 19\n\t" \
    "s_add_u32 s69, s69, s70\n\t" \
    "s_add_u32 s46, s36, s69\n\t" \
    "s_addc_u32 s47, s37, 0\n\t" \
    "s_add_u32 s69, s65, 1\n\t" \
    "s_bitcmp1_b32 s69, 0\n\t" \
    "s_cselect_b32 s70, s63, s62\n\t" \
    "s_bfe_u32 s69, s69, 0x20001\n\t" \
    "s_lshl_b32 s69, s69, 19\n\t" \
    "s_add_u32 s69, s69, s70\n\t" \
    "s_add_u32 s48, s36, s69\n\t" \
    "s_addc_u32 s49, s37, 0\n\t" \
    "s_add_u32 s69, s65, 0\n\t" \
    "s_mul_i32 s70, s69, 171\n\t" \
    "s_lshr_b32 s70, s70, 9\n\t" \
    "s_mul_i32 s70, s70, 3\n\t" \
    "s_sub_u32 s69, s69, s70\n\t" \
    "s_mul_i32 s69, s69, 36\n\t" \
    "s_add_u32 s69, s69, s67\n\t" \
    "s_lshl_b32 s69, s69, 14\n\t" \
    "s_add_u32 s69, s69, s64\n\t" \
    "s_add_u32 s69, s69, 4194304\n\t" \
    "s_add_u32 s40, s36, s69\n\t" \
    "s_addc_u32 s41, s37, 0\n\t" \
    "s_add_u32 s69, s65, 2\n\t" \
    "s_cmp_ge_i32 s83, s69\n\t" \
    "s_cbranch_scc1 rr_nf66_%=\n\t" \
    "global_load_dword v177, v6, s[36:37] sc1\n\t" \
    "rr_nf66_%=:\n\t" \
    "s_mov_b32 s71, 3\n\t" \
    "rr_E67_%=:\n\t" \
    "s_memtime s[54:55]\n\t" \
    "s_nop 0\n\t" \
    "s_memrealtime s[4:5]\n\t" \
    "s_nop 0\n\t" \
    "s_memrealtime s[6:7]\n\t" \
    "s_nop 0\n\t" \
    "s_memrealtime s[8:9]\n\t" \
    "s_nop 0\n\t" \
    "s_memrealtime s[10:11]\n\t" \
    "s_nop 0\n\t" \
    "s_memrealtime s[12:13]\n\t" \
    "s_nop 0\n\t" \
    "s_memrealtime s[14:15]\n\t" \
    "s_nop 0\n\t" \
    "s_memrealtime s[16:17]\n\t" \
    "s_nop 0\n\t" \
    "s_memrealtime s[18:19]\n\t" \
    "s_nop 0\n\t" \
    "s_memrealtime s[20:21]\n\t" \
    "s_nop 0\n\t" \
    "s_memrealtime s[22:23]\n\t" \
    "s_nop 0\n\t" \
    "s_memrealtime s[24:25]\n\t" \
    "s_nop 0\n\t" \
    "s_memrealtime s[26:27]\n\t" \
    "s_nop 0\n\t" \
    "s_memrealtime s[28:29]\n\t" \
    "s_nop 0\n\t" \
    "s_memtime s[56:57]\n\t" \
    "s_mov_b32 s70, 1\n\t" \
    "s_sub_u32 s71, s71, 1\n\t" \
    "s_cselect_b32 s69, 0, -1\n\t" \
    "s_waitcnt lgkmcnt(0)\n\t" \
    "s_sub_u32 s54, s56, s54\n\t" \
    "s_and_b32 s54, s54, s69\n\t" \
    "s_cmp_gt_u32 s54, 124\n\t" \
    "s_cbranch_scc1 rr_E67_%=\n\t" \
    "s_cmp_eq_u32 s4, s28\n\t" \
    "s_addc_u32 s70, s70, 0\n\t" \
    "s_cmp_eq_u32 s6, s28\n\t" \
    "s_addc_u32 s70, s70, 0\n\t" \
    "s_cmp_eq_u32 s8, s28\n\t" \
    "s_addc_u32 s70, s70, 0\n\t" \
    "s_cmp_eq_u32 s10, s28\n\t" \
    "s_addc_u32 s70, s70, 0\n\t" \
    "s_cmp_eq_u32 s12, s28\n\t" \
    "s_addc_u32 s70, s70, 0\n\t" \
    "s_cmp_eq_u32 s14, s28\n\t" \
    "s_addc_u32 s70, s70, 0\n\t" \
    "s_cmp_eq_u32 s16, s28\n\t" \
    "s_addc_u32 s70, s70, 0\n\t" \
    "s_cmp_eq_u32 s18, s28\n\t" \
    "s_addc_u32 s70, s70, 0\n\t" \
    "s_cmp_eq_u32 s20, s28\n\t" \
    "s_addc_u32 s70, s70, 0\n\t" \
    "s_cmp_eq_u32 s22, s28\n\t" \
    "s_addc_u32 s70, s70, 0\n\t" \
    "s_cmp_eq_u32 s24, s28\n\t" \
    "s_addc_u32 s70, s70, 0\n\t" \
    "s_cmp_eq_u32 s26, s28\n\t" \
    "s_addc_u32 s70, s70, 0\n\t" \
    "s_mov_b32 s75, s73\n\t" \
    "s_mov_b32 s76, s74\n\t" \
    "s_lshl_b32 s70, s70, 3\n\t" \
    "s_sub_u32 s73, s56, s70\n\t" \
    "s_lshl_b32 s74, s28, 4\n\t" \
    "s_sub_u32 s69, s73, s75\n\t" \
    "s_sub_u32 s70, s74, s76\n\t" \
    "s_sub_u32 s71, s70, 1600\n\t" \
    "s_cmp_lt_u32 s71, 400000\n\t" \
    "s_cbranch_scc0 rr_K69_%=\n\t" \
    "v_cvt_f32_u32 v184, s69\n\t" \
    "v_cvt_f32_u32 v185, s70\n\t" \
    "v_rcp_f32 v186, v184\n\t" \
    "v_rcp_f32 v187, v185\n\t" \
    "v_mul_f32 v186, v186, v185\n\t" \
    "v_mul_f32 v187, v187, v184\n\t" \
    "v_mul_f32 v186, 0x4f800000, v186\n\t" \
    "v_mul_f32 v187, 0x4f000000, v187\n\t" \
    "v_cvt_u32_f32 v186, v186\n\t" \
    "v_cvt_u32_f32 v187, v187\n\t" \
    "v_readfirstlane_b32 s69, v186\n\t" \
    "v_readfirstlane_b32 s70, v187\n\t" \
    "s_nop 1\n\t" \
    "s_sub_u32 s69, s69, s78\n\t" \
    "s_ashr_i32 s69, s69, 2\n\t" \
    "s_add_u32 s78, s78, s69\n\t" \
    "s_sub_u32 s70, s70, s77\n\t" \
    "s_ashr_i32 s70, s70, 2\n\t" \
    "s_add_u32 s77, s77, s70\n\t" \
    "rr_K69_%=:\n\t" \
    "s_mov_b32 s91, 0\n\t" \
    "v_mov_b32 v160, 0\n\t" \
    "v_mov_b32 v161, 0\n\t" \
    "v_mov_b32 v162, 0\n\t" \
    "v_mov_b32 v163, 0\n\t" \
    "s_sub_u32 s70, s80, s74\n\t" \
    "s_max_i32 s70, s70, 0\n\t" \
    "s_min_i32 s70, s70, 16000\n\t" \
    "s_lshl_b32 s70, s70, 1\n\t" \
    "s_mul_hi_u32 s70, s70, s77\n\t" \
    "s_add_u32 s70, s70, s73\n\t" \
    "s_sub_u32 s70, s70, 100\n\t" \
    "s_getpc_b64 s[30:31]\n\t" \
    "rr_Wp76_%=:\n\t" \
    "s_add_u32 s30, s30, rr_We77_%=-rr_Wp76_%=\n\t" \
    "s_addc_u32 s31, s31, 0\n\t" \
    "rr_W75_%=:\n\t" \
    "s_memtime s[56:57]\n\t" \
    "s_waitcnt lgkmcnt(0)\n\t" \
    "s_sub_u32 s69, s70, s56\n\t" \
    "s_cmp_gt_i32 s69, 111\n\t" \
    "s_cbranch_scc1 rr_W75_%=\n\t" \
    "s_max_i32 s69, s69, 0\n\t" \
    "s_lshr_b32 s69, s69, 1\n\t" \
    "s_and_b32 s69, s69, 0x3c\n\t" \
    "s_sub_u32 s30, s30, s69\n\t" \
    "s_subb_u32 s31, s31, 0\n\t" \
    "s_setpc_b64 s[30:31]\n\t" \
    ".p2align 6\n\t" \
    "s_nop 0\n\t" \
    "s_nop 0\n\t" \
    "s_nop 0\n\t" \
    "s_nop 1\n\t" \
    "s_nop 1\n\t" \
    "s_nop 1\n\t" \
    "s_nop 1\n\t" \
    "s_nop 1\n\t" \
    "s_nop 1\n\t" \
    "s_nop 1\n\t" \
    "s_nop 1\n\t" \
    "s_nop 1\n\t" \
    "s_nop 1\n\t" \
    "s_nop 1\n\t" \
    "s_nop 1\n\t" \
    "s_nop 1\n\t" \
    "rr_We77_%=:\n\t" \
    "global_load_dword v184, v7, s[46:47] sc1\n\t" \
    "ds_read_b32 v188, v179 offset:8\n\t" \
    "s_sub_u32 s97, s74, s86\n\t" \
    "s_sub_u32 s71, s73, 190\n\t" \
    "s_getpc_b64 s[30:31]\n\t" \
    "rr_wp78_%=:\n\t" \
    "s_add_u32 s30, s30, rr_again9_%=-rr_wp78_%=\n\t" \
    "s_addc_u32 s31, s31, (rr_again9_%=-rr_wp78_%=)>>32\n\t" \
    "s_waitcnt vmcnt(0) lgkmcnt(0)\n\t" \
    "rr_hok70_%=:\n\t" \
    "s_memtime s[56:57]\n\t" \
    "v_cmp_eq_u32 vcc, -1, v184\n\t" \
    "v_subrev_u32 v185, s97, v184\n\t" \
    "s_nop 1\n\t" \
    "v_max_i32_dpp v185, v185, v185 row_ror:1 row_mask:0xf bank_mask:0xf\n\t" \
    "s_cbranch_vccnz rr_hlate71_%=\n\t" \
    "s_nop 0\n\t" \
    "v_max_i32_dpp v185, v185, v185 row_ror:2 row_mask:0xf bank_mask:0xf\n\t" \
    "s_nop 1\n\t" \
    "v_max_i32_dpp v185, v185, v185 row_ror:4 row_mask:0xf bank_mask:0xf\n\t" \
    "s_nop 1\n\t" \
    "v_max_i32_dpp v185, v185, v185 row_ror:8 row_mask:0xf bank_mask:0xf\n\t" \
    "s_nop 0\n\t" \
    "v_readlane_b32 s69, v185, 0\n\t" \
    "v_readlane_b32 s70, v185, 16\n\t" \
    "s_waitcnt lgkmcnt(0)\n\t" \
    "s_sub_u32 s71, s71, s56\n\t" \
    "s_max_i32 s81, s69, s70\n\t" \
    "s_max_i32 s69, s81, 0\n\t" \
    "s_lshl_b32 s69, s69, 1\n\t" \
    "s_mul_hi_u32 s69, s69, s77\n\t" \
    "s_add_u32 s69, s69, s71\n\t" \
    "s_cmp_gt_i32 s69, 111\n\t" \
    "s_cbranch_scc1 rr_wl72_%=\n\t" \
    "rr_wf74_%=:\n\t" \
    "s_max_i32 s69, s69, 0\n\t" \
    "s_lshr_b32 s69, s69, 1\n\t" \
    "s_and_b32 s69, s69, 0x3c\n\t" \
    "s_sub_u32 s30, s30, s69\n\t" \
    "s_subb_u32 s31, s31, 0\n\t" \
    "s_setpc_b64 s[30:31]\n\t" \
    "rr_wl72_%=:\n\t" \
    "s_min_i32 s69, s69, 24000\n\t" \
    "s_add_u32 s70, s69, s56\n\t" \
    "s_add_u32 s70, s70, 62\n\t" \
    "rr_wp73_%=:\n\t" \
    "s_memtime s[56:57]\n\t" \
    "s_waitcnt lgkmcnt(0)\n\t" \
    "s_sub_u32 s69, s70, s56\n\t" \
    "s_cmp_gt_i32 s69, 111\n\t" \
    "s_cbranch_scc1 rr_wp73_%=\n\t" \
    "s_branch rr_wf74_%=\n\t" \
    ".p2align 6\n\t" \
    "s_nop 0\n\t" \
    "s_nop 0\n\t" \
    "s_nop 0\n\t" \
    "s_nop 1\n\t" \
    "s_nop 1\n\t" \
    "s_nop 1\n\t" \
    "s_nop 1\n\t" \
    "s_nop 1\n\t" \
    "s_nop 1\n\t" \
    "s_nop 1\n\t" \
    "s_nop 1\n\t" \
    "s_nop 1\n\t" \
    "s_nop 1\n\t" \
    "s_nop 1\n\t" \
    "s_nop 1\n\t" \
    "s_nop 1\n\t" \
    "rr_again9_%=:\n\t" \
    "global_load_dwordx4 v[48:51], v10, s[46:47] sc1\n\t" \
    "global_load_dwordx4 v[52:55], v11, s[46:47] sc1\n\t" \
    "global_load_dwordx4 v[56:59], v12, s[46:47] sc1\n\t" \
    "global_load_dwordx4 v[60:63], v13, s[46:47] sc1\n\t" \
    "global_load_dwordx4 v[64:67], v14, s[46:47] sc1\n\t" \
    "global_load_dwordx4 v[68:71], v15, s[46:47] sc1\n\t" \
    "global_load_dwordx4 v[72:75], v16, s[46:47] sc1\n\t" \
    "global_load_dwordx4 v[76:79], v17, s[46:47] sc1\n\t" \
    "global_load_dwordx4 v[80:83], v18, s[46:47] sc1\n\t" \
    "global_load_dwordx4 v[84:87], v19, s[46:47] sc1\n\t" \
    "global_load_dwordx4 v[88:91], v20, s[46:47] sc1\n\t" \
    "global_load_dwordx4 v[92:95], v21, s[46:47] sc1\n\t" \
    "global_load_dwordx4 v[96:99], v22, s[46:47] sc1\n\t" \
    "global_load_dwordx4 v[100:103], v23, s[46:47] sc1\n\t" \
    "global_load_dwordx4 v[104:107], v24, s[46:47] sc1\n\t" \
    "global_load_dwordx4 v[108:111], v25, s[46:47] sc1\n\t" \
    "global_load_dwordx4 v[112:115], v42, s[46:47] sc1\n\t" \
    "global_load_dwordx4 v[116:119], v43, s[46:47] sc1\n\t" \
    "global_load_dwordx4 v[120:123], v44, s[46:47] sc1\n\t" \
    "global_load_dwordx4 v[124:127], v45, s[46:47] sc1\n\t" \
    "global_load_dwordx4 v[128:131], v0, s[40:41] offset:0 sc1\n\t" \
    "global_load_dwordx4 v[132:135], v0, s[40:41] offset:1024 sc1\n\t" \
    "global_load_dwordx4 v[136:139], v0, s[40:41] offset:2048 sc1\n\t" \
    "global_load_dwordx4 v[140:143], v0, s[40:41] offset:3072 sc1\n\t" \
    "global_load_dwordx4 v[144:147], v1, s[40:41] offset:0 sc1\n\t" \
    "global_load_dwordx4 v[148:151], v1, s[40:41] offset:1024 sc1\n\t" \
    "global_load_dwordx4 v[152:155], v1, s[40:41] offset:2048 sc1\n\t" \
    "global_load_dwordx4 v[156:159], v1, s[40:41] offset:3072 sc1\n\t" \
    "s_cmp_lg_u32 s91, 0\n\t" \
    "s_cbranch_scc1 rr_nob12_%=\n\t" \
    "s_add_u32 s81, s81, s97\n\t" \
    "v_mov_b32 v184, s81\n\t" \
    "v_mov_b32 v185, s65\n\t" \
    "ds_write_b64 v9, v[184:185]\n\t" \
    "rr_al79_%=:\n\t" \
    "v_cmp_ge_i32 vcc, s65, v188\n\t" \
    "s_cbranch_vccnz rr_alate80_%=\n\t" \
    "rr_nob12_%=:\n\t" \
    "s_waitcnt vmcnt(27)\n\t" \
    "v_smfmac_f32_16x16x64_bf16 v[160:163], v[48:51], a[0:7], v4\n\t" \
    "s_waitcnt vmcnt(26)\n\t" \
    "v_smfmac_f32_16x16x64_bf16 v[160:163], v[52:55], a[8:15], v4\n\t" \
    "s_waitcnt vmcnt(25)\n\t" \
    "v_smfmac_f32_16x16x64_bf16 v[160:163], v[56:59], a[16:23], v4\n\t" \
    "s_waitcnt vmcnt(24)\n\t" \
    "v_smfmac_f32_16x16x64_bf16 v[160:163], v[60:63], a[24:31], v4\n\t" \
    "s_waitcnt vmcnt(23)\n\t" \
    "v_smfmac_f32_16x16x64_bf16 v[160:163], v[64:67], a[32:39], v4\n\t" \
    "s_waitcnt vmcnt(22)\n\t" \
    "v_smfmac_f32_16x16x64_bf16 v[160:163], v[68:71], a[40:47], v4\n\t" \
    "s_waitcnt vmcnt(21)\n\t" \
    "v_smfmac_f32_16x16x64_bf16 v[160:163], v[72:75], a[48:55], v4\n\t" \
    "s_waitcnt vmcnt(20)\n\t" \
    "v_smfmac_f32_16x16x64_bf16 v[160:163], v[76:79], a[56:63], v4\n\t" \
    "s_waitcnt vmcnt(19)\n\t" \
    "v_smfmac_f32_16x16x64_bf16 v[160:163], v[80:83], a[64:71], v4\n\t" \
    "s_waitcnt vmcnt(18)\n\t" \
    "v_smfmac_f32_16x16x64_bf16 v[160:163], v[84:87], a[72:79], v4\n\t" \
    "s_waitcnt vmcnt(17)\n\t" \
    "v_smfmac_f32_16x16x64_bf16 v[160:163], v[88:91], a[80:87], v4\n\t" \
    "s_waitcnt vmcnt(16)\n\t" \
    "v_smfmac_f32_16x16x64_bf16 v[160:163], v[92:95], a[88:95], v4\n\t" \
    "s_waitcnt vmcnt(15)\n\t" \
    "v_smfmac_f32_16x16x64_bf16 v[160:163], v[96:99], a[96:103], v4\n\t" \
    "s_waitcnt vmcnt(14)\n\t" \
    "v_smfmac_f32_16x16x64_bf16 v[160:163], v[100:103], a[104:111], v4\n\t" \
    "s_waitcnt vmcnt(13)\n\t" \
    "v_smfmac_f32_16x16x64_bf16 v[160:163], v[104:107], a[112:119], v4\n\t" \
    "s_waitcnt vmcnt(12)\n\t" \
    "v_smfmac_f32_16x16x64_bf16 v[160:163], v[108:111], a[120:127], v4\n\t" \
    "s_waitcnt vmcnt(6)\n\t" \
    "v_smfmac_f32_16x16x64_bf16 v[160:163], v[112:115], v[128:135], v4\n\t" \
    "s_waitcnt vmcnt(4)\n\t" \
    "v_smfmac_f32_16x16x64_bf16 v[160:163], v[116:119], v[136:143], v4\n\t" \
    "s_waitcnt vmcnt(2)\n\t" \
    "v_smfmac_f32_16x16x64_bf16 v[160:163], v[120:123], v[144:151], v4\n\t" \
    "s_waitcnt vmcnt(0)\n\t" \
    "v_smfmac_f32_16x16x64_bf16 v[160:163], v[124:127], v[152:159], v4\n\t" \
    "s_nop 7\n\t" \
    "v_pk_add_f32 v[160:161], v[160:161], v[162:163]\n\t" \
    "v_cvt_pk_bf16_f32 v184, v160, v161\n\t" \
    "v_cmp_u_f32 vcc, v160, v161\n\t" \
    "s_cbranch_vccnz rr_bad10_%=\n\t" \
    "rr_cont11_%=:\n\t" \
    "v_mov_b32_dpp v185, v184 quad_perm:[1,0,3,2] row_mask:0xf bank_mask:0xf\n\t" \
    "v_perm_b32 v184, v185, v184, v5\n\t" \
    "global_store_dword v2, v184, s[48:49] sc1\n\t" \
    "global_store_dword v181, v184, s[48:49] sc1\n\t" \
    "global_store_dword v182, v184, s[48:49] sc1\n\t" \
    "global_store_dword v183, v184, s[48:49] sc1\n\t" \
    "s_cmp_eq_u32 s65, 62\n\t" \
    "s_cbranch_scc1 rr_end8_%=\n\t" \
    "s_add_u32 s65, s65, 2\n\t" \
    "s_branch rr_pf13_%=\n\t" \
    "rr_alate80_%=:\n\t" \
    "ds_read_b32 v188, v179 offset:8\n\t" \
    "s_waitcnt lgkmcnt(0)\n\t" \
    "s_cmp_eq_u32 s68, 0\n\t" \
    "s_cbranch_scc1 rr_F81_%=\n\t" \
    "s_sub_u32 s68, s68, 1\n\t" \
    "s_branch rr_al79_%=\n\t" \
    "rr_F81_%=:\n\t" \
    "v_mov_b32 v188, 6095104\n\t" \
    "v_mov_b32 v189, 15\n\t" \
    "global_store_dword v188, v189, s[36:37] sc1\n\t" \
    "s_waitcnt vmcnt(0)\n\t" \
    "s_branch rr_nob12_%=\n\t" \
    "rr_bad10_%=:\n\t" \
    "s_waitcnt lgkmcnt(0)\n\t" \
    "v_max_u32 v185, v48, v49\n\t" \
    "v_max3_u32 v185, v185, v50, v51\n\t" \
    "v_max3_u32 v185, v185, v52, v53\n\t" \
    "v_max3_u32 v185, v185, v54, v55\n\t" \
    "v_max3_u32 v185, v185, v56, v57\n\t" \
    "v_max3_u32 v185, v185, v58, v59\n\t" \
    "v_max3_u32 v185, v185, v60, v61\n\t" \
    "v_max3_u32 v185, v185, v62, v63\n\t" \
    "v_max3_u32 v185, v185, v64, v65\n\t" \
    "v_max3_u32 v185, v185, v66, v67\n\t" \
    "v_max3_u32 v185, v185, v68, v69\n\t" \
    "v_max3_u32 v185, v185, v70, v71\n\t" \
    "v_max3_u32 v185, v185, v72, v73\n\t" \
    "v_max3_u32 v185, v185, v74, v75\n\t" \
    "v_max3_u32 v185, v185, v76, v77\n\t" \
    "v_max3_u32 v185, v185, v78, v79\n\t" \
    "v_max3_u32 v185, v185, v80, v81\n\t" \
    "v_max3_u32 v185, v185, v82, v83\n\t" \
    "v_max3_u32 v185, v185, v84, v85\n\t" \
    "v_max3_u32 v185, v185, v86, v87\n\t" \
    "v_max3_u32 v185, v185, v88, v89\n\t" \
    "v_max3_u32 v185, v185, v90, v91\n\t" \
    "v_max3_u32 v185, v185, v92, v93\n\t" \
    "v_max3_u32 v185, v185, v94, v95\n\t" \
    "v_max3_u32 v185, v185, v96, v97\n\t" \
    "v_max3_u32 v185, v185, v98, v99\n\t" \
    "v_max3_u32 v185, v185, v100, v101\n\t" \
    "v_max3_u32 v185, v185, v102, v103\n\t" \
    "v_max3_u32 v185, v185, v104, v105\n\t" \
    "v_max3_u32 v185, v185, v106, v107\n\t" \
    "v_max3_u32 v185, v185, v108, v109\n\t" \
    "v_max3_u32 v185, v185, v110, v111\n\t" \
    "v_max3_u32 v185, v185, v112, v113\n\t" \
    "v_max3_u32 v185, v185, v114, v115\n\t" \
    "v_max3_u32 v185, v185, v116, v117\n\t" \
    "v_max3_u32 v185, v185, v118, v119\n\t" \
    "v_max3_u32 v185, v185, v120, v121\n\t" \
    "v_max3_u32 v185, v185, v122, v123\n\t" \
    "v_max3_u32 v185, v185, v124, v125\n\t" \
    "v_max3_u32 v185, v185, v126, v127\n\t" \
    "v_max_u32 v186, v128, v129\n\t" \
    "v_max3_u32 v186, v186, v130, v131\n\t" \
    "v_max3_u32 v186, v186, v132, v133\n\t" \
    "v_max3_u32 v186, v186, v134, v135\n\t" \
    "v_max3_u32 v186, v186, v136, v137\n\t" \
    "v_max3_u32 v186, v186, v138, v139\n\t" \
    "v_max3_u32 v186, v186, v140, v141\n\t" \
    "v_max3_u32 v186, v186, v142, v143\n\t" \
    "v_max3_u32 v186, v186, v144, v145\n\t" \
    "v_max3_u32 v186, v186, v146, v147\n\t" \
    "v_max3_u32 v186, v186, v148, v149\n\t" \
    "v_max3_u32 v186, v186, v150, v151\n\t" \
    "v_max3_u32 v186, v186, v152, v153\n\t" \
    "v_max3_u32 v186, v186, v154, v155\n\t" \
    "v_max3_u32 v186, v186, v156, v157\n\t" \
    "v_max3_u32 v186, v186, v158, v159\n\t" \
    "v_max_u32 v185, v185, v186\n\t" \
    "v_cmp_eq_u32 vcc, -1, v185\n\t" \
    "s_cbranch_vccz rr_cont11_%=\n\t" \
    "s_mov_b32 s91, 1\n\t" \
    "v_mov_b32 v160, 0\n\t" \
    "v_mov_b32 v161, 0\n\t" \
    "v_mov_b32 v162, 0\n\t" \
    "v_mov_b32 v163, 0\n\t" \
    "s_cmp_eq_u32 s68, 0\n\t" \
    "s_cbranch_scc1 rr_F82_%=\n\t" \
    "s_sub_u32 s68, s68, 1\n\t" \
    "s_branch rr_again9_%=\n\t" \
    "rr_F82_%=:\n\t" \
    "v_mov_b32 v186, 6095104\n\t" \
    "v_mov_b32 v187, 3\n\t" \
    "global_store_dword v186, v187, s[36:37] sc1\n\t" \
    "s_waitcnt vmcnt(0)\n\t" \
    "s_branch rr_cont11_%=\n\t" \
    "rr_hlate71_%=:\n\t" \
    "s_waitcnt lgkmcnt(0)\n\t" \
    "v_add_u32 v189, 1024, v7\n\t" \
    "global_load_dword v186, v189, s[46:47] sc1\n\t" \
    "s_sleep 4\n\t" \
    "v_add_u32 v189, 2048, v7\n\t" \
    "global_load_dword v187, v189, s[46:47] sc1\n\t" \
    "s_waitcnt vmcnt(1)\n\t" \
    "v_cmp_eq_u32 vcc, -1, v186\n\t" \
    "s_cbranch_vccz rr_hg83_%=\n\t" \
    "v_add_u32 v189, 3072, v7\n\t" \
    "global_load_dword v186, v189, s[46:47] sc1\n\t" \
    "s_waitcnt vmcnt(1)\n\t" \
    "v_cmp_eq_u32 vcc, -1, v187\n\t" \
    "s_cbranch_vccz rr_hg84_%=\n\t" \
    "s_waitcnt vmcnt(0)\n\t" \
    "v_cmp_eq_u32 vcc, -1, v186\n\t" \
    "s_cbranch_vccz rr_hg83_%=\n\t" \
    "rr_hp85_%=:\n\t" \
    "global_load_dword v184, v189, s[46:47] sc1\n\t" \
    "s_waitcnt vmcnt(0)\n\t" \
    "v_cmp_eq_u32 vcc, -1, v184\n\t" \
    "s_cbranch_vccz rr_hpd86_%=\n\t" \
    "s_cmp_eq_u32 s68, 0\n\t" \
    "s_cbranch_scc1 rr_F87_%=\n\t" \
    "s_sub_u32 s68, s68, 1\n\t" \
    "s_sleep 1\n\t" \
    "s_branch rr_hp85_%=\n\t" \
    "rr_F87_%=:\n\t" \
    "v_mov_b32 v186, 6095104\n\t" \
    "v_mov_b32 v187, 7\n\t" \
    "global_store_dword v186, v187, s[36:37] sc1\n\t" \
    "s_waitcnt vmcnt(0)\n\t" \
    "s_branch rr_hpd86_%=\n\t" \
    "rr_hpd86_%=:\n\t" \
    "rr_h044_%=:\n\t" \
    "v_and_b32 v184, -2, v184\n\t" \
    "s_mov_b32 s71, 3\n\t" \
    "rr_E88_%=:\n\t" \
    "s_memtime s[54:55]\n\t" \
    "s_nop 0\n\t" \
    "s_memrealtime s[4:5]\n\t" \
    "s_nop 0\n\t" \
    "s_memrealtime s[6:7]\n\t" \
    "s_nop 0\n\t" \
    "s_memrealtime s[8:9]\n\t" \
    "s_nop 0\n\t" \
    "s_memrealtime s[10:11]\n\t" \
    "s_nop 0\n\t" \
    "s_memrealtime s[12:13]\n\t" \
    "s_nop 0\n\t" \
    "s_memrealtime s[14:15]\n\t" \
    "s_nop 0\n\t" \
    "s_memrealtime s[16:17]\n\t" \
    "s_nop 0\n\t" \
    "s_memrealtime s[18:19]\n\t" \
    "s_nop 0\n\t" \
    "s_memrealtime s[20:21]\n\t" \
    "s_nop 0\n\t" \
    "s_memrealtime s[22:23]\n\t" \
    "s_nop 0\n\t" \
    "s_memrealtime s[24:25]\n\t" \
    "s_nop 0\n\t" \
    "s_memrealtime s[26:27]\n\t" \
    "s_nop 0\n\t" \
    "s_memrealtime s[28:29]\n\t" \
    "s_nop 0\n\t" \
    "s_memtime s[56:57]\n\t" \
    "s_mov_b32 s70, 1\n\t" \
    "s_sub_u32 s71, s71, 1\n\t" \
    "s_cselect_b32 s69, 0, -1\n\t" \
    "s_waitcnt lgkmcnt(0)\n\t" \
    "s_sub_u32 s54, s56, s54\n\t" \
    "s_and_b32 s54, s54, s69\n\t" \
    "s_cmp_gt_u32 s54, 124\n\t" \
    "s_cbranch_scc1 rr_E88_%=\n\t" \
    "s_cmp_eq_u32 s4, s28\n\t" \
    "s_addc_u32 s70, s70, 0\n\t" \
    "s_cmp_eq_u32 s6, s28\n\t" \
    "s_addc_u32 s70, s70, 0\n\t" \
    "s_cmp_eq_u32 s8, s28\n\t" \
    "s_addc_u32 s70, s70, 0\n\t" \
    "s_cmp_eq_u32 s10, s28\n\t" \
    "s_addc_u32 s70, s70, 0\n\t" \
    "s_cmp_eq_u32 s12, s28\n\t" \
    "s_addc_u32 s70, s70, 0\n\t" \
    "s_cmp_eq_u32 s14, s28\n\t" \
    "s_addc_u32 s70, s70, 0\n\t" \
    "s_cmp_eq_u32 s16, s28\n\t" \
    "s_addc_u32 s70, s70, 0\n\t" \
    "s_cmp_eq_u32 s18, s28\n\t" \
    "s_addc_u32 s70, s70, 0\n\t" \
    "s_cmp_eq_u32 s20, s28\n\t" \
    "s_addc_u32 s70, s70, 0\n\t" \
    "s_cmp_eq_u32 s22, s28\n\t" \
    "s_addc_u32 s70, s70, 0\n\t" \
    "s_cmp_eq_u32 s24, s28\n\t" \
    "s_addc_u32 s70, s70, 0\n\t" \
    "s_cmp_eq_u32 s26, s28\n\t" \
    "s_addc_u32 s70, s70, 0\n\t" \
    "s_mov_b32 s75, s73\n\t" \
    "s_mov_b32 s76, s74\n\t" \
    "s_lshl_b32 s70, s70, 3\n\t" \
    "s_sub_u32 s73, s56, s70\n\t" \
    "s_lshl_b32 s74, s28, 4\n\t" \
    "s_sub_u32 s97, s74, s86\n\t" \
    "s_sub_u32 s71, s73, 190\n\t" \
    "s_getpc_b64 s[30:31]\n\t" \
    "rr_wp90_%=:\n\t" \
    "s_add_u32 s30, s30, rr_again9_%=-rr_wp90_%=\n\t" \
    "s_addc_u32 s31, s31, (rr_again9_%=-rr_wp90_%=)>>32\n\t" \
    "s_branch rr_hok70_%=\n\t" \
    "rr_hg83_%=:\n\t" \
    "v_mov_b32 v184, v186\n\t" \
    "s_branch rr_hok70_%=\n\t" \
    "rr_hg84_%=:\n\t" \
    "v_mov_b32 v184, v187\n\t" \
    "s_branch rr_hok70_%=\n\t" \
    "rr_end8_%=:\n\t" \
    "s_waitcnt vmcnt(0) lgkmcnt(0)\n\t" \
    "s_endpgm\n\t" \
    "rr_last_%=:\n\t" \
    ""
#define ASM_CV \
    "v_mov_b32 v0, %0\n\t" \
    "v_mov_b32 v2, %2\n\t" \
    "v_subrev_u32 v1, 768, %1\n\t" \
    "v_mov_b32 v4, 0\n\t" \
    "v_mov_b32 v5, 0\n\t" \
    "v_mov_b32 v6, 0\n\t" \
    "v_mov_b32 v7, 0\n\t" \
    "v_mov_b32 v8, 0\n\t" \
    "v_mov_b32 v9, 0\n\t" \
    "v_mov_b32 v10, 0\n\t" \
    "v_mov_b32 v11, 0\n\t" \
    "v_mov_b32 v12, 0\n\t" \
    "v_mov_b32 v13, 0\n\t" \
    "v_mov_b32 v14, 0\n\t" \
    "v_mov_b32 v15, 0\n\t" \
    "v_mov_b32 v16, 0\n\t" \
    "v_mov_b32 v17, 0\n\t" \
    "v_mov_b32 v18, 0\n\t" \
    "v_mov_b32 v19, 0\n\t" \
    "v_mov_b32 v20, 0\n\t" \
    "v_mov_b32 v21, 0\n\t" \
    "v_mov_b32 v22, 0\n\t" \
    "v_mov_b32 v23, 0\n\t" \
    "v_mov_b32 v24, 0\n\t" \
    "v_mov_b32 v25, 0\n\t" \
    "v_mov_b32 v26, 0\n\t" \
    "v_mov_b32 v27, 0\n\t" \
    "v_mov_b32 v28, 0\n\t" \
    "v_mov_b32 v29, 0\n\t" \
    "v_mov_b32 v30, 0\n\t" \
    "v_mov_b32 v31, 0\n\t" \
    "v_mov_b32 v32, 0\n\t" \
    "v_mov_b32 v33, 0\n\t" \
    "v_mov_b32 v34, 0\n\t" \
    "v_mov_b32 v35, 0\n\t" \
    "s_waitcnt lgkmcnt(0)\n\t" \
    "ds_read_b64 v[36:37], v0 offset:0\n\t" \
    "ds_read_b64 v[38:39], v0 offset:8\n\t" \
    "ds_read_b64 v[40:41], v0 offset:32\n\t" \
    "ds_read_b64 v[42:43], v0 offset:40\n\t" \
    "ds_read_b128 v[44:47], v1 offset:768\n\t" \
    "ds_read_b128 v[48:51], v1 offset:2304\n\t" \
    "ds_read_b128 v[52:55], v1 offset:3840\n\t" \
    "ds_read_b128 v[56:59], v1 offset:5376\n\t" \
    "ds_read_b128 v[60:63], v1 offset:8448\n\t" \
    "ds_read_b128 v[64:67], v1 offset:9984\n\t" \
    "ds_read_b128 v[68:71], v1 offset:11520\n\t" \
    "ds_read_b128 v[72:75], v1 offset:13056\n\t" \
    "s_mov_b32 s69, 8\n\t" \
    "cv_c1_%=:\n\t" \
    "s_waitcnt lgkmcnt(7)\n\t" \
    "v_mfma_f32_16x16x32_bf16 v[4:7], v[36:39], v[44:47], v[4:7]\n\t" \
    "ds_read_b64 v[76:77], v0 offset:64\n\t" \
    "ds_read_b64 v[78:79], v0 offset:72\n\t" \
    "v_mfma_f32_16x16x32_bf16 v[20:23], v[40:43], v[44:47], v[20:23]\n\t" \
    "ds_read_b64 v[80:81], v0 offset:96\n\t" \
    "ds_read_b64 v[82:83], v0 offset:104\n\t" \
    "s_waitcnt lgkmcnt(10)\n\t" \
    "v_mfma_f32_16x16x32_bf16 v[8:11], v[36:39], v[48:51], v[8:11]\n\t" \
    "ds_read_b128 v[84:87], v1 offset:672\n\t" \
    "ds_read_b128 v[88:91], v1 offset:2208\n\t" \
    "v_mfma_f32_16x16x32_bf16 v[24:27], v[40:43], v[48:51], v[24:27]\n\t" \
    "ds_read_b128 v[92:95], v1 offset:3744\n\t" \
    "ds_read_b128 v[96:99], v1 offset:5280\n\t" \
    "s_waitcnt lgkmcnt(13)\n\t" \
    "v_mfma_f32_16x16x32_bf16 v[12:15], v[36:39], v[52:55], v[12:15]\n\t" \
    "ds_read_b128 v[100:103], v1 offset:8352\n\t" \
    "ds_read_b128 v[104:107], v1 offset:9888\n\t" \
    "v_mfma_f32_16x16x32_bf16 v[28:31], v[40:43], v[52:55], v[28:31]\n\t" \
    "s_waitcnt lgkmcnt(14)\n\t" \
    "v_mfma_f32_16x16x32_bf16 v[16:19], v[36:39], v[56:59], v[16:19]\n\t" \
    "ds_read_b128 v[108:111], v1 offset:11424\n\t" \
    "v_mfma_f32_16x16x32_bf16 v[32:35], v[40:43], v[56:59], v[32:35]\n\t" \
    "s_waitcnt lgkmcnt(14)\n\t" \
    "v_mfma_f32_16x16x32_bf16 v[4:7], v[36:39], v[60:63], v[4:7]\n\t" \
    "ds_read_b128 v[112:115], v1 offset:12960\n\t" \
    "v_mfma_f32_16x16x32_bf16 v[20:23], v[40:43], v[60:63], v[20:23]\n\t" \
    "s_waitcnt lgkmcnt(14)\n\t" \
    "v_mfma_f32_16x16x32_bf16 v[8:11], v[36:39], v[64:67], v[8:11]\n\t" \
    "v_mfma_f32_16x16x32_bf16 v[24:27], v[40:43], v[64:67], v[24:27]\n\t" \
    "s_waitcnt lgkmcnt(13)\n\t" \
    "v_mfma_f32_16x16x32_bf16 v[12:15], v[36:39], v[68:71], v[12:15]\n\t" \
    "v_mfma_f32_16x16x32_bf16 v[28:31], v[40:43], v[68:71], v[28:31]\n\t" \
    "s_waitcnt lgkmcnt(12)\n\t" \
    "v_mfma_f32_16x16x32_bf16 v[16:19], v[36:39], v[72:75], v[16:19]\n\t" \
    "v_mfma_f32_16x16x32_bf16 v[32:35], v[40:43], v[72:75], v[32:35]\n\t" \
    "s_waitcnt lgkmcnt(7)\n\t" \
    "v_mfma_f32_16x16x32_bf16 v[4:7], v[76:79], v[84:87], v[4:7]\n\t" \
    "ds_read_b64 v[36:37], v0 offset:128\n\t" \
    "ds_read_b64 v[38:39], v0 offset:136\n\t" \
    "v_mfma_f32_16x16x32_bf16 v[20:23], v[80:83], v[84:87], v[20:23]\n\t" \
    "ds_read_b64 v[40:41], v0 offset:160\n\t" \
    "ds_read_b64 v[42:43], v0 offset:168\n\t" \
    "s_waitcnt lgkmcnt(10)\n\t" \
    "v_mfma_f32_16x16x32_bf16 v[8:11], v[76:79], v[88:91], v[8:11]\n\t" \
    "ds_read_b128 v[44:47], v1 offset:576\n\t" \
    "ds_read_b128 v[48:51], v1 offset:2112\n\t" \
    "v_mfma_f32_16x16x32_bf16 v[24:27], v[80:83], v[88:91], v[24:27]\n\t" \
    "ds_read_b128 v[52:55], v1 offset:3648\n\t" \
    "ds_read_b128 v[56:59], v1 offset:5184\n\t" \
    "s_waitcnt lgkmcnt(13)\n\t" \
    "v_mfma_f32_16x16x32_bf16 v[12:15], v[76:79], v[92:95], v[12:15]\n\t" \
    "ds_read_b128 v[60:63], v1 offset:8256\n\t" \
    "ds_read_b128 v[64:67], v1 offset:9792\n\t" \
    "v_mfma_f32_16x16x32_bf16 v[28:31], v[80:83], v[92:95], v[28:31]\n\t" \
    "s_waitcnt lgkmcnt(14)\n\t" \
    "v_mfma_f32_16x16x32_bf16 v[16:19], v[76:79], v[96:99], v[16:19]\n\t" \
    "ds_read_b128 v[68:71], v1 offset:11328\n\t" \
    "v_mfma_f32_16x16x32_bf16 v[32:35], v[80:83], v[96:99], v[32:35]\n\t" \
    "s_waitcnt lgkmcnt(14)\n\t" \
    "v_mfma_f32_16x16x32_bf16 v[4:7], v[76:79], v[100:103], v[4:7]\n\t" \
    "ds_read_b128 v[72:75], v1 offset:12864\n\t" \
    "v_mfma_f32_16x16x32_bf16 v[20:23], v[80:83], v[100:103], v[20:23]\n\t" \
    "s_waitcnt lgkmcnt(14)\n\t" \
    "v_mfma_f32_16x16x32_bf16 v[8:11], v[76:79], v[104:107], v[8:11]\n\t" \
    "v_mfma_f32_16x16x32_bf16 v[24:27], v[80:83], v[104:107], v[24:27]\n\t" \
    "s_waitcnt lgkmcnt(13)\n\t" \
    "v_mfma_f32_16x16x32_bf16 v[12:15], v[76:79], v[108:111], v[12:15]\n\t" \
    "v_mfma_f32_16x16x32_bf16 v[28:31], v[80:83], v[108:111], v[28:31]\n\t" \
    "s_waitcnt lgkmcnt(12)\n\t" \
    "v_mfma_f32_16x16x32_bf16 v[16:19], v[76:79], v[112:115], v[16:19]\n\t" \
    "v_mfma_f32_16x16x32_bf16 v[32:35], v[80:83], v[112:115], v[32:35]\n\t" \
    "v_add_u32 v0, 128, v0\n\t" \
    "v_subrev_u32 v1, 192, v1\n\t" \
    "s_sub_u32 s69, s69, 1\n\t" \
    "s_cmp_lg_u32 s69, 0\n\t" \
    "s_cbranch_scc1 cv_c1_%=\n\t" \
    "s_waitcnt lgkmcnt(0)\n\t" \
    "s_mov_b32 s69, 8\n\t" \
    "cv_c2_%=:\n\t" \
    "s_waitcnt lgkmcnt(5)\n\t" \
    "v_mfma_f32_16x16x32_bf16 v[8:11], v[36:39], v[48:51], v[8:11]\n\t" \
    "ds_read_b64 v[76:77], v0 offset:64\n\t" \
    "ds_read_b64 v[78:79], v0 offset:72\n\t" \
    "v_mfma_f32_16x16x32_bf16 v[24:27], v[40:43], v[48:51], v[24:27]\n\t" \
    "ds_read_b64 v[80:81], v0 offset:96\n\t" \
    "ds_read_b64 v[82:83], v0 offset:104\n\t" \
    "s_waitcnt lgkmcnt(8)\n\t" \
    "v_mfma_f32_16x16x32_bf16 v[12:15], v[36:39], v[52:55], v[12:15]\n\t" \
    "ds_read_b128 v[88:91], v1 offset:2208\n\t" \
    "ds_read_b128 v[92:95], v1 offset:3744\n\t" \
    "v_mfma_f32_16x16x32_bf16 v[28:31], v[40:43], v[52:55], v[28:31]\n\t" \
    "ds_read_b128 v[96:99], v1 offset:5280\n\t" \
    "ds_read_b128 v[104:107], v1 offset:9888\n\t" \
    "s_waitcnt lgkmcnt(11)\n\t" \
    "v_mfma_f32_16x16x32_bf16 v[16:19], v[36:39], v[56:59], v[16:19]\n\t" \
    "ds_read_b128 v[108:111], v1 offset:11424\n\t" \
    "ds_read_b128 v[112:115], v1 offset:12960\n\t" \
    "v_mfma_f32_16x16x32_bf16 v[32:35], v[40:43], v[56:59], v[32:35]\n\t" \
    "s_waitcnt lgkmcnt(12)\n\t" \
    "v_mfma_f32_16x16x32_bf16 v[8:11], v[36:39], v[64:67], v[8:11]\n\t" \
    "v_mfma_f32_16x16x32_bf16 v[24:27], v[40:43], v[64:67], v[24:27]\n\t" \
    "s_waitcnt lgkmcnt(11)\n\t" \
    "v_mfma_f32_16x16x32_bf16 v[12:15], v[36:39], v[68:71], v[12:15]\n\t" \
    "v_mfma_f32_16x16x32_bf16 v[28:31], v[40:43], v[68:71], v[28:31]\n\t" \
    "s_waitcnt lgkmcnt(10)\n\t" \
    "v_mfma_f32_16x16x32_bf16 v[16:19], v[36:39], v[72:75], v[16:19]\n\t" \
    "v_mfma_f32_16x16x32_bf16 v[32:35], v[40:43], v[72:75], v[32:35]\n\t" \
    "s_waitcnt lgkmcnt(5)\n\t" \
    "v_mfma_f32_16x16x32_bf16 v[8:11], v[76:79], v[88:91], v[8:11]\n\t" \
    "ds_read_b64 v[36:37], v0 offset:128\n\t" \
    "ds_read_b64 v[38:39], v0 offset:136\n\t" \
    "v_mfma_f32_16x16x32_bf16 v[24:27], v[80:83], v[88:91], v[24:27]\n\t" \
    "ds_read_b64 v[40:41], v0 offset:160\n\t" \
    "ds_read_b64 v[42:43], v0 offset:168\n\t" \
    "s_waitcnt lgkmcnt(8)\n\t" \
    "v_mfma_f32_16x16x32_bf16 v[12:15], v[76:79], v[92:95], v[12:15]\n\t" \
    "ds_read_b128 v[48:51], v1 offset:2112\n\t" \
    "ds_read_b128 v[52:55], v1 offset:3648\n\t" \
    "v_mfma_f32_16x16x32_bf16 v[28:31], v[80:83], v[92:95], v[28:31]\n\t" \
    "ds_read_b128 v[56:59], v1 offset:5184\n\t" \
    "ds_read_b128 v[64:67], v1 offset:9792\n\t" \
    "s_waitcnt lgkmcnt(11)\n\t" \
    "v_mfma_f32_16x16x32_bf16 v[16:19], v[76:79], v[96:99], v[16:19]\n\t" \
    "ds_read_b128 v[68:71], v1 offset:11328\n\t" \
    "ds_read_b128 v[72:75], v1 offset:12864\n\t" \
    "v_mfma_f32_16x16x32_bf16 v[32:35], v[80:83], v[96:99], v[32:35]\n\t" \
    "s_waitcnt lgkmcnt(12)\n\t" \
    "v_mfma_f32_16x16x32_bf16 v[8:11], v[76:79], v[104:107], v[8:11]\n\t" \
    "v_mfma_f32_16x16x32_bf16 v[24:27], v[80:83], v[104:107], v[24:27]\n\t" \
    "s_waitcnt lgkmcnt(11)\n\t" \
    "v_mfma_f32_16x16x32_bf16 v[12:15], v[76:79], v[108:111], v[12:15]\n\t" \
    "v_mfma_f32_16x16x32_bf16 v[28:31], v[80:83], v[108:111], v[28:31]\n\t" \
    "s_waitcnt lgkmcnt(10)\n\t" \
    "v_mfma_f32_16x16x32_bf16 v[16:19], v[76:79], v[112:115], v[16:19]\n\t" \
    "v_mfma_f32_16x16x32_bf16 v[32:35], v[80:83], v[112:115], v[32:35]\n\t" \
    "v_add_u32 v0, 128, v0\n\t" \
    "v_subrev_u32 v1, 192, v1\n\t" \
    "s_sub_u32 s69, s69, 1\n\t" \
    "s_cmp_lg_u32 s69, 0\n\t" \
    "s_cbranch_scc1 cv_c2_%=\n\t" \
    "s_waitcnt lgkmcnt(0)\n\t" \
    "s_mov_b32 s69, 8\n\t" \
    "cv_c3_%=:\n\t" \
    "s_waitcnt lgkmcnt(3)\n\t" \
    "v_mfma_f32_16x16x32_bf16 v[12:15], v[36:39], v[52:55], v[12:15]\n\t" \
    "ds_read_b64 v[76:77], v0 offset:64\n\t" \
    "ds_read_b64 v[78:79], v0 offset:72\n\t" \
    "v_mfma_f32_16x16x32_bf16 v[28:31], v[40:43], v[52:55], v[28:31]\n\t" \
    "ds_read_b64 v[80:81], v0 offset:96\n\t" \
    "ds_read_b64 v[82:83], v0 offset:104\n\t" \
    "s_waitcnt lgkmcnt(6)\n\t" \
    "v_mfma_f32_16x16x32_bf16 v[16:19], v[36:39], v[56:59], v[16:19]\n\t" \
    "ds_read_b128 v[92:95], v1 offset:3744\n\t" \
    "ds_read_b128 v[96:99], v1 offset:5280\n\t" \
    "v_mfma_f32_16x16x32_bf16 v[32:35], v[40:43], v[56:59], v[32:35]\n\t" \
    "ds_read_b128 v[108:111], v1 offset:11424\n\t" \
    "ds_read_b128 v[112:115], v1 offset:12960\n\t" \
    "s_waitcnt lgkmcnt(9)\n\t" \
    "v_mfma_f32_16x16x32_bf16 v[12:15], v[36:39], v[68:71], v[12:15]\n\t" \
    "v_mfma_f32_16x16x32_bf16 v[28:31], v[40:43], v[68:71], v[28:31]\n\t" \
    "s_waitcnt lgkmcnt(8)\n\t" \
    "v_mfma_f32_16x16x32_bf16 v[16:19], v[36:39], v[72:75], v[16:19]\n\t" \
    "v_mfma_f32_16x16x32_bf16 v[32:35], v[40:43], v[72:75], v[32:35]\n\t" \
    "s_waitcnt lgkmcnt(3)\n\t" \
    "v_mfma_f32_16x16x32_bf16 v[12:15], v[76:79], v[92:95], v[12:15]\n\t" \
    "ds_read_b64 v[36:37], v0 offset:128\n\t" \
    "ds_read_b64 v[38:39], v0 offset:136\n\t" \
    "v_mfma_f32_16x16x32_bf16 v[28:31], v[80:83], v[92:95], v[28:31]\n\t" \
    "ds_read_b64 v[40:41], v0 offset:160\n\t" \
    "ds_read_b64 v[42:43], v0 offset:168\n\t" \
    "s_waitcnt lgkmcnt(6)\n\t" \
    "v_mfma_f32_16x16x32_bf16 v[16:19], v[76:79], v[96:99], v[16:19]\n\t" \
    "ds_read_b128 v[52:55], v1 offset:3648\n\t" \
    "ds_read_b128 v[56:59], v1 offset:5184\n\t" \
    "v_mfma_f32_16x16x32_bf16 v[32:35], v[80:83], v[96:99], v[32:35]\n\t" \
    "ds_read_b128 v[68:71], v1 offset:11328\n\t" \
    "ds_read_b128 v[72:75], v1 offset:12864\n\t" \
    "s_waitcnt lgkmcnt(9)\n\t" \
    "v_mfma_f32_16x16x32_bf16 v[12:15], v[76:79], v[108:111], v[12:15]\n\t" \
    "v_mfma_f32_16x16x32_bf16 v[28:31], v[80:83], v[108:111], v[28:31]\n\t" \
    "s_waitcnt lgkmcnt(8)\n\t" \
    "v_mfma_f32_16x16x32_bf16 v[16:19], v[76:79], v[112:115], v[16:19]\n\t" \
    "v_mfma_f32_16x16x32_bf16 v[32:35], v[80:83], v[112:115], v[32:35]\n\t" \
    "v_add_u32 v0, 128, v0\n\t" \
    "v_subrev_u32 v1, 192, v1\n\t" \
    "s_sub_u32 s69, s69, 1\n\t" \
    "s_cmp_lg_u32 s69, 0\n\t" \
    "s_cbranch_scc1 cv_c3_%=\n\t" \
    "s_waitcnt lgkmcnt(0)\n\t" \
    "ds_read_b64 v[36:37], v0 offset:0\n\t" \
    "ds_read_b64 v[38:39], v0 offset:8\n\t" \
    "ds_read_b64 v[40:41], v0 offset:32\n\t" \
    "ds_read_b64 v[42:43], v0 offset:40\n\t" \
    "ds_read_b128 v[56:59], v1 offset:5376\n\t" \
    "ds_read_b128 v[72:75], v1 offset:13056\n\t" \
    "ds_read_b64 v[76:77], v0 offset:64\n\t" \
    "ds_read_b64 v[78:79], v0 offset:72\n\t" \
    "ds_read_b64 v[80:81], v0 offset:96\n\t" \
    "ds_read_b64 v[82:83], v0 offset:104\n\t" \
    "ds_read_b128 v[96:99], v1 offset:5280\n\t" \
    "ds_read_b128 v[112:115], v1 offset:12960\n\t" \
    "s_mov_b32 s69, 4\n\t" \
    "cv_c4_%=:\n\t" \
    "s_waitcnt lgkmcnt(7)\n\t" \
    "v_mfma_f32_16x16x32_bf16 v[16:19], v[36:39], v[56:59], v[16:19]\n\t" \
    "ds_read_b64 v[116:117], v0 offset:128\n\t" \
    "ds_read_b64 v[118:119], v0 offset:136\n\t" \
    "v_mfma_f32_16x16x32_bf16 v[32:35], v[40:43], v[56:59], v[32:35]\n\t" \
    "ds_read_b64 v[120:121], v0 offset:160\n\t" \
    "ds_read_b64 v[122:123], v0 offset:168\n\t" \
    "s_waitcnt lgkmcnt(10)\n\t" \
    "v_mfma_f32_16x16x32_bf16 v[16:19], v[36:39], v[72:75], v[16:19]\n\t" \
    "ds_read_b128 v[136:139], v1 offset:5184\n\t" \
    "ds_read_b128 v[152:155], v1 offset:12864\n\t" \
    "v_mfma_f32_16x16x32_bf16 v[32:35], v[40:43], v[72:75], v[32:35]\n\t" \
    "ds_read_b64 v[156:157], v0 offset:192\n\t" \
    "ds_read_b64 v[158:159], v0 offset:200\n\t" \
    "s_waitcnt lgkmcnt(9)\n\t" \
    "v_mfma_f32_16x16x32_bf16 v[16:19], v[76:79], v[96:99], v[16:19]\n\t" \
    "ds_read_b64 v[160:161], v0 offset:224\n\t" \
    "ds_read_b64 v[162:163], v0 offset:232\n\t" \
    "v_mfma_f32_16x16x32_bf16 v[32:35], v[80:83], v[96:99], v[32:35]\n\t" \
    "ds_read_b128 v[176:179], v1 offset:5088\n\t" \
    "ds_read_b128 v[192:195], v1 offset:12768\n\t" \
    "s_waitcnt lgkmcnt(12)\n\t" \
    "v_mfma_f32_16x16x32_bf16 v[16:19], v[76:79], v[112:115], v[16:19]\n\t" \
    "v_mfma_f32_16x16x32_bf16 v[32:35], v[80:83], v[112:115], v[32:35]\n\t" \
    "s_waitcnt lgkmcnt(7)\n\t" \
    "v_mfma_f32_16x16x32_bf16 v[16:19], v[116:119], v[136:139], v[16:19]\n\t" \
    "ds_read_b64 v[36:37], v0 offset:256\n\t" \
    "ds_read_b64 v[38:39], v0 offset:264\n\t" \
    "v_mfma_f32_16x16x32_bf16 v[32:35], v[120:123], v[136:139], v[32:35]\n\t" \
    "ds_read_b64 v[40:41], v0 offset:288\n\t" \
    "ds_read_b64 v[42:43], v0 offset:296\n\t" \
    "s_waitcnt lgkmcnt(10)\n\t" \
    "v_mfma_f32_16x16x32_bf16 v[16:19], v[116:119], v[152:155], v[16:19]\n\t" \
    "ds_read_b128 v[56:59], v1 offset:4992\n\t" \
    "ds_read_b128 v[72:75], v1 offset:12672\n\t" \
    "v_mfma_f32_16x16x32_bf16 v[32:35], v[120:123], v[152:155], v[32:35]\n\t" \
    "ds_read_b64 v[76:77], v0 offset:320\n\t" \
    "ds_read_b64 v[78:79], v0 offset:328\n\t" \
    "s_waitcnt lgkmcnt(9)\n\t" \
    "v_mfma_f32_16x16x32_bf16 v[16:19], v[156:159], v[176:179], v[16:19]\n\t" \
    "ds_read_b64 v[80:81], v0 offset:352\n\t" \
    "ds_read_b64 v[82:83], v0 offset:360\n\t" \
    "v_mfma_f32_16x16x32_bf16 v[32:35], v[160:163], v[176:179], v[32:35]\n\t" \
    "ds_read_b128 v[96:99], v1 offset:4896\n\t" \
    "ds_read_b128 v[112:115], v1 offset:12576\n\t" \
    "s_waitcnt lgkmcnt(12)\n\t" \
    "v_mfma_f32_16x16x32_bf16 v[16:19], v[156:159], v[192:195], v[16:19]\n\t" \
    "v_mfma_f32_16x16x32_bf16 v[32:35], v[160:163], v[192:195], v[32:35]\n\t" \
    "v_add_u32 v0, 256, v0\n\t" \
    "v_subrev_u32 v1, 384, v1\n\t" \
    "s_sub_u32 s69, s69, 1\n\t" \
    "s_cmp_lg_u32 s69, 0\n\t" \
    "s_cbranch_scc1 cv_c4_%=\n\t" \
    "s_waitcnt lgkmcnt(0)\n\t" \
    "s_nop 7\n\t" \
    "ds_write_b128 v2, v[4:7] offset:0\n\t" \
    "ds_write_b128 v2, v[8:11] offset:1024\n\t" \
    "ds_write_b128 v2, v[12:15] offset:2048\n\t" \
    "ds_write_b128 v2, v[16:19] offset:3072\n\t" \
    "ds_write_b128 v2, v[20:23] offset:4096\n\t" \
    "ds_write_b128 v2, v[24:27] offset:5120\n\t" \
    "ds_write_b128 v2, v[28:31] offset:6144\n\t" \
    "ds_write_b128 v2, v[32:35] offset:7168\n\t" \
    "s_waitcnt lgkmcnt(0)\n\t" \
    "cv_last_%=:\n\t" \
    ""
#define ASM_CLOB "v0", "v1", "v2", "v3", "v4", "v5", "v6", "v7", "v8", "v9", "v10", "v11", "v12", "v13", "v14", "v15", "v16", "v17", "v18", "v19", "v20", "v21", "v22", "v23", "v24", "v25", "v26", "v27", "v28", "v29", "v30", "v31", "v32", "v33", "v34", "v35", "v36", "v37", "v38", "v39", "v40", "v41", "v42", "v43", "v44", "v45", "v46", "v47", "v48", "v49", "v50", "v51", "v52", "v53", "v54", "v55", "v56", "v57", "v58", "v59", "v60", "v61", "v62", "v63", "v64", "v65", "v66", "v67", "v68", "v69", "v70", "v71", "v72", "v73", "v74", "v75", "v76", "v77", "v78", "v79", "v80", "v81", "v82", "v83", "v84", "v85", "v86", "v87", "v88", "v89", "v90", "v91", "v92", "v93", "v94", "v95", "v96", "v97", "v98", "v99", "v100", "v101", "v102", "v103", "v104", "v105", "v106", "v107", "v108", "v109", "v110", "v111", "v112", "v113", "v114", "v115", "v116", "v117", "v118", "v119", "v120", "v121", "v122", "v123", "v124", "v125", "v126", "v127", "v128", "v129", "v130", "v131", "v132", "v133", "v134", "v135", "v136", "v137", "v138", "v139", "v140", "v141", "v142", "v143", "v144", "v145", "v146", "v147", "v148", "v149", "v150", "v151", "v152", "v153", "v154", "v155", "v156", "v157", "v158", "v159", "v160", "v161", "v162", "v163", "v164", "v165", "v166", "v167", "v168", "v169", "v170", "v171", "v172", "v173", "v174", "v175", "v176", "v177", "v178", "v179", "v180", "v181", "v182", "v183", "v184", "v185", "v186", "v187", "v188", "v189", "v190", "v191", "v192", "v193", "v194", "v195", "v196", "v197", "v198", "v199", "v200", "v201", "v202", "v203", "v204", "v205", "v206", "v207", "v208", "v209", "v210", "v211", "v212", "v213", "v214", "v215", "v222", "v223", "v224", "v225", "v226", "v227", "v228", "v229", "v230", "v231", "v232", "v233", "v234", "v235", "v236", "v237", "v238", "v239", "v240", "v241", "v242", "v243", "v244", "v245", "v246", "v247", "v248", "v249", "v250", "v251", "v252", "v253", "v254", "v255", "a0", "a1", "a2", "a3", "a4", "a5", "a6", "a7", "a8", "a9", "a10", "a11", "a12", "a13", "a14", "a15", "a16", "a17", "a18", "a19", "a20", "a21", "a22", "a23", "a24", "a25", "a26", "a27", "a28", "a29", "a30", "a31", "a32", "a33", "a34", "a35", "a36", "a37", "a38", "a39", "a40", "a41", "a42", "a43", "a44", "a45", "a46", "a47", "a48", "a49", "a50", "a51", "a52", "a53", "a54", "a55", "a56", "a57", "a58", "a59", "a60", "a61", "a62", "a63", "a64", "a65", "a66", "a67", "a68", "a69", "a70", "a71", "a72", "a73", "a74", "a75", "a76", "a77", "a78", "a79", "a80", "a81", "a82", "a83", "a84", "a85", "a86", "a87", "a88", "a89", "a90", "a91", "a92", "a93", "a94", "a95", "a96", "a97", "a98", "a99", "a100", "a101", "a102", "a103", "a104", "a105", "a106", "a107", "a108", "a109", "a110", "a111", "a112", "a113", "a114", "a115", "a116", "a117", "a118", "a119", "a120", "a121", "a122", "a123", "a124", "a125", "a126", "a127", "a128", "a129", "a130", "a131", "a132", "a133", "a134", "a135", "a136", "a137", "a138", "a139", "a140", "a141", "a142", "a143", "a144", "a145", "a146", "a147", "a148", "a149", "a150", "a151", "a152", "a153", "a154", "a155", "a156", "a157", "a158", "a159", "a160", "a161", "a162", "a163", "a164", "a165", "a166", "a167", "a168", "a169", "a170", "a171", "a172", "a173", "a174", "a175", "a176", "a177", "a178", "a179", "a180", "a181", "a182", "a183", "a184", "a185", "a186", "a187", "a188", "a189", "a190", "a191", "a192", "a193", "a194", "a195", "a196", "a197", "a198", "a199", "a200", "a201", "a202", "a203", "a204", "a205", "a206", "a207", "a208", "a209", "a210", "a211", "a212", "a213", "a214", "a215", "a216", "a217", "a218", "a219", "a220", "a221", "a222", "a223", "a224", "a225", "a226", "a227", "a228", "a229", "a230", "a231", "a232", "a233", "a234", "a235", "a236", "a237", "a238", "a239", "a240", "a241", "a242", "a243", "a244", "a245", "a246", "a247", "a248", "a249", "a250", "a251", "a252", "a253", "a254", "a255", "s33", "s34", "s35", "s36", "s37", "s38", "s39", "s40", "s41", "s42", "s43", "s44", "s45", "s46", "s47", "s48", "s49", "s50", "s51", "s52", "s53", "s54", "s55", "s56", "s57", "s58", "s59", "s60", "s61", "s62", "s63", "s64", "s65", "s66", "s67", "s68", "s69", "s70", "s71", "s72", "s73", "s74", "s75", "s76", "s77", "s78", "s79", "s80", "s81", "s82", "s83", "s84", "s85", "s86", "s87", "s88", "s89", "s90", "s91", "s92", "s93", "s94", "s95", "s96", "s97", "s98", "s99", "vcc", "scc", "memory"
#define CV_CLOB "v0", "v1", "v2", "v3", "v4", "v5", "v6", "v7", "v8", "v9", "v10", "v11", "v12", "v13", "v14", "v15", "v16", "v17", "v18", "v19", "v20", "v21", "v22", "v23", "v24", "v25", "v26", "v27", "v28", "v29", "v30", "v31", "v32", "v33", "v34", "v35", "v36", "v37", "v38", "v39", "v40", "v41", "v42", "v43", "v44", "v45", "v46", "v47", "v48", "v49", "v50", "v51", "v52", "v53", "v54", "v55", "v56", "v57", "v58", "v59", "v60", "v61", "v62", "v63", "v64", "v65", "v66", "v67", "v68", "v69", "v70", "v71", "v72", "v73", "v74", "v75", "v76", "v77", "v78", "v79", "v80", "v81", "v82", "v83", "v84", "v85", "v86", "v87", "v88", "v89", "v90", "v91", "v92", "v93", "v94", "v95", "v96", "v97", "v98", "v99", "v100", "v101", "v102", "v103", "v104", "v105", "v106", "v107", "v108", "v109", "v110", "v111", "v112", "v113", "v114", "v115", "v116", "v117", "v118", "v119", "v120", "v121", "v122", "v123", "v124", "v125", "v126", "v127", "v128", "v129", "v130", "v131", "v132", "v133", "v134", "v135", "v136", "v137", "v138", "v139", "v140", "v141", "v142", "v143", "v144", "v145", "v146", "v147", "v148", "v149", "v150", "v151", "v152", "v153", "v154", "v155", "v156", "v157", "v158", "v159", "v160", "v161", "v162", "v163", "v164", "v165", "v166", "v167", "v168", "v169", "v170", "v171", "v172", "v173", "v174", "v175", "v176", "v177", "v178", "v179", "v180", "v181", "v182", "v183", "v184", "v185", "v186", "v187", "v188", "v189", "v190", "v191", "v192", "v193", "v194", "v195", "s69", "scc", "memory"
static constexpr unsigned G_STEP = 0x80000;
static constexpr unsigned G_CPY = 0x10000;
static constexpr unsigned G_HOFF = 0x40000;
static constexpr unsigned G_RM = 0x4;
static constexpr unsigned G_RP = 0x2;
static constexpr unsigned G_HP = 0x20;
static constexpr unsigned G_HKB = 0x8;
static constexpr unsigned G_RHO = 0x1400;
static constexpr unsigned G_RHS = 0x600;
static constexpr unsigned G_NARM = 0x3;
static constexpr unsigned G_WS_UM = 0x400000;
static constexpr unsigned G_WS_CTL = 0x5d0000;
static constexpr unsigned G_WS_DONE = 0x5f0000;
static constexpr unsigned G_WS_EP = 0x5f2000;
static constexpr unsigned G_WS_STAMPS = 0xc000000;
static constexpr unsigned G_C_TASK = 0x200;
static constexpr unsigned G_C_GX = 0x1000;
static constexpr unsigned G_C_GV = 0x4000;
static constexpr unsigned G_C_KM = 0x8000;
static constexpr unsigned G_C_UM = 0x9000;
static constexpr unsigned G_C_PC = 0xc000;
static constexpr unsigned G_C_UT = 0xd000;
static constexpr unsigned G_C_GK = 0x10000;
static constexpr unsigned G_C_YC = 0x11000;
static constexpr unsigned G_C_ERR = 0x100;
static constexpr unsigned G_C_ARR = 0x14000;
static constexpr unsigned G_C_KG = 0x14100;
static constexpr unsigned G_C_END = 0x1a000;
static constexpr unsigned G_CTF = 0x2000;
static constexpr unsigned G_HS8 = 0x80000;
static constexpr unsigned G_HS4 = 0x40000;
static constexpr unsigned G_UXS = 0x200000;
static constexpr unsigned G_UMH = 0xa0000;
static constexpr unsigned G_D_QX = 0x600000;
static constexpr unsigned G_D_KX = 0x1600000;
static constexpr unsigned G_D_KM = 0x2600000;
static constexpr unsigned G_D_UX = 0x2e00000;
static constexpr unsigned G_D_UMX = 0x7600000;
static constexpr unsigned G_D_PX = 0x8c00000;
static constexpr unsigned G_D_PM = 0x9000000;
static constexpr unsigned G_D_KT = 0x9100000;
static constexpr unsigned G_D_UT = 0x9600000;
static constexpr unsigned G_D_YB = 0x9900000;
static constexpr unsigned G_D_END = 0x9c00000;
static constexpr unsigned G_SKM = 0x7;
static constexpr unsigned G_GVN = 0x2;
static constexpr unsigned G_STG = 0x9000;
static constexpr unsigned G_KROW = 0x30;
#define FLG(x_) (kDefault[5])
static constexpr int kDefault[8] = {PRM_DEFAULT};
#define MFMA32(a_, b_, c_) __builtin_amdgcn_mfma_f32_16x16x32_bf16(__builtin_bit_cast(bf16x8, a_), __builtin_bit_cast(bf16x8, b_), c_, 0, 0, 0)
#define MFMA16(a_, b_, c_) __builtin_amdgcn_mfma_f32_16x16x16bf16_1k(__builtin_bit_cast(s16x4, a_), __builtin_bit_cast(s16x4, b_), c_, 0, 0, 0)
#define WLD8(d_, voff_, base_, off_) asm volatile("s_nop 4\n\tglobal_load_dwordx2 %0, %1, %2 offset:%3 sc1" : "=v"(d_) : "v"(voff_), "s"(base_), "i"(off_) : "memory")
#define WLD16(d_, voff_, base_, off_) asm volatile("s_nop 4\n\tglobal_load_dwordx4 %0, %1, %2 offset:%3 sc1" : "=v"(d_) : "v"(voff_), "s"(base_), "i"(off_) : "memory")
#define WST16(voff_, d_, base_, off_) asm volatile("s_nop 4\n\tglobal_store_dwordx4 %0, %1, %2 offset:%3 sc1\n\ts_nop 1" :: "v"(voff_), "v"(d_), "s"(base_), "i"(off_) : "memory")
#define WLD16X4(d0, d1, d2, d3, voff_, base_) asm volatile("s_nop 4\n\tglobal_load_dwordx4 %0, %4, %5 sc1\n\tglobal_load_dwordx4 %1, %4, %5 offset:1024 sc1\n\t" \
    "global_load_dwordx4 %2, %4, %5 offset:2048 sc1\n\tglobal_load_dwordx4 %3, %4, %5 offset:3072 sc1" \
    : "=&v"(d0), "=&v"(d1), "=&v"(d2), "=&v"(d3) : "v"(voff_), "s"(base_) : "memory")

__device__ __forceinline__ u32x4 pack8(const f32x4& a, const f32x4& b) {
    return u32x4{pk2bf(a[0], a[1]), pk2bf(a[2], a[3]), pk2bf(b[0], b[1]), pk2bf(b[2], b[3])};
}
template <class T> __device__ __forceinline__ T* uni(T* q) {
    const u64 v = (u64)q;
    unsigned lo = __builtin_amdgcn_readfirstlane((unsigned)v), hi = __builtin_amdgcn_readfirstlane((unsigned)(v >> 32));
    asm volatile("s_nop 4" : "+s"(lo), "+s"(hi));
    return (T*)(((u64)hi << 32) | lo);
}
#define CTLO (G_WS_CTL + (unsigned)p.prm[0])
template <bool FAST = false>
__device__ __forceinline__ void wait_word(const MainP& p, const char* f, int want, unsigned code) {
    unsigned tries = 0;
    while (__any((int)__hip_atomic_load((const gu32*)f, RLX_AGENT) < want)) {
        if (++tries > kWaitCap) { __hip_atomic_store((gu32*)(p.ws + G_WS_CTL + G_C_ERR), code, RLX_AGENT); break; }
        if ((tries & 1023u) == 0u && __hip_atomic_load((const gu32*)(p.ws + G_WS_CTL + G_C_ERR), RLX_AGENT) != 0xffffffffu) break;
        if (FAST) __builtin_amdgcn_s_sleep(1); else __builtin_amdgcn_s_sleep(10);
    }
}

__device__ __forceinline__ int skew(int s, int id) { return (s ^ (G_SKM * id)) & 63; }
__device__ __forceinline__ unsigned umx_base(int h) { return G_D_UMX + (h / 3) * 0x200000 + (h % 3) * G_UMH; }
typedef GAS unsigned long long gu64;
__device__ __forceinline__ void init_done(char* ws, int slot, unsigned val) {
    const unsigned e = __hip_atomic_load((const gu32*)(ws + G_WS_EP), RLX_AGENT);
    __hip_atomic_store((gu64*)(ws + G_WS_DONE + slot * 64), ((u64)val << 32) | (e + 1u), RLX_AGENT);
}
__device__ __forceinline__ void init_wait(char* ws, int w, int lane) {
    const unsigned e = __hip_atomic_load((const gu32*)(ws + G_WS_EP), RLX_AGENT);
    for (unsigned tries = 0; tries < kWaitCap; ++tries) {
        if (!__any(__hip_atomic_load((const gu32*)(ws + G_WS_DONE + min(lane, 59) * 64), RLX_AGENT) != e + 1u)) break;
        __builtin_amdgcn_s_sleep(8);
    }
    __syncthreads();
    if (w == 0 && lane == 0) __hip_atomic_fetch_add((gu32*)(ws + G_WS_CTL + G_C_ARR), 1u, RLX_AGENT);
}
__device__ __forceinline__ void fill_block(char* ws, int b, int tid, const u32x4& v0, const u32x4& v1) {
    const unsigned o = b * 8192 + tid * 16, o2 = o + 4096;
    asm volatile("global_store_dwordx4 %0, %1, %2 sc1\n\ts_nop 1" :: "v"(o), "v"(v0), "s"(ws) : "memory");
    asm volatile("global_store_dwordx4 %0, %1, %2 sc1\n\ts_nop 1" :: "v"(o2), "v"(v1), "s"(ws) : "memory");
}
__device__ __forceinline__ void init_fill(char* ws, const u16* mem0, int f, int tid) {
    const u32x4 sent = {0xffffffffu, 0xffffffffu, 0xffffffffu, 0xffffffffu};
    u32x4 v0 = sent, v1 = sent;
    if (f < 16) {
#pragma unroll
        for (int k = 0; k < 2; ++k) {
            const int q = tid + 256 * k + ((f & 2) ? 256 : 0), m = q & 15;
            if (!(f & 2) || (q >= 512 && q < 576)) {
                const unsigned* src = (const unsigned*)(mem0 + (2 * (m >> 2) + (m & 1)) * DM + 16 * (q >> 4) + (m & 2));
                const u32x4 o = {min(src[0], 0xfffffffeu), min(src[2], 0xfffffffeu), min(src[4], 0xfffffffeu), min(src[6], 0xfffffffeu)};
                if (k) v1 = o; else v0 = o;
            }
        }
    }
    constexpr int HB2 = 2 * ((G_HKB + 7) / 8), NH = 32 + 8 * G_RP, NP = NH + 8 * HB2;
#pragma unroll 1
    for (int q = f; q < NP + 144 + (int)(G_C_END >> 13); q += 48) {
        const int u = q - 32, y = q - NH;
        fill_block(ws, q < 32 ? (q >> 4) * 256 + ((q >> 2) & 3) * 8 + 4 + (q & 3) : q < NH ? ((u >> 2) & 1) * 256 + (u >> 3) * 8 + (u & 3) : q < NP ? (y & 1) * 256 + 32 + ((y >> 1) & 3) * 8 + (y >> 3) : q < NP + 144 ? q - NP + 512 : q - NP - 144 + (int)(G_WS_CTL >> 13), tid, v0, v1);
        v0 = sent; v1 = sent;
    }
    const unsigned e = __hip_atomic_load((const gu32*)(ws + G_WS_EP), RLX_AGENT);
    asm volatile("s_waitcnt vmcnt(0)" :: "v"(e) : "memory");
    __syncthreads();
    if (tid == 0) __hip_atomic_store((gu64*)(ws + G_WS_DONE + (12 + f) * 64), (u64)(e + 1u), RLX_AGENT);
}
__device__ __forceinline__ void init_cal(char* ws, char* smem, int id, int tid, int n) {
    const int sl = id % 6;
    char* a = ws + (size_t)(sl >> 1) * 0x200000 + 0x1B0000 + 49152 + (sl & 1) * 8192 + 5120 + (id / 6) * 65536 + (tid >> 6) * 768;
    const u32x4 z = {0u, 0u, 0u, 0u};
    if ((tid & 63) < 48) asm volatile("global_store_dwordx4 %0, %1, off sc1\n\ts_waitcnt vmcnt(0)" :: "v"(a + (tid & 63) * 16), "v"(z) : "memory");
    if ((tid & 63) == 0) {
        unsigned best = 0xfffffffeu;
#pragma unroll 1
        for (int it = 0; it < n; ++it) {
            const unsigned c0 = (unsigned)__builtin_amdgcn_s_memtime();
            const unsigned v = __hip_atomic_fetch_or((gu32*)(a + it * 128), 0u, RLX_AGENT);
            asm volatile("s_waitcnt vmcnt(0)" :: "v"(v) : "memory");
            best = min(best, (unsigned)__builtin_amdgcn_s_memtime() - c0);
        }
        ((unsigned*)smem)[tid >> 6] = best;
    }
    __syncthreads();
    if (tid == 0) { const u32x4 b = *(const u32x4*)smem; init_done(ws, id, min(min(b[0], b[1]), min(b[2], b[3]))); }
    __syncthreads();
}

#define TST(slot_, i_)
#define PIN8(a) asm volatile("" : "+v"(a[0]), "+v"(a[1]), "+v"(a[2]), "+v"(a[3]), "+v"(a[4]), "+v"(a[5]), "+v"(a[6]), "+v"(a[7]))
struct AOps { u32x4 q[4], k[8]; int fl; };
struct APtr { const char* q; const char* k; const char* m; unsigned voff, vkm, voffm, vfl; };
__device__ __forceinline__ void attn_batch(const MainP& p, AOps& o, const APtr& a) {
    const char* q2 = a.q + 4096; const char* k2 = a.k + 4096;
    WLD16(o.q[0], a.voff, a.q, 0); WLD16(o.q[1], a.voff, a.q, 2048); WLD16(o.q[2], a.voff, q2, 0); WLD16(o.q[3], a.voff, q2, 2048);
    WLD16X4(o.k[0], o.k[1], o.k[2], o.k[3], a.voff, a.k); WLD16X4(o.k[4], o.k[5], o.k[6], o.k[7], a.voff, k2);
    asm volatile("global_load_dword %0, %1, %2 sc1" : "=v"(o.fl) : "v"(a.vfl), "s"(p.ws) : "memory");
}
__device__ __forceinline__ void attn_pin(AOps& o) {
    asm volatile("" : "+v"(o.fl), "+v"(o.q[0]), "+v"(o.q[1]), "+v"(o.q[2]), "+v"(o.q[3])); PIN8(o.k);
}
static constexpr int NFINE = 6;
static constexpr int NG1 = 64 - NFINE;
__device__ __forceinline__ void score_round4(float* r, const f32x4& x) {
    asm volatile("v_cvt_pk_bf16_f32 %0, 0, %4\n\tv_cvt_pk_bf16_f32 %1, 0, %5\n\tv_cvt_pk_bf16_f32 %2, 0, %6\n\tv_cvt_pk_bf16_f32 %3, 0, %7\n\t"
                 "v_mul_f32 %0, 0x3db504f3, %0\n\tv_mul_f32 %1, 0x3db504f3, %1\n\tv_mul_f32 %2, 0x3db504f3, %2\n\tv_mul_f32 %3, 0x3db504f3, %3\n\t"
                 "v_cvt_pk_bf16_f32 %0, 0, %0\n\tv_cvt_pk_bf16_f32 %1, 0, %1\n\tv_cvt_pk_bf16_f32 %2, 0, %2\n\tv_cvt_pk_bf16_f32 %3, 0, %3"
                 : "=&v"(r[0]), "=&v"(r[1]), "=&v"(r[2]), "=&v"(r[3]) : "v"(x[0]), "v"(x[1]), "v"(x[2]), "v"(x[3]));
}
__device__ __forceinline__ float vmax3(float a, float b, float c) { float r; asm("v_max3_f32 %0, %1, %2, %3" : "=v"(r) : "v"(a), "v"(b), "v"(c)); return r; }
template <int N>
__device__ __forceinline__ void attn_soft(float (&q)[12], u32x2& Pm, u32x4& Px) {
    float m = vmax3(q[0], q[1], q[2]);
#pragma unroll
    for (int i = 3; i < N; i += 2) m = vmax3(m, q[i], q[i + 1 < N ? i + 1 : i]);
    { auto a = __builtin_amdgcn_permlane16_swap(__float_as_uint(m), __float_as_uint(m), false, false); m = vmax3(m, __uint_as_float(a[0]), __uint_as_float(a[1])); }
    { auto a = __builtin_amdgcn_permlane32_swap(__float_as_uint(m), __float_as_uint(m), false, false); m = vmax3(m, __uint_as_float(a[0]), __uint_as_float(a[1])); }
    const float ml = -m * 1.4426950408889634f;
#pragma unroll
    for (int i = 0; i < N; ++i) q[i] = __builtin_amdgcn_exp2f(fmaf(q[i], 1.4426950408889634f, ml));
    float sum = 0.5f * ((q[0] + q[1]) + (q[2] + q[3])) + ((q[4] + q[5]) + (q[6] + q[7]));
    if (N > 8) sum += (q[8] + q[9]) + (q[10] + q[11]);
    sum = rowsum4(sum);
    const float inv = __builtin_amdgcn_rcpf(sum);
    Pm = u32x2{pk2bf(q[0] * inv, q[1] * inv), pk2bf(q[2] * inv, q[3] * inv)};
    Px = u32x4{pk2bf(q[4] * inv, q[5] * inv), pk2bf(q[6] * inv, q[7] * inv), N > 8 ? pk2bf(q[8] * inv, q[9] * inv) : 0u, N > 8 ? pk2bf(q[10] * inv, q[11] * inv) : 0u};
}
#define NASPLIT 28
static constexpr int NA = NASPLIT;
__device__ __forceinline__ int attn_next(int s) { return s < NA ? s + 1 : s + 2; }
__device__ __forceinline__ unsigned attn_flag(const MainP& p, int s, int hq) { return CTLO + G_C_PC + ((s > NA && (s & 1)) ? 1024 : 0) + hq * 128; }
struct BOps { u32x4 q[8], k[8]; };
#define WLD16X4A(d0, d1, d2, d3, voff_, base_) asm volatile("s_nop 4\n\tglobal_load_dwordx4 %0, %4, %5 sc1\n\tglobal_load_dwordx4 %1, %4, %5 offset:1024 sc1\n\t" \
    "global_load_dwordx4 %2, %4, %5 offset:2048 sc1\n\tglobal_load_dwordx4 %3, %4, %5 offset:3072 sc1" \
    : "=&a"(d0), "=&a"(d1), "=&a"(d2), "=&a"(d3) : "v"(voff_), "s"(base_) : "memory")
#define PIN8A(a) asm volatile("" : "+a"(a[0]), "+a"(a[1]), "+a"(a[2]), "+a"(a[3]), "+a"(a[4]), "+a"(a[5]), "+a"(a[6]), "+a"(a[7]))
#define MFMA_AV0(acc, a_, b_) asm volatile("v_mfma_f32_16x16x32_bf16 %0, %1, %2, 0" : "=&v"(acc) : "a"(a_), "v"(b_))
#define MFMA_AV(acc, a_, b_) asm volatile("v_mfma_f32_16x16x32_bf16 %0, %1, %2, %0" : "+v"(acc) : "a"(a_), "v"(b_))
__device__ __forceinline__ void attn_batch8(const MainP& p, BOps& o, const APtr& a) {
    const char* q2 = a.q + 4096; const char* k2 = a.k + 4096;
    WLD16X4(o.q[0], o.q[1], o.q[2], o.q[3], a.voff, a.q); WLD16X4(o.q[4], o.q[5], o.q[6], o.q[7], a.voff, q2);
    WLD16X4A(o.k[0], o.k[1], o.k[2], o.k[3], a.voff, a.k); WLD16X4A(o.k[4], o.k[5], o.k[6], o.k[7], a.voff, k2);
}
__device__ __forceinline__ void attn_step(const MainP& p, BOps& o, BOps& n1, BOps& n2, APtr& a, int& kg, int& kk, int s, int h, int w, int lane) {
    const int g = lane >> 4, c = lane & 15;
    const float scale = 0.08838834764831845f;
    TST(s * 64 + h * 2, 0);
    const int s2 = attn_next(attn_next(s));
    if (kg <= s2 || kk < s) {
        VMC0();
        wait_word<true>(p, p.ws + a.vfl, (lane & 1) ? s : s2 + 1, 13u);
    }
    TST(s * 64 + h * 2, 1);
    a.q = p.ws + G_D_QX + (size_t)h * G_HS8 + skew(s2, h) * 8192; a.k = a.q + (G_D_KX - G_D_QX); a.m = p.ws + G_D_KM + (size_t)h * G_HS4 + skew(s, h) * 4096;
    __builtin_amdgcn_sched_barrier(0);
    u32x4 km[4];
    int fl;
    WLD16X4A(km[0], km[1], km[2], km[3], a.vkm, a.m);
    asm volatile("global_load_dword %0, %1, %2 sc1" : "=v"(fl) : "v"(a.vfl), "s"(p.ws) : "memory");
    attn_batch8(p, n2, a);
    f32x4 y[5];
#pragma unroll
    for (int wc = 0; wc < 4; ++wc)
#pragma unroll
        for (int i = 0; i < 3; ++i) { if (wc == 0) MFMA_AV0(y[i], o.k[i >> 1], o.q[(i + 1) >> 1]); else MFMA_AV(y[i], o.k[wc * 2 + (i >> 1)], o.q[wc * 2 + ((i + 1) >> 1)]); }
    asm volatile("s_nop 7" : "+v"(y[0]), "+v"(y[1]), "+v"(y[2]));
    float q0[12], q1[12];
    score_round4(q0 + 4, y[0]); score_round4(q1 + 4, y[1]); score_round4(q1 + 8, y[2]);
#pragma unroll
    for (int i = 0; i < 4; ++i) {
        const bool vis = 4 * g + i <= c;
        q0[4 + i] = vis ? q0[4 + i] : -INFINITY; q1[8 + i] = vis ? q1[8 + i] : -INFINITY;
    }
    __builtin_amdgcn_sched_barrier(0);
    asm volatile("s_waitcnt vmcnt(16)\n\ts_barrier" : "+a"(km[0]), "+a"(km[1]), "+a"(km[2]), "+a"(km[3]), "+v"(fl) :: "memory");
    PIN8(n1.q); PIN8A(n1.k);
    { const unsigned fo = attn_flag(p, s, h >> 2); const int fv = s - 1;
      asm volatile("s_cmp_lg_u32 %3, 0\n\ts_cbranch_scc1 1f\n\tglobal_store_dword %0, %1, %2 sc1\n1:" :: "v"(fo), "v"(fv), "s"(p.ws), "s"(w) : "scc", "memory"); }
#pragma unroll
    for (int wc = 0; wc < 4; ++wc)
#pragma unroll
        for (int i = 0; i < 2; ++i) { if (wc == 0) MFMA_AV0(y[3 + i], km[0], o.q[i]); else MFMA_AV(y[3 + i], km[wc], o.q[wc * 2 + i]); }
    asm volatile("s_nop 7" : "+v"(y[3]), "+v"(y[4]));
    kg = __builtin_amdgcn_readlane(fl, 0); kk = __builtin_amdgcn_readlane(fl, 1);
    score_round4(q0, y[3]); score_round4(q1, y[4]);
    u32x2 Pm0, Pm1; u32x4 Px0, Px1;
    attn_soft<8>(q0, Pm0, Px0); attn_soft<12>(q1, Pm1, Px1);
    char* pxb = p.ws + G_D_PX + (size_t)(s * 32 + h) * 2048;
    char* pmb = p.ws + G_D_PM + (size_t)s * 16384 + h * 256; char* pm2 = pmb + 8192;
    WST16(a.voff, Px0, pxb, 0); WST16(a.voff, Px1, pxb, 1024);
    asm volatile("global_store_dwordx2 %0, %1, %3 sc1\n\tglobal_store_dwordx2 %0, %2, %4 sc1" :: "v"(a.voffm), "v"(Pm0), "v"(Pm1), "s"(pmb), "s"(pm2) : "memory");
    { const unsigned fo = attn_flag(p, s, h >> 2);
      asm volatile("s_cmp_lt_u32 %4, 46\n\ts_cbranch_scc1 1f\n\ts_waitcnt vmcnt(0)\n\ts_barrier\n\ts_cmp_lg_u32 %3, 0\n\ts_cbranch_scc1 1f\n\tglobal_store_dword %0, %1, %2 sc1\n1:"
                   :: "v"(fo), "v"(s), "s"(p.ws), "s"(w), "s"(s) : "scc", "memory"); }
    TST(s * 64 + h * 2, 2);
}
__device__ __forceinline__ void attn_role(const MainP& p, int s, int h, int w, int lane) {
    APtr a;
    a.q = p.ws + G_D_QX + (size_t)h * G_HS8 + skew(s, h) * 8192; a.k = a.q + (G_D_KX - G_D_QX);
    a.voff = lane * 16; a.vkm = (lane & 55) * 16; a.voffm = (lane & 15) * 16 + ((lane >> 4) & 1) * 8;
    a.vfl = CTLO + ((lane & 1) ? G_C_KM : G_C_GX) + h * 128;
    BOps o0, o1, o2;
    wait_word(p, p.ws + CTLO + G_C_GX + h * 128, attn_next(s) + 1, 10u);
    attn_batch8(p, o0, a);
    a.q = p.ws + G_D_QX + (size_t)h * G_HS8 + skew(attn_next(s), h) * 8192; a.k = a.q + (G_D_KX - G_D_QX);
    attn_batch8(p, o1, a);
    VMC0();
    PIN8(o0.q); PIN8A(o0.k); PIN8(o1.q); PIN8A(o1.k);
    int kg = -1, kk = -1;
    static_assert(NA % 2 == 0 && NG1 + 3 < NSEG, "ranges");
#pragma unroll 1
    for (;;) {
        attn_step(p, o0, o1, o2, a, kg, kk, s, h, w, lane);
        if ((s = attn_next(s)) >= NG1) break;
        attn_step(p, o1, o2, o0, a, kg, kk, s, h, w, lane);
        if ((s = attn_next(s)) >= NG1) break;
        attn_step(p, o2, o0, o1, a, kg, kk, s, h, w, lane);
        if ((s = attn_next(s)) >= NG1) break;
    }
    VMC0();
}
__device__ __forceinline__ unsigned umax4(const u32x4& v) { return max(max(v[0], v[1]), max(v[2], v[3])); }
__device__ __forceinline__ bool give_up(const MainP& p, unsigned& tries, unsigned code) {
    if (++tries <= kWaitCap / 4) return false;
    __hip_atomic_store((gu32*)(p.ws + G_WS_CTL + G_C_ERR), code, RLX_AGENT);
    return true;
}
__device__ __forceinline__ void task_attn_tail(const MainP& p, int s, int h, int w, int lane) {
    const int g = lane >> 4, c = lane & 15, qt = w & 1;
    const float scale = 0.08838834764831845f;
    APtr a;
    a.q = p.ws + G_D_QX + (size_t)h * G_HS8 + skew(s, h) * 8192 + qt * 1024; a.k = p.ws + G_D_KX + (size_t)h * G_HS8 + skew(s, h) * 8192; a.m = p.ws + G_D_KM + (size_t)h * G_HS4 + skew(s, h) * 4096;
    a.voff = lane * 16; a.vkm = (lane & 55) * 16; a.voffm = c * 16 + (g & 1) * 8; a.vfl = G_WS_CTL;
    AOps o;
    wait_word(p, p.ws + CTLO + G_C_GX + h * 128, s + 1, 14u);
    attn_batch(p, o, a);
    VMC0();
    attn_pin(o);
    float q[12];
    u32x4 km[4];
#pragma unroll
    for (int kt = 1; kt >= -1; --kt) {
        if (kt < 0) {
            TST(s * 64 + h * 2 + qt, 0);
            wait_word(p, p.ws + CTLO + G_C_KM + h * 128, s - 2, 15u);
            if (FLG(p.prm[5]) & 2) wait_word<true>(p, p.ws + CTLO + G_C_KM + h * 128, s - 1, 15u);
            for (unsigned tries = 0;;) {
                WLD16X4(km[0], km[1], km[2], km[3], a.vkm, a.m);
                asm volatile("s_waitcnt vmcnt(0)" : "+v"(km[0]), "+v"(km[1]), "+v"(km[2]), "+v"(km[3]) :: "memory");
                if (!__any(max(max(umax4(km[0]), umax4(km[1])), max(umax4(km[2]), umax4(km[3]))) == 0xffffffffu) || give_up(p, tries, 16u)) break;
            }
            TST(s * 64 + h * 2 + qt, 1);
        }
        f32x4 y = {0.f, 0.f, 0.f, 0.f};
#pragma unroll
        for (int wc = 0; wc < 4; ++wc) y = MFMA32(kt < 0 ? km[wc] : o.k[wc * 2 + (kt < 0 ? 0 : kt)], o.q[wc], y);
#pragma unroll
        for (int i = 0; i < 4; ++i) q[4 + 4 * kt + i] = (16 * kt + 4 * g + i <= 16 * qt + c) ? bfr(bfr(y[i]) * scale) : -INFINITY;
    }
    u32x2 Pm; u32x4 Px;
    attn_soft<12>(q, Pm, Px);
    char* pxb = p.ws + G_D_PX + (size_t)(s * 32 + h) * 2048 + qt * 1024;
    char* pmb = p.ws + G_D_PM + (size_t)s * 16384 + h * 256 + qt * 8192;
    WST16(a.voff, Px, pxb, 0);
    asm volatile("global_store_dwordx2 %0, %1, %2 sc1" :: "v"(a.voffm), "v"(Pm), "s"(pmb) : "memory");
    TST(s * 64 + h * 2 + qt, 2);
}

#define RLD(d_, voff_, base_, off_) asm volatile("global_load_dwordx4 %0, %1, %2 offset:%3 sc1" : "=v"(d_) : "v"(voff_), "s"(base_), "i"(off_) : "memory")
#define RLDA(d_, voff_, base_, off_) asm volatile("global_load_dwordx4 %0, %1, %2 offset:%3 sc1" : "=a"(d_) : "v"(voff_), "s"(base_), "i"(off_) : "memory")
#define MFMA_ACC_A(acc, a_, b_) asm volatile("v_mfma_f32_16x16x32_bf16 %0, %1, %2, %0" : "+v"(acc) : "a"(a_), "v"(b_))
#define VMW(n_) asm volatile("s_waitcnt vmcnt(%0)" :: "i"(n_) : "memory")
template <int NT, int DEPTH, bool TAIL>
__device__ __forceinline__ void task_out(const MainP& p, char* smem, int s, int ct0, int w, int lane) {
    const int g = lane >> 4, c = lane & 15;
    const unsigned voff = lane * 16;
    f32x4 acc[NT][2];
#pragma unroll
    for (int t = 0; t < NT; ++t) { acc[t][0] = f32x4{0.f, 0.f, 0.f, 0.f}; acc[t][1] = acc[t][0]; asm volatile("" : "+v"(acc[t][0]), "+v"(acc[t][1])); }
    const char* ub[NT];
#pragma unroll
    for (int t = 0; t < NT; ++t) ub[t] = uni(p.ws + G_D_UX + (size_t)(ct0 + t) * G_UXS + skew(s, ct0 + t) * 32768 + w * 8192);
    const char* pb = uni(p.ws + G_D_PX + (size_t)(s * 32 + w * 8) * 2048);
    const char* um = uni(p.ws + s * 9216 + ct0 * 256);
    const unsigned vu0 = umx_base(w * 8 + g) + c * 16, vu1 = umx_base(w * 8 + 4 + g) + c * 16;
    const char* pm = uni(p.ws + G_D_PM + (size_t)s * 16384 + w * 2048);
    constexpr int NR = DEPTH + 1 > 10 ? 10 : DEPTH + 1;
    u32x4 a[NR][NT], b[10][2];
    unsigned vb = voff, va = voff;
    TST(4096 + (s * 36 + ct0) * 4 + w, 0);
    const int f0 = (int)__hip_atomic_load((const gu32*)(p.ws + CTLO + (lane < 8 ? G_C_UM + (w * 8 + lane) * 128 : lane < 16 ? G_C_GV + ((s & (G_GVN - 1)) * 32 + w * 8 + lane - 8) * 128
                                                                                                                                : attn_flag(p, s, w * 2 + (lane & 1)) - CTLO)), RLX_AGENT);
    const bool early = !TAIL && !__any(f0 < s);
    if (!early) wait_word(p, p.ws + CTLO + (lane < 8 ? G_C_GV + ((s & (G_GVN - 1)) * 32 + w * 8 + lane) * 128 : G_C_UM + w * 8 * 128), lane < 8 ? s : s - 3, 21u);
#define ALOAD(q) do { if ((q) < 8) { _Pragma("unroll") for (int t = 0; t < NT; ++t) RLDA(a[(q) % NR][t], va, ub[t], 0);  \
                                     asm volatile("v_add_u32 %0, 0x400, %0" : "+v"(va)); }                                            \
                      else { _Pragma("unroll") for (int t = 0; t < NT; ++t) RLDA(a[(q) % NR][t], ((q) & 1) ? vu1 : vu0, um, t * 256); } } while (0)
    constexpr int D0 = DEPTH < 8 ? DEPTH : 8;
    asm volatile("s_nop 4");
#pragma unroll
    for (int q = 0; q < D0; ++q) ALOAD(q);
    asm volatile("s_cmp_lg_u32 %0, 0\n\ts_cbranch_scc1 1f\n\ts_waitcnt vmcnt(0)\n1:\n\ts_waitcnt vmcnt(30)" :: "s"(__builtin_amdgcn_readfirstlane((int)early)) : "scc", "memory");
    unsigned tries = 0;
    if (TAIL) {
        const char* sp = pm + (lane & 7) * 256 + ((lane >> 3) & 1) * 8192;
        const int dl = (FLG(p.prm[5]) >> 26) & 31;
        if (dl) {
            wait_word<true>(p, p.ws + CTLO + G_C_KM + ((ct0 >> 1) + w) * 128, s - 1, 25u);
            for (int i = 0; i < dl + (s < 63 ? 5 : 0); ++i) __builtin_amdgcn_s_sleep(2);
        } else
        while (__any(__hip_atomic_load((const gu32*)sp, RLX_AGENT) == 0xffffffffu) && !give_up(p, tries, 23u)) __builtin_amdgcn_s_sleep(1);
    } else if (!early) wait_word(p, p.ws + CTLO + (lane < 8 ? G_C_UM + (w * 8 + lane) * 128 : attn_flag(p, s, w * 2 + (lane & 1)) - CTLO), s, 22u);
    if (!early) TST(4096 + (s * 36 + ct0) * 4 + w, 1);
    for (;;) {
    asm volatile("s_nop 4");
#pragma unroll
    for (int q = 0; q < 8; ++q) { RLD(b[q][0], vb, pb, 0); RLD(b[q][1], vb, pb, 1024); asm volatile("v_add_u32 %0, 0x800, %0" : "+v"(vb)); }
#pragma unroll
    for (int q = 8; q < 10; ++q) { RLD(b[q][0], voff, pm, (q & 1) * 1024); RLD(b[q][1], voff, pm + 8192, (q & 1) * 1024); }
#pragma unroll
    for (int q = D0; q < NR; ++q) ALOAD(q);
#pragma unroll
    for (int q = 0; q < 10; ++q) {
        const int issuedA = (NR + q < 10 ? NR + q : 10) - D0;
        if (q < D0) VMW(2 * (9 - q) + issuedA * NT); else VMW((issuedA - (q - D0 + 1)) * NT);
#pragma unroll
        for (int t = 0; t < NT; ++t) { MFMA_ACC_A(acc[t][0], a[q % NR][t], b[q][0]); MFMA_ACC_A(acc[t][1], a[q % NR][t], b[q][1]); }
        if (NR + q < 10) ALOAD(NR + q);
    }
    asm volatile("s_nop 7" ::: "memory");
#pragma unroll
    for (int t = 0; t < NT; ++t) asm volatile("" : "+v"(acc[t][0]), "+v"(acc[t][1]));
    if (!TAIL) break;
    f32x4 chk = acc[0][0];
#pragma unroll
    for (int t = 0; t < NT; ++t) chk += acc[t][1] + acc[t][0];
    const float ck = (chk[0] + chk[1]) + (chk[2] + chk[3]);
    if (!__any(ck != ck)) break;
    unsigned mx = 0;
#pragma unroll
    for (int q = 0; q < 10; ++q) { asm volatile("" : "+v"(b[q][0]), "+v"(b[q][1])); mx = max(mx, max(umax4(b[q][0]), umax4(b[q][1]))); }
#pragma unroll
    for (int t = 0; t < NT; ++t) { asm volatile("" : "+a"(a[8 % NR][t]), "+a"(a[9 % NR][t])); mx = max(mx, max(umax4(a[8 % NR][t]), umax4(a[9 % NR][t]))); }
    if (!__any(mx == 0xffffffffu) || give_up(p, tries, 24u)) break;
    vb = voff;
#pragma unroll
    for (int t = 0; t < NT; ++t) { acc[t][0] = f32x4{0.f, 0.f, 0.f, 0.f}; acc[t][1] = acc[t][0]; asm volatile("" : "+v"(acc[t][0]), "+v"(acc[t][1])); }
    }
    TST(4096 + (s * 36 + ct0) * 4 + w, 2);
    f32x4* red = (f32x4*)smem;
#pragma unroll
    for (int t = 0; t < NT; ++t) { red[((w * NT + t) * 2 + 0) * 64 + lane] = acc[t][0]; red[((w * NT + t) * 2 + 1) * 64 + lane] = acc[t][1]; }
    __syncthreads();
    u16* orow = p.out + (size_t)(32 * s + 16 * (w & 1) + c) * OUTW + 16 * ct0 + 4 * g;
#pragma unroll
    for (int t = 0; t < NT; ++t)
        if ((t & 1) == (w >> 1)) {
            const int m = w & 1;
            const f32x4 v = (red[((0 * NT + t) * 2 + m) * 64 + lane] + red[((1 * NT + t) * 2 + m) * 64 + lane]) +
                            (red[((2 * NT + t) * 2 + m) * 64 + lane] + red[((3 * NT + t) * 2 + m) * 64 + lane]);
            *(u32x2*)(orow + 16 * t) = u32x2{pk2bf(v[0], v[1]), pk2bf(v[2], v[3])};
        }
}

__device__ __forceinline__ int index_of_l(int l) {
    const int pv = (int)bfr((float)l);
    if (pv <= 256) return pv;
    if (pv <= 512) return 256 + ((pv - 256) >> 1);
    if (pv <= 1024) return 384 + ((pv - 512) >> 2);
    return 512 + ((pv - 1024) >> 3);
}
static constexpr int KROW = G_KROW;
static constexpr int KARR = 80 * KROW;
static constexpr int UCS = 2208;
struct BCh {
    u16 uc[4][UCS];
    f32x4 cst[NST];
    unsigned Kd[648];
    u16 Kh[KARR];
    u16 Kl[KARR];
};
static_assert(4 * sizeof(BCh) <= LDS_TOTAL - 32, "LDS");
__device__ __forceinline__ unsigned prm(unsigned hi, unsigned lo, unsigned sel) { return __builtin_amdgcn_perm(hi, lo, sel); }
#define BST(i)
#define BST2(i)
__device__ __forceinline__ void task_ut(const MainP& p, char* smem, int rb, int w, int lane) {
    const int tid = w * 64 + lane, g = lane >> 4, c = lane & 15;
    const u32x4* src = (const u32x4*)(p.U + (size_t)rb * 64 * DM);
    u32x4 v[18];
#pragma unroll
    for (int i = 0; i < 18; ++i) {
        v[i] = src[tid + 256 * i];
    }
    BST2(0);
#pragma unroll
    for (int i = 0; i < 18; ++i) { const int ch = tid + 256 * i, row = ch / 72; *(u32x4*)(smem + row * 1184 + (ch - 72 * row) * 16) = v[i]; }
    __syncthreads();
    const unsigned ad = (unsigned)(uintptr_t)smem + (16 * g + (c >> 2)) * 1184 + (w * 144 + 4 * (c & 3)) * 2;
#pragma unroll
    for (int i = 0; i < 9; ++i) {
        u32x2 x0, x1, x2, x3;
        asm volatile("ds_read_b64_tr_b16 %0, %4 offset:%5\n\tds_read_b64_tr_b16 %1, %4 offset:%6\n\tds_read_b64_tr_b16 %2, %4 offset:%7\n\tds_read_b64_tr_b16 %3, %4 offset:%8\n\ts_waitcnt lgkmcnt(0)"
                     : "=&v"(x0), "=&v"(x1), "=&v"(x2), "=&v"(x3) : "v"(ad), "i"(i * 32), "i"(4 * 1184 + i * 32), "i"(8 * 1184 + i * 32), "i"(12 * 1184 + i * 32) : "memory");
        v[2 * i] = u32x4{x0[0], x0[1], x1[0], x1[1]}; v[2 * i + 1] = u32x4{x2[0], x2[1], x3[0], x3[1]};
    }
    __syncthreads();
    BST2(1);
    char* tw = smem + w * (144 * 144);
#pragma unroll
    for (int i = 0; i < 9; ++i) { *(u32x4*)(tw + (16 * i + c) * 144 + g * 32) = v[2 * i]; *(u32x4*)(tw + (16 * i + c) * 144 + g * 32 + 16) = v[2 * i + 1]; }
    const char* dst = uni(p.ws + G_D_UT + (size_t)rb * 73728 + w * 18432);
#pragma unroll
    for (int i = 0; i < 18; ++i) {
        const u32x4 o = *(const u32x4*)(tw + (8 * i + (lane >> 3)) * 144 + (lane & 7) * 16);
        const unsigned vi = lane * 16 + i * 1024;
        WST16(vi, o, dst, 0);
    }
    BST2(2);
    const u32x4 sent = {0xffffffffu, 0xffffffffu, 0xffffffffu, 0xffffffffu};
#pragma unroll 1
    for (int sg = NG1; sg < 64; ++sg) {
        const unsigned ov = umx_base(rb) + sg * 9216 + tid * 16, ok = G_D_KM + rb * G_HS4 + skew(sg, rb) * 4096 + tid * 16;
        const unsigned op = tid < 128 ? G_D_PX + (sg * 32 + rb) * 2048 + tid * 16
                          : tid < 192 ? ov + 6144 : G_D_PM + sg * 16384 + ((tid >> 4) & 1) * 8192 + rb * 256 + (tid & 15) * 16;
        asm volatile("global_store_dwordx4 %0, %4, %5 sc1\n\tglobal_store_dwordx4 %1, %4, %5 sc1\n\tglobal_store_dwordx4 %2, %4, %5 sc1\n\t"
                     "global_store_dwordx4 %3, %4, %5 sc1\n\ts_nop 1" :: "v"(ov), "v"(ov + 4096), "v"(ok), "v"(op), "v"(sent), "s"(p.ws) : "memory");
    }
    BST2(3);
    VMC0();
    BST2(4);
    __syncthreads();
    if (tid == 0) __hip_atomic_store((gu32*)(p.ws + CTLO + G_C_UT + rb * 128), 1u, RLX_AGENT);
}
struct KPar { u16 ldt, pa[2][4]; };
__device__ __forceinline__ KPar kgen_par(const MainP& p, int d, int lane) {
    KPar k;
    k.ldt = p.logdt[d];
#pragma unroll
    for (int q = 0; q < 2; ++q) { const int i = d * NST + lane + 64 * q; k.pa[q][0] = p.logA[i]; k.pa[q][1] = p.Aim[i]; k.pa[q][2] = p.Cre[i]; k.pa[q][3] = p.Cim[i]; }
    return k;
}
struct Pull { const char* a; const char* b; const char* c; unsigned at; };
__device__ __forceinline__ void pull_inputs(Pull& q, int w, bool now) {
    if (q.a && (now || (int)((unsigned)__builtin_amdgcn_s_memrealtime() - q.at) >= 0)) {
#pragma unroll
        for (int i = 0; i < 3; ++i)
            if (i < 2 || w == 0) asm volatile("s_mov_b32 m0, %3\n\ts_nop 0\n\tglobal_load_lds_dword %0, %1\n\tglobal_load_lds_dword %0, %2" :: "v"(threadIdx.x * 128 + i * 32768), "s"(q.a), "s"(q.b), "s"(155648) : "memory");
        asm volatile("global_load_lds_dword %0, %1" :: "v"(min(threadIdx.x, 143u) * 128), "s"(q.c) : "memory");
        q.a = nullptr;
    }
}
__device__ __forceinline__ void task_kgen(const MainP& p, char* smem, int d0, int w, int lane, const KPar& kp, bool first, Pull& pl) {
    BCh& L = reinterpret_cast<BCh*>(smem)[w];
    const int g = lane >> 4, c = lane & 15, d = d0 + w;
    const int tid = w * 64 + lane;
    BST(10);
    const auto& pa = kp.pa;
    const float dt = bfr(expf(bf2f(kp.ldt)));
#pragma unroll
    for (int q = 0; q < 2; ++q) {
        const float ar = -bfr(expf(bf2f(pa[q][0])));
        const float cr = bf2f(pa[q][2]) * dt, ci = bf2f(pa[q][3]) * dt;
        L.cst[lane + 64 * q] = f32x4{ar * dt * 1.4426950408889634f, bf2f(pa[q][1]) * dt * 0.15915494309189535f,
                                     __log2f(hypotf(cr, ci)), atan2f(ci, cr) * 0.15915494309189535f};
    }
    BST(11);
    {
        f32x4 acc[3];
#pragma unroll
        for (int v = 0; v < 3; ++v) acc[v] = f32x4{0.f, 0.f, 0.f, 0.f};
        auto base_of = [](int v, int r) { return v == 0 ? (r & 7) + 128 * (r >> 3) : v == 1 ? (r < 8 ? 256 + 2 * (r & 3) + 128 * (r >> 2) : 512 + 4 * (r & 1) + 128 * ((r - 8) >> 1))
                                                                                           : 1024 + 128 * min(r, 8); };
        const float b0 = (float)base_of(0, c), b1 = (float)base_of(1, c), b2 = (float)base_of(2, c), off = (float)(8 * c);
#define KG_POLAR(h_, l_, lg_, f_, a_, b_) do { const float m_ = __builtin_amdgcn_exp2f(fmaf(cc[0], b_, lg_)), p_ = __builtin_amdgcn_fractf(fmaf(a_, b_, f_)); \
            const float re_ = m_ * __builtin_amdgcn_cosf(p_), im_ = m_ * __builtin_amdgcn_sinf(p_);                                                            \
            h_ = pk2bf(re_, im_); l_ = pk2bf(re_ - lo2f(h_), im_ - hi2f(h_)); } while (0)
#pragma unroll 1
        for (int t = 0; t < NST / 16; ++t) {
            u32x4 fh, fl, ah[3], al[3];
            pull_inputs(pl, w, false);
#pragma unroll
            for (int j = 0; j < 4; ++j) {
                const f32x4 cc = L.cst[16 * t + 4 * g + j];
                KG_POLAR(fh[j], fl[j], 0.f, 0.f, -cc[1], off);
                KG_POLAR(ah[0][j], al[0][j], cc[2], cc[3], cc[1], b0); KG_POLAR(ah[1][j], al[1][j], cc[2], cc[3], cc[1], b1); KG_POLAR(ah[2][j], al[2][j], cc[2], cc[3], cc[1], b2);
            }
#pragma unroll
            for (int v = 0; v < 3; ++v) { acc[v] = MFMA32(al[v], fh, acc[v]); acc[v] = MFMA32(ah[v], fl, acc[v]); acc[v] = MFMA32(ah[v], fh, acc[v]); }
        }
        BST(12);
        auto split = [](float k) { const unsigned hi = pk2bf(k, 0.f) & 0xffffu; return hi | (pk2bf(0.f, k - __uint_as_float(hi << 16)) & 0xffff0000u); };
#pragma unroll
        for (int v = 0; v < 3; ++v)
#pragma unroll
            for (int i = 0; i < 4; ++i) {
                const int pos = base_of(v, 4 * g + i) + 8 * c;
                if (pos <= 2048) L.Kd[index_of_l(pos)] = split(2.0f * acc[v][i]);
            }
    }
    char* kt = p.ws + G_D_KT + (size_t)d * 8192;
    char* kt2 = kt + 4096;
    const unsigned voff = lane * 16;
#pragma unroll
    for (int q = 0; q < 4; ++q) {
        const int e = lane + 64 * q;
        unsigned k[8];
#pragma unroll
        for (int j = 0; j < 8; ++j) k[j] = L.Kd[index_of_l(8 * e + 7 - j)];
        u32x4 kh, kl;
#pragma unroll
        for (int j = 0; j < 4; ++j) { kh[j] = prm(k[2 * j + 1], k[2 * j], 0x05040100u); kl[j] = prm(k[2 * j + 1], k[2 * j], 0x07060302u); }
        WST16(voff, kh, kt, q * 1024); WST16(voff, kl, kt2, q * 1024);
    }
    BST(13);
    VMC0();
    BST(14);
    if (first) init_wait(p.ws, w, lane);
    BST(15);
    __syncthreads();
    if (w == 0 && lane == 0) __hip_atomic_store((gu32*)(p.ws + G_WS_CTL + G_C_KG + (d0 >> 2) * 128), 1u, RLX_AGENT);
}
__device__ __forceinline__ void task_s4d(const MainP& p, char* smem, int d0, int w, int lane, int& nb) {
    BCh* LL = reinterpret_cast<BCh*>(smem);
    BCh& L = LL[w];
    const int tid = w * 64 + lane, g = lane >> 4, c = lane & 15, d = d0 + w;
    BST(0);
    wait_word(p, p.ws + (lane < 32 ? CTLO + G_C_UT + lane * 128 : G_WS_CTL + G_C_KG + (d0 >> 2) * 128), 1, 30u);
    const float dsk = bf2f(p.Dskip[d]);
    const char* kt = p.ws + G_D_KT + (size_t)d * 8192;
    const char* kt2 = kt + 4096;
    const char* ut = p.ws + G_D_UT + d * 128;
    const unsigned voff = lane * 16;
    u32x4 kh[4], kl[4], uu[4];
#pragma unroll
    for (int q = 0; q < 4; ++q) { const unsigned vu = (8 * q + (lane >> 3)) * 73728 + (lane & 7) * 16; WLD16(uu[q], vu, ut, 0); }
    WLD16X4(kh[0], kh[1], kh[2], kh[3], voff, kt); WLD16X4(kl[0], kl[1], kl[2], kl[3], voff, kt2);
    const u32x4 z4 = {0u, 0u, 0u, 0u};
    *(u32x4*)&L.Kh[8 * lane] = z4; *(u32x4*)&L.Kl[8 * lane] = z4;
    *(u32x4*)&L.Kh[512 + 8 * (lane & 31)] = z4; *(u32x4*)&L.Kl[512 + 8 * (lane & 31)] = z4;
    *(u32x4*)&L.uc[0][64 + SL] = z4;
    *(u32x4*)&L.uc[(lane >> 3) & 3][8 * (lane & 7)] = z4;
    asm volatile("s_waitcnt vmcnt(0)" : "+v"(kh[0]), "+v"(kh[1]), "+v"(kh[2]), "+v"(kh[3]), "+v"(kl[0]), "+v"(kl[1]), "+v"(kl[2]), "+v"(kl[3]),
                 "+v"(uu[0]), "+v"(uu[1]), "+v"(uu[2]), "+v"(uu[3]) :: "memory");
    BST(1);
#pragma unroll
    for (int q = 0; q < 4; ++q) {
        const int e = lane + 64 * q, at = (16 + (e >> 2)) * KROW + 24 - 8 * (e & 3);
        *(u32x4*)&L.uc[0][64 + 8 * e] = uu[q];
        *(u32x4*)&L.Kh[at] = kh[q]; *(u32x4*)&L.Kl[at] = kl[q];
    }
    BST(2);
    BST(3);
#pragma unroll
    for (int q = 0; q < 4; ++q) {
        const int e = lane + 64 * q;
        const int ue = 56 + 8 * e;
        const u32x4 a = *(const u32x4*)&L.uc[0][ue];
        const u32x2 b = *(const u32x2*)&L.uc[0][ue + 8];
        const unsigned s0 = __builtin_amdgcn_alignbit(a[1], a[0], 16), s1 = __builtin_amdgcn_alignbit(a[2], a[1], 16), s2 = __builtin_amdgcn_alignbit(a[3], a[2], 16),
                       s3 = __builtin_amdgcn_alignbit(b[0], a[3], 16), s4 = __builtin_amdgcn_alignbit(b[1], b[0], 16);
        *(u32x4*)&L.uc[1][ue] = u32x4{s0, s1, s2, s3};
        *(u32x4*)&L.uc[2][ue] = u32x4{a[1], a[2], a[3], b[0]};
        *(u32x4*)&L.uc[3][ue] = u32x4{s1, s2, s3, s4};
    }
    if (lane == 0) {
        const u32x4 a = *(const u32x4*)&L.uc[0][56 + SL];
        const unsigned s0 = __builtin_amdgcn_alignbit(a[1], a[0], 16), s1 = __builtin_amdgcn_alignbit(a[2], a[1], 16), s2 = __builtin_amdgcn_alignbit(a[3], a[2], 16), s3 = a[3] >> 16;
        *(u32x4*)&L.uc[1][56 + SL] = u32x4{s0, s1, s2, s3};
        *(u32x4*)&L.uc[2][56 + SL] = u32x4{a[1], a[2], a[3], 0u};
        *(u32x4*)&L.uc[3][56 + SL] = u32x4{s1, s2, s3, 0u};
    }
    BST(4);
    {
        const int o = 33 + c + 8 * g;
        const unsigned ua = (unsigned)(uintptr_t)&L.uc[o & 3][o & ~3], ka = (unsigned)(uintptr_t)&L.Kh[(16 + c) * KROW + 8 * g];
        const f32x4* res = (const f32x4*)L.Kh + lane;
        asm volatile(ASM_CV :: "v"(ua), "v"(ka), "v"((unsigned)(uintptr_t)res) : CV_CLOB);
        BST(5);
#pragma unroll
        for (int par = 0; par < 2; ++par)
#pragma unroll
            for (int t = 0; t < 4; ++t) {
                const int n = 16 * (par + 2 * (16 * t + c)) + 4 * g;
                const u32x2 uu = *(const u32x2*)&L.uc[0][64 + n];
                const f32x4 y4 = res[(par * 4 + t) * 64];
                const float r0 = bfr(y4[0]) + bfr(lo2f(uu[0]) * dsk), r1 = bfr(y4[1]) + bfr(hi2f(uu[0]) * dsk);
                const float r2 = bfr(y4[2]) + bfr(lo2f(uu[1]) * dsk), r3 = bfr(y4[3]) + bfr(hi2f(uu[1]) * dsk);
                *(u32x2*)&L.uc[1][n] = u32x2{pk2bf(r0, r1), pk2bf(r2, r3)};
            }
    }
    __syncthreads();
    BST(6);
    u32x4 y[4];
#pragma unroll
    for (int ch = 0; ch < 4; ++ch) y[ch] = *(const u32x4*)&LL[ch].uc[1][8 * tid];
    const unsigned vt = tid * 16;
    char* yb = p.ws + G_D_YB + (size_t)(d0 >> 2) * 16384;
#pragma unroll
    for (int i = 0; i < 4; ++i) {
        const u32x4 o = {prm(y[1][i], y[0][i], 0x05040100u), prm(y[3][i], y[2][i], 0x05040100u), prm(y[1][i], y[0][i], 0x07060302u), prm(y[3][i], y[2][i], 0x07060302u)};
        char* yi = yb + i * 4096;
        WST16(vt, o, yi, 0);
    }
    BST(7);
    if (tid == 0) nb = (int)(__hip_atomic_fetch_add((gu32*)(p.ws + CTLO + G_C_TASK + 128), 1u, RLX_AGENT) + 1u);
    VMC0();
    __syncthreads();
    if (tid == 0) __hip_atomic_fetch_add((gu32*)(p.ws + CTLO + G_C_YC + (d0 >> 6) * 128), 1u, RLX_AGENT);
    BST(8);
}
__device__ __forceinline__ void task_merge(const MainP& p, char* smem, int d0, int i, int w, int lane) {
    const unsigned vt = (w * 64 + lane) * 16;
    wait_word(p, p.ws + CTLO + G_C_YC + (d0 >> 6) * 128, 15, 31u);
    {
        const char* sb = p.ws + G_D_YB + (size_t)(d0 >> 6) * 262144 + i * 4096;
        u32x4 z[16];
#pragma unroll
        for (int j = 0; j < 16; ++j) { const char* sj = sb + j * 16384; WLD16(z[j], vt, sj, 0); }
        asm volatile("s_waitcnt vmcnt(0)" : "+v"(z[0]), "+v"(z[1]), "+v"(z[2]), "+v"(z[3]), "+v"(z[4]), "+v"(z[5]), "+v"(z[6]), "+v"(z[7]),
                     "+v"(z[8]), "+v"(z[9]), "+v"(z[10]), "+v"(z[11]), "+v"(z[12]), "+v"(z[13]), "+v"(z[14]), "+v"(z[15]) :: "memory");
        char* tw = smem + w * 18432;
#pragma unroll
        for (int j = 0; j < 8; ++j) {
            *(u32x4*)(tw + lane * 144 + j * 16) = u32x4{z[2 * j][0], z[2 * j][1], z[2 * j + 1][0], z[2 * j + 1][1]};
            *(u32x4*)(tw + (64 + lane) * 144 + j * 16) = u32x4{z[2 * j][2], z[2 * j][3], z[2 * j + 1][2], z[2 * j + 1][3]};
        }
#pragma unroll
        for (int k = 0; k < 16; ++k) {
            const int q = 8 * k + (lane >> 3);
            const int row = 8 * (64 * w + (q & 63)) + 2 * i + (q >> 6);
            *(u32x4*)(p.out + (size_t)row * OUTW + DM + (d0 & ~63) + 8 * (lane & 7)) = *(const u32x4*)(tw + q * 144 + (lane & 7) * 16);
        }
    }
}

static constexpr int NKG = 144, E_B = NKG + 36;
static constexpr int UT0 = 2, UTA = UT0 * 6, UTB = UTA + 80;
static constexpr int Q1 = NG1 * 6 + 32, Q2 = Q1 + (NFINE - 2) * 25;
static constexpr int NTASK = E_B + Q2 + 68;

__device__ __forceinline__ void worker_body(const MainP& p, char* smem, const int w, const int lane, int quota) {
    const int tid = w * 64 + lane;
    int* slot = (int*)(smem + LDS_TOTAL - 16);
    int nb = -1;
    int nst = 0, nxt = -1;
    gu32* qa = (gu32*)(p.ws + CTLO + G_C_TASK);
    for (;;) {
        if (tid == 0) {
            int t = E_B;
            if (quota > 0) {
                const int k = nb >= 0 ? nb : (int)(__hip_atomic_fetch_add(qa + 32, 1u, RLX_AGENT) + 1u);
                if (k < E_B) t = k;
                nb = -1;
            }
            if (t >= E_B) {
                t = E_B + (nxt >= 0 ? nxt : (int)(__hip_atomic_fetch_add(qa, 1u, RLX_AGENT) + 1u));
                nxt = -1;
            }
            *slot = t;
        }
        __syncthreads();
        const int t = __builtin_amdgcn_readfirstlane(*slot);
        if (t >= NTASK) break;
        quota = t < E_B ? quota - 1 : 0;
        if (t < E_B) {
            if (t < 128 || (t >= 144 && t < 160)) task_s4d(p, smem, 4 * (t < 128 ? t : t - 16), w, lane, nb);
            else task_merge(p, smem, 64 * ((t < 144 ? t - 128 : t - 144) >> 2), t & 3, w, lane);
        } else {
            const int u = t - E_B;
            if (tid == 0 && u < Q1 - 140) nxt = (int)(__hip_atomic_fetch_add(qa, 1u, RLX_AGENT) + 1u);
            const bool last2 = u >= Q2;
            const int sg = last2 ? 62 + (u - Q2) / 34 : NG1 + (u - Q1) / 25, r = last2 ? (u - Q2) % 34 : (u - Q1) % 25;
            const int v = u < UTA ? u : u < UTB ? ((u - UTA) % 10 < 6 ? UTA + (u - UTA) / 10 * 6 + (u - UTA) % 10 : -1) : u - 32;
            if (v < 0) task_ut(p, smem, (u - UTA) / 10 * 4 + (u - UTA) % 10 - 6, w, lane);
            else if (u < Q1) task_out<6, 6, false>(p, smem, v / 6, 6 * (v % 6), w, lane);
            else if (r < 16) task_attn_tail(p, sg, 2 * r + (w >> 1), w, lane);
            else if (last2) task_out<2, 9, true>(p, smem, sg, 2 * (r - 16), w, lane);
            else task_out<4, 9, true>(p, smem, sg, 4 * (r - 16), w, lane);
            if (last2) break;
        }
        __syncthreads();
    }
}

__host__ __device__ constexpr unsigned phys(unsigned x) { return (x >> 13) * 16384 + (x & 8191); }
__host__ __device__ constexpr unsigned aMB(int f, int b) { return phys(f < 8 ? 16384 + f * 1024 + b : 28672 + b); }
__host__ __device__ constexpr unsigned aPX(int pr, int b) { return phys(pr * 1024 + b); }
__host__ __device__ constexpr unsigned aPM(int u, int b) { return phys(24576 + u * 1024 + b); }
#define IST(k)

__device__ __forceinline__ int lane_id() { int l; asm volatile("v_mbcnt_lo_u32_b32 %0, -1, 0\n\tv_mbcnt_hi_u32_b32 %0, -1, %0" : "=v"(l)); return l; }
typedef const __attribute__((address_space(4))) MainP* KArg;

__global__ __launch_bounds__(256, 1) void main_kernel(MainP p_) {
    __shared__ __attribute__((aligned(16))) char smem[LDS_TOTAL];
    KArg ka = (KArg)__builtin_amdgcn_kernarg_segment_ptr();
    static_assert(offsetof(MainP, W) == 0 && offsetof(MainP, ws) == 0x20 && offsetof(MainP, X) == 0x28 && offsetof(MainP, prm) == 0x40, "argument layout");
    asm volatile(ASM_EA :: "s"(ka), "s"(blockIdx.x), "v"(threadIdx.x) : ASM_CLOB);
    asm volatile("" : "+s"(ka));
    const unsigned t00 = (unsigned)__builtin_amdgcn_s_memrealtime();
    const int b = blockIdx.x, x = b & 7, ix = b >> 3;
    const int w = __builtin_amdgcn_readfirstlane(threadIdx.x >> 6);
    const bool isr = x < 4 && ix >= 16;
    if (x < 4 && ix >= 29) init_cal(ka->ws, smem, x * 3 + ix - 29, threadIdx.x, min((FLG(ka->prm[5]) >> 16) & 31, 6));
    if (x >= 4 ? ix >= 24 : (ix >= 25 && ix < 29)) init_fill(ka->ws, ka->mem0, x >= 4 ? (ix - 24) * 4 + (x & 3) : 32 + (ix - 25) * 4 + x, threadIdx.x);
    IST(0);
    if (x >= 4 && ix == 24) {
        const char* xs = (const char*)ka->X + threadIdx.x * 128;
#pragma unroll
        for (int i = 0; i < 7; ++i) asm volatile("s_mov_b32 m0, %1\n\ts_nop 0\n\tglobal_load_lds_dword %0, off" :: "v"(xs + i * 32768), "s"(155648) : "memory");
    }
    {
        const int q = x < 4 ? ix - 25 : ix - 26;
        if (q >= 0 && q < 4 && (FLG(ka->prm[5]) & 1)) {
            const int hs = (x & 3) * 8 + 4 * (q & 1) + ((1 - (int)((u64)ka->W[q & 2] >> 8)) & 3);
            const char* src = (q & 2) ? (const char*)ka->W[3] + (size_t)hs * 147456 + threadIdx.x * 128 : (const char*)ka->W[x < 4] + hs * 256 + (threadIdx.x >> 1) * 8192 + (threadIdx.x & 1) * 128;
            const unsigned st = (q & 2) ? 32768u : 1048576u;
#pragma unroll
            for (int i = 0; i < 5; ++i)
                if (i < 4 || w < 2) asm volatile("s_mov_b32 m0, %1\n\ts_nop 0\n\tglobal_load_lds_dword %0, off nt" :: "v"(src + (size_t)i * st), "s"(155648) : "memory");
        }
    }
    const int RK = (FLG(ka->prm[5]) >> 10) & 1, NS = (FLG(ka->prm[5]) >> 8) & 3, NF = (FLG(ka->prm[5]) >> 21) & 3;
    const int NKE = 36 * RK + 28 * NS + 24 * NF + (NF ? 8 : 0);
    static_assert(36 * ((kDefault[5] >> 10) & 1) + 28 * ((kDefault[5] >> 8) & 3) + 24 * ((kDefault[5] >> 21) & 3) + 8 <= NKG, "kernels");
    if (ix >= (x < 4 ? 16 : 24)) {
        MainP p;
        p.ws = ka->ws; p.logA = ka->logA; p.Aim = ka->Aim; p.Cre = ka->Cre; p.Cim = ka->Cim; p.logdt = ka->logdt;
        const int t0 = x >= 4 ? (ix >= 30 ? NKE - 8 + (ix - 30) * 4 + (x & 3) : 36 * RK + 28 * NS + (ix - 24) * 4 + (x & 3)) : ix < 25 ? (ix - 16) * 4 + x : 36 * RK + (ix - 25) * 4 + x;
        const int nr = x >= 4 ? (ix >= 30 ? min(NF, 1) : NF) : ix < 25 ? RK : NS, dt = x >= 4 ? 24 : 28;
        Pull pl = {nullptr, nullptr, nullptr, t00 + 400 + 50 * ((FLG(ka->prm[5]) >> 4) & 15)};
        if (x >= 4) { const size_t o = (size_t)((ix - 24) * 4 + (x & 3)) * 73728; pl.a = (const char*)ka->X + o; pl.b = (const char*)ka->U + o;
            pl.c = (const char*)((x & 2) ? ((x & 1) ? ka->Cim : ka->Cre) : ((x & 1) ? ka->Aim : ka->logA)) + (ix - 24) * 18432; }
        if (nr > 0) {
            KPar kn = kgen_par(p, 4 * t0 + w, threadIdx.x & 63);
#pragma unroll 1
            for (int r = 0; r < nr; ++r) {
                const KPar kp = kn;
                kn = kgen_par(p, 4 * (t0 + dt * min(r + 1, nr - 1)) + w, threadIdx.x & 63);
                __builtin_amdgcn_sched_barrier(0);
                task_kgen(p, smem, 4 * (t0 + dt * r), w, threadIdx.x & 63, kp, r == 0, pl);
            }
        } else init_wait(p.ws, w, threadIdx.x & 63);
        pull_inputs(pl, w, true);
        if (x < 4 && ix >= 25) return;
        asm volatile("" : "+s"(ka));
    }
    if (isr) {
        const int lane = threadIdx.x & 63, g = lane >> 4, c = lane & 15;
        const unsigned lb = w * G_STG;
        unsigned* ct = (unsigned*)(smem + lb) + lane;
        asm volatile("" :: "v"(ct) : "memory");
#define CTW(k, val) ct[(k) * 64] = (unsigned)(val)
        const u64 ws = (u64)ka->ws, prm = (u64)ka + 0x40;
        const u64 stb = 0;
        IST(1);
        CTW(0, lane * 16);
        const int j = x * 9 + ix - 16;
        const int rot = ((ix - 16) * 16) / 9;
        if (w >= 2) return;
        CTW(1, lane * 16 + 4096);
        CTW(2, aMB(j >> 2, (j & 3) * 256 + (4 * g + 2 * ((c >> 1) & 1) + (c & 1)) * 16 + (c >> 2) * 4));
        for (int k = 0; k < (int)G_NARM; ++k) {
            constexpr int N0 = G_HKB, N1 = N0 + (9 * G_RM + 3) / 4, N2 = N1 + 5 * G_RP;
            static_assert(N2 <= 9 * G_NARM, "shares");
            const int q = min(ix - 16 + 9 * k, N2 - 1), qm = min(x * (N1 - N0) + q - N0, (int)(9 * G_RM) - 1), qp = x * 5 * G_RP + q - N1;
            const int cp = q < N0 ? 4 + x : q < N1 ? qm / 9 : qp / 20, kb = q < N0 ? q : q < N1 ? (qm % 9 == 8 ? 28 : 16 + qm % 9) : (qp % 20 < 16 ? qp % 20 : 8 + qp % 20);
            CTW(47 + k, cp * G_CPY + phys(kb * 1024 + lane * 16));
        }
        const unsigned pc = x * G_RP / 4 * G_CPY;
        CTW(3, (lane & 3) * G_CPY + G_HOFF + G_RHO + ((lane >> 2) & 1) * G_RHS + j * 32); CTW(46, x * G_CPY + G_HOFF + G_RHO + min(lane, 35) * 32);
        CTW(4, (c & 2) ? 0xEEEE : 0x4444); CTW(5, (c & 1) ? 0x03020706u : 0x05040100u);
        CTW(6, G_WS_CTL + G_C_GV + ((w & (G_GVN - 1)) * 32 + (lane & 31)) * 128); CTW(7, x * G_CPY + G_HOFF + (lane & 31) * G_HP); CTW(8, lane * 4); CTW(9, 131072);
        for (int k = 0; k < 16; ++k) { const int i = (k + rot) & 15; CTW(10 + k, pc + aPX(i, lane * 16)); CTW(26 + k, i * 2048 + lane * 16); }
        for (int k = 0; k < 4; ++k) CTW(42 + k, pc + aPM(k, lane * 16));
        IST(2);
        asm volatile(ASM_R :: "s"(ws), "s"(ws), "s"(ws), "s"(w), "s"(j), "s"((u64)(2 * rot)), "s"(prm), "s"(lb), "s"(stb) : ASM_CLOB);
    }
    asm volatile("" : "+s"(ka));
    MainP p;
    p.prm[5] = ka->prm[5];
    p.U = ka->U; p.ws = ka->ws; p.logA = ka->logA; p.Aim = ka->Aim; p.Cre = ka->Cre; p.Cim = ka->Cim; p.logdt = ka->logdt; p.Dskip = ka->Dskip; p.out = ka->out;
    {
        auto cal = [&](int k) { return __hip_atomic_load((const gu32*)(p.ws + G_WS_DONE + k * 64 + 4), RLX_AGENT); };
        p.prm[0] = __builtin_amdgcn_readfirstlane((min(cal(5), cal(11)) < min(cal(4), cal(10)) ? 8192 : 0) ^ G_CTF);
    }
    const bool s4 = ix < 22 - ((FLG(p.prm[5]) >> 12) & 15);
    if (s4 && ix >= 8 && ix < 24) for (int i = 0; i < (int)(((ix & 7) * 4 + (x & 3)) * ((FLG(p.prm[5]) >> 23) & 7)); ++i) __builtin_amdgcn_s_sleep(3);
    if (s4 && ix >= 8 && ix < 16) {
        const int lane = lane_id();
        int* slot = (int*)(smem + LDS_TOTAL - 16);
#pragma unroll 1
        for (int n = s4 ? 1000 : 1; n > 0; --n) {
            if (w == 0 && lane == 0) *slot = (int)(__hip_atomic_fetch_add((gu32*)(p.ws + CTLO + G_C_TASK + 1024), 1u, RLX_AGENT) + 1u);
            __syncthreads();
            const int q = __builtin_amdgcn_readfirstlane(*slot);
            if (q >= NKG - NKE) break;
            const int d0 = 4 * (q + NKE);
            Pull pl = {nullptr, nullptr, nullptr, 0u};
            task_kgen(p, smem, d0, w, lane, kgen_par(p, d0 + w, lane), false, pl);
        }
        __syncthreads();
    }
    if ((ix & 22) == 22) { attn_role(p, ix >= 30 ? 0 : NA + 1, (x & 3) * 8 + (ix & 1) * 4 + w, w, lane_id());
        if (b == 255) {
            wait_word(p, p.ws + G_WS_CTL + G_C_ARR, 255, 50u);
            if (w == 0 && lane_id() == 0) __hip_atomic_fetch_add((gu32*)(p.ws + G_WS_EP), 1u, RLX_AGENT);
        }
        __syncthreads();
    }
    worker_body(p, smem, w, lane_id(), s4 ? 1000 : 0);
}

extern "C" __global__ void code_end_marker() {}

static constexpr int g_prm[8] = {PRM_DEFAULT};

extern "C" void kernel_launch(void* const* d_in, const int* in_sizes, int n_in, void* d_out, int out_size,
                              void* d_ws, size_t ws_size, hipStream_t stream) {
    char* ws = (char*)(((uintptr_t)d_ws + 0x1FFFFF) & ~(uintptr_t)0x1FFFFF);
    MainP p;
    for (int i = 0; i < 8; ++i) p.prm[i] = g_prm[i];
    p.mem0 = (const u16*)d_in[1];
    for (int i = 0; i < 4; ++i) p.W[i] = (const u16*)d_in[2 + i];
    p.X = (const u16*)d_in[0]; p.U = (const u16*)d_in[7]; p.ws = ws;
    p.logA = (const u16*)d_in[8]; p.Aim = (const u16*)d_in[9]; p.Cre = (const u16*)d_in[10]; p.Cim = (const u16*)d_in[11];
    p.logdt = (const u16*)d_in[12]; p.Dskip = (const u16*)d_in[13]; p.out = (u16*)d_out;
    main_kernel<<<256, 256, 0, stream>>>(p);
}
```

```cpp
#include <hip/hip_runtime.h>
#include <hip/hip_bf16.h>
#include <stdint.h>
#include <stddef.h>

typedef __attribute__((ext_vector_type(8))) __bf16 bf16x8;
typedef __attribute__((ext_vector_type(4))) short s16x4;
typedef __attribute__((ext_vector_type(4))) float f32x4;
typedef __attribute__((ext_vector_type(4))) unsigned u32x4;
typedef __attribute__((ext_vector_type(2))) unsigned u32x2;
typedef __attribute__((ext_vector_type(2))) float f32x2;
typedef unsigned short u16;
typedef unsigned long long u64;

#define GAS __attribute__((address_space(1)))
typedef GAS unsigned gu32;
#define RLX_AGENT __ATOMIC_RELAXED, __HIP_MEMORY_SCOPE_AGENT

static constexpr int DM = 576, NH = 32, DH = 128, MM = 8, SEG = 32, NSEG = 64, SL = 2048;
static constexpr int HD = NH * DH;
static constexpr int OUTW = 2 * DM;
static constexpr int NST = 128;

__device__ __forceinline__ float bf2f(u16 h) { return __uint_as_float(((unsigned)h) << 16); }
typedef __attribute__((ext_vector_type(2))) __bf16 bf16x2_t;
__device__ __forceinline__ unsigned pk2bf(float a, float b) {
    bf16x2_t v; v[0] = (__bf16)a; v[1] = (__bf16)b;
    return __builtin_bit_cast(unsigned, v);
}
__device__ __forceinline__ float bfr(float f) { return __uint_as_float(pk2bf(f, f) << 16); }
__device__ __forceinline__ float lo2f(unsigned u) { return __uint_as_float(u << 16); }
__device__ __forceinline__ float hi2f(unsigned u) { return __uint_as_float(u & 0xffff0000u); }
__device__ __forceinline__ float rowmax4(float x) {
    auto a = __builtin_amdgcn_permlane16_swap(__float_as_uint(x), __float_as_uint(x), false, false);
    x = fmaxf(__uint_as_float(a[0]), __uint_as_float(a[1]));
    auto b = __builtin_amdgcn_permlane32_swap(__float_as_uint(x), __float_as_uint(x), false, false);
    return fmaxf(__uint_as_float(b[0]), __uint_as_float(b[1]));
}
__device__ __forceinline__ float rowsum4(float x) {
    auto a = __builtin_amdgcn_permlane16_swap(__float_as_uint(x), __float_as_uint(x), false, false);
    x = __uint_as_float(a[0]) + __uint_as_float(a[1]);
    auto b = __builtin_amdgcn_permlane32_swap(__float_as_uint(x), __float_as_uint(x), false, false);
    return __uint_as_float(b[0]) + __uint_as_float(b[1]);
}

struct MainP {
    const u16* W[4];
    char* ws; const u16* X; const u16* mem0; const u16* U;
    int prm[8];
    const u16* logA; const u16* Aim; const u16* Cre; const u16* Cim; const u16* logdt; const u16* Dskip;
    u16* out;
};
static constexpr int LDS_TOTAL = 163840;
static constexpr unsigned kWaitCap = 1u << 18;
#define VMC0() asm volatile("s_waitcnt vmcnt(0)" ::: "memory")
#define PRM_DEFAULT 1090, 600, 96, 680, 824, 1615006578, 15728816, 7
#define ASM_EA \
    "s_load_dwordx8 s[40:47], %0, 0\n\t" \
    "s_load_dwordx4 s[36:39], %0, 0x20\n\t" \
    "s_load_dwordx8 s[84:91], %0, 0x40\n\t" \
    "s_getpc_b64 s[54:55]\n\t" \
    "ea_pc_%=:\n\t" \
    "s_bfe_u32 s69, %1, 0x20003\n\t" \
    "s_lshl_b32 s69, s69, 15\n\t" \
    "v_lshl_add_u32 v1, %2, 7, s69\n\t" \
    "v_min_u32 v1, code_end_marker-ea_pc_%=, v1\n\t" \
    "s_mov_b32 m0, 155648\n\t" \
    "v_readfirstlane_b32 s66, %2\n\t" \
    "global_load_lds_dword v1, s[54:55]\n\t" \
    "s_bitcmp1_b32 %1, 2\n\t" \
    "s_cselect_b32 s69, 24, 16\n\t" \
    "s_lshr_b32 s70, %1, 3\n\t" \
    "s_cmp_ge_u32 s70, s69\n\t" \
    "s_cbranch_scc1 ea_done_%=\n\t" \
    "s_lshr_b32 s66, s66, 6\n\t" \
    "s_and_b32 s69, %1, 0xc4\n\t" \
    "s_cmp_eq_u32 s69, 4\n\t" \
    "s_cselect_b32 s69, 72, 36\n\t" \
    "s_add_u32 s82, s66, s69\n\t" \
    "s_lshl_b32 s82, s82, 10\n\t" \
    "s_mov_b32 m0, s82\n\t" \
    "s_and_b32 s67, %1, 3\n\t" \
    "s_lshl_b32 s67, s67, 3\n\t" \
    "s_and_b32 s69, s70, 7\n\t" \
    "s_add_u32 s67, s67, s69\n\t" \
    "s_lshl_b32 s69, s67, 8\n\t" \
    "v_lshrrev_b32 v1, 4, %2\n\t" \
    "v_lshrrev_b32 v2, 3, %2\n\t" \
    "v_and_b32 v2, 6, v2\n\t" \
    "v_and_or_b32 v2, v1, 8, v2\n\t" \
    "v_xor_b32 v2, v2, %2\n\t" \
    "v_and_b32 v2, 15, v2\n\t" \
    "v_lshl_add_u32 v2, v2, 4, s69\n\t" \
    "v_lshl_add_u32 v200, v1, 13, v2\n\t" \
    "s_mov_b32 s71, 16\n\t" \
    "s_bitcmp1_b32 %1, 2\n\t" \
    "s_waitcnt lgkmcnt(0)\n\t" \
    "s_cselect_b64 s[48:49], s[42:43], s[40:41]\n\t" \
    "s_cselect_b64 s[40:41], s[40:41], s[42:43]\n\t" \
    "s_cmp_ge_u32 s70, 8\n\t" \
    "s_cselect_b64 s[42:43], s[44:45], s[48:49]\n\t" \
    "s_cselect_b64 s[44:45], s[46:47], s[40:41]\n\t" \
    "ea_lp1_%=:\n\t" \
    "global_load_lds_dwordx4 v200, s[42:43]\n\t" \
    "v_add_u32 v200, 131072, v200\n\t" \
    "s_add_u32 m0, m0, 4096\n\t" \
    "s_sub_u32 s71, s71, 1\n\t" \
    "s_cmp_lg_u32 s71, 0\n\t" \
    "s_cbranch_scc1 ea_lp1_%=\n\t" \
    "s_mov_b64 s[50:51], s[38:39]\n\t" \
    "s_cmp_lt_u32 s70, 8\n\t" \
    "s_cbranch_scc1 ea_w_%=\n\t" \
    "s_mul_i32 s69, s67, 147456\n\t" \
    "s_add_u32 s44, s44, s69\n\t" \
    "s_addc_u32 s45, s45, 0\n\t" \
    "s_and_b32 s69, s70, 16\n\t" \
    "s_lshl_b32 s69, s69, 1\n\t" \
    "s_add_u32 s67, s67, s69\n\t" \
    "s_bitcmp1_b32 %1, 2\n\t" \
    "s_cbranch_scc1 gv_begin_%=\n\t" \
    "s_branch vv_begin_%=\n\t" \
    "ea_w_%=:\n\t" \
    "s_bitcmp1_b32 %1, 2\n\t" \
    "s_cbranch_scc1 gx_begin_%=\n\t" \
    "qk_begin_%=:\n\t" \
    "v_mbcnt_lo_u32_b32 v223, -1, 0\n\t" \
    "v_mbcnt_hi_u32_b32 v223, -1, v223\n\t" \
    "s_mov_b32 s68, 262144\n\t" \
    "s_mov_b32 s72, 0\n\t" \
    "s_mov_b32 s83, -1\n\t" \
    "s_mov_b32 s65, 0\n\t" \
    "v_and_b32 v180, 3, v223\n\t" \
    "v_lshlrev_b32 v202, 3, v180\n\t" \
    "v_lshrrev_b32 v181, 5, v223\n\t" \
    "v_lshl_add_u32 v202, v181, 12, v202\n\t" \
    "v_bfe_u32 v182, v223, 2, 2\n\t" \
    "s_mov_b32 s70, 288\n\t" \
    "v_mad_u32_u24 v202, v182, s70, v202\n\t" \
    "v_bfe_u32 v183, v223, 4, 1\n\t" \
    "s_mov_b32 s70, 2176\n\t" \
    "v_mad_u32_u24 v202, v183, s70, v202\n\t" \
    "s_lshl_b32 s69, s66, 6\n\t" \
    "v_xor_b32 v202, s69, v202\n\t" \
    "v_xor_b32 v203, 32, v202\n\t" \
    "v_lshlrev_b32 v0, 4, v223\n\t" \
    "s_mul_i32 s69, s66, 2048\n\t" \
    "s_bfe_u32 s71, s67, 0x20003\n\t" \
    "s_mul_i32 s71, s71, 65536\n\t" \
    "s_add_u32 s69, s69, s71\n\t" \
    "s_add_u32 s69, s69, 32768\n\t" \
    "v_lshl_add_u32 v1, v223, 4, s69\n\t" \
    "s_bfe_u32 s71, s67, 0x20003\n\t" \
    "s_mul_i32 s69, s71, 65536\n\t" \
    "s_add_u32 s69, s69, 53248\n\t" \
    "v_lshl_add_u32 v2, v223, 4, s69\n\t" \
    "s_bfe_u32 s71, s67, 0x20003\n\t" \
    "s_mul_i32 s69, s71, 65536\n\t" \
    "s_add_u32 s69, s69, 32768\n\t" \
    "v_lshl_add_u32 v34, v223, 4, s69\n\t" \
    "s_mov_b32 s69, 17476\n\t" \
    "v_bfe_u32 v184, v223, 1, 1\n\t" \
    "v_mov_b32 v10, s69\n\t" \
    "s_mov_b32 s70, 43690\n\t" \
    "v_mad_u32_u24 v10, v184, s70, v10\n\t" \
    "v_mov_b32 v11, 0\n\t" \
    "s_waitcnt vmcnt(12) lgkmcnt(0)\n\t" \
    "s_barrier\n\t" \
    "s_add_u32 m0, s82, 65536\n\t" \
    "s_nop 0\n\t" \
    "global_load_lds_dwordx4 v200, s[42:43]\n\t" \
    "v_add_u32 v200, 131072, v200\n\t" \
    "s_add_u32 m0, s82, 69632\n\t" \
    "s_nop 0\n\t" \
    "global_load_lds_dwordx4 v200, s[42:43]\n\t" \
    "v_add_u32 v200, 131072, v200\n\t" \
    "s_add_u32 m0, s82, 73728\n\t" \
    "s_nop 0\n\t" \
    "global_load_lds_dwordx4 v200, s[42:43]\n\t" \
    "v_add_u32 v200, 131072, v200\n\t" \
    "s_add_u32 m0, s82, 77824\n\t" \
    "s_nop 0\n\t" \
    "global_load_lds_dwordx4 v200, s[42:43]\n\t" \
    "v_add_u32 v200, 131072, v200\n\t" \
    "ds_read_b64_tr_b16 a[0:1], v202 offset:36864\n\t" \
    "ds_read_b64_tr_b16 a[2:3], v202 offset:37888\n\t" \
    "ds_read_b64_tr_b16 a[4:5], v202 offset:45056\n\t" \
    "ds_read_b64_tr_b16 a[6:7], v202 offset:46080\n\t" \
    "ds_read_b64_tr_b16 a[72:73], v203 offset:36864\n\t" \
    "ds_read_b64_tr_b16 a[74:75], v203 offset:37888\n\t" \
    "ds_read_b64_tr_b16 a[76:77], v203 offset:45056\n\t" \
    "ds_read_b64_tr_b16 a[78:79], v203 offset:46080\n\t" \
    "s_mul_i32 s69, s66, 512\n\t" \
    "s_add_u32 s69, s69, 16384\n\t" \
    "v_and_b32 v185, 13, v223\n\t" \
    "v_lshl_add_u32 v12, v185, 5, s69\n\t" \
    "v_lshl_add_u32 v12, v184, 2, v12\n\t" \
    "v_lshrrev_b32 v186, 4, v223\n\t" \
    "v_lshl_add_u32 v12, v186, 3, v12\n\t" \
    "s_mov_b32 s69, 16384\n\t" \
    "v_and_b32 v187, 15, v223\n\t" \
    "v_lshl_add_u32 v13, v187, 3, s69\n\t" \
    "v_lshl_add_u32 v13, v186, 8, v13\n\t" \
    "s_bfe_u32 s71, s67, 0x20003\n\t" \
    "s_mul_i32 s69, s71, 65536\n\t" \
    "s_add_u32 s69, s69, 266240\n\t" \
    "v_and_b32 v188, 31, v223\n\t" \
    "v_lshl_add_u32 v20, v188, 5, s69\n\t" \
    "v_lshl_add_u32 v20, v181, 2, v20\n\t" \
    "s_mul_i32 s69, s67, 32\n\t" \
    "s_add_u32 s69, s69, 262144\n\t" \
    "v_lshl_add_u32 v21, v180, 16, s69\n\t" \
    "v_bfe_u32 v189, v223, 2, 3\n\t" \
    "v_lshlrev_b32 v199, 10, v189\n\t" \
    "v_min_u32 v199, 4096, v199\n\t" \
    "v_add_u32 v21, v199, v21\n\t" \
    "s_bfe_u32 s71, s67, 0x20003\n\t" \
    "s_mul_i32 s69, s71, 65536\n\t" \
    "s_add_u32 s69, s69, 267264\n\t" \
    "v_min_u32 v35, 35, v223\n\t" \
    "v_lshl_add_u32 v35, v35, 5, s69\n\t" \
    "v_lshlrev_b32 v22, 2, v223\n\t" \
    "s_waitcnt vmcnt(12) lgkmcnt(0)\n\t" \
    "s_barrier\n\t" \
    "s_add_u32 m0, s82, 81920\n\t" \
    "s_nop 0\n\t" \
    "global_load_lds_dwordx4 v200, s[42:43]\n\t" \
    "v_add_u32 v200, 131072, v200\n\t" \
    "s_add_u32 m0, s82, 86016\n\t" \
    "s_nop 0\n\t" \
    "global_load_lds_dwordx4 v200, s[42:43]\n\t" \
    "v_add_u32 v200, 131072, v200\n\t" \
    "s_add_u32 m0, s82, 90112\n\t" \
    "s_nop 0\n\t" \
    "global_load_lds_dwordx4 v200, s[42:43]\n\t" \
    "v_add_u32 v200, 131072, v200\n\t" \
    "s_add_u32 m0, s82, 94208\n\t" \
    "s_nop 0\n\t" \
    "global_load_lds_dwordx4 v200, s[42:43]\n\t" \
    "v_add_u32 v200, 131072, v200\n\t" \
    "ds_read_b64_tr_b16 a[8:9], v202 offset:53248\n\t" \
    "ds_read_b64_tr_b16 a[10:11], v202 offset:54272\n\t" \
    "ds_read_b64_tr_b16 a[12:13], v202 offset:61440\n\t" \
    "ds_read_b64_tr_b16 a[14:15], v202 offset:62464\n\t" \
    "ds_read_b64_tr_b16 a[80:81], v203 offset:53248\n\t" \
    "ds_read_b64_tr_b16 a[82:83], v203 offset:54272\n\t" \
    "ds_read_b64_tr_b16 a[84:85], v203 offset:61440\n\t" \
    "ds_read_b64_tr_b16 a[86:87], v203 offset:62464\n\t" \
    "s_mul_i32 s69, s67, 128\n\t" \
    "s_add_u32 s69, s69, 6127616\n\t" \
    "v_mov_b32 v14, s69\n\t" \
    "v_and_b32 v190, 57, v223\n\t" \
    "v_lshlrev_b32 v23, 4, v190\n\t" \
    "v_lshl_add_u32 v23, v184, 6, v23\n\t" \
    "v_bfe_u32 v191, v223, 2, 1\n\t" \
    "v_lshl_add_u32 v23, v191, 5, v23\n\t" \
    "v_add_u32 v15, 4096, v23\n\t" \
    "s_mul_i32 s69, s66, 512\n\t" \
    "s_add_u32 s69, s69, 8192\n\t" \
    "v_lshl_add_u32 v16, v187, 4, s69\n\t" \
    "v_lshl_add_u32 v16, v186, 2, v16\n\t" \
    "s_mov_b32 s69, 8192\n\t" \
    "v_and_b32 v192, 1, v223\n\t" \
    "v_lshl_add_u32 v17, v192, 3, s69\n\t" \
    "v_lshrrev_b32 v193, 2, v223\n\t" \
    "v_lshl_add_u32 v17, v193, 4, v17\n\t" \
    "s_mul_i32 s69, s67, 128\n\t" \
    "s_add_u32 s69, s69, 6160384\n\t" \
    "v_mov_b32 v24, s69\n\t" \
    "s_waitcnt vmcnt(12) lgkmcnt(0)\n\t" \
    "s_barrier\n\t" \
    "s_add_u32 m0, s82, 98304\n\t" \
    "s_nop 0\n\t" \
    "global_load_lds_dwordx4 v200, s[42:43]\n\t" \
    "v_add_u32 v200, 131072, v200\n\t" \
    "s_add_u32 m0, s82, 102400\n\t" \
    "s_nop 0\n\t" \
    "global_load_lds_dwordx4 v200, s[42:43]\n\t" \
    "v_add_u32 v200, 131072, v200\n\t" \
    "s_add_u32 m0, s82, 106496\n\t" \
    "s_nop 0\n\t" \
    "global_load_lds_dwordx4 v200, s[42:43]\n\t" \
    "v_add_u32 v200, 131072, v200\n\t" \
    "s_add_u32 m0, s82, 110592\n\t" \
    "s_nop 0\n\t" \
    "global_load_lds_dwordx4 v200, s[42:43]\n\t" \
    "v_add_u32 v200, 131072, v200\n\t" \
    "v_add_u32 v202, 69632, v202\n\t" \
    "v_add_u32 v203, 69632, v203\n\t" \
    "ds_read_b64_tr_b16 a[16:17], v202 offset:0\n\t" \
    "ds_read_b64_tr_b16 a[18:19], v202 offset:1024\n\t" \
    "ds_read_b64_tr_b16 a[20:21], v202 offset:8192\n\t" \
    "ds_read_b64_tr_b16 a[22:23], v202 offset:9216\n\t" \
    "ds_read_b64_tr_b16 a[88:89], v203 offset:0\n\t" \
    "ds_read_b64_tr_b16 a[90:91], v203 offset:1024\n\t" \
    "ds_read_b64_tr_b16 a[92:93], v203 offset:8192\n\t" \
    "ds_read_b64_tr_b16 a[94:95], v203 offset:9216\n\t" \
    "s_bfe_u32 s71, s66, 0x10000\n\t" \
    "s_mul_i32 s69, s71, 128\n\t" \
    "s_bfe_u32 s71, s67, 0x40000\n\t" \
    "s_mul_i32 s71, s71, 512\n\t" \
    "s_add_u32 s69, s69, s71\n\t" \
    "s_bfe_u32 s71, s67, 0x10004\n\t" \
    "s_mul_i32 s71, s71, 16384\n\t" \
    "s_add_u32 s69, s69, s71\n\t" \
    "v_lshl_add_u32 v18, v180, 2, s69\n\t" \
    "v_lshl_add_u32 v18, v191, 5, v18\n\t" \
    "v_bfe_u32 v194, v223, 3, 1\n\t" \
    "v_lshl_add_u32 v18, v194, 8, v18\n\t" \
    "v_lshl_add_u32 v18, v183, 4, v18\n\t" \
    "v_lshl_add_u32 v18, v181, 6, v18\n\t" \
    "s_bfe_u32 s71, s66, 0x10000\n\t" \
    "s_mul_i32 s69, s71, 128\n\t" \
    "s_bfe_u32 s71, s67, 0x10000\n\t" \
    "s_mul_i32 s71, s71, 8\n\t" \
    "s_add_u32 s69, s69, s71\n\t" \
    "s_bfe_u32 s71, s67, 0x40001\n\t" \
    "s_mul_i32 s71, s71, 256\n\t" \
    "s_add_u32 s69, s69, s71\n\t" \
    "s_add_u32 s69, s69, 49152\n\t" \
    "v_lshl_add_u32 v19, v184, 5, s69\n\t" \
    "v_and_b32 v195, 5, v193\n\t" \
    "v_lshl_add_u32 v19, v195, 2, v19\n\t" \
    "v_lshl_add_u32 v19, v181, 6, v19\n\t" \
    "v_add_u32 v3, 65536, v18\n\t" \
    "v_add_u32 v4, 65536, v19\n\t" \
    "s_waitcnt vmcnt(12) lgkmcnt(0)\n\t" \
    "s_barrier\n\t" \
    "s_add_u32 m0, s82, 0\n\t" \
    "s_nop 0\n\t" \
    "global_load_lds_dwordx4 v200, s[42:43]\n\t" \
    "v_add_u32 v200, 131072, v200\n\t" \
    "s_add_u32 m0, s82, 4096\n\t" \
    "s_nop 0\n\t" \
    "global_load_lds_dwordx4 v200, s[42:43]\n\t" \
    "v_add_u32 v200, 131072, v200\n\t" \
    "s_add_u32 m0, s82, 8192\n\t" \
    "s_nop 0\n\t" \
    "global_load_lds_dwordx4 v200, s[42:43]\n\t" \
    "v_add_u32 v200, 131072, v200\n\t" \
    "s_add_u32 m0, s82, 12288\n\t" \
    "s_nop 0\n\t" \
    "global_load_lds_dwordx4 v200, s[42:43]\n\t" \
    "v_add_u32 v200, 131072, v200\n\t" \
    "ds_read_b64_tr_b16 a[24:25], v202 offset:16384\n\t" \
    "ds_read_b64_tr_b16 a[26:27], v202 offset:17408\n\t" \
    "ds_read_b64_tr_b16 a[28:29], v202 offset:24576\n\t" \
    "ds_read_b64_tr_b16 a[30:31], v202 offset:25600\n\t" \
    "ds_read_b64_tr_b16 a[96:97], v203 offset:16384\n\t" \
    "ds_read_b64_tr_b16 a[98:99], v203 offset:17408\n\t" \
    "ds_read_b64_tr_b16 a[100:101], v203 offset:24576\n\t" \
    "ds_read_b64_tr_b16 a[102:103], v203 offset:25600\n\t" \
    "s_mov_b32 s77, 3019898880\n\t" \
    "s_mov_b32 s78, 3054198966\n\t" \
    "s_mov_b32 s71, 3\n\t" \
    "qk_E1_%=:\n\t" \
    "s_memtime s[54:55]\n\t" \
    "s_nop 0\n\t" \
    "s_memrealtime s[4:5]\n\t" \
    "s_nop 0\n\t" \
    "s_memrealtime s[6:7]\n\t" \
    "s_nop 0\n\t" \
    "s_memrealtime s[8:9]\n\t" \
    "s_nop 0\n\t" \
    "s_memrealtime s[10:11]\n\t" \
    "s_nop 0\n\t" \
    "s_memrealtime s[12:13]\n\t" \
    "s_nop 0\n\t" \
    "s_memrealtime s[14:15]\n\t" \
    "s_nop 0\n\t" \
    "s_memrealtime s[16:17]\n\t" \
    "s_nop 0\n\t" \
    "s_memrealtime s[18:19]\n\t" \
    "s_nop 0\n\t" \
    "s_memrealtime s[20:21]\n\t" \
    "s_nop 0\n\t" \
    "s_memrealtime s[22:23]\n\t" \
    "s_nop 0\n\t" \
    "s_memrealtime s[24:25]\n\t" \
    "s_nop 0\n\t" \
    "s_memrealtime s[26:27]\n\t" \
    "s_nop 0\n\t" \
    "s_memrealtime s[28:29]\n\t" \
    "s_nop 0\n\t" \
    "s_memtime s[56:57]\n\t" \
    "s_mov_b32 s70, 1\n\t" \
    "s_sub_u32 s71, s71, 1\n\t" \
    "s_cselect_b32 s69, 0, -1\n\t" \
    "s_waitcnt lgkmcnt(0)\n\t" \
    "s_sub_u32 s54, s56, s54\n\t" \
    "s_and_b32 s54, s54, s69\n\t" \
    "s_cmp_gt_u32 s54, 124\n\t" \
    "s_cbranch_scc1 qk_E1_%=\n\t" \
    "s_cmp_eq_u32 s4, s28\n\t" \
    "s_addc_u32 s70, s70, 0\n\t" \
    "s_cmp_eq_u32 s6, s28\n\t" \
    "s_addc_u32 s70, s70, 0\n\t" \
    "s_cmp_eq_u32 s8, s28\n\t" \
    "s_addc_u32 s70, s70, 0\n\t" \
    "s_cmp_eq_u32 s10, s28\n\t" \
    "s_addc_u32 s70, s70, 0\n\t" \
    "s_cmp_eq_u32 s12, s28\n\t" \
    "s_addc_u32 s70, s70, 0\n\t" \
    "s_cmp_eq_u32 s14, s28\n\t" \
    "s_addc_u32 s70, s70, 0\n\t" \
    "s_cmp_eq_u32 s16, s28\n\t" \
    "s_addc_u32 s70, s70, 0\n\t" \
    "s_cmp_eq_u32 s18, s28\n\t" \
    "s_addc_u32 s70, s70, 0\n\t" \
    "s_cmp_eq_u32 s20, s28\n\t" \
    "s_addc_u32 s70, s70, 0\n\t" \
    "s_cmp_eq_u32 s22, s28\n\t" \
    "s_addc_u32 s70, s70, 0\n\t" \
    "s_cmp_eq_u32 s24, s28\n\t" \
    "s_addc_u32 s70, s70, 0\n\t" \
    "s_cmp_eq_u32 s26, s28\n\t" \
    "s_addc_u32 s70, s70, 0\n\t" \
    "s_mov_b32 s75, s73\n\t" \
    "s_mov_b32 s76, s74\n\t" \
    "s_lshl_b32 s70, s70, 3\n\t" \
    "s_sub_u32 s73, s56, s70\n\t" \
    "s_lshl_b32 s74, s28, 4\n\t" \
    "s_waitcnt vmcnt(12) lgkmcnt(0)\n\t" \
    "s_barrier\n\t" \
    "s_add_u32 m0, s82, 16384\n\t" \
    "s_nop 0\n\t" \
    "global_load_lds_dwordx4 v200, s[42:43]\n\t" \
    "v_add_u32 v200, 131072, v200\n\t" \
    "s_add_u32 m0, s82, 20480\n\t" \
    "s_nop 0\n\t" \
    "global_load_lds_dwordx4 v200, s[42:43]\n\t" \
    "v_add_u32 v200, 131072, v200\n\t" \
    "s_add_u32 m0, s82, 24576\n\t" \
    "s_nop 0\n\t" \
    "global_load_lds_dwordx4 v200, s[42:43]\n\t" \
    "v_add_u32 v200, 131072, v200\n\t" \
    "s_add_u32 m0, s82, 28672\n\t" \
    "s_nop 0\n\t" \
    "global_load_lds_dwordx4 v200, s[42:43]\n\t" \
    "v_add_u32 v200, 131072, v200\n\t" \
    "ds_read_b64_tr_b16 a[32:33], v202 offset:32768\n\t" \
    "ds_read_b64_tr_b16 a[34:35], v202 offset:33792\n\t" \
    "ds_read_b64_tr_b16 a[36:37], v202 offset:40960\n\t" \
    "ds_read_b64_tr_b16 a[38:39], v202 offset:41984\n\t" \
    "ds_read_b64_tr_b16 a[104:105], v203 offset:32768\n\t" \
    "ds_read_b64_tr_b16 a[106:107], v203 offset:33792\n\t" \
    "ds_read_b64_tr_b16 a[108:109], v203 offset:40960\n\t" \
    "ds_read_b64_tr_b16 a[110:111], v203 offset:41984\n\t" \
    "s_waitcnt vmcnt(12) lgkmcnt(0)\n\t" \
    "s_barrier\n\t" \
    "v_subrev_u32 v200, 4718592, v200\n\t" \
    "s_add_u32 m0, s82, 32768\n\t" \
    "s_nop 0\n\t" \
    "global_load_lds_dwordx4 v200, s[44:45]\n\t" \
    "v_add_u32 v200, 131072, v200\n\t" \
    "s_add_u32 m0, s82, 36864\n\t" \
    "s_nop 0\n\t" \
    "global_load_lds_dwordx4 v200, s[44:45]\n\t" \
    "v_add_u32 v200, 131072, v200\n\t" \
    "s_add_u32 m0, s82, 40960\n\t" \
    "s_nop 0\n\t" \
    "global_load_lds_dwordx4 v200, s[44:45]\n\t" \
    "v_add_u32 v200, 131072, v200\n\t" \
    "s_add_u32 m0, s82, 45056\n\t" \
    "s_nop 0\n\t" \
    "global_load_lds_dwordx4 v200, s[44:45]\n\t" \
    "v_add_u32 v200, 131072, v200\n\t" \
    "ds_read_b64_tr_b16 a[40:41], v202 offset:49152\n\t" \
    "ds_read_b64_tr_b16 a[42:43], v202 offset:50176\n\t" \
    "ds_read_b64_tr_b16 a[44:45], v202 offset:57344\n\t" \
    "ds_read_b64_tr_b16 a[46:47], v202 offset:58368\n\t" \
    "ds_read_b64_tr_b16 a[112:113], v203 offset:49152\n\t" \
    "ds_read_b64_tr_b16 a[114:115], v203 offset:50176\n\t" \
    "ds_read_b64_tr_b16 a[116:117], v203 offset:57344\n\t" \
    "ds_read_b64_tr_b16 a[118:119], v203 offset:58368\n\t" \
    "s_waitcnt vmcnt(12) lgkmcnt(0)\n\t" \
    "s_barrier\n\t" \
    "s_add_u32 m0, s82, 49152\n\t" \
    "s_nop 0\n\t" \
    "global_load_lds_dwordx4 v200, s[44:45]\n\t" \
    "v_add_u32 v200, 131072, v200\n\t" \
    "s_add_u32 m0, s82, 53248\n\t" \
    "s_nop 0\n\t" \
    "global_load_lds_dwordx4 v200, s[44:45]\n\t" \
    "v_add_u32 v200, 131072, v200\n\t" \
    "s_add_u32 m0, s82, 57344\n\t" \
    "s_nop 0\n\t" \
    "global_load_lds_dwordx4 v200, s[44:45]\n\t" \
    "v_add_u32 v200, 131072, v200\n\t" \
    "s_add_u32 m0, s82, 61440\n\t" \
    "s_nop 0\n\t" \
    "global_load_lds_dwordx4 v200, s[44:45]\n\t" \
    "v_add_u32 v200, 131072, v200\n\t" \
    "v_add_u32 v202, 65536, v202\n\t" \
    "v_add_u32 v203, 65536, v203\n\t" \
    "ds_read_b64_tr_b16 a[48:49], v202 offset:0\n\t" \
    "ds_read_b64_tr_b16 a[50:51], v202 offset:1024\n\t" \
    "ds_read_b64_tr_b16 a[52:53], v202 offset:8192\n\t" \
    "ds_read_b64_tr_b16 a[54:55], v202 offset:9216\n\t" \
    "ds_read_b64_tr_b16 a[120:121], v203 offset:0\n\t" \
    "ds_read_b64_tr_b16 a[122:123], v203 offset:1024\n\t" \
    "ds_read_b64_tr_b16 a[124:125], v203 offset:8192\n\t" \
    "ds_read_b64_tr_b16 a[126:127], v203 offset:9216\n\t" \
    "s_waitcnt vmcnt(12) lgkmcnt(0)\n\t" \
    "s_barrier\n\t" \
    "s_add_u32 m0, s82, 65536\n\t" \
    "s_nop 0\n\t" \
    "global_load_lds_dwordx4 v200, s[44:45]\n\t" \
    "v_add_u32 v200, 131072, v200\n\t" \
    "s_add_u32 m0, s82, 69632\n\t" \
    "s_nop 0\n\t" \
    "global_load_lds_dwordx4 v200, s[44:45]\n\t" \
    "v_add_u32 v200, 131072, v200\n\t" \
    "s_add_u32 m0, s82, 73728\n\t" \
    "s_nop 0\n\t" \
    "global_load_lds_dwordx4 v200, s[44:45]\n\t" \
    "v_add_u32 v200, 131072, v200\n\t" \
    "s_add_u32 m0, s82, 77824\n\t" \
    "s_nop 0\n\t" \
    "global_load_lds_dwordx4 v200, s[44:45]\n\t" \
    "v_add_u32 v200, 131072, v200\n\t" \
    "v_add_u32 v202, -98304, v202\n\t" \
    "v_add_u32 v203, -98304, v203\n\t" \
    "ds_read_b64_tr_b16 a[56:57], v202 offset:0\n\t" \
    "ds_read_b64_tr_b16 a[58:59], v202 offset:1024\n\t" \
    "ds_read_b64_tr_b16 a[60:61], v202 offset:8192\n\t" \
    "ds_read_b64_tr_b16 a[62:63], v202 offset:9216\n\t" \
    "ds_read_b64_tr_b16 a[128:129], v203 offset:0\n\t" \
    "ds_read_b64_tr_b16 a[130:131], v203 offset:1024\n\t" \
    "ds_read_b64_tr_b16 a[132:133], v203 offset:8192\n\t" \
    "ds_read_b64_tr_b16 a[134:135], v203 offset:9216\n\t" \
    "s_waitcnt vmcnt(12) lgkmcnt(0)\n\t" \
    "s_barrier\n\t" \
    "s_add_u32 m0, s82, 81920\n\t" \
    "s_nop 0\n\t" \
    "global_load_lds_dwordx4 v200, s[44:45]\n\t" \
    "v_add_u32 v200, 131072, v200\n\t" \
    "s_add_u32 m0, s82, 86016\n\t" \
    "s_nop 0\n\t" \
    "global_load_lds_dwordx4 v200, s[44:45]\n\t" \
    "v_add_u32 v200, 131072, v200\n\t" \
    "s_add_u32 m0, s82, 90112\n\t" \
    "s_nop 0\n\t" \
    "global_load_lds_dwordx4 v200, s[44:45]\n\t" \
    "v_add_u32 v200, 131072, v200\n\t" \
    "s_add_u32 m0, s82, 94208\n\t" \
    "s_nop 0\n\t" \
    "global_load_lds_dwordx4 v200, s[44:45]\n\t" \
    "v_add_u32 v200, 131072, v200\n\t" \
    "ds_read_b64_tr_b16 a[64:65], v202 offset:16384\n\t" \
    "ds_read_b64_tr_b16 a[66:67], v202 offset:17408\n\t" \
    "ds_read_b64_tr_b16 a[68:69], v202 offset:24576\n\t" \
    "ds_read_b64_tr_b16 a[70:71], v202 offset:25600\n\t" \
    "ds_read_b64_tr_b16 a[136:137], v203 offset:16384\n\t" \
    "ds_read_b64_tr_b16 a[138:139], v203 offset:17408\n\t" \
    "ds_read_b64_tr_b16 a[140:141], v203 offset:24576\n\t" \
    "ds_read_b64_tr_b16 a[142:143], v203 offset:25600\n\t" \
    "s_waitcnt vmcnt(12) lgkmcnt(0)\n\t" \
    "s_barrier\n\t" \
    "s_add_u32 m0, s82, 98304\n\t" \
    "s_nop 0\n\t" \
    "global_load_lds_dwordx4 v200, s[44:45]\n\t" \
    "v_add_u32 v200, 131072, v200\n\t" \
    "s_add_u32 m0, s82, 102400\n\t" \
    "s_nop 0\n\t" \
    "global_load_lds_dwordx4 v200, s[44:45]\n\t" \
    "v_add_u32 v200, 131072, v200\n\t" \
    "s_add_u32 m0, s82, 106496\n\t" \
    "s_nop 0\n\t" \
    "global_load_lds_dwordx4 v200, s[44:45]\n\t" \
    "v_add_u32 v200, 131072, v200\n\t" \
    "s_add_u32 m0, s82, 110592\n\t" \
    "s_nop 0\n\t" \
    "global_load_lds_dwordx4 v200, s[44:45]\n\t" \
    "v_add_u32 v200, 131072, v200\n\t" \
    "ds_read_b64_tr_b16 a[144:145], v202 offset:32768\n\t" \
    "ds_read_b64_tr_b16 a[146:147], v202 offset:33792\n\t" \
    "ds_read_b64_tr_b16 a[148:149], v202 offset:40960\n\t" \
    "ds_read_b64_tr_b16 a[150:151], v202 offset:41984\n\t" \
    "ds_read_b64_tr_b16 a[216:217], v203 offset:32768\n\t" \
    "ds_read_b64_tr_b16 a[218:219], v203 offset:33792\n\t" \
    "ds_read_b64_tr_b16 a[220:221], v203 offset:40960\n\t" \
    "ds_read_b64_tr_b16 a[222:223], v203 offset:41984\n\t" \
    "s_waitcnt vmcnt(12) lgkmcnt(0)\n\t" \
    "s_barrier\n\t" \
    "s_add_u32 m0, s82, 0\n\t" \
    "s_nop 0\n\t" \
    "global_load_lds_dwordx4 v200, s[44:45]\n\t" \
    "v_add_u32 v200, 131072, v200\n\t" \
    "s_add_u32 m0, s82, 4096\n\t" \
    "s_nop 0\n\t" \
    "global_load_lds_dwordx4 v200, s[44:45]\n\t" \
    "v_add_u32 v200, 131072, v200\n\t" \
    "s_add_u32 m0, s82, 8192\n\t" \
    "s_nop 0\n\t" \
    "global_load_lds_dwordx4 v200, s[44:45]\n\t" \
    "v_add_u32 v200, 131072, v200\n\t" \
    "s_add_u32 m0, s82, 12288\n\t" \
    "s_nop 0\n\t" \
    "global_load_lds_dwordx4 v200, s[44:45]\n\t" \
    "v_add_u32 v200, 131072, v200\n\t" \
    "ds_read_b64_tr_b16 a[152:153], v202 offset:49152\n\t" \
    "ds_read_b64_tr_b16 a[154:155], v202 offset:50176\n\t" \
    "ds_read_b64_tr_b16 a[156:157], v202 offset:57344\n\t" \
    "ds_read_b64_tr_b16 a[158:159], v202 offset:58368\n\t" \
    "ds_read_b64_tr_b16 a[224:225], v203 offset:49152\n\t" \
    "ds_read_b64_tr_b16 a[226:227], v203 offset:50176\n\t" \
    "ds_read_b64_tr_b16 a[228:229], v203 offset:57344\n\t" \
    "ds_read_b64_tr_b16 a[230:231], v203 offset:58368\n\t" \
    "s_waitcnt vmcnt(12) lgkmcnt(0)\n\t" \
    "s_barrier\n\t" \
    "s_add_u32 m0, s82, 16384\n\t" \
    "s_nop 0\n\t" \
    "global_load_lds_dwordx4 v200, s[44:45]\n\t" \
    "v_add_u32 v200, 131072, v200\n\t" \
    "s_add_u32 m0, s82, 20480\n\t" \
    "s_nop 0\n\t" \
    "global_load_lds_dwordx4 v200, s[44:45]\n\t" \
    "v_add_u32 v200, 131072, v200\n\t" \
    "s_add_u32 m0, s82, 24576\n\t" \
    "s_nop 0\n\t" \
    "global_load_lds_dwordx4 v200, s[44:45]\n\t" \
    "v_add_u32 v200, 131072, v200\n\t" \
    "s_add_u32 m0, s82, 28672\n\t" \
    "s_nop 0\n\t" \
    "global_load_lds_dwordx4 v200, s[44:45]\n\t" \
    "v_add_u32 v200, 131072, v200\n\t" \
    "v_add_u32 v202, 65536, v202\n\t" \
    "v_add_u32 v203, 65536, v203\n\t" \
    "ds_read_b64_tr_b16 a[160:161], v202 offset:0\n\t" \
    "ds_read_b64_tr_b16 a[162:163], v202 offset:1024\n\t" \
    "ds_read_b64_tr_b16 a[164:165], v202 offset:8192\n\t" \
    "ds_read_b64_tr_b16 a[166:167], v202 offset:9216\n\t" \
    "ds_read_b64_tr_b16 a[232:233], v203 offset:0\n\t" \
    "ds_read_b64_tr_b16 a[234:235], v203 offset:1024\n\t" \
    "ds_read_b64_tr_b16 a[236:237], v203 offset:8192\n\t" \
    "ds_read_b64_tr_b16 a[238:239], v203 offset:9216\n\t" \
    "s_waitcnt vmcnt(12) lgkmcnt(0)\n\t" \
    "s_barrier\n\t" \
    "s_add_u32 m0, s82, 32768\n\t" \
    "s_nop 0\n\t" \
    "global_load_lds_dwordx4 v200, s[44:45]\n\t" \
    "v_add_u32 v200, 131072, v200\n\t" \
    "s_add_u32 m0, s82, 36864\n\t" \
    "s_nop 0\n\t" \
    "global_load_lds_dwordx4 v200, s[44:45]\n\t" \
    "v_add_u32 v200, 131072, v200\n\t" \
    "s_add_u32 m0, s82, 40960\n\t" \
    "s_nop 0\n\t" \
    "global_load_lds_dwordx4 v200, s[44:45]\n\t" \
    "v_add_u32 v200, 131072, v200\n\t" \
    "s_add_u32 m0, s82, 45056\n\t" \
    "s_nop 0\n\t" \
    "global_load_lds_dwordx4 v200, s[44:45]\n\t" \
    "v_add_u32 v200, 131072, v200\n\t" \
    "ds_read_b64_tr_b16 a[168:169], v202 offset:16384\n\t" \
    "ds_read_b64_tr_b16 a[170:171], v202 offset:17408\n\t" \
    "ds_read_b64_tr_b16 a[172:173], v202 offset:24576\n\t" \
    "ds_read_b64_tr_b16 a[174:175], v202 offset:25600\n\t" \
    "ds_read_b64_tr_b16 a[240:241], v203 offset:16384\n\t" \
    "ds_read_b64_tr_b16 a[242:243], v203 offset:17408\n\t" \
    "ds_read_b64_tr_b16 a[244:245], v203 offset:24576\n\t" \
    "ds_read_b64_tr_b16 a[246:247], v203 offset:25600\n\t" \
    "s_waitcnt vmcnt(12) lgkmcnt(0)\n\t" \
    "s_barrier\n\t" \
    "s_add_u32 m0, s82, 49152\n\t" \
    "s_nop 0\n\t" \
    "global_load_lds_dwordx4 v200, s[44:45]\n\t" \
    "v_add_u32 v200, 131072, v200\n\t" \
    "s_add_u32 m0, s82, 53248\n\t" \
    "s_nop 0\n\t" \
    "global_load_lds_dwordx4 v200, s[44:45]\n\t" \
    "v_add_u32 v200, 131072, v200\n\t" \
    "s_add_u32 m0, s82, 57344\n\t" \
    "s_nop 0\n\t" \
    "global_load_lds_dwordx4 v200, s[44:45]\n\t" \
    "v_add_u32 v200, 131072, v200\n\t" \
    "s_add_u32 m0, s82, 61440\n\t" \
    "s_nop 0\n\t" \
    "global_load_lds_dwordx4 v200, s[44:45]\n\t" \
    "v_add_u32 v200, 131072, v200\n\t" \
    "ds_read_b64_tr_b16 a[176:177], v202 offset:32768\n\t" \
    "ds_read_b64_tr_b16 a[178:179], v202 offset:33792\n\t" \
    "ds_read_b64_tr_b16 a[180:181], v202 offset:40960\n\t" \
    "ds_read_b64_tr_b16 a[182:183], v202 offset:41984\n\t" \
    "ds_read_b64_tr_b16 a[248:249], v203 offset:32768\n\t" \
    "ds_read_b64_tr_b16 a[250:251], v203 offset:33792\n\t" \
    "ds_read_b64_tr_b16 a[252:253], v203 offset:40960\n\t" \
    "ds_read_b64_tr_b16 a[254:255], v203 offset:41984\n\t" \
    "s_waitcnt vmcnt(12) lgkmcnt(0)\n\t" \
    "s_barrier\n\t" \
    "v_mbcnt_lo_u32_b32 v160, -1, 0\n\t" \
    "v_mbcnt_hi_u32_b32 v160, -1, v160\n\t" \
    "v_min_u32 v160, 59, v160\n\t" \
    "v_lshlrev_b32 v160, 6, v160\n\t" \
    "v_add_u32 v160, 6225920, v160\n\t" \
    "v_mov_b32 v161, 6234112\n\t" \
    "global_load_dword v161, v161, s[36:37] sc1\n\t" \
    "global_load_dwordx2 v[162:163], v160, s[36:37] sc1\n\t" \
    "v_add_u32 v202, -65536, v202\n\t" \
    "v_add_u32 v203, -65536, v203\n\t" \
    "ds_read_b64_tr_b16 a[184:185], v202 offset:0\n\t" \
    "ds_read_b64_tr_b16 a[186:187], v202 offset:1024\n\t" \
    "ds_read_b64_tr_b16 a[188:189], v202 offset:8192\n\t" \
    "ds_read_b64_tr_b16 a[190:191], v202 offset:9216\n\t" \
    "ds_read_b64_tr_b16 v[224:225], v203 offset:0\n\t" \
    "ds_read_b64_tr_b16 v[226:227], v203 offset:1024\n\t" \
    "ds_read_b64_tr_b16 v[228:229], v203 offset:8192\n\t" \
    "ds_read_b64_tr_b16 v[230:231], v203 offset:9216\n\t" \
    "s_waitcnt vmcnt(10) lgkmcnt(0)\n\t" \
    "s_barrier\n\t" \
    "ds_read_b64_tr_b16 a[192:193], v202 offset:16384\n\t" \
    "ds_read_b64_tr_b16 a[194:195], v202 offset:17408\n\t" \
    "ds_read_b64_tr_b16 a[196:197], v202 offset:24576\n\t" \
    "ds_read_b64_tr_b16 a[198:199], v202 offset:25600\n\t" \
    "ds_read_b64_tr_b16 v[232:233], v203 offset:16384\n\t" \
    "ds_read_b64_tr_b16 v[234:235], v203 offset:17408\n\t" \
    "ds_read_b64_tr_b16 v[236:237], v203 offset:24576\n\t" \
    "ds_read_b64_tr_b16 v[238:239], v203 offset:25600\n\t" \
    "s_waitcnt vmcnt(6) lgkmcnt(0)\n\t" \
    "s_barrier\n\t" \
    "ds_read_b64_tr_b16 a[200:201], v202 offset:32768\n\t" \
    "ds_read_b64_tr_b16 a[202:203], v202 offset:33792\n\t" \
    "ds_read_b64_tr_b16 a[204:205], v202 offset:40960\n\t" \
    "ds_read_b64_tr_b16 a[206:207], v202 offset:41984\n\t" \
    "ds_read_b64_tr_b16 v[240:241], v203 offset:32768\n\t" \
    "ds_read_b64_tr_b16 v[242:243], v203 offset:33792\n\t" \
    "ds_read_b64_tr_b16 v[244:245], v203 offset:40960\n\t" \
    "ds_read_b64_tr_b16 v[246:247], v203 offset:41984\n\t" \
    "s_waitcnt vmcnt(2) lgkmcnt(0)\n\t" \
    "s_barrier\n\t" \
    "ds_read_b64_tr_b16 a[208:209], v202 offset:49152\n\t" \
    "ds_read_b64_tr_b16 a[210:211], v202 offset:50176\n\t" \
    "ds_read_b64_tr_b16 a[212:213], v202 offset:57344\n\t" \
    "ds_read_b64_tr_b16 a[214:215], v202 offset:58368\n\t" \
    "ds_read_b64_tr_b16 v[248:249], v203 offset:49152\n\t" \
    "ds_read_b64_tr_b16 v[250:251], v203 offset:50176\n\t" \
    "ds_read_b64_tr_b16 v[252:253], v203 offset:57344\n\t" \
    "ds_read_b64_tr_b16 v[254:255], v203 offset:58368\n\t" \
    "s_waitcnt vmcnt(0) lgkmcnt(0)\n\t" \
    "s_barrier\n\t" \
    "s_mul_i32 s70, s67, 7\n\t" \
    "s_add_u32 s69, s65, 0\n\t" \
    "s_xor_b32 s69, s69, s70\n\t" \
    "s_and_b32 s69, s69, 63\n\t" \
    "s_lshl_b32 s69, s69, 13\n\t" \
    "s_mul_i32 s70, s67, 524288\n\t" \
    "s_add_u32 s69, s69, s70\n\t" \
    "s_add_u32 s69, s69, 23068672\n\t" \
    "s_add_u32 s50, s36, s69\n\t" \
    "s_addc_u32 s51, s37, 0\n\t" \
    "v_sub_u32 v162, v162, v161\n\t" \
    "v_cmp_ne_u32 vcc, 1, v162\n\t" \
    "s_cbranch_vccz qk_Cok4_%=\n\t" \
    "qk_C3_%=:\n\t" \
    "global_load_dwordx2 v[162:163], v160, s[36:37] sc1\n\t" \
    "s_waitcnt vmcnt(0)\n\t" \
    "v_sub_u32 v162, v162, v161\n\t" \
    "v_cmp_ne_u32 vcc, 1, v162\n\t" \
    "s_cbranch_vccz qk_Cok4_%=\n\t" \
    "s_cmp_eq_u32 s68, 0\n\t" \
    "s_cbranch_scc1 qk_F5_%=\n\t" \
    "s_sub_u32 s68, s68, 1\n\t" \
    "s_sleep 4\n\t" \
    "s_branch qk_C3_%=\n\t" \
    "qk_F5_%=:\n\t" \
    "v_mov_b32 v164, 6095104\n\t" \
    "v_mov_b32 v165, 4\n\t" \
    "global_store_dword v164, v165, s[36:37] sc1\n\t" \
    "s_waitcnt vmcnt(0)\n\t" \
    "s_branch qk_Cok4_%=\n\t" \
    "qk_Cok4_%=:\n\t" \
    "s_cmp_lg_u32 s66, 0\n\t" \
    "s_cbranch_scc1 qk_Ca6_%=\n\t" \
    "v_mov_b32 v160, 6176768\n\t" \
    "s_mov_b64 exec, 1\n\t" \
    "global_atomic_add v160, v162, s[36:37] sc1\n\t" \
    "s_mov_b64 exec, -1\n\t" \
    "qk_Ca6_%=:\n\t" \
    "v_min_u32_dpp v163, v163, v163 row_shl:6 row_mask:0xf bank_mask:0xf\n\t" \
    "s_nop 0\n\t" \
    "v_readlane_b32 s69, v163, 0\n\t" \
    "v_readlane_b32 s70, v163, 1\n\t" \
    "s_nop 1\n\t" \
    "s_cmp_lt_u32 s70, s69\n\t" \
    "s_cselect_b32 s62, 8192, 0\n\t" \
    "s_cselect_b32 s69, s70, s69\n\t" \
    "s_mov_b32 s71, s69\n\t" \
    "v_readlane_b32 s69, v163, 2\n\t" \
    "v_readlane_b32 s70, v163, 3\n\t" \
    "s_nop 1\n\t" \
    "s_cmp_lt_u32 s70, s69\n\t" \
    "s_cselect_b32 s63, 8192, 0\n\t" \
    "s_cselect_b32 s69, s70, s69\n\t" \
    "s_max_u32 s71, s69, s71\n\t" \
    "v_readlane_b32 s69, v163, 4\n\t" \
    "v_readlane_b32 s70, v163, 5\n\t" \
    "s_nop 1\n\t" \
    "s_cmp_lt_u32 s70, s69\n\t" \
    "s_cselect_b32 s64, 8192, 0\n\t" \
    "s_cselect_b32 s69, s70, s69\n\t" \
    "s_max_u32 s71, s69, s71\n\t" \
    "s_add_u32 s63, s63, 0x200000\n\t" \
    "s_xor_b32 s52, s64, 8192\n\t" \
    "s_cmp_gt_u32 s71, 625\n\t" \
    "s_cselect_b32 s69, 1, 0\n\t" \
    "s_mul_i32 s70, s69, 160\n\t" \
    "s_add_u32 s86, s86, s70\n\t" \
    "s_mul_i32 s70, s69, 536\n\t" \
    "s_add_u32 s87, s87, s70\n\t" \
    "s_mul_i32 s70, s69, 256\n\t" \
    "s_add_u32 s88, s88, s70\n\t" \
    "v_add_u32 v14, s52, v14\n\t" \
    "v_add_u32 v24, s52, v24\n\t" \
    "v_mov_b32 v26, 0x3db504f3\n\t" \
    "v_mov_b32 v28, 0x3fb8aa3b\n\t" \
    "v_mov_b32 v27, 0x3db504f3\n\t" \
    "v_mov_b32 v29, 0x3fb8aa3b\n\t" \
    "v_mov_b32 v29, 0x3e0293ee\n\t" \
    "s_mov_b32 s48, 0xaaaaaaaa\n\t" \
    "s_mov_b32 s49, 0xaaaaaaaa\n\t" \
    "s_mov_b32 s58, 0x00ff00ff\n\t" \
    "s_mov_b32 s59, 0x00ff00ff\n\t" \
    "s_lshl_b32 s69, s66, 2\n\t" \
    "v_mov_b32 v30, s69\n\t" \
    "v_mov_b32 v31, 0\n\t" \
    "v_and_b32 v33, 12, v22\n\t" \
    "ds_write_b32 v30, v31 offset:20496\n\t" \
    "s_waitcnt lgkmcnt(0)\n\t" \
    "s_cmp_gt_u32 s66, 1\n\t" \
    "s_cbranch_scc1 qk_nk7_%=\n\t" \
    "qk_WF8_%=:\n\t" \
    "s_cmp_ge_i32 s83, s65\n\t" \
    "s_cbranch_scc1 qk_WFok9_%=\n\t" \
    "global_load_dword v160, v24, s[36:37] sc1\n\t" \
    "s_waitcnt vmcnt(0)\n\t" \
    "v_readfirstlane_b32 s83, v160\n\t" \
    "s_nop 1\n\t" \
    "s_cmp_ge_i32 s83, s65\n\t" \
    "s_cbranch_scc1 qk_WFok9_%=\n\t" \
    "s_cmp_eq_u32 s68, 0\n\t" \
    "s_cbranch_scc1 qk_F10_%=\n\t" \
    "s_sub_u32 s68, s68, 1\n\t" \
    "s_sleep 2\n\t" \
    "s_branch qk_WF8_%=\n\t" \
    "qk_F10_%=:\n\t" \
    "v_mov_b32 v160, 6095104\n\t" \
    "v_mov_b32 v161, 8\n\t" \
    "global_store_dword v160, v161, s[36:37] sc1\n\t" \
    "s_waitcnt vmcnt(0)\n\t" \
    "s_branch qk_WFok9_%=\n\t" \
    "qk_WFok9_%=:\n\t" \
    "global_load_dwordx4 v[100:103], v23, s[50:51] offset:0 sc1\n\t" \
    "global_load_dwordx4 v[104:107], v23, s[50:51] offset:2048 sc1\n\t" \
    "global_load_dwordx4 v[108:111], v15, s[50:51] offset:0 sc1\n\t" \
    "global_load_dwordx4 v[112:115], v15, s[50:51] offset:2048 sc1\n\t" \
    "global_load_dwordx4 v[116:119], v23, s[50:51] offset:1024 sc1\n\t" \
    "global_load_dwordx4 v[120:123], v23, s[50:51] offset:3072 sc1\n\t" \
    "global_load_dwordx4 v[124:127], v15, s[50:51] offset:1024 sc1\n\t" \
    "global_load_dwordx4 v[128:131], v15, s[50:51] offset:3072 sc1\n\t" \
    "qk_nk7_%=:\n\t" \
    "s_barrier\n\t" \
    "s_mov_b32 s71, 3\n\t" \
    "qk_E11_%=:\n\t" \
    "s_memtime s[54:55]\n\t" \
    "s_nop 0\n\t" \
    "s_memrealtime s[4:5]\n\t" \
    "s_nop 0\n\t" \
    "s_memrealtime s[6:7]\n\t" \
    "s_nop 0\n\t" \
    "s_memrealtime s[8:9]\n\t" \
    "s_nop 0\n\t" \
    "s_memrealtime s[10:11]\n\t" \
    "s_nop 0\n\t" \
    "s_memrealtime s[12:13]\n\t" \
    "s_nop 0\n\t" \
    "s_memrealtime s[14:15]\n\t" \
    "s_nop 0\n\t" \
    "s_memrealtime s[16:17]\n\t" \
    "s_nop 0\n\t" \
    "s_memrealtime s[18:19]\n\t" \
    "s_nop 0\n\t" \
    "s_memrealtime s[20:21]\n\t" \
    "s_nop 0\n\t" \
    "s_memrealtime s[22:23]\n\t" \
    "s_nop 0\n\t" \
    "s_memrealtime s[24:25]\n\t" \
    "s_nop 0\n\t" \
    "s_memrealtime s[26:27]\n\t" \
    "s_nop 0\n\t" \
    "s_memrealtime s[28:29]\n\t" \
    "s_nop 0\n\t" \
    "s_memtime s[56:57]\n\t" \
    "s_mov_b32 s70, 1\n\t" \
    "s_sub_u32 s71, s71, 1\n\t" \
    "s_cselect_b32 s69, 0, -1\n\t" \
    "s_waitcnt lgkmcnt(0)\n\t" \
    "s_sub_u32 s54, s56, s54\n\t" \
    "s_and_b32 s54, s54, s69\n\t" \
    "s_cmp_gt_u32 s54, 124\n\t" \
    "s_cbranch_scc1 qk_E11_%=\n\t" \
    "s_cmp_eq_u32 s4, s28\n\t" \
    "s_addc_u32 s70, s70, 0\n\t" \
    "s_cmp_eq_u32 s6, s28\n\t" \
    "s_addc_u32 s70, s70, 0\n\t" \
    "s_cmp_eq_u32 s8, s28\n\t" \
    "s_addc_u32 s70, s70, 0\n\t" \
    "s_cmp_eq_u32 s10, s28\n\t" \
    "s_addc_u32 s70, s70, 0\n\t" \
    "s_cmp_eq_u32 s12, s28\n\t" \
    "s_addc_u32 s70, s70, 0\n\t" \
    "s_cmp_eq_u32 s14, s28\n\t" \
    "s_addc_u32 s70, s70, 0\n\t" \
    "s_cmp_eq_u32 s16, s28\n\t" \
    "s_addc_u32 s70, s70, 0\n\t" \
    "s_cmp_eq_u32 s18, s28\n\t" \
    "s_addc_u32 s70, s70, 0\n\t" \
    "s_cmp_eq_u32 s20, s28\n\t" \
    "s_addc_u32 s70, s70, 0\n\t" \
    "s_cmp_eq_u32 s22, s28\n\t" \
    "s_addc_u32 s70, s70, 0\n\t" \
    "s_cmp_eq_u32 s24, s28\n\t" \
    "s_addc_u32 s70, s70, 0\n\t" \
    "s_cmp_eq_u32 s26, s28\n\t" \
    "s_addc_u32 s70, s70, 0\n\t" \
    "s_mov_b32 s75, s73\n\t" \
    "s_mov_b32 s76, s74\n\t" \
    "s_lshl_b32 s70, s70, 3\n\t" \
    "s_sub_u32 s73, s56, s70\n\t" \
    "s_lshl_b32 s74, s28, 4\n\t" \
    "s_sub_u32 s69, s73, s75\n\t" \
    "s_sub_u32 s70, s74, s76\n\t" \
    "s_sub_u32 s71, s70, 1600\n\t" \
    "s_cmp_lt_u32 s71, 400000\n\t" \
    "s_cbranch_scc0 qk_K13_%=\n\t" \
    "v_cvt_f32_u32 v160, s69\n\t" \
    "v_cvt_f32_u32 v161, s70\n\t" \
    "v_rcp_f32 v162, v160\n\t" \
    "v_rcp_f32 v163, v161\n\t" \
    "v_mul_f32 v162, v162, v161\n\t" \
    "v_mul_f32 v163, v163, v160\n\t" \
    "v_mul_f32 v162, 0x4f800000, v162\n\t" \
    "v_mul_f32 v163, 0x4f000000, v163\n\t" \
    "v_cvt_u32_f32 v162, v162\n\t" \
    "v_cvt_u32_f32 v163, v163\n\t" \
    "v_readfirstlane_b32 s69, v162\n\t" \
    "v_readfirstlane_b32 s70, v163\n\t" \
    "s_nop 1\n\t" \
    "s_mov_b32 s78, s69\n\t" \
    "s_mov_b32 s77, s70\n\t" \
    "qk_K13_%=:\n\t" \
    "s_mov_b32 s79, s74\n\t" \
    "s_add_u32 s80, s74, 2400\n\t" \
    "s_cmp_eq_u32 s66, 1\n\t" \
    "s_cbranch_scc1 qk_loop15_%=\n\t" \
    "s_cmp_eq_u32 s66, 2\n\t" \
    "s_cbranch_scc1 qk_loop16_%=\n\t" \
    "s_cmp_eq_u32 s66, 3\n\t" \
    "s_cbranch_scc1 qk_loop17_%=\n\t" \
    "qk_loop14_%=:\n\t" \
    "v_mov_b32 v72, 0\n\t" \
    "v_mov_b32 v73, 0\n\t" \
    "v_mov_b32 v74, 0\n\t" \
    "v_mov_b32 v75, 0\n\t" \
    "v_mov_b32 v76, 0\n\t" \
    "v_mov_b32 v77, 0\n\t" \
    "v_mov_b32 v78, 0\n\t" \
    "v_mov_b32 v79, 0\n\t" \
    "v_mov_b32 v80, 0\n\t" \
    "v_mov_b32 v81, 0\n\t" \
    "v_mov_b32 v82, 0\n\t" \
    "v_mov_b32 v83, 0\n\t" \
    "v_mov_b32 v84, 0\n\t" \
    "v_mov_b32 v85, 0\n\t" \
    "v_mov_b32 v86, 0\n\t" \
    "v_mov_b32 v87, 0\n\t" \
    "v_mov_b32 v88, 0\n\t" \
    "v_mov_b32 v89, 0\n\t" \
    "v_mov_b32 v90, 0\n\t" \
    "v_mov_b32 v91, 0\n\t" \
    "v_mov_b32 v92, 0\n\t" \
    "v_mov_b32 v93, 0\n\t" \
    "v_mov_b32 v94, 0\n\t" \
    "v_mov_b32 v95, 0\n\t" \
    "v_mov_b32 v96, 0\n\t" \
    "v_mov_b32 v97, 0\n\t" \
    "v_mov_b32 v98, 0\n\t" \
    "v_mov_b32 v99, 0\n\t" \
    "v_add_u32 v31, 1, v31\n\t" \
    "s_add_u32 s69, s65, 0\n\t" \
    "s_bitcmp1_b32 s69, 0\n\t" \
    "s_cselect_b32 s70, s63, s62\n\t" \
    "s_bfe_u32 s69, s69, 0x20001\n\t" \
    "s_lshl_b32 s69, s69, 19\n\t" \
    "s_add_u32 s69, s69, s70\n\t" \
    "s_add_u32 s46, s36, s69\n\t" \
    "s_addc_u32 s47, s37, 0\n\t" \
    "s_add_u32 s69, s65, 3\n\t" \
    "s_cmp_ge_i32 s83, s69\n\t" \
    "s_cselect_b64 vcc, -1, 0\n\t" \
    "v_mov_b32 v9, 6201216\n\t" \
    "v_cndmask_b32 v9, v24, v9, vcc\n\t" \
    "s_mul_i32 s70, s67, 7\n\t" \
    "s_add_u32 s69, s65, 1\n\t" \
    "s_xor_b32 s69, s69, s70\n\t" \
    "s_and_b32 s69, s69, 63\n\t" \
    "s_lshl_b32 s69, s69, 13\n\t" \
    "s_mul_i32 s70, s67, 524288\n\t" \
    "s_add_u32 s69, s69, s70\n\t" \
    "s_add_u32 s69, s69, 23068672\n\t" \
    "s_add_u32 s38, s36, s69\n\t" \
    "s_addc_u32 s39, s37, 0\n\t" \
    "qk_go21_%=:\n\t" \
    "s_sub_u32 s70, s79, s74\n\t" \
    "s_max_i32 s70, s70, 0\n\t" \
    "s_min_i32 s70, s70, 16000\n\t" \
    "s_lshl_b32 s70, s70, 1\n\t" \
    "s_mul_hi_u32 s70, s70, s77\n\t" \
    "s_add_u32 s70, s70, s73\n\t" \
    "s_sub_u32 s70, s70, 100\n\t" \
    "s_getpc_b64 s[30:31]\n\t" \
    "qk_Wp23_%=:\n\t" \
    "s_add_u32 s30, s30, qk_We24_%=-qk_Wp23_%=\n\t" \
    "s_addc_u32 s31, s31, 0\n\t" \
    "qk_W22_%=:\n\t" \
    "s_memtime s[56:57]\n\t" \
    "s_waitcnt lgkmcnt(0)\n\t" \
    "s_sub_u32 s69, s70, s56\n\t" \
    "s_cmp_gt_i32 s69, 111\n\t" \
    "s_cbranch_scc1 qk_W22_%=\n\t" \
    "s_max_i32 s69, s69, 0\n\t" \
    "s_lshr_b32 s69, s69, 1\n\t" \
    "s_and_b32 s69, s69, 0x3c\n\t" \
    "s_sub_u32 s30, s30, s69\n\t" \
    "s_subb_u32 s31, s31, 0\n\t" \
    "s_setpc_b64 s[30:31]\n\t" \
    ".p2align 6\n\t" \
    "s_nop 0\n\t" \
    "s_nop 0\n\t" \
    "s_nop 0\n\t" \
    "s_nop 1\n\t" \
    "s_nop 1\n\t" \
    "s_nop 1\n\t" \
    "s_nop 1\n\t" \
    "s_nop 1\n\t" \
    "s_nop 1\n\t" \
    "s_nop 1\n\t" \
    "s_nop 1\n\t" \
    "s_nop 1\n\t" \
    "s_nop 1\n\t" \
    "s_nop 1\n\t" \
    "s_nop 1\n\t" \
    "s_nop 1\n\t" \
    "qk_We24_%=:\n\t" \
    "qk_G25_%=:\n\t" \
    "global_load_dwordx4 v[36:39], v1, s[46:47] sc1\n\t" \
    "s_nop 9\n\t" \
    "global_load_dwordx4 v[40:43], v1, s[46:47] offset:1024 sc1\n\t" \
    "s_nop 9\n\t" \
    "global_load_dwordx4 v[44:47], v2, s[46:47] sc1\n\t" \
    "s_waitcnt vmcnt(2)\n\t" \
    "v_smfmac_f32_16x16x64_bf16 v[72:75], v[36:39], a[0:7], v10\n\t" \
    "ds_write_b128 v0, v[36:39] offset:0\n\t" \
    "v_smfmac_f32_16x16x64_bf16 v[76:79], v[36:39], a[72:79], v10\n\t" \
    "v_max3_u32 v160, v36, v37, v38\n\t" \
    "v_smfmac_f32_16x16x64_bf16 v[80:83], v[36:39], a[144:151], v10\n\t" \
    "v_smfmac_f32_16x16x64_bf16 v[84:87], v[36:39], a[216:223], v10\n\t" \
    "s_waitcnt vmcnt(1)\n\t" \
    "ds_write_b128 v0, v[40:43] offset:1024\n\t" \
    "v_smfmac_f32_16x16x64_bf16 v[72:75], v[40:43], a[8:15], v10\n\t" \
    "v_max3_u32 v160, v160, v39, v40\n\t" \
    "v_max3_u32 v160, v160, v41, v42\n\t" \
    "v_smfmac_f32_16x16x64_bf16 v[76:79], v[40:43], a[80:87], v10\n\t" \
    "v_max_u32 v160, v160, v43\n\t" \
    "v_cmp_eq_u32 vcc, -1, v160\n\t" \
    "v_smfmac_f32_16x16x64_bf16 v[80:83], v[40:43], a[152:159], v10\n\t" \
    "v_smfmac_f32_16x16x64_bf16 v[84:87], v[40:43], a[224:231], v10\n\t" \
    "s_cbranch_vccnz qk_Gbad26_%=\n\t" \
    "qk_Gc28_%=:\n\t" \
    "s_waitcnt lgkmcnt(0)\n\t" \
    "s_barrier\n\t" \
    "ds_read_b128 v[48:51], v0 offset:2048\n\t" \
    "ds_read_b128 v[52:55], v0 offset:3072\n\t" \
    "s_waitcnt vmcnt(0)\n\t" \
    "v_smfmac_f32_16x16x64_bf16 v[72:75], v[44:47], a[64:71], v10\n\t" \
    "ds_read_b128 v[56:59], v0 offset:4096\n\t" \
    "v_max3_u32 v160, v44, v45, v46\n\t" \
    "v_smfmac_f32_16x16x64_bf16 v[76:79], v[44:47], a[136:143], v10\n\t" \
    "ds_read_b128 v[60:63], v0 offset:5120\n\t" \
    "v_max_u32 v160, v160, v47\n\t" \
    "v_smfmac_f32_16x16x64_bf16 v[80:83], v[44:47], a[208:215], v10\n\t" \
    "ds_read_b128 v[64:67], v0 offset:6144\n\t" \
    "v_cmp_eq_u32 vcc, -1, v160\n\t" \
    "v_smfmac_f32_16x16x64_bf16 v[84:87], v[44:47], v[248:255], v10\n\t" \
    "ds_read_b128 v[68:71], v0 offset:7168\n\t" \
    "s_cbranch_vccnz qk_Gbad227_%=\n\t" \
    "qk_Gc229_%=:\n\t" \
    "s_waitcnt lgkmcnt(5)\n\t" \
    "v_smfmac_f32_16x16x64_bf16 v[72:75], v[48:51], a[16:23], v10\n\t" \
    "s_waitcnt lgkmcnt(4)\n\t" \
    "v_smfmac_f32_16x16x64_bf16 v[72:75], v[52:55], a[24:31], v10\n\t" \
    "s_waitcnt lgkmcnt(3)\n\t" \
    "v_smfmac_f32_16x16x64_bf16 v[72:75], v[56:59], a[32:39], v10\n\t" \
    "global_load_dword v25, v9, s[36:37] sc1\n\t" \
    "s_waitcnt lgkmcnt(2)\n\t" \
    "v_smfmac_f32_16x16x64_bf16 v[72:75], v[60:63], a[40:47], v10\n\t" \
    "s_waitcnt lgkmcnt(1)\n\t" \
    "v_smfmac_f32_16x16x64_bf16 v[72:75], v[64:67], a[48:55], v10\n\t" \
    "s_waitcnt lgkmcnt(0)\n\t" \
    "v_smfmac_f32_16x16x64_bf16 v[72:75], v[68:71], a[56:63], v10\n\t" \
    "v_smfmac_f32_16x16x64_bf16 v[76:79], v[48:51], a[88:95], v10\n\t" \
    "s_nop 1\n\t" \
    "s_nop 0\n\t" \
    "v_smfmac_f32_16x16x64_bf16 v[76:79], v[52:55], a[96:103], v10\n\t" \
    "s_nop 1\n\t" \
    "s_nop 0\n\t" \
    "v_smfmac_f32_16x16x64_bf16 v[76:79], v[56:59], a[104:111], v10\n\t" \
    "v_add_f32 v72, v72, v74\n\t" \
    "v_smfmac_f32_16x16x64_bf16 v[76:79], v[60:63], a[112:119], v10\n\t" \
    "v_add_f32 v73, v73, v75\n\t" \
    "v_smfmac_f32_16x16x64_bf16 v[76:79], v[64:67], a[120:127], v10\n\t" \
    "v_cvt_pk_bf16_f32 v156, v72, v73\n\t" \
    "v_smfmac_f32_16x16x64_bf16 v[76:79], v[68:71], a[128:135], v10\n\t" \
    "ds_write_b32 v12, v156 offset:0\n\t" \
    "v_smfmac_f32_16x16x64_bf16 v[80:83], v[48:51], a[160:167], v10\n\t" \
    "s_nop 1\n\t" \
    "s_nop 0\n\t" \
    "v_smfmac_f32_16x16x64_bf16 v[80:83], v[52:55], a[168:175], v10\n\t" \
    "s_nop 1\n\t" \
    "s_nop 0\n\t" \
    "v_smfmac_f32_16x16x64_bf16 v[80:83], v[56:59], a[176:183], v10\n\t" \
    "v_add_f32 v76, v76, v78\n\t" \
    "v_smfmac_f32_16x16x64_bf16 v[80:83], v[60:63], a[184:191], v10\n\t" \
    "v_add_f32 v77, v77, v79\n\t" \
    "v_smfmac_f32_16x16x64_bf16 v[80:83], v[64:67], a[192:199], v10\n\t" \
    "v_cvt_pk_bf16_f32 v157, v76, v77\n\t" \
    "v_smfmac_f32_16x16x64_bf16 v[80:83], v[68:71], a[200:207], v10\n\t" \
    "ds_write_b32 v12, v157 offset:64\n\t" \
    "v_smfmac_f32_16x16x64_bf16 v[84:87], v[48:51], a[232:239], v10\n\t" \
    "s_nop 1\n\t" \
    "s_nop 0\n\t" \
    "v_smfmac_f32_16x16x64_bf16 v[84:87], v[52:55], a[240:247], v10\n\t" \
    "s_nop 1\n\t" \
    "s_nop 0\n\t" \
    "v_smfmac_f32_16x16x64_bf16 v[84:87], v[56:59], a[248:255], v10\n\t" \
    "v_add_f32 v80, v80, v82\n\t" \
    "v_smfmac_f32_16x16x64_bf16 v[84:87], v[60:63], v[224:231], v10\n\t" \
    "v_add_f32 v81, v81, v83\n\t" \
    "v_smfmac_f32_16x16x64_bf16 v[84:87], v[64:67], v[232:239], v10\n\t" \
    "v_cvt_pk_bf16_f32 v158, v80, v81\n\t" \
    "v_smfmac_f32_16x16x64_bf16 v[84:87], v[68:71], v[240:247], v10\n\t" \
    "ds_write_b32 v16, v158 offset:0\n\t" \
    "s_nop 6\n\t" \
    "v_pk_add_f32 v[84:85], v[84:85], v[86:87]\n\t" \
    "v_cvt_pk_bf16_f32 v159, v84, v85\n\t" \
    "ds_write_b32 v16, v159 offset:256\n\t" \
    "s_waitcnt lgkmcnt(0)\n\t" \
    "s_barrier\n\t" \
    "ds_read_b64_tr_b16 v[132:133], v13 offset:0\n\t" \
    "ds_read_b64_tr_b16 v[134:135], v13 offset:128\n\t" \
    "ds_read_b64_tr_b16 v[136:137], v13 offset:1024\n\t" \
    "ds_read_b64_tr_b16 v[138:139], v13 offset:1152\n\t" \
    "ds_read_b64_tr_b16 v[140:141], v17 offset:0\n\t" \
    "ds_read_b64_tr_b16 v[142:143], v17 offset:256\n\t" \
    "ds_read_b64_tr_b16 v[144:145], v17 offset:512\n\t" \
    "ds_read_b64_tr_b16 v[146:147], v17 offset:768\n\t" \
    "ds_read_b64_tr_b16 v[148:149], v17 offset:1024\n\t" \
    "ds_read_b64_tr_b16 v[150:151], v17 offset:1280\n\t" \
    "ds_read_b64_tr_b16 v[152:153], v17 offset:1536\n\t" \
    "ds_read_b64_tr_b16 v[154:155], v17 offset:1792\n\t" \
    "s_waitcnt lgkmcnt(10)\n\t" \
    "v_smfmac_f32_16x16x64_bf16 v[88:91], v[132:135], v[100:107], v10\n\t" \
    "v_smfmac_f32_16x16x64_bf16 v[92:95], v[132:135], v[116:123], v10\n\t" \
    "s_waitcnt lgkmcnt(8)\n\t" \
    "v_smfmac_f32_16x16x64_bf16 v[88:91], v[136:139], v[108:115], v10\n\t" \
    "v_smfmac_f32_16x16x64_bf16 v[92:95], v[136:139], v[124:131], v10\n\t" \
    "s_waitcnt lgkmcnt(4)\n\t" \
    "v_smfmac_f32_16x16x64_bf16 v[96:99], v[132:135], v[140:147], v10\n\t" \
    "s_waitcnt lgkmcnt(0)\n\t" \
    "s_nop 1\n\t" \
    "v_smfmac_f32_16x16x64_bf16 v[96:99], v[136:139], v[148:155], v10\n\t" \
    "s_nop 0\n\t" \
    "v_add_f32 v168, v88, v90\n\t" \
    "v_add_f32 v169, v92, v94\n\t" \
    "v_max_f32 v178, v168, v169\n\t" \
    "v_mul_f32 v168, v168, v29\n\t" \
    "v_mul_f32 v169, v169, v29\n\t" \
    "v_max_f32_dpp v178, v178, v178 row_mirror row_mask:0xf bank_mask:0xf\n\t" \
    "s_nop 0\n\t" \
    "v_add_f32 v172, v96, v98\n\t" \
    "v_max_f32 v173, v178, v172\n\t" \
    "v_mul_f32 v172, v172, v29\n\t" \
    "s_nop 0\n\t" \
    "v_max_f32_dpp v173, v173, v173 quad_perm:[1,0,3,2] row_mask:0xf bank_mask:0xf\n\t" \
    "s_nop 1\n\t" \
    "v_max_f32_dpp v173, v173, v173 quad_perm:[2,3,0,1] row_mask:0xf bank_mask:0xf\n\t" \
    "s_nop 1\n\t" \
    "v_max_f32_dpp v173, v173, v173 row_half_mirror row_mask:0xf bank_mask:0xf\n\t" \
    "v_fma_f32 v172, -v173, v29, v172\n\t" \
    "v_fma_f32 v168, -v173, v29, v168\n\t" \
    "v_fma_f32 v169, -v173, v29, v169\n\t" \
    "v_exp_f32 v172, v172\n\t" \
    "v_exp_f32 v168, v168\n\t" \
    "v_exp_f32 v169, v169\n\t" \
    "v_fma_f32 v173, v172, 0.5, v168\n\t" \
    "v_add_f32 v173, v173, v169\n\t" \
    "v_mov_b32_dpp v174, v172 quad_perm:[0,0,2,2] row_mask:0xf bank_mask:0xf\n\t" \
    "v_mov_b32_dpp v175, v172 quad_perm:[1,1,3,3] row_mask:0xf bank_mask:0xf\n\t" \
    "v_add_f32_dpp v173, v173, v173 quad_perm:[1,0,3,2] row_mask:0xf bank_mask:0xf\n\t" \
    "v_mov_b32_dpp v176, v168 quad_perm:[1,0,3,2] row_mask:0xf bank_mask:0xf\n\t" \
    "v_mov_b32_dpp v177, v169 quad_perm:[1,0,3,2] row_mask:0xf bank_mask:0xf\n\t" \
    "v_add_f32_dpp v173, v173, v173 quad_perm:[2,3,0,1] row_mask:0xf bank_mask:0xf\n\t" \
    "v_cndmask_b32 v168, v168, v177, s[48:49]\n\t" \
    "v_cndmask_b32 v169, v176, v169, s[48:49]\n\t" \
    "v_add_f32_dpp v173, v173, v173 row_half_mirror row_mask:0xf bank_mask:0xf\n\t" \
    "s_nop 1\n\t" \
    "v_add_f32_dpp v173, v173, v173 row_mirror row_mask:0xf bank_mask:0xf\n\t" \
    "v_rcp_f32 v173, v173\n\t" \
    "s_nop 0\n\t" \
    "v_pk_mul_f32 v[168:169], v[168:169], v[172:173] op_sel:[0,1] op_sel_hi:[1,1]\n\t" \
    "v_pk_mul_f32 v[174:175], v[174:175], v[172:173] op_sel:[0,1] op_sel_hi:[1,1]\n\t" \
    "v_cvt_pk_bf16_f32 v176, v168, v169\n\t" \
    "global_store_dword v18, v176, s[46:47] sc1\n\t" \
    "global_store_dword v3, v176, s[46:47] sc1\n\t" \
    "v_cvt_pk_bf16_f32 v177, v174, v175\n\t" \
    "global_store_dword v19, v177, s[46:47] sc1\n\t" \
    "global_store_dword v4, v177, s[46:47] sc1\n\t" \
    "s_cmp_eq_u32 s65, 63\n\t" \
    "s_cbranch_scc1 qk_last18_%=\n\t" \
    "s_mov_b32 s71, 3\n\t" \
    "qk_E31_%=:\n\t" \
    "s_memtime s[54:55]\n\t" \
    "s_nop 0\n\t" \
    "s_memrealtime s[4:5]\n\t" \
    "s_nop 0\n\t" \
    "s_memrealtime s[6:7]\n\t" \
    "s_nop 0\n\t" \
    "s_memrealtime s[8:9]\n\t" \
    "s_nop 0\n\t" \
    "s_memrealtime s[10:11]\n\t" \
    "s_nop 0\n\t" \
    "s_memrealtime s[12:13]\n\t" \
    "s_nop 0\n\t" \
    "s_memrealtime s[14:15]\n\t" \
    "s_nop 0\n\t" \
    "s_memrealtime s[16:17]\n\t" \
    "s_nop 0\n\t" \
    "s_memrealtime s[18:19]\n\t" \
    "s_nop 0\n\t" \
    "s_memrealtime s[20:21]\n\t" \
    "s_nop 0\n\t" \
    "s_memrealtime s[22:23]\n\t" \
    "s_nop 0\n\t" \
    "s_memrealtime s[24:25]\n\t" \
    "s_nop 0\n\t" \
    "s_memrealtime s[26:27]\n\t" \
    "s_nop 0\n\t" \
    "s_memrealtime s[28:29]\n\t" \
    "s_nop 0\n\t" \
    "s_memtime s[56:57]\n\t" \
    "s_mov_b32 s70, 1\n\t" \
    "s_sub_u32 s71, s71, 1\n\t" \
    "s_cselect_b32 s69, 0, -1\n\t" \
    "s_waitcnt lgkmcnt(0)\n\t" \
    "s_sub_u32 s54, s56, s54\n\t" \
    "s_and_b32 s54, s54, s69\n\t" \
    "s_cmp_gt_u32 s54, 124\n\t" \
    "s_cbranch_scc1 qk_E31_%=\n\t" \
    "s_cmp_eq_u32 s4, s28\n\t" \
    "s_addc_u32 s70, s70, 0\n\t" \
    "s_cmp_eq_u32 s6, s28\n\t" \
    "s_addc_u32 s70, s70, 0\n\t" \
    "s_cmp_eq_u32 s8, s28\n\t" \
    "s_addc_u32 s70, s70, 0\n\t" \
    "s_cmp_eq_u32 s10, s28\n\t" \
    "s_addc_u32 s70, s70, 0\n\t" \
    "s_cmp_eq_u32 s12, s28\n\t" \
    "s_addc_u32 s70, s70, 0\n\t" \
    "s_cmp_eq_u32 s14, s28\n\t" \
    "s_addc_u32 s70, s70, 0\n\t" \
    "s_cmp_eq_u32 s16, s28\n\t" \
    "s_addc_u32 s70, s70, 0\n\t" \
    "s_cmp_eq_u32 s18, s28\n\t" \
    "s_addc_u32 s70, s70, 0\n\t" \
    "s_cmp_eq_u32 s20, s28\n\t" \
    "s_addc_u32 s70, s70, 0\n\t" \
    "s_cmp_eq_u32 s22, s28\n\t" \
    "s_addc_u32 s70, s70, 0\n\t" \
    "s_cmp_eq_u32 s24, s28\n\t" \
    "s_addc_u32 s70, s70, 0\n\t" \
    "s_cmp_eq_u32 s26, s28\n\t" \
    "s_addc_u32 s70, s70, 0\n\t" \
    "s_mov_b32 s75, s73\n\t" \
    "s_mov_b32 s76, s74\n\t" \
    "s_lshl_b32 s70, s70, 3\n\t" \
    "s_sub_u32 s73, s56, s70\n\t" \
    "s_lshl_b32 s74, s28, 4\n\t" \
    "s_sub_u32 s69, s73, s75\n\t" \
    "s_sub_u32 s70, s74, s76\n\t" \
    "s_sub_u32 s71, s70, 1600\n\t" \
    "s_cmp_lt_u32 s71, 400000\n\t" \
    "s_cbranch_scc0 qk_K33_%=\n\t" \
    "v_cvt_f32_u32 v160, s69\n\t" \
    "v_cvt_f32_u32 v161, s70\n\t" \
    "v_rcp_f32 v162, v160\n\t" \
    "v_rcp_f32 v163, v161\n\t" \
    "v_mul_f32 v162, v162, v161\n\t" \
    "v_mul_f32 v163, v163, v160\n\t" \
    "v_mul_f32 v162, 0x4f800000, v162\n\t" \
    "v_mul_f32 v163, 0x4f000000, v163\n\t" \
    "v_cvt_u32_f32 v162, v162\n\t" \
    "v_cvt_u32_f32 v163, v163\n\t" \
    "v_readfirstlane_b32 s69, v162\n\t" \
    "v_readfirstlane_b32 s70, v163\n\t" \
    "s_nop 1\n\t" \
    "s_sub_u32 s69, s69, s78\n\t" \
    "s_ashr_i32 s69, s69, 2\n\t" \
    "s_add_u32 s78, s78, s69\n\t" \
    "s_sub_u32 s70, s70, s77\n\t" \
    "s_ashr_i32 s70, s70, 2\n\t" \
    "s_add_u32 s77, s77, s70\n\t" \
    "qk_K33_%=:\n\t" \
    "s_waitcnt vmcnt(4)\n\t" \
    "v_readfirstlane_b32 s69, v25\n\t" \
    "s_add_u32 s71, s65, 1\n\t" \
    "s_max_i32 s83, s83, s69\n\t" \
    "qk_WF34_%=:\n\t" \
    "s_cmp_ge_i32 s83, s71\n\t" \
    "s_cbranch_scc1 qk_WFok35_%=\n\t" \
    "global_load_dword v160, v24, s[36:37] sc1\n\t" \
    "s_waitcnt vmcnt(0)\n\t" \
    "v_readfirstlane_b32 s83, v160\n\t" \
    "s_nop 1\n\t" \
    "s_cmp_ge_i32 s83, s71\n\t" \
    "s_cbranch_scc1 qk_WFok35_%=\n\t" \
    "s_cmp_eq_u32 s68, 0\n\t" \
    "s_cbranch_scc1 qk_F36_%=\n\t" \
    "s_sub_u32 s68, s68, 1\n\t" \
    "s_sleep 2\n\t" \
    "s_branch qk_WF34_%=\n\t" \
    "qk_F36_%=:\n\t" \
    "v_mov_b32 v160, 6095104\n\t" \
    "v_mov_b32 v161, 9\n\t" \
    "global_store_dword v160, v161, s[36:37] sc1\n\t" \
    "s_waitcnt vmcnt(0)\n\t" \
    "s_branch qk_WFok35_%=\n\t" \
    "qk_WFok35_%=:\n\t" \
    "v_mov_b32 v72, 0\n\t" \
    "v_mov_b32 v73, 0\n\t" \
    "v_mov_b32 v74, 0\n\t" \
    "v_mov_b32 v75, 0\n\t" \
    "v_mov_b32 v76, 0\n\t" \
    "v_mov_b32 v77, 0\n\t" \
    "v_mov_b32 v78, 0\n\t" \
    "v_mov_b32 v79, 0\n\t" \
    "v_mov_b32 v80, 0\n\t" \
    "v_mov_b32 v81, 0\n\t" \
    "v_mov_b32 v82, 0\n\t" \
    "v_mov_b32 v83, 0\n\t" \
    "v_mov_b32 v84, 0\n\t" \
    "v_mov_b32 v85, 0\n\t" \
    "v_mov_b32 v86, 0\n\t" \
    "v_mov_b32 v87, 0\n\t" \
    "v_mov_b32 v88, 0\n\t" \
    "v_mov_b32 v89, 0\n\t" \
    "v_mov_b32 v90, 0\n\t" \
    "v_mov_b32 v91, 0\n\t" \
    "v_mov_b32 v92, 0\n\t" \
    "v_mov_b32 v93, 0\n\t" \
    "v_mov_b32 v94, 0\n\t" \
    "v_mov_b32 v95, 0\n\t" \
    "v_mov_b32 v96, 0\n\t" \
    "v_mov_b32 v97, 0\n\t" \
    "v_mov_b32 v98, 0\n\t" \
    "v_mov_b32 v99, 0\n\t" \
    "s_add_u32 s79, s74, 416\n\t" \
    "s_sub_u32 s70, s79, s74\n\t" \
    "s_max_i32 s70, s70, 0\n\t" \
    "s_min_i32 s70, s70, 16000\n\t" \
    "s_lshl_b32 s70, s70, 1\n\t" \
    "s_mul_hi_u32 s70, s70, s77\n\t" \
    "s_add_u32 s70, s70, s73\n\t" \
    "qk_W37_%=:\n\t" \
    "s_memtime s[56:57]\n\t" \
    "s_waitcnt lgkmcnt(0)\n\t" \
    "s_sub_u32 s69, s70, s56\n\t" \
    "s_cmp_gt_i32 s69, 24\n\t" \
    "s_cbranch_scc1 qk_W37_%=\n\t" \
    "global_load_dwordx4 v[100:103], v23, s[38:39] offset:0 sc1\n\t" \
    "global_load_dwordx4 v[104:107], v23, s[38:39] offset:2048 sc1\n\t" \
    "global_load_dwordx4 v[108:111], v15, s[38:39] offset:0 sc1\n\t" \
    "global_load_dwordx4 v[112:115], v15, s[38:39] offset:2048 sc1\n\t" \
    "global_load_dwordx4 v[116:119], v23, s[38:39] offset:1024 sc1\n\t" \
    "global_load_dwordx4 v[120:123], v23, s[38:39] offset:3072 sc1\n\t" \
    "global_load_dwordx4 v[124:127], v15, s[38:39] offset:1024 sc1\n\t" \
    "global_load_dwordx4 v[128:131], v15, s[38:39] offset:3072 sc1\n\t" \
    "s_add_u32 s65, s65, 1\n\t" \
    "v_add_u32 v31, 1, v31\n\t" \
    "s_add_u32 s69, s65, 0\n\t" \
    "s_bitcmp1_b32 s69, 0\n\t" \
    "s_cselect_b32 s70, s63, s62\n\t" \
    "s_bfe_u32 s69, s69, 0x20001\n\t" \
    "s_lshl_b32 s69, s69, 19\n\t" \
    "s_add_u32 s69, s69, s70\n\t" \
    "s_add_u32 s46, s36, s69\n\t" \
    "s_addc_u32 s47, s37, 0\n\t" \
    "s_add_u32 s69, s65, 3\n\t" \
    "s_cmp_ge_i32 s83, s69\n\t" \
    "s_cselect_b64 vcc, -1, 0\n\t" \
    "v_mov_b32 v9, 6201216\n\t" \
    "v_cndmask_b32 v9, v24, v9, vcc\n\t" \
    "s_mul_i32 s70, s67, 7\n\t" \
    "s_add_u32 s69, s65, 1\n\t" \
    "s_xor_b32 s69, s69, s70\n\t" \
    "s_and_b32 s69, s69, 63\n\t" \
    "s_lshl_b32 s69, s69, 13\n\t" \
    "s_mul_i32 s70, s67, 524288\n\t" \
    "s_add_u32 s69, s69, s70\n\t" \
    "s_add_u32 s69, s69, 23068672\n\t" \
    "s_add_u32 s38, s36, s69\n\t" \
    "s_addc_u32 s39, s37, 0\n\t" \
    "s_waitcnt lgkmcnt(0)\n\t" \
    "s_barrier\n\t" \
    "ds_read_b32 v160, v11 offset:20480\n\t" \
    "s_waitcnt lgkmcnt(0)\n\t" \
    "v_readfirstlane_b32 s81, v160\n\t" \
    "s_nop 1\n\t" \
    "s_mul_hi_u32 s69, s85, s78\n\t" \
    "s_add_u32 s79, s81, s69\n\t" \
    "s_add_u32 s79, s79, s87\n\t" \
    "s_cmp_ge_u32 s65, 37\n\t" \
    "s_cselect_b32 s69, 28, 0\n\t" \
    "s_sub_u32 s79, s79, s69\n\t" \
    "s_add_u32 s80, s79, s88\n\t" \
    "s_branch qk_go21_%=\n\t" \
    "qk_Gbad26_%=:\n\t" \
    "s_waitcnt vmcnt(0) lgkmcnt(0)\n\t" \
    "s_nop 7\n\t" \
    "v_mov_b32 v72, 0\n\t" \
    "v_mov_b32 v73, 0\n\t" \
    "v_mov_b32 v74, 0\n\t" \
    "v_mov_b32 v75, 0\n\t" \
    "v_mov_b32 v76, 0\n\t" \
    "v_mov_b32 v77, 0\n\t" \
    "v_mov_b32 v78, 0\n\t" \
    "v_mov_b32 v79, 0\n\t" \
    "v_mov_b32 v80, 0\n\t" \
    "v_mov_b32 v81, 0\n\t" \
    "v_mov_b32 v82, 0\n\t" \
    "v_mov_b32 v83, 0\n\t" \
    "v_mov_b32 v84, 0\n\t" \
    "v_mov_b32 v85, 0\n\t" \
    "v_mov_b32 v86, 0\n\t" \
    "v_mov_b32 v87, 0\n\t" \
    "s_cmp_eq_u32 s68, 0\n\t" \
    "s_cbranch_scc1 qk_F38_%=\n\t" \
    "s_sub_u32 s68, s68, 1\n\t" \
    "s_branch qk_G25_%=\n\t" \
    "qk_F38_%=:\n\t" \
    "v_mov_b32 v160, 6095104\n\t" \
    "v_mov_b32 v161, 1\n\t" \
    "global_store_dword v160, v161, s[36:37] sc1\n\t" \
    "s_waitcnt vmcnt(0)\n\t" \
    "s_branch qk_Gc28_%=\n\t" \
    "qk_Gbad227_%=:\n\t" \
    "global_load_dwordx4 v[44:47], v2, s[46:47] sc1\n\t" \
    "s_waitcnt vmcnt(0)\n\t" \
    "v_max3_u32 v160, v44, v45, v46\n\t" \
    "v_max_u32 v160, v160, v47\n\t" \
    "v_cmp_eq_u32 vcc, -1, v160\n\t" \
    "s_cbranch_vccz qk_Gre230_%=\n\t" \
    "s_cmp_eq_u32 s68, 0\n\t" \
    "s_cbranch_scc1 qk_F39_%=\n\t" \
    "s_sub_u32 s68, s68, 1\n\t" \
    "s_sleep 1\n\t" \
    "s_branch qk_Gbad227_%=\n\t" \
    "qk_F39_%=:\n\t" \
    "v_mov_b32 v160, 6095104\n\t" \
    "v_mov_b32 v161, 2\n\t" \
    "global_store_dword v160, v161, s[36:37] sc1\n\t" \
    "s_waitcnt vmcnt(0)\n\t" \
    "s_branch qk_Gre230_%=\n\t" \
    "qk_Gre230_%=:\n\t" \
    "v_mov_b32 v72, 0\n\t" \
    "v_mov_b32 v73, 0\n\t" \
    "v_mov_b32 v74, 0\n\t" \
    "v_mov_b32 v75, 0\n\t" \
    "v_mov_b32 v76, 0\n\t" \
    "v_mov_b32 v77, 0\n\t" \
    "v_mov_b32 v78, 0\n\t" \
    "v_mov_b32 v79, 0\n\t" \
    "v_mov_b32 v80, 0\n\t" \
    "v_mov_b32 v81, 0\n\t" \
    "v_mov_b32 v82, 0\n\t" \
    "v_mov_b32 v83, 0\n\t" \
    "v_mov_b32 v84, 0\n\t" \
    "v_mov_b32 v85, 0\n\t" \
    "v_mov_b32 v86, 0\n\t" \
    "v_mov_b32 v87, 0\n\t" \
    "v_smfmac_f32_16x16x64_bf16 v[72:75], v[36:39], a[0:7], v10\n\t" \
    "v_smfmac_f32_16x16x64_bf16 v[76:79], v[36:39], a[72:79], v10\n\t" \
    "v_smfmac_f32_16x16x64_bf16 v[80:83], v[36:39], a[144:151], v10\n\t" \
    "v_smfmac_f32_16x16x64_bf16 v[84:87], v[36:39], a[216:223], v10\n\t" \
    "v_smfmac_f32_16x16x64_bf16 v[72:75], v[40:43], a[8:15], v10\n\t" \
    "v_smfmac_f32_16x16x64_bf16 v[76:79], v[40:43], a[80:87], v10\n\t" \
    "v_smfmac_f32_16x16x64_bf16 v[80:83], v[40:43], a[152:159], v10\n\t" \
    "v_smfmac_f32_16x16x64_bf16 v[84:87], v[40:43], a[224:231], v10\n\t" \
    "v_smfmac_f32_16x16x64_bf16 v[72:75], v[44:47], a[64:71], v10\n\t" \
    "v_smfmac_f32_16x16x64_bf16 v[76:79], v[44:47], a[136:143], v10\n\t" \
    "v_smfmac_f32_16x16x64_bf16 v[80:83], v[44:47], a[208:215], v10\n\t" \
    "v_smfmac_f32_16x16x64_bf16 v[84:87], v[44:47], v[248:255], v10\n\t" \
    "s_branch qk_Gc229_%=\n\t" \
    "qk_last18_%=:\n\t" \
    "s_endpgm\n\t" \
    "qk_loop15_%=:\n\t" \
    "v_mov_b32 v72, 0\n\t" \
    "v_mov_b32 v73, 0\n\t" \
    "v_mov_b32 v74, 0\n\t" \
    "v_mov_b32 v75, 0\n\t" \
    "v_mov_b32 v76, 0\n\t" \
    "v_mov_b32 v77, 0\n\t" \
    "v_mov_b32 v78, 0\n\t" \
    "v_mov_b32 v79, 0\n\t" \
    "v_mov_b32 v80, 0\n\t" \
    "v_mov_b32 v81, 0\n\t" \
    "v_mov_b32 v82, 0\n\t" \
    "v_mov_b32 v83, 0\n\t" \
    "v_mov_b32 v84, 0\n\t" \
    "v_mov_b32 v85, 0\n\t" \
    "v_mov_b32 v86, 0\n\t" \
    "v_mov_b32 v87, 0\n\t" \
    "v_mov_b32 v88, 0\n\t" \
    "v_mov_b32 v89, 0\n\t" \
    "v_mov_b32 v90, 0\n\t" \
    "v_mov_b32 v91, 0\n\t" \
    "v_mov_b32 v92, 0\n\t" \
    "v_mov_b32 v93, 0\n\t" \
    "v_mov_b32 v94, 0\n\t" \
    "v_mov_b32 v95, 0\n\t" \
    "v_mov_b32 v96, 0\n\t" \
    "v_mov_b32 v97, 0\n\t" \
    "v_mov_b32 v98, 0\n\t" \
    "v_mov_b32 v99, 0\n\t" \
    "v_add_u32 v31, 1, v31\n\t" \
    "s_add_u32 s69, s65, 0\n\t" \
    "s_bitcmp1_b32 s69, 0\n\t" \
    "s_cselect_b32 s70, s63, s62\n\t" \
    "s_bfe_u32 s69, s69, 0x20001\n\t" \
    "s_lshl_b32 s69, s69, 19\n\t" \
    "s_add_u32 s69, s69, s70\n\t" \
    "s_add_u32 s46, s36, s69\n\t" \
    "s_addc_u32 s47, s37, 0\n\t" \
    "s_add_u32 s69, s65, 3\n\t" \
    "s_cmp_ge_i32 s83, s69\n\t" \
    "s_cselect_b64 vcc, -1, 0\n\t" \
    "v_mov_b32 v9, 6201216\n\t" \
    "v_cndmask_b32 v9, v24, v9, vcc\n\t" \
    "s_mul_i32 s70, s67, 7\n\t" \
    "s_add_u32 s69, s65, 1\n\t" \
    "s_xor_b32 s69, s69, s70\n\t" \
    "s_and_b32 s69, s69, 63\n\t" \
    "s_lshl_b32 s69, s69, 13\n\t" \
    "s_mul_i32 s70, s67, 524288\n\t" \
    "s_add_u32 s69, s69, s70\n\t" \
    "s_add_u32 s69, s69, 23068672\n\t" \
    "s_add_u32 s38, s36, s69\n\t" \
    "s_addc_u32 s39, s37, 0\n\t" \
    "qk_go43_%=:\n\t" \
    "s_sub_u32 s70, s79, s74\n\t" \
    "s_max_i32 s70, s70, 0\n\t" \
    "s_min_i32 s70, s70, 16000\n\t" \
    "s_lshl_b32 s70, s70, 1\n\t" \
    "s_mul_hi_u32 s70, s70, s77\n\t" \
    "s_add_u32 s70, s70, s73\n\t" \
    "s_sub_u32 s70, s70, 100\n\t" \
    "s_getpc_b64 s[30:31]\n\t" \
    "qk_Wp45_%=:\n\t" \
    "s_add_u32 s30, s30, qk_We46_%=-qk_Wp45_%=\n\t" \
    "s_addc_u32 s31, s31, 0\n\t" \
    "qk_W44_%=:\n\t" \
    "s_memtime s[56:57]\n\t" \
    "s_waitcnt lgkmcnt(0)\n\t" \
    "s_sub_u32 s69, s70, s56\n\t" \
    "s_cmp_gt_i32 s69, 111\n\t" \
    "s_cbranch_scc1 qk_W44_%=\n\t" \
    "s_max_i32 s69, s69, 0\n\t" \
    "s_lshr_b32 s69, s69, 1\n\t" \
    "s_and_b32 s69, s69, 0x3c\n\t" \
    "s_sub_u32 s30, s30, s69\n\t" \
    "s_subb_u32 s31, s31, 0\n\t" \
    "s_setpc_b64 s[30:31]\n\t" \
    ".p2align 6\n\t" \
    "s_nop 0\n\t" \
    "s_nop 0\n\t" \
    "s_nop 0\n\t" \
    "s_nop 1\n\t" \
    "s_nop 1\n\t" \
    "s_nop 1\n\t" \
    "s_nop 1\n\t" \
    "s_nop 1\n\t" \
    "s_nop 1\n\t" \
    "s_nop 1\n\t" \
    "s_nop 1\n\t" \
    "s_nop 1\n\t" \
    "s_nop 1\n\t" \
    "s_nop 1\n\t" \
    "s_nop 1\n\t" \
    "s_nop 1\n\t" \
    "qk_We46_%=:\n\t" \
    "qk_G47_%=:\n\t" \
    "global_load_dwordx4 v[36:39], v1, s[46:47] sc1\n\t" \
    "s_nop 9\n\t" \
    "global_load_dwordx4 v[40:43], v1, s[46:47] offset:1024 sc1\n\t" \
    "s_nop 9\n\t" \
    "global_load_dwordx4 v[44:47], v2, s[46:47] sc1\n\t" \
    "s_waitcnt vmcnt(2)\n\t" \
    "v_smfmac_f32_16x16x64_bf16 v[72:75], v[36:39], a[16:23], v10\n\t" \
    "ds_write_b128 v0, v[36:39] offset:2048\n\t" \
    "v_smfmac_f32_16x16x64_bf16 v[76:79], v[36:39], a[88:95], v10\n\t" \
    "v_max3_u32 v160, v36, v37, v38\n\t" \
    "v_smfmac_f32_16x16x64_bf16 v[80:83], v[36:39], a[160:167], v10\n\t" \
    "v_smfmac_f32_16x16x64_bf16 v[84:87], v[36:39], a[232:239], v10\n\t" \
    "s_waitcnt vmcnt(1)\n\t" \
    "ds_write_b128 v0, v[40:43] offset:3072\n\t" \
    "v_smfmac_f32_16x16x64_bf16 v[72:75], v[40:43], a[24:31], v10\n\t" \
    "v_max3_u32 v160, v160, v39, v40\n\t" \
    "v_max3_u32 v160, v160, v41, v42\n\t" \
    "v_smfmac_f32_16x16x64_bf16 v[76:79], v[40:43], a[96:103], v10\n\t" \
    "v_max_u32 v160, v160, v43\n\t" \
    "v_cmp_eq_u32 vcc, -1, v160\n\t" \
    "v_smfmac_f32_16x16x64_bf16 v[80:83], v[40:43], a[168:175], v10\n\t" \
    "v_smfmac_f32_16x16x64_bf16 v[84:87], v[40:43], a[240:247], v10\n\t" \
    "s_cbranch_vccnz qk_Gbad48_%=\n\t" \
    "qk_Gc50_%=:\n\t" \
    "s_waitcnt lgkmcnt(0)\n\t" \
    "s_barrier\n\t" \
    "ds_read_b128 v[48:51], v0 offset:4096\n\t" \
    "ds_read_b128 v[52:55], v0 offset:5120\n\t" \
    "s_waitcnt vmcnt(0)\n\t" \
    "v_smfmac_f32_16x16x64_bf16 v[72:75], v[44:47], a[64:71], v10\n\t" \
    "ds_read_b128 v[56:59], v0 offset:6144\n\t" \
    "v_max3_u32 v160, v44, v45, v46\n\t" \
    "v_smfmac_f32_16x16x64_bf16 v[76:79], v[44:47], a[136:143], v10\n\t" \
    "ds_read_b128 v[60:63], v0 offset:7168\n\t" \
    "v_max_u32 v160, v160, v47\n\t" \
    "v_smfmac_f32_16x16x64_bf16 v[80:83], v[44:47], a[208:215], v10\n\t" \
    "ds_read_b128 v[64:67], v0 offset:0\n\t" \
    "v_cmp_eq_u32 vcc, -1, v160\n\t" \
    "v_smfmac_f32_16x16x64_bf16 v[84:87], v[44:47], v[248:255], v10\n\t" \
    "ds_read_b128 v[68:71], v0 offset:1024\n\t" \
    "s_cbranch_vccnz qk_Gbad249_%=\n\t" \
    "qk_Gc251_%=:\n\t" \
    "s_waitcnt lgkmcnt(5)\n\t" \
    "v_smfmac_f32_16x16x64_bf16 v[72:75], v[48:51], a[32:39], v10\n\t" \
    "s_waitcnt lgkmcnt(4)\n\t" \
    "v_smfmac_f32_16x16x64_bf16 v[72:75], v[52:55], a[40:47], v10\n\t" \
    "s_waitcnt lgkmcnt(3)\n\t" \
    "v_smfmac_f32_16x16x64_bf16 v[72:75], v[56:59], a[48:55], v10\n\t" \
    "global_load_dword v25, v9, s[36:37] sc1\n\t" \
    "s_waitcnt lgkmcnt(2)\n\t" \
    "v_smfmac_f32_16x16x64_bf16 v[72:75], v[60:63], a[56:63], v10\n\t" \
    "s_waitcnt lgkmcnt(1)\n\t" \
    "v_smfmac_f32_16x16x64_bf16 v[72:75], v[64:67], a[0:7], v10\n\t" \
    "s_waitcnt lgkmcnt(0)\n\t" \
    "v_smfmac_f32_16x16x64_bf16 v[72:75], v[68:71], a[8:15], v10\n\t" \
    "v_smfmac_f32_16x16x64_bf16 v[76:79], v[48:51], a[104:111], v10\n\t" \
    "s_nop 1\n\t" \
    "s_nop 0\n\t" \
    "v_smfmac_f32_16x16x64_bf16 v[76:79], v[52:55], a[112:119], v10\n\t" \
    "s_nop 1\n\t" \
    "s_nop 0\n\t" \
    "v_smfmac_f32_16x16x64_bf16 v[76:79], v[56:59], a[120:127], v10\n\t" \
    "v_add_f32 v72, v72, v74\n\t" \
    "v_smfmac_f32_16x16x64_bf16 v[76:79], v[60:63], a[128:135], v10\n\t" \
    "v_add_f32 v73, v73, v75\n\t" \
    "v_smfmac_f32_16x16x64_bf16 v[76:79], v[64:67], a[72:79], v10\n\t" \
    "v_cvt_pk_bf16_f32 v156, v72, v73\n\t" \
    "v_smfmac_f32_16x16x64_bf16 v[76:79], v[68:71], a[80:87], v10\n\t" \
    "ds_write_b32 v12, v156 offset:0\n\t" \
    "v_smfmac_f32_16x16x64_bf16 v[80:83], v[48:51], a[176:183], v10\n\t" \
    "s_nop 1\n\t" \
    "s_nop 0\n\t" \
    "v_smfmac_f32_16x16x64_bf16 v[80:83], v[52:55], a[184:191], v10\n\t" \
    "s_nop 1\n\t" \
    "s_nop 0\n\t" \
    "v_smfmac_f32_16x16x64_bf16 v[80:83], v[56:59], a[192:199], v10\n\t" \
    "v_add_f32 v76, v76, v78\n\t" \
    "v_smfmac_f32_16x16x64_bf16 v[80:83], v[60:63], a[200:207], v10\n\t" \
    "v_add_f32 v77, v77, v79\n\t" \
    "v_smfmac_f32_16x16x64_bf16 v[80:83], v[64:67], a[144:151], v10\n\t" \
    "v_cvt_pk_bf16_f32 v157, v76, v77\n\t" \
    "v_smfmac_f32_16x16x64_bf16 v[80:83], v[68:71], a[152:159], v10\n\t" \
    "ds_write_b32 v12, v157 offset:64\n\t" \
    "v_smfmac_f32_16x16x64_bf16 v[84:87], v[48:51], a[248:255], v10\n\t" \
    "s_nop 1\n\t" \
    "s_nop 0\n\t" \
    "v_smfmac_f32_16x16x64_bf16 v[84:87], v[52:55], v[224:231], v10\n\t" \
    "s_nop 1\n\t" \
    "s_nop 0\n\t" \
    "v_smfmac_f32_16x16x64_bf16 v[84:87], v[56:59], v[232:239], v10\n\t" \
    "v_add_f32 v80, v80, v82\n\t" \
    "v_smfmac_f32_16x16x64_bf16 v[84:87], v[60:63], v[240:247], v10\n\t" \
    "v_add_f32 v81, v81, v83\n\t" \
    "v_smfmac_f32_16x16x64_bf16 v[84:87], v[64:67], a[216:223], v10\n\t" \
    "v_cvt_pk_bf16_f32 v158, v80, v81\n\t" \
    "v_smfmac_f32_16x16x64_bf16 v[84:87], v[68:71], a[224:231], v10\n\t" \
    "ds_write_b32 v16, v158 offset:0\n\t" \
    "s_nop 6\n\t" \
    "v_pk_add_f32 v[84:85], v[84:85], v[86:87]\n\t" \
    "v_cvt_pk_bf16_f32 v159, v84, v85\n\t" \
    "ds_write_b32 v16, v159 offset:256\n\t" \
    "s_waitcnt lgkmcnt(0)\n\t" \
    "s_barrier\n\t" \
    "ds_read_b64_tr_b16 v[132:133], v13 offset:0\n\t" \
    "ds_read_b64_tr_b16 v[134:135], v13 offset:128\n\t" \
    "ds_read_b64_tr_b16 v[136:137], v13 offset:1024\n\t" \
    "ds_read_b64_tr_b16 v[138:139], v13 offset:1152\n\t" \
    "ds_read_b64_tr_b16 v[140:141], v17 offset:0\n\t" \
    "ds_read_b64_tr_b16 v[142:143], v17 offset:256\n\t" \
    "ds_read_b64_tr_b16 v[144:145], v17 offset:512\n\t" \
    "ds_read_b64_tr_b16 v[146:147], v17 offset:768\n\t" \
    "ds_read_b64_tr_b16 v[148:149], v17 offset:1024\n\t" \
    "ds_read_b64_tr_b16 v[150:151], v17 offset:1280\n\t" \
    "ds_read_b64_tr_b16 v[152:153], v17 offset:1536\n\t" \
    "ds_read_b64_tr_b16 v[154:155], v17 offset:1792\n\t" \
    "s_waitcnt lgkmcnt(10)\n\t" \
    "v_smfmac_f32_16x16x64_bf16 v[88:91], v[132:135], v[100:107], v10\n\t" \
    "v_smfmac_f32_16x16x64_bf16 v[92:95], v[132:135], v[116:123], v10\n\t" \
    "s_waitcnt lgkmcnt(8)\n\t" \
    "v_smfmac_f32_16x16x64_bf16 v[88:91], v[136:139], v[108:115], v10\n\t" \
    "v_smfmac_f32_16x16x64_bf16 v[92:95], v[136:139], v[124:131], v10\n\t" \
    "s_waitcnt lgkmcnt(4)\n\t" \
    "v_smfmac_f32_16x16x64_bf16 v[96:99], v[132:135], v[140:147], v10\n\t" \
    "s_waitcnt lgkmcnt(0)\n\t" \
    "s_nop 1\n\t" \
    "v_smfmac_f32_16x16x64_bf16 v[96:99], v[136:139], v[148:155], v10\n\t" \
    "s_nop 0\n\t" \
    "v_add_f32 v168, v89, v91\n\t" \
    "v_add_f32 v169, v93, v95\n\t" \
    "v_max_f32 v178, v168, v169\n\t" \
    "v_mul_f32 v168, v168, v29\n\t" \
    "v_mul_f32 v169, v169, v29\n\t" \
    "v_max_f32_dpp v178, v178, v178 row_mirror row_mask:0xf bank_mask:0xf\n\t" \
    "s_nop 0\n\t" \
    "v_add_f32 v172, v97, v99\n\t" \
    "v_max_f32 v173, v178, v172\n\t" \
    "v_mul_f32 v172, v172, v29\n\t" \
    "s_nop 0\n\t" \
    "v_max_f32_dpp v173, v173, v173 quad_perm:[1,0,3,2] row_mask:0xf bank_mask:0xf\n\t" \
    "s_nop 1\n\t" \
    "v_max_f32_dpp v173, v173, v173 quad_perm:[2,3,0,1] row_mask:0xf bank_mask:0xf\n\t" \
    "s_nop 1\n\t" \
    "v_max_f32_dpp v173, v173, v173 row_half_mirror row_mask:0xf bank_mask:0xf\n\t" \
    "v_fma_f32 v172, -v173, v29, v172\n\t" \
    "v_fma_f32 v168, -v173, v29, v168\n\t" \
    "v_fma_f32 v169, -v173, v29, v169\n\t" \
    "v_exp_f32 v172, v172\n\t" \
    "v_exp_f32 v168, v168\n\t" \
    "v_exp_f32 v169, v169\n\t" \
    "v_fma_f32 v173, v172, 0.5, v168\n\t" \
    "v_add_f32 v173, v173, v169\n\t" \
    "v_mov_b32_dpp v174, v172 quad_perm:[0,0,2,2] row_mask:0xf bank_mask:0xf\n\t" \
    "v_mov_b32_dpp v175, v172 quad_perm:[1,1,3,3] row_mask:0xf bank_mask:0xf\n\t" \
    "v_add_f32_dpp v173, v173, v173 quad_perm:[1,0,3,2] row_mask:0xf bank_mask:0xf\n\t" \
    "v_mov_b32_dpp v176, v168 quad_perm:[1,0,3,2] row_mask:0xf bank_mask:0xf\n\t" \
    "v_mov_b32_dpp v177, v169 quad_perm:[1,0,3,2] row_mask:0xf bank_mask:0xf\n\t" \
    "v_add_f32_dpp v173, v173, v173 quad_perm:[2,3,0,1] row_mask:0xf bank_mask:0xf\n\t" \
    "v_cndmask_b32 v168, v168, v177, s[48:49]\n\t" \
    "v_cndmask_b32 v169, v176, v169, s[48:49]\n\t" \
    "v_add_f32_dpp v173, v173, v173 row_half_mirror row_mask:0xf bank_mask:0xf\n\t" \
    "s_nop 1\n\t" \
    "v_add_f32_dpp v173, v173, v173 row_mirror row_mask:0xf bank_mask:0xf\n\t" \
    "v_rcp_f32 v173, v173\n\t" \
    "s_nop 0\n\t" \
    "v_pk_mul_f32 v[168:169], v[168:169], v[172:173] op_sel:[0,1] op_sel_hi:[1,1]\n\t" \
    "v_pk_mul_f32 v[174:175], v[174:175], v[172:173] op_sel:[0,1] op_sel_hi:[1,1]\n\t" \
    "v_cvt_pk_bf16_f32 v176, v168, v169\n\t" \
    "global_store_dword v18, v176, s[46:47] sc1\n\t" \
    "global_store_dword v3, v176, s[46:47] sc1\n\t" \
    "v_cvt_pk_bf16_f32 v177, v174, v175\n\t" \
    "global_store_dword v19, v177, s[46:47] sc1\n\t" \
    "global_store_dword v4, v177, s[46:47] sc1\n\t" \
    "s_cmp_eq_u32 s65, 63\n\t" \
    "s_cbranch_scc1 qk_last40_%=\n\t" \
    "s_mov_b32 s71, 3\n\t" \
    "qk_E53_%=:\n\t" \
    "s_memtime s[54:55]\n\t" \
    "s_nop 0\n\t" \
    "s_memrealtime s[4:5]\n\t" \
    "s_nop 0\n\t" \
    "s_memrealtime s[6:7]\n\t" \
    "s_nop 0\n\t" \
    "s_memrealtime s[8:9]\n\t" \
    "s_nop 0\n\t" \
    "s_memrealtime s[10:11]\n\t" \
    "s_nop 0\n\t" \
    "s_memrealtime s[12:13]\n\t" \
    "s_nop 0\n\t" \
    "s_memrealtime s[14:15]\n\t" \
    "s_nop 0\n\t" \
    "s_memrealtime s[16:17]\n\t" \
    "s_nop 0\n\t" \
    "s_memrealtime s[18:19]\n\t" \
    "s_nop 0\n\t" \
    "s_memrealtime s[20:21]\n\t" \
    "s_nop 0\n\t" \
    "s_memrealtime s[22:23]\n\t" \
    "s_nop 0\n\t" \
    "s_memrealtime s[24:25]\n\t" \
    "s_nop 0\n\t" \
    "s_memrealtime s[26:27]\n\t" \
    "s_nop 0\n\t" \
    "s_memrealtime s[28:29]\n\t" \
    "s_nop 0\n\t" \
    "s_memtime s[56:57]\n\t" \
    "s_mov_b32 s70, 1\n\t" \
    "s_sub_u32 s71, s71, 1\n\t" \
    "s_cselect_b32 s69, 0, -1\n\t" \
    "s_waitcnt lgkmcnt(0)\n\t" \
    "s_sub_u32 s54, s56, s54\n\t" \
    "s_and_b32 s54, s54, s69\n\t" \
    "s_cmp_gt_u32 s54, 124\n\t" \
    "s_cbranch_scc1 qk_E53_%=\n\t" \
    "s_cmp_eq_u32 s4, s28\n\t" \
    "s_addc_u32 s70, s70, 0\n\t" \
    "s_cmp_eq_u32 s6, s28\n\t" \
    "s_addc_u32 s70, s70, 0\n\t" \
    "s_cmp_eq_u32 s8, s28\n\t" \
    "s_addc_u32 s70, s70, 0\n\t" \
    "s_cmp_eq_u32 s10, s28\n\t" \
    "s_addc_u32 s70, s70, 0\n\t" \
    "s_cmp_eq_u32 s12, s28\n\t" \
    "s_addc_u32 s70, s70, 0\n\t" \
    "s_cmp_eq_u32 s14, s28\n\t" \
    "s_addc_u32 s70, s70, 0\n\t" \
    "s_cmp_eq_u32 s16, s28\n\t" \
    "s_addc_u32 s70, s70, 0\n\t" \
    "s_cmp_eq_u32 s18, s28\n\t" \
    "s_addc_u32 s70, s70, 0\n\t" \
    "s_cmp_eq_u32 s20, s28\n\t" \
    "s_addc_u32 s70, s70, 0\n\t" \
    "s_cmp_eq_u32 s22, s28\n\t" \
    "s_addc_u32 s70, s70, 0\n\t" \
    "s_cmp_eq_u32 s24, s28\n\t" \
    "s_addc_u32 s70, s70, 0\n\t" \
    "s_cmp_eq_u32 s26, s28\n\t" \
    "s_addc_u32 s70, s70, 0\n\t" \
    "s_mov_b32 s75, s73\n\t" \
    "s_mov_b32 s76, s74\n\t" \
    "s_lshl_b32 s70, s70, 3\n\t" \
    "s_sub_u32 s73, s56, s70\n\t" \
    "s_lshl_b32 s74, s28, 4\n\t" \
    "s_sub_u32 s69, s73, s75\n\t" \
    "s_sub_u32 s70, s74, s76\n\t" \
    "s_sub_u32 s71, s70, 1600\n\t" \
    "s_cmp_lt_u32 s71, 400000\n\t" \
    "s_cbranch_scc0 qk_K55_%=\n\t" \
    "v_cvt_f32_u32 v160, s69\n\t" \
    "v_cvt_f32_u32 v161, s70\n\t" \
    "v_rcp_f32 v162, v160\n\t" \
    "v_rcp_f32 v163, v161\n\t" \
    "v_mul_f32 v162, v162, v161\n\t" \
    "v_mul_f32 v163, v163, v160\n\t" \
    "v_mul_f32 v162, 0x4f800000, v162\n\t" \
    "v_mul_f32 v163, 0x4f000000, v163\n\t" \
    "v_cvt_u32_f32 v162, v162\n\t" \
    "v_cvt_u32_f32 v163, v163\n\t" \
    "v_readfirstlane_b32 s69, v162\n\t" \
    "v_readfirstlane_b32 s70, v163\n\t" \
    "s_nop 1\n\t" \
    "s_sub_u32 s69, s69, s78\n\t" \
    "s_ashr_i32 s69, s69, 2\n\t" \
    "s_add_u32 s78, s78, s69\n\t" \
    "s_sub_u32 s70, s70, s77\n\t" \
    "s_ashr_i32 s70, s70, 2\n\t" \
    "s_add_u32 s77, s77, s70\n\t" \
    "qk_K55_%=:\n\t" \
    "s_waitcnt vmcnt(4)\n\t" \
    "v_readfirstlane_b32 s69, v25\n\t" \
    "s_add_u32 s71, s65, 1\n\t" \
    "s_max_i32 s83, s83, s69\n\t" \
    "qk_WF56_%=:\n\t" \
    "s_cmp_ge_i32 s83, s71\n\t" \
    "s_cbranch_scc1 qk_WFok57_%=\n\t" \
    "global_load_dword v160, v24, s[36:37] sc1\n\t" \
    "s_waitcnt vmcnt(0)\n\t" \
    "v_readfirstlane_b32 s83, v160\n\t" \
    "s_nop 1\n\t" \
    "s_cmp_ge_i32 s83, s71\n\t" \
    "s_cbranch_scc1 qk_WFok57_%=\n\t" \
    "s_cmp_eq_u32 s68, 0\n\t" \
    "s_cbranch_scc1 qk_F58_%=\n\t" \
    "s_sub_u32 s68, s68, 1\n\t" \
    "s_sleep 2\n\t" \
    "s_branch qk_WF56_%=\n\t" \
    "qk_F58_%=:\n\t" \
    "v_mov_b32 v160, 6095104\n\t" \
    "v_mov_b32 v161, 9\n\t" \
    "global_store_dword v160, v161, s[36:37] sc1\n\t" \
    "s_waitcnt vmcnt(0)\n\t" \
    "s_branch qk_WFok57_%=\n\t" \
    "qk_WFok57_%=:\n\t" \
    "v_mov_b32 v72, 0\n\t" \
    "v_mov_b32 v73, 0\n\t" \
    "v_mov_b32 v74, 0\n\t" \
    "v_mov_b32 v75, 0\n\t" \
    "v_mov_b32 v76, 0\n\t" \
    "v_mov_b32 v77, 0\n\t" \
    "v_mov_b32 v78, 0\n\t" \
    "v_mov_b32 v79, 0\n\t" \
    "v_mov_b32 v80, 0\n\t" \
    "v_mov_b32 v81, 0\n\t" \
    "v_mov_b32 v82, 0\n\t" \
    "v_mov_b32 v83, 0\n\t" \
    "v_mov_b32 v84, 0\n\t" \
    "v_mov_b32 v85, 0\n\t" \
    "v_mov_b32 v86, 0\n\t" \
    "v_mov_b32 v87, 0\n\t" \
    "v_mov_b32 v88, 0\n\t" \
    "v_mov_b32 v89, 0\n\t" \
    "v_mov_b32 v90, 0\n\t" \
    "v_mov_b32 v91, 0\n\t" \
    "v_mov_b32 v92, 0\n\t" \
    "v_mov_b32 v93, 0\n\t" \
    "v_mov_b32 v94, 0\n\t" \
    "v_mov_b32 v95, 0\n\t" \
    "v_mov_b32 v96, 0\n\t" \
    "v_mov_b32 v97, 0\n\t" \
    "v_mov_b32 v98, 0\n\t" \
    "v_mov_b32 v99, 0\n\t" \
    "s_add_u32 s79, s74, 416\n\t" \
    "s_sub_u32 s70, s79, s74\n\t" \
    "s_max_i32 s70, s70, 0\n\t" \
    "s_min_i32 s70, s70, 16000\n\t" \
    "s_lshl_b32 s70, s70, 1\n\t" \
    "s_mul_hi_u32 s70, s70, s77\n\t" \
    "s_add_u32 s70, s70, s73\n\t" \
    "qk_W59_%=:\n\t" \
    "s_memtime s[56:57]\n\t" \
    "s_waitcnt lgkmcnt(0)\n\t" \
    "s_sub_u32 s69, s70, s56\n\t" \
    "s_cmp_gt_i32 s69, 24\n\t" \
    "s_cbranch_scc1 qk_W59_%=\n\t" \
    "global_load_dwordx4 v[100:103], v23, s[38:39] offset:0 sc1\n\t" \
    "global_load_dwordx4 v[104:107], v23, s[38:39] offset:2048 sc1\n\t" \
    "global_load_dwordx4 v[108:111], v15, s[38:39] offset:0 sc1\n\t" \
    "global_load_dwordx4 v[112:115], v15, s[38:39] offset:2048 sc1\n\t" \
    "global_load_dwordx4 v[116:119], v23, s[38:39] offset:1024 sc1\n\t" \
    "global_load_dwordx4 v[120:123], v23, s[38:39] offset:3072 sc1\n\t" \
    "global_load_dwordx4 v[124:127], v15, s[38:39] offset:1024 sc1\n\t" \
    "global_load_dwordx4 v[128:131], v15, s[38:39] offset:3072 sc1\n\t" \
    "s_add_u32 s65, s65, 1\n\t" \
    "v_add_u32 v31, 1, v31\n\t" \
    "s_add_u32 s69, s65, 0\n\t" \
    "s_bitcmp1_b32 s69, 0\n\t" \
    "s_cselect_b32 s70, s63, s62\n\t" \
    "s_bfe_u32 s69, s69, 0x20001\n\t" \
    "s_lshl_b32 s69, s69, 19\n\t" \
    "s_add_u32 s69, s69, s70\n\t" \
    "s_add_u32 s46, s36, s69\n\t" \
    "s_addc_u32 s47, s37, 0\n\t" \
    "s_add_u32 s69, s65, 3\n\t" \
    "s_cmp_ge_i32 s83, s69\n\t" \
    "s_cselect_b64 vcc, -1, 0\n\t" \
    "v_mov_b32 v9, 6201216\n\t" \
    "v_cndmask_b32 v9, v24, v9, vcc\n\t" \
    "s_mul_i32 s70, s67, 7\n\t" \
    "s_add_u32 s69, s65, 1\n\t" \
    "s_xor_b32 s69, s69, s70\n\t" \
    "s_and_b32 s69, s69, 63\n\t" \
    "s_lshl_b32 s69, s69, 13\n\t" \
    "s_mul_i32 s70, s67, 524288\n\t" \
    "s_add_u32 s69, s69, s70\n\t" \
    "s_add_u32 s69, s69, 23068672\n\t" \
    "s_add_u32 s38, s36, s69\n\t" \
    "s_addc_u32 s39, s37, 0\n\t" \
    "s_waitcnt lgkmcnt(0)\n\t" \
    "s_barrier\n\t" \
    "ds_read_b32 v160, v11 offset:20480\n\t" \
    "s_waitcnt lgkmcnt(0)\n\t" \
    "v_readfirstlane_b32 s81, v160\n\t" \
    "s_nop 1\n\t" \
    "s_mul_hi_u32 s69, s85, s78\n\t" \
    "s_add_u32 s79, s81, s69\n\t" \
    "s_add_u32 s79, s79, s87\n\t" \
    "s_cmp_ge_u32 s65, 37\n\t" \
    "s_cselect_b32 s69, 28, 0\n\t" \
    "s_sub_u32 s79, s79, s69\n\t" \
    "s_add_u32 s80, s79, s88\n\t" \
    "s_branch qk_go43_%=\n\t" \
    "qk_Gbad48_%=:\n\t" \
    "s_waitcnt vmcnt(0) lgkmcnt(0)\n\t" \
    "s_nop 7\n\t" \
    "v_mov_b32 v72, 0\n\t" \
    "v_mov_b32 v73, 0\n\t" \
    "v_mov_b32 v74, 0\n\t" \
    "v_mov_b32 v75, 0\n\t" \
    "v_mov_b32 v76, 0\n\t" \
    "v_mov_b32 v77, 0\n\t" \
    "v_mov_b32 v78, 0\n\t" \
    "v_mov_b32 v79, 0\n\t" \
    "v_mov_b32 v80, 0\n\t" \
    "v_mov_b32 v81, 0\n\t" \
    "v_mov_b32 v82, 0\n\t" \
    "v_mov_b32 v83, 0\n\t" \
    "v_mov_b32 v84, 0\n\t" \
    "v_mov_b32 v85, 0\n\t" \
    "v_mov_b32 v86, 0\n\t" \
    "v_mov_b32 v87, 0\n\t" \
    "s_cmp_eq_u32 s68, 0\n\t" \
    "s_cbranch_scc1 qk_F60_%=\n\t" \
    "s_sub_u32 s68, s68, 1\n\t" \
    "s_branch qk_G47_%=\n\t" \
    "qk_F60_%=:\n\t" \
    "v_mov_b32 v160, 6095104\n\t" \
    "v_mov_b32 v161, 1\n\t" \
    "global_store_dword v160, v161, s[36:37] sc1\n\t" \
    "s_waitcnt vmcnt(0)\n\t" \
    "s_branch qk_Gc50_%=\n\t" \
    "qk_Gbad249_%=:\n\t" \
    "global_load_dwordx4 v[44:47], v2, s[46:47] sc1\n\t" \
    "s_waitcnt vmcnt(0)\n\t" \
    "v_max3_u32 v160, v44, v45, v46\n\t" \
    "v_max_u32 v160, v160, v47\n\t" \
    "v_cmp_eq_u32 vcc, -1, v160\n\t" \
    "s_cbranch_vccz qk_Gre252_%=\n\t" \
    "s_cmp_eq_u32 s68, 0\n\t" \
    "s_cbranch_scc1 qk_F61_%=\n\t" \
    "s_sub_u32 s68, s68, 1\n\t" \
    "s_sleep 1\n\t" \
    "s_branch qk_Gbad249_%=\n\t" \
    "qk_F61_%=:\n\t" \
    "v_mov_b32 v160, 6095104\n\t" \
    "v_mov_b32 v161, 2\n\t" \
    "global_store_dword v160, v161, s[36:37] sc1\n\t" \
    "s_waitcnt vmcnt(0)\n\t" \
    "s_branch qk_Gre252_%=\n\t" \
    "qk_Gre252_%=:\n\t" \
    "v_mov_b32 v72, 0\n\t" \
    "v_mov_b32 v73, 0\n\t" \
    "v_mov_b32 v74, 0\n\t" \
    "v_mov_b32 v75, 0\n\t" \
    "v_mov_b32 v76, 0\n\t" \
    "v_mov_b32 v77, 0\n\t" \
    "v_mov_b32 v78, 0\n\t" \
    "v_mov_b32 v79, 0\n\t" \
    "v_mov_b32 v80, 0\n\t" \
    "v_mov_b32 v81, 0\n\t" \
    "v_mov_b32 v82, 0\n\t" \
    "v_mov_b32 v83, 0\n\t" \
    "v_mov_b32 v84, 0\n\t" \
    "v_mov_b32 v85, 0\n\t" \
    "v_mov_b32 v86, 0\n\t" \
    "v_mov_b32 v87, 0\n\t" \
    "v_smfmac_f32_16x16x64_bf16 v[72:75], v[36:39], a[16:23], v10\n\t" \
    "v_smfmac_f32_16x16x64_bf16 v[76:79], v[36:39], a[88:95], v10\n\t" \
    "v_smfmac_f32_16x16x64_bf16 v[80:83], v[36:39], a[160:167], v10\n\t" \
    "v_smfmac_f32_16x16x64_bf16 v[84:87], v[36:39], a[232:239], v10\n\t" \
    "v_smfmac_f32_16x16x64_bf16 v[72:75], v[40:43], a[24:31], v10\n\t" \
    "v_smfmac_f32_16x16x64_bf16 v[76:79], v[40:43], a[96:103], v10\n\t" \
    "v_smfmac_f32_16x16x64_bf16 v[80:83], v[40:43], a[168:175], v10\n\t" \
    "v_smfmac_f32_16x16x64_bf16 v[84:87], v[40:43], a[240:247], v10\n\t" \
    "v_smfmac_f32_16x16x64_bf16 v[72:75], v[44:47], a[64:71], v10\n\t" \
    "v_smfmac_f32_16x16x64_bf16 v[76:79], v[44:47], a[136:143], v10\n\t" \
    "v_smfmac_f32_16x16x64_bf16 v[80:83], v[44:47], a[208:215], v10\n\t" \
    "v_smfmac_f32_16x16x64_bf16 v[84:87], v[44:47], v[248:255], v10\n\t" \
    "s_branch qk_Gc251_%=\n\t" \
    "qk_last40_%=:\n\t" \
    "s_endpgm\n\t" \
    "qk_loop16_%=:\n\t" \
    "v_mov_b32 v72, 0\n\t" \
    "v_mov_b32 v73, 0\n\t" \
    "v_mov_b32 v74, 0\n\t" \
    "v_mov_b32 v75, 0\n\t" \
    "v_mov_b32 v76, 0\n\t" \
    "v_mov_b32 v77, 0\n\t" \
    "v_mov_b32 v78, 0\n\t" \
    "v_mov_b32 v79, 0\n\t" \
    "v_mov_b32 v80, 0\n\t" \
    "v_mov_b32 v81, 0\n\t" \
    "v_mov_b32 v82, 0\n\t" \
    "v_mov_b32 v83, 0\n\t" \
    "v_mov_b32 v84, 0\n\t" \
    "v_mov_b32 v85, 0\n\t" \
    "v_mov_b32 v86, 0\n\t" \
    "v_mov_b32 v87, 0\n\t" \
    "v_add_u32 v31, 1, v31\n\t" \
    "s_add_u32 s69, s65, 0\n\t" \
    "s_bitcmp1_b32 s69, 0\n\t" \
    "s_cselect_b32 s70, s63, s62\n\t" \
    "s_bfe_u32 s69, s69, 0x20001\n\t" \
    "s_lshl_b32 s69, s69, 19\n\t" \
    "s_add_u32 s69, s69, s70\n\t" \
    "s_add_u32 s46, s36, s69\n\t" \
    "s_addc_u32 s47, s37, 0\n\t" \
    "s_mul_i32 s70, s67, 7\n\t" \
    "s_add_u32 s69, s65, 0\n\t" \
    "s_xor_b32 s69, s69, s70\n\t" \
    "s_and_b32 s69, s69, 63\n\t" \
    "s_lshl_b32 s69, s69, 12\n\t" \
    "s_mul_i32 s70, s67, 262144\n\t" \
    "s_add_u32 s69, s69, s70\n\t" \
    "s_add_u32 s69, s69, 39845888\n\t" \
    "s_add_u32 s40, s36, s69\n\t" \
    "s_addc_u32 s41, s37, 0\n\t" \
    "s_sub_u32 s69, s65, 1\n\t" \
    "v_mov_b32 v25, s69\n\t" \
    "qk_go65_%=:\n\t" \
    "s_sub_u32 s70, s79, s74\n\t" \
    "s_max_i32 s70, s70, 0\n\t" \
    "s_min_i32 s70, s70, 16000\n\t" \
    "s_lshl_b32 s70, s70, 1\n\t" \
    "s_mul_hi_u32 s70, s70, s77\n\t" \
    "s_add_u32 s70, s70, s73\n\t" \
    "s_sub_u32 s70, s70, 100\n\t" \
    "s_getpc_b64 s[30:31]\n\t" \
    "qk_Wp67_%=:\n\t" \
    "s_add_u32 s30, s30, qk_We68_%=-qk_Wp67_%=\n\t" \
    "s_addc_u32 s31, s31, 0\n\t" \
    "qk_W66_%=:\n\t" \
    "s_memtime s[56:57]\n\t" \
    "s_waitcnt lgkmcnt(0)\n\t" \
    "s_sub_u32 s69, s70, s56\n\t" \
    "s_cmp_gt_i32 s69, 111\n\t" \
    "s_cbranch_scc1 qk_W66_%=\n\t" \
    "s_max_i32 s69, s69, 0\n\t" \
    "s_lshr_b32 s69, s69, 1\n\t" \
    "s_and_b32 s69, s69, 0x3c\n\t" \
    "s_sub_u32 s30, s30, s69\n\t" \
    "s_subb_u32 s31, s31, 0\n\t" \
    "s_setpc_b64 s[30:31]\n\t" \
    ".p2align 6\n\t" \
    "s_nop 0\n\t" \
    "s_nop 0\n\t" \
    "s_nop 0\n\t" \
    "s_nop 1\n\t" \
    "s_nop 1\n\t" \
    "s_nop 1\n\t" \
    "s_nop 1\n\t" \
    "s_nop 1\n\t" \
    "s_nop 1\n\t" \
    "s_nop 1\n\t" \
    "s_nop 1\n\t" \
    "s_nop 1\n\t" \
    "s_nop 1\n\t" \
    "s_nop 1\n\t" \
    "s_nop 1\n\t" \
    "s_nop 1\n\t" \
    "qk_We68_%=:\n\t" \
    "qk_G69_%=:\n\t" \
    "global_load_dwordx4 v[36:39], v1, s[46:47] sc1\n\t" \
    "s_nop 9\n\t" \
    "global_load_dwordx4 v[40:43], v1, s[46:47] offset:1024 sc1\n\t" \
    "s_nop 9\n\t" \
    "global_load_dwordx4 v[44:47], v2, s[46:47] sc1\n\t" \
    "s_waitcnt vmcnt(2)\n\t" \
    "v_smfmac_f32_16x16x64_bf16 v[72:75], v[36:39], a[32:39], v10\n\t" \
    "ds_write_b128 v0, v[36:39] offset:4096\n\t" \
    "v_smfmac_f32_16x16x64_bf16 v[76:79], v[36:39], a[104:111], v10\n\t" \
    "v_max3_u32 v160, v36, v37, v38\n\t" \
    "v_smfmac_f32_16x16x64_bf16 v[80:83], v[36:39], a[176:183], v10\n\t" \
    "v_smfmac_f32_16x16x64_bf16 v[84:87], v[36:39], a[248:255], v10\n\t" \
    "s_waitcnt vmcnt(1)\n\t" \
    "ds_write_b128 v0, v[40:43] offset:5120\n\t" \
    "v_smfmac_f32_16x16x64_bf16 v[72:75], v[40:43], a[40:47], v10\n\t" \
    "v_max3_u32 v160, v160, v39, v40\n\t" \
    "v_max3_u32 v160, v160, v41, v42\n\t" \
    "v_smfmac_f32_16x16x64_bf16 v[76:79], v[40:43], a[112:119], v10\n\t" \
    "v_max_u32 v160, v160, v43\n\t" \
    "v_cmp_eq_u32 vcc, -1, v160\n\t" \
    "v_smfmac_f32_16x16x64_bf16 v[80:83], v[40:43], a[184:191], v10\n\t" \
    "v_smfmac_f32_16x16x64_bf16 v[84:87], v[40:43], v[224:231], v10\n\t" \
    "s_cbranch_vccnz qk_Gbad70_%=\n\t" \
    "qk_Gc72_%=:\n\t" \
    "s_waitcnt lgkmcnt(0)\n\t" \
    "s_barrier\n\t" \
    "ds_read_b128 v[48:51], v0 offset:6144\n\t" \
    "ds_read_b128 v[52:55], v0 offset:7168\n\t" \
    "s_waitcnt vmcnt(0)\n\t" \
    "v_smfmac_f32_16x16x64_bf16 v[72:75], v[44:47], a[64:71], v10\n\t" \
    "ds_read_b128 v[56:59], v0 offset:0\n\t" \
    "v_max3_u32 v160, v44, v45, v46\n\t" \
    "v_smfmac_f32_16x16x64_bf16 v[76:79], v[44:47], a[136:143], v10\n\t" \
    "ds_read_b128 v[60:63], v0 offset:1024\n\t" \
    "v_max_u32 v160, v160, v47\n\t" \
    "v_smfmac_f32_16x16x64_bf16 v[80:83], v[44:47], a[208:215], v10\n\t" \
    "ds_read_b128 v[64:67], v0 offset:2048\n\t" \
    "v_cmp_eq_u32 vcc, -1, v160\n\t" \
    "v_smfmac_f32_16x16x64_bf16 v[84:87], v[44:47], v[248:255], v10\n\t" \
    "ds_read_b128 v[68:71], v0 offset:3072\n\t" \
    "s_cbranch_vccnz qk_Gbad271_%=\n\t" \
    "qk_Gc273_%=:\n\t" \
    "s_waitcnt lgkmcnt(5)\n\t" \
    "v_smfmac_f32_16x16x64_bf16 v[72:75], v[48:51], a[48:55], v10\n\t" \
    "global_store_dword v14, v25, s[36:37] sc1\n\t" \
    "s_waitcnt lgkmcnt(4)\n\t" \
    "v_smfmac_f32_16x16x64_bf16 v[72:75], v[52:55], a[56:63], v10\n\t" \
    "s_waitcnt lgkmcnt(3)\n\t" \
    "v_smfmac_f32_16x16x64_bf16 v[72:75], v[56:59], a[0:7], v10\n\t" \
    "s_waitcnt lgkmcnt(2)\n\t" \
    "v_smfmac_f32_16x16x64_bf16 v[72:75], v[60:63], a[8:15], v10\n\t" \
    "s_waitcnt lgkmcnt(1)\n\t" \
    "v_smfmac_f32_16x16x64_bf16 v[72:75], v[64:67], a[16:23], v10\n\t" \
    "s_waitcnt lgkmcnt(0)\n\t" \
    "v_smfmac_f32_16x16x64_bf16 v[72:75], v[68:71], a[24:31], v10\n\t" \
    "v_smfmac_f32_16x16x64_bf16 v[76:79], v[48:51], a[120:127], v10\n\t" \
    "s_nop 1\n\t" \
    "s_nop 0\n\t" \
    "v_smfmac_f32_16x16x64_bf16 v[76:79], v[52:55], a[128:135], v10\n\t" \
    "s_nop 1\n\t" \
    "s_nop 0\n\t" \
    "v_smfmac_f32_16x16x64_bf16 v[76:79], v[56:59], a[72:79], v10\n\t" \
    "v_add_f32 v72, v72, v74\n\t" \
    "v_smfmac_f32_16x16x64_bf16 v[76:79], v[60:63], a[80:87], v10\n\t" \
    "v_add_f32 v73, v73, v75\n\t" \
    "v_smfmac_f32_16x16x64_bf16 v[76:79], v[64:67], a[88:95], v10\n\t" \
    "v_cvt_pk_bf16_f32 v156, v72, v73\n\t" \
    "v_smfmac_f32_16x16x64_bf16 v[76:79], v[68:71], a[96:103], v10\n\t" \
    "ds_write_b32 v12, v156 offset:0\n\t" \
    "v_smfmac_f32_16x16x64_bf16 v[80:83], v[48:51], a[192:199], v10\n\t" \
    "s_nop 1\n\t" \
    "s_nop 0\n\t" \
    "v_smfmac_f32_16x16x64_bf16 v[80:83], v[52:55], a[200:207], v10\n\t" \
    "s_nop 1\n\t" \
    "s_nop 0\n\t" \
    "v_smfmac_f32_16x16x64_bf16 v[80:83], v[56:59], a[144:151], v10\n\t" \
    "v_add_f32 v76, v76, v78\n\t" \
    "v_smfmac_f32_16x16x64_bf16 v[80:83], v[60:63], a[152:159], v10\n\t" \
    "v_add_f32 v77, v77, v79\n\t" \
    "v_smfmac_f32_16x16x64_bf16 v[80:83], v[64:67], a[160:167], v10\n\t" \
    "v_cvt_pk_bf16_f32 v157, v76, v77\n\t" \
    "v_smfmac_f32_16x16x64_bf16 v[80:83], v[68:71], a[168:175], v10\n\t" \
    "ds_write_b32 v12, v157 offset:64\n\t" \
    "v_smfmac_f32_16x16x64_bf16 v[84:87], v[48:51], v[232:239], v10\n\t" \
    "s_nop 1\n\t" \
    "s_nop 0\n\t" \
    "v_smfmac_f32_16x16x64_bf16 v[84:87], v[52:55], v[240:247], v10\n\t" \
    "s_nop 1\n\t" \
    "s_nop 0\n\t" \
    "v_smfmac_f32_16x16x64_bf16 v[84:87], v[56:59], a[216:223], v10\n\t" \
    "v_add_f32 v80, v80, v82\n\t" \
    "v_smfmac_f32_16x16x64_bf16 v[84:87], v[60:63], a[224:231], v10\n\t" \
    "v_add_f32 v81, v81, v83\n\t" \
    "v_smfmac_f32_16x16x64_bf16 v[84:87], v[64:67], a[232:239], v10\n\t" \
    "v_cvt_pk_bf16_f32 v158, v80, v81\n\t" \
    "v_smfmac_f32_16x16x64_bf16 v[84:87], v[68:71], a[240:247], v10\n\t" \
    "ds_write_b32 v16, v158 offset:0\n\t" \
    "s_nop 6\n\t" \
    "v_pk_add_f32 v[84:85], v[84:85], v[86:87]\n\t" \
    "v_cvt_pk_bf16_f32 v159, v84, v85\n\t" \
    "ds_write_b32 v16, v159 offset:256\n\t" \
    "s_waitcnt lgkmcnt(0)\n\t" \
    "s_barrier\n\t" \
    "s_sleep 1\n\t" \
    "ds_read_b64_tr_b16 v[140:141], v17 offset:0\n\t" \
    "ds_read_b64_tr_b16 v[142:143], v17 offset:256\n\t" \
    "ds_read_b64_tr_b16 v[144:145], v17 offset:512\n\t" \
    "ds_read_b64_tr_b16 v[146:147], v17 offset:768\n\t" \
    "ds_read_b64_tr_b16 v[148:149], v17 offset:1024\n\t" \
    "ds_read_b64_tr_b16 v[150:151], v17 offset:1280\n\t" \
    "ds_read_b64_tr_b16 v[152:153], v17 offset:1536\n\t" \
    "ds_read_b64_tr_b16 v[154:155], v17 offset:1792\n\t" \
    "s_waitcnt lgkmcnt(0)\n\t" \
    "s_cmp_eq_u32 s65, 63\n\t" \
    "s_cbranch_scc1 qk_last62_%=\n\t" \
    "s_mov_b32 s71, 3\n\t" \
    "qk_E75_%=:\n\t" \
    "s_memtime s[54:55]\n\t" \
    "s_nop 0\n\t" \
    "s_memrealtime s[4:5]\n\t" \
    "s_nop 0\n\t" \
    "s_memrealtime s[6:7]\n\t" \
    "s_nop 0\n\t" \
    "s_memrealtime s[8:9]\n\t" \
    "s_nop 0\n\t" \
    "s_memrealtime s[10:11]\n\t" \
    "s_nop 0\n\t" \
    "s_memrealtime s[12:13]\n\t" \
    "s_nop 0\n\t" \
    "s_memrealtime s[14:15]\n\t" \
    "s_nop 0\n\t" \
    "s_memrealtime s[16:17]\n\t" \
    "s_nop 0\n\t" \
    "s_memrealtime s[18:19]\n\t" \
    "s_nop 0\n\t" \
    "s_memrealtime s[20:21]\n\t" \
    "s_nop 0\n\t" \
    "s_memrealtime s[22:23]\n\t" \
    "s_nop 0\n\t" \
    "s_memrealtime s[24:25]\n\t" \
    "s_nop 0\n\t" \
    "s_memrealtime s[26:27]\n\t" \
    "s_nop 0\n\t" \
    "s_memrealtime s[28:29]\n\t" \
    "s_nop 0\n\t" \
    "s_memtime s[56:57]\n\t" \
    "s_mov_b32 s70, 1\n\t" \
    "s_sub_u32 s71, s71, 1\n\t" \
    "s_cselect_b32 s69, 0, -1\n\t" \
    "s_waitcnt lgkmcnt(0)\n\t" \
    "s_sub_u32 s54, s56, s54\n\t" \
    "s_and_b32 s54, s54, s69\n\t" \
    "s_cmp_gt_u32 s54, 124\n\t" \
    "s_cbranch_scc1 qk_E75_%=\n\t" \
    "s_cmp_eq_u32 s4, s28\n\t" \
    "s_addc_u32 s70, s70, 0\n\t" \
    "s_cmp_eq_u32 s6, s28\n\t" \
    "s_addc_u32 s70, s70, 0\n\t" \
    "s_cmp_eq_u32 s8, s28\n\t" \
    "s_addc_u32 s70, s70, 0\n\t" \
    "s_cmp_eq_u32 s10, s28\n\t" \
    "s_addc_u32 s70, s70, 0\n\t" \
    "s_cmp_eq_u32 s12, s28\n\t" \
    "s_addc_u32 s70, s70, 0\n\t" \
    "s_cmp_eq_u32 s14, s28\n\t" \
    "s_addc_u32 s70, s70, 0\n\t" \
    "s_cmp_eq_u32 s16, s28\n\t" \
    "s_addc_u32 s70, s70, 0\n\t" \
    "s_cmp_eq_u32 s18, s28\n\t" \
    "s_addc_u32 s70, s70, 0\n\t" \
    "s_cmp_eq_u32 s20, s28\n\t" \
    "s_addc_u32 s70, s70, 0\n\t" \
    "s_cmp_eq_u32 s22, s28\n\t" \
    "s_addc_u32 s70, s70, 0\n\t" \
    "s_cmp_eq_u32 s24, s28\n\t" \
    "s_addc_u32 s70, s70, 0\n\t" \
    "s_cmp_eq_u32 s26, s28\n\t" \
    "s_addc_u32 s70, s70, 0\n\t" \
    "s_mov_b32 s75, s73\n\t" \
    "s_mov_b32 s76, s74\n\t" \
    "s_lshl_b32 s70, s70, 3\n\t" \
    "s_sub_u32 s73, s56, s70\n\t" \
    "s_lshl_b32 s74, s28, 4\n\t" \
    "s_sub_u32 s69, s73, s75\n\t" \
    "s_sub_u32 s70, s74, s76\n\t" \
    "s_sub_u32 s71, s70, 1600\n\t" \
    "s_cmp_lt_u32 s71, 400000\n\t" \
    "s_cbranch_scc0 qk_K77_%=\n\t" \
    "v_cvt_f32_u32 v160, s69\n\t" \
    "v_cvt_f32_u32 v161, s70\n\t" \
    "v_rcp_f32 v162, v160\n\t" \
    "v_rcp_f32 v163, v161\n\t" \
    "v_mul_f32 v162, v162, v161\n\t" \
    "v_mul_f32 v163, v163, v160\n\t" \
    "v_mul_f32 v162, 0x4f800000, v162\n\t" \
    "v_mul_f32 v163, 0x4f000000, v163\n\t" \
    "v_cvt_u32_f32 v162, v162\n\t" \
    "v_cvt_u32_f32 v163, v163\n\t" \
    "v_readfirstlane_b32 s69, v162\n\t" \
    "v_readfirstlane_b32 s70, v163\n\t" \
    "s_nop 1\n\t" \
    "s_sub_u32 s69, s69, s78\n\t" \
    "s_ashr_i32 s69, s69, 2\n\t" \
    "s_add_u32 s78, s78, s69\n\t" \
    "s_sub_u32 s70, s70, s77\n\t" \
    "s_ashr_i32 s70, s70, 2\n\t" \
    "s_add_u32 s77, s77, s70\n\t" \
    "qk_K77_%=:\n\t" \
    "v_mov_b32 v72, 0\n\t" \
    "v_mov_b32 v73, 0\n\t" \
    "v_mov_b32 v74, 0\n\t" \
    "v_mov_b32 v75, 0\n\t" \
    "v_mov_b32 v76, 0\n\t" \
    "v_mov_b32 v77, 0\n\t" \
    "v_mov_b32 v78, 0\n\t" \
    "v_mov_b32 v79, 0\n\t" \
    "v_mov_b32 v80, 0\n\t" \
    "v_mov_b32 v81, 0\n\t" \
    "v_mov_b32 v82, 0\n\t" \
    "v_mov_b32 v83, 0\n\t" \
    "v_mov_b32 v84, 0\n\t" \
    "v_mov_b32 v85, 0\n\t" \
    "v_mov_b32 v86, 0\n\t" \
    "v_mov_b32 v87, 0\n\t" \
    "s_add_u32 s79, s74, 560\n\t" \
    "s_cmp_ge_u32 s65, 58\n\t" \
    "s_cselect_b32 s79, s74, s79\n\t" \
    "s_sub_u32 s70, s79, s74\n\t" \
    "s_max_i32 s70, s70, 0\n\t" \
    "s_min_i32 s70, s70, 16000\n\t" \
    "s_lshl_b32 s70, s70, 1\n\t" \
    "s_mul_hi_u32 s70, s70, s77\n\t" \
    "s_add_u32 s70, s70, s73\n\t" \
    "qk_W78_%=:\n\t" \
    "s_memtime s[56:57]\n\t" \
    "s_waitcnt lgkmcnt(0)\n\t" \
    "s_sub_u32 s69, s70, s56\n\t" \
    "s_cmp_gt_i32 s69, 24\n\t" \
    "s_cbranch_scc1 qk_W78_%=\n\t" \
    "s_mov_b64 exec, s[58:59]\n\t" \
    "global_store_dwordx4 v0, v[140:143], s[40:41] offset:0 sc1\n\t" \
    "global_store_dwordx4 v0, v[144:147], s[40:41] offset:1024 sc1\n\t" \
    "global_store_dwordx4 v0, v[148:151], s[40:41] offset:2048 sc1\n\t" \
    "global_store_dwordx4 v0, v[152:155], s[40:41] offset:3072 sc1\n\t" \
    "s_mov_b64 exec, -1\n\t" \
    "s_add_u32 s65, s65, 1\n\t" \
    "v_add_u32 v31, 1, v31\n\t" \
    "s_add_u32 s69, s65, 0\n\t" \
    "s_bitcmp1_b32 s69, 0\n\t" \
    "s_cselect_b32 s70, s63, s62\n\t" \
    "s_bfe_u32 s69, s69, 0x20001\n\t" \
    "s_lshl_b32 s69, s69, 19\n\t" \
    "s_add_u32 s69, s69, s70\n\t" \
    "s_add_u32 s46, s36, s69\n\t" \
    "s_addc_u32 s47, s37, 0\n\t" \
    "s_mul_i32 s70, s67, 7\n\t" \
    "s_add_u32 s69, s65, 0\n\t" \
    "s_xor_b32 s69, s69, s70\n\t" \
    "s_and_b32 s69, s69, 63\n\t" \
    "s_lshl_b32 s69, s69, 12\n\t" \
    "s_mul_i32 s70, s67, 262144\n\t" \
    "s_add_u32 s69, s69, s70\n\t" \
    "s_add_u32 s69, s69, 39845888\n\t" \
    "s_add_u32 s40, s36, s69\n\t" \
    "s_addc_u32 s41, s37, 0\n\t" \
    "s_sub_u32 s69, s65, 1\n\t" \
    "v_mov_b32 v25, s69\n\t" \
    "s_waitcnt lgkmcnt(0)\n\t" \
    "s_barrier\n\t" \
    "ds_read_b32 v160, v11 offset:20480\n\t" \
    "s_waitcnt lgkmcnt(0)\n\t" \
    "v_readfirstlane_b32 s81, v160\n\t" \
    "s_nop 1\n\t" \
    "s_mul_hi_u32 s69, s85, s78\n\t" \
    "s_add_u32 s79, s81, s69\n\t" \
    "s_add_u32 s79, s79, s87\n\t" \
    "s_cmp_ge_u32 s65, 37\n\t" \
    "s_cselect_b32 s69, 28, 0\n\t" \
    "s_sub_u32 s79, s79, s69\n\t" \
    "s_add_u32 s80, s79, s88\n\t" \
    "s_branch qk_go65_%=\n\t" \
    "qk_Gbad70_%=:\n\t" \
    "s_waitcnt vmcnt(0) lgkmcnt(0)\n\t" \
    "s_nop 7\n\t" \
    "v_mov_b32 v72, 0\n\t" \
    "v_mov_b32 v73, 0\n\t" \
    "v_mov_b32 v74, 0\n\t" \
    "v_mov_b32 v75, 0\n\t" \
    "v_mov_b32 v76, 0\n\t" \
    "v_mov_b32 v77, 0\n\t" \
    "v_mov_b32 v78, 0\n\t" \
    "v_mov_b32 v79, 0\n\t" \
    "v_mov_b32 v80, 0\n\t" \
    "v_mov_b32 v81, 0\n\t" \
    "v_mov_b32 v82, 0\n\t" \
    "v_mov_b32 v83, 0\n\t" \
    "v_mov_b32 v84, 0\n\t" \
    "v_mov_b32 v85, 0\n\t" \
    "v_mov_b32 v86, 0\n\t" \
    "v_mov_b32 v87, 0\n\t" \
    "s_cmp_eq_u32 s68, 0\n\t" \
    "s_cbranch_scc1 qk_F79_%=\n\t" \
    "s_sub_u32 s68, s68, 1\n\t" \
    "s_branch qk_G69_%=\n\t" \
    "qk_F79_%=:\n\t" \
    "v_mov_b32 v160, 6095104\n\t" \
    "v_mov_b32 v161, 1\n\t" \
    "global_store_dword v160, v161, s[36:37] sc1\n\t" \
    "s_waitcnt vmcnt(0)\n\t" \
    "s_branch qk_Gc72_%=\n\t" \
    "qk_Gbad271_%=:\n\t" \
    "global_load_dwordx4 v[44:47], v2, s[46:47] sc1\n\t" \
    "s_waitcnt vmcnt(0)\n\t" \
    "v_max3_u32 v160, v44, v45, v46\n\t" \
    "v_max_u32 v160, v160, v47\n\t" \
    "v_cmp_eq_u32 vcc, -1, v160\n\t" \
    "s_cbranch_vccz qk_Gre274_%=\n\t" \
    "s_cmp_eq_u32 s68, 0\n\t" \
    "s_cbranch_scc1 qk_F80_%=\n\t" \
    "s_sub_u32 s68, s68, 1\n\t" \
    "s_sleep 1\n\t" \
    "s_branch qk_Gbad271_%=\n\t" \
    "qk_F80_%=:\n\t" \
    "v_mov_b32 v160, 6095104\n\t" \
    "v_mov_b32 v161, 2\n\t" \
    "global_store_dword v160, v161, s[36:37] sc1\n\t" \
    "s_waitcnt vmcnt(0)\n\t" \
    "s_branch qk_Gre274_%=\n\t" \
    "qk_Gre274_%=:\n\t" \
    "v_mov_b32 v72, 0\n\t" \
    "v_mov_b32 v73, 0\n\t" \
    "v_mov_b32 v74, 0\n\t" \
    "v_mov_b32 v75, 0\n\t" \
    "v_mov_b32 v76, 0\n\t" \
    "v_mov_b32 v77, 0\n\t" \
    "v_mov_b32 v78, 0\n\t" \
    "v_mov_b32 v79, 0\n\t" \
    "v_mov_b32 v80, 0\n\t" \
    "v_mov_b32 v81, 0\n\t" \
    "v_mov_b32 v82, 0\n\t" \
    "v_mov_b32 v83, 0\n\t" \
    "v_mov_b32 v84, 0\n\t" \
    "v_mov_b32 v85, 0\n\t" \
    "v_mov_b32 v86, 0\n\t" \
    "v_mov_b32 v87, 0\n\t" \
    "v_smfmac_f32_16x16x64_bf16 v[72:75], v[36:39], a[32:39], v10\n\t" \
    "v_smfmac_f32_16x16x64_bf16 v[76:79], v[36:39], a[104:111], v10\n\t" \
    "v_smfmac_f32_16x16x64_bf16 v[80:83], v[36:39], a[176:183], v10\n\t" \
    "v_smfmac_f32_16x16x64_bf16 v[84:87], v[36:39], a[248:255], v10\n\t" \
    "v_smfmac_f32_16x16x64_bf16 v[72:75], v[40:43], a[40:47], v10\n\t" \
    "v_smfmac_f32_16x16x64_bf16 v[76:79], v[40:43], a[112:119], v10\n\t" \
    "v_smfmac_f32_16x16x64_bf16 v[80:83], v[40:43], a[184:191], v10\n\t" \
    "v_smfmac_f32_16x16x64_bf16 v[84:87], v[40:43], v[224:231], v10\n\t" \
    "v_smfmac_f32_16x16x64_bf16 v[72:75], v[44:47], a[64:71], v10\n\t" \
    "v_smfmac_f32_16x16x64_bf16 v[76:79], v[44:47], a[136:143], v10\n\t" \
    "v_smfmac_f32_16x16x64_bf16 v[80:83], v[44:47], a[208:215], v10\n\t" \
    "v_smfmac_f32_16x16x64_bf16 v[84:87], v[44:47], v[248:255], v10\n\t" \
    "s_branch qk_Gc273_%=\n\t" \
    "qk_last62_%=:\n\t" \
    "s_mov_b64 exec, s[58:59]\n\t" \
    "global_store_dwordx4 v0, v[140:143], s[40:41] offset:0 sc1\n\t" \
    "global_store_dwordx4 v0, v[144:147], s[40:41] offset:1024 sc1\n\t" \
    "global_store_dwordx4 v0, v[148:151], s[40:41] offset:2048 sc1\n\t" \
    "global_store_dwordx4 v0, v[152:155], s[40:41] offset:3072 sc1\n\t" \
    "s_mov_b64 exec, -1\n\t" \
    "s_endpgm\n\t" \
    "qk_loop17_%=:\n\t" \
    "v_mov_b32 v72, 0\n\t" \
    "v_mov_b32 v73, 0\n\t" \
    "v_mov_b32 v74, 0\n\t" \
    "v_mov_b32 v75, 0\n\t" \
    "v_mov_b32 v76, 0\n\t" \
    "v_mov_b32 v77, 0\n\t" \
    "v_mov_b32 v78, 0\n\t" \
    "v_mov_b32 v79, 0\n\t" \
    "v_mov_b32 v80, 0\n\t" \
    "v_mov_b32 v81, 0\n\t" \
    "v_mov_b32 v82, 0\n\t" \
    "v_mov_b32 v83, 0\n\t" \
    "v_mov_b32 v84, 0\n\t" \
    "v_mov_b32 v85, 0\n\t" \
    "v_mov_b32 v86, 0\n\t" \
    "v_mov_b32 v87, 0\n\t" \
    "v_add_u32 v31, 1, v31\n\t" \
    "s_add_u32 s69, s65, 0\n\t" \
    "s_bitcmp1_b32 s69, 0\n\t" \
    "s_cselect_b32 s70, s63, s62\n\t" \
    "s_bfe_u32 s69, s69, 0x20001\n\t" \
    "s_lshl_b32 s69, s69, 19\n\t" \
    "s_add_u32 s69, s69, s70\n\t" \
    "s_add_u32 s46, s36, s69\n\t" \
    "s_addc_u32 s47, s37, 0\n\t" \
    "qk_go84_%=:\n\t" \
    "s_sub_u32 s70, s79, s74\n\t" \
    "s_max_i32 s70, s70, 0\n\t" \
    "s_min_i32 s70, s70, 16000\n\t" \
    "s_lshl_b32 s70, s70, 1\n\t" \
    "s_mul_hi_u32 s70, s70, s77\n\t" \
    "s_add_u32 s70, s70, s73\n\t" \
    "s_sub_u32 s70, s70, 100\n\t" \
    "s_getpc_b64 s[30:31]\n\t" \
    "qk_Wp86_%=:\n\t" \
    "s_add_u32 s30, s30, qk_We87_%=-qk_Wp86_%=\n\t" \
    "s_addc_u32 s31, s31, 0\n\t" \
    "qk_W85_%=:\n\t" \
    "s_memtime s[56:57]\n\t" \
    "s_waitcnt lgkmcnt(0)\n\t" \
    "s_sub_u32 s69, s70, s56\n\t" \
    "s_cmp_gt_i32 s69, 111\n\t" \
    "s_cbranch_scc1 qk_W85_%=\n\t" \
    "s_max_i32 s69, s69, 0\n\t" \
    "s_lshr_b32 s69, s69, 1\n\t" \
    "s_and_b32 s69, s69, 0x3c\n\t" \
    "s_sub_u32 s30, s30, s69\n\t" \
    "s_subb_u32 s31, s31, 0\n\t" \
    "s_setpc_b64 s[30:31]\n\t" \
    ".p2align 6\n\t" \
    "s_nop 0\n\t" \
    "s_nop 0\n\t" \
    "s_nop 0\n\t" \
    "s_nop 1\n\t" \
    "s_nop 1\n\t" \
    "s_nop 1\n\t" \
    "s_nop 1\n\t" \
    "s_nop 1\n\t" \
    "s_nop 1\n\t" \
    "s_nop 1\n\t" \
    "s_nop 1\n\t" \
    "s_nop 1\n\t" \
    "s_nop 1\n\t" \
    "s_nop 1\n\t" \
    "s_nop 1\n\t" \
    "s_nop 1\n\t" \
    "qk_We87_%=:\n\t" \
    "qk_G88_%=:\n\t" \
    "global_load_dwordx4 v[36:39], v1, s[46:47] sc1\n\t" \
    "s_nop 9\n\t" \
    "global_load_dwordx4 v[40:43], v1, s[46:47] offset:1024 sc1\n\t" \
    "s_nop 9\n\t" \
    "global_load_dwordx4 v[44:47], v2, s[46:47] sc1\n\t" \
    "s_mul_hi_u32 s69, s84, s78\n\t" \
    "s_add_u32 s33, s74, s69\n\t" \
    "s_lshr_b32 s69, s90, 16\n\t" \
    "s_add_u32 s34, s33, s69\n\t" \
    "s_and_b32 s69, s90, 0xffff\n\t" \
    "s_sub_u32 s35, s80, s74\n\t" \
    "s_sub_u32 s35, s35, s69\n\t" \
    "s_add_u32 s99, s35, 1280\n\t" \
    "s_add_u32 s98, s34, 600\n\t" \
    "s_waitcnt vmcnt(2)\n\t" \
    "v_smfmac_f32_16x16x64_bf16 v[72:75], v[36:39], a[48:55], v10\n\t" \
    "ds_write_b128 v0, v[36:39] offset:6144\n\t" \
    "v_smfmac_f32_16x16x64_bf16 v[76:79], v[36:39], a[120:127], v10\n\t" \
    "v_max3_u32 v160, v36, v37, v38\n\t" \
    "v_smfmac_f32_16x16x64_bf16 v[80:83], v[36:39], a[192:199], v10\n\t" \
    "v_smfmac_f32_16x16x64_bf16 v[84:87], v[36:39], v[232:239], v10\n\t" \
    "s_waitcnt vmcnt(1)\n\t" \
    "ds_write_b128 v0, v[40:43] offset:7168\n\t" \
    "v_smfmac_f32_16x16x64_bf16 v[72:75], v[40:43], a[56:63], v10\n\t" \
    "v_max3_u32 v160, v160, v39, v40\n\t" \
    "v_max3_u32 v160, v160, v41, v42\n\t" \
    "v_smfmac_f32_16x16x64_bf16 v[76:79], v[40:43], a[128:135], v10\n\t" \
    "v_max_u32 v160, v160, v43\n\t" \
    "v_cmp_eq_u32 vcc, -1, v160\n\t" \
    "v_smfmac_f32_16x16x64_bf16 v[80:83], v[40:43], a[200:207], v10\n\t" \
    "v_smfmac_f32_16x16x64_bf16 v[84:87], v[40:43], v[240:247], v10\n\t" \
    "s_cbranch_vccnz qk_Gbad89_%=\n\t" \
    "qk_Gc91_%=:\n\t" \
    "s_waitcnt lgkmcnt(0)\n\t" \
    "s_barrier\n\t" \
    "s_memtime s[56:57]\n\t" \
    "ds_read_b128 v[48:51], v0 offset:0\n\t" \
    "ds_read_b128 v[52:55], v0 offset:1024\n\t" \
    "s_waitcnt vmcnt(0)\n\t" \
    "v_smfmac_f32_16x16x64_bf16 v[72:75], v[44:47], a[64:71], v10\n\t" \
    "ds_read_b128 v[56:59], v0 offset:2048\n\t" \
    "v_max3_u32 v160, v44, v45, v46\n\t" \
    "v_smfmac_f32_16x16x64_bf16 v[76:79], v[44:47], a[136:143], v10\n\t" \
    "ds_read_b128 v[60:63], v0 offset:3072\n\t" \
    "v_max_u32 v160, v160, v47\n\t" \
    "v_smfmac_f32_16x16x64_bf16 v[80:83], v[44:47], a[208:215], v10\n\t" \
    "ds_read_b128 v[64:67], v0 offset:4096\n\t" \
    "v_cmp_eq_u32 vcc, -1, v160\n\t" \
    "v_smfmac_f32_16x16x64_bf16 v[84:87], v[44:47], v[248:255], v10\n\t" \
    "ds_read_b128 v[68:71], v0 offset:5120\n\t" \
    "s_cbranch_vccnz qk_Gbad290_%=\n\t" \
    "qk_Gc292_%=:\n\t" \
    "s_sub_u32 s69, s56, s73\n\t" \
    "s_mul_hi_u32 s69, s69, s78\n\t" \
    "s_cmp_gt_i32 s69, s35\n\t" \
    "s_waitcnt lgkmcnt(5)\n\t" \
    "v_smfmac_f32_16x16x64_bf16 v[72:75], v[48:51], a[0:7], v10\n\t" \
    "s_cselect_b32 s70, s34, s33\n\t" \
    "s_cmp_gt_i32 s69, s99\n\t" \
    "s_cselect_b32 s70, s98, s70\n\t" \
    "s_waitcnt lgkmcnt(4)\n\t" \
    "v_smfmac_f32_16x16x64_bf16 v[72:75], v[52:55], a[8:15], v10\n\t" \
    "s_add_u32 s70, s69, s70\n\t" \
    "v_and_b32_e64 v162, -2, s70\n\t" \
    "s_waitcnt lgkmcnt(3)\n\t" \
    "v_smfmac_f32_16x16x64_bf16 v[72:75], v[56:59], a[16:23], v10\n\t" \
    "s_mov_b64 exec, 0xfffff\n\t" \
    "global_store_dword v21, v162, s[46:47] sc1\n\t" \
    "s_mov_b64 exec, -1\n\t" \
    "s_waitcnt lgkmcnt(2)\n\t" \
    "v_smfmac_f32_16x16x64_bf16 v[72:75], v[60:63], a[24:31], v10\n\t" \
    "s_waitcnt lgkmcnt(1)\n\t" \
    "v_smfmac_f32_16x16x64_bf16 v[72:75], v[64:67], a[32:39], v10\n\t" \
    "s_waitcnt lgkmcnt(0)\n\t" \
    "v_smfmac_f32_16x16x64_bf16 v[72:75], v[68:71], a[40:47], v10\n\t" \
    "v_smfmac_f32_16x16x64_bf16 v[76:79], v[48:51], a[72:79], v10\n\t" \
    "s_nop 1\n\t" \
    "s_nop 0\n\t" \
    "v_smfmac_f32_16x16x64_bf16 v[76:79], v[52:55], a[80:87], v10\n\t" \
    "s_nop 1\n\t" \
    "s_nop 0\n\t" \
    "v_smfmac_f32_16x16x64_bf16 v[76:79], v[56:59], a[88:95], v10\n\t" \
    "v_add_f32 v72, v72, v74\n\t" \
    "v_smfmac_f32_16x16x64_bf16 v[76:79], v[60:63], a[96:103], v10\n\t" \
    "v_add_f32 v73, v73, v75\n\t" \
    "v_smfmac_f32_16x16x64_bf16 v[76:79], v[64:67], a[104:111], v10\n\t" \
    "v_cvt_pk_bf16_f32 v156, v72, v73\n\t" \
    "v_smfmac_f32_16x16x64_bf16 v[76:79], v[68:71], a[112:119], v10\n\t" \
    "ds_write_b32 v12, v156 offset:0\n\t" \
    "v_smfmac_f32_16x16x64_bf16 v[80:83], v[48:51], a[144:151], v10\n\t" \
    "s_nop 1\n\t" \
    "s_nop 0\n\t" \
    "v_smfmac_f32_16x16x64_bf16 v[80:83], v[52:55], a[152:159], v10\n\t" \
    "s_nop 1\n\t" \
    "s_nop 0\n\t" \
    "v_smfmac_f32_16x16x64_bf16 v[80:83], v[56:59], a[160:167], v10\n\t" \
    "v_add_f32 v76, v76, v78\n\t" \
    "v_smfmac_f32_16x16x64_bf16 v[80:83], v[60:63], a[168:175], v10\n\t" \
    "v_add_f32 v77, v77, v79\n\t" \
    "v_smfmac_f32_16x16x64_bf16 v[80:83], v[64:67], a[176:183], v10\n\t" \
    "v_cvt_pk_bf16_f32 v157, v76, v77\n\t" \
    "v_smfmac_f32_16x16x64_bf16 v[80:83], v[68:71], a[184:191], v10\n\t" \
    "ds_write_b32 v12, v157 offset:64\n\t" \
    "v_smfmac_f32_16x16x64_bf16 v[84:87], v[48:51], a[216:223], v10\n\t" \
    "s_nop 1\n\t" \
    "s_nop 0\n\t" \
    "v_smfmac_f32_16x16x64_bf16 v[84:87], v[52:55], a[224:231], v10\n\t" \
    "s_nop 1\n\t" \
    "s_nop 0\n\t" \
    "v_smfmac_f32_16x16x64_bf16 v[84:87], v[56:59], a[232:239], v10\n\t" \
    "v_add_f32 v80, v80, v82\n\t" \
    "v_smfmac_f32_16x16x64_bf16 v[84:87], v[60:63], a[240:247], v10\n\t" \
    "v_add_f32 v81, v81, v83\n\t" \
    "v_smfmac_f32_16x16x64_bf16 v[84:87], v[64:67], a[248:255], v10\n\t" \
    "v_cvt_pk_bf16_f32 v158, v80, v81\n\t" \
    "v_smfmac_f32_16x16x64_bf16 v[84:87], v[68:71], v[224:231], v10\n\t" \
    "ds_write_b32 v16, v158 offset:0\n\t" \
    "s_nop 6\n\t" \
    "v_pk_add_f32 v[84:85], v[84:85], v[86:87]\n\t" \
    "v_cvt_pk_bf16_f32 v159, v84, v85\n\t" \
    "ds_write_b32 v16, v159 offset:256\n\t" \
    "s_waitcnt lgkmcnt(0)\n\t" \
    "s_barrier\n\t" \
    "s_cmp_eq_u32 s65, 63\n\t" \
    "s_cbranch_scc1 qk_last81_%=\n\t" \
    "s_sub_u32 s70, s80, s74\n\t" \
    "s_max_i32 s70, s70, 0\n\t" \
    "s_min_i32 s70, s70, 16000\n\t" \
    "s_lshl_b32 s70, s70, 1\n\t" \
    "s_mul_hi_u32 s70, s70, s77\n\t" \
    "s_add_u32 s70, s70, s73\n\t" \
    "qk_W94_%=:\n\t" \
    "s_memtime s[56:57]\n\t" \
    "s_waitcnt lgkmcnt(0)\n\t" \
    "s_sub_u32 s69, s70, s56\n\t" \
    "s_cmp_gt_i32 s69, 24\n\t" \
    "s_cbranch_scc1 qk_W94_%=\n\t" \
    "global_load_dword v160, v20, s[46:47] sc1\n\t" \
    "s_mov_b32 s71, 3\n\t" \
    "qk_E98_%=:\n\t" \
    "s_memtime s[54:55]\n\t" \
    "s_nop 0\n\t" \
    "s_memrealtime s[4:5]\n\t" \
    "s_nop 0\n\t" \
    "s_memrealtime s[6:7]\n\t" \
    "s_nop 0\n\t" \
    "s_memrealtime s[8:9]\n\t" \
    "s_nop 0\n\t" \
    "s_memrealtime s[10:11]\n\t" \
    "s_nop 0\n\t" \
    "s_memrealtime s[12:13]\n\t" \
    "s_nop 0\n\t" \
    "s_memrealtime s[14:15]\n\t" \
    "s_nop 0\n\t" \
    "s_memrealtime s[16:17]\n\t" \
    "s_nop 0\n\t" \
    "s_memrealtime s[18:19]\n\t" \
    "s_nop 0\n\t" \
    "s_memrealtime s[20:21]\n\t" \
    "s_nop 0\n\t" \
    "s_memrealtime s[22:23]\n\t" \
    "s_nop 0\n\t" \
    "s_memrealtime s[24:25]\n\t" \
    "s_nop 0\n\t" \
    "s_memrealtime s[26:27]\n\t" \
    "s_nop 0\n\t" \
    "s_memrealtime s[28:29]\n\t" \
    "s_nop 0\n\t" \
    "s_memtime s[56:57]\n\t" \
    "s_mov_b32 s70, 1\n\t" \
    "s_sub_u32 s71, s71, 1\n\t" \
    "s_cselect_b32 s69, 0, -1\n\t" \
    "s_waitcnt lgkmcnt(0)\n\t" \
    "s_sub_u32 s54, s56, s54\n\t" \
    "s_and_b32 s54, s54, s69\n\t" \
    "s_cmp_gt_u32 s54, 124\n\t" \
    "s_cbranch_scc1 qk_E98_%=\n\t" \
    "s_cmp_eq_u32 s4, s28\n\t" \
    "s_addc_u32 s70, s70, 0\n\t" \
    "s_cmp_eq_u32 s6, s28\n\t" \
    "s_addc_u32 s70, s70, 0\n\t" \
    "s_cmp_eq_u32 s8, s28\n\t" \
    "s_addc_u32 s70, s70, 0\n\t" \
    "s_cmp_eq_u32 s10, s28\n\t" \
    "s_addc_u32 s70, s70, 0\n\t" \
    "s_cmp_eq_u32 s12, s28\n\t" \
    "s_addc_u32 s70, s70, 0\n\t" \
    "s_cmp_eq_u32 s14, s28\n\t" \
    "s_addc_u32 s70, s70, 0\n\t" \
    "s_cmp_eq_u32 s16, s28\n\t" \
    "s_addc_u32 s70, s70, 0\n\t" \
    "s_cmp_eq_u32 s18, s28\n\t" \
    "s_addc_u32 s70, s70, 0\n\t" \
    "s_cmp_eq_u32 s20, s28\n\t" \
    "s_addc_u32 s70, s70, 0\n\t" \
    "s_cmp_eq_u32 s22, s28\n\t" \
    "s_addc_u32 s70, s70, 0\n\t" \
    "s_cmp_eq_u32 s24, s28\n\t" \
    "s_addc_u32 s70, s70, 0\n\t" \
    "s_cmp_eq_u32 s26, s28\n\t" \
    "s_addc_u32 s70, s70, 0\n\t" \
    "s_mov_b32 s75, s73\n\t" \
    "s_mov_b32 s76, s74\n\t" \
    "s_lshl_b32 s70, s70, 3\n\t" \
    "s_sub_u32 s73, s56, s70\n\t" \
    "s_lshl_b32 s74, s28, 4\n\t" \
    "s_sub_u32 s69, s73, s75\n\t" \
    "s_sub_u32 s70, s74, s76\n\t" \
    "s_sub_u32 s71, s70, 1600\n\t" \
    "s_cmp_lt_u32 s71, 400000\n\t" \
    "s_cbranch_scc0 qk_K100_%=\n\t" \
    "v_cvt_f32_u32 v161, s69\n\t" \
    "v_cvt_f32_u32 v162, s70\n\t" \
    "v_rcp_f32 v163, v161\n\t" \
    "v_rcp_f32 v164, v162\n\t" \
    "v_mul_f32 v163, v163, v162\n\t" \
    "v_mul_f32 v164, v164, v161\n\t" \
    "v_mul_f32 v163, 0x4f800000, v163\n\t" \
    "v_mul_f32 v164, 0x4f000000, v164\n\t" \
    "v_cvt_u32_f32 v163, v163\n\t" \
    "v_cvt_u32_f32 v164, v164\n\t" \
    "v_readfirstlane_b32 s69, v163\n\t" \
    "v_readfirstlane_b32 s70, v164\n\t" \
    "s_nop 1\n\t" \
    "s_sub_u32 s69, s69, s78\n\t" \
    "s_ashr_i32 s69, s69, 2\n\t" \
    "s_add_u32 s78, s78, s69\n\t" \
    "s_sub_u32 s70, s70, s77\n\t" \
    "s_ashr_i32 s70, s70, 2\n\t" \
    "s_add_u32 s77, s77, s70\n\t" \
    "qk_K100_%=:\n\t" \
    "s_waitcnt vmcnt(0)\n\t" \
    "v_cmp_eq_u32 vcc, -1, v160\n\t" \
    "s_cbranch_vccz qk_Hok96_%=\n\t" \
    "qk_H95_%=:\n\t" \
    "global_load_dword v160, v20, s[46:47] sc1\n\t" \
    "s_waitcnt vmcnt(0)\n\t" \
    "v_cmp_eq_u32 vcc, -1, v160\n\t" \
    "s_cbranch_vccz qk_Hc97_%=\n\t" \
    "s_cmp_eq_u32 s68, 0\n\t" \
    "s_cbranch_scc1 qk_F101_%=\n\t" \
    "s_sub_u32 s68, s68, 1\n\t" \
    "s_sleep 1\n\t" \
    "s_branch qk_H95_%=\n\t" \
    "qk_F101_%=:\n\t" \
    "v_mov_b32 v161, 6095104\n\t" \
    "v_mov_b32 v162, 5\n\t" \
    "global_store_dword v161, v162, s[36:37] sc1\n\t" \
    "s_waitcnt vmcnt(0)\n\t" \
    "s_branch qk_Hc97_%=\n\t" \
    "qk_Hc97_%=:\n\t" \
    "qk_Hok96_%=:\n\t" \
    "v_subrev_u32 v160, s80, v160\n\t" \
    "v_and_b32 v20, -5, v20\n\t" \
    "s_nop 0\n\t" \
    "v_max_i32_dpp v160, v160, v160 row_ror:1 row_mask:0xf bank_mask:0xf\n\t" \
    "s_nop 1\n\t" \
    "v_max_i32_dpp v160, v160, v160 row_ror:2 row_mask:0xf bank_mask:0xf\n\t" \
    "s_nop 1\n\t" \
    "v_max_i32_dpp v160, v160, v160 row_ror:4 row_mask:0xf bank_mask:0xf\n\t" \
    "s_nop 1\n\t" \
    "v_max_i32_dpp v160, v160, v160 row_ror:8 row_mask:0xf bank_mask:0xf\n\t" \
    "s_nop 0\n\t" \
    "v_readlane_b32 s69, v160, 0\n\t" \
    "v_readlane_b32 s71, v160, 16\n\t" \
    "v_readlane_b32 s70, v160, 32\n\t" \
    "v_readlane_b32 s81, v160, 48\n\t" \
    "s_max_i32 s69, s69, s71\n\t" \
    "s_nop 0\n\t" \
    "s_max_i32 s71, s70, s81\n\t" \
    "s_max_i32 s69, s69, s71\n\t" \
    "s_add_u32 s81, s69, s80\n\t" \
    "v_mov_b32 v72, 0\n\t" \
    "v_mov_b32 v73, 0\n\t" \
    "v_mov_b32 v74, 0\n\t" \
    "v_mov_b32 v75, 0\n\t" \
    "v_mov_b32 v76, 0\n\t" \
    "v_mov_b32 v77, 0\n\t" \
    "v_mov_b32 v78, 0\n\t" \
    "v_mov_b32 v79, 0\n\t" \
    "v_mov_b32 v80, 0\n\t" \
    "v_mov_b32 v81, 0\n\t" \
    "v_mov_b32 v82, 0\n\t" \
    "v_mov_b32 v83, 0\n\t" \
    "v_mov_b32 v84, 0\n\t" \
    "v_mov_b32 v85, 0\n\t" \
    "v_mov_b32 v86, 0\n\t" \
    "v_mov_b32 v87, 0\n\t" \
    "s_cmp_lg_u32 s65, 0\n\t" \
    "s_cbranch_scc1 qk_Oc104_%=\n\t" \
    "s_add_u32 s79, s81, 240\n\t" \
    "s_sub_u32 s70, s79, s74\n\t" \
    "s_max_i32 s70, s70, 0\n\t" \
    "s_min_i32 s70, s70, 16000\n\t" \
    "s_lshl_b32 s70, s70, 1\n\t" \
    "s_mul_hi_u32 s70, s70, s77\n\t" \
    "s_add_u32 s70, s70, s73\n\t" \
    "qk_W105_%=:\n\t" \
    "s_memtime s[56:57]\n\t" \
    "s_waitcnt lgkmcnt(0)\n\t" \
    "s_sub_u32 s69, s70, s56\n\t" \
    "s_cmp_gt_i32 s69, 24\n\t" \
    "s_cbranch_scc1 qk_W105_%=\n\t" \
    "global_load_dword v160, v35, s[46:47] sc1\n\t" \
    "s_waitcnt vmcnt(0)\n\t" \
    "v_cmp_eq_u32 vcc, -1, v160\n\t" \
    "s_cbranch_vccz qk_Ook103_%=\n\t" \
    "qk_O102_%=:\n\t" \
    "global_load_dword v160, v35, s[46:47] offset:1536 sc1\n\t" \
    "s_waitcnt vmcnt(0)\n\t" \
    "v_cmp_eq_u32 vcc, -1, v160\n\t" \
    "s_cbranch_vccz qk_Ook103_%=\n\t" \
    "s_cmp_eq_u32 s68, 0\n\t" \
    "s_cbranch_scc1 qk_F106_%=\n\t" \
    "s_sub_u32 s68, s68, 1\n\t" \
    "s_sleep 3\n\t" \
    "s_branch qk_O102_%=\n\t" \
    "qk_F106_%=:\n\t" \
    "v_mov_b32 v161, 6095104\n\t" \
    "v_mov_b32 v162, 16\n\t" \
    "global_store_dword v161, v162, s[36:37] sc1\n\t" \
    "s_waitcnt vmcnt(0)\n\t" \
    "s_branch qk_Ook103_%=\n\t" \
    "qk_Ook103_%=:\n\t" \
    "v_subrev_u32 v160, s80, v160\n\t" \
    "s_nop 1\n\t" \
    "v_max_i32_dpp v160, v160, v160 row_ror:1 row_mask:0xf bank_mask:0xf\n\t" \
    "s_nop 1\n\t" \
    "v_max_i32_dpp v160, v160, v160 row_ror:2 row_mask:0xf bank_mask:0xf\n\t" \
    "s_nop 1\n\t" \
    "v_max_i32_dpp v160, v160, v160 row_ror:4 row_mask:0xf bank_mask:0xf\n\t" \
    "s_nop 1\n\t" \
    "v_max_i32_dpp v160, v160, v160 row_ror:8 row_mask:0xf bank_mask:0xf\n\t" \
    "s_nop 0\n\t" \
    "v_readlane_b32 s69, v160, 0\n\t" \
    "v_readlane_b32 s71, v160, 16\n\t" \
    "v_readlane_b32 s70, v160, 32\n\t" \
    "v_readlane_b32 s81, v160, 48\n\t" \
    "s_max_i32 s69, s69, s71\n\t" \
    "s_nop 0\n\t" \
    "s_max_i32 s71, s70, s81\n\t" \
    "s_max_i32 s69, s69, s71\n\t" \
    "s_add_u32 s81, s69, s80\n\t" \
    "qk_Oc104_%=:\n\t" \
    "v_mov_b32 v160, s81\n\t" \
    "ds_write_b32 v11, v160 offset:20480\n\t" \
    "s_add_u32 s65, s65, 1\n\t" \
    "v_add_u32 v31, 1, v31\n\t" \
    "s_add_u32 s69, s65, 0\n\t" \
    "s_bitcmp1_b32 s69, 0\n\t" \
    "s_cselect_b32 s70, s63, s62\n\t" \
    "s_bfe_u32 s69, s69, 0x20001\n\t" \
    "s_lshl_b32 s69, s69, 19\n\t" \
    "s_add_u32 s69, s69, s70\n\t" \
    "s_add_u32 s46, s36, s69\n\t" \
    "s_addc_u32 s47, s37, 0\n\t" \
    "s_waitcnt lgkmcnt(0)\n\t" \
    "s_barrier\n\t" \
    "ds_read_b32 v160, v11 offset:20480\n\t" \
    "s_waitcnt lgkmcnt(0)\n\t" \
    "v_readfirstlane_b32 s81, v160\n\t" \
    "s_nop 1\n\t" \
    "s_mul_hi_u32 s69, s85, s78\n\t" \
    "s_add_u32 s79, s81, s69\n\t" \
    "s_add_u32 s79, s79, s87\n\t" \
    "s_cmp_ge_u32 s65, 37\n\t" \
    "s_cselect_b32 s69, 28, 0\n\t" \
    "s_sub_u32 s79, s79, s69\n\t" \
    "s_add_u32 s80, s79, s88\n\t" \
    "s_branch qk_go84_%=\n\t" \
    "qk_Gbad89_%=:\n\t" \
    "s_waitcnt vmcnt(0) lgkmcnt(0)\n\t" \
    "s_nop 7\n\t" \
    "v_mov_b32 v72, 0\n\t" \
    "v_mov_b32 v73, 0\n\t" \
    "v_mov_b32 v74, 0\n\t" \
    "v_mov_b32 v75, 0\n\t" \
    "v_mov_b32 v76, 0\n\t" \
    "v_mov_b32 v77, 0\n\t" \
    "v_mov_b32 v78, 0\n\t" \
    "v_mov_b32 v79, 0\n\t" \
    "v_mov_b32 v80, 0\n\t" \
    "v_mov_b32 v81, 0\n\t" \
    "v_mov_b32 v82, 0\n\t" \
    "v_mov_b32 v83, 0\n\t" \
    "v_mov_b32 v84, 0\n\t" \
    "v_mov_b32 v85, 0\n\t" \
    "v_mov_b32 v86, 0\n\t" \
    "v_mov_b32 v87, 0\n\t" \
    "s_cmp_eq_u32 s68, 0\n\t" \
    "s_cbranch_scc1 qk_F107_%=\n\t" \
    "s_sub_u32 s68, s68, 1\n\t" \
    "s_branch qk_G88_%=\n\t" \
    "qk_F107_%=:\n\t" \
    "v_mov_b32 v160, 6095104\n\t" \
    "v_mov_b32 v161, 1\n\t" \
    "global_store_dword v160, v161, s[36:37] sc1\n\t" \
    "s_waitcnt vmcnt(0)\n\t" \
    "s_branch qk_Gc91_%=\n\t" \
    "qk_Gbad290_%=:\n\t" \
    "global_load_dwordx4 v[44:47], v2, s[46:47] sc1\n\t" \
    "s_waitcnt vmcnt(0)\n\t" \
    "v_max3_u32 v160, v44, v45, v46\n\t" \
    "v_max_u32 v160, v160, v47\n\t" \
    "v_cmp_eq_u32 vcc, -1, v160\n\t" \
    "s_cbranch_vccz qk_Gre293_%=\n\t" \
    "s_cmp_eq_u32 s68, 0\n\t" \
    "s_cbranch_scc1 qk_F108_%=\n\t" \
    "s_sub_u32 s68, s68, 1\n\t" \
    "s_sleep 1\n\t" \
    "s_branch qk_Gbad290_%=\n\t" \
    "qk_F108_%=:\n\t" \
    "v_mov_b32 v160, 6095104\n\t" \
    "v_mov_b32 v161, 2\n\t" \
    "global_store_dword v160, v161, s[36:37] sc1\n\t" \
    "s_waitcnt vmcnt(0)\n\t" \
    "s_branch qk_Gre293_%=\n\t" \
    "qk_Gre293_%=:\n\t" \
    "v_mov_b32 v72, 0\n\t" \
    "v_mov_b32 v73, 0\n\t" \
    "v_mov_b32 v74, 0\n\t" \
    "v_mov_b32 v75, 0\n\t" \
    "v_mov_b32 v76, 0\n\t" \
    "v_mov_b32 v77, 0\n\t" \
    "v_mov_b32 v78, 0\n\t" \
    "v_mov_b32 v79, 0\n\t" \
    "v_mov_b32 v80, 0\n\t" \
    "v_mov_b32 v81, 0\n\t" \
    "v_mov_b32 v82, 0\n\t" \
    "v_mov_b32 v83, 0\n\t" \
    "v_mov_b32 v84, 0\n\t" \
    "v_mov_b32 v85, 0\n\t" \
    "v_mov_b32 v86, 0\n\t" \
    "v_mov_b32 v87, 0\n\t" \
    "v_smfmac_f32_16x16x64_bf16 v[72:75], v[36:39], a[48:55], v10\n\t" \
    "v_smfmac_f32_16x16x64_bf16 v[76:79], v[36:39], a[120:127], v10\n\t" \
    "v_smfmac_f32_16x16x64_bf16 v[80:83], v[36:39], a[192:199], v10\n\t" \
    "v_smfmac_f32_16x16x64_bf16 v[84:87], v[36:39], v[232:239], v10\n\t" \
    "v_smfmac_f32_16x16x64_bf16 v[72:75], v[40:43], a[56:63], v10\n\t" \
    "v_smfmac_f32_16x16x64_bf16 v[76:79], v[40:43], a[128:135], v10\n\t" \
    "v_smfmac_f32_16x16x64_bf16 v[80:83], v[40:43], a[200:207], v10\n\t" \
    "v_smfmac_f32_16x16x64_bf16 v[84:87], v[40:43], v[240:247], v10\n\t" \
    "v_smfmac_f32_16x16x64_bf16 v[72:75], v[44:47], a[64:71], v10\n\t" \
    "v_smfmac_f32_16x16x64_bf16 v[76:79], v[44:47], a[136:143], v10\n\t" \
    "v_smfmac_f32_16x16x64_bf16 v[80:83], v[44:47], a[208:215], v10\n\t" \
    "v_smfmac_f32_16x16x64_bf16 v[84:87], v[44:47], v[248:255], v10\n\t" \
    "s_waitcnt lgkmcnt(0)\n\t" \
    "s_memtime s[56:57]\n\t" \
    "s_waitcnt lgkmcnt(0)\n\t" \
    "s_branch qk_Gc292_%=\n\t" \
    "qk_last81_%=:\n\t" \
    "s_endpgm\n\t" \
    "vv_begin_%=:\n\t" \
    "v_mbcnt_lo_u32_b32 v223, -1, 0\n\t" \
    "v_mbcnt_hi_u32_b32 v223, -1, v223\n\t" \
    "s_mov_b32 s68, 262144\n\t" \
    "s_mov_b32 s72, 0\n\t" \
    "s_mov_b32 s83, -1\n\t" \
    "s_mov_b32 s65, 0\n\t" \
    "v_and_b32 v180, 3, v223\n\t" \
    "v_lshlrev_b32 v202, 3, v180\n\t" \
    "v_lshrrev_b32 v181, 5, v223\n\t" \
    "v_lshl_add_u32 v202, v181, 12, v202\n\t" \
    "v_bfe_u32 v182, v223, 2, 2\n\t" \
    "s_mov_b32 s70, 288\n\t" \
    "v_mad_u32_u24 v202, v182, s70, v202\n\t" \
    "v_bfe_u32 v183, v223, 4, 1\n\t" \
    "s_mov_b32 s70, 2176\n\t" \
    "v_mad_u32_u24 v202, v183, s70, v202\n\t" \
    "s_lshl_b32 s69, s66, 6\n\t" \
    "v_xor_b32 v202, s69, v202\n\t" \
    "v_xor_b32 v203, 32, v202\n\t" \
    "s_mul_i32 s69, s66, 9216\n\t" \
    "v_and_b32 v184, 1, v223\n\t" \
    "v_lshl_add_u32 v201, v184, 4, s69\n\t" \
    "v_lshrrev_b32 v185, 3, v223\n\t" \
    "s_mov_b32 s70, 1152\n\t" \
    "v_mad_u32_u24 v201, v185, s70, v201\n\t" \
    "v_bfe_u32 v186, v223, 1, 2\n\t" \
    "v_lshlrev_b32 v204, 5, v186\n\t" \
    "v_lshrrev_b32 v187, 4, v223\n\t" \
    "v_lshlrev_b32 v205, 5, v187\n\t" \
    "v_xor_b32 v204, v204, v205\n\t" \
    "v_add_u32 v201, v201, v204\n\t" \
    "s_mul_i32 s96, s66, 9216\n\t" \
    "v_lshlrev_b32 v207, 3, v180\n\t" \
    "v_bfe_u32 v188, v223, 2, 1\n\t" \
    "v_lshl_add_u32 v207, v188, 7, v207\n\t" \
    "v_bfe_u32 v189, v223, 3, 2\n\t" \
    "s_mov_b32 s70, 288\n\t" \
    "v_mad_u32_u24 v207, v189, s70, v207\n\t" \
    "s_mov_b32 s70, 9216\n\t" \
    "v_mad_u32_u24 v207, v181, s70, v207\n\t" \
    "s_mul_i32 s69, s66, 9\n\t" \
    "s_add_u32 s69, s69, 0\n\t" \
    "s_and_b32 s70, s69, 3\n\t" \
    "s_lshl_b32 s70, s70, 5\n\t" \
    "s_lshr_b32 s69, s69, 2\n\t" \
    "s_lshl_b32 s69, s69, 10\n\t" \
    "v_xor_b32 v204, s70, v207\n\t" \
    "v_add_u32 v204, s69, v204\n\t" \
    "s_mul_i32 s69, s66, 9\n\t" \
    "s_add_u32 s69, s69, 1\n\t" \
    "s_and_b32 s70, s69, 3\n\t" \
    "s_lshl_b32 s70, s70, 5\n\t" \
    "s_lshr_b32 s69, s69, 2\n\t" \
    "s_lshl_b32 s69, s69, 10\n\t" \
    "v_xor_b32 v205, s70, v207\n\t" \
    "v_add_u32 v205, s69, v205\n\t" \
    "s_mul_i32 s69, s66, 9\n\t" \
    "s_add_u32 s69, s69, 2\n\t" \
    "s_and_b32 s70, s69, 3\n\t" \
    "s_lshl_b32 s70, s70, 5\n\t" \
    "s_lshr_b32 s69, s69, 2\n\t" \
    "s_lshl_b32 s69, s69, 10\n\t" \
    "v_xor_b32 v206, s70, v207\n\t" \
    "v_add_u32 v206, s69, v206\n\t" \
    "s_mul_i32 s69, s66, 9\n\t" \
    "s_add_u32 s69, s69, 3\n\t" \
    "s_and_b32 s70, s69, 3\n\t" \
    "s_lshl_b32 s70, s70, 5\n\t" \
    "s_lshr_b32 s69, s69, 2\n\t" \
    "s_lshl_b32 s69, s69, 10\n\t" \
    "v_xor_b32 v207, s70, v207\n\t" \
    "v_add_u32 v207, s69, v207\n\t" \
    "s_waitcnt vmcnt(12) lgkmcnt(0)\n\t" \
    "s_barrier\n\t" \
    "s_add_u32 m0, s82, 65536\n\t" \
    "s_nop 0\n\t" \
    "global_load_lds_dwordx4 v200, s[42:43]\n\t" \
    "v_add_u32 v200, 131072, v200\n\t" \
    "s_add_u32 m0, s82, 69632\n\t" \
    "s_nop 0\n\t" \
    "global_load_lds_dwordx4 v200, s[42:43]\n\t" \
    "v_add_u32 v200, 131072, v200\n\t" \
    "s_add_u32 m0, s82, 73728\n\t" \
    "s_nop 0\n\t" \
    "global_load_lds_dwordx4 v200, s[42:43]\n\t" \
    "v_add_u32 v200, 131072, v200\n\t" \
    "s_add_u32 m0, s82, 77824\n\t" \
    "s_nop 0\n\t" \
    "global_load_lds_dwordx4 v200, s[42:43]\n\t" \
    "v_add_u32 v200, 131072, v200\n\t" \
    "ds_read_b64_tr_b16 a[0:1], v202 offset:36864\n\t" \
    "ds_read_b64_tr_b16 a[2:3], v202 offset:37888\n\t" \
    "ds_read_b64_tr_b16 a[4:5], v202 offset:45056\n\t" \
    "ds_read_b64_tr_b16 a[6:7], v202 offset:46080\n\t" \
    "ds_read_b64_tr_b16 a[72:73], v203 offset:36864\n\t" \
    "ds_read_b64_tr_b16 a[74:75], v203 offset:37888\n\t" \
    "ds_read_b64_tr_b16 a[76:77], v203 offset:45056\n\t" \
    "ds_read_b64_tr_b16 a[78:79], v203 offset:46080\n\t" \
    "v_lshlrev_b32 v0, 4, v223\n\t" \
    "s_mul_i32 s69, s66, 2048\n\t" \
    "s_bfe_u32 s71, s67, 0x20003\n\t" \
    "s_mul_i32 s71, s71, 65536\n\t" \
    "s_add_u32 s69, s69, s71\n\t" \
    "s_add_u32 s69, s69, 32768\n\t" \
    "v_lshl_add_u32 v1, v223, 4, s69\n\t" \
    "s_bfe_u32 s71, s67, 0x20003\n\t" \
    "s_mul_i32 s69, s71, 65536\n\t" \
    "s_add_u32 s69, s69, 53248\n\t" \
    "v_lshl_add_u32 v2, v223, 4, s69\n\t" \
    "s_bfe_u32 s71, s67, 0x20003\n\t" \
    "s_mul_i32 s69, s71, 65536\n\t" \
    "s_add_u32 s69, s69, 32768\n\t" \
    "v_lshl_add_u32 v34, v223, 4, s69\n\t" \
    "s_mov_b32 s69, 17476\n\t" \
    "v_bfe_u32 v190, v223, 1, 1\n\t" \
    "v_mov_b32 v10, s69\n\t" \
    "s_mov_b32 s70, 43690\n\t" \
    "v_mad_u32_u24 v10, v190, s70, v10\n\t" \
    "v_mov_b32 v11, 0\n\t" \
    "s_waitcnt vmcnt(12) lgkmcnt(0)\n\t" \
    "s_barrier\n\t" \
    "s_add_u32 m0, s82, 81920\n\t" \
    "s_nop 0\n\t" \
    "global_load_lds_dwordx4 v200, s[42:43]\n\t" \
    "v_add_u32 v200, 131072, v200\n\t" \
    "s_add_u32 m0, s82, 86016\n\t" \
    "s_nop 0\n\t" \
    "global_load_lds_dwordx4 v200, s[42:43]\n\t" \
    "v_add_u32 v200, 131072, v200\n\t" \
    "s_add_u32 m0, s82, 90112\n\t" \
    "s_nop 0\n\t" \
    "global_load_lds_dwordx4 v200, s[42:43]\n\t" \
    "v_add_u32 v200, 131072, v200\n\t" \
    "s_add_u32 m0, s82, 94208\n\t" \
    "s_nop 0\n\t" \
    "global_load_lds_dwordx4 v200, s[42:43]\n\t" \
    "v_add_u32 v200, 131072, v200\n\t" \
    "ds_read_b64_tr_b16 a[8:9], v202 offset:53248\n\t" \
    "ds_read_b64_tr_b16 a[10:11], v202 offset:54272\n\t" \
    "ds_read_b64_tr_b16 a[12:13], v202 offset:61440\n\t" \
    "ds_read_b64_tr_b16 a[14:15], v202 offset:62464\n\t" \
    "ds_read_b64_tr_b16 a[80:81], v203 offset:53248\n\t" \
    "ds_read_b64_tr_b16 a[82:83], v203 offset:54272\n\t" \
    "ds_read_b64_tr_b16 a[84:85], v203 offset:61440\n\t" \
    "ds_read_b64_tr_b16 a[86:87], v203 offset:62464\n\t" \
    "s_mul_i32 s69, s66, 512\n\t" \
    "s_add_u32 s69, s69, 16384\n\t" \
    "v_and_b32 v191, 13, v223\n\t" \
    "v_lshl_add_u32 v12, v191, 5, s69\n\t" \
    "v_lshl_add_u32 v12, v190, 2, v12\n\t" \
    "v_lshl_add_u32 v12, v187, 3, v12\n\t" \
    "s_mov_b32 s69, 16384\n\t" \
    "v_and_b32 v192, 15, v223\n\t" \
    "v_lshl_add_u32 v13, v192, 3, s69\n\t" \
    "v_lshl_add_u32 v13, v187, 8, v13\n\t" \
    "s_bfe_u32 s71, s67, 0x20003\n\t" \
    "s_mul_i32 s69, s71, 65536\n\t" \
    "s_add_u32 s69, s69, 266240\n\t" \
    "v_and_b32 v193, 31, v223\n\t" \
    "v_lshl_add_u32 v20, v193, 5, s69\n\t" \
    "v_lshl_add_u32 v20, v181, 2, v20\n\t" \
    "s_mul_i32 s69, s67, 32\n\t" \
    "s_add_u32 s69, s69, 262144\n\t" \
    "v_lshl_add_u32 v21, v180, 16, s69\n\t" \
    "v_bfe_u32 v194, v223, 2, 3\n\t" \
    "v_lshlrev_b32 v199, 10, v194\n\t" \
    "v_min_u32 v199, 4096, v199\n\t" \
    "v_add_u32 v21, v199, v21\n\t" \
    "s_bfe_u32 s71, s67, 0x20003\n\t" \
    "s_mul_i32 s69, s71, 65536\n\t" \
    "s_add_u32 s69, s69, 267264\n\t" \
    "v_min_u32 v35, 35, v223\n\t" \
    "v_lshl_add_u32 v35, v35, 5, s69\n\t" \
    "v_lshlrev_b32 v22, 2, v223\n\t" \
    "s_waitcnt vmcnt(12) lgkmcnt(0)\n\t" \
    "s_barrier\n\t" \
    "s_add_u32 m0, s82, 98304\n\t" \
    "s_nop 0\n\t" \
    "global_load_lds_dwordx4 v200, s[42:43]\n\t" \
    "v_add_u32 v200, 131072, v200\n\t" \
    "s_add_u32 m0, s82, 102400\n\t" \
    "s_nop 0\n\t" \
    "global_load_lds_dwordx4 v200, s[42:43]\n\t" \
    "v_add_u32 v200, 131072, v200\n\t" \
    "s_add_u32 m0, s82, 106496\n\t" \
    "s_nop 0\n\t" \
    "global_load_lds_dwordx4 v200, s[42:43]\n\t" \
    "v_add_u32 v200, 131072, v200\n\t" \
    "s_add_u32 m0, s82, 110592\n\t" \
    "s_nop 0\n\t" \
    "global_load_lds_dwordx4 v200, s[42:43]\n\t" \
    "v_add_u32 v200, 131072, v200\n\t" \
    "v_add_u32 v202, 69632, v202\n\t" \
    "v_add_u32 v203, 69632, v203\n\t" \
    "ds_read_b64_tr_b16 a[16:17], v202 offset:0\n\t" \
    "ds_read_b64_tr_b16 a[18:19], v202 offset:1024\n\t" \
    "ds_read_b64_tr_b16 a[20:21], v202 offset:8192\n\t" \
    "ds_read_b64_tr_b16 a[22:23], v202 offset:9216\n\t" \
    "ds_read_b64_tr_b16 a[88:89], v203 offset:0\n\t" \
    "ds_read_b64_tr_b16 a[90:91], v203 offset:1024\n\t" \
    "ds_read_b64_tr_b16 a[92:93], v203 offset:8192\n\t" \
    "ds_read_b64_tr_b16 a[94:95], v203 offset:9216\n\t" \
    "s_mul_i32 s69, s66, 147456\n\t" \
    "s_mul_i32 s71, s67, 256\n\t" \
    "s_add_u32 s69, s69, s71\n\t" \
    "v_lshl_add_u32 v14, v192, 4, s69\n\t" \
    "v_lshl_add_u32 v14, v187, 14, v14\n\t" \
    "s_mul_i32 s69, s66, 147456\n\t" \
    "s_mul_i32 s71, s67, 256\n\t" \
    "s_add_u32 s69, s69, s71\n\t" \
    "s_add_u32 s69, s69, 65536\n\t" \
    "v_lshl_add_u32 v15, v192, 4, s69\n\t" \
    "v_lshl_add_u32 v15, v187, 14, v15\n\t" \
    "s_mul_i32 s69, s66, 147456\n\t" \
    "s_mul_i32 s71, s67, 256\n\t" \
    "s_add_u32 s69, s69, s71\n\t" \
    "s_add_u32 s69, s69, 131072\n\t" \
    "v_lshl_add_u32 v16, v192, 4, s69\n\t" \
    "v_lshl_add_u32 v16, v187, 2, v16\n\t" \
    "s_mul_i32 s97, s67, 171\n\t" \
    "s_lshr_b32 s97, s97, 9\n\t" \
    "s_mul_i32 s97, s97, 131072\n\t" \
    "s_mul_i32 s71, s66, 2304\n\t" \
    "s_add_u32 s69, s97, s71\n\t" \
    "s_mul_i32 s71, s67, 655360\n\t" \
    "s_add_u32 s69, s69, s71\n\t" \
    "v_lshl_add_u32 v17, v223, 4, s69\n\t" \
    "s_mul_i32 s71, s66, 2304\n\t" \
    "s_add_u32 s69, s97, s71\n\t" \
    "s_mul_i32 s71, s67, 655360\n\t" \
    "s_add_u32 s69, s69, s71\n\t" \
    "s_add_u32 s69, s69, 2048\n\t" \
    "v_lshl_add_u32 v18, v192, 4, s69\n\t" \
    "v_lshl_add_u32 v18, v187, 2, v18\n\t" \
    "s_waitcnt vmcnt(12) lgkmcnt(0)\n\t" \
    "s_barrier\n\t" \
    "s_add_u32 m0, s82, 0\n\t" \
    "s_nop 0\n\t" \
    "global_load_lds_dwordx4 v200, s[42:43]\n\t" \
    "v_add_u32 v200, 131072, v200\n\t" \
    "s_add_u32 m0, s82, 4096\n\t" \
    "s_nop 0\n\t" \
    "global_load_lds_dwordx4 v200, s[42:43]\n\t" \
    "v_add_u32 v200, 131072, v200\n\t" \
    "s_add_u32 m0, s82, 8192\n\t" \
    "s_nop 0\n\t" \
    "global_load_lds_dwordx4 v200, s[42:43]\n\t" \
    "v_add_u32 v200, 131072, v200\n\t" \
    "s_add_u32 m0, s82, 12288\n\t" \
    "s_nop 0\n\t" \
    "global_load_lds_dwordx4 v200, s[42:43]\n\t" \
    "v_add_u32 v200, 131072, v200\n\t" \
    "ds_read_b64_tr_b16 a[24:25], v202 offset:16384\n\t" \
    "ds_read_b64_tr_b16 a[26:27], v202 offset:17408\n\t" \
    "ds_read_b64_tr_b16 a[28:29], v202 offset:24576\n\t" \
    "ds_read_b64_tr_b16 a[30:31], v202 offset:25600\n\t" \
    "ds_read_b64_tr_b16 a[96:97], v203 offset:16384\n\t" \
    "ds_read_b64_tr_b16 a[98:99], v203 offset:17408\n\t" \
    "ds_read_b64_tr_b16 a[100:101], v203 offset:24576\n\t" \
    "ds_read_b64_tr_b16 a[102:103], v203 offset:25600\n\t" \
    "s_mov_b32 s77, 3019898880\n\t" \
    "s_mov_b32 s78, 3054198966\n\t" \
    "s_mov_b32 s71, 3\n\t" \
    "vv_E1_%=:\n\t" \
    "s_memtime s[54:55]\n\t" \
    "s_nop 0\n\t" \
    "s_memrealtime s[4:5]\n\t" \
    "s_nop 0\n\t" \
    "s_memrealtime s[6:7]\n\t" \
    "s_nop 0\n\t" \
    "s_memrealtime s[8:9]\n\t" \
    "s_nop 0\n\t" \
    "s_memrealtime s[10:11]\n\t" \
    "s_nop 0\n\t" \
    "s_memrealtime s[12:13]\n\t" \
    "s_nop 0\n\t" \
    "s_memrealtime s[14:15]\n\t" \
    "s_nop 0\n\t" \
    "s_memrealtime s[16:17]\n\t" \
    "s_nop 0\n\t" \
    "s_memrealtime s[18:19]\n\t" \
    "s_nop 0\n\t" \
    "s_memrealtime s[20:21]\n\t" \
    "s_nop 0\n\t" \
    "s_memrealtime s[22:23]\n\t" \
    "s_nop 0\n\t" \
    "s_memrealtime s[24:25]\n\t" \
    "s_nop 0\n\t" \
    "s_memrealtime s[26:27]\n\t" \
    "s_nop 0\n\t" \
    "s_memrealtime s[28:29]\n\t" \
    "s_nop 0\n\t" \
    "s_memtime s[56:57]\n\t" \
    "s_mov_b32 s70, 1\n\t" \
    "s_sub_u32 s71, s71, 1\n\t" \
    "s_cselect_b32 s69, 0, -1\n\t" \
    "s_waitcnt lgkmcnt(0)\n\t" \
    "s_sub_u32 s54, s56, s54\n\t" \
    "s_and_b32 s54, s54, s69\n\t" \
    "s_cmp_gt_u32 s54, 124\n\t" \
    "s_cbranch_scc1 vv_E1_%=\n\t" \
    "s_cmp_eq_u32 s4, s28\n\t" \
    "s_addc_u32 s70, s70, 0\n\t" \
    "s_cmp_eq_u32 s6, s28\n\t" \
    "s_addc_u32 s70, s70, 0\n\t" \
    "s_cmp_eq_u32 s8, s28\n\t" \
    "s_addc_u32 s70, s70, 0\n\t" \
    "s_cmp_eq_u32 s10, s28\n\t" \
    "s_addc_u32 s70, s70, 0\n\t" \
    "s_cmp_eq_u32 s12, s28\n\t" \
    "s_addc_u32 s70, s70, 0\n\t" \
    "s_cmp_eq_u32 s14, s28\n\t" \
    "s_addc_u32 s70, s70, 0\n\t" \
    "s_cmp_eq_u32 s16, s28\n\t" \
    "s_addc_u32 s70, s70, 0\n\t" \
    "s_cmp_eq_u32 s18, s28\n\t" \
    "s_addc_u32 s70, s70, 0\n\t" \
    "s_cmp_eq_u32 s20, s28\n\t" \
    "s_addc_u32 s70, s70, 0\n\t" \
    "s_cmp_eq_u32 s22, s28\n\t" \
    "s_addc_u32 s70, s70, 0\n\t" \
    "s_cmp_eq_u32 s24, s28\n\t" \
    "s_addc_u32 s70, s70, 0\n\t" \
    "s_cmp_eq_u32 s26, s28\n\t" \
    "s_addc_u32 s70, s70, 0\n\t" \
    "s_mov_b32 s75, s73\n\t" \
    "s_mov_b32 s76, s74\n\t" \
    "s_lshl_b32 s70, s70, 3\n\t" \
    "s_sub_u32 s73, s56, s70\n\t" \
    "s_lshl_b32 s74, s28, 4\n\t" \
    "s_waitcnt vmcnt(12) lgkmcnt(0)\n\t" \
    "s_barrier\n\t" \
    "s_add_u32 m0, s82, 16384\n\t" \
    "s_nop 0\n\t" \
    "global_load_lds_dwordx4 v200, s[42:43]\n\t" \
    "v_add_u32 v200, 131072, v200\n\t" \
    "s_add_u32 m0, s82, 20480\n\t" \
    "s_nop 0\n\t" \
    "global_load_lds_dwordx4 v200, s[42:43]\n\t" \
    "v_add_u32 v200, 131072, v200\n\t" \
    "s_add_u32 m0, s82, 24576\n\t" \
    "s_nop 0\n\t" \
    "global_load_lds_dwordx4 v200, s[42:43]\n\t" \
    "v_add_u32 v200, 131072, v200\n\t" \
    "s_add_u32 m0, s82, 28672\n\t" \
    "s_nop 0\n\t" \
    "global_load_lds_dwordx4 v200, s[42:43]\n\t" \
    "v_add_u32 v200, 131072, v200\n\t" \
    "ds_read_b64_tr_b16 a[32:33], v202 offset:32768\n\t" \
    "ds_read_b64_tr_b16 a[34:35], v202 offset:33792\n\t" \
    "ds_read_b64_tr_b16 a[36:37], v202 offset:40960\n\t" \
    "ds_read_b64_tr_b16 a[38:39], v202 offset:41984\n\t" \
    "ds_read_b64_tr_b16 a[104:105], v203 offset:32768\n\t" \
    "ds_read_b64_tr_b16 a[106:107], v203 offset:33792\n\t" \
    "ds_read_b64_tr_b16 a[108:109], v203 offset:40960\n\t" \
    "ds_read_b64_tr_b16 a[110:111], v203 offset:41984\n\t" \
    "s_waitcnt vmcnt(12) lgkmcnt(0)\n\t" \
    "s_barrier\n\t" \
    "s_add_u32 m0, s96, 0\n\t" \
    "s_nop 0\n\t" \
    "global_load_lds_dwordx4 v201, s[44:45] offset:0\n\t" \
    "s_add_u32 m0, s96, 896\n\t" \
    "s_nop 0\n\t" \
    "global_load_lds_dwordx4 v201, s[44:45] offset:128\n\t" \
    "s_add_u32 m0, s96, 1792\n\t" \
    "s_nop 0\n\t" \
    "global_load_lds_dwordx4 v201, s[44:45] offset:256\n\t" \
    "s_add_u32 m0, s96, 2688\n\t" \
    "s_nop 0\n\t" \
    "global_load_lds_dwordx4 v201, s[44:45] offset:384\n\t" \
    "s_add_u32 m0, s96, 3584\n\t" \
    "s_nop 0\n\t" \
    "global_load_lds_dwordx4 v201, s[44:45] offset:512\n\t" \
    "s_add_u32 m0, s96, 4480\n\t" \
    "s_nop 0\n\t" \
    "global_load_lds_dwordx4 v201, s[44:45] offset:640\n\t" \
    "s_add_u32 m0, s96, 5376\n\t" \
    "s_nop 0\n\t" \
    "global_load_lds_dwordx4 v201, s[44:45] offset:768\n\t" \
    "s_add_u32 m0, s96, 6272\n\t" \
    "s_nop 0\n\t" \
    "global_load_lds_dwordx4 v201, s[44:45] offset:896\n\t" \
    "s_add_u32 m0, s96, 7168\n\t" \
    "s_nop 0\n\t" \
    "global_load_lds_dwordx4 v201, s[44:45] offset:1024\n\t" \
    "v_add_u32 v201, 36864, v201\n\t" \
    "ds_read_b64_tr_b16 a[40:41], v202 offset:49152\n\t" \
    "ds_read_b64_tr_b16 a[42:43], v202 offset:50176\n\t" \
    "ds_read_b64_tr_b16 a[44:45], v202 offset:57344\n\t" \
    "ds_read_b64_tr_b16 a[46:47], v202 offset:58368\n\t" \
    "ds_read_b64_tr_b16 a[112:113], v203 offset:49152\n\t" \
    "ds_read_b64_tr_b16 a[114:115], v203 offset:50176\n\t" \
    "ds_read_b64_tr_b16 a[116:117], v203 offset:57344\n\t" \
    "ds_read_b64_tr_b16 a[118:119], v203 offset:58368\n\t" \
    "s_waitcnt vmcnt(17) lgkmcnt(0)\n\t" \
    "s_barrier\n\t" \
    "v_add_u32 v202, 65536, v202\n\t" \
    "v_add_u32 v203, 65536, v203\n\t" \
    "ds_read_b64_tr_b16 a[48:49], v202 offset:0\n\t" \
    "ds_read_b64_tr_b16 a[50:51], v202 offset:1024\n\t" \
    "ds_read_b64_tr_b16 a[52:53], v202 offset:8192\n\t" \
    "ds_read_b64_tr_b16 a[54:55], v202 offset:9216\n\t" \
    "ds_read_b64_tr_b16 a[120:121], v203 offset:0\n\t" \
    "ds_read_b64_tr_b16 a[122:123], v203 offset:1024\n\t" \
    "ds_read_b64_tr_b16 a[124:125], v203 offset:8192\n\t" \
    "ds_read_b64_tr_b16 a[126:127], v203 offset:9216\n\t" \
    "s_waitcnt vmcnt(13) lgkmcnt(0)\n\t" \
    "s_barrier\n\t" \
    "s_add_u32 m0, s96, 73728\n\t" \
    "s_nop 0\n\t" \
    "global_load_lds_dwordx4 v201, s[44:45] offset:0\n\t" \
    "s_add_u32 m0, s96, 74624\n\t" \
    "s_nop 0\n\t" \
    "global_load_lds_dwordx4 v201, s[44:45] offset:128\n\t" \
    "s_add_u32 m0, s96, 75520\n\t" \
    "s_nop 0\n\t" \
    "global_load_lds_dwordx4 v201, s[44:45] offset:256\n\t" \
    "s_add_u32 m0, s96, 76416\n\t" \
    "s_nop 0\n\t" \
    "global_load_lds_dwordx4 v201, s[44:45] offset:384\n\t" \
    "s_add_u32 m0, s96, 77312\n\t" \
    "s_nop 0\n\t" \
    "global_load_lds_dwordx4 v201, s[44:45] offset:512\n\t" \
    "s_add_u32 m0, s96, 78208\n\t" \
    "s_nop 0\n\t" \
    "global_load_lds_dwordx4 v201, s[44:45] offset:640\n\t" \
    "s_add_u32 m0, s96, 79104\n\t" \
    "s_nop 0\n\t" \
    "global_load_lds_dwordx4 v201, s[44:45] offset:768\n\t" \
    "s_add_u32 m0, s96, 80000\n\t" \
    "s_nop 0\n\t" \
    "global_load_lds_dwordx4 v201, s[44:45] offset:896\n\t" \
    "s_add_u32 m0, s96, 80896\n\t" \
    "s_nop 0\n\t" \
    "global_load_lds_dwordx4 v201, s[44:45] offset:1024\n\t" \
    "v_add_u32 v201, 36864, v201\n\t" \
    "v_add_u32 v202, -98304, v202\n\t" \
    "v_add_u32 v203, -98304, v203\n\t" \
    "ds_read_b64_tr_b16 a[56:57], v202 offset:0\n\t" \
    "ds_read_b64_tr_b16 a[58:59], v202 offset:1024\n\t" \
    "ds_read_b64_tr_b16 a[60:61], v202 offset:8192\n\t" \
    "ds_read_b64_tr_b16 a[62:63], v202 offset:9216\n\t" \
    "ds_read_b64_tr_b16 a[128:129], v203 offset:0\n\t" \
    "ds_read_b64_tr_b16 a[130:131], v203 offset:1024\n\t" \
    "ds_read_b64_tr_b16 a[132:133], v203 offset:8192\n\t" \
    "ds_read_b64_tr_b16 a[134:135], v203 offset:9216\n\t" \
    "s_waitcnt vmcnt(18) lgkmcnt(0)\n\t" \
    "s_barrier\n\t" \
    "s_add_u32 m0, s96, 110592\n\t" \
    "s_nop 0\n\t" \
    "global_load_lds_dwordx4 v201, s[44:45] offset:0\n\t" \
    "s_add_u32 m0, s96, 111488\n\t" \
    "s_nop 0\n\t" \
    "global_load_lds_dwordx4 v201, s[44:45] offset:128\n\t" \
    "s_add_u32 m0, s96, 112384\n\t" \
    "s_nop 0\n\t" \
    "global_load_lds_dwordx4 v201, s[44:45] offset:256\n\t" \
    "s_add_u32 m0, s96, 113280\n\t" \
    "s_nop 0\n\t" \
    "global_load_lds_dwordx4 v201, s[44:45] offset:384\n\t" \
    "s_add_u32 m0, s96, 114176\n\t" \
    "s_nop 0\n\t" \
    "global_load_lds_dwordx4 v201, s[44:45] offset:512\n\t" \
    "s_add_u32 m0, s96, 115072\n\t" \
    "s_nop 0\n\t" \
    "global_load_lds_dwordx4 v201, s[44:45] offset:640\n\t" \
    "s_add_u32 m0, s96, 115968\n\t" \
    "s_nop 0\n\t" \
    "global_load_lds_dwordx4 v201, s[44:45] offset:768\n\t" \
    "s_add_u32 m0, s96, 116864\n\t" \
    "s_nop 0\n\t" \
    "global_load_lds_dwordx4 v201, s[44:45] offset:896\n\t" \
    "s_add_u32 m0, s96, 117760\n\t" \
    "s_nop 0\n\t" \
    "global_load_lds_dwordx4 v201, s[44:45] offset:1024\n\t" \
    "v_add_u32 v201, 36864, v201\n\t" \
    "ds_read_b64_tr_b16 a[64:65], v202 offset:16384\n\t" \
    "ds_read_b64_tr_b16 a[66:67], v202 offset:17408\n\t" \
    "ds_read_b64_tr_b16 a[68:69], v202 offset:24576\n\t" \
    "ds_read_b64_tr_b16 a[70:71], v202 offset:25600\n\t" \
    "ds_read_b64_tr_b16 a[136:137], v203 offset:16384\n\t" \
    "ds_read_b64_tr_b16 a[138:139], v203 offset:17408\n\t" \
    "ds_read_b64_tr_b16 a[140:141], v203 offset:24576\n\t" \
    "ds_read_b64_tr_b16 a[142:143], v203 offset:25600\n\t" \
    "s_waitcnt vmcnt(18) lgkmcnt(0)\n\t" \
    "s_barrier\n\t" \
    "s_add_u32 m0, s96, 36864\n\t" \
    "s_nop 0\n\t" \
    "global_load_lds_dwordx4 v201, s[44:45] offset:0\n\t" \
    "s_add_u32 m0, s96, 37760\n\t" \
    "s_nop 0\n\t" \
    "global_load_lds_dwordx4 v201, s[44:45] offset:128\n\t" \
    "s_add_u32 m0, s96, 38656\n\t" \
    "s_nop 0\n\t" \
    "global_load_lds_dwordx4 v201, s[44:45] offset:256\n\t" \
    "s_add_u32 m0, s96, 39552\n\t" \
    "s_nop 0\n\t" \
    "global_load_lds_dwordx4 v201, s[44:45] offset:384\n\t" \
    "s_add_u32 m0, s96, 40448\n\t" \
    "s_nop 0\n\t" \
    "global_load_lds_dwordx4 v201, s[44:45] offset:512\n\t" \
    "s_add_u32 m0, s96, 41344\n\t" \
    "s_nop 0\n\t" \
    "global_load_lds_dwordx4 v201, s[44:45] offset:640\n\t" \
    "s_add_u32 m0, s96, 42240\n\t" \
    "s_nop 0\n\t" \
    "global_load_lds_dwordx4 v201, s[44:45] offset:768\n\t" \
    "s_add_u32 m0, s96, 43136\n\t" \
    "s_nop 0\n\t" \
    "global_load_lds_dwordx4 v201, s[44:45] offset:896\n\t" \
    "s_add_u32 m0, s96, 44032\n\t" \
    "s_nop 0\n\t" \
    "global_load_lds_dwordx4 v201, s[44:45] offset:1024\n\t" \
    "v_add_u32 v201, 36864, v201\n\t" \
    "ds_read_b64_tr_b16 a[144:145], v204 offset:0\n\t" \
    "ds_read_b64_tr_b16 a[146:147], v204 offset:18432\n\t" \
    "ds_read_b64_tr_b16 a[160:161], v205 offset:0\n\t" \
    "ds_read_b64_tr_b16 a[162:163], v205 offset:18432\n\t" \
    "ds_read_b64_tr_b16 a[176:177], v206 offset:0\n\t" \
    "ds_read_b64_tr_b16 a[178:179], v206 offset:18432\n\t" \
    "s_waitcnt lgkmcnt(6)\n\t" \
    "ds_read_b64_tr_b16 a[192:193], v207 offset:0\n\t" \
    "ds_read_b64_tr_b16 a[194:195], v207 offset:18432\n\t" \
    "ds_read_b64_tr_b16 a[208:209], v204 offset:1024\n\t" \
    "ds_read_b64_tr_b16 a[210:211], v204 offset:19456\n\t" \
    "ds_read_b64_tr_b16 a[224:225], v205 offset:1024\n\t" \
    "ds_read_b64_tr_b16 a[226:227], v205 offset:19456\n\t" \
    "s_waitcnt lgkmcnt(6)\n\t" \
    "ds_read_b64_tr_b16 a[240:241], v206 offset:1024\n\t" \
    "ds_read_b64_tr_b16 a[242:243], v206 offset:19456\n\t" \
    "ds_read_b64_tr_b16 v[224:225], v207 offset:1024\n\t" \
    "ds_read_b64_tr_b16 v[226:227], v207 offset:19456\n\t" \
    "ds_read_b64_tr_b16 v[240:241], v204 offset:2048\n\t" \
    "ds_read_b64_tr_b16 v[242:243], v204 offset:20480\n\t" \
    "s_waitcnt lgkmcnt(6)\n\t" \
    "s_waitcnt vmcnt(18) lgkmcnt(0)\n\t" \
    "s_barrier\n\t" \
    "v_mbcnt_lo_u32_b32 v140, -1, 0\n\t" \
    "v_mbcnt_hi_u32_b32 v140, -1, v140\n\t" \
    "v_min_u32 v140, 59, v140\n\t" \
    "v_lshlrev_b32 v140, 6, v140\n\t" \
    "v_add_u32 v140, 6225920, v140\n\t" \
    "v_mov_b32 v141, 6234112\n\t" \
    "global_load_dword v141, v141, s[36:37] sc1\n\t" \
    "global_load_dwordx2 v[142:143], v140, s[36:37] sc1\n\t" \
    "v_add_u32 v204, 73728, v204\n\t" \
    "v_add_u32 v205, 73728, v205\n\t" \
    "v_add_u32 v206, 73728, v206\n\t" \
    "v_add_u32 v207, 73728, v207\n\t" \
    "ds_read_b64_tr_b16 a[148:149], v204 offset:0\n\t" \
    "ds_read_b64_tr_b16 a[150:151], v204 offset:18432\n\t" \
    "ds_read_b64_tr_b16 a[164:165], v205 offset:0\n\t" \
    "ds_read_b64_tr_b16 a[166:167], v205 offset:18432\n\t" \
    "ds_read_b64_tr_b16 a[180:181], v206 offset:0\n\t" \
    "ds_read_b64_tr_b16 a[182:183], v206 offset:18432\n\t" \
    "s_waitcnt lgkmcnt(6)\n\t" \
    "ds_read_b64_tr_b16 a[196:197], v207 offset:0\n\t" \
    "ds_read_b64_tr_b16 a[198:199], v207 offset:18432\n\t" \
    "ds_read_b64_tr_b16 a[212:213], v204 offset:1024\n\t" \
    "ds_read_b64_tr_b16 a[214:215], v204 offset:19456\n\t" \
    "ds_read_b64_tr_b16 a[228:229], v205 offset:1024\n\t" \
    "ds_read_b64_tr_b16 a[230:231], v205 offset:19456\n\t" \
    "s_waitcnt lgkmcnt(6)\n\t" \
    "ds_read_b64_tr_b16 a[244:245], v206 offset:1024\n\t" \
    "ds_read_b64_tr_b16 a[246:247], v206 offset:19456\n\t" \
    "ds_read_b64_tr_b16 v[228:229], v207 offset:1024\n\t" \
    "ds_read_b64_tr_b16 v[230:231], v207 offset:19456\n\t" \
    "ds_read_b64_tr_b16 v[244:245], v204 offset:2048\n\t" \
    "ds_read_b64_tr_b16 v[246:247], v204 offset:20480\n\t" \
    "s_waitcnt lgkmcnt(6)\n\t" \
    "s_waitcnt vmcnt(11) lgkmcnt(0)\n\t" \
    "s_barrier\n\t" \
    "ds_read_b64_tr_b16 a[152:153], v204 offset:36864\n\t" \
    "ds_read_b64_tr_b16 a[154:155], v204 offset:55296\n\t" \
    "ds_read_b64_tr_b16 a[168:169], v205 offset:36864\n\t" \
    "ds_read_b64_tr_b16 a[170:171], v205 offset:55296\n\t" \
    "ds_read_b64_tr_b16 a[184:185], v206 offset:36864\n\t" \
    "ds_read_b64_tr_b16 a[186:187], v206 offset:55296\n\t" \
    "s_waitcnt lgkmcnt(6)\n\t" \
    "ds_read_b64_tr_b16 a[200:201], v207 offset:36864\n\t" \
    "ds_read_b64_tr_b16 a[202:203], v207 offset:55296\n\t" \
    "ds_read_b64_tr_b16 a[216:217], v204 offset:37888\n\t" \
    "ds_read_b64_tr_b16 a[218:219], v204 offset:56320\n\t" \
    "ds_read_b64_tr_b16 a[232:233], v205 offset:37888\n\t" \
    "ds_read_b64_tr_b16 a[234:235], v205 offset:56320\n\t" \
    "s_waitcnt lgkmcnt(6)\n\t" \
    "ds_read_b64_tr_b16 a[248:249], v206 offset:37888\n\t" \
    "ds_read_b64_tr_b16 a[250:251], v206 offset:56320\n\t" \
    "ds_read_b64_tr_b16 v[232:233], v207 offset:37888\n\t" \
    "ds_read_b64_tr_b16 v[234:235], v207 offset:56320\n\t" \
    "ds_read_b64_tr_b16 v[248:249], v204 offset:38912\n\t" \
    "ds_read_b64_tr_b16 v[250:251], v204 offset:57344\n\t" \
    "s_waitcnt lgkmcnt(6)\n\t" \
    "s_waitcnt vmcnt(2) lgkmcnt(0)\n\t" \
    "s_barrier\n\t" \
    "v_add_u32 v204, -36864, v204\n\t" \
    "v_add_u32 v205, -36864, v205\n\t" \
    "v_add_u32 v206, -36864, v206\n\t" \
    "v_add_u32 v207, -36864, v207\n\t" \
    "ds_read_b64_tr_b16 a[156:157], v204 offset:0\n\t" \
    "ds_read_b64_tr_b16 a[158:159], v204 offset:18432\n\t" \
    "ds_read_b64_tr_b16 a[172:173], v205 offset:0\n\t" \
    "ds_read_b64_tr_b16 a[174:175], v205 offset:18432\n\t" \
    "ds_read_b64_tr_b16 a[188:189], v206 offset:0\n\t" \
    "ds_read_b64_tr_b16 a[190:191], v206 offset:18432\n\t" \
    "s_waitcnt lgkmcnt(6)\n\t" \
    "ds_read_b64_tr_b16 a[204:205], v207 offset:0\n\t" \
    "ds_read_b64_tr_b16 a[206:207], v207 offset:18432\n\t" \
    "ds_read_b64_tr_b16 a[220:221], v204 offset:1024\n\t" \
    "ds_read_b64_tr_b16 a[222:223], v204 offset:19456\n\t" \
    "ds_read_b64_tr_b16 a[236:237], v205 offset:1024\n\t" \
    "ds_read_b64_tr_b16 a[238:239], v205 offset:19456\n\t" \
    "s_waitcnt lgkmcnt(6)\n\t" \
    "ds_read_b64_tr_b16 a[252:253], v206 offset:1024\n\t" \
    "ds_read_b64_tr_b16 a[254:255], v206 offset:19456\n\t" \
    "ds_read_b64_tr_b16 v[236:237], v207 offset:1024\n\t" \
    "ds_read_b64_tr_b16 v[238:239], v207 offset:19456\n\t" \
    "ds_read_b64_tr_b16 v[252:253], v204 offset:2048\n\t" \
    "ds_read_b64_tr_b16 v[254:255], v204 offset:20480\n\t" \
    "s_waitcnt lgkmcnt(6)\n\t" \
    "s_waitcnt vmcnt(0) lgkmcnt(0)\n\t" \
    "s_barrier\n\t" \
    "v_sub_u32 v142, v142, v141\n\t" \
    "v_cmp_ne_u32 vcc, 1, v142\n\t" \
    "s_cbranch_vccz vv_Cok4_%=\n\t" \
    "vv_C3_%=:\n\t" \
    "global_load_dwordx2 v[142:143], v140, s[36:37] sc1\n\t" \
    "s_waitcnt vmcnt(0)\n\t" \
    "v_sub_u32 v142, v142, v141\n\t" \
    "v_cmp_ne_u32 vcc, 1, v142\n\t" \
    "s_cbranch_vccz vv_Cok4_%=\n\t" \
    "s_cmp_eq_u32 s68, 0\n\t" \
    "s_cbranch_scc1 vv_F5_%=\n\t" \
    "s_sub_u32 s68, s68, 1\n\t" \
    "s_sleep 4\n\t" \
    "s_branch vv_C3_%=\n\t" \
    "vv_F5_%=:\n\t" \
    "v_mov_b32 v144, 6095104\n\t" \
    "v_mov_b32 v145, 4\n\t" \
    "global_store_dword v144, v145, s[36:37] sc1\n\t" \
    "s_waitcnt vmcnt(0)\n\t" \
    "s_branch vv_Cok4_%=\n\t" \
    "vv_Cok4_%=:\n\t" \
    "s_cmp_lg_u32 s66, 0\n\t" \
    "s_cbranch_scc1 vv_Ca6_%=\n\t" \
    "v_mov_b32 v140, 6176768\n\t" \
    "s_mov_b64 exec, 1\n\t" \
    "global_atomic_add v140, v142, s[36:37] sc1\n\t" \
    "s_mov_b64 exec, -1\n\t" \
    "vv_Ca6_%=:\n\t" \
    "v_min_u32_dpp v143, v143, v143 row_shl:6 row_mask:0xf bank_mask:0xf\n\t" \
    "s_nop 0\n\t" \
    "v_readlane_b32 s69, v143, 0\n\t" \
    "v_readlane_b32 s70, v143, 1\n\t" \
    "s_nop 1\n\t" \
    "s_cmp_lt_u32 s70, s69\n\t" \
    "s_cselect_b32 s62, 8192, 0\n\t" \
    "s_cselect_b32 s69, s70, s69\n\t" \
    "s_mov_b32 s71, s69\n\t" \
    "v_readlane_b32 s69, v143, 2\n\t" \
    "v_readlane_b32 s70, v143, 3\n\t" \
    "s_nop 1\n\t" \
    "s_cmp_lt_u32 s70, s69\n\t" \
    "s_cselect_b32 s63, 8192, 0\n\t" \
    "s_cselect_b32 s69, s70, s69\n\t" \
    "s_max_u32 s71, s69, s71\n\t" \
    "v_readlane_b32 s69, v143, 4\n\t" \
    "v_readlane_b32 s70, v143, 5\n\t" \
    "s_nop 1\n\t" \
    "s_cmp_lt_u32 s70, s69\n\t" \
    "s_cselect_b32 s64, 8192, 0\n\t" \
    "s_cselect_b32 s69, s70, s69\n\t" \
    "s_max_u32 s71, s69, s71\n\t" \
    "s_add_u32 s63, s63, 0x200000\n\t" \
    "s_xor_b32 s52, s64, 8192\n\t" \
    "s_cmp_gt_u32 s71, 625\n\t" \
    "s_cselect_b32 s69, 1, 0\n\t" \
    "s_mul_i32 s70, s69, 160\n\t" \
    "s_add_u32 s86, s86, s70\n\t" \
    "s_mul_i32 s70, s69, 536\n\t" \
    "s_add_u32 s87, s87, s70\n\t" \
    "s_mul_i32 s70, s69, 256\n\t" \
    "s_add_u32 s88, s88, s70\n\t" \
    "v_mov_b32 v148, -1\n\t" \
    "v_mov_b32 v149, -1\n\t" \
    "v_mov_b32 v150, -1\n\t" \
    "v_mov_b32 v151, -1\n\t" \
    "s_mov_b32 s71, 3\n\t" \
    "vv_E7_%=:\n\t" \
    "s_memtime s[54:55]\n\t" \
    "s_nop 0\n\t" \
    "s_memrealtime s[4:5]\n\t" \
    "s_nop 0\n\t" \
    "s_memrealtime s[6:7]\n\t" \
    "s_nop 0\n\t" \
    "s_memrealtime s[8:9]\n\t" \
    "s_nop 0\n\t" \
    "s_memrealtime s[10:11]\n\t" \
    "s_nop 0\n\t" \
    "s_memrealtime s[12:13]\n\t" \
    "s_nop 0\n\t" \
    "s_memrealtime s[14:15]\n\t" \
    "s_nop 0\n\t" \
    "s_memrealtime s[16:17]\n\t" \
    "s_nop 0\n\t" \
    "s_memrealtime s[18:19]\n\t" \
    "s_nop 0\n\t" \
    "s_memrealtime s[20:21]\n\t" \
    "s_nop 0\n\t" \
    "s_memrealtime s[22:23]\n\t" \
    "s_nop 0\n\t" \
    "s_memrealtime s[24:25]\n\t" \
    "s_nop 0\n\t" \
    "s_memrealtime s[26:27]\n\t" \
    "s_nop 0\n\t" \
    "s_memrealtime s[28:29]\n\t" \
    "s_nop 0\n\t" \
    "s_memtime s[56:57]\n\t" \
    "s_mov_b32 s70, 1\n\t" \
    "s_sub_u32 s71, s71, 1\n\t" \
    "s_cselect_b32 s69, 0, -1\n\t" \
    "s_waitcnt lgkmcnt(0)\n\t" \
    "s_sub_u32 s54, s56, s54\n\t" \
    "s_and_b32 s54, s54, s69\n\t" \
    "s_cmp_gt_u32 s54, 124\n\t" \
    "s_cbranch_scc1 vv_E7_%=\n\t" \
    "s_cmp_eq_u32 s4, s28\n\t" \
    "s_addc_u32 s70, s70, 0\n\t" \
    "s_cmp_eq_u32 s6, s28\n\t" \
    "s_addc_u32 s70, s70, 0\n\t" \
    "s_cmp_eq_u32 s8, s28\n\t" \
    "s_addc_u32 s70, s70, 0\n\t" \
    "s_cmp_eq_u32 s10, s28\n\t" \
    "s_addc_u32 s70, s70, 0\n\t" \
    "s_cmp_eq_u32 s12, s28\n\t" \
    "s_addc_u32 s70, s70, 0\n\t" \
    "s_cmp_eq_u32 s14, s28\n\t" \
    "s_addc_u32 s70, s70, 0\n\t" \
    "s_cmp_eq_u32 s16, s28\n\t" \
    "s_addc_u32 s70, s70, 0\n\t" \
    "s_cmp_eq_u32 s18, s28\n\t" \
    "s_addc_u32 s70, s70, 0\n\t" \
    "s_cmp_eq_u32 s20, s28\n\t" \
    "s_addc_u32 s70, s70, 0\n\t" \
    "s_cmp_eq_u32 s22, s28\n\t" \
    "s_addc_u32 s70, s70, 0\n\t" \
    "s_cmp_eq_u32 s24, s28\n\t" \
    "s_addc_u32 s70, s70, 0\n\t" \
    "s_cmp_eq_u32 s26, s28\n\t" \
    "s_addc_u32 s70, s70, 0\n\t" \
    "s_mov_b32 s75, s73\n\t" \
    "s_mov_b32 s76, s74\n\t" \
    "s_lshl_b32 s70, s70, 3\n\t" \
    "s_sub_u32 s73, s56, s70\n\t" \
    "s_lshl_b32 s74, s28, 4\n\t" \
    "s_sub_u32 s69, s73, s75\n\t" \
    "s_sub_u32 s70, s74, s76\n\t" \
    "s_sub_u32 s71, s70, 1600\n\t" \
    "s_cmp_lt_u32 s71, 400000\n\t" \
    "s_cbranch_scc0 vv_K9_%=\n\t" \
    "v_cvt_f32_u32 v140, s69\n\t" \
    "v_cvt_f32_u32 v141, s70\n\t" \
    "v_rcp_f32 v142, v140\n\t" \
    "v_rcp_f32 v143, v141\n\t" \
    "v_mul_f32 v142, v142, v141\n\t" \
    "v_mul_f32 v143, v143, v140\n\t" \
    "v_mul_f32 v142, 0x4f800000, v142\n\t" \
    "v_mul_f32 v143, 0x4f000000, v143\n\t" \
    "v_cvt_u32_f32 v142, v142\n\t" \
    "v_cvt_u32_f32 v143, v143\n\t" \
    "v_readfirstlane_b32 s69, v142\n\t" \
    "v_readfirstlane_b32 s70, v143\n\t" \
    "s_nop 1\n\t" \
    "s_mov_b32 s78, s69\n\t" \
    "s_mov_b32 s77, s70\n\t" \
    "vv_K9_%=:\n\t" \
    "s_mov_b32 s79, s74\n\t" \
    "s_sub_u32 s80, s74, 32000\n\t" \
    "s_cmp_eq_u32 s66, 1\n\t" \
    "s_cbranch_scc1 vv_loop11_%=\n\t" \
    "s_cmp_eq_u32 s66, 2\n\t" \
    "s_cbranch_scc1 vv_loop12_%=\n\t" \
    "s_cmp_eq_u32 s66, 3\n\t" \
    "s_cbranch_scc1 vv_loop13_%=\n\t" \
    "vv_loop10_%=:\n\t" \
    "v_mov_b32 v72, 0\n\t" \
    "v_mov_b32 v73, 0\n\t" \
    "v_mov_b32 v74, 0\n\t" \
    "v_mov_b32 v75, 0\n\t" \
    "v_mov_b32 v76, 0\n\t" \
    "v_mov_b32 v77, 0\n\t" \
    "v_mov_b32 v78, 0\n\t" \
    "v_mov_b32 v79, 0\n\t" \
    "v_mov_b32 v92, 0\n\t" \
    "v_mov_b32 v93, 0\n\t" \
    "v_mov_b32 v94, 0\n\t" \
    "v_mov_b32 v95, 0\n\t" \
    "v_mov_b32 v96, 0\n\t" \
    "v_mov_b32 v97, 0\n\t" \
    "v_mov_b32 v98, 0\n\t" \
    "v_mov_b32 v99, 0\n\t" \
    "v_mov_b32 v100, 0\n\t" \
    "v_mov_b32 v101, 0\n\t" \
    "v_mov_b32 v102, 0\n\t" \
    "v_mov_b32 v103, 0\n\t" \
    "v_mov_b32 v104, 0\n\t" \
    "v_mov_b32 v105, 0\n\t" \
    "v_mov_b32 v106, 0\n\t" \
    "v_mov_b32 v107, 0\n\t" \
    "v_mov_b32 v108, 0\n\t" \
    "v_mov_b32 v109, 0\n\t" \
    "v_mov_b32 v110, 0\n\t" \
    "v_mov_b32 v111, 0\n\t" \
    "v_mov_b32 v112, 0\n\t" \
    "v_mov_b32 v113, 0\n\t" \
    "v_mov_b32 v114, 0\n\t" \
    "v_mov_b32 v115, 0\n\t" \
    "v_mov_b32 v116, 0\n\t" \
    "v_mov_b32 v117, 0\n\t" \
    "v_mov_b32 v118, 0\n\t" \
    "v_mov_b32 v119, 0\n\t" \
    "v_mov_b32 v120, 0\n\t" \
    "v_mov_b32 v121, 0\n\t" \
    "v_mov_b32 v122, 0\n\t" \
    "v_mov_b32 v123, 0\n\t" \
    "v_mov_b32 v124, 0\n\t" \
    "v_mov_b32 v125, 0\n\t" \
    "v_mov_b32 v126, 0\n\t" \
    "v_mov_b32 v127, 0\n\t" \
    "vv_go15_%=:\n\t" \
    "s_add_u32 s69, s65, 0\n\t" \
    "s_bitcmp1_b32 s69, 0\n\t" \
    "s_cselect_b32 s70, s63, s62\n\t" \
    "s_bfe_u32 s69, s69, 0x20001\n\t" \
    "s_lshl_b32 s69, s69, 19\n\t" \
    "s_add_u32 s69, s69, s70\n\t" \
    "s_add_u32 s46, s36, s69\n\t" \
    "s_addc_u32 s47, s37, 0\n\t" \
    "s_add_u32 s69, s65, 0\n\t" \
    "s_mul_i32 s70, s69, 171\n\t" \
    "s_lshr_b32 s70, s70, 9\n\t" \
    "s_mul_i32 s70, s70, 3\n\t" \
    "s_sub_u32 s69, s69, s70\n\t" \
    "s_mul_i32 s69, s69, 36\n\t" \
    "s_lshl_b32 s69, s69, 14\n\t" \
    "s_add_u32 s69, s69, s64\n\t" \
    "s_add_u32 s69, s69, 4194304\n\t" \
    "s_add_u32 s38, s36, s69\n\t" \
    "s_addc_u32 s39, s37, 0\n\t" \
    "s_add_u32 s69, s65, 2\n\t" \
    "s_mul_i32 s70, s69, 171\n\t" \
    "s_lshr_b32 s70, s70, 9\n\t" \
    "s_mul_i32 s70, s70, 3\n\t" \
    "s_sub_u32 s69, s69, s70\n\t" \
    "s_mul_i32 s69, s69, 36\n\t" \
    "s_lshl_b32 s69, s69, 14\n\t" \
    "s_add_u32 s69, s69, s64\n\t" \
    "s_add_u32 s69, s69, 4194304\n\t" \
    "s_add_u32 s42, s36, s69\n\t" \
    "s_addc_u32 s43, s37, 0\n\t" \
    "s_mul_i32 s69, s65, 9216\n\t" \
    "s_add_u32 s69, s69, 123731968\n\t" \
    "s_add_u32 s40, s36, s69\n\t" \
    "s_addc_u32 s41, s37, 0\n\t" \
    "s_sub_u32 s70, s79, s74\n\t" \
    "s_max_i32 s70, s70, 0\n\t" \
    "s_min_i32 s70, s70, 16000\n\t" \
    "s_lshl_b32 s70, s70, 1\n\t" \
    "s_mul_hi_u32 s70, s70, s77\n\t" \
    "s_add_u32 s70, s70, s73\n\t" \
    "s_sub_u32 s70, s70, 100\n\t" \
    "s_getpc_b64 s[30:31]\n\t" \
    "vv_Wp17_%=:\n\t" \
    "s_add_u32 s30, s30, vv_We18_%=-vv_Wp17_%=\n\t" \
    "s_addc_u32 s31, s31, 0\n\t" \
    "vv_W16_%=:\n\t" \
    "s_memtime s[56:57]\n\t" \
    "s_waitcnt lgkmcnt(0)\n\t" \
    "s_sub_u32 s69, s70, s56\n\t" \
    "s_cmp_gt_i32 s69, 111\n\t" \
    "s_cbranch_scc1 vv_W16_%=\n\t" \
    "s_max_i32 s69, s69, 0\n\t" \
    "s_lshr_b32 s69, s69, 1\n\t" \
    "s_and_b32 s69, s69, 0x3c\n\t" \
    "s_sub_u32 s30, s30, s69\n\t" \
    "s_subb_u32 s31, s31, 0\n\t" \
    "s_setpc_b64 s[30:31]\n\t" \
    ".p2align 6\n\t" \
    "s_nop 0\n\t" \
    "s_nop 0\n\t" \
    "s_nop 0\n\t" \
    "s_nop 1\n\t" \
    "s_nop 1\n\t" \
    "s_nop 1\n\t" \
    "s_nop 1\n\t" \
    "s_nop 1\n\t" \
    "s_nop 1\n\t" \
    "s_nop 1\n\t" \
    "s_nop 1\n\t" \
    "s_nop 1\n\t" \
    "s_nop 1\n\t" \
    "s_nop 1\n\t" \
    "s_nop 1\n\t" \
    "s_nop 1\n\t" \
    "vv_We18_%=:\n\t" \
    "vv_G19_%=:\n\t" \
    "global_load_dwordx4 v[36:39], v1, s[46:47] sc1\n\t" \
    "s_nop 9\n\t" \
    "global_load_dwordx4 v[40:43], v1, s[46:47] offset:1024 sc1\n\t" \
    "s_nop 9\n\t" \
    "global_load_dwordx4 v[44:47], v2, s[46:47] sc1\n\t" \
    "s_waitcnt vmcnt(2)\n\t" \
    "v_smfmac_f32_16x16x64_bf16 v[72:75], v[36:39], a[0:7], v10\n\t" \
    "ds_write_b128 v0, v[36:39] offset:0\n\t" \
    "v_smfmac_f32_16x16x64_bf16 v[76:79], v[36:39], a[72:79], v10\n\t" \
    "v_max3_u32 v140, v36, v37, v38\n\t" \
    "s_waitcnt vmcnt(1)\n\t" \
    "ds_write_b128 v0, v[40:43] offset:1024\n\t" \
    "v_smfmac_f32_16x16x64_bf16 v[72:75], v[40:43], a[8:15], v10\n\t" \
    "v_max3_u32 v140, v140, v39, v40\n\t" \
    "v_max3_u32 v140, v140, v41, v42\n\t" \
    "v_max_u32 v140, v140, v43\n\t" \
    "v_cmp_eq_u32 vcc, -1, v140\n\t" \
    "v_smfmac_f32_16x16x64_bf16 v[76:79], v[40:43], a[80:87], v10\n\t" \
    "s_cbranch_vccnz vv_Gbad20_%=\n\t" \
    "vv_Gc22_%=:\n\t" \
    "s_waitcnt lgkmcnt(0)\n\t" \
    "s_barrier\n\t" \
    "ds_read_b128 v[48:51], v0 offset:2048\n\t" \
    "ds_read_b128 v[52:55], v0 offset:3072\n\t" \
    "s_waitcnt vmcnt(0)\n\t" \
    "v_smfmac_f32_16x16x64_bf16 v[72:75], v[44:47], a[64:71], v10\n\t" \
    "ds_read_b128 v[56:59], v0 offset:4096\n\t" \
    "ds_read_b128 v[60:63], v0 offset:5120\n\t" \
    "v_max3_u32 v140, v44, v45, v46\n\t" \
    "v_max_u32 v140, v140, v47\n\t" \
    "v_smfmac_f32_16x16x64_bf16 v[76:79], v[44:47], a[136:143], v10\n\t" \
    "ds_read_b128 v[64:67], v0 offset:6144\n\t" \
    "ds_read_b128 v[68:71], v0 offset:7168\n\t" \
    "v_cmp_eq_u32 vcc, -1, v140\n\t" \
    "s_cbranch_vccnz vv_Gbad221_%=\n\t" \
    "vv_Gc223_%=:\n\t" \
    "s_waitcnt lgkmcnt(5)\n\t" \
    "v_smfmac_f32_16x16x64_bf16 v[72:75], v[48:51], a[16:23], v10\n\t" \
    "s_waitcnt lgkmcnt(4)\n\t" \
    "v_smfmac_f32_16x16x64_bf16 v[72:75], v[52:55], a[24:31], v10\n\t" \
    "s_waitcnt lgkmcnt(3)\n\t" \
    "v_smfmac_f32_16x16x64_bf16 v[72:75], v[56:59], a[32:39], v10\n\t" \
    "s_waitcnt lgkmcnt(2)\n\t" \
    "v_smfmac_f32_16x16x64_bf16 v[72:75], v[60:63], a[40:47], v10\n\t" \
    "s_waitcnt lgkmcnt(1)\n\t" \
    "v_smfmac_f32_16x16x64_bf16 v[72:75], v[64:67], a[48:55], v10\n\t" \
    "s_waitcnt lgkmcnt(0)\n\t" \
    "v_smfmac_f32_16x16x64_bf16 v[72:75], v[68:71], a[56:63], v10\n\t" \
    "v_smfmac_f32_16x16x64_bf16 v[76:79], v[48:51], a[88:95], v10\n\t" \
    "s_nop 1\n\t" \
    "s_nop 0\n\t" \
    "v_smfmac_f32_16x16x64_bf16 v[76:79], v[52:55], a[96:103], v10\n\t" \
    "s_nop 1\n\t" \
    "s_nop 0\n\t" \
    "v_smfmac_f32_16x16x64_bf16 v[76:79], v[56:59], a[104:111], v10\n\t" \
    "v_add_f32 v72, v72, v74\n\t" \
    "v_smfmac_f32_16x16x64_bf16 v[76:79], v[60:63], a[112:119], v10\n\t" \
    "v_add_f32 v73, v73, v75\n\t" \
    "v_smfmac_f32_16x16x64_bf16 v[76:79], v[64:67], a[120:127], v10\n\t" \
    "v_cvt_pk_bf16_f32 v80, v72, v73\n\t" \
    "v_smfmac_f32_16x16x64_bf16 v[76:79], v[68:71], a[128:135], v10\n\t" \
    "ds_write_b32 v12, v80 offset:0\n\t" \
    "s_nop 6\n\t" \
    "v_add_f32 v76, v76, v78\n\t" \
    "v_add_f32 v77, v77, v79\n\t" \
    "v_cvt_pk_bf16_f32 v81, v76, v77\n\t" \
    "ds_write_b32 v12, v81 offset:64\n\t" \
    "s_waitcnt lgkmcnt(0)\n\t" \
    "s_barrier\n\t" \
    "ds_read_b64_tr_b16 v[84:85], v13 offset:0\n\t" \
    "ds_read_b64_tr_b16 v[86:87], v13 offset:128\n\t" \
    "ds_read_b64_tr_b16 v[88:89], v13 offset:1024\n\t" \
    "ds_read_b64_tr_b16 v[90:91], v13 offset:1152\n\t" \
    "s_waitcnt lgkmcnt(2)\n\t" \
    "v_smfmac_f32_16x16x64_bf16 v[92:95], v[84:87], a[144:151], v10\n\t" \
    "s_nop 2\n\t" \
    "s_waitcnt lgkmcnt(0)\n\t" \
    "v_smfmac_f32_16x16x64_bf16 v[92:95], v[88:91], a[152:159], v10\n\t" \
    "s_nop 2\n\t" \
    "v_smfmac_f32_16x16x64_bf16 v[96:99], v[84:87], a[160:167], v10\n\t" \
    "s_nop 2\n\t" \
    "v_smfmac_f32_16x16x64_bf16 v[96:99], v[88:91], a[168:175], v10\n\t" \
    "s_nop 2\n\t" \
    "v_smfmac_f32_16x16x64_bf16 v[100:103], v[84:87], a[176:183], v10\n\t" \
    "s_nop 0\n\t" \
    "v_add_f32 v92, v92, v94\n\t" \
    "v_add_f32 v93, v93, v95\n\t" \
    "v_smfmac_f32_16x16x64_bf16 v[100:103], v[88:91], a[184:191], v10\n\t" \
    "s_nop 1\n\t" \
    "v_cvt_pk_bf16_f32 v128, v92, v93\n\t" \
    "v_smfmac_f32_16x16x64_bf16 v[104:107], v[84:87], a[192:199], v10\n\t" \
    "s_nop 0\n\t" \
    "v_add_f32 v96, v96, v98\n\t" \
    "v_add_f32 v97, v97, v99\n\t" \
    "v_smfmac_f32_16x16x64_bf16 v[104:107], v[88:91], a[200:207], v10\n\t" \
    "s_nop 1\n\t" \
    "v_cvt_pk_bf16_f32 v129, v96, v97\n\t" \
    "v_smfmac_f32_16x16x64_bf16 v[124:127], v[84:87], v[240:247], v10\n\t" \
    "s_nop 0\n\t" \
    "v_add_f32 v100, v100, v102\n\t" \
    "v_add_f32 v101, v101, v103\n\t" \
    "v_smfmac_f32_16x16x64_bf16 v[124:127], v[88:91], v[248:255], v10\n\t" \
    "s_nop 1\n\t" \
    "global_load_dword v140, v20, s[46:47] sc1\n\t" \
    "v_cvt_pk_bf16_f32 v130, v100, v101\n\t" \
    "v_smfmac_f32_16x16x64_bf16 v[108:111], v[84:87], a[208:215], v10\n\t" \
    "s_nop 0\n\t" \
    "v_add_f32 v104, v104, v106\n\t" \
    "v_add_f32 v105, v105, v107\n\t" \
    "v_smfmac_f32_16x16x64_bf16 v[108:111], v[88:91], a[216:223], v10\n\t" \
    "s_nop 0\n\t" \
    "v_cvt_pk_bf16_f32 v131, v104, v105\n\t" \
    "v_permlane32_swap_b32 v128, v130\n\t" \
    "v_smfmac_f32_16x16x64_bf16 v[112:115], v[84:87], a[224:231], v10\n\t" \
    "s_nop 0\n\t" \
    "v_permlane32_swap_b32 v129, v131\n\t" \
    "s_nop 1\n\t" \
    "v_permlane16_swap_b32 v128, v129\n\t" \
    "v_smfmac_f32_16x16x64_bf16 v[112:115], v[88:91], a[232:239], v10\n\t" \
    "v_permlane16_swap_b32 v130, v131\n\t" \
    "global_store_dwordx4 v14, v[128:131], s[38:39] sc1\n\t" \
    "v_add_f32 v124, v124, v126\n\t" \
    "v_smfmac_f32_16x16x64_bf16 v[116:119], v[84:87], a[240:247], v10\n\t" \
    "s_nop 0\n\t" \
    "v_add_f32 v125, v125, v127\n\t" \
    "v_cvt_pk_bf16_f32 v136, v124, v125\n\t" \
    "v_smfmac_f32_16x16x64_bf16 v[116:119], v[88:91], a[248:255], v10\n\t" \
    "global_store_dword v16, v136, s[38:39] sc1\n\t" \
    "v_add_f32 v108, v108, v110\n\t" \
    "v_add_f32 v109, v109, v111\n\t" \
    "v_smfmac_f32_16x16x64_bf16 v[120:123], v[84:87], v[224:231], v10\n\t" \
    "s_nop 0\n\t" \
    "v_cvt_pk_bf16_f32 v132, v108, v109\n\t" \
    "v_add_f32 v112, v112, v114\n\t" \
    "v_smfmac_f32_16x16x64_bf16 v[120:123], v[88:91], v[232:239], v10\n\t" \
    "v_add_f32 v113, v113, v115\n\t" \
    "v_cvt_pk_bf16_f32 v133, v112, v113\n\t" \
    "v_add_f32 v116, v116, v118\n\t" \
    "v_add_f32 v117, v117, v119\n\t" \
    "v_cvt_pk_bf16_f32 v134, v116, v117\n\t" \
    "s_nop 2\n\t" \
    "v_pk_add_f32 v[120:121], v[120:121], v[122:123]\n\t" \
    "v_cvt_pk_bf16_f32 v135, v120, v121\n\t" \
    "v_permlane32_swap_b32 v132, v134\n\t" \
    "s_nop 0\n\t" \
    "v_permlane32_swap_b32 v133, v135\n\t" \
    "s_nop 1\n\t" \
    "v_permlane16_swap_b32 v132, v133\n\t" \
    "v_permlane16_swap_b32 v134, v135\n\t" \
    "global_store_dwordx4 v15, v[132:135], s[38:39] sc1\n\t" \
    "s_nop 1\n\t" \
    "global_store_dwordx4 v17, v[128:131], s[40:41] sc1\n\t" \
    "global_store_dwordx4 v17, v[132:135], s[40:41] offset:1024 sc1\n\t" \
    "global_store_dword v18, v136, s[40:41] sc1\n\t" \
    "s_cmp_eq_u32 s65, 63\n\t" \
    "s_cbranch_scc1 vv_end14_%=\n\t" \
    "s_mov_b32 s71, 3\n\t" \
    "vv_E25_%=:\n\t" \
    "s_memtime s[54:55]\n\t" \
    "s_nop 0\n\t" \
    "s_memrealtime s[4:5]\n\t" \
    "s_nop 0\n\t" \
    "s_memrealtime s[6:7]\n\t" \
    "s_nop 0\n\t" \
    "s_memrealtime s[8:9]\n\t" \
    "s_nop 0\n\t" \
    "s_memrealtime s[10:11]\n\t" \
    "s_nop 0\n\t" \
    "s_memrealtime s[12:13]\n\t" \
    "s_nop 0\n\t" \
    "s_memrealtime s[14:15]\n\t" \
    "s_nop 0\n\t" \
    "s_memrealtime s[16:17]\n\t" \
    "s_nop 0\n\t" \
    "s_memrealtime s[18:19]\n\t" \
    "s_nop 0\n\t" \
    "s_memrealtime s[20:21]\n\t" \
    "s_nop 0\n\t" \
    "s_memrealtime s[22:23]\n\t" \
    "s_nop 0\n\t" \
    "s_memrealtime s[24:25]\n\t" \
    "s_nop 0\n\t" \
    "s_memrealtime s[26:27]\n\t" \
    "s_nop 0\n\t" \
    "s_memrealtime s[28:29]\n\t" \
    "s_nop 0\n\t" \
    "s_memtime s[56:57]\n\t" \
    "s_mov_b32 s70, 1\n\t" \
    "s_sub_u32 s71, s71, 1\n\t" \
    "s_cselect_b32 s69, 0, -1\n\t" \
    "s_waitcnt lgkmcnt(0)\n\t" \
    "s_sub_u32 s54, s56, s54\n\t" \
    "s_and_b32 s54, s54, s69\n\t" \
    "s_cmp_gt_u32 s54, 124\n\t" \
    "s_cbranch_scc1 vv_E25_%=\n\t" \
    "s_cmp_eq_u32 s4, s28\n\t" \
    "s_addc_u32 s70, s70, 0\n\t" \
    "s_cmp_eq_u32 s6, s28\n\t" \
    "s_addc_u32 s70, s70, 0\n\t" \
    "s_cmp_eq_u32 s8, s28\n\t" \
    "s_addc_u32 s70, s70, 0\n\t" \
    "s_cmp_eq_u32 s10, s28\n\t" \
    "s_addc_u32 s70, s70, 0\n\t" \
    "s_cmp_eq_u32 s12, s28\n\t" \
    "s_addc_u32 s70, s70, 0\n\t" \
    "s_cmp_eq_u32 s14, s28\n\t" \
    "s_addc_u32 s70, s70, 0\n\t" \
    "s_cmp_eq_u32 s16, s28\n\t" \
    "s_addc_u32 s70, s70, 0\n\t" \
    "s_cmp_eq_u32 s18, s28\n\t" \
    "s_addc_u32 s70, s70, 0\n\t" \
    "s_cmp_eq_u32 s20, s28\n\t" \
    "s_addc_u32 s70, s70, 0\n\t" \
    "s_cmp_eq_u32 s22, s28\n\t" \
    "s_addc_u32 s70, s70, 0\n\t" \
    "s_cmp_eq_u32 s24, s28\n\t" \
    "s_addc_u32 s70, s70, 0\n\t" \
    "s_cmp_eq_u32 s26, s28\n\t" \
    "s_addc_u32 s70, s70, 0\n\t" \
    "s_mov_b32 s75, s73\n\t" \
    "s_mov_b32 s76, s74\n\t" \
    "s_lshl_b32 s70, s70, 3\n\t" \
    "s_sub_u32 s73, s56, s70\n\t" \
    "s_lshl_b32 s74, s28, 4\n\t" \
    "s_sub_u32 s69, s73, s75\n\t" \
    "s_sub_u32 s70, s74, s76\n\t" \
    "s_sub_u32 s71, s70, 1600\n\t" \
    "s_cmp_lt_u32 s71, 400000\n\t" \
    "s_cbranch_scc0 vv_K27_%=\n\t" \
    "v_cvt_f32_u32 v141, s69\n\t" \
    "v_cvt_f32_u32 v142, s70\n\t" \
    "v_rcp_f32 v143, v141\n\t" \
    "v_rcp_f32 v144, v142\n\t" \
    "v_mul_f32 v143, v143, v142\n\t" \
    "v_mul_f32 v144, v144, v141\n\t" \
    "v_mul_f32 v143, 0x4f800000, v143\n\t" \
    "v_mul_f32 v144, 0x4f000000, v144\n\t" \
    "v_cvt_u32_f32 v143, v143\n\t" \
    "v_cvt_u32_f32 v144, v144\n\t" \
    "v_readfirstlane_b32 s69, v143\n\t" \
    "v_readfirstlane_b32 s70, v144\n\t" \
    "s_nop 1\n\t" \
    "s_sub_u32 s69, s69, s78\n\t" \
    "s_ashr_i32 s69, s69, 2\n\t" \
    "s_add_u32 s78, s78, s69\n\t" \
    "s_sub_u32 s70, s70, s77\n\t" \
    "s_ashr_i32 s70, s70, 2\n\t" \
    "s_add_u32 s77, s77, s70\n\t" \
    "vv_K27_%=:\n\t" \
    "s_waitcnt vmcnt(6)\n\t" \
    "v_cmp_eq_u32 vcc, -1, v140\n\t" \
    "s_cbranch_vccz vv_Hok29_%=\n\t" \
    "vv_H28_%=:\n\t" \
    "global_load_dword v140, v20, s[46:47] sc1\n\t" \
    "s_waitcnt vmcnt(0)\n\t" \
    "v_cmp_eq_u32 vcc, -1, v140\n\t" \
    "s_cbranch_vccz vv_Hc30_%=\n\t" \
    "s_cmp_eq_u32 s68, 0\n\t" \
    "s_cbranch_scc1 vv_F31_%=\n\t" \
    "s_sub_u32 s68, s68, 1\n\t" \
    "s_sleep 5\n\t" \
    "s_branch vv_H28_%=\n\t" \
    "vv_F31_%=:\n\t" \
    "v_mov_b32 v141, 6095104\n\t" \
    "v_mov_b32 v142, 6\n\t" \
    "global_store_dword v141, v142, s[36:37] sc1\n\t" \
    "s_waitcnt vmcnt(0)\n\t" \
    "s_branch vv_Hc30_%=\n\t" \
    "vv_Hc30_%=:\n\t" \
    "vv_Hok29_%=:\n\t" \
    "v_subrev_u32 v140, s80, v140\n\t" \
    "v_and_b32 v20, -5, v20\n\t" \
    "s_nop 0\n\t" \
    "v_max_i32_dpp v140, v140, v140 row_ror:1 row_mask:0xf bank_mask:0xf\n\t" \
    "s_nop 1\n\t" \
    "v_max_i32_dpp v140, v140, v140 row_ror:2 row_mask:0xf bank_mask:0xf\n\t" \
    "s_nop 1\n\t" \
    "v_max_i32_dpp v140, v140, v140 row_ror:4 row_mask:0xf bank_mask:0xf\n\t" \
    "s_nop 1\n\t" \
    "v_max_i32_dpp v140, v140, v140 row_ror:8 row_mask:0xf bank_mask:0xf\n\t" \
    "s_nop 0\n\t" \
    "v_readlane_b32 s69, v140, 0\n\t" \
    "v_readlane_b32 s71, v140, 16\n\t" \
    "v_readlane_b32 s70, v140, 32\n\t" \
    "v_readlane_b32 s81, v140, 48\n\t" \
    "s_max_i32 s69, s69, s71\n\t" \
    "s_nop 0\n\t" \
    "s_max_i32 s71, s70, s81\n\t" \
    "s_max_i32 s69, s69, s71\n\t" \
    "s_add_u32 s81, s69, s80\n\t" \
    "s_cmp_lg_u32 s66, 0\n\t" \
    "s_cbranch_scc1 vv_SF32_%=\n\t" \
    "s_lshl_b32 s69, s67, 7\n\t" \
    "s_add_u32 s69, s69, 6131712\n\t" \
    "s_add_u32 s69, s69, s52\n\t" \
    "s_add_u32 s70, s65, -1\n\t" \
    "v_mov_b32 v140, s69\n\t" \
    "v_mov_b32 v141, s70\n\t" \
    "s_mov_b64 exec, 1\n\t" \
    "global_store_dword v140, v141, s[36:37] sc1\n\t" \
    "s_mov_b64 exec, -1\n\t" \
    "vv_SF32_%=:\n\t" \
    "v_mov_b32 v72, 0\n\t" \
    "v_mov_b32 v73, 0\n\t" \
    "v_mov_b32 v74, 0\n\t" \
    "v_mov_b32 v75, 0\n\t" \
    "v_mov_b32 v76, 0\n\t" \
    "v_mov_b32 v77, 0\n\t" \
    "v_mov_b32 v78, 0\n\t" \
    "v_mov_b32 v79, 0\n\t" \
    "v_mov_b32 v92, 0\n\t" \
    "v_mov_b32 v93, 0\n\t" \
    "v_mov_b32 v94, 0\n\t" \
    "v_mov_b32 v95, 0\n\t" \
    "v_mov_b32 v96, 0\n\t" \
    "v_mov_b32 v97, 0\n\t" \
    "v_mov_b32 v98, 0\n\t" \
    "v_mov_b32 v99, 0\n\t" \
    "v_mov_b32 v100, 0\n\t" \
    "v_mov_b32 v101, 0\n\t" \
    "v_mov_b32 v102, 0\n\t" \
    "v_mov_b32 v103, 0\n\t" \
    "v_mov_b32 v104, 0\n\t" \
    "v_mov_b32 v105, 0\n\t" \
    "v_mov_b32 v106, 0\n\t" \
    "v_mov_b32 v107, 0\n\t" \
    "v_mov_b32 v108, 0\n\t" \
    "v_mov_b32 v109, 0\n\t" \
    "v_mov_b32 v110, 0\n\t" \
    "v_mov_b32 v111, 0\n\t" \
    "v_mov_b32 v112, 0\n\t" \
    "v_mov_b32 v113, 0\n\t" \
    "v_mov_b32 v114, 0\n\t" \
    "v_mov_b32 v115, 0\n\t" \
    "v_mov_b32 v116, 0\n\t" \
    "v_mov_b32 v117, 0\n\t" \
    "v_mov_b32 v118, 0\n\t" \
    "v_mov_b32 v119, 0\n\t" \
    "v_mov_b32 v120, 0\n\t" \
    "v_mov_b32 v121, 0\n\t" \
    "v_mov_b32 v122, 0\n\t" \
    "v_mov_b32 v123, 0\n\t" \
    "v_mov_b32 v124, 0\n\t" \
    "v_mov_b32 v125, 0\n\t" \
    "v_mov_b32 v126, 0\n\t" \
    "v_mov_b32 v127, 0\n\t" \
    "s_add_u32 s79, s81, 400\n\t" \
    "s_sub_u32 s70, s79, s74\n\t" \
    "s_max_i32 s70, s70, 0\n\t" \
    "s_min_i32 s70, s70, 16000\n\t" \
    "s_lshl_b32 s70, s70, 1\n\t" \
    "s_mul_hi_u32 s70, s70, s77\n\t" \
    "s_add_u32 s70, s70, s73\n\t" \
    "vv_W36_%=:\n\t" \
    "s_memtime s[56:57]\n\t" \
    "s_waitcnt lgkmcnt(0)\n\t" \
    "s_sub_u32 s69, s70, s56\n\t" \
    "s_cmp_gt_i32 s69, 24\n\t" \
    "s_cbranch_scc1 vv_W36_%=\n\t" \
    "global_store_dwordx4 v14, v[148:151], s[42:43] sc1\n\t" \
    "global_store_dwordx4 v15, v[148:151], s[42:43] sc1\n\t" \
    "global_store_dword v16, v148, s[42:43] sc1\n\t" \
    "s_cmp_lg_u32 s65, 0\n\t" \
    "s_cbranch_scc1 vv_Oc35_%=\n\t" \
    "s_add_u32 s79, s81, 240\n\t" \
    "s_sub_u32 s70, s79, s74\n\t" \
    "s_max_i32 s70, s70, 0\n\t" \
    "s_min_i32 s70, s70, 16000\n\t" \
    "s_lshl_b32 s70, s70, 1\n\t" \
    "s_mul_hi_u32 s70, s70, s77\n\t" \
    "s_add_u32 s70, s70, s73\n\t" \
    "vv_W37_%=:\n\t" \
    "s_memtime s[56:57]\n\t" \
    "s_waitcnt lgkmcnt(0)\n\t" \
    "s_sub_u32 s69, s70, s56\n\t" \
    "s_cmp_gt_i32 s69, 24\n\t" \
    "s_cbranch_scc1 vv_W37_%=\n\t" \
    "global_load_dword v140, v35, s[46:47] sc1\n\t" \
    "s_waitcnt vmcnt(0)\n\t" \
    "v_cmp_eq_u32 vcc, -1, v140\n\t" \
    "s_cbranch_vccz vv_Ook34_%=\n\t" \
    "vv_O33_%=:\n\t" \
    "global_load_dword v140, v35, s[46:47] offset:1536 sc1\n\t" \
    "s_waitcnt vmcnt(0)\n\t" \
    "v_cmp_eq_u32 vcc, -1, v140\n\t" \
    "s_cbranch_vccz vv_Ook34_%=\n\t" \
    "s_cmp_eq_u32 s68, 0\n\t" \
    "s_cbranch_scc1 vv_F38_%=\n\t" \
    "s_sub_u32 s68, s68, 1\n\t" \
    "s_sleep 3\n\t" \
    "s_branch vv_O33_%=\n\t" \
    "vv_F38_%=:\n\t" \
    "v_mov_b32 v141, 6095104\n\t" \
    "v_mov_b32 v142, 17\n\t" \
    "global_store_dword v141, v142, s[36:37] sc1\n\t" \
    "s_waitcnt vmcnt(0)\n\t" \
    "s_branch vv_Ook34_%=\n\t" \
    "vv_Ook34_%=:\n\t" \
    "v_subrev_u32 v140, s80, v140\n\t" \
    "s_nop 1\n\t" \
    "v_max_i32_dpp v140, v140, v140 row_ror:1 row_mask:0xf bank_mask:0xf\n\t" \
    "s_nop 1\n\t" \
    "v_max_i32_dpp v140, v140, v140 row_ror:2 row_mask:0xf bank_mask:0xf\n\t" \
    "s_nop 1\n\t" \
    "v_max_i32_dpp v140, v140, v140 row_ror:4 row_mask:0xf bank_mask:0xf\n\t" \
    "s_nop 1\n\t" \
    "v_max_i32_dpp v140, v140, v140 row_ror:8 row_mask:0xf bank_mask:0xf\n\t" \
    "s_nop 0\n\t" \
    "v_readlane_b32 s69, v140, 0\n\t" \
    "v_readlane_b32 s71, v140, 16\n\t" \
    "v_readlane_b32 s70, v140, 32\n\t" \
    "v_readlane_b32 s81, v140, 48\n\t" \
    "s_max_i32 s69, s69, s71\n\t" \
    "s_nop 0\n\t" \
    "s_max_i32 s71, s70, s81\n\t" \
    "s_max_i32 s69, s69, s71\n\t" \
    "s_add_u32 s81, s69, s80\n\t" \
    "vv_Oc35_%=:\n\t" \
    "s_mul_hi_u32 s69, s85, s78\n\t" \
    "s_add_u32 s79, s81, s69\n\t" \
    "s_add_u32 s79, s79, s87\n\t" \
    "s_cmp_ge_u32 s65, 36\n\t" \
    "s_cselect_b32 s69, 28, 0\n\t" \
    "s_sub_u32 s79, s79, s69\n\t" \
    "s_add_u32 s80, s79, s88\n\t" \
    "s_add_u32 s65, s65, 1\n\t" \
    "s_branch vv_go15_%=\n\t" \
    "vv_Gbad20_%=:\n\t" \
    "s_waitcnt vmcnt(0) lgkmcnt(0)\n\t" \
    "s_nop 7\n\t" \
    "v_mov_b32 v72, 0\n\t" \
    "v_mov_b32 v73, 0\n\t" \
    "v_mov_b32 v74, 0\n\t" \
    "v_mov_b32 v75, 0\n\t" \
    "v_mov_b32 v76, 0\n\t" \
    "v_mov_b32 v77, 0\n\t" \
    "v_mov_b32 v78, 0\n\t" \
    "v_mov_b32 v79, 0\n\t" \
    "s_cmp_eq_u32 s68, 0\n\t" \
    "s_cbranch_scc1 vv_F39_%=\n\t" \
    "s_sub_u32 s68, s68, 1\n\t" \
    "s_branch vv_G19_%=\n\t" \
    "vv_F39_%=:\n\t" \
    "v_mov_b32 v140, 6095104\n\t" \
    "v_mov_b32 v141, 1\n\t" \
    "global_store_dword v140, v141, s[36:37] sc1\n\t" \
    "s_waitcnt vmcnt(0)\n\t" \
    "s_branch vv_Gc22_%=\n\t" \
    "vv_Gbad221_%=:\n\t" \
    "global_load_dwordx4 v[44:47], v2, s[46:47] sc1\n\t" \
    "s_waitcnt vmcnt(0)\n\t" \
    "v_max3_u32 v140, v44, v45, v46\n\t" \
    "v_max_u32 v140, v140, v47\n\t" \
    "v_cmp_eq_u32 vcc, -1, v140\n\t" \
    "s_cbranch_vccz vv_Gre224_%=\n\t" \
    "s_cmp_eq_u32 s68, 0\n\t" \
    "s_cbranch_scc1 vv_F40_%=\n\t" \
    "s_sub_u32 s68, s68, 1\n\t" \
    "s_sleep 1\n\t" \
    "s_branch vv_Gbad221_%=\n\t" \
    "vv_F40_%=:\n\t" \
    "v_mov_b32 v140, 6095104\n\t" \
    "v_mov_b32 v141, 2\n\t" \
    "global_store_dword v140, v141, s[36:37] sc1\n\t" \
    "s_waitcnt vmcnt(0)\n\t" \
    "s_branch vv_Gre224_%=\n\t" \
    "vv_Gre224_%=:\n\t" \
    "v_mov_b32 v72, 0\n\t" \
    "v_mov_b32 v73, 0\n\t" \
    "v_mov_b32 v74, 0\n\t" \
    "v_mov_b32 v75, 0\n\t" \
    "v_mov_b32 v76, 0\n\t" \
    "v_mov_b32 v77, 0\n\t" \
    "v_mov_b32 v78, 0\n\t" \
    "v_mov_b32 v79, 0\n\t" \
    "v_smfmac_f32_16x16x64_bf16 v[72:75], v[36:39], a[0:7], v10\n\t" \
    "s_nop 0\n\t" \
    "v_smfmac_f32_16x16x64_bf16 v[76:79], v[36:39], a[72:79], v10\n\t" \
    "v_smfmac_f32_16x16x64_bf16 v[72:75], v[40:43], a[8:15], v10\n\t" \
    "v_smfmac_f32_16x16x64_bf16 v[76:79], v[40:43], a[80:87], v10\n\t" \
    "v_smfmac_f32_16x16x64_bf16 v[72:75], v[44:47], a[64:71], v10\n\t" \
    "v_smfmac_f32_16x16x64_bf16 v[76:79], v[44:47], a[136:143], v10\n\t" \
    "s_branch vv_Gc223_%=\n\t" \
    "vv_end14_%=:\n\t" \
    "s_endpgm\n\t" \
    "vv_loop11_%=:\n\t" \
    "v_mov_b32 v72, 0\n\t" \
    "v_mov_b32 v73, 0\n\t" \
    "v_mov_b32 v74, 0\n\t" \
    "v_mov_b32 v75, 0\n\t" \
    "v_mov_b32 v76, 0\n\t" \
    "v_mov_b32 v77, 0\n\t" \
    "v_mov_b32 v78, 0\n\t" \
    "v_mov_b32 v79, 0\n\t" \
    "v_mov_b32 v92, 0\n\t" \
    "v_mov_b32 v93, 0\n\t" \
    "v_mov_b32 v94, 0\n\t" \
    "v_mov_b32 v95, 0\n\t" \
    "v_mov_b32 v96, 0\n\t" \
    "v_mov_b32 v97, 0\n\t" \
    "v_mov_b32 v98, 0\n\t" \
    "v_mov_b32 v99, 0\n\t" \
    "v_mov_b32 v100, 0\n\t" \
    "v_mov_b32 v101, 0\n\t" \
    "v_mov_b32 v102, 0\n\t" \
    "v_mov_b32 v103, 0\n\t" \
    "v_mov_b32 v104, 0\n\t" \
    "v_mov_b32 v105, 0\n\t" \
    "v_mov_b32 v106, 0\n\t" \
    "v_mov_b32 v107, 0\n\t" \
    "v_mov_b32 v108, 0\n\t" \
    "v_mov_b32 v109, 0\n\t" \
    "v_mov_b32 v110, 0\n\t" \
    "v_mov_b32 v111, 0\n\t" \
    "v_mov_b32 v112, 0\n\t" \
    "v_mov_b32 v113, 0\n\t" \
    "v_mov_b32 v114, 0\n\t" \
    "v_mov_b32 v115, 0\n\t" \
    "v_mov_b32 v116, 0\n\t" \
    "v_mov_b32 v117, 0\n\t" \
    "v_mov_b32 v118, 0\n\t" \
    "v_mov_b32 v119, 0\n\t" \
    "v_mov_b32 v120, 0\n\t" \
    "v_mov_b32 v121, 0\n\t" \
    "v_mov_b32 v122, 0\n\t" \
    "v_mov_b32 v123, 0\n\t" \
    "v_mov_b32 v124, 0\n\t" \
    "v_mov_b32 v125, 0\n\t" \
    "v_mov_b32 v126, 0\n\t" \
    "v_mov_b32 v127, 0\n\t" \
    "vv_go42_%=:\n\t" \
    "s_add_u32 s69, s65, 0\n\t" \
    "s_bitcmp1_b32 s69, 0\n\t" \
    "s_cselect_b32 s70, s63, s62\n\t" \
    "s_bfe_u32 s69, s69, 0x20001\n\t" \
    "s_lshl_b32 s69, s69, 19\n\t" \
    "s_add_u32 s69, s69, s70\n\t" \
    "s_add_u32 s46, s36, s69\n\t" \
    "s_addc_u32 s47, s37, 0\n\t" \
    "s_add_u32 s69, s65, 0\n\t" \
    "s_mul_i32 s70, s69, 171\n\t" \
    "s_lshr_b32 s70, s70, 9\n\t" \
    "s_mul_i32 s70, s70, 3\n\t" \
    "s_sub_u32 s69, s69, s70\n\t" \
    "s_mul_i32 s69, s69, 36\n\t" \
    "s_lshl_b32 s69, s69, 14\n\t" \
    "s_add_u32 s69, s69, s64\n\t" \
    "s_add_u32 s69, s69, 4194304\n\t" \
    "s_add_u32 s38, s36, s69\n\t" \
    "s_addc_u32 s39, s37, 0\n\t" \
    "s_add_u32 s69, s65, 2\n\t" \
    "s_mul_i32 s70, s69, 171\n\t" \
    "s_lshr_b32 s70, s70, 9\n\t" \
    "s_mul_i32 s70, s70, 3\n\t" \
    "s_sub_u32 s69, s69, s70\n\t" \
    "s_mul_i32 s69, s69, 36\n\t" \
    "s_lshl_b32 s69, s69, 14\n\t" \
    "s_add_u32 s69, s69, s64\n\t" \
    "s_add_u32 s69, s69, 4194304\n\t" \
    "s_add_u32 s42, s36, s69\n\t" \
    "s_addc_u32 s43, s37, 0\n\t" \
    "s_mul_i32 s69, s65, 9216\n\t" \
    "s_add_u32 s69, s69, 123731968\n\t" \
    "s_add_u32 s40, s36, s69\n\t" \
    "s_addc_u32 s41, s37, 0\n\t" \
    "s_sub_u32 s70, s79, s74\n\t" \
    "s_max_i32 s70, s70, 0\n\t" \
    "s_min_i32 s70, s70, 16000\n\t" \
    "s_lshl_b32 s70, s70, 1\n\t" \
    "s_mul_hi_u32 s70, s70, s77\n\t" \
    "s_add_u32 s70, s70, s73\n\t" \
    "s_sub_u32 s70, s70, 100\n\t" \
    "s_getpc_b64 s[30:31]\n\t" \
    "vv_Wp44_%=:\n\t" \
    "s_add_u32 s30, s30, vv_We45_%=-vv_Wp44_%=\n\t" \
    "s_addc_u32 s31, s31, 0\n\t" \
    "vv_W43_%=:\n\t" \
    "s_memtime s[56:57]\n\t" \
    "s_waitcnt lgkmcnt(0)\n\t" \
    "s_sub_u32 s69, s70, s56\n\t" \
    "s_cmp_gt_i32 s69, 111\n\t" \
    "s_cbranch_scc1 vv_W43_%=\n\t" \
    "s_max_i32 s69, s69, 0\n\t" \
    "s_lshr_b32 s69, s69, 1\n\t" \
    "s_and_b32 s69, s69, 0x3c\n\t" \
    "s_sub_u32 s30, s30, s69\n\t" \
    "s_subb_u32 s31, s31, 0\n\t" \
    "s_setpc_b64 s[30:31]\n\t" \
    ".p2align 6\n\t" \
    "s_nop 0\n\t" \
    "s_nop 0\n\t" \
    "s_nop 0\n\t" \
    "s_nop 1\n\t" \
    "s_nop 1\n\t" \
    "s_nop 1\n\t" \
    "s_nop 1\n\t" \
    "s_nop 1\n\t" \
    "s_nop 1\n\t" \
    "s_nop 1\n\t" \
    "s_nop 1\n\t" \
    "s_nop 1\n\t" \
    "s_nop 1\n\t" \
    "s_nop 1\n\t" \
    "s_nop 1\n\t" \
    "s_nop 1\n\t" \
    "vv_We45_%=:\n\t" \
    "vv_G46_%=:\n\t" \
    "global_load_dwordx4 v[36:39], v1, s[46:47] sc1\n\t" \
    "s_nop 9\n\t" \
    "global_load_dwordx4 v[40:43], v1, s[46:47] offset:1024 sc1\n\t" \
    "s_nop 9\n\t" \
    "global_load_dwordx4 v[44:47], v2, s[46:47] sc1\n\t" \
    "s_waitcnt vmcnt(2)\n\t" \
    "v_smfmac_f32_16x16x64_bf16 v[72:75], v[36:39], a[16:23], v10\n\t" \
    "ds_write_b128 v0, v[36:39] offset:2048\n\t" \
    "v_smfmac_f32_16x16x64_bf16 v[76:79], v[36:39], a[88:95], v10\n\t" \
    "v_max3_u32 v140, v36, v37, v38\n\t" \
    "s_waitcnt vmcnt(1)\n\t" \
    "ds_write_b128 v0, v[40:43] offset:3072\n\t" \
    "v_smfmac_f32_16x16x64_bf16 v[72:75], v[40:43], a[24:31], v10\n\t" \
    "v_max3_u32 v140, v140, v39, v40\n\t" \
    "v_max3_u32 v140, v140, v41, v42\n\t" \
    "v_max_u32 v140, v140, v43\n\t" \
    "v_cmp_eq_u32 vcc, -1, v140\n\t" \
    "v_smfmac_f32_16x16x64_bf16 v[76:79], v[40:43], a[96:103], v10\n\t" \
    "s_cbranch_vccnz vv_Gbad47_%=\n\t" \
    "vv_Gc49_%=:\n\t" \
    "s_waitcnt lgkmcnt(0)\n\t" \
    "s_barrier\n\t" \
    "ds_read_b128 v[48:51], v0 offset:4096\n\t" \
    "ds_read_b128 v[52:55], v0 offset:5120\n\t" \
    "s_waitcnt vmcnt(0)\n\t" \
    "v_smfmac_f32_16x16x64_bf16 v[72:75], v[44:47], a[64:71], v10\n\t" \
    "ds_read_b128 v[56:59], v0 offset:6144\n\t" \
    "ds_read_b128 v[60:63], v0 offset:7168\n\t" \
    "v_max3_u32 v140, v44, v45, v46\n\t" \
    "v_max_u32 v140, v140, v47\n\t" \
    "v_smfmac_f32_16x16x64_bf16 v[76:79], v[44:47], a[136:143], v10\n\t" \
    "ds_read_b128 v[64:67], v0 offset:0\n\t" \
    "ds_read_b128 v[68:71], v0 offset:1024\n\t" \
    "v_cmp_eq_u32 vcc, -1, v140\n\t" \
    "s_cbranch_vccnz vv_Gbad248_%=\n\t" \
    "vv_Gc250_%=:\n\t" \
    "s_waitcnt lgkmcnt(5)\n\t" \
    "v_smfmac_f32_16x16x64_bf16 v[72:75], v[48:51], a[32:39], v10\n\t" \
    "s_waitcnt lgkmcnt(4)\n\t" \
    "v_smfmac_f32_16x16x64_bf16 v[72:75], v[52:55], a[40:47], v10\n\t" \
    "s_waitcnt lgkmcnt(3)\n\t" \
    "v_smfmac_f32_16x16x64_bf16 v[72:75], v[56:59], a[48:55], v10\n\t" \
    "s_waitcnt lgkmcnt(2)\n\t" \
    "v_smfmac_f32_16x16x64_bf16 v[72:75], v[60:63], a[56:63], v10\n\t" \
    "s_waitcnt lgkmcnt(1)\n\t" \
    "v_smfmac_f32_16x16x64_bf16 v[72:75], v[64:67], a[0:7], v10\n\t" \
    "s_waitcnt lgkmcnt(0)\n\t" \
    "v_smfmac_f32_16x16x64_bf16 v[72:75], v[68:71], a[8:15], v10\n\t" \
    "v_smfmac_f32_16x16x64_bf16 v[76:79], v[48:51], a[104:111], v10\n\t" \
    "s_nop 1\n\t" \
    "s_nop 0\n\t" \
    "v_smfmac_f32_16x16x64_bf16 v[76:79], v[52:55], a[112:119], v10\n\t" \
    "s_nop 1\n\t" \
    "s_nop 0\n\t" \
    "v_smfmac_f32_16x16x64_bf16 v[76:79], v[56:59], a[120:127], v10\n\t" \
    "v_add_f32 v72, v72, v74\n\t" \
    "v_smfmac_f32_16x16x64_bf16 v[76:79], v[60:63], a[128:135], v10\n\t" \
    "v_add_f32 v73, v73, v75\n\t" \
    "v_smfmac_f32_16x16x64_bf16 v[76:79], v[64:67], a[72:79], v10\n\t" \
    "v_cvt_pk_bf16_f32 v80, v72, v73\n\t" \
    "v_smfmac_f32_16x16x64_bf16 v[76:79], v[68:71], a[80:87], v10\n\t" \
    "ds_write_b32 v12, v80 offset:0\n\t" \
    "s_nop 6\n\t" \
    "v_add_f32 v76, v76, v78\n\t" \
    "v_add_f32 v77, v77, v79\n\t" \
    "v_cvt_pk_bf16_f32 v81, v76, v77\n\t" \
    "ds_write_b32 v12, v81 offset:64\n\t" \
    "s_waitcnt lgkmcnt(0)\n\t" \
    "s_barrier\n\t" \
    "ds_read_b64_tr_b16 v[84:85], v13 offset:0\n\t" \
    "ds_read_b64_tr_b16 v[86:87], v13 offset:128\n\t" \
    "ds_read_b64_tr_b16 v[88:89], v13 offset:1024\n\t" \
    "ds_read_b64_tr_b16 v[90:91], v13 offset:1152\n\t" \
    "s_waitcnt lgkmcnt(2)\n\t" \
    "v_smfmac_f32_16x16x64_bf16 v[92:95], v[84:87], a[144:151], v10\n\t" \
    "s_nop 2\n\t" \
    "s_waitcnt lgkmcnt(0)\n\t" \
    "v_smfmac_f32_16x16x64_bf16 v[92:95], v[88:91], a[152:159], v10\n\t" \
    "s_nop 2\n\t" \
    "v_smfmac_f32_16x16x64_bf16 v[96:99], v[84:87], a[160:167], v10\n\t" \
    "s_nop 2\n\t" \
    "v_smfmac_f32_16x16x64_bf16 v[96:99], v[88:91], a[168:175], v10\n\t" \
    "s_nop 2\n\t" \
    "v_smfmac_f32_16x16x64_bf16 v[100:103], v[84:87], a[176:183], v10\n\t" \
    "s_nop 0\n\t" \
    "v_add_f32 v92, v92, v94\n\t" \
    "v_add_f32 v93, v93, v95\n\t" \
    "v_smfmac_f32_16x16x64_bf16 v[100:103], v[88:91], a[184:191], v10\n\t" \
    "s_nop 1\n\t" \
    "v_cvt_pk_bf16_f32 v128, v92, v93\n\t" \
    "v_smfmac_f32_16x16x64_bf16 v[104:107], v[84:87], a[192:199], v10\n\t" \
    "s_nop 0\n\t" \
    "v_add_f32 v96, v96, v98\n\t" \
    "v_add_f32 v97, v97, v99\n\t" \
    "v_smfmac_f32_16x16x64_bf16 v[104:107], v[88:91], a[200:207], v10\n\t" \
    "s_nop 1\n\t" \
    "v_cvt_pk_bf16_f32 v129, v96, v97\n\t" \
    "v_smfmac_f32_16x16x64_bf16 v[124:127], v[84:87], v[240:247], v10\n\t" \
    "s_nop 0\n\t" \
    "v_add_f32 v100, v100, v102\n\t" \
    "v_add_f32 v101, v101, v103\n\t" \
    "v_smfmac_f32_16x16x64_bf16 v[124:127], v[88:91], v[248:255], v10\n\t" \
    "s_nop 1\n\t" \
    "global_load_dword v140, v20, s[46:47] sc1\n\t" \
    "v_cvt_pk_bf16_f32 v130, v100, v101\n\t" \
    "v_smfmac_f32_16x16x64_bf16 v[108:111], v[84:87], a[208:215], v10\n\t" \
    "s_nop 0\n\t" \
    "v_add_f32 v104, v104, v106\n\t" \
    "v_add_f32 v105, v105, v107\n\t" \
    "v_smfmac_f32_16x16x64_bf16 v[108:111], v[88:91], a[216:223], v10\n\t" \
    "s_nop 0\n\t" \
    "v_cvt_pk_bf16_f32 v131, v104, v105\n\t" \
    "v_permlane32_swap_b32 v128, v130\n\t" \
    "v_smfmac_f32_16x16x64_bf16 v[112:115], v[84:87], a[224:231], v10\n\t" \
    "s_nop 0\n\t" \
    "v_permlane32_swap_b32 v129, v131\n\t" \
    "s_nop 1\n\t" \
    "v_permlane16_swap_b32 v128, v129\n\t" \
    "v_smfmac_f32_16x16x64_bf16 v[112:115], v[88:91], a[232:239], v10\n\t" \
    "v_permlane16_swap_b32 v130, v131\n\t" \
    "global_store_dwordx4 v14, v[128:131], s[38:39] sc1\n\t" \
    "v_add_f32 v124, v124, v126\n\t" \
    "v_smfmac_f32_16x16x64_bf16 v[116:119], v[84:87], a[240:247], v10\n\t" \
    "s_nop 0\n\t" \
    "v_add_f32 v125, v125, v127\n\t" \
    "v_cvt_pk_bf16_f32 v136, v124, v125\n\t" \
    "v_smfmac_f32_16x16x64_bf16 v[116:119], v[88:91], a[248:255], v10\n\t" \
    "global_store_dword v16, v136, s[38:39] sc1\n\t" \
    "v_add_f32 v108, v108, v110\n\t" \
    "v_add_f32 v109, v109, v111\n\t" \
    "v_smfmac_f32_16x16x64_bf16 v[120:123], v[84:87], v[224:231], v10\n\t" \
    "s_nop 0\n\t" \
    "v_cvt_pk_bf16_f32 v132, v108, v109\n\t" \
    "v_add_f32 v112, v112, v114\n\t" \
    "v_smfmac_f32_16x16x64_bf16 v[120:123], v[88:91], v[232:239], v10\n\t" \
    "v_add_f32 v113, v113, v115\n\t" \
    "v_cvt_pk_bf16_f32 v133, v112, v113\n\t" \
    "v_add_f32 v116, v116, v118\n\t" \
    "v_add_f32 v117, v117, v119\n\t" \
    "v_cvt_pk_bf16_f32 v134, v116, v117\n\t" \
    "s_nop 2\n\t" \
    "v_pk_add_f32 v[120:121], v[120:121], v[122:123]\n\t" \
    "v_cvt_pk_bf16_f32 v135, v120, v121\n\t" \
    "v_permlane32_swap_b32 v132, v134\n\t" \
    "s_nop 0\n\t" \
    "v_permlane32_swap_b32 v133, v135\n\t" \
    "s_nop 1\n\t" \
    "v_permlane16_swap_b32 v132, v133\n\t" \
    "v_permlane16_swap_b32 v134, v135\n\t" \
    "global_store_dwordx4 v15, v[132:135], s[38:39] sc1\n\t" \
    "s_nop 1\n\t" \
    "global_store_dwordx4 v17, v[128:131], s[40:41] sc1\n\t" \
    "global_store_dwordx4 v17, v[132:135], s[40:41] offset:1024 sc1\n\t" \
    "global_store_dword v18, v136, s[40:41] sc1\n\t" \
    "s_cmp_eq_u32 s65, 63\n\t" \
    "s_cbranch_scc1 vv_end41_%=\n\t" \
    "s_mov_b32 s71, 3\n\t" \
    "vv_E52_%=:\n\t" \
    "s_memtime s[54:55]\n\t" \
    "s_nop 0\n\t" \
    "s_memrealtime s[4:5]\n\t" \
    "s_nop 0\n\t" \
    "s_memrealtime s[6:7]\n\t" \
    "s_nop 0\n\t" \
    "s_memrealtime s[8:9]\n\t" \
    "s_nop 0\n\t" \
    "s_memrealtime s[10:11]\n\t" \
    "s_nop 0\n\t" \
    "s_memrealtime s[12:13]\n\t" \
    "s_nop 0\n\t" \
    "s_memrealtime s[14:15]\n\t" \
    "s_nop 0\n\t" \
    "s_memrealtime s[16:17]\n\t" \
    "s_nop 0\n\t" \
    "s_memrealtime s[18:19]\n\t" \
    "s_nop 0\n\t" \
    "s_memrealtime s[20:21]\n\t" \
    "s_nop 0\n\t" \
    "s_memrealtime s[22:23]\n\t" \
    "s_nop 0\n\t" \
    "s_memrealtime s[24:25]\n\t" \
    "s_nop 0\n\t" \
    "s_memrealtime s[26:27]\n\t" \
    "s_nop 0\n\t" \
    "s_memrealtime s[28:29]\n\t" \
    "s_nop 0\n\t" \
    "s_memtime s[56:57]\n\t" \
    "s_mov_b32 s70, 1\n\t" \
    "s_sub_u32 s71, s71, 1\n\t" \
    "s_cselect_b32 s69, 0, -1\n\t" \
    "s_waitcnt lgkmcnt(0)\n\t" \
    "s_sub_u32 s54, s56, s54\n\t" \
    "s_and_b32 s54, s54, s69\n\t" \
    "s_cmp_gt_u32 s54, 124\n\t" \
    "s_cbranch_scc1 vv_E52_%=\n\t" \
    "s_cmp_eq_u32 s4, s28\n\t" \
    "s_addc_u32 s70, s70, 0\n\t" \
    "s_cmp_eq_u32 s6, s28\n\t" \
    "s_addc_u32 s70, s70, 0\n\t" \
    "s_cmp_eq_u32 s8, s28\n\t" \
    "s_addc_u32 s70, s70, 0\n\t" \
    "s_cmp_eq_u32 s10, s28\n\t" \
    "s_addc_u32 s70, s70, 0\n\t" \
    "s_cmp_eq_u32 s12, s28\n\t" \
    "s_addc_u32 s70, s70, 0\n\t" \
    "s_cmp_eq_u32 s14, s28\n\t" \
    "s_addc_u32 s70, s70, 0\n\t" \
    "s_cmp_eq_u32 s16, s28\n\t" \
    "s_addc_u32 s70, s70, 0\n\t" \
    "s_cmp_eq_u32 s18, s28\n\t" \
    "s_addc_u32 s70, s70, 0\n\t" \
    "s_cmp_eq_u32 s20, s28\n\t" \
    "s_addc_u32 s70, s70, 0\n\t" \
    "s_cmp_eq_u32 s22, s28\n\t" \
    "s_addc_u32 s70, s70, 0\n\t" \
    "s_cmp_eq_u32 s24, s28\n\t" \
    "s_addc_u32 s70, s70, 0\n\t" \
    "s_cmp_eq_u32 s26, s28\n\t" \
    "s_addc_u32 s70, s70, 0\n\t" \
    "s_mov_b32 s75, s73\n\t" \
    "s_mov_b32 s76, s74\n\t" \
    "s_lshl_b32 s70, s70, 3\n\t" \
    "s_sub_u32 s73, s56, s70\n\t" \
    "s_lshl_b32 s74, s28, 4\n\t" \
    "s_sub_u32 s69, s73, s75\n\t" \
    "s_sub_u32 s70, s74, s76\n\t" \
    "s_sub_u32 s71, s70, 1600\n\t" \
    "s_cmp_lt_u32 s71, 400000\n\t" \
    "s_cbranch_scc0 vv_K54_%=\n\t" \
    "v_cvt_f32_u32 v141, s69\n\t" \
    "v_cvt_f32_u32 v142, s70\n\t" \
    "v_rcp_f32 v143, v141\n\t" \
    "v_rcp_f32 v144, v142\n\t" \
    "v_mul_f32 v143, v143, v142\n\t" \
    "v_mul_f32 v144, v144, v141\n\t" \
    "v_mul_f32 v143, 0x4f800000, v143\n\t" \
    "v_mul_f32 v144, 0x4f000000, v144\n\t" \
    "v_cvt_u32_f32 v143, v143\n\t" \
    "v_cvt_u32_f32 v144, v144\n\t" \
    "v_readfirstlane_b32 s69, v143\n\t" \
    "v_readfirstlane_b32 s70, v144\n\t" \
    "s_nop 1\n\t" \
    "s_sub_u32 s69, s69, s78\n\t" \
    "s_ashr_i32 s69, s69, 2\n\t" \
    "s_add_u32 s78, s78, s69\n\t" \
    "s_sub_u32 s70, s70, s77\n\t" \
    "s_ashr_i32 s70, s70, 2\n\t" \
    "s_add_u32 s77, s77, s70\n\t" \
    "vv_K54_%=:\n\t" \
    "s_waitcnt vmcnt(6)\n\t" \
    "v_cmp_eq_u32 vcc, -1, v140\n\t" \
    "s_cbranch_vccz vv_Hok56_%=\n\t" \
    "vv_H55_%=:\n\t" \
    "global_load_dword v140, v20, s[46:47] sc1\n\t" \
    "s_waitcnt vmcnt(0)\n\t" \
    "v_cmp_eq_u32 vcc, -1, v140\n\t" \
    "s_cbranch_vccz vv_Hc57_%=\n\t" \
    "s_cmp_eq_u32 s68, 0\n\t" \
    "s_cbranch_scc1 vv_F58_%=\n\t" \
    "s_sub_u32 s68, s68, 1\n\t" \
    "s_sleep 5\n\t" \
    "s_branch vv_H55_%=\n\t" \
    "vv_F58_%=:\n\t" \
    "v_mov_b32 v141, 6095104\n\t" \
    "v_mov_b32 v142, 6\n\t" \
    "global_store_dword v141, v142, s[36:37] sc1\n\t" \
    "s_waitcnt vmcnt(0)\n\t" \
    "s_branch vv_Hc57_%=\n\t" \
    "vv_Hc57_%=:\n\t" \
    "vv_Hok56_%=:\n\t" \
    "v_subrev_u32 v140, s80, v140\n\t" \
    "v_and_b32 v20, -5, v20\n\t" \
    "s_nop 0\n\t" \
    "v_max_i32_dpp v140, v140, v140 row_ror:1 row_mask:0xf bank_mask:0xf\n\t" \
    "s_nop 1\n\t" \
    "v_max_i32_dpp v140, v140, v140 row_ror:2 row_mask:0xf bank_mask:0xf\n\t" \
    "s_nop 1\n\t" \
    "v_max_i32_dpp v140, v140, v140 row_ror:4 row_mask:0xf bank_mask:0xf\n\t" \
    "s_nop 1\n\t" \
    "v_max_i32_dpp v140, v140, v140 row_ror:8 row_mask:0xf bank_mask:0xf\n\t" \
    "s_nop 0\n\t" \
    "v_readlane_b32 s69, v140, 0\n\t" \
    "v_readlane_b32 s71, v140, 16\n\t" \
    "v_readlane_b32 s70, v140, 32\n\t" \
    "v_readlane_b32 s81, v140, 48\n\t" \
    "s_max_i32 s69, s69, s71\n\t" \
    "s_nop 0\n\t" \
    "s_max_i32 s71, s70, s81\n\t" \
    "s_max_i32 s69, s69, s71\n\t" \
    "s_add_u32 s81, s69, s80\n\t" \
    "v_mov_b32 v72, 0\n\t" \
    "v_mov_b32 v73, 0\n\t" \
    "v_mov_b32 v74, 0\n\t" \
    "v_mov_b32 v75, 0\n\t" \
    "v_mov_b32 v76, 0\n\t" \
    "v_mov_b32 v77, 0\n\t" \
    "v_mov_b32 v78, 0\n\t" \
    "v_mov_b32 v79, 0\n\t" \
    "v_mov_b32 v92, 0\n\t" \
    "v_mov_b32 v93, 0\n\t" \
    "v_mov_b32 v94, 0\n\t" \
    "v_mov_b32 v95, 0\n\t" \
    "v_mov_b32 v96, 0\n\t" \
    "v_mov_b32 v97, 0\n\t" \
    "v_mov_b32 v98, 0\n\t" \
    "v_mov_b32 v99, 0\n\t" \
    "v_mov_b32 v100, 0\n\t" \
    "v_mov_b32 v101, 0\n\t" \
    "v_mov_b32 v102, 0\n\t" \
    "v_mov_b32 v103, 0\n\t" \
    "v_mov_b32 v104, 0\n\t" \
    "v_mov_b32 v105, 0\n\t" \
    "v_mov_b32 v106, 0\n\t" \
    "v_mov_b32 v107, 0\n\t" \
    "v_mov_b32 v108, 0\n\t" \
    "v_mov_b32 v109, 0\n\t" \
    "v_mov_b32 v110, 0\n\t" \
    "v_mov_b32 v111, 0\n\t" \
    "v_mov_b32 v112, 0\n\t" \
    "v_mov_b32 v113, 0\n\t" \
    "v_mov_b32 v114, 0\n\t" \
    "v_mov_b32 v115, 0\n\t" \
    "v_mov_b32 v116, 0\n\t" \
    "v_mov_b32 v117, 0\n\t" \
    "v_mov_b32 v118, 0\n\t" \
    "v_mov_b32 v119, 0\n\t" \
    "v_mov_b32 v120, 0\n\t" \
    "v_mov_b32 v121, 0\n\t" \
    "v_mov_b32 v122, 0\n\t" \
    "v_mov_b32 v123, 0\n\t" \
    "v_mov_b32 v124, 0\n\t" \
    "v_mov_b32 v125, 0\n\t" \
    "v_mov_b32 v126, 0\n\t" \
    "v_mov_b32 v127, 0\n\t" \
    "s_add_u32 s79, s81, 400\n\t" \
    "s_sub_u32 s70, s79, s74\n\t" \
    "s_max_i32 s70, s70, 0\n\t" \
    "s_min_i32 s70, s70, 16000\n\t" \
    "s_lshl_b32 s70, s70, 1\n\t" \
    "s_mul_hi_u32 s70, s70, s77\n\t" \
    "s_add_u32 s70, s70, s73\n\t" \
    "vv_W62_%=:\n\t" \
    "s_memtime s[56:57]\n\t" \
    "s_waitcnt lgkmcnt(0)\n\t" \
    "s_sub_u32 s69, s70, s56\n\t" \
    "s_cmp_gt_i32 s69, 24\n\t" \
    "s_cbranch_scc1 vv_W62_%=\n\t" \
    "global_store_dwordx4 v14, v[148:151], s[42:43] sc1\n\t" \
    "global_store_dwordx4 v15, v[148:151], s[42:43] sc1\n\t" \
    "global_store_dword v16, v148, s[42:43] sc1\n\t" \
    "s_cmp_lg_u32 s65, 0\n\t" \
    "s_cbranch_scc1 vv_Oc61_%=\n\t" \
    "s_add_u32 s79, s81, 240\n\t" \
    "s_sub_u32 s70, s79, s74\n\t" \
    "s_max_i32 s70, s70, 0\n\t" \
    "s_min_i32 s70, s70, 16000\n\t" \
    "s_lshl_b32 s70, s70, 1\n\t" \
    "s_mul_hi_u32 s70, s70, s77\n\t" \
    "s_add_u32 s70, s70, s73\n\t" \
    "vv_W63_%=:\n\t" \
    "s_memtime s[56:57]\n\t" \
    "s_waitcnt lgkmcnt(0)\n\t" \
    "s_sub_u32 s69, s70, s56\n\t" \
    "s_cmp_gt_i32 s69, 24\n\t" \
    "s_cbranch_scc1 vv_W63_%=\n\t" \
    "global_load_dword v140, v35, s[46:47] sc1\n\t" \
    "s_waitcnt vmcnt(0)\n\t" \
    "v_cmp_eq_u32 vcc, -1, v140\n\t" \
    "s_cbranch_vccz vv_Ook60_%=\n\t" \
    "vv_O59_%=:\n\t" \
    "global_load_dword v140, v35, s[46:47] offset:1536 sc1\n\t" \
    "s_waitcnt vmcnt(0)\n\t" \
    "v_cmp_eq_u32 vcc, -1, v140\n\t" \
    "s_cbranch_vccz vv_Ook60_%=\n\t" \
    "s_cmp_eq_u32 s68, 0\n\t" \
    "s_cbranch_scc1 vv_F64_%=\n\t" \
    "s_sub_u32 s68, s68, 1\n\t" \
    "s_sleep 3\n\t" \
    "s_branch vv_O59_%=\n\t" \
    "vv_F64_%=:\n\t" \
    "v_mov_b32 v141, 6095104\n\t" \
    "v_mov_b32 v142, 17\n\t" \
    "global_store_dword v141, v142, s[36:37] sc1\n\t" \
    "s_waitcnt vmcnt(0)\n\t" \
    "s_branch vv_Ook60_%=\n\t" \
    "vv_Ook60_%=:\n\t" \
    "v_subrev_u32 v140, s80, v140\n\t" \
    "s_nop 1\n\t" \
    "v_max_i32_dpp v140, v140, v140 row_ror:1 row_mask:0xf bank_mask:0xf\n\t" \
    "s_nop 1\n\t" \
    "v_max_i32_dpp v140, v140, v140 row_ror:2 row_mask:0xf bank_mask:0xf\n\t" \
    "s_nop 1\n\t" \
    "v_max_i32_dpp v140, v140, v140 row_ror:4 row_mask:0xf bank_mask:0xf\n\t" \
    "s_nop 1\n\t" \
    "v_max_i32_dpp v140, v140, v140 row_ror:8 row_mask:0xf bank_mask:0xf\n\t" \
    "s_nop 0\n\t" \
    "v_readlane_b32 s69, v140, 0\n\t" \
    "v_readlane_b32 s71, v140, 16\n\t" \
    "v_readlane_b32 s70, v140, 32\n\t" \
    "v_readlane_b32 s81, v140, 48\n\t" \
    "s_max_i32 s69, s69, s71\n\t" \
    "s_nop 0\n\t" \
    "s_max_i32 s71, s70, s81\n\t" \
    "s_max_i32 s69, s69, s71\n\t" \
    "s_add_u32 s81, s69, s80\n\t" \
    "vv_Oc61_%=:\n\t" \
    "s_mul_hi_u32 s69, s85, s78\n\t" \
    "s_add_u32 s79, s81, s69\n\t" \
    "s_add_u32 s79, s79, s87\n\t" \
    "s_cmp_ge_u32 s65, 36\n\t" \
    "s_cselect_b32 s69, 28, 0\n\t" \
    "s_sub_u32 s79, s79, s69\n\t" \
    "s_add_u32 s80, s79, s88\n\t" \
    "s_add_u32 s65, s65, 1\n\t" \
    "s_branch vv_go42_%=\n\t" \
    "vv_Gbad47_%=:\n\t" \
    "s_waitcnt vmcnt(0) lgkmcnt(0)\n\t" \
    "s_nop 7\n\t" \
    "v_mov_b32 v72, 0\n\t" \
    "v_mov_b32 v73, 0\n\t" \
    "v_mov_b32 v74, 0\n\t" \
    "v_mov_b32 v75, 0\n\t" \
    "v_mov_b32 v76, 0\n\t" \
    "v_mov_b32 v77, 0\n\t" \
    "v_mov_b32 v78, 0\n\t" \
    "v_mov_b32 v79, 0\n\t" \
    "s_cmp_eq_u32 s68, 0\n\t" \
    "s_cbranch_scc1 vv_F65_%=\n\t" \
    "s_sub_u32 s68, s68, 1\n\t" \
    "s_branch vv_G46_%=\n\t" \
    "vv_F65_%=:\n\t" \
    "v_mov_b32 v140, 6095104\n\t" \
    "v_mov_b32 v141, 1\n\t" \
    "global_store_dword v140, v141, s[36:37] sc1\n\t" \
    "s_waitcnt vmcnt(0)\n\t" \
    "s_branch vv_Gc49_%=\n\t" \
    "vv_Gbad248_%=:\n\t" \
    "global_load_dwordx4 v[44:47], v2, s[46:47] sc1\n\t" \
    "s_waitcnt vmcnt(0)\n\t" \
    "v_max3_u32 v140, v44, v45, v46\n\t" \
    "v_max_u32 v140, v140, v47\n\t" \
    "v_cmp_eq_u32 vcc, -1, v140\n\t" \
    "s_cbranch_vccz vv_Gre251_%=\n\t" \
    "s_cmp_eq_u32 s68, 0\n\t" \
    "s_cbranch_scc1 vv_F66_%=\n\t" \
    "s_sub_u32 s68, s68, 1\n\t" \
    "s_sleep 1\n\t" \
    "s_branch vv_Gbad248_%=\n\t" \
    "vv_F66_%=:\n\t" \
    "v_mov_b32 v140, 6095104\n\t" \
    "v_mov_b32 v141, 2\n\t" \
    "global_store_dword v140, v141, s[36:37] sc1\n\t" \
    "s_waitcnt vmcnt(0)\n\t" \
    "s_branch vv_Gre251_%=\n\t" \
    "vv_Gre251_%=:\n\t" \
    "v_mov_b32 v72, 0\n\t" \
    "v_mov_b32 v73, 0\n\t" \
    "v_mov_b32 v74, 0\n\t" \
    "v_mov_b32 v75, 0\n\t" \
    "v_mov_b32 v76, 0\n\t" \
    "v_mov_b32 v77, 0\n\t" \
    "v_mov_b32 v78, 0\n\t" \
    "v_mov_b32 v79, 0\n\t" \
    "v_smfmac_f32_16x16x64_bf16 v[72:75], v[36:39], a[16:23], v10\n\t" \
    "s_nop 0\n\t" \
    "v_smfmac_f32_16x16x64_bf16 v[76:79], v[36:39], a[88:95], v10\n\t" \
    "v_smfmac_f32_16x16x64_bf16 v[72:75], v[40:43], a[24:31], v10\n\t" \
    "v_smfmac_f32_16x16x64_bf16 v[76:79], v[40:43], a[96:103], v10\n\t" \
    "v_smfmac_f32_16x16x64_bf16 v[72:75], v[44:47], a[64:71], v10\n\t" \
    "v_smfmac_f32_16x16x64_bf16 v[76:79], v[44:47], a[136:143], v10\n\t" \
    "s_branch vv_Gc250_%=\n\t" \
    "vv_end41_%=:\n\t" \
    "s_endpgm\n\t" \
    "vv_loop12_%=:\n\t" \
    "v_mov_b32 v72, 0\n\t" \
    "v_mov_b32 v73, 0\n\t" \
    "v_mov_b32 v74, 0\n\t" \
    "v_mov_b32 v75, 0\n\t" \
    "v_mov_b32 v76, 0\n\t" \
    "v_mov_b32 v77, 0\n\t" \
    "v_mov_b32 v78, 0\n\t" \
    "v_mov_b32 v79, 0\n\t" \
    "v_mov_b32 v92, 0\n\t" \
    "v_mov_b32 v93, 0\n\t" \
    "v_mov_b32 v94, 0\n\t" \
    "v_mov_b32 v95, 0\n\t" \
    "v_mov_b32 v96, 0\n\t" \
    "v_mov_b32 v97, 0\n\t" \
    "v_mov_b32 v98, 0\n\t" \
    "v_mov_b32 v99, 0\n\t" \
    "v_mov_b32 v100, 0\n\t" \
    "v_mov_b32 v101, 0\n\t" \
    "v_mov_b32 v102, 0\n\t" \
    "v_mov_b32 v103, 0\n\t" \
    "v_mov_b32 v104, 0\n\t" \
    "v_mov_b32 v105, 0\n\t" \
    "v_mov_b32 v106, 0\n\t" \
    "v_mov_b32 v107, 0\n\t" \
    "v_mov_b32 v108, 0\n\t" \
    "v_mov_b32 v109, 0\n\t" \
    "v_mov_b32 v110, 0\n\t" \
    "v_mov_b32 v111, 0\n\t" \
    "v_mov_b32 v112, 0\n\t" \
    "v_mov_b32 v113, 0\n\t" \
    "v_mov_b32 v114, 0\n\t" \
    "v_mov_b32 v115, 0\n\t" \
    "v_mov_b32 v116, 0\n\t" \
    "v_mov_b32 v117, 0\n\t" \
    "v_mov_b32 v118, 0\n\t" \
    "v_mov_b32 v119, 0\n\t" \
    "v_mov_b32 v120, 0\n\t" \
    "v_mov_b32 v121, 0\n\t" \
    "v_mov_b32 v122, 0\n\t" \
    "v_mov_b32 v123, 0\n\t" \
    "v_mov_b32 v124, 0\n\t" \
    "v_mov_b32 v125, 0\n\t" \
    "v_mov_b32 v126, 0\n\t" \
    "v_mov_b32 v127, 0\n\t" \
    "vv_go68_%=:\n\t" \
    "s_add_u32 s69, s65, 0\n\t" \
    "s_bitcmp1_b32 s69, 0\n\t" \
    "s_cselect_b32 s70, s63, s62\n\t" \
    "s_bfe_u32 s69, s69, 0x20001\n\t" \
    "s_lshl_b32 s69, s69, 19\n\t" \
    "s_add_u32 s69, s69, s70\n\t" \
    "s_add_u32 s46, s36, s69\n\t" \
    "s_addc_u32 s47, s37, 0\n\t" \
    "s_add_u32 s69, s65, 0\n\t" \
    "s_mul_i32 s70, s69, 171\n\t" \
    "s_lshr_b32 s70, s70, 9\n\t" \
    "s_mul_i32 s70, s70, 3\n\t" \
    "s_sub_u32 s69, s69, s70\n\t" \
    "s_mul_i32 s69, s69, 36\n\t" \
    "s_lshl_b32 s69, s69, 14\n\t" \
    "s_add_u32 s69, s69, s64\n\t" \
    "s_add_u32 s69, s69, 4194304\n\t" \
    "s_add_u32 s38, s36, s69\n\t" \
    "s_addc_u32 s39, s37, 0\n\t" \
    "s_add_u32 s69, s65, 2\n\t" \
    "s_mul_i32 s70, s69, 171\n\t" \
    "s_lshr_b32 s70, s70, 9\n\t" \
    "s_mul_i32 s70, s70, 3\n\t" \
    "s_sub_u32 s69, s69, s70\n\t" \
    "s_mul_i32 s69, s69, 36\n\t" \
    "s_lshl_b32 s69, s69, 14\n\t" \
    "s_add_u32 s69, s69, s64\n\t" \
    "s_add_u32 s69, s69, 4194304\n\t" \
    "s_add_u32 s42, s36, s69\n\t" \
    "s_addc_u32 s43, s37, 0\n\t" \
    "s_mul_i32 s69, s65, 9216\n\t" \
    "s_add_u32 s69, s69, 123731968\n\t" \
    "s_add_u32 s40, s36, s69\n\t" \
    "s_addc_u32 s41, s37, 0\n\t" \
    "s_sub_u32 s70, s79, s74\n\t" \
    "s_max_i32 s70, s70, 0\n\t" \
    "s_min_i32 s70, s70, 16000\n\t" \
    "s_lshl_b32 s70, s70, 1\n\t" \
    "s_mul_hi_u32 s70, s70, s77\n\t" \
    "s_add_u32 s70, s70, s73\n\t" \
    "s_sub_u32 s70, s70, 100\n\t" \
    "s_getpc_b64 s[30:31]\n\t" \
    "vv_Wp70_%=:\n\t" \
    "s_add_u32 s30, s30, vv_We71_%=-vv_Wp70_%=\n\t" \
    "s_addc_u32 s31, s31, 0\n\t" \
    "vv_W69_%=:\n\t" \
    "s_memtime s[56:57]\n\t" \
    "s_waitcnt lgkmcnt(0)\n\t" \
    "s_sub_u32 s69, s70, s56\n\t" \
    "s_cmp_gt_i32 s69, 111\n\t" \
    "s_cbranch_scc1 vv_W69_%=\n\t" \
    "s_max_i32 s69, s69, 0\n\t" \
    "s_lshr_b32 s69, s69, 1\n\t" \
    "s_and_b32 s69, s69, 0x3c\n\t" \
    "s_sub_u32 s30, s30, s69\n\t" \
    "s_subb_u32 s31, s31, 0\n\t" \
    "s_setpc_b64 s[30:31]\n\t" \
    ".p2align 6\n\t" \
    "s_nop 0\n\t" \
    "s_nop 0\n\t" \
    "s_nop 0\n\t" \
    "s_nop 1\n\t" \
    "s_nop 1\n\t" \
    "s_nop 1\n\t" \
    "s_nop 1\n\t" \
    "s_nop 1\n\t" \
    "s_nop 1\n\t" \
    "s_nop 1\n\t" \
    "s_nop 1\n\t" \
    "s_nop 1\n\t" \
    "s_nop 1\n\t" \
    "s_nop 1\n\t" \
    "s_nop 1\n\t" \
    "s_nop 1\n\t" \
    "vv_We71_%=:\n\t" \
    "vv_G72_%=:\n\t" \
    "global_load_dwordx4 v[36:39], v1, s[46:47] sc1\n\t" \
    "s_nop 9\n\t" \
    "global_load_dwordx4 v[40:43], v1, s[46:47] offset:1024 sc1\n\t" \
    "s_nop 9\n\t" \
    "global_load_dwordx4 v[44:47], v2, s[46:47] sc1\n\t" \
    "s_waitcnt vmcnt(2)\n\t" \
    "v_smfmac_f32_16x16x64_bf16 v[72:75], v[36:39], a[32:39], v10\n\t" \
    "ds_write_b128 v0, v[36:39] offset:4096\n\t" \
    "v_smfmac_f32_16x16x64_bf16 v[76:79], v[36:39], a[104:111], v10\n\t" \
    "v_max3_u32 v140, v36, v37, v38\n\t" \
    "s_waitcnt vmcnt(1)\n\t" \
    "ds_write_b128 v0, v[40:43] offset:5120\n\t" \
    "v_smfmac_f32_16x16x64_bf16 v[72:75], v[40:43], a[40:47], v10\n\t" \
    "v_max3_u32 v140, v140, v39, v40\n\t" \
    "v_max3_u32 v140, v140, v41, v42\n\t" \
    "v_max_u32 v140, v140, v43\n\t" \
    "v_cmp_eq_u32 vcc, -1, v140\n\t" \
    "v_smfmac_f32_16x16x64_bf16 v[76:79], v[40:43], a[112:119], v10\n\t" \
    "s_cbranch_vccnz vv_Gbad73_%=\n\t" \
    "vv_Gc75_%=:\n\t" \
    "s_waitcnt lgkmcnt(0)\n\t" \
    "s_barrier\n\t" \
    "ds_read_b128 v[48:51], v0 offset:6144\n\t" \
    "ds_read_b128 v[52:55], v0 offset:7168\n\t" \
    "s_waitcnt vmcnt(0)\n\t" \
    "v_smfmac_f32_16x16x64_bf16 v[72:75], v[44:47], a[64:71], v10\n\t" \
    "ds_read_b128 v[56:59], v0 offset:0\n\t" \
    "ds_read_b128 v[60:63], v0 offset:1024\n\t" \
    "v_max3_u32 v140, v44, v45, v46\n\t" \
    "v_max_u32 v140, v140, v47\n\t" \
    "v_smfmac_f32_16x16x64_bf16 v[76:79], v[44:47], a[136:143], v10\n\t" \
    "ds_read_b128 v[64:67], v0 offset:2048\n\t" \
    "ds_read_b128 v[68:71], v0 offset:3072\n\t" \
    "v_cmp_eq_u32 vcc, -1, v140\n\t" \
    "s_cbranch_vccnz vv_Gbad274_%=\n\t" \
    "vv_Gc276_%=:\n\t" \
    "s_waitcnt lgkmcnt(5)\n\t" \
    "v_smfmac_f32_16x16x64_bf16 v[72:75], v[48:51], a[48:55], v10\n\t" \
    "s_waitcnt lgkmcnt(4)\n\t" \
    "v_smfmac_f32_16x16x64_bf16 v[72:75], v[52:55], a[56:63], v10\n\t" \
    "s_waitcnt lgkmcnt(3)\n\t" \
    "v_smfmac_f32_16x16x64_bf16 v[72:75], v[56:59], a[0:7], v10\n\t" \
    "s_waitcnt lgkmcnt(2)\n\t" \
    "v_smfmac_f32_16x16x64_bf16 v[72:75], v[60:63], a[8:15], v10\n\t" \
    "s_waitcnt lgkmcnt(1)\n\t" \
    "v_smfmac_f32_16x16x64_bf16 v[72:75], v[64:67], a[16:23], v10\n\t" \
    "s_waitcnt lgkmcnt(0)\n\t" \
    "v_smfmac_f32_16x16x64_bf16 v[72:75], v[68:71], a[24:31], v10\n\t" \
    "v_smfmac_f32_16x16x64_bf16 v[76:79], v[48:51], a[120:127], v10\n\t" \
    "s_nop 1\n\t" \
    "s_nop 0\n\t" \
    "v_smfmac_f32_16x16x64_bf16 v[76:79], v[52:55], a[128:135], v10\n\t" \
    "s_nop 1\n\t" \
    "s_nop 0\n\t" \
    "v_smfmac_f32_16x16x64_bf16 v[76:79], v[56:59], a[72:79], v10\n\t" \
    "v_add_f32 v72, v72, v74\n\t" \
    "v_smfmac_f32_16x16x64_bf16 v[76:79], v[60:63], a[80:87], v10\n\t" \
    "v_add_f32 v73, v73, v75\n\t" \
    "v_smfmac_f32_16x16x64_bf16 v[76:79], v[64:67], a[88:95], v10\n\t" \
    "v_cvt_pk_bf16_f32 v80, v72, v73\n\t" \
    "v_smfmac_f32_16x16x64_bf16 v[76:79], v[68:71], a[96:103], v10\n\t" \
    "ds_write_b32 v12, v80 offset:0\n\t" \
    "s_nop 6\n\t" \
    "v_add_f32 v76, v76, v78\n\t" \
    "v_add_f32 v77, v77, v79\n\t" \
    "v_cvt_pk_bf16_f32 v81, v76, v77\n\t" \
    "ds_write_b32 v12, v81 offset:64\n\t" \
    "s_waitcnt lgkmcnt(0)\n\t" \
    "s_barrier\n\t" \
    "ds_read_b64_tr_b16 v[84:85], v13 offset:0\n\t" \
    "ds_read_b64_tr_b16 v[86:87], v13 offset:128\n\t" \
    "ds_read_b64_tr_b16 v[88:89], v13 offset:1024\n\t" \
    "ds_read_b64_tr_b16 v[90:91], v13 offset:1152\n\t" \
    "s_waitcnt lgkmcnt(2)\n\t" \
    "v_smfmac_f32_16x16x64_bf16 v[92:95], v[84:87], a[144:151], v10\n\t" \
    "s_nop 2\n\t" \
    "s_waitcnt lgkmcnt(0)\n\t" \
    "v_smfmac_f32_16x16x64_bf16 v[92:95], v[88:91], a[152:159], v10\n\t" \
    "s_nop 2\n\t" \
    "v_smfmac_f32_16x16x64_bf16 v[96:99], v[84:87], a[160:167], v10\n\t" \
    "s_nop 2\n\t" \
    "v_smfmac_f32_16x16x64_bf16 v[96:99], v[88:91], a[168:175], v10\n\t" \
    "s_nop 2\n\t" \
    "v_smfmac_f32_16x16x64_bf16 v[100:103], v[84:87], a[176:183], v10\n\t" \
    "s_nop 0\n\t" \
    "v_add_f32 v92, v92, v94\n\t" \
    "v_add_f32 v93, v93, v95\n\t" \
    "v_smfmac_f32_16x16x64_bf16 v[100:103], v[88:91], a[184:191], v10\n\t" \
    "s_nop 1\n\t" \
    "v_cvt_pk_bf16_f32 v128, v92, v93\n\t" \
    "v_smfmac_f32_16x16x64_bf16 v[104:107], v[84:87], a[192:199], v10\n\t" \
    "s_nop 0\n\t" \
    "v_add_f32 v96, v96, v98\n\t" \
    "v_add_f32 v97, v97, v99\n\t" \
    "v_smfmac_f32_16x16x64_bf16 v[104:107], v[88:91], a[200:207], v10\n\t" \
    "s_nop 1\n\t" \
    "v_cvt_pk_bf16_f32 v129, v96, v97\n\t" \
    "v_smfmac_f32_16x16x64_bf16 v[124:127], v[84:87], v[240:247], v10\n\t" \
    "s_nop 0\n\t" \
    "v_add_f32 v100, v100, v102\n\t" \
    "v_add_f32 v101, v101, v103\n\t" \
    "v_smfmac_f32_16x16x64_bf16 v[124:127], v[88:91], v[248:255], v10\n\t" \
    "s_nop 1\n\t" \
    "global_load_dword v140, v20, s[46:47] sc1\n\t" \
    "v_cvt_pk_bf16_f32 v130, v100, v101\n\t" \
    "v_smfmac_f32_16x16x64_bf16 v[108:111], v[84:87], a[208:215], v10\n\t" \
    "s_nop 0\n\t" \
    "v_add_f32 v104, v104, v106\n\t" \
    "v_add_f32 v105, v105, v107\n\t" \
    "v_smfmac_f32_16x16x64_bf16 v[108:111], v[88:91], a[216:223], v10\n\t" \
    "s_nop 0\n\t" \
    "v_cvt_pk_bf16_f32 v131, v104, v105\n\t" \
    "v_permlane32_swap_b32 v128, v130\n\t" \
    "v_smfmac_f32_16x16x64_bf16 v[112:115], v[84:87], a[224:231], v10\n\t" \
    "s_nop 0\n\t" \
    "v_permlane32_swap_b32 v129, v131\n\t" \
    "s_nop 1\n\t" \
    "v_permlane16_swap_b32 v128, v129\n\t" \
    "v_smfmac_f32_16x16x64_bf16 v[112:115], v[88:91], a[232:239], v10\n\t" \
    "v_permlane16_swap_b32 v130, v131\n\t" \
    "global_store_dwordx4 v14, v[128:131], s[38:39] sc1\n\t" \
    "v_add_f32 v124, v124, v126\n\t" \
    "v_smfmac_f32_16x16x64_bf16 v[116:119], v[84:87], a[240:247], v10\n\t" \
    "s_nop 0\n\t" \
    "v_add_f32 v125, v125, v127\n\t" \
    "v_cvt_pk_bf16_f32 v136, v124, v125\n\t" \
    "v_smfmac_f32_16x16x64_bf16 v[116:119], v[88:91], a[248:255], v10\n\t" \
    "global_store_dword v16, v136, s[38:39] sc1\n\t" \
    "v_add_f32 v108, v108, v110\n\t" \
    "v_add_f32 v109, v109, v111\n\t" \
    "v_smfmac_f32_16x16x64_bf16 v[120:123], v[84:87], v[224:231], v10\n\t" \
    "s_nop 0\n\t" \
    "v_cvt_pk_bf16_f32 v132, v108, v109\n\t" \
    "v_add_f32 v112, v112, v114\n\t" \
    "v_smfmac_f32_16x16x64_bf16 v[120:123], v[88:91], v[232:239], v10\n\t" \
    "v_add_f32 v113, v113, v115\n\t" \
    "v_cvt_pk_bf16_f32 v133, v112, v113\n\t" \
    "v_add_f32 v116, v116, v118\n\t" \
    "v_add_f32 v117, v117, v119\n\t" \
    "v_cvt_pk_bf16_f32 v134, v116, v117\n\t" \
    "s_nop 2\n\t" \
    "v_pk_add_f32 v[120:121], v[120:121], v[122:123]\n\t" \
    "v_cvt_pk_bf16_f32 v135, v120, v121\n\t" \
    "v_permlane32_swap_b32 v132, v134\n\t" \
    "s_nop 0\n\t" \
    "v_permlane32_swap_b32 v133, v135\n\t" \
    "s_nop 1\n\t" \
    "v_permlane16_swap_b32 v132, v133\n\t" \
    "v_permlane16_swap_b32 v134, v135\n\t" \
    "global_store_dwordx4 v15, v[132:135], s[38:39] sc1\n\t" \
    "s_nop 1\n\t" \
    "global_store_dwordx4 v17, v[128:131], s[40:41] sc1\n\t" \
    "global_store_dwordx4 v17, v[132:135], s[40:41] offset:1024 sc1\n\t" \
    "global_store_dword v18, v136, s[40:41] sc1\n\t" \
    "s_cmp_eq_u32 s65, 63\n\t" \
    "s_cbranch_scc1 vv_end67_%=\n\t" \
    "s_mov_b32 s71, 3\n\t" \
    "vv_E78_%=:\n\t" \
    "s_memtime s[54:55]\n\t" \
    "s_nop 0\n\t" \
    "s_memrealtime s[4:5]\n\t" \
    "s_nop 0\n\t" \
    "s_memrealtime s[6:7]\n\t" \
    "s_nop 0\n\t" \
    "s_memrealtime s[8:9]\n\t" \
    "s_nop 0\n\t" \
    "s_memrealtime s[10:11]\n\t" \
    "s_nop 0\n\t" \
    "s_memrealtime s[12:13]\n\t" \
    "s_nop 0\n\t" \
    "s_memrealtime s[14:15]\n\t" \
    "s_nop 0\n\t" \
    "s_memrealtime s[16:17]\n\t" \
    "s_nop 0\n\t" \
    "s_memrealtime s[18:19]\n\t" \
    "s_nop 0\n\t" \
    "s_memrealtime s[20:21]\n\t" \
    "s_nop 0\n\t" \
    "s_memrealtime s[22:23]\n\t" \
    "s_nop 0\n\t" \
    "s_memrealtime s[24:25]\n\t" \
    "s_nop 0\n\t" \
    "s_memrealtime s[26:27]\n\t" \
    "s_nop 0\n\t" \
    "s_memrealtime s[28:29]\n\t" \
    "s_nop 0\n\t" \
    "s_memtime s[56:57]\n\t" \
    "s_mov_b32 s70, 1\n\t" \
    "s_sub_u32 s71, s71, 1\n\t" \
    "s_cselect_b32 s69, 0, -1\n\t" \
    "s_waitcnt lgkmcnt(0)\n\t" \
    "s_sub_u32 s54, s56, s54\n\t" \
    "s_and_b32 s54, s54, s69\n\t" \
    "s_cmp_gt_u32 s54, 124\n\t" \
    "s_cbranch_scc1 vv_E78_%=\n\t" \
    "s_cmp_eq_u32 s4, s28\n\t" \
    "s_addc_u32 s70, s70, 0\n\t" \
    "s_cmp_eq_u32 s6, s28\n\t" \
    "s_addc_u32 s70, s70, 0\n\t" \
    "s_cmp_eq_u32 s8, s28\n\t" \
    "s_addc_u32 s70, s70, 0\n\t" \
    "s_cmp_eq_u32 s10, s28\n\t" \
    "s_addc_u32 s70, s70, 0\n\t" \
    "s_cmp_eq_u32 s12, s28\n\t" \
    "s_addc_u32 s70, s70, 0\n\t" \
    "s_cmp_eq_u32 s14, s28\n\t" \
    "s_addc_u32 s70, s70, 0\n\t" \
    "s_cmp_eq_u32 s16, s28\n\t" \
    "s_addc_u32 s70, s70, 0\n\t" \
    "s_cmp_eq_u32 s18, s28\n\t" \
    "s_addc_u32 s70, s70, 0\n\t" \
    "s_cmp_eq_u32 s20, s28\n\t" \
    "s_addc_u32 s70, s70, 0\n\t" \
    "s_cmp_eq_u32 s22, s28\n\t" \
    "s_addc_u32 s70, s70, 0\n\t" \
    "s_cmp_eq_u32 s24, s28\n\t" \
    "s_addc_u32 s70, s70, 0\n\t" \
    "s_cmp_eq_u32 s26, s28\n\t" \
    "s_addc_u32 s70, s70, 0\n\t" \
    "s_mov_b32 s75, s73\n\t" \
    "s_mov_b32 s76, s74\n\t" \
    "s_lshl_b32 s70, s70, 3\n\t" \
    "s_sub_u32 s73, s56, s70\n\t" \
    "s_lshl_b32 s74, s28, 4\n\t" \
    "s_sub_u32 s69, s73, s75\n\t" \
    "s_sub_u32 s70, s74, s76\n\t" \
    "s_sub_u32 s71, s70, 1600\n\t" \
    "s_cmp_lt_u32 s71, 400000\n\t" \
    "s_cbranch_scc0 vv_K80_%=\n\t" \
    "v_cvt_f32_u32 v141, s69\n\t" \
    "v_cvt_f32_u32 v142, s70\n\t" \
    "v_rcp_f32 v143, v141\n\t" \
    "v_rcp_f32 v144, v142\n\t" \
    "v_mul_f32 v143, v143, v142\n\t" \
    "v_mul_f32 v144, v144, v141\n\t" \
    "v_mul_f32 v143, 0x4f800000, v143\n\t" \
    "v_mul_f32 v144, 0x4f000000, v144\n\t" \
    "v_cvt_u32_f32 v143, v143\n\t" \
    "v_cvt_u32_f32 v144, v144\n\t" \
    "v_readfirstlane_b32 s69, v143\n\t" \
    "v_readfirstlane_b32 s70, v144\n\t" \
    "s_nop 1\n\t" \
    "s_sub_u32 s69, s69, s78\n\t" \
    "s_ashr_i32 s69, s69, 2\n\t" \
    "s_add_u32 s78, s78, s69\n\t" \
    "s_sub_u32 s70, s70, s77\n\t" \
    "s_ashr_i32 s70, s70, 2\n\t" \
    "s_add_u32 s77, s77, s70\n\t" \
    "vv_K80_%=:\n\t" \
    "s_waitcnt vmcnt(6)\n\t" \
    "v_cmp_eq_u32 vcc, -1, v140\n\t" \
    "s_cbranch_vccz vv_Hok82_%=\n\t" \
    "vv_H81_%=:\n\t" \
    "global_load_dword v140, v20, s[46:47] sc1\n\t" \
    "s_waitcnt vmcnt(0)\n\t" \
    "v_cmp_eq_u32 vcc, -1, v140\n\t" \
    "s_cbranch_vccz vv_Hc83_%=\n\t" \
    "s_cmp_eq_u32 s68, 0\n\t" \
    "s_cbranch_scc1 vv_F84_%=\n\t" \
    "s_sub_u32 s68, s68, 1\n\t" \
    "s_sleep 5\n\t" \
    "s_branch vv_H81_%=\n\t" \
    "vv_F84_%=:\n\t" \
    "v_mov_b32 v141, 6095104\n\t" \
    "v_mov_b32 v142, 6\n\t" \
    "global_store_dword v141, v142, s[36:37] sc1\n\t" \
    "s_waitcnt vmcnt(0)\n\t" \
    "s_branch vv_Hc83_%=\n\t" \
    "vv_Hc83_%=:\n\t" \
    "vv_Hok82_%=:\n\t" \
    "v_subrev_u32 v140, s80, v140\n\t" \
    "v_and_b32 v20, -5, v20\n\t" \
    "s_nop 0\n\t" \
    "v_max_i32_dpp v140, v140, v140 row_ror:1 row_mask:0xf bank_mask:0xf\n\t" \
    "s_nop 1\n\t" \
    "v_max_i32_dpp v140, v140, v140 row_ror:2 row_mask:0xf bank_mask:0xf\n\t" \
    "s_nop 1\n\t" \
    "v_max_i32_dpp v140, v140, v140 row_ror:4 row_mask:0xf bank_mask:0xf\n\t" \
    "s_nop 1\n\t" \
    "v_max_i32_dpp v140, v140, v140 row_ror:8 row_mask:0xf bank_mask:0xf\n\t" \
    "s_nop 0\n\t" \
    "v_readlane_b32 s69, v140, 0\n\t" \
    "v_readlane_b32 s71, v140, 16\n\t" \
    "v_readlane_b32 s70, v140, 32\n\t" \
    "v_readlane_b32 s81, v140, 48\n\t" \
    "s_max_i32 s69, s69, s71\n\t" \
    "s_nop 0\n\t" \
    "s_max_i32 s71, s70, s81\n\t" \
    "s_max_i32 s69, s69, s71\n\t" \
    "s_add_u32 s81, s69, s80\n\t" \
    "v_mov_b32 v72, 0\n\t" \
    "v_mov_b32 v73, 0\n\t" \
    "v_mov_b32 v74, 0\n\t" \
    "v_mov_b32 v75, 0\n\t" \
    "v_mov_b32 v76, 0\n\t" \
    "v_mov_b32 v77, 0\n\t" \
    "v_mov_b32 v78, 0\n\t" \
    "v_mov_b32 v79, 0\n\t" \
    "v_mov_b32 v92, 0\n\t" \
    "v_mov_b32 v93, 0\n\t" \
    "v_mov_b32 v94, 0\n\t" \
    "v_mov_b32 v95, 0\n\t" \
    "v_mov_b32 v96, 0\n\t" \
    "v_mov_b32 v97, 0\n\t" \
    "v_mov_b32 v98, 0\n\t" \
    "v_mov_b32 v99, 0\n\t" \
    "v_mov_b32 v100, 0\n\t" \
    "v_mov_b32 v101, 0\n\t" \
    "v_mov_b32 v102, 0\n\t" \
    "v_mov_b32 v103, 0\n\t" \
    "v_mov_b32 v104, 0\n\t" \
    "v_mov_b32 v105, 0\n\t" \
    "v_mov_b32 v106, 0\n\t" \
    "v_mov_b32 v107, 0\n\t" \
    "v_mov_b32 v108, 0\n\t" \
    "v_mov_b32 v109, 0\n\t" \
    "v_mov_b32 v110, 0\n\t" \
    "v_mov_b32 v111, 0\n\t" \
    "v_mov_b32 v112, 0\n\t" \
    "v_mov_b32 v113, 0\n\t" \
    "v_mov_b32 v114, 0\n\t" \
    "v_mov_b32 v115, 0\n\t" \
    "v_mov_b32 v116, 0\n\t" \
    "v_mov_b32 v117, 0\n\t" \
    "v_mov_b32 v118, 0\n\t" \
    "v_mov_b32 v119, 0\n\t" \
    "v_mov_b32 v120, 0\n\t" \
    "v_mov_b32 v121, 0\n\t" \
    "v_mov_b32 v122, 0\n\t" \
    "v_mov_b32 v123, 0\n\t" \
    "v_mov_b32 v124, 0\n\t" \
    "v_mov_b32 v125, 0\n\t" \
    "v_mov_b32 v126, 0\n\t" \
    "v_mov_b32 v127, 0\n\t" \
    "s_add_u32 s79, s81, 400\n\t" \
    "s_sub_u32 s70, s79, s74\n\t" \
    "s_max_i32 s70, s70, 0\n\t" \
    "s_min_i32 s70, s70, 16000\n\t" \
    "s_lshl_b32 s70, s70, 1\n\t" \
    "s_mul_hi_u32 s70, s70, s77\n\t" \
    "s_add_u32 s70, s70, s73\n\t" \
    "vv_W88_%=:\n\t" \
    "s_memtime s[56:57]\n\t" \
    "s_waitcnt lgkmcnt(0)\n\t" \
    "s_sub_u32 s69, s70, s56\n\t" \
    "s_cmp_gt_i32 s69, 24\n\t" \
    "s_cbranch_scc1 vv_W88_%=\n\t" \
    "global_store_dwordx4 v14, v[148:151], s[42:43] sc1\n\t" \
    "global_store_dwordx4 v15, v[148:151], s[42:43] sc1\n\t" \
    "global_store_dword v16, v148, s[42:43] sc1\n\t" \
    "s_cmp_lg_u32 s65, 0\n\t" \
    "s_cbranch_scc1 vv_Oc87_%=\n\t" \
    "s_add_u32 s79, s81, 240\n\t" \
    "s_sub_u32 s70, s79, s74\n\t" \
    "s_max_i32 s70, s70, 0\n\t" \
    "s_min_i32 s70, s70, 16000\n\t" \
    "s_lshl_b32 s70, s70, 1\n\t" \
    "s_mul_hi_u32 s70, s70, s77\n\t" \
    "s_add_u32 s70, s70, s73\n\t" \
    "vv_W89_%=:\n\t" \
    "s_memtime s[56:57]\n\t" \
    "s_waitcnt lgkmcnt(0)\n\t" \
    "s_sub_u32 s69, s70, s56\n\t" \
    "s_cmp_gt_i32 s69, 24\n\t" \
    "s_cbranch_scc1 vv_W89_%=\n\t" \
    "global_load_dword v140, v35, s[46:47] sc1\n\t" \
    "s_waitcnt vmcnt(0)\n\t" \
    "v_cmp_eq_u32 vcc, -1, v140\n\t" \
    "s_cbranch_vccz vv_Ook86_%=\n\t" \
    "vv_O85_%=:\n\t" \
    "global_load_dword v140, v35, s[46:47] offset:1536 sc1\n\t" \
    "s_waitcnt vmcnt(0)\n\t" \
    "v_cmp_eq_u32 vcc, -1, v140\n\t" \
    "s_cbranch_vccz vv_Ook86_%=\n\t" \
    "s_cmp_eq_u32 s68, 0\n\t" \
    "s_cbranch_scc1 vv_F90_%=\n\t" \
    "s_sub_u32 s68, s68, 1\n\t" \
    "s_sleep 3\n\t" \
    "s_branch vv_O85_%=\n\t" \
    "vv_F90_%=:\n\t" \
    "v_mov_b32 v141, 6095104\n\t" \
    "v_mov_b32 v142, 17\n\t" \
    "global_store_dword v141, v142, s[36:37] sc1\n\t" \
    "s_waitcnt vmcnt(0)\n\t" \
    "s_branch vv_Ook86_%=\n\t" \
    "vv_Ook86_%=:\n\t" \
    "v_subrev_u32 v140, s80, v140\n\t" \
    "s_nop 1\n\t" \
    "v_max_i32_dpp v140, v140, v140 row_ror:1 row_mask:0xf bank_mask:0xf\n\t" \
    "s_nop 1\n\t" \
    "v_max_i32_dpp v140, v140, v140 row_ror:2 row_mask:0xf bank_mask:0xf\n\t" \
    "s_nop 1\n\t" \
    "v_max_i32_dpp v140, v140, v140 row_ror:4 row_mask:0xf bank_mask:0xf\n\t" \
    "s_nop 1\n\t" \
    "v_max_i32_dpp v140, v140, v140 row_ror:8 row_mask:0xf bank_mask:0xf\n\t" \
    "s_nop 0\n\t" \
    "v_readlane_b32 s69, v140, 0\n\t" \
    "v_readlane_b32 s71, v140, 16\n\t" \
    "v_readlane_b32 s70, v140, 32\n\t" \
    "v_readlane_b32 s81, v140, 48\n\t" \
    "s_max_i32 s69, s69, s71\n\t" \
    "s_nop 0\n\t" \
    "s_max_i32 s71, s70, s81\n\t" \
    "s_max_i32 s69, s69, s71\n\t" \
    "s_add_u32 s81, s69, s80\n\t" \
    "vv_Oc87_%=:\n\t" \
    "s_mul_hi_u32 s69, s85, s78\n\t" \
    "s_add_u32 s79, s81, s69\n\t" \
    "s_add_u32 s79, s79, s87\n\t" \
    "s_cmp_ge_u32 s65, 36\n\t" \
    "s_cselect_b32 s69, 28, 0\n\t" \
    "s_sub_u32 s79, s79, s69\n\t" \
    "s_add_u32 s80, s79, s88\n\t" \
    "s_add_u32 s65, s65, 1\n\t" \
    "s_branch vv_go68_%=\n\t" \
    "vv_Gbad73_%=:\n\t" \
    "s_waitcnt vmcnt(0) lgkmcnt(0)\n\t" \
    "s_nop 7\n\t" \
    "v_mov_b32 v72, 0\n\t" \
    "v_mov_b32 v73, 0\n\t" \
    "v_mov_b32 v74, 0\n\t" \
    "v_mov_b32 v75, 0\n\t" \
    "v_mov_b32 v76, 0\n\t" \
    "v_mov_b32 v77, 0\n\t" \
    "v_mov_b32 v78, 0\n\t" \
    "v_mov_b32 v79, 0\n\t" \
    "s_cmp_eq_u32 s68, 0\n\t" \
    "s_cbranch_scc1 vv_F91_%=\n\t" \
    "s_sub_u32 s68, s68, 1\n\t" \
    "s_branch vv_G72_%=\n\t" \
    "vv_F91_%=:\n\t" \
    "v_mov_b32 v140, 6095104\n\t" \
    "v_mov_b32 v141, 1\n\t" \
    "global_store_dword v140, v141, s[36:37] sc1\n\t" \
    "s_waitcnt vmcnt(0)\n\t" \
    "s_branch vv_Gc75_%=\n\t" \
    "vv_Gbad274_%=:\n\t" \
    "global_load_dwordx4 v[44:47], v2, s[46:47] sc1\n\t" \
    "s_waitcnt vmcnt(0)\n\t" \
    "v_max3_u32 v140, v44, v45, v46\n\t" \
    "v_max_u32 v140, v140, v47\n\t" \
    "v_cmp_eq_u32 vcc, -1, v140\n\t" \
    "s_cbranch_vccz vv_Gre277_%=\n\t" \
    "s_cmp_eq_u32 s68, 0\n\t" \
    "s_cbranch_scc1 vv_F92_%=\n\t" \
    "s_sub_u32 s68, s68, 1\n\t" \
    "s_sleep 1\n\t" \
    "s_branch vv_Gbad274_%=\n\t" \
    "vv_F92_%=:\n\t" \
    "v_mov_b32 v140, 6095104\n\t" \
    "v_mov_b32 v141, 2\n\t" \
    "global_store_dword v140, v141, s[36:37] sc1\n\t" \
    "s_waitcnt vmcnt(0)\n\t" \
    "s_branch vv_Gre277_%=\n\t" \
    "vv_Gre277_%=:\n\t" \
    "v_mov_b32 v72, 0\n\t" \
    "v_mov_b32 v73, 0\n\t" \
    "v_mov_b32 v74, 0\n\t" \
    "v_mov_b32 v75, 0\n\t" \
    "v_mov_b32 v76, 0\n\t" \
    "v_mov_b32 v77, 0\n\t" \
    "v_mov_b32 v78, 0\n\t" \
    "v_mov_b32 v79, 0\n\t" \
    "v_smfmac_f32_16x16x64_bf16 v[72:75], v[36:39], a[32:39], v10\n\t" \
    "s_nop 0\n\t" \
    "v_smfmac_f32_16x16x64_bf16 v[76:79], v[36:39], a[104:111], v10\n\t" \
    "v_smfmac_f32_16x16x64_bf16 v[72:75], v[40:43], a[40:47], v10\n\t" \
    "v_smfmac_f32_16x16x64_bf16 v[76:79], v[40:43], a[112:119], v10\n\t" \
    "v_smfmac_f32_16x16x64_bf16 v[72:75], v[44:47], a[64:71], v10\n\t" \
    "v_smfmac_f32_16x16x64_bf16 v[76:79], v[44:47], a[136:143], v10\n\t" \
    "s_branch vv_Gc276_%=\n\t" \
    "vv_end67_%=:\n\t" \
    "s_endpgm\n\t" \
    "vv_loop13_%=:\n\t" \
    "v_mov_b32 v72, 0\n\t" \
    "v_mov_b32 v73, 0\n\t" \
    "v_mov_b32 v74, 0\n\t" \
    "v_mov_b32 v75, 0\n\t" \
    "v_mov_b32 v76, 0\n\t" \
    "v_mov_b32 v77, 0\n\t" \
    "v_mov_b32 v78, 0\n\t" \
    "v_mov_b32 v79, 0\n\t" \
    "v_mov_b32 v92, 0\n\t" \
    "v_mov_b32 v93, 0\n\t" \
    "v_mov_b32 v94, 0\n\t" \
    "v_mov_b32 v95, 0\n\t" \
    "v_mov_b32 v96, 0\n\t" \
    "v_mov_b32 v97, 0\n\t" \
    "v_mov_b32 v98, 0\n\t" \
    "v_mov_b32 v99, 0\n\t" \
    "v_mov_b32 v100, 0\n\t" \
    "v_mov_b32 v101, 0\n\t" \
    "v_mov_b32 v102, 0\n\t" \
    "v_mov_b32 v103, 0\n\t" \
    "v_mov_b32 v104, 0\n\t" \
    "v_mov_b32 v105, 0\n\t" \
    "v_mov_b32 v106, 0\n\t" \
    "v_mov_b32 v107, 0\n\t" \
    "v_mov_b32 v108, 0\n\t" \
    "v_mov_b32 v109, 0\n\t" \
    "v_mov_b32 v110, 0\n\t" \
    "v_mov_b32 v111, 0\n\t" \
    "v_mov_b32 v112, 0\n\t" \
    "v_mov_b32 v113, 0\n\t" \
    "v_mov_b32 v114, 0\n\t" \
    "v_mov_b32 v115, 0\n\t" \
    "v_mov_b32 v116, 0\n\t" \
    "v_mov_b32 v117, 0\n\t" \
    "v_mov_b32 v118, 0\n\t" \
    "v_mov_b32 v119, 0\n\t" \
    "v_mov_b32 v120, 0\n\t" \
    "v_mov_b32 v121, 0\n\t" \
    "v_mov_b32 v122, 0\n\t" \
    "v_mov_b32 v123, 0\n\t" \
    "v_mov_b32 v124, 0\n\t" \
    "v_mov_b32 v125, 0\n\t" \
    "v_mov_b32 v126, 0\n\t" \
    "v_mov_b32 v127, 0\n\t" \
    "vv_go94_%=:\n\t" \
    "s_add_u32 s69, s65, 0\n\t" \
    "s_bitcmp1_b32 s69, 0\n\t" \
    "s_cselect_b32 s70, s63, s62\n\t" \
    "s_bfe_u32 s69, s69, 0x20001\n\t" \
    "s_lshl_b32 s69, s69, 19\n\t" \
    "s_add_u32 s69, s69, s70\n\t" \
    "s_add_u32 s46, s36, s69\n\t" \
    "s_addc_u32 s47, s37, 0\n\t" \
    "s_add_u32 s69, s65, 0\n\t" \
    "s_mul_i32 s70, s69, 171\n\t" \
    "s_lshr_b32 s70, s70, 9\n\t" \
    "s_mul_i32 s70, s70, 3\n\t" \
    "s_sub_u32 s69, s69, s70\n\t" \
    "s_mul_i32 s69, s69, 36\n\t" \
    "s_lshl_b32 s69, s69, 14\n\t" \
    "s_add_u32 s69, s69, s64\n\t" \
    "s_add_u32 s69, s69, 4194304\n\t" \
    "s_add_u32 s38, s36, s69\n\t" \
    "s_addc_u32 s39, s37, 0\n\t" \
    "s_add_u32 s69, s65, 2\n\t" \
    "s_mul_i32 s70, s69, 171\n\t" \
    "s_lshr_b32 s70, s70, 9\n\t" \
    "s_mul_i32 s70, s70, 3\n\t" \
    "s_sub_u32 s69, s69, s70\n\t" \
    "s_mul_i32 s69, s69, 36\n\t" \
    "s_lshl_b32 s69, s69, 14\n\t" \
    "s_add_u32 s69, s69, s64\n\t" \
    "s_add_u32 s69, s69, 4194304\n\t" \
    "s_add_u32 s42, s36, s69\n\t" \
    "s_addc_u32 s43, s37, 0\n\t" \
    "s_mul_i32 s69, s65, 9216\n\t" \
    "s_add_u32 s69, s69, 123731968\n\t" \
    "s_add_u32 s40, s36, s69\n\t" \
    "s_addc_u32 s41, s37, 0\n\t" \
    "s_sub_u32 s70, s79, s74\n\t" \
    "s_max_i32 s70, s70, 0\n\t" \
    "s_min_i32 s70, s70, 16000\n\t" \
    "s_lshl_b32 s70, s70, 1\n\t" \
    "s_mul_hi_u32 s70, s70, s77\n\t" \
    "s_add_u32 s70, s70, s73\n\t" \
    "s_sub_u32 s70, s70, 100\n\t" \
    "s_getpc_b64 s[30:31]\n\t" \
    "vv_Wp96_%=:\n\t" \
    "s_add_u32 s30, s30, vv_We97_%=-vv_Wp96_%=\n\t" \
    "s_addc_u32 s31, s31, 0\n\t" \
    "vv_W95_%=:\n\t" \
    "s_memtime s[56:57]\n\t" \
    "s_waitcnt lgkmcnt(0)\n\t" \
    "s_sub_u32 s69, s70, s56\n\t" \
    "s_cmp_gt_i32 s69, 111\n\t" \
    "s_cbranch_scc1 vv_W95_%=\n\t" \
    "s_max_i32 s69, s69, 0\n\t" \
    "s_lshr_b32 s69, s69, 1\n\t" \
    "s_and_b32 s69, s69, 0x3c\n\t" \
    "s_sub_u32 s30, s30, s69\n\t" \
    "s_subb_u32 s31, s31, 0\n\t" \
    "s_setpc_b64 s[30:31]\n\t" \
    ".p2align 6\n\t" \
    "s_nop 0\n\t" \
    "s_nop 0\n\t" \
    "s_nop 0\n\t" \
    "s_nop 1\n\t" \
    "s_nop 1\n\t" \
    "s_nop 1\n\t" \
    "s_nop 1\n\t" \
    "s_nop 1\n\t" \
    "s_nop 1\n\t" \
    "s_nop 1\n\t" \
    "s_nop 1\n\t" \
    "s_nop 1\n\t" \
    "s_nop 1\n\t" \
    "s_nop 1\n\t" \
    "s_nop 1\n\t" \
    "s_nop 1\n\t" \
    "vv_We97_%=:\n\t" \
    "vv_G98_%=:\n\t" \
    "global_load_dwordx4 v[36:39], v1, s[46:47] sc1\n\t" \
    "s_nop 9\n\t" \
    "global_load_dwordx4 v[40:43], v1, s[46:47] offset:1024 sc1\n\t" \
    "s_nop 9\n\t" \
    "global_load_dwordx4 v[44:47], v2, s[46:47] sc1\n\t" \
    "s_waitcnt vmcnt(2)\n\t" \
    "v_smfmac_f32_16x16x64_bf16 v[72:75], v[36:39], a[48:55], v10\n\t" \
    "ds_write_b128 v0, v[36:39] offset:6144\n\t" \
    "v_smfmac_f32_16x16x64_bf16 v[76:79], v[36:39], a[120:127], v10\n\t" \
    "v_max3_u32 v140, v36, v37, v38\n\t" \
    "s_waitcnt vmcnt(1)\n\t" \
    "ds_write_b128 v0, v[40:43] offset:7168\n\t" \
    "v_smfmac_f32_16x16x64_bf16 v[72:75], v[40:43], a[56:63], v10\n\t" \
    "v_max3_u32 v140, v140, v39, v40\n\t" \
    "v_max3_u32 v140, v140, v41, v42\n\t" \
    "v_max_u32 v140, v140, v43\n\t" \
    "v_cmp_eq_u32 vcc, -1, v140\n\t" \
    "v_smfmac_f32_16x16x64_bf16 v[76:79], v[40:43], a[128:135], v10\n\t" \
    "s_cbranch_vccnz vv_Gbad99_%=\n\t" \
    "vv_Gc101_%=:\n\t" \
    "s_waitcnt lgkmcnt(0)\n\t" \
    "s_barrier\n\t" \
    "ds_read_b128 v[48:51], v0 offset:0\n\t" \
    "ds_read_b128 v[52:55], v0 offset:1024\n\t" \
    "s_waitcnt vmcnt(0)\n\t" \
    "v_smfmac_f32_16x16x64_bf16 v[72:75], v[44:47], a[64:71], v10\n\t" \
    "ds_read_b128 v[56:59], v0 offset:2048\n\t" \
    "ds_read_b128 v[60:63], v0 offset:3072\n\t" \
    "v_max3_u32 v140, v44, v45, v46\n\t" \
    "v_max_u32 v140, v140, v47\n\t" \
    "v_smfmac_f32_16x16x64_bf16 v[76:79], v[44:47], a[136:143], v10\n\t" \
    "ds_read_b128 v[64:67], v0 offset:4096\n\t" \
    "ds_read_b128 v[68:71], v0 offset:5120\n\t" \
    "v_cmp_eq_u32 vcc, -1, v140\n\t" \
    "s_cbranch_vccnz vv_Gbad2100_%=\n\t" \
    "vv_Gc2102_%=:\n\t" \
    "s_waitcnt lgkmcnt(5)\n\t" \
    "v_smfmac_f32_16x16x64_bf16 v[72:75], v[48:51], a[0:7], v10\n\t" \
    "s_waitcnt lgkmcnt(4)\n\t" \
    "v_smfmac_f32_16x16x64_bf16 v[72:75], v[52:55], a[8:15], v10\n\t" \
    "s_waitcnt lgkmcnt(3)\n\t" \
    "v_smfmac_f32_16x16x64_bf16 v[72:75], v[56:59], a[16:23], v10\n\t" \
    "s_waitcnt lgkmcnt(2)\n\t" \
    "v_smfmac_f32_16x16x64_bf16 v[72:75], v[60:63], a[24:31], v10\n\t" \
    "s_waitcnt lgkmcnt(1)\n\t" \
    "v_smfmac_f32_16x16x64_bf16 v[72:75], v[64:67], a[32:39], v10\n\t" \
    "s_waitcnt lgkmcnt(0)\n\t" \
    "v_smfmac_f32_16x16x64_bf16 v[72:75], v[68:71], a[40:47], v10\n\t" \
    "v_smfmac_f32_16x16x64_bf16 v[76:79], v[48:51], a[72:79], v10\n\t" \
    "s_nop 1\n\t" \
    "s_nop 0\n\t" \
    "v_smfmac_f32_16x16x64_bf16 v[76:79], v[52:55], a[80:87], v10\n\t" \
    "s_nop 1\n\t" \
    "s_nop 0\n\t" \
    "v_smfmac_f32_16x16x64_bf16 v[76:79], v[56:59], a[88:95], v10\n\t" \
    "v_add_f32 v72, v72, v74\n\t" \
    "v_smfmac_f32_16x16x64_bf16 v[76:79], v[60:63], a[96:103], v10\n\t" \
    "v_add_f32 v73, v73, v75\n\t" \
    "v_smfmac_f32_16x16x64_bf16 v[76:79], v[64:67], a[104:111], v10\n\t" \
    "v_cvt_pk_bf16_f32 v80, v72, v73\n\t" \
    "v_smfmac_f32_16x16x64_bf16 v[76:79], v[68:71], a[112:119], v10\n\t" \
    "ds_write_b32 v12, v80 offset:0\n\t" \
    "s_nop 6\n\t" \
    "v_add_f32 v76, v76, v78\n\t" \
    "v_add_f32 v77, v77, v79\n\t" \
    "v_cvt_pk_bf16_f32 v81, v76, v77\n\t" \
    "ds_write_b32 v12, v81 offset:64\n\t" \
    "s_waitcnt lgkmcnt(0)\n\t" \
    "s_barrier\n\t" \
    "ds_read_b64_tr_b16 v[84:85], v13 offset:0\n\t" \
    "ds_read_b64_tr_b16 v[86:87], v13 offset:128\n\t" \
    "ds_read_b64_tr_b16 v[88:89], v13 offset:1024\n\t" \
    "ds_read_b64_tr_b16 v[90:91], v13 offset:1152\n\t" \
    "s_waitcnt lgkmcnt(2)\n\t" \
    "v_smfmac_f32_16x16x64_bf16 v[92:95], v[84:87], a[144:151], v10\n\t" \
    "s_nop 2\n\t" \
    "s_waitcnt lgkmcnt(0)\n\t" \
    "v_smfmac_f32_16x16x64_bf16 v[92:95], v[88:91], a[152:159], v10\n\t" \
    "s_nop 2\n\t" \
    "v_smfmac_f32_16x16x64_bf16 v[96:99], v[84:87], a[160:167], v10\n\t" \
    "s_nop 2\n\t" \
    "v_smfmac_f32_16x16x64_bf16 v[96:99], v[88:91], a[168:175], v10\n\t" \
    "s_nop 2\n\t" \
    "v_smfmac_f32_16x16x64_bf16 v[100:103], v[84:87], a[176:183], v10\n\t" \
    "s_nop 0\n\t" \
    "v_add_f32 v92, v92, v94\n\t" \
    "v_add_f32 v93, v93, v95\n\t" \
    "v_smfmac_f32_16x16x64_bf16 v[100:103], v[88:91], a[184:191], v10\n\t" \
    "s_nop 1\n\t" \
    "v_cvt_pk_bf16_f32 v128, v92, v93\n\t" \
    "v_smfmac_f32_16x16x64_bf16 v[104:107], v[84:87], a[192:199], v10\n\t" \
    "s_nop 0\n\t" \
    "v_add_f32 v96, v96, v98\n\t" \
    "v_add_f32 v97, v97, v99\n\t" \
    "v_smfmac_f32_16x16x64_bf16 v[104:107], v[88:91], a[200:207], v10\n\t" \
    "s_nop 1\n\t" \
    "v_cvt_pk_bf16_f32 v129, v96, v97\n\t" \
    "v_smfmac_f32_16x16x64_bf16 v[124:127], v[84:87], v[240:247], v10\n\t" \
    "s_nop 0\n\t" \
    "v_add_f32 v100, v100, v102\n\t" \
    "v_add_f32 v101, v101, v103\n\t" \
    "v_smfmac_f32_16x16x64_bf16 v[124:127], v[88:91], v[248:255], v10\n\t" \
    "s_nop 1\n\t" \
    "global_load_dword v140, v20, s[46:47] sc1\n\t" \
    "v_cvt_pk_bf16_f32 v130, v100, v101\n\t" \
    "v_smfmac_f32_16x16x64_bf16 v[108:111], v[84:87], a[208:215], v10\n\t" \
    "s_nop 0\n\t" \
    "v_add_f32 v104, v104, v106\n\t" \
    "v_add_f32 v105, v105, v107\n\t" \
    "v_smfmac_f32_16x16x64_bf16 v[108:111], v[88:91], a[216:223], v10\n\t" \
    "s_nop 0\n\t" \
    "v_cvt_pk_bf16_f32 v131, v104, v105\n\t" \
    "v_permlane32_swap_b32 v128, v130\n\t" \
    "v_smfmac_f32_16x16x64_bf16 v[112:115], v[84:87], a[224:231], v10\n\t" \
    "s_nop 0\n\t" \
    "v_permlane32_swap_b32 v129, v131\n\t" \
    "s_nop 1\n\t" \
    "v_permlane16_swap_b32 v128, v129\n\t" \
    "v_smfmac_f32_16x16x64_bf16 v[112:115], v[88:91], a[232:239], v10\n\t" \
    "v_permlane16_swap_b32 v130, v131\n\t" \
    "global_store_dwordx4 v14, v[128:131], s[38:39] sc1\n\t" \
    "v_add_f32 v124, v124, v126\n\t" \
    "v_smfmac_f32_16x16x64_bf16 v[116:119], v[84:87], a[240:247], v10\n\t" \
    "s_nop 0\n\t" \
    "v_add_f32 v125, v125, v127\n\t" \
    "v_cvt_pk_bf16_f32 v136, v124, v125\n\t" \
    "v_smfmac_f32_16x16x64_bf16 v[116:119], v[88:91], a[248:255], v10\n\t" \
    "global_store_dword v16, v136, s[38:39] sc1\n\t" \
    "v_add_f32 v108, v108, v110\n\t" \
    "v_add_f32 v109, v109, v111\n\t" \
    "v_smfmac_f32_16x16x64_bf16 v[120:123], v[84:87], v[224:231], v10\n\t" \
    "s_nop 0\n\t" \
    "v_cvt_pk_bf16_f32 v132, v108, v109\n\t" \
    "v_add_f32 v112, v112, v114\n\t" \
    "v_smfmac_f32_16x16x64_bf16 v[120:123], v[88:91], v[232:239], v10\n\t" \
    "v_add_f32 v113, v113, v115\n\t" \
    "v_cvt_pk_bf16_f32 v133, v112, v113\n\t" \
    "v_add_f32 v116, v116, v118\n\t" \
    "v_add_f32 v117, v117, v119\n\t" \
    "v_cvt_pk_bf16_f32 v134, v116, v117\n\t" \
    "s_nop 2\n\t" \
    "v_pk_add_f32 v[120:121], v[120:121], v[122:123]\n\t" \
    "v_cvt_pk_bf16_f32 v135, v120, v121\n\t" \
    "v_permlane32_swap_b32 v132, v134\n\t" \
    "s_nop 0\n\t" \
    "v_permlane32_swap_b32 v133, v135\n\t" \
    "s_nop 1\n\t" \
    "v_permlane16_swap_b32 v132, v133\n\t" \
    "v_permlane16_swap_b32 v134, v135\n\t" \
    "global_store_dwordx4 v15, v[132:135], s[38:39] sc1\n\t" \
    "s_nop 1\n\t" \
    "global_store_dwordx4 v17, v[128:131], s[40:41] sc1\n\t" \
    "global_store_dwordx4 v17, v[132:135], s[40:41] offset:1024 sc1\n\t" \
    "global_store_dword v18, v136, s[40:41] sc1\n\t" \
    "s_cmp_eq_u32 s65, 63\n\t" \
    "s_cbranch_scc1 vv_end93_%=\n\t" \
    "s_mov_b32 s71, 3\n\t" \
    "vv_E104_%=:\n\t" \
    "s_memtime s[54:55]\n\t" \
    "s_nop 0\n\t" \
    "s_memrealtime s[4:5]\n\t" \
    "s_nop 0\n\t" \
    "s_memrealtime s[6:7]\n\t" \
    "s_nop 0\n\t" \
    "s_memrealtime s[8:9]\n\t" \
    "s_nop 0\n\t" \
    "s_memrealtime s[10:11]\n\t" \
    "s_nop 0\n\t" \
    "s_memrealtime s[12:13]\n\t" \
    "s_nop 0\n\t" \
    "s_memrealtime s[14:15]\n\t" \
    "s_nop 0\n\t" \
    "s_memrealtime s[16:17]\n\t" \
    "s_nop 0\n\t" \
    "s_memrealtime s[18:19]\n\t" \
    "s_nop 0\n\t" \
    "s_memrealtime s[20:21]\n\t" \
    "s_nop 0\n\t" \
    "s_memrealtime s[22:23]\n\t" \
    "s_nop 0\n\t" \
    "s_memrealtime s[24:25]\n\t" \
    "s_nop 0\n\t" \
    "s_memrealtime s[26:27]\n\t" \
    "s_nop 0\n\t" \
    "s_memrealtime s[28:29]\n\t" \
    "s_nop 0\n\t" \
    "s_memtime s[56:57]\n\t" \
    "s_mov_b32 s70, 1\n\t" \
    "s_sub_u32 s71, s71, 1\n\t" \
    "s_cselect_b32 s69, 0, -1\n\t" \
    "s_waitcnt lgkmcnt(0)\n\t" \
    "s_sub_u32 s54, s56, s54\n\t" \
    "s_and_b32 s54, s54, s69\n\t" \
    "s_cmp_gt_u32 s54, 124\n\t" \
    "s_cbranch_scc1 vv_E104_%=\n\t" \
    "s_cmp_eq_u32 s4, s28\n\t" \
    "s_addc_u32 s70, s70, 0\n\t" \
    "s_cmp_eq_u32 s6, s28\n\t" \
    "s_addc_u32 s70, s70, 0\n\t" \
    "s_cmp_eq_u32 s8, s28\n\t" \
    "s_addc_u32 s70, s70, 0\n\t" \
    "s_cmp_eq_u32 s10, s28\n\t" \
    "s_addc_u32 s70, s70, 0\n\t" \
    "s_cmp_eq_u32 s12, s28\n\t" \
    "s_addc_u32 s70, s70, 0\n\t" \
    "s_cmp_eq_u32 s14, s28\n\t" \
    "s_addc_u32 s70, s70, 0\n\t" \
    "s_cmp_eq_u32 s16, s28\n\t" \
    "s_addc_u32 s70, s70, 0\n\t" \
    "s_cmp_eq_u32 s18, s28\n\t" \
    "s_addc_u32 s70, s70, 0\n\t" \
    "s_cmp_eq_u32 s20, s28\n\t" \
    "s_addc_u32 s70, s70, 0\n\t" \
    "s_cmp_eq_u32 s22, s28\n\t" \
    "s_addc_u32 s70, s70, 0\n\t" \
    "s_cmp_eq_u32 s24, s28\n\t" \
    "s_addc_u32 s70, s70, 0\n\t" \
    "s_cmp_eq_u32 s26, s28\n\t" \
    "s_addc_u32 s70, s70, 0\n\t" \
    "s_mov_b32 s75, s73\n\t" \
    "s_mov_b32 s76, s74\n\t" \
    "s_lshl_b32 s70, s70, 3\n\t" \
    "s_sub_u32 s73, s56, s70\n\t" \
    "s_lshl_b32 s74, s28, 4\n\t" \
    "s_sub_u32 s69, s73, s75\n\t" \
    "s_sub_u32 s70, s74, s76\n\t" \
    "s_sub_u32 s71, s70, 1600\n\t" \
    "s_cmp_lt_u32 s71, 400000\n\t" \
    "s_cbranch_scc0 vv_K106_%=\n\t" \
    "v_cvt_f32_u32 v141, s69\n\t" \
    "v_cvt_f32_u32 v142, s70\n\t" \
    "v_rcp_f32 v143, v141\n\t" \
    "v_rcp_f32 v144, v142\n\t" \
    "v_mul_f32 v143, v143, v142\n\t" \
    "v_mul_f32 v144, v144, v141\n\t" \
    "v_mul_f32 v143, 0x4f800000, v143\n\t" \
    "v_mul_f32 v144, 0x4f000000, v144\n\t" \
    "v_cvt_u32_f32 v143, v143\n\t" \
    "v_cvt_u32_f32 v144, v144\n\t" \
    "v_readfirstlane_b32 s69, v143\n\t" \
    "v_readfirstlane_b32 s70, v144\n\t" \
    "s_nop 1\n\t" \
    "s_sub_u32 s69, s69, s78\n\t" \
    "s_ashr_i32 s69, s69, 2\n\t" \
    "s_add_u32 s78, s78, s69\n\t" \
    "s_sub_u32 s70, s70, s77\n\t" \
    "s_ashr_i32 s70, s70, 2\n\t" \
    "s_add_u32 s77, s77, s70\n\t" \
    "vv_K106_%=:\n\t" \
    "s_waitcnt vmcnt(6)\n\t" \
    "v_cmp_eq_u32 vcc, -1, v140\n\t" \
    "s_cbranch_vccz vv_Hok108_%=\n\t" \
    "vv_H107_%=:\n\t" \
    "global_load_dword v140, v20, s[46:47] sc1\n\t" \
    "s_waitcnt vmcnt(0)\n\t" \
    "v_cmp_eq_u32 vcc, -1, v140\n\t" \
    "s_cbranch_vccz vv_Hc109_%=\n\t" \
    "s_cmp_eq_u32 s68, 0\n\t" \
    "s_cbranch_scc1 vv_F110_%=\n\t" \
    "s_sub_u32 s68, s68, 1\n\t" \
    "s_sleep 5\n\t" \
    "s_branch vv_H107_%=\n\t" \
    "vv_F110_%=:\n\t" \
    "v_mov_b32 v141, 6095104\n\t" \
    "v_mov_b32 v142, 6\n\t" \
    "global_store_dword v141, v142, s[36:37] sc1\n\t" \
    "s_waitcnt vmcnt(0)\n\t" \
    "s_branch vv_Hc109_%=\n\t" \
    "vv_Hc109_%=:\n\t" \
    "vv_Hok108_%=:\n\t" \
    "v_subrev_u32 v140, s80, v140\n\t" \
    "v_and_b32 v20, -5, v20\n\t" \
    "s_nop 0\n\t" \
    "v_max_i32_dpp v140, v140, v140 row_ror:1 row_mask:0xf bank_mask:0xf\n\t" \
    "s_nop 1\n\t" \
    "v_max_i32_dpp v140, v140, v140 row_ror:2 row_mask:0xf bank_mask:0xf\n\t" \
    "s_nop 1\n\t" \
    "v_max_i32_dpp v140, v140, v140 row_ror:4 row_mask:0xf bank_mask:0xf\n\t" \
    "s_nop 1\n\t" \
    "v_max_i32_dpp v140, v140, v140 row_ror:8 row_mask:0xf bank_mask:0xf\n\t" \
    "s_nop 0\n\t" \
    "v_readlane_b32 s69, v140, 0\n\t" \
    "v_readlane_b32 s71, v140, 16\n\t" \
    "v_readlane_b32 s70, v140, 32\n\t" \
    "v_readlane_b32 s81, v140, 48\n\t" \
    "s_max_i32 s69, s69, s71\n\t" \
    "s_nop 0\n\t" \
    "s_max_i32 s71, s70, s81\n\t" \
    "s_max_i32 s69, s69, s71\n\t" \
    "s_add_u32 s81, s69, s80\n\t" \
    "v_mov_b32 v72, 0\n\t" \
    "v_mov_b32 v73, 0\n\t" \
    "v_mov_b32 v74, 0\n\t" \
    "v_mov_b32 v75, 0\n\t" \
    "v_mov_b32 v76, 0\n\t" \
    "v_mov_b32 v77, 0\n\t" \
    "v_mov_b32 v78, 0\n\t" \
    "v_mov_b32 v79, 0\n\t" \
    "v_mov_b32 v92, 0\n\t" \
    "v_mov_b32 v93, 0\n\t" \
    "v_mov_b32 v94, 0\n\t" \
    "v_mov_b32 v95, 0\n\t" \
    "v_mov_b32 v96, 0\n\t" \
    "v_mov_b32 v97, 0\n\t" \
    "v_mov_b32 v98, 0\n\t" \
    "v_mov_b32 v99, 0\n\t" \
    "v_mov_b32 v100, 0\n\t" \
    "v_mov_b32 v101, 0\n\t" \
    "v_mov_b32 v102, 0\n\t" \
    "v_mov_b32 v103, 0\n\t" \
    "v_mov_b32 v104, 0\n\t" \
    "v_mov_b32 v105, 0\n\t" \
    "v_mov_b32 v106, 0\n\t" \
    "v_mov_b32 v107, 0\n\t" \
    "v_mov_b32 v108, 0\n\t" \
    "v_mov_b32 v109, 0\n\t" \
    "v_mov_b32 v110, 0\n\t" \
    "v_mov_b32 v111, 0\n\t" \
    "v_mov_b32 v112, 0\n\t" \
    "v_mov_b32 v113, 0\n\t" \
    "v_mov_b32 v114, 0\n\t" \
    "v_mov_b32 v115, 0\n\t" \
    "v_mov_b32 v116, 0\n\t" \
    "v_mov_b32 v117, 0\n\t" \
    "v_mov_b32 v118, 0\n\t" \
    "v_mov_b32 v119, 0\n\t" \
    "v_mov_b32 v120, 0\n\t" \
    "v_mov_b32 v121, 0\n\t" \
    "v_mov_b32 v122, 0\n\t" \
    "v_mov_b32 v123, 0\n\t" \
    "v_mov_b32 v124, 0\n\t" \
    "v_mov_b32 v125, 0\n\t" \
    "v_mov_b32 v126, 0\n\t" \
    "v_mov_b32 v127, 0\n\t" \
    "s_add_u32 s79, s81, 400\n\t" \
    "s_sub_u32 s70, s79, s74\n\t" \
    "s_max_i32 s70, s70, 0\n\t" \
    "s_min_i32 s70, s70, 16000\n\t" \
    "s_lshl_b32 s70, s70, 1\n\t" \
    "s_mul_hi_u32 s70, s70, s77\n\t" \
    "s_add_u32 s70, s70, s73\n\t" \
    "vv_W114_%=:\n\t" \
    "s_memtime s[56:57]\n\t" \
    "s_waitcnt lgkmcnt(0)\n\t" \
    "s_sub_u32 s69, s70, s56\n\t" \
    "s_cmp_gt_i32 s69, 24\n\t" \
    "s_cbranch_scc1 vv_W114_%=\n\t" \
    "global_store_dwordx4 v14, v[148:151], s[42:43] sc1\n\t" \
    "global_store_dwordx4 v15, v[148:151], s[42:43] sc1\n\t" \
    "global_store_dword v16, v148, s[42:43] sc1\n\t" \
    "s_cmp_lg_u32 s65, 0\n\t" \
    "s_cbranch_scc1 vv_Oc113_%=\n\t" \
    "s_add_u32 s79, s81, 240\n\t" \
    "s_sub_u32 s70, s79, s74\n\t" \
    "s_max_i32 s70, s70, 0\n\t" \
    "s_min_i32 s70, s70, 16000\n\t" \
    "s_lshl_b32 s70, s70, 1\n\t" \
    "s_mul_hi_u32 s70, s70, s77\n\t" \
    "s_add_u32 s70, s70, s73\n\t" \
    "vv_W115_%=:\n\t" \
    "s_memtime s[56:57]\n\t" \
    "s_waitcnt lgkmcnt(0)\n\t" \
    "s_sub_u32 s69, s70, s56\n\t" \
    "s_cmp_gt_i32 s69, 24\n\t" \
    "s_cbranch_scc1 vv_W115_%=\n\t" \
    "global_load_dword v140, v35, s[46:47] sc1\n\t" \
    "s_waitcnt vmcnt(0)\n\t" \
    "v_cmp_eq_u32 vcc, -1, v140\n\t" \
    "s_cbranch_vccz vv_Ook112_%=\n\t" \
    "vv_O111_%=:\n\t" \
    "global_load_dword v140, v35, s[46:47] offset:1536 sc1\n\t" \
    "s_waitcnt vmcnt(0)\n\t" \
    "v_cmp_eq_u32 vcc, -1, v140\n\t" \
    "s_cbranch_vccz vv_Ook112_%=\n\t" \
    "s_cmp_eq_u32 s68, 0\n\t" \
    "s_cbranch_scc1 vv_F116_%=\n\t" \
    "s_sub_u32 s68, s68, 1\n\t" \
    "s_sleep 3\n\t" \
    "s_branch vv_O111_%=\n\t" \
    "vv_F116_%=:\n\t" \
    "v_mov_b32 v141, 6095104\n\t" \
    "v_mov_b32 v142, 17\n\t" \
    "global_store_dword v141, v142, s[36:37] sc1\n\t" \
    "s_waitcnt vmcnt(0)\n\t" \
    "s_branch vv_Ook112_%=\n\t" \
    "vv_Ook112_%=:\n\t" \
    "v_subrev_u32 v140, s80, v140\n\t" \
    "s_nop 1\n\t" \
    "v_max_i32_dpp v140, v140, v140 row_ror:1 row_mask:0xf bank_mask:0xf\n\t" \
    "s_nop 1\n\t" \
    "v_max_i32_dpp v140, v140, v140 row_ror:2 row_mask:0xf bank_mask:0xf\n\t" \
    "s_nop 1\n\t" \
    "v_max_i32_dpp v140, v140, v140 row_ror:4 row_mask:0xf bank_mask:0xf\n\t" \
    "s_nop 1\n\t" \
    "v_max_i32_dpp v140, v140, v140 row_ror:8 row_mask:0xf bank_mask:0xf\n\t" \
    "s_nop 0\n\t" \
    "v_readlane_b32 s69, v140, 0\n\t" \
    "v_readlane_b32 s71, v140, 16\n\t" \
    "v_readlane_b32 s70, v140, 32\n\t" \
    "v_readlane_b32 s81, v140, 48\n\t" \
    "s_max_i32 s69, s69, s71\n\t" \
    "s_nop 0\n\t" \
    "s_max_i32 s71, s70, s81\n\t" \
    "s_max_i32 s69, s69, s71\n\t" \
    "s_add_u32 s81, s69, s80\n\t" \
    "vv_Oc113_%=:\n\t" \
    "s_mul_hi_u32 s69, s85, s78\n\t" \
    "s_add_u32 s79, s81, s69\n\t" \
    "s_add_u32 s79, s79, s87\n\t" \
    "s_cmp_ge_u32 s65, 36\n\t" \
    "s_cselect_b32 s69, 28, 0\n\t" \
    "s_sub_u32 s79, s79, s69\n\t" \
    "s_add_u32 s80, s79, s88\n\t" \
    "s_add_u32 s65, s65, 1\n\t" \
    "s_branch vv_go94_%=\n\t" \
    "vv_Gbad99_%=:\n\t" \
    "s_waitcnt vmcnt(0) lgkmcnt(0)\n\t" \
    "s_nop 7\n\t" \
    "v_mov_b32 v72, 0\n\t" \
    "v_mov_b32 v73, 0\n\t" \
    "v_mov_b32 v74, 0\n\t" \
    "v_mov_b32 v75, 0\n\t" \
    "v_mov_b32 v76, 0\n\t" \
    "v_mov_b32 v77, 0\n\t" \
    "v_mov_b32 v78, 0\n\t" \
    "v_mov_b32 v79, 0\n\t" \
    "s_cmp_eq_u32 s68, 0\n\t" \
    "s_cbranch_scc1 vv_F117_%=\n\t" \
    "s_sub_u32 s68, s68, 1\n\t" \
    "s_branch vv_G98_%=\n\t" \
    "vv_F117_%=:\n\t" \
    "v_mov_b32 v140, 6095104\n\t" \
    "v_mov_b32 v141, 1\n\t" \
    "global_store_dword v140, v141, s[36:37] sc1\n\t" \
    "s_waitcnt vmcnt(0)\n\t" \
    "s_branch vv_Gc101_%=\n\t" \
    "vv_Gbad2100_%=:\n\t" \
    "global_load_dwordx4 v[44:47], v2, s[46:47] sc1\n\t" \
    "s_waitcnt vmcnt(0)\n\t" \
    "v_max3_u32 v140, v44, v45, v46\n\t" \
    "v_max_u32 v140, v140, v47\n\t" \
    "v_cmp_eq_u32 vcc, -1, v140\n\t" \
    "s_cbranch_vccz vv_Gre2103_%=\n\t" \
    "s_cmp_eq_u32 s68, 0\n\t" \
    "s_cbranch_scc1 vv_F118_%=\n\t" \
    "s_sub_u32 s68, s68, 1\n\t" \
    "s_sleep 1\n\t" \
    "s_branch vv_Gbad2100_%=\n\t" \
    "vv_F118_%=:\n\t" \
    "v_mov_b32 v140, 6095104\n\t" \
    "v_mov_b32 v141, 2\n\t" \
    "global_store_dword v140, v141, s[36:37] sc1\n\t" \
    "s_waitcnt vmcnt(0)\n\t" \
    "s_branch vv_Gre2103_%=\n\t" \
    "vv_Gre2103_%=:\n\t" \
    "v_mov_b32 v72, 0\n\t" \
    "v_mov_b32 v73, 0\n\t" \
    "v_mov_b32 v74, 0\n\t" \
    "v_mov_b32 v75, 0\n\t" \
    "v_mov_b32 v76, 0\n\t" \
    "v_mov_b32 v77, 0\n\t" \
    "v_mov_b32 v78, 0\n\t" \
    "v_mov_b32 v79, 0\n\t" \
    "v_smfmac_f32_16x16x64_bf16 v[72:75], v[36:39], a[48:55], v10\n\t" \
    "s_nop 0\n\t" \
    "v_smfmac_f32_16x16x64_bf16 v[76:79], v[36:39], a[120:127], v10\n\t" \
    "v_smfmac_f32_16x16x64_bf16 v[72:75], v[40:43], a[56:63], v10\n\t" \
    "v_smfmac_f32_16x16x64_bf16 v[76:79], v[40:43], a[128:135], v10\n\t" \
    "v_smfmac_f32_16x16x64_bf16 v[72:75], v[44:47], a[64:71], v10\n\t" \
    "v_smfmac_f32_16x16x64_bf16 v[76:79], v[44:47], a[136:143], v10\n\t" \
    "s_branch vv_Gc2102_%=\n\t" \
    "vv_end93_%=:\n\t" \
    "s_endpgm\n\t" \
    "gx_begin_%=:\n\t" \
    "v_mbcnt_lo_u32_b32 v223, -1, 0\n\t" \
    "v_mbcnt_hi_u32_b32 v223, -1, v223\n\t" \
    "s_mov_b32 s68, 262144\n\t" \
    "s_mov_b32 s72, 0\n\t" \
    "s_mov_b32 s83, -1\n\t" \
    "s_mov_b32 s65, 0\n\t" \
    "v_and_b32 v180, 3, v223\n\t" \
    "v_lshlrev_b32 v202, 3, v180\n\t" \
    "v_lshrrev_b32 v181, 5, v223\n\t" \
    "v_lshl_add_u32 v202, v181, 12, v202\n\t" \
    "v_bfe_u32 v182, v223, 2, 2\n\t" \
    "s_mov_b32 s70, 288\n\t" \
    "v_mad_u32_u24 v202, v182, s70, v202\n\t" \
    "v_bfe_u32 v183, v223, 4, 1\n\t" \
    "s_mov_b32 s70, 2176\n\t" \
    "v_mad_u32_u24 v202, v183, s70, v202\n\t" \
    "s_lshl_b32 s69, s66, 6\n\t" \
    "v_xor_b32 v202, s69, v202\n\t" \
    "v_xor_b32 v203, 32, v202\n\t" \
    "v_lshlrev_b32 v3, 4, v180\n\t" \
    "v_lshlrev_b32 v2, 5, v181\n\t" \
    "v_xor_b32 v3, v3, v2\n\t" \
    "s_bfe_u32 s71, s66, 0x10000\n\t" \
    "s_mul_i32 s69, s71, 576\n\t" \
    "s_bfe_u32 s71, s66, 0x10001\n\t" \
    "s_mul_i32 s71, s71, 18432\n\t" \
    "s_add_u32 s69, s69, s71\n\t" \
    "v_lshrrev_b32 v184, 2, v223\n\t" \
    "v_mov_b32 v1, s69\n\t" \
    "s_mov_b32 s70, 1152\n\t" \
    "v_mad_u32_u24 v1, v184, s70, v1\n\t" \
    "v_add_u32 v1, v1, v3\n\t" \
    "v_and_b32 v185, 15, v223\n\t" \
    "v_lshlrev_b32 v3, 6, v185\n\t" \
    "v_lshrrev_b32 v186, 4, v223\n\t" \
    "v_lshl_add_u32 v3, v186, 4, v3\n\t" \
    "v_bfe_u32 v187, v223, 3, 1\n\t" \
    "v_lshlrev_b32 v2, 5, v187\n\t" \
    "v_xor_b32 v3, v3, v2\n\t" \
    "v_mov_b32 v2, v3\n\t" \
    "s_mul_i32 s89, s66, 9216\n\t" \
    "s_mov_b32 s90, 0\n\t" \
    "s_waitcnt vmcnt(12) lgkmcnt(0)\n\t" \
    "s_barrier\n\t" \
    "s_add_u32 m0, s82, 65536\n\t" \
    "s_nop 0\n\t" \
    "global_load_lds_dwordx4 v200, s[42:43]\n\t" \
    "v_add_u32 v200, 131072, v200\n\t" \
    "s_add_u32 m0, s82, 69632\n\t" \
    "s_nop 0\n\t" \
    "global_load_lds_dwordx4 v200, s[42:43]\n\t" \
    "v_add_u32 v200, 131072, v200\n\t" \
    "s_add_u32 m0, s82, 73728\n\t" \
    "s_nop 0\n\t" \
    "global_load_lds_dwordx4 v200, s[42:43]\n\t" \
    "v_add_u32 v200, 131072, v200\n\t" \
    "s_add_u32 m0, s82, 77824\n\t" \
    "s_nop 0\n\t" \
    "global_load_lds_dwordx4 v200, s[42:43]\n\t" \
    "v_add_u32 v200, 131072, v200\n\t" \
    "v_add_u32 v202, 73728, v202\n\t" \
    "v_add_u32 v203, 73728, v203\n\t" \
    "ds_read_b64_tr_b16 a[144:145], v202 offset:0\n\t" \
    "ds_read_b64_tr_b16 a[146:147], v202 offset:1024\n\t" \
    "ds_read_b64_tr_b16 a[148:149], v202 offset:8192\n\t" \
    "ds_read_b64_tr_b16 a[150:151], v202 offset:9216\n\t" \
    "ds_read_b64_tr_b16 a[216:217], v203 offset:0\n\t" \
    "ds_read_b64_tr_b16 a[218:219], v203 offset:1024\n\t" \
    "ds_read_b64_tr_b16 a[220:221], v203 offset:8192\n\t" \
    "ds_read_b64_tr_b16 a[222:223], v203 offset:9216\n\t" \
    "v_lshlrev_b32 v0, 4, v223\n\t" \
    "s_mul_i32 s69, s66, 2048\n\t" \
    "s_add_u32 s69, s69, 147456\n\t" \
    "v_lshl_add_u32 v4, v223, 4, s69\n\t" \
    "s_mov_b32 s69, 147456\n\t" \
    "v_lshl_add_u32 v5, v223, 4, s69\n\t" \
    "v_lshlrev_b32 v6, 2, v223\n\t" \
    "v_mov_b32 v7, 0\n\t" \
    "s_mul_i32 s69, s66, 2048\n\t" \
    "v_lshl_add_u32 v8, v223, 4, s69\n\t" \
    "s_waitcnt vmcnt(12) lgkmcnt(0)\n\t" \
    "s_barrier\n\t" \
    "s_add_u32 m0, s82, 0\n\t" \
    "s_nop 0\n\t" \
    "global_load_lds_dwordx4 v200, s[42:43]\n\t" \
    "v_add_u32 v200, 131072, v200\n\t" \
    "s_add_u32 m0, s82, 4096\n\t" \
    "s_nop 0\n\t" \
    "global_load_lds_dwordx4 v200, s[42:43]\n\t" \
    "v_add_u32 v200, 131072, v200\n\t" \
    "s_add_u32 m0, s82, 8192\n\t" \
    "s_nop 0\n\t" \
    "global_load_lds_dwordx4 v200, s[42:43]\n\t" \
    "v_add_u32 v200, 131072, v200\n\t" \
    "s_add_u32 m0, s82, 12288\n\t" \
    "s_nop 0\n\t" \
    "global_load_lds_dwordx4 v200, s[42:43]\n\t" \
    "v_add_u32 v200, 131072, v200\n\t" \
    "ds_read_b64_tr_b16 a[152:153], v202 offset:16384\n\t" \
    "ds_read_b64_tr_b16 a[154:155], v202 offset:17408\n\t" \
    "ds_read_b64_tr_b16 a[156:157], v202 offset:24576\n\t" \
    "ds_read_b64_tr_b16 a[158:159], v202 offset:25600\n\t" \
    "ds_read_b64_tr_b16 a[224:225], v203 offset:16384\n\t" \
    "ds_read_b64_tr_b16 a[226:227], v203 offset:17408\n\t" \
    "ds_read_b64_tr_b16 a[228:229], v203 offset:24576\n\t" \
    "ds_read_b64_tr_b16 a[230:231], v203 offset:25600\n\t" \
    "s_waitcnt vmcnt(12) lgkmcnt(0)\n\t" \
    "s_barrier\n\t" \
    "s_add_u32 m0, s82, 16384\n\t" \
    "s_nop 0\n\t" \
    "global_load_lds_dwordx4 v200, s[42:43]\n\t" \
    "v_add_u32 v200, 131072, v200\n\t" \
    "s_add_u32 m0, s82, 20480\n\t" \
    "s_nop 0\n\t" \
    "global_load_lds_dwordx4 v200, s[42:43]\n\t" \
    "v_add_u32 v200, 131072, v200\n\t" \
    "s_add_u32 m0, s82, 24576\n\t" \
    "s_nop 0\n\t" \
    "global_load_lds_dwordx4 v200, s[42:43]\n\t" \
    "v_add_u32 v200, 131072, v200\n\t" \
    "s_add_u32 m0, s82, 28672\n\t" \
    "s_nop 0\n\t" \
    "global_load_lds_dwordx4 v200, s[42:43]\n\t" \
    "v_add_u32 v200, 131072, v200\n\t" \
    "s_add_u32 m0, s89, 0\n\t" \
    "s_nop 0\n\t" \
    "global_load_lds_dwordx4 v1, s[50:51] offset:0\n\t" \
    "s_add_u32 m0, s89, 960\n\t" \
    "s_nop 0\n\t" \
    "global_load_lds_dwordx4 v1, s[50:51] offset:64\n\t" \
    "s_add_u32 m0, s89, 1920\n\t" \
    "s_nop 0\n\t" \
    "global_load_lds_dwordx4 v1, s[50:51] offset:128\n\t" \
    "s_add_u32 m0, s89, 2880\n\t" \
    "s_nop 0\n\t" \
    "global_load_lds_dwordx4 v1, s[50:51] offset:192\n\t" \
    "s_add_u32 m0, s89, 3840\n\t" \
    "s_nop 0\n\t" \
    "global_load_lds_dwordx4 v1, s[50:51] offset:256\n\t" \
    "s_add_u32 m0, s89, 4800\n\t" \
    "s_nop 0\n\t" \
    "global_load_lds_dwordx4 v1, s[50:51] offset:320\n\t" \
    "s_add_u32 m0, s89, 5760\n\t" \
    "s_nop 0\n\t" \
    "global_load_lds_dwordx4 v1, s[50:51] offset:384\n\t" \
    "s_add_u32 m0, s89, 6720\n\t" \
    "s_nop 0\n\t" \
    "global_load_lds_dwordx4 v1, s[50:51] offset:448\n\t" \
    "s_add_u32 m0, s89, 7680\n\t" \
    "s_nop 0\n\t" \
    "global_load_lds_dwordx4 v1, s[50:51] offset:512\n\t" \
    "ds_read_b64_tr_b16 a[160:161], v202 offset:32768\n\t" \
    "ds_read_b64_tr_b16 a[162:163], v202 offset:33792\n\t" \
    "ds_read_b64_tr_b16 a[164:165], v202 offset:40960\n\t" \
    "ds_read_b64_tr_b16 a[166:167], v202 offset:41984\n\t" \
    "ds_read_b64_tr_b16 a[232:233], v203 offset:32768\n\t" \
    "ds_read_b64_tr_b16 a[234:235], v203 offset:33792\n\t" \
    "ds_read_b64_tr_b16 a[236:237], v203 offset:40960\n\t" \
    "ds_read_b64_tr_b16 a[238:239], v203 offset:41984\n\t" \
    "s_waitcnt vmcnt(21) lgkmcnt(0)\n\t" \
    "s_barrier\n\t" \
    "s_add_u32 m0, s82, 32768\n\t" \
    "s_nop 0\n\t" \
    "global_load_lds_dwordx4 v200, s[42:43]\n\t" \
    "v_add_u32 v200, 131072, v200\n\t" \
    "s_add_u32 m0, s82, 36864\n\t" \
    "s_nop 0\n\t" \
    "global_load_lds_dwordx4 v200, s[42:43]\n\t" \
    "v_add_u32 v200, 131072, v200\n\t" \
    "s_add_u32 m0, s82, 40960\n\t" \
    "s_nop 0\n\t" \
    "global_load_lds_dwordx4 v200, s[42:43]\n\t" \
    "v_add_u32 v200, 131072, v200\n\t" \
    "s_add_u32 m0, s82, 45056\n\t" \
    "s_nop 0\n\t" \
    "global_load_lds_dwordx4 v200, s[42:43]\n\t" \
    "v_add_u32 v200, 131072, v200\n\t" \
    "s_add_u32 s50, s50, 36864\n\t" \
    "s_addc_u32 s51, s51, 0\n\t" \
    "s_add_u32 m0, s89, 36864\n\t" \
    "s_nop 0\n\t" \
    "global_load_lds_dwordx4 v1, s[50:51] offset:0\n\t" \
    "s_add_u32 m0, s89, 37824\n\t" \
    "s_nop 0\n\t" \
    "global_load_lds_dwordx4 v1, s[50:51] offset:64\n\t" \
    "s_add_u32 m0, s89, 38784\n\t" \
    "s_nop 0\n\t" \
    "global_load_lds_dwordx4 v1, s[50:51] offset:128\n\t" \
    "s_add_u32 m0, s89, 39744\n\t" \
    "s_nop 0\n\t" \
    "global_load_lds_dwordx4 v1, s[50:51] offset:192\n\t" \
    "s_add_u32 m0, s89, 40704\n\t" \
    "s_nop 0\n\t" \
    "global_load_lds_dwordx4 v1, s[50:51] offset:256\n\t" \
    "s_add_u32 m0, s89, 41664\n\t" \
    "s_nop 0\n\t" \
    "global_load_lds_dwordx4 v1, s[50:51] offset:320\n\t" \
    "s_add_u32 m0, s89, 42624\n\t" \
    "s_nop 0\n\t" \
    "global_load_lds_dwordx4 v1, s[50:51] offset:384\n\t" \
    "s_add_u32 m0, s89, 43584\n\t" \
    "s_nop 0\n\t" \
    "global_load_lds_dwordx4 v1, s[50:51] offset:448\n\t" \
    "s_add_u32 m0, s89, 44544\n\t" \
    "s_nop 0\n\t" \
    "global_load_lds_dwordx4 v1, s[50:51] offset:512\n\t" \
    "ds_read_b64_tr_b16 a[168:169], v202 offset:49152\n\t" \
    "ds_read_b64_tr_b16 a[170:171], v202 offset:50176\n\t" \
    "ds_read_b64_tr_b16 a[172:173], v202 offset:57344\n\t" \
    "ds_read_b64_tr_b16 a[174:175], v202 offset:58368\n\t" \
    "ds_read_b64_tr_b16 a[240:241], v203 offset:49152\n\t" \
    "ds_read_b64_tr_b16 a[242:243], v203 offset:50176\n\t" \
    "ds_read_b64_tr_b16 a[244:245], v203 offset:57344\n\t" \
    "ds_read_b64_tr_b16 a[246:247], v203 offset:58368\n\t" \
    "s_waitcnt vmcnt(30) lgkmcnt(0)\n\t" \
    "s_barrier\n\t" \
    "s_add_u32 m0, s82, 49152\n\t" \
    "s_nop 0\n\t" \
    "global_load_lds_dwordx4 v200, s[42:43]\n\t" \
    "v_add_u32 v200, 131072, v200\n\t" \
    "s_add_u32 m0, s82, 53248\n\t" \
    "s_nop 0\n\t" \
    "global_load_lds_dwordx4 v200, s[42:43]\n\t" \
    "v_add_u32 v200, 131072, v200\n\t" \
    "s_add_u32 m0, s82, 57344\n\t" \
    "s_nop 0\n\t" \
    "global_load_lds_dwordx4 v200, s[42:43]\n\t" \
    "v_add_u32 v200, 131072, v200\n\t" \
    "s_add_u32 m0, s82, 61440\n\t" \
    "s_nop 0\n\t" \
    "global_load_lds_dwordx4 v200, s[42:43]\n\t" \
    "v_add_u32 v200, 131072, v200\n\t" \
    "v_add_u32 v202, 65536, v202\n\t" \
    "v_add_u32 v203, 65536, v203\n\t" \
    "ds_read_b64_tr_b16 a[176:177], v202 offset:0\n\t" \
    "ds_read_b64_tr_b16 a[178:179], v202 offset:1024\n\t" \
    "ds_read_b64_tr_b16 a[180:181], v202 offset:8192\n\t" \
    "ds_read_b64_tr_b16 a[182:183], v202 offset:9216\n\t" \
    "ds_read_b64_tr_b16 a[248:249], v203 offset:0\n\t" \
    "ds_read_b64_tr_b16 a[250:251], v203 offset:1024\n\t" \
    "ds_read_b64_tr_b16 a[252:253], v203 offset:8192\n\t" \
    "ds_read_b64_tr_b16 a[254:255], v203 offset:9216\n\t" \
    "s_waitcnt vmcnt(30) lgkmcnt(0)\n\t" \
    "s_barrier\n\t" \
    "v_subrev_u32 v200, 4718592, v200\n\t" \
    "s_add_u32 m0, s82, 65536\n\t" \
    "s_nop 0\n\t" \
    "global_load_lds_dwordx4 v200, s[44:45]\n\t" \
    "v_add_u32 v200, 131072, v200\n\t" \
    "s_add_u32 m0, s82, 69632\n\t" \
    "s_nop 0\n\t" \
    "global_load_lds_dwordx4 v200, s[44:45]\n\t" \
    "v_add_u32 v200, 131072, v200\n\t" \
    "s_add_u32 m0, s82, 73728\n\t" \
    "s_nop 0\n\t" \
    "global_load_lds_dwordx4 v200, s[44:45]\n\t" \
    "v_add_u32 v200, 131072, v200\n\t" \
    "s_add_u32 m0, s82, 77824\n\t" \
    "s_nop 0\n\t" \
    "global_load_lds_dwordx4 v200, s[44:45]\n\t" \
    "v_add_u32 v200, 131072, v200\n\t" \
    "v_add_u32 v202, -65536, v202\n\t" \
    "v_add_u32 v203, -65536, v203\n\t" \
    "ds_read_b64_tr_b16 a[184:185], v202 offset:0\n\t" \
    "ds_read_b64_tr_b16 a[186:187], v202 offset:1024\n\t" \
    "ds_read_b64_tr_b16 a[188:189], v202 offset:8192\n\t" \
    "ds_read_b64_tr_b16 a[190:191], v202 offset:9216\n\t" \
    "ds_read_b64_tr_b16 v[224:225], v203 offset:0\n\t" \
    "ds_read_b64_tr_b16 v[226:227], v203 offset:1024\n\t" \
    "ds_read_b64_tr_b16 v[228:229], v203 offset:8192\n\t" \
    "ds_read_b64_tr_b16 v[230:231], v203 offset:9216\n\t" \
    "s_waitcnt vmcnt(30) lgkmcnt(0)\n\t" \
    "s_barrier\n\t" \
    "s_add_u32 m0, s82, 0\n\t" \
    "s_nop 0\n\t" \
    "global_load_lds_dwordx4 v200, s[44:45]\n\t" \
    "v_add_u32 v200, 131072, v200\n\t" \
    "s_add_u32 m0, s82, 4096\n\t" \
    "s_nop 0\n\t" \
    "global_load_lds_dwordx4 v200, s[44:45]\n\t" \
    "v_add_u32 v200, 131072, v200\n\t" \
    "s_add_u32 m0, s82, 8192\n\t" \
    "s_nop 0\n\t" \
    "global_load_lds_dwordx4 v200, s[44:45]\n\t" \
    "v_add_u32 v200, 131072, v200\n\t" \
    "s_add_u32 m0, s82, 12288\n\t" \
    "s_nop 0\n\t" \
    "global_load_lds_dwordx4 v200, s[44:45]\n\t" \
    "v_add_u32 v200, 131072, v200\n\t" \
    "ds_read_b64_tr_b16 a[192:193], v202 offset:16384\n\t" \
    "ds_read_b64_tr_b16 a[194:195], v202 offset:17408\n\t" \
    "ds_read_b64_tr_b16 a[196:197], v202 offset:24576\n\t" \
    "ds_read_b64_tr_b16 a[198:199], v202 offset:25600\n\t" \
    "ds_read_b64_tr_b16 v[232:233], v203 offset:16384\n\t" \
    "ds_read_b64_tr_b16 v[234:235], v203 offset:17408\n\t" \
    "ds_read_b64_tr_b16 v[236:237], v203 offset:24576\n\t" \
    "ds_read_b64_tr_b16 v[238:239], v203 offset:25600\n\t" \
    "s_waitcnt vmcnt(21) lgkmcnt(0)\n\t" \
    "s_barrier\n\t" \
    "s_add_u32 m0, s82, 16384\n\t" \
    "s_nop 0\n\t" \
    "global_load_lds_dwordx4 v200, s[44:45]\n\t" \
    "v_add_u32 v200, 131072, v200\n\t" \
    "s_add_u32 m0, s82, 20480\n\t" \
    "s_nop 0\n\t" \
    "global_load_lds_dwordx4 v200, s[44:45]\n\t" \
    "v_add_u32 v200, 131072, v200\n\t" \
    "s_add_u32 m0, s82, 24576\n\t" \
    "s_nop 0\n\t" \
    "global_load_lds_dwordx4 v200, s[44:45]\n\t" \
    "v_add_u32 v200, 131072, v200\n\t" \
    "s_add_u32 m0, s82, 28672\n\t" \
    "s_nop 0\n\t" \
    "global_load_lds_dwordx4 v200, s[44:45]\n\t" \
    "v_add_u32 v200, 131072, v200\n\t" \
    "v_mbcnt_lo_u32_b32 v164, -1, 0\n\t" \
    "v_mbcnt_hi_u32_b32 v164, -1, v164\n\t" \
    "s_cmp_eq_u32 s66, 0\n\t" \
    "s_cselect_b32 s69, 59, 11\n\t" \
    "v_min_u32 v164, s69, v164\n\t" \
    "v_lshlrev_b32 v164, 6, v164\n\t" \
    "v_add_u32 v164, 6225920, v164\n\t" \
    "v_mov_b32 v165, 6234112\n\t" \
    "global_load_dword v165, v165, s[36:37] sc1\n\t" \
    "global_load_dwordx2 v[166:167], v164, s[36:37] sc1\n\t" \
    "ds_read_b64_tr_b16 a[200:201], v202 offset:32768\n\t" \
    "ds_read_b64_tr_b16 a[202:203], v202 offset:33792\n\t" \
    "ds_read_b64_tr_b16 a[204:205], v202 offset:40960\n\t" \
    "ds_read_b64_tr_b16 a[206:207], v202 offset:41984\n\t" \
    "ds_read_b64_tr_b16 v[240:241], v203 offset:32768\n\t" \
    "ds_read_b64_tr_b16 v[242:243], v203 offset:33792\n\t" \
    "ds_read_b64_tr_b16 v[244:245], v203 offset:40960\n\t" \
    "ds_read_b64_tr_b16 v[246:247], v203 offset:41984\n\t" \
    "s_waitcnt vmcnt(14) lgkmcnt(0)\n\t" \
    "s_barrier\n\t" \
    "s_add_u32 m0, s82, 32768\n\t" \
    "s_nop 0\n\t" \
    "global_load_lds_dwordx4 v200, s[44:45]\n\t" \
    "v_add_u32 v200, 131072, v200\n\t" \
    "s_add_u32 m0, s82, 36864\n\t" \
    "s_nop 0\n\t" \
    "global_load_lds_dwordx4 v200, s[44:45]\n\t" \
    "v_add_u32 v200, 131072, v200\n\t" \
    "s_add_u32 m0, s82, 40960\n\t" \
    "s_nop 0\n\t" \
    "global_load_lds_dwordx4 v200, s[44:45]\n\t" \
    "v_add_u32 v200, 131072, v200\n\t" \
    "s_add_u32 m0, s82, 45056\n\t" \
    "s_nop 0\n\t" \
    "global_load_lds_dwordx4 v200, s[44:45]\n\t" \
    "v_add_u32 v200, 131072, v200\n\t" \
    "ds_read_b64_tr_b16 a[208:209], v202 offset:49152\n\t" \
    "ds_read_b64_tr_b16 a[210:211], v202 offset:50176\n\t" \
    "ds_read_b64_tr_b16 a[212:213], v202 offset:57344\n\t" \
    "ds_read_b64_tr_b16 a[214:215], v202 offset:58368\n\t" \
    "ds_read_b64_tr_b16 v[248:249], v203 offset:49152\n\t" \
    "ds_read_b64_tr_b16 v[250:251], v203 offset:50176\n\t" \
    "ds_read_b64_tr_b16 v[252:253], v203 offset:57344\n\t" \
    "ds_read_b64_tr_b16 v[254:255], v203 offset:58368\n\t" \
    "s_waitcnt lgkmcnt(0)\n\t" \
    "s_waitcnt lgkmcnt(6)\n\t" \
    "v_add_u32 v2, 36864, v3\n\t" \
    "ds_read_b128 v[24:27], v2 offset:0\n\t" \
    "ds_read_b128 v[28:31], v2 offset:18432\n\t" \
    "ds_read_b128 v[32:35], v2 offset:1024\n\t" \
    "ds_read_b128 v[36:39], v2 offset:19456\n\t" \
    "s_waitcnt lgkmcnt(3)\n\t" \
    "v_mfma_f32_16x16x32_bf16 v[88:91], a[144:147], v[24:27], 0\n\t" \
    "ds_read_b128 v[40:43], v2 offset:2048\n\t" \
    "s_waitcnt lgkmcnt(3)\n\t" \
    "v_mfma_f32_16x16x32_bf16 v[92:95], a[144:147], v[28:31], 0\n\t" \
    "ds_read_b128 v[44:47], v2 offset:20480\n\t" \
    "v_mfma_f32_16x16x32_bf16 v[96:99], a[216:219], v[24:27], 0\n\t" \
    "v_mfma_f32_16x16x32_bf16 v[100:103], a[216:219], v[28:31], 0\n\t" \
    "s_waitcnt lgkmcnt(3)\n\t" \
    "v_mfma_f32_16x16x32_bf16 v[88:91], a[148:151], v[32:35], v[88:91]\n\t" \
    "ds_read_b128 v[48:51], v2 offset:3072\n\t" \
    "s_waitcnt lgkmcnt(3)\n\t" \
    "v_mfma_f32_16x16x32_bf16 v[92:95], a[148:151], v[36:39], v[92:95]\n\t" \
    "ds_read_b128 v[52:55], v2 offset:21504\n\t" \
    "v_mfma_f32_16x16x32_bf16 v[96:99], a[220:223], v[32:35], v[96:99]\n\t" \
    "v_mfma_f32_16x16x32_bf16 v[100:103], a[220:223], v[36:39], v[100:103]\n\t" \
    "s_waitcnt lgkmcnt(3)\n\t" \
    "v_mfma_f32_16x16x32_bf16 v[88:91], a[152:155], v[40:43], v[88:91]\n\t" \
    "ds_read_b128 v[56:59], v2 offset:4096\n\t" \
    "s_waitcnt lgkmcnt(3)\n\t" \
    "v_mfma_f32_16x16x32_bf16 v[92:95], a[152:155], v[44:47], v[92:95]\n\t" \
    "ds_read_b128 v[60:63], v2 offset:22528\n\t" \
    "v_mfma_f32_16x16x32_bf16 v[96:99], a[224:227], v[40:43], v[96:99]\n\t" \
    "v_mfma_f32_16x16x32_bf16 v[100:103], a[224:227], v[44:47], v[100:103]\n\t" \
    "s_waitcnt lgkmcnt(3)\n\t" \
    "v_mfma_f32_16x16x32_bf16 v[88:91], a[156:159], v[48:51], v[88:91]\n\t" \
    "ds_read_b128 v[64:67], v2 offset:5120\n\t" \
    "s_waitcnt lgkmcnt(3)\n\t" \
    "v_mfma_f32_16x16x32_bf16 v[92:95], a[156:159], v[52:55], v[92:95]\n\t" \
    "ds_read_b128 v[68:71], v2 offset:23552\n\t" \
    "v_mfma_f32_16x16x32_bf16 v[96:99], a[228:231], v[48:51], v[96:99]\n\t" \
    "v_mfma_f32_16x16x32_bf16 v[100:103], a[228:231], v[52:55], v[100:103]\n\t" \
    "s_waitcnt lgkmcnt(3)\n\t" \
    "v_mfma_f32_16x16x32_bf16 v[88:91], a[160:163], v[56:59], v[88:91]\n\t" \
    "ds_read_b128 v[24:27], v2 offset:6144\n\t" \
    "s_waitcnt lgkmcnt(3)\n\t" \
    "v_mfma_f32_16x16x32_bf16 v[92:95], a[160:163], v[60:63], v[92:95]\n\t" \
    "ds_read_b128 v[28:31], v2 offset:24576\n\t" \
    "v_mfma_f32_16x16x32_bf16 v[96:99], a[232:235], v[56:59], v[96:99]\n\t" \
    "v_mfma_f32_16x16x32_bf16 v[100:103], a[232:235], v[60:63], v[100:103]\n\t" \
    "s_waitcnt lgkmcnt(3)\n\t" \
    "v_mfma_f32_16x16x32_bf16 v[88:91], a[164:167], v[64:67], v[88:91]\n\t" \
    "ds_read_b128 v[32:35], v2 offset:7168\n\t" \
    "s_waitcnt lgkmcnt(3)\n\t" \
    "v_mfma_f32_16x16x32_bf16 v[92:95], a[164:167], v[68:71], v[92:95]\n\t" \
    "ds_read_b128 v[36:39], v2 offset:25600\n\t" \
    "v_mfma_f32_16x16x32_bf16 v[96:99], a[236:239], v[64:67], v[96:99]\n\t" \
    "v_mfma_f32_16x16x32_bf16 v[100:103], a[236:239], v[68:71], v[100:103]\n\t" \
    "s_waitcnt lgkmcnt(3)\n\t" \
    "v_mfma_f32_16x16x32_bf16 v[88:91], a[168:171], v[24:27], v[88:91]\n\t" \
    "ds_read_b128 v[40:43], v2 offset:8192\n\t" \
    "s_waitcnt lgkmcnt(3)\n\t" \
    "v_mfma_f32_16x16x32_bf16 v[92:95], a[168:171], v[28:31], v[92:95]\n\t" \
    "ds_read_b128 v[44:47], v2 offset:26624\n\t" \
    "v_mfma_f32_16x16x32_bf16 v[96:99], a[240:243], v[24:27], v[96:99]\n\t" \
    "v_mfma_f32_16x16x32_bf16 v[100:103], a[240:243], v[28:31], v[100:103]\n\t" \
    "s_waitcnt lgkmcnt(3)\n\t" \
    "v_mfma_f32_16x16x32_bf16 v[88:91], a[172:175], v[32:35], v[88:91]\n\t" \
    "ds_read_b128 v[48:51], v2 offset:9216\n\t" \
    "s_waitcnt lgkmcnt(3)\n\t" \
    "v_mfma_f32_16x16x32_bf16 v[92:95], a[172:175], v[36:39], v[92:95]\n\t" \
    "ds_read_b128 v[52:55], v2 offset:27648\n\t" \
    "v_mfma_f32_16x16x32_bf16 v[96:99], a[244:247], v[32:35], v[96:99]\n\t" \
    "v_mfma_f32_16x16x32_bf16 v[100:103], a[244:247], v[36:39], v[100:103]\n\t" \
    "s_waitcnt lgkmcnt(3)\n\t" \
    "v_mfma_f32_16x16x32_bf16 v[88:91], a[176:179], v[40:43], v[88:91]\n\t" \
    "ds_read_b128 v[56:59], v2 offset:10240\n\t" \
    "s_waitcnt lgkmcnt(3)\n\t" \
    "v_mfma_f32_16x16x32_bf16 v[92:95], a[176:179], v[44:47], v[92:95]\n\t" \
    "ds_read_b128 v[60:63], v2 offset:28672\n\t" \
    "v_mfma_f32_16x16x32_bf16 v[96:99], a[248:251], v[40:43], v[96:99]\n\t" \
    "v_mfma_f32_16x16x32_bf16 v[100:103], a[248:251], v[44:47], v[100:103]\n\t" \
    "s_waitcnt lgkmcnt(3)\n\t" \
    "v_mfma_f32_16x16x32_bf16 v[88:91], a[180:183], v[48:51], v[88:91]\n\t" \
    "ds_read_b128 v[64:67], v2 offset:11264\n\t" \
    "s_waitcnt lgkmcnt(3)\n\t" \
    "v_mfma_f32_16x16x32_bf16 v[92:95], a[180:183], v[52:55], v[92:95]\n\t" \
    "ds_read_b128 v[68:71], v2 offset:29696\n\t" \
    "v_mfma_f32_16x16x32_bf16 v[96:99], a[252:255], v[48:51], v[96:99]\n\t" \
    "v_mfma_f32_16x16x32_bf16 v[100:103], a[252:255], v[52:55], v[100:103]\n\t" \
    "s_waitcnt lgkmcnt(3)\n\t" \
    "v_mfma_f32_16x16x32_bf16 v[88:91], a[184:187], v[56:59], v[88:91]\n\t" \
    "ds_read_b128 v[24:27], v2 offset:12288\n\t" \
    "s_waitcnt lgkmcnt(3)\n\t" \
    "v_mfma_f32_16x16x32_bf16 v[92:95], a[184:187], v[60:63], v[92:95]\n\t" \
    "ds_read_b128 v[28:31], v2 offset:30720\n\t" \
    "v_mfma_f32_16x16x32_bf16 v[96:99], v[224:227], v[56:59], v[96:99]\n\t" \
    "v_mfma_f32_16x16x32_bf16 v[100:103], v[224:227], v[60:63], v[100:103]\n\t" \
    "s_waitcnt lgkmcnt(3)\n\t" \
    "v_mfma_f32_16x16x32_bf16 v[88:91], a[188:191], v[64:67], v[88:91]\n\t" \
    "ds_read_b128 v[32:35], v2 offset:13312\n\t" \
    "s_waitcnt lgkmcnt(3)\n\t" \
    "v_mfma_f32_16x16x32_bf16 v[92:95], a[188:191], v[68:71], v[92:95]\n\t" \
    "ds_read_b128 v[36:39], v2 offset:31744\n\t" \
    "v_mfma_f32_16x16x32_bf16 v[96:99], v[228:231], v[64:67], v[96:99]\n\t" \
    "v_mfma_f32_16x16x32_bf16 v[100:103], v[228:231], v[68:71], v[100:103]\n\t" \
    "s_waitcnt lgkmcnt(3)\n\t" \
    "v_mfma_f32_16x16x32_bf16 v[88:91], a[192:195], v[24:27], v[88:91]\n\t" \
    "ds_read_b128 v[40:43], v2 offset:14336\n\t" \
    "s_waitcnt lgkmcnt(3)\n\t" \
    "v_mfma_f32_16x16x32_bf16 v[92:95], a[192:195], v[28:31], v[92:95]\n\t" \
    "ds_read_b128 v[44:47], v2 offset:32768\n\t" \
    "v_mfma_f32_16x16x32_bf16 v[96:99], v[232:235], v[24:27], v[96:99]\n\t" \
    "v_mfma_f32_16x16x32_bf16 v[100:103], v[232:235], v[28:31], v[100:103]\n\t" \
    "s_waitcnt lgkmcnt(3)\n\t" \
    "v_mfma_f32_16x16x32_bf16 v[88:91], a[196:199], v[32:35], v[88:91]\n\t" \
    "ds_read_b128 v[48:51], v2 offset:15360\n\t" \
    "s_waitcnt lgkmcnt(3)\n\t" \
    "v_mfma_f32_16x16x32_bf16 v[92:95], a[196:199], v[36:39], v[92:95]\n\t" \
    "ds_read_b128 v[52:55], v2 offset:33792\n\t" \
    "v_mfma_f32_16x16x32_bf16 v[96:99], v[236:239], v[32:35], v[96:99]\n\t" \
    "v_mfma_f32_16x16x32_bf16 v[100:103], v[236:239], v[36:39], v[100:103]\n\t" \
    "s_waitcnt lgkmcnt(3)\n\t" \
    "v_mfma_f32_16x16x32_bf16 v[88:91], a[200:203], v[40:43], v[88:91]\n\t" \
    "ds_read_b128 v[56:59], v2 offset:16384\n\t" \
    "s_waitcnt lgkmcnt(3)\n\t" \
    "v_mfma_f32_16x16x32_bf16 v[92:95], a[200:203], v[44:47], v[92:95]\n\t" \
    "ds_read_b128 v[60:63], v2 offset:34816\n\t" \
    "v_mfma_f32_16x16x32_bf16 v[96:99], v[240:243], v[40:43], v[96:99]\n\t" \
    "v_mfma_f32_16x16x32_bf16 v[100:103], v[240:243], v[44:47], v[100:103]\n\t" \
    "s_waitcnt lgkmcnt(3)\n\t" \
    "v_mfma_f32_16x16x32_bf16 v[88:91], a[204:207], v[48:51], v[88:91]\n\t" \
    "ds_read_b128 v[64:67], v2 offset:17408\n\t" \
    "s_waitcnt lgkmcnt(3)\n\t" \
    "v_mfma_f32_16x16x32_bf16 v[92:95], a[204:207], v[52:55], v[92:95]\n\t" \
    "ds_read_b128 v[68:71], v2 offset:35840\n\t" \
    "v_mfma_f32_16x16x32_bf16 v[96:99], v[244:247], v[48:51], v[96:99]\n\t" \
    "v_mfma_f32_16x16x32_bf16 v[100:103], v[244:247], v[52:55], v[100:103]\n\t" \
    "s_waitcnt lgkmcnt(3)\n\t" \
    "v_mfma_f32_16x16x32_bf16 v[88:91], a[208:211], v[56:59], v[88:91]\n\t" \
    "s_waitcnt lgkmcnt(2)\n\t" \
    "v_mfma_f32_16x16x32_bf16 v[92:95], a[208:211], v[60:63], v[92:95]\n\t" \
    "v_mfma_f32_16x16x32_bf16 v[96:99], v[248:251], v[56:59], v[96:99]\n\t" \
    "v_mfma_f32_16x16x32_bf16 v[100:103], v[248:251], v[60:63], v[100:103]\n\t" \
    "s_waitcnt lgkmcnt(1)\n\t" \
    "v_mfma_f32_16x16x32_bf16 v[88:91], a[212:215], v[64:67], v[88:91]\n\t" \
    "s_waitcnt lgkmcnt(0)\n\t" \
    "v_mfma_f32_16x16x32_bf16 v[92:95], a[212:215], v[68:71], v[92:95]\n\t" \
    "v_mfma_f32_16x16x32_bf16 v[96:99], v[252:255], v[64:67], v[96:99]\n\t" \
    "v_mfma_f32_16x16x32_bf16 v[100:103], v[252:255], v[68:71], v[100:103]\n\t" \
    "s_mul_i32 s70, s67, 7\n\t" \
    "s_add_u32 s69, s65, 1\n\t" \
    "s_xor_b32 s69, s69, s70\n\t" \
    "s_and_b32 s69, s69, 63\n\t" \
    "s_lshl_b32 s69, s69, 13\n\t" \
    "s_mul_i32 s70, s67, 524288\n\t" \
    "s_add_u32 s69, s69, s70\n\t" \
    "s_add_u32 s69, s69, 6291456\n\t" \
    "s_add_u32 s38, s36, s69\n\t" \
    "s_addc_u32 s39, s37, 0\n\t" \
    "s_add_u32 s40, s38, 16777216\n\t" \
    "s_addc_u32 s41, s39, 0\n\t" \
    "v_cvt_pk_bf16_f32 v112, v88, v89\n\t" \
    "v_cvt_pk_bf16_f32 v113, v90, v91\n\t" \
    "v_cvt_pk_bf16_f32 v114, v96, v97\n\t" \
    "v_cvt_pk_bf16_f32 v115, v98, v99\n\t" \
    "global_store_dwordx4 v8, v[112:115], s[40:41] offset:0 sc1\n\t" \
    "v_cvt_pk_bf16_f32 v116, v92, v93\n\t" \
    "v_cvt_pk_bf16_f32 v117, v94, v95\n\t" \
    "v_cvt_pk_bf16_f32 v118, v100, v101\n\t" \
    "v_cvt_pk_bf16_f32 v119, v102, v103\n\t" \
    "global_store_dwordx4 v8, v[116:119], s[40:41] offset:1024 sc1\n\t" \
    "s_waitcnt lgkmcnt(6)\n\t" \
    "v_add_u32 v2, 0, v3\n\t" \
    "ds_read_b128 v[24:27], v2 offset:0\n\t" \
    "ds_read_b128 v[28:31], v2 offset:18432\n\t" \
    "ds_read_b128 v[32:35], v2 offset:1024\n\t" \
    "ds_read_b128 v[36:39], v2 offset:19456\n\t" \
    "s_waitcnt lgkmcnt(3)\n\t" \
    "v_mfma_f32_16x16x32_bf16 v[88:91], a[144:147], v[24:27], 0\n\t" \
    "ds_read_b128 v[40:43], v2 offset:2048\n\t" \
    "s_waitcnt lgkmcnt(3)\n\t" \
    "v_mfma_f32_16x16x32_bf16 v[92:95], a[144:147], v[28:31], 0\n\t" \
    "ds_read_b128 v[44:47], v2 offset:20480\n\t" \
    "v_mfma_f32_16x16x32_bf16 v[96:99], a[216:219], v[24:27], 0\n\t" \
    "v_mfma_f32_16x16x32_bf16 v[100:103], a[216:219], v[28:31], 0\n\t" \
    "s_waitcnt lgkmcnt(3)\n\t" \
    "v_mfma_f32_16x16x32_bf16 v[88:91], a[148:151], v[32:35], v[88:91]\n\t" \
    "ds_read_b128 v[48:51], v2 offset:3072\n\t" \
    "s_waitcnt lgkmcnt(3)\n\t" \
    "v_mfma_f32_16x16x32_bf16 v[92:95], a[148:151], v[36:39], v[92:95]\n\t" \
    "ds_read_b128 v[52:55], v2 offset:21504\n\t" \
    "v_mfma_f32_16x16x32_bf16 v[96:99], a[220:223], v[32:35], v[96:99]\n\t" \
    "v_mfma_f32_16x16x32_bf16 v[100:103], a[220:223], v[36:39], v[100:103]\n\t" \
    "s_waitcnt lgkmcnt(3)\n\t" \
    "v_mfma_f32_16x16x32_bf16 v[88:91], a[152:155], v[40:43], v[88:91]\n\t" \
    "ds_read_b128 v[56:59], v2 offset:4096\n\t" \
    "s_waitcnt lgkmcnt(3)\n\t" \
    "v_mfma_f32_16x16x32_bf16 v[92:95], a[152:155], v[44:47], v[92:95]\n\t" \
    "ds_read_b128 v[60:63], v2 offset:22528\n\t" \
    "v_mfma_f32_16x16x32_bf16 v[96:99], a[224:227], v[40:43], v[96:99]\n\t" \
    "v_mfma_f32_16x16x32_bf16 v[100:103], a[224:227], v[44:47], v[100:103]\n\t" \
    "s_waitcnt lgkmcnt(3)\n\t" \
    "v_mfma_f32_16x16x32_bf16 v[88:91], a[156:159], v[48:51], v[88:91]\n\t" \
    "ds_read_b128 v[64:67], v2 offset:5120\n\t" \
    "s_waitcnt lgkmcnt(3)\n\t" \
    "v_mfma_f32_16x16x32_bf16 v[92:95], a[156:159], v[52:55], v[92:95]\n\t" \
    "ds_read_b128 v[68:71], v2 offset:23552\n\t" \
    "v_mfma_f32_16x16x32_bf16 v[96:99], a[228:231], v[48:51], v[96:99]\n\t" \
    "v_mfma_f32_16x16x32_bf16 v[100:103], a[228:231], v[52:55], v[100:103]\n\t" \
    "s_waitcnt lgkmcnt(3)\n\t" \
    "v_mfma_f32_16x16x32_bf16 v[88:91], a[160:163], v[56:59], v[88:91]\n\t" \
    "ds_read_b128 v[24:27], v2 offset:6144\n\t" \
    "s_waitcnt lgkmcnt(3)\n\t" \
    "v_mfma_f32_16x16x32_bf16 v[92:95], a[160:163], v[60:63], v[92:95]\n\t" \
    "ds_read_b128 v[28:31], v2 offset:24576\n\t" \
    "v_mfma_f32_16x16x32_bf16 v[96:99], a[232:235], v[56:59], v[96:99]\n\t" \
    "v_mfma_f32_16x16x32_bf16 v[100:103], a[232:235], v[60:63], v[100:103]\n\t" \
    "s_waitcnt lgkmcnt(3)\n\t" \
    "v_mfma_f32_16x16x32_bf16 v[88:91], a[164:167], v[64:67], v[88:91]\n\t" \
    "ds_read_b128 v[32:35], v2 offset:7168\n\t" \
    "s_waitcnt lgkmcnt(3)\n\t" \
    "v_mfma_f32_16x16x32_bf16 v[92:95], a[164:167], v[68:71], v[92:95]\n\t" \
    "ds_read_b128 v[36:39], v2 offset:25600\n\t" \
    "v_mfma_f32_16x16x32_bf16 v[96:99], a[236:239], v[64:67], v[96:99]\n\t" \
    "v_mfma_f32_16x16x32_bf16 v[100:103], a[236:239], v[68:71], v[100:103]\n\t" \
    "s_waitcnt lgkmcnt(3)\n\t" \
    "v_mfma_f32_16x16x32_bf16 v[88:91], a[168:171], v[24:27], v[88:91]\n\t" \
    "ds_read_b128 v[40:43], v2 offset:8192\n\t" \
    "s_waitcnt lgkmcnt(3)\n\t" \
    "v_mfma_f32_16x16x32_bf16 v[92:95], a[168:171], v[28:31], v[92:95]\n\t" \
    "ds_read_b128 v[44:47], v2 offset:26624\n\t" \
    "v_mfma_f32_16x16x32_bf16 v[96:99], a[240:243], v[24:27], v[96:99]\n\t" \
    "v_mfma_f32_16x16x32_bf16 v[100:103], a[240:243], v[28:31], v[100:103]\n\t" \
    "s_waitcnt lgkmcnt(3)\n\t" \
    "v_mfma_f32_16x16x32_bf16 v[88:91], a[172:175], v[32:35], v[88:91]\n\t" \
    "ds_read_b128 v[48:51], v2 offset:9216\n\t" \
    "s_waitcnt lgkmcnt(3)\n\t" \
    "v_mfma_f32_16x16x32_bf16 v[92:95], a[172:175], v[36:39], v[92:95]\n\t" \
    "ds_read_b128 v[52:55], v2 offset:27648\n\t" \
    "v_mfma_f32_16x16x32_bf16 v[96:99], a[244:247], v[32:35], v[96:99]\n\t" \
    "v_mfma_f32_16x16x32_bf16 v[100:103], a[244:247], v[36:39], v[100:103]\n\t" \
    "s_waitcnt lgkmcnt(3)\n\t" \
    "v_mfma_f32_16x16x32_bf16 v[88:91], a[176:179], v[40:43], v[88:91]\n\t" \
    "ds_read_b128 v[56:59], v2 offset:10240\n\t" \
    "s_waitcnt lgkmcnt(3)\n\t" \
    "v_mfma_f32_16x16x32_bf16 v[92:95], a[176:179], v[44:47], v[92:95]\n\t" \
    "ds_read_b128 v[60:63], v2 offset:28672\n\t" \
    "v_mfma_f32_16x16x32_bf16 v[96:99], a[248:251], v[40:43], v[96:99]\n\t" \
    "v_mfma_f32_16x16x32_bf16 v[100:103], a[248:251], v[44:47], v[100:103]\n\t" \
    "s_waitcnt lgkmcnt(3)\n\t" \
    "v_mfma_f32_16x16x32_bf16 v[88:91], a[180:183], v[48:51], v[88:91]\n\t" \
    "ds_read_b128 v[64:67], v2 offset:11264\n\t" \
    "s_waitcnt lgkmcnt(3)\n\t" \
    "v_mfma_f32_16x16x32_bf16 v[92:95], a[180:183], v[52:55], v[92:95]\n\t" \
    "ds_read_b128 v[68:71], v2 offset:29696\n\t" \
    "v_mfma_f32_16x16x32_bf16 v[96:99], a[252:255], v[48:51], v[96:99]\n\t" \
    "v_mfma_f32_16x16x32_bf16 v[100:103], a[252:255], v[52:55], v[100:103]\n\t" \
    "s_waitcnt lgkmcnt(3)\n\t" \
    "v_mfma_f32_16x16x32_bf16 v[88:91], a[184:187], v[56:59], v[88:91]\n\t" \
    "ds_read_b128 v[24:27], v2 offset:12288\n\t" \
    "s_waitcnt lgkmcnt(3)\n\t" \
    "v_mfma_f32_16x16x32_bf16 v[92:95], a[184:187], v[60:63], v[92:95]\n\t" \
    "ds_read_b128 v[28:31], v2 offset:30720\n\t" \
    "v_mfma_f32_16x16x32_bf16 v[96:99], v[224:227], v[56:59], v[96:99]\n\t" \
    "v_mfma_f32_16x16x32_bf16 v[100:103], v[224:227], v[60:63], v[100:103]\n\t" \
    "s_waitcnt lgkmcnt(3)\n\t" \
    "v_mfma_f32_16x16x32_bf16 v[88:91], a[188:191], v[64:67], v[88:91]\n\t" \
    "ds_read_b128 v[32:35], v2 offset:13312\n\t" \
    "s_waitcnt lgkmcnt(3)\n\t" \
    "v_mfma_f32_16x16x32_bf16 v[92:95], a[188:191], v[68:71], v[92:95]\n\t" \
    "ds_read_b128 v[36:39], v2 offset:31744\n\t" \
    "v_mfma_f32_16x16x32_bf16 v[96:99], v[228:231], v[64:67], v[96:99]\n\t" \
    "v_mfma_f32_16x16x32_bf16 v[100:103], v[228:231], v[68:71], v[100:103]\n\t" \
    "s_waitcnt lgkmcnt(3)\n\t" \
    "v_mfma_f32_16x16x32_bf16 v[88:91], a[192:195], v[24:27], v[88:91]\n\t" \
    "ds_read_b128 v[40:43], v2 offset:14336\n\t" \
    "s_waitcnt lgkmcnt(3)\n\t" \
    "v_mfma_f32_16x16x32_bf16 v[92:95], a[192:195], v[28:31], v[92:95]\n\t" \
    "ds_read_b128 v[44:47], v2 offset:32768\n\t" \
    "v_mfma_f32_16x16x32_bf16 v[96:99], v[232:235], v[24:27], v[96:99]\n\t" \
    "v_mfma_f32_16x16x32_bf16 v[100:103], v[232:235], v[28:31], v[100:103]\n\t" \
    "s_waitcnt lgkmcnt(3)\n\t" \
    "v_mfma_f32_16x16x32_bf16 v[88:91], a[196:199], v[32:35], v[88:91]\n\t" \
    "ds_read_b128 v[48:51], v2 offset:15360\n\t" \
    "s_waitcnt lgkmcnt(3)\n\t" \
    "v_mfma_f32_16x16x32_bf16 v[92:95], a[196:199], v[36:39], v[92:95]\n\t" \
    "ds_read_b128 v[52:55], v2 offset:33792\n\t" \
    "v_mfma_f32_16x16x32_bf16 v[96:99], v[236:239], v[32:35], v[96:99]\n\t" \
    "v_mfma_f32_16x16x32_bf16 v[100:103], v[236:239], v[36:39], v[100:103]\n\t" \
    "s_waitcnt lgkmcnt(3)\n\t" \
    "v_mfma_f32_16x16x32_bf16 v[88:91], a[200:203], v[40:43], v[88:91]\n\t" \
    "ds_read_b128 v[56:59], v2 offset:16384\n\t" \
    "s_waitcnt lgkmcnt(3)\n\t" \
    "v_mfma_f32_16x16x32_bf16 v[92:95], a[200:203], v[44:47], v[92:95]\n\t" \
    "ds_read_b128 v[60:63], v2 offset:34816\n\t" \
    "v_mfma_f32_16x16x32_bf16 v[96:99], v[240:243], v[40:43], v[96:99]\n\t" \
    "v_mfma_f32_16x16x32_bf16 v[100:103], v[240:243], v[44:47], v[100:103]\n\t" \
    "s_waitcnt lgkmcnt(3)\n\t" \
    "v_mfma_f32_16x16x32_bf16 v[88:91], a[204:207], v[48:51], v[88:91]\n\t" \
    "ds_read_b128 v[64:67], v2 offset:17408\n\t" \
    "s_waitcnt lgkmcnt(3)\n\t" \
    "v_mfma_f32_16x16x32_bf16 v[92:95], a[204:207], v[52:55], v[92:95]\n\t" \
    "ds_read_b128 v[68:71], v2 offset:35840\n\t" \
    "v_mfma_f32_16x16x32_bf16 v[96:99], v[244:247], v[48:51], v[96:99]\n\t" \
    "v_mfma_f32_16x16x32_bf16 v[100:103], v[244:247], v[52:55], v[100:103]\n\t" \
    "s_waitcnt lgkmcnt(3)\n\t" \
    "v_mfma_f32_16x16x32_bf16 v[88:91], a[208:211], v[56:59], v[88:91]\n\t" \
    "s_waitcnt lgkmcnt(2)\n\t" \
    "v_mfma_f32_16x16x32_bf16 v[92:95], a[208:211], v[60:63], v[92:95]\n\t" \
    "v_mfma_f32_16x16x32_bf16 v[96:99], v[248:251], v[56:59], v[96:99]\n\t" \
    "v_mfma_f32_16x16x32_bf16 v[100:103], v[248:251], v[60:63], v[100:103]\n\t" \
    "s_waitcnt lgkmcnt(1)\n\t" \
    "v_mfma_f32_16x16x32_bf16 v[88:91], a[212:215], v[64:67], v[88:91]\n\t" \
    "s_waitcnt lgkmcnt(0)\n\t" \
    "v_mfma_f32_16x16x32_bf16 v[92:95], a[212:215], v[68:71], v[92:95]\n\t" \
    "v_mfma_f32_16x16x32_bf16 v[96:99], v[252:255], v[64:67], v[96:99]\n\t" \
    "v_mfma_f32_16x16x32_bf16 v[100:103], v[252:255], v[68:71], v[100:103]\n\t" \
    "s_mul_i32 s70, s67, 7\n\t" \
    "s_add_u32 s69, s65, 0\n\t" \
    "s_xor_b32 s69, s69, s70\n\t" \
    "s_and_b32 s69, s69, 63\n\t" \
    "s_lshl_b32 s69, s69, 13\n\t" \
    "s_mul_i32 s70, s67, 524288\n\t" \
    "s_add_u32 s69, s69, s70\n\t" \
    "s_add_u32 s69, s69, 6291456\n\t" \
    "s_add_u32 s38, s36, s69\n\t" \
    "s_addc_u32 s39, s37, 0\n\t" \
    "s_add_u32 s40, s38, 16777216\n\t" \
    "s_addc_u32 s41, s39, 0\n\t" \
    "v_cvt_pk_bf16_f32 v112, v88, v89\n\t" \
    "v_cvt_pk_bf16_f32 v113, v90, v91\n\t" \
    "v_cvt_pk_bf16_f32 v114, v96, v97\n\t" \
    "v_cvt_pk_bf16_f32 v115, v98, v99\n\t" \
    "global_store_dwordx4 v8, v[112:115], s[40:41] offset:0 sc1\n\t" \
    "v_cvt_pk_bf16_f32 v116, v92, v93\n\t" \
    "v_cvt_pk_bf16_f32 v117, v94, v95\n\t" \
    "v_cvt_pk_bf16_f32 v118, v100, v101\n\t" \
    "v_cvt_pk_bf16_f32 v119, v102, v103\n\t" \
    "global_store_dwordx4 v8, v[116:119], s[40:41] offset:1024 sc1\n\t" \
    "s_waitcnt vmcnt(18) lgkmcnt(0)\n\t" \
    "s_barrier\n\t" \
    "s_add_u32 m0, s82, 49152\n\t" \
    "s_nop 0\n\t" \
    "global_load_lds_dwordx4 v200, s[44:45]\n\t" \
    "v_add_u32 v200, 131072, v200\n\t" \
    "s_add_u32 m0, s82, 53248\n\t" \
    "s_nop 0\n\t" \
    "global_load_lds_dwordx4 v200, s[44:45]\n\t" \
    "v_add_u32 v200, 131072, v200\n\t" \
    "s_add_u32 m0, s82, 57344\n\t" \
    "s_nop 0\n\t" \
    "global_load_lds_dwordx4 v200, s[44:45]\n\t" \
    "v_add_u32 v200, 131072, v200\n\t" \
    "s_add_u32 m0, s82, 61440\n\t" \
    "s_nop 0\n\t" \
    "global_load_lds_dwordx4 v200, s[44:45]\n\t" \
    "v_add_u32 v200, 131072, v200\n\t" \
    "v_add_u32 v202, 65536, v202\n\t" \
    "v_add_u32 v203, 65536, v203\n\t" \
    "ds_read_b64_tr_b16 a[0:1], v202 offset:0\n\t" \
    "ds_read_b64_tr_b16 a[2:3], v202 offset:1024\n\t" \
    "ds_read_b64_tr_b16 a[4:5], v202 offset:8192\n\t" \
    "ds_read_b64_tr_b16 a[6:7], v202 offset:9216\n\t" \
    "ds_read_b64_tr_b16 a[72:73], v203 offset:0\n\t" \
    "ds_read_b64_tr_b16 a[74:75], v203 offset:1024\n\t" \
    "ds_read_b64_tr_b16 a[76:77], v203 offset:8192\n\t" \
    "ds_read_b64_tr_b16 a[78:79], v203 offset:9216\n\t" \
    "s_waitcnt vmcnt(18) lgkmcnt(0)\n\t" \
    "s_barrier\n\t" \
    "s_add_u32 m0, s82, 65536\n\t" \
    "s_nop 0\n\t" \
    "global_load_lds_dwordx4 v200, s[44:45]\n\t" \
    "v_add_u32 v200, 131072, v200\n\t" \
    "s_add_u32 m0, s82, 69632\n\t" \
    "s_nop 0\n\t" \
    "global_load_lds_dwordx4 v200, s[44:45]\n\t" \
    "v_add_u32 v200, 131072, v200\n\t" \
    "s_add_u32 m0, s82, 73728\n\t" \
    "s_nop 0\n\t" \
    "global_load_lds_dwordx4 v200, s[44:45]\n\t" \
    "v_add_u32 v200, 131072, v200\n\t" \
    "s_add_u32 m0, s82, 77824\n\t" \
    "s_nop 0\n\t" \
    "global_load_lds_dwordx4 v200, s[44:45]\n\t" \
    "v_add_u32 v200, 131072, v200\n\t" \
    "v_add_u32 v202, -65536, v202\n\t" \
    "v_add_u32 v203, -65536, v203\n\t" \
    "ds_read_b64_tr_b16 a[8:9], v202 offset:0\n\t" \
    "ds_read_b64_tr_b16 a[10:11], v202 offset:1024\n\t" \
    "ds_read_b64_tr_b16 a[12:13], v202 offset:8192\n\t" \
    "ds_read_b64_tr_b16 a[14:15], v202 offset:9216\n\t" \
    "ds_read_b64_tr_b16 a[80:81], v203 offset:0\n\t" \
    "ds_read_b64_tr_b16 a[82:83], v203 offset:1024\n\t" \
    "ds_read_b64_tr_b16 a[84:85], v203 offset:8192\n\t" \
    "ds_read_b64_tr_b16 a[86:87], v203 offset:9216\n\t" \
    "s_waitcnt vmcnt(18) lgkmcnt(0)\n\t" \
    "s_barrier\n\t" \
    "s_add_u32 m0, s82, 0\n\t" \
    "s_nop 0\n\t" \
    "global_load_lds_dwordx4 v200, s[44:45]\n\t" \
    "v_add_u32 v200, 131072, v200\n\t" \
    "s_add_u32 m0, s82, 4096\n\t" \
    "s_nop 0\n\t" \
    "global_load_lds_dwordx4 v200, s[44:45]\n\t" \
    "v_add_u32 v200, 131072, v200\n\t" \
    "s_add_u32 m0, s82, 8192\n\t" \
    "s_nop 0\n\t" \
    "global_load_lds_dwordx4 v200, s[44:45]\n\t" \
    "v_add_u32 v200, 131072, v200\n\t" \
    "s_add_u32 m0, s82, 12288\n\t" \
    "s_nop 0\n\t" \
    "global_load_lds_dwordx4 v200, s[44:45]\n\t" \
    "v_add_u32 v200, 131072, v200\n\t" \
    "ds_read_b64_tr_b16 a[16:17], v202 offset:16384\n\t" \
    "ds_read_b64_tr_b16 a[18:19], v202 offset:17408\n\t" \
    "ds_read_b64_tr_b16 a[20:21], v202 offset:24576\n\t" \
    "ds_read_b64_tr_b16 a[22:23], v202 offset:25600\n\t" \
    "ds_read_b64_tr_b16 a[88:89], v203 offset:16384\n\t" \
    "ds_read_b64_tr_b16 a[90:91], v203 offset:17408\n\t" \
    "ds_read_b64_tr_b16 a[92:93], v203 offset:24576\n\t" \
    "ds_read_b64_tr_b16 a[94:95], v203 offset:25600\n\t" \
    "s_waitcnt vmcnt(16) lgkmcnt(0)\n\t" \
    "s_barrier\n\t" \
    "s_add_u32 m0, s82, 16384\n\t" \
    "s_nop 0\n\t" \
    "global_load_lds_dwordx4 v200, s[44:45]\n\t" \
    "v_add_u32 v200, 131072, v200\n\t" \
    "s_add_u32 m0, s82, 20480\n\t" \
    "s_nop 0\n\t" \
    "global_load_lds_dwordx4 v200, s[44:45]\n\t" \
    "v_add_u32 v200, 131072, v200\n\t" \
    "s_add_u32 m0, s82, 24576\n\t" \
    "s_nop 0\n\t" \
    "global_load_lds_dwordx4 v200, s[44:45]\n\t" \
    "v_add_u32 v200, 131072, v200\n\t" \
    "s_add_u32 m0, s82, 28672\n\t" \
    "s_nop 0\n\t" \
    "global_load_lds_dwordx4 v200, s[44:45]\n\t" \
    "v_add_u32 v200, 131072, v200\n\t" \
    "ds_read_b64_tr_b16 a[24:25], v202 offset:32768\n\t" \
    "ds_read_b64_tr_b16 a[26:27], v202 offset:33792\n\t" \
    "ds_read_b64_tr_b16 a[28:29], v202 offset:40960\n\t" \
    "ds_read_b64_tr_b16 a[30:31], v202 offset:41984\n\t" \
    "ds_read_b64_tr_b16 a[96:97], v203 offset:32768\n\t" \
    "ds_read_b64_tr_b16 a[98:99], v203 offset:33792\n\t" \
    "ds_read_b64_tr_b16 a[100:101], v203 offset:40960\n\t" \
    "ds_read_b64_tr_b16 a[102:103], v203 offset:41984\n\t" \
    "s_waitcnt vmcnt(12) lgkmcnt(0)\n\t" \
    "s_barrier\n\t" \
    "s_add_u32 m0, s82, 32768\n\t" \
    "s_nop 0\n\t" \
    "global_load_lds_dwordx4 v200, s[44:45]\n\t" \
    "v_add_u32 v200, 131072, v200\n\t" \
    "s_add_u32 m0, s82, 36864\n\t" \
    "s_nop 0\n\t" \
    "global_load_lds_dwordx4 v200, s[44:45]\n\t" \
    "v_add_u32 v200, 131072, v200\n\t" \
    "s_add_u32 m0, s82, 40960\n\t" \
    "s_nop 0\n\t" \
    "global_load_lds_dwordx4 v200, s[44:45]\n\t" \
    "v_add_u32 v200, 131072, v200\n\t" \
    "s_add_u32 m0, s82, 45056\n\t" \
    "s_nop 0\n\t" \
    "global_load_lds_dwordx4 v200, s[44:45]\n\t" \
    "v_add_u32 v200, 131072, v200\n\t" \
    "v_sub_u32 v166, v166, v165\n\t" \
    "v_cmp_ne_u32 vcc, 1, v166\n\t" \
    "s_cbranch_vccz gx_Cok2_%=\n\t" \
    "gx_C1_%=:\n\t" \
    "global_load_dwordx2 v[166:167], v164, s[36:37] sc1\n\t" \
    "s_waitcnt vmcnt(0)\n\t" \
    "v_sub_u32 v166, v166, v165\n\t" \
    "v_cmp_ne_u32 vcc, 1, v166\n\t" \
    "s_cbranch_vccz gx_Cok2_%=\n\t" \
    "s_cmp_eq_u32 s68, 0\n\t" \
    "s_cbranch_scc1 gx_F3_%=\n\t" \
    "s_sub_u32 s68, s68, 1\n\t" \
    "s_sleep 4\n\t" \
    "s_branch gx_C1_%=\n\t" \
    "gx_F3_%=:\n\t" \
    "v_mov_b32 v168, 6095104\n\t" \
    "v_mov_b32 v169, 4\n\t" \
    "global_store_dword v168, v169, s[36:37] sc1\n\t" \
    "s_waitcnt vmcnt(0)\n\t" \
    "s_branch gx_Cok2_%=\n\t" \
    "gx_Cok2_%=:\n\t" \
    "s_cmp_lg_u32 s66, 0\n\t" \
    "s_cbranch_scc1 gx_Ca4_%=\n\t" \
    "v_mov_b32 v164, 6176768\n\t" \
    "s_mov_b64 exec, 1\n\t" \
    "global_atomic_add v164, v166, s[36:37] sc1\n\t" \
    "s_mov_b64 exec, -1\n\t" \
    "gx_Ca4_%=:\n\t" \
    "v_min_u32_dpp v167, v167, v167 row_shl:6 row_mask:0xf bank_mask:0xf\n\t" \
    "s_nop 0\n\t" \
    "v_readlane_b32 s69, v167, 4\n\t" \
    "v_readlane_b32 s70, v167, 5\n\t" \
    "s_nop 1\n\t" \
    "s_cmp_lt_u32 s70, s69\n\t" \
    "s_cselect_b32 s52, 8192, 0\n\t" \
    "s_xor_b32 s52, s52, 8192\n\t" \
    "s_cmp_lg_u32 s66, 0\n\t" \
    "s_cbranch_scc1 gx_SF5_%=\n\t" \
    "s_lshl_b32 s69, s67, 7\n\t" \
    "s_add_u32 s69, s69, 6160384\n\t" \
    "s_add_u32 s69, s69, s52\n\t" \
    "s_add_u32 s70, s65, 1\n\t" \
    "v_mov_b32 v156, s69\n\t" \
    "v_mov_b32 v157, s70\n\t" \
    "s_mov_b64 exec, 1\n\t" \
    "global_store_dword v156, v157, s[36:37] sc1\n\t" \
    "s_mov_b64 exec, -1\n\t" \
    "gx_SF5_%=:\n\t" \
    "ds_read_b64_tr_b16 a[32:33], v202 offset:49152\n\t" \
    "ds_read_b64_tr_b16 a[34:35], v202 offset:50176\n\t" \
    "ds_read_b64_tr_b16 a[36:37], v202 offset:57344\n\t" \
    "ds_read_b64_tr_b16 a[38:39], v202 offset:58368\n\t" \
    "ds_read_b64_tr_b16 a[104:105], v203 offset:49152\n\t" \
    "ds_read_b64_tr_b16 a[106:107], v203 offset:50176\n\t" \
    "ds_read_b64_tr_b16 a[108:109], v203 offset:57344\n\t" \
    "ds_read_b64_tr_b16 a[110:111], v203 offset:58368\n\t" \
    "s_waitcnt vmcnt(12) lgkmcnt(0)\n\t" \
    "s_barrier\n\t" \
    "v_add_u32 v202, 65536, v202\n\t" \
    "v_add_u32 v203, 65536, v203\n\t" \
    "ds_read_b64_tr_b16 a[40:41], v202 offset:0\n\t" \
    "ds_read_b64_tr_b16 a[42:43], v202 offset:1024\n\t" \
    "ds_read_b64_tr_b16 a[44:45], v202 offset:8192\n\t" \
    "ds_read_b64_tr_b16 a[46:47], v202 offset:9216\n\t" \
    "ds_read_b64_tr_b16 a[112:113], v203 offset:0\n\t" \
    "ds_read_b64_tr_b16 a[114:115], v203 offset:1024\n\t" \
    "ds_read_b64_tr_b16 a[116:117], v203 offset:8192\n\t" \
    "ds_read_b64_tr_b16 a[118:119], v203 offset:9216\n\t" \
    "s_waitcnt vmcnt(8) lgkmcnt(0)\n\t" \
    "s_barrier\n\t" \
    "v_add_u32 v202, -65536, v202\n\t" \
    "v_add_u32 v203, -65536, v203\n\t" \
    "ds_read_b64_tr_b16 a[48:49], v202 offset:0\n\t" \
    "ds_read_b64_tr_b16 a[50:51], v202 offset:1024\n\t" \
    "ds_read_b64_tr_b16 a[52:53], v202 offset:8192\n\t" \
    "ds_read_b64_tr_b16 a[54:55], v202 offset:9216\n\t" \
    "ds_read_b64_tr_b16 a[120:121], v203 offset:0\n\t" \
    "ds_read_b64_tr_b16 a[122:123], v203 offset:1024\n\t" \
    "ds_read_b64_tr_b16 a[124:125], v203 offset:8192\n\t" \
    "ds_read_b64_tr_b16 a[126:127], v203 offset:9216\n\t" \
    "s_waitcnt vmcnt(4) lgkmcnt(0)\n\t" \
    "s_barrier\n\t" \
    "ds_read_b64_tr_b16 a[56:57], v202 offset:16384\n\t" \
    "ds_read_b64_tr_b16 a[58:59], v202 offset:17408\n\t" \
    "ds_read_b64_tr_b16 a[60:61], v202 offset:24576\n\t" \
    "ds_read_b64_tr_b16 a[62:63], v202 offset:25600\n\t" \
    "ds_read_b64_tr_b16 a[128:129], v203 offset:16384\n\t" \
    "ds_read_b64_tr_b16 a[130:131], v203 offset:17408\n\t" \
    "ds_read_b64_tr_b16 a[132:133], v203 offset:24576\n\t" \
    "ds_read_b64_tr_b16 a[134:135], v203 offset:25600\n\t" \
    "s_waitcnt vmcnt(0) lgkmcnt(0)\n\t" \
    "s_barrier\n\t" \
    "ds_read_b64_tr_b16 a[64:65], v202 offset:32768\n\t" \
    "ds_read_b64_tr_b16 a[66:67], v202 offset:33792\n\t" \
    "ds_read_b64_tr_b16 a[68:69], v202 offset:40960\n\t" \
    "ds_read_b64_tr_b16 a[70:71], v202 offset:41984\n\t" \
    "ds_read_b64_tr_b16 a[136:137], v203 offset:32768\n\t" \
    "ds_read_b64_tr_b16 a[138:139], v203 offset:33792\n\t" \
    "ds_read_b64_tr_b16 a[140:141], v203 offset:40960\n\t" \
    "ds_read_b64_tr_b16 a[142:143], v203 offset:41984\n\t" \
    "s_waitcnt lgkmcnt(0)\n\t" \
    "s_barrier\n\t" \
    "ds_read_b128 v[24:27], v2 offset:0\n\t" \
    "ds_read_b128 v[28:31], v2 offset:18432\n\t" \
    "ds_read_b128 v[32:35], v2 offset:1024\n\t" \
    "ds_read_b128 v[36:39], v2 offset:19456\n\t" \
    "ds_read_b128 v[40:43], v2 offset:2048\n\t" \
    "ds_read_b128 v[44:47], v2 offset:20480\n\t" \
    "s_cmp_lt_u32 s65, 62\n\t" \
    "s_cselect_b32 s69, 36864, 0\n\t" \
    "s_add_u32 s50, s50, s69\n\t" \
    "s_addc_u32 s51, s51, 0\n\t" \
    "s_add_u32 s90, s90, 36864\n\t" \
    "s_cmp_eq_u32 s90, 147456\n\t" \
    "s_cselect_b32 s90, 0, s90\n\t" \
    "s_add_u32 s91, s90, 36864\n\t" \
    "s_cmp_eq_u32 s91, 147456\n\t" \
    "s_cselect_b32 s91, 0, s91\n\t" \
    "s_add_u32 s91, s91, s89\n\t" \
    "v_add_u32 v17, s90, v3\n\t" \
    "s_branch gx_mid8_%=\n\t" \
    ".p2align 6\n\t" \
    "s_nop 0\n\t" \
    "s_nop 0\n\t" \
    "gx_loop6_%=:\n\t" \
    "s_cmp_lt_u32 s65, 62\n\t" \
    "s_cselect_b32 s69, 36864, 0\n\t" \
    "s_add_u32 s50, s50, s69\n\t" \
    "s_addc_u32 s51, s51, 0\n\t" \
    "s_add_u32 s90, s90, 36864\n\t" \
    "s_cmp_eq_u32 s90, 147456\n\t" \
    "s_cselect_b32 s90, 0, s90\n\t" \
    "s_add_u32 s91, s90, 36864\n\t" \
    "s_cmp_eq_u32 s91, 147456\n\t" \
    "s_cselect_b32 s91, 0, s91\n\t" \
    "s_add_u32 s91, s91, s89\n\t" \
    "v_add_u32 v17, s90, v3\n\t" \
    "s_waitcnt lgkmcnt(5)\n\t" \
    "v_mfma_f32_16x16x32_bf16 v[88:91], a[144:147], v[24:27], 0\n\t" \
    "ds_read_b128 v[48:51], v2 offset:3072\n\t" \
    "s_waitcnt lgkmcnt(5)\n\t" \
    "v_mfma_f32_16x16x32_bf16 v[92:95], a[144:147], v[28:31], 0\n\t" \
    "ds_read_b128 v[52:55], v2 offset:21504\n\t" \
    "v_mfma_f32_16x16x32_bf16 v[96:99], a[216:219], v[24:27], 0\n\t" \
    "v_mfma_f32_16x16x32_bf16 v[100:103], a[216:219], v[28:31], 0\n\t" \
    "s_waitcnt lgkmcnt(5)\n\t" \
    "v_mfma_f32_16x16x32_bf16 v[88:91], a[148:151], v[32:35], v[88:91]\n\t" \
    "ds_read_b128 v[56:59], v2 offset:4096\n\t" \
    "s_waitcnt lgkmcnt(5)\n\t" \
    "v_mfma_f32_16x16x32_bf16 v[92:95], a[148:151], v[36:39], v[92:95]\n\t" \
    "ds_read_b128 v[60:63], v2 offset:22528\n\t" \
    "v_mfma_f32_16x16x32_bf16 v[96:99], a[220:223], v[32:35], v[96:99]\n\t" \
    "v_mfma_f32_16x16x32_bf16 v[100:103], a[220:223], v[36:39], v[100:103]\n\t" \
    "s_waitcnt lgkmcnt(5)\n\t" \
    "v_mfma_f32_16x16x32_bf16 v[88:91], a[152:155], v[40:43], v[88:91]\n\t" \
    "ds_read_b128 v[64:67], v2 offset:5120\n\t" \
    "s_waitcnt lgkmcnt(5)\n\t" \
    "v_mfma_f32_16x16x32_bf16 v[92:95], a[152:155], v[44:47], v[92:95]\n\t" \
    "ds_read_b128 v[68:71], v2 offset:23552\n\t" \
    "v_mfma_f32_16x16x32_bf16 v[96:99], a[224:227], v[40:43], v[96:99]\n\t" \
    "v_mfma_f32_16x16x32_bf16 v[100:103], a[224:227], v[44:47], v[100:103]\n\t" \
    "s_waitcnt vmcnt(0)\n\t" \
    "s_barrier\n\t" \
    "s_cmp_lg_u32 s66, 0\n\t" \
    "s_cbranch_scc1 gx_SF9_%=\n\t" \
    "s_lshl_b32 s69, s67, 7\n\t" \
    "s_add_u32 s69, s69, 6098944\n\t" \
    "s_add_u32 s69, s69, s52\n\t" \
    "s_add_u32 s70, s65, -1\n\t" \
    "v_mov_b32 v156, s69\n\t" \
    "v_mov_b32 v157, s70\n\t" \
    "s_add_u32 s69, s69, 61440\n\t" \
    "s_max_i32 s70, s70, 1\n\t" \
    "v_writelane_b32 v156, s69, 1\n\t" \
    "v_writelane_b32 v157, s70, 1\n\t" \
    "s_mov_b64 exec, 3\n\t" \
    "global_store_dword v156, v157, s[36:37] sc1\n\t" \
    "s_mov_b64 exec, -1\n\t" \
    "gx_SF9_%=:\n\t" \
    "s_waitcnt lgkmcnt(5)\n\t" \
    "v_mfma_f32_16x16x32_bf16 v[88:91], a[156:159], v[48:51], v[88:91]\n\t" \
    "ds_read_b128 v[24:27], v2 offset:6144\n\t" \
    "s_waitcnt lgkmcnt(5)\n\t" \
    "v_mfma_f32_16x16x32_bf16 v[92:95], a[156:159], v[52:55], v[92:95]\n\t" \
    "ds_read_b128 v[28:31], v2 offset:24576\n\t" \
    "v_mfma_f32_16x16x32_bf16 v[96:99], a[228:231], v[48:51], v[96:99]\n\t" \
    "v_mfma_f32_16x16x32_bf16 v[100:103], a[228:231], v[52:55], v[100:103]\n\t" \
    "s_waitcnt lgkmcnt(5)\n\t" \
    "v_mfma_f32_16x16x32_bf16 v[88:91], a[160:163], v[56:59], v[88:91]\n\t" \
    "ds_read_b128 v[32:35], v2 offset:7168\n\t" \
    "s_waitcnt lgkmcnt(5)\n\t" \
    "v_mfma_f32_16x16x32_bf16 v[92:95], a[160:163], v[60:63], v[92:95]\n\t" \
    "ds_read_b128 v[36:39], v2 offset:25600\n\t" \
    "v_mfma_f32_16x16x32_bf16 v[96:99], a[232:235], v[56:59], v[96:99]\n\t" \
    "v_cvt_pk_bf16_f32 v104, v72, v73\n\t" \
    "v_mfma_f32_16x16x32_bf16 v[100:103], a[232:235], v[60:63], v[100:103]\n\t" \
    "v_cvt_pk_bf16_f32 v105, v74, v75\n\t" \
    "s_waitcnt lgkmcnt(5)\n\t" \
    "v_mfma_f32_16x16x32_bf16 v[88:91], a[164:167], v[64:67], v[88:91]\n\t" \
    "ds_read_b128 v[40:43], v2 offset:8192\n\t" \
    "s_waitcnt lgkmcnt(5)\n\t" \
    "v_mfma_f32_16x16x32_bf16 v[92:95], a[164:167], v[68:71], v[92:95]\n\t" \
    "ds_read_b128 v[44:47], v2 offset:26624\n\t" \
    "v_mfma_f32_16x16x32_bf16 v[96:99], a[236:239], v[64:67], v[96:99]\n\t" \
    "v_cvt_pk_bf16_f32 v106, v80, v81\n\t" \
    "v_mfma_f32_16x16x32_bf16 v[100:103], a[236:239], v[68:71], v[100:103]\n\t" \
    "v_cvt_pk_bf16_f32 v107, v82, v83\n\t" \
    "global_store_dwordx4 v8, v[104:107], s[38:39] offset:0 sc1\n\t" \
    "s_waitcnt lgkmcnt(5)\n\t" \
    "v_mfma_f32_16x16x32_bf16 v[88:91], a[168:171], v[24:27], v[88:91]\n\t" \
    "ds_read_b128 v[48:51], v2 offset:9216\n\t" \
    "s_waitcnt lgkmcnt(5)\n\t" \
    "v_mfma_f32_16x16x32_bf16 v[92:95], a[168:171], v[28:31], v[92:95]\n\t" \
    "ds_read_b128 v[52:55], v2 offset:27648\n\t" \
    "v_mfma_f32_16x16x32_bf16 v[96:99], a[240:243], v[24:27], v[96:99]\n\t" \
    "v_cvt_pk_bf16_f32 v108, v76, v77\n\t" \
    "v_mfma_f32_16x16x32_bf16 v[100:103], a[240:243], v[28:31], v[100:103]\n\t" \
    "v_cvt_pk_bf16_f32 v109, v78, v79\n\t" \
    "s_waitcnt lgkmcnt(5)\n\t" \
    "v_mfma_f32_16x16x32_bf16 v[88:91], a[172:175], v[32:35], v[88:91]\n\t" \
    "ds_read_b128 v[56:59], v2 offset:10240\n\t" \
    "s_waitcnt lgkmcnt(5)\n\t" \
    "v_mfma_f32_16x16x32_bf16 v[92:95], a[172:175], v[36:39], v[92:95]\n\t" \
    "ds_read_b128 v[60:63], v2 offset:28672\n\t" \
    "v_mfma_f32_16x16x32_bf16 v[96:99], a[244:247], v[32:35], v[96:99]\n\t" \
    "v_cvt_pk_bf16_f32 v110, v84, v85\n\t" \
    "v_mfma_f32_16x16x32_bf16 v[100:103], a[244:247], v[36:39], v[100:103]\n\t" \
    "v_cvt_pk_bf16_f32 v111, v86, v87\n\t" \
    "global_store_dwordx4 v8, v[108:111], s[38:39] offset:1024 sc1\n\t" \
    "s_waitcnt lgkmcnt(5)\n\t" \
    "v_mfma_f32_16x16x32_bf16 v[88:91], a[176:179], v[40:43], v[88:91]\n\t" \
    "ds_read_b128 v[64:67], v2 offset:11264\n\t" \
    "s_waitcnt lgkmcnt(5)\n\t" \
    "v_mfma_f32_16x16x32_bf16 v[92:95], a[176:179], v[44:47], v[92:95]\n\t" \
    "ds_read_b128 v[68:71], v2 offset:29696\n\t" \
    "v_mfma_f32_16x16x32_bf16 v[96:99], a[248:251], v[40:43], v[96:99]\n\t" \
    "v_mfma_f32_16x16x32_bf16 v[100:103], a[248:251], v[44:47], v[100:103]\n\t" \
    "s_waitcnt lgkmcnt(5)\n\t" \
    "v_mfma_f32_16x16x32_bf16 v[88:91], a[180:183], v[48:51], v[88:91]\n\t" \
    "ds_read_b128 v[24:27], v2 offset:12288\n\t" \
    "s_waitcnt lgkmcnt(5)\n\t" \
    "v_mfma_f32_16x16x32_bf16 v[92:95], a[180:183], v[52:55], v[92:95]\n\t" \
    "ds_read_b128 v[28:31], v2 offset:30720\n\t" \
    "v_mfma_f32_16x16x32_bf16 v[96:99], a[252:255], v[48:51], v[96:99]\n\t" \
    "s_add_u32 m0, s91, 0\n\t" \
    "v_mfma_f32_16x16x32_bf16 v[100:103], a[252:255], v[52:55], v[100:103]\n\t" \
    "global_load_lds_dwordx4 v1, s[50:51] offset:0\n\t" \
    "s_waitcnt lgkmcnt(5)\n\t" \
    "v_mfma_f32_16x16x32_bf16 v[88:91], a[184:187], v[56:59], v[88:91]\n\t" \
    "ds_read_b128 v[32:35], v2 offset:13312\n\t" \
    "s_waitcnt lgkmcnt(5)\n\t" \
    "v_mfma_f32_16x16x32_bf16 v[92:95], a[184:187], v[60:63], v[92:95]\n\t" \
    "ds_read_b128 v[36:39], v2 offset:31744\n\t" \
    "v_mfma_f32_16x16x32_bf16 v[96:99], v[224:227], v[56:59], v[96:99]\n\t" \
    "s_add_u32 m0, s91, 960\n\t" \
    "v_mfma_f32_16x16x32_bf16 v[100:103], v[224:227], v[60:63], v[100:103]\n\t" \
    "global_load_lds_dwordx4 v1, s[50:51] offset:64\n\t" \
    "s_waitcnt lgkmcnt(5)\n\t" \
    "v_mfma_f32_16x16x32_bf16 v[88:91], a[188:191], v[64:67], v[88:91]\n\t" \
    "ds_read_b128 v[40:43], v2 offset:14336\n\t" \
    "s_waitcnt lgkmcnt(5)\n\t" \
    "v_mfma_f32_16x16x32_bf16 v[92:95], a[188:191], v[68:71], v[92:95]\n\t" \
    "ds_read_b128 v[44:47], v2 offset:32768\n\t" \
    "v_mfma_f32_16x16x32_bf16 v[96:99], v[228:231], v[64:67], v[96:99]\n\t" \
    "s_add_u32 m0, s91, 1920\n\t" \
    "v_mfma_f32_16x16x32_bf16 v[100:103], v[228:231], v[68:71], v[100:103]\n\t" \
    "global_load_lds_dwordx4 v1, s[50:51] offset:128\n\t" \
    "s_waitcnt lgkmcnt(5)\n\t" \
    "v_mfma_f32_16x16x32_bf16 v[88:91], a[192:195], v[24:27], v[88:91]\n\t" \
    "ds_read_b128 v[48:51], v2 offset:15360\n\t" \
    "s_waitcnt lgkmcnt(5)\n\t" \
    "v_mfma_f32_16x16x32_bf16 v[92:95], a[192:195], v[28:31], v[92:95]\n\t" \
    "ds_read_b128 v[52:55], v2 offset:33792\n\t" \
    "v_mfma_f32_16x16x32_bf16 v[96:99], v[232:235], v[24:27], v[96:99]\n\t" \
    "s_add_u32 m0, s91, 2880\n\t" \
    "v_mfma_f32_16x16x32_bf16 v[100:103], v[232:235], v[28:31], v[100:103]\n\t" \
    "global_load_lds_dwordx4 v1, s[50:51] offset:192\n\t" \
    "s_waitcnt lgkmcnt(5)\n\t" \
    "v_mfma_f32_16x16x32_bf16 v[88:91], a[196:199], v[32:35], v[88:91]\n\t" \
    "ds_read_b128 v[56:59], v2 offset:16384\n\t" \
    "s_waitcnt lgkmcnt(5)\n\t" \
    "v_mfma_f32_16x16x32_bf16 v[92:95], a[196:199], v[36:39], v[92:95]\n\t" \
    "ds_read_b128 v[60:63], v2 offset:34816\n\t" \
    "v_mfma_f32_16x16x32_bf16 v[96:99], v[236:239], v[32:35], v[96:99]\n\t" \
    "s_add_u32 m0, s91, 3840\n\t" \
    "v_mfma_f32_16x16x32_bf16 v[100:103], v[236:239], v[36:39], v[100:103]\n\t" \
    "global_load_lds_dwordx4 v1, s[50:51] offset:256\n\t" \
    "s_waitcnt lgkmcnt(5)\n\t" \
    "v_mfma_f32_16x16x32_bf16 v[88:91], a[200:203], v[40:43], v[88:91]\n\t" \
    "ds_read_b128 v[64:67], v2 offset:17408\n\t" \
    "s_waitcnt lgkmcnt(5)\n\t" \
    "v_mfma_f32_16x16x32_bf16 v[92:95], a[200:203], v[44:47], v[92:95]\n\t" \
    "ds_read_b128 v[68:71], v2 offset:35840\n\t" \
    "v_mfma_f32_16x16x32_bf16 v[96:99], v[240:243], v[40:43], v[96:99]\n\t" \
    "s_add_u32 m0, s91, 4800\n\t" \
    "v_mfma_f32_16x16x32_bf16 v[100:103], v[240:243], v[44:47], v[100:103]\n\t" \
    "global_load_lds_dwordx4 v1, s[50:51] offset:320\n\t" \
    "s_waitcnt lgkmcnt(5)\n\t" \
    "v_mfma_f32_16x16x32_bf16 v[88:91], a[204:207], v[48:51], v[88:91]\n\t" \
    "ds_read_b128 v[24:27], v2 offset:0\n\t" \
    "s_waitcnt lgkmcnt(5)\n\t" \
    "v_mfma_f32_16x16x32_bf16 v[92:95], a[204:207], v[52:55], v[92:95]\n\t" \
    "ds_read_b128 v[28:31], v2 offset:18432\n\t" \
    "v_mfma_f32_16x16x32_bf16 v[96:99], v[244:247], v[48:51], v[96:99]\n\t" \
    "s_add_u32 m0, s91, 5760\n\t" \
    "v_mfma_f32_16x16x32_bf16 v[100:103], v[244:247], v[52:55], v[100:103]\n\t" \
    "global_load_lds_dwordx4 v1, s[50:51] offset:384\n\t" \
    "s_waitcnt lgkmcnt(5)\n\t" \
    "v_mfma_f32_16x16x32_bf16 v[88:91], a[208:211], v[56:59], v[88:91]\n\t" \
    "ds_read_b128 v[32:35], v2 offset:1024\n\t" \
    "s_waitcnt lgkmcnt(5)\n\t" \
    "v_mfma_f32_16x16x32_bf16 v[92:95], a[208:211], v[60:63], v[92:95]\n\t" \
    "ds_read_b128 v[36:39], v2 offset:19456\n\t" \
    "v_mfma_f32_16x16x32_bf16 v[96:99], v[248:251], v[56:59], v[96:99]\n\t" \
    "s_add_u32 m0, s91, 6720\n\t" \
    "v_mfma_f32_16x16x32_bf16 v[100:103], v[248:251], v[60:63], v[100:103]\n\t" \
    "global_load_lds_dwordx4 v1, s[50:51] offset:448\n\t" \
    "s_waitcnt lgkmcnt(5)\n\t" \
    "v_mfma_f32_16x16x32_bf16 v[88:91], a[212:215], v[64:67], v[88:91]\n\t" \
    "ds_read_b128 v[40:43], v2 offset:2048\n\t" \
    "s_waitcnt lgkmcnt(5)\n\t" \
    "v_mfma_f32_16x16x32_bf16 v[92:95], a[212:215], v[68:71], v[92:95]\n\t" \
    "ds_read_b128 v[44:47], v2 offset:20480\n\t" \
    "v_mfma_f32_16x16x32_bf16 v[96:99], v[252:255], v[64:67], v[96:99]\n\t" \
    "s_add_u32 m0, s91, 7680\n\t" \
    "v_mfma_f32_16x16x32_bf16 v[100:103], v[252:255], v[68:71], v[100:103]\n\t" \
    "global_load_lds_dwordx4 v1, s[50:51] offset:512\n\t" \
    "gx_mid8_%=:\n\t" \
    "s_waitcnt lgkmcnt(5)\n\t" \
    "v_mfma_f32_16x16x32_bf16 v[72:75], a[0:3], v[24:27], 0\n\t" \
    "ds_read_b128 v[48:51], v2 offset:3072\n\t" \
    "s_waitcnt lgkmcnt(5)\n\t" \
    "v_mfma_f32_16x16x32_bf16 v[76:79], a[0:3], v[28:31], 0\n\t" \
    "ds_read_b128 v[52:55], v2 offset:21504\n\t" \
    "v_mfma_f32_16x16x32_bf16 v[80:83], a[72:75], v[24:27], 0\n\t" \
    "s_mul_i32 s70, s67, 7\n\t" \
    "s_add_u32 s69, s65, 0\n\t" \
    "s_xor_b32 s69, s69, s70\n\t" \
    "s_and_b32 s69, s69, 63\n\t" \
    "s_lshl_b32 s69, s69, 13\n\t" \
    "s_mul_i32 s70, s67, 524288\n\t" \
    "s_add_u32 s69, s69, s70\n\t" \
    "s_add_u32 s69, s69, 6291456\n\t" \
    "s_add_u32 s38, s36, s69\n\t" \
    "s_addc_u32 s39, s37, 0\n\t" \
    "v_mfma_f32_16x16x32_bf16 v[84:87], a[72:75], v[28:31], 0\n\t" \
    "s_add_u32 s40, s38, 16777216\n\t" \
    "s_addc_u32 s41, s39, 0\n\t" \
    "s_waitcnt lgkmcnt(5)\n\t" \
    "v_mfma_f32_16x16x32_bf16 v[72:75], a[4:7], v[32:35], v[72:75]\n\t" \
    "ds_read_b128 v[56:59], v2 offset:4096\n\t" \
    "s_waitcnt lgkmcnt(5)\n\t" \
    "v_mfma_f32_16x16x32_bf16 v[76:79], a[4:7], v[36:39], v[76:79]\n\t" \
    "ds_read_b128 v[60:63], v2 offset:22528\n\t" \
    "v_mfma_f32_16x16x32_bf16 v[80:83], a[76:79], v[32:35], v[80:83]\n\t" \
    "v_mfma_f32_16x16x32_bf16 v[84:87], a[76:79], v[36:39], v[84:87]\n\t" \
    "s_waitcnt lgkmcnt(5)\n\t" \
    "v_mfma_f32_16x16x32_bf16 v[72:75], a[8:11], v[40:43], v[72:75]\n\t" \
    "ds_read_b128 v[64:67], v2 offset:5120\n\t" \
    "s_waitcnt lgkmcnt(5)\n\t" \
    "v_mfma_f32_16x16x32_bf16 v[76:79], a[8:11], v[44:47], v[76:79]\n\t" \
    "ds_read_b128 v[68:71], v2 offset:23552\n\t" \
    "v_mfma_f32_16x16x32_bf16 v[80:83], a[80:83], v[40:43], v[80:83]\n\t" \
    "v_cvt_pk_bf16_f32 v112, v88, v89\n\t" \
    "v_mfma_f32_16x16x32_bf16 v[84:87], a[80:83], v[44:47], v[84:87]\n\t" \
    "v_cvt_pk_bf16_f32 v113, v90, v91\n\t" \
    "s_waitcnt lgkmcnt(5)\n\t" \
    "v_mfma_f32_16x16x32_bf16 v[72:75], a[12:15], v[48:51], v[72:75]\n\t" \
    "ds_read_b128 v[24:27], v2 offset:6144\n\t" \
    "s_waitcnt lgkmcnt(5)\n\t" \
    "v_mfma_f32_16x16x32_bf16 v[76:79], a[12:15], v[52:55], v[76:79]\n\t" \
    "ds_read_b128 v[28:31], v2 offset:24576\n\t" \
    "v_mfma_f32_16x16x32_bf16 v[80:83], a[84:87], v[48:51], v[80:83]\n\t" \
    "v_cvt_pk_bf16_f32 v114, v96, v97\n\t" \
    "v_mfma_f32_16x16x32_bf16 v[84:87], a[84:87], v[52:55], v[84:87]\n\t" \
    "v_cvt_pk_bf16_f32 v115, v98, v99\n\t" \
    "global_store_dwordx4 v8, v[112:115], s[40:41] offset:0 sc1\n\t" \
    "s_waitcnt lgkmcnt(5)\n\t" \
    "v_mfma_f32_16x16x32_bf16 v[72:75], a[16:19], v[56:59], v[72:75]\n\t" \
    "ds_read_b128 v[32:35], v2 offset:7168\n\t" \
    "s_waitcnt lgkmcnt(5)\n\t" \
    "v_mfma_f32_16x16x32_bf16 v[76:79], a[16:19], v[60:63], v[76:79]\n\t" \
    "ds_read_b128 v[36:39], v2 offset:25600\n\t" \
    "v_mfma_f32_16x16x32_bf16 v[80:83], a[88:91], v[56:59], v[80:83]\n\t" \
    "v_cvt_pk_bf16_f32 v116, v92, v93\n\t" \
    "v_mfma_f32_16x16x32_bf16 v[84:87], a[88:91], v[60:63], v[84:87]\n\t" \
    "v_cvt_pk_bf16_f32 v117, v94, v95\n\t" \
    "s_waitcnt lgkmcnt(5)\n\t" \
    "v_mfma_f32_16x16x32_bf16 v[72:75], a[20:23], v[64:67], v[72:75]\n\t" \
    "ds_read_b128 v[40:43], v2 offset:8192\n\t" \
    "s_waitcnt lgkmcnt(5)\n\t" \
    "v_mfma_f32_16x16x32_bf16 v[76:79], a[20:23], v[68:71], v[76:79]\n\t" \
    "ds_read_b128 v[44:47], v2 offset:26624\n\t" \
    "v_mfma_f32_16x16x32_bf16 v[80:83], a[92:95], v[64:67], v[80:83]\n\t" \
    "v_cvt_pk_bf16_f32 v118, v100, v101\n\t" \
    "v_mfma_f32_16x16x32_bf16 v[84:87], a[92:95], v[68:71], v[84:87]\n\t" \
    "v_cvt_pk_bf16_f32 v119, v102, v103\n\t" \
    "global_store_dwordx4 v8, v[116:119], s[40:41] offset:1024 sc1\n\t" \
    "s_waitcnt lgkmcnt(5)\n\t" \
    "v_mfma_f32_16x16x32_bf16 v[72:75], a[24:27], v[24:27], v[72:75]\n\t" \
    "ds_read_b128 v[48:51], v2 offset:9216\n\t" \
    "s_waitcnt lgkmcnt(5)\n\t" \
    "v_mfma_f32_16x16x32_bf16 v[76:79], a[24:27], v[28:31], v[76:79]\n\t" \
    "ds_read_b128 v[52:55], v2 offset:27648\n\t" \
    "v_mfma_f32_16x16x32_bf16 v[80:83], a[96:99], v[24:27], v[80:83]\n\t" \
    "v_mfma_f32_16x16x32_bf16 v[84:87], a[96:99], v[28:31], v[84:87]\n\t" \
    "s_waitcnt lgkmcnt(5)\n\t" \
    "v_mfma_f32_16x16x32_bf16 v[72:75], a[28:31], v[32:35], v[72:75]\n\t" \
    "ds_read_b128 v[56:59], v2 offset:10240\n\t" \
    "s_waitcnt lgkmcnt(5)\n\t" \
    "v_mfma_f32_16x16x32_bf16 v[76:79], a[28:31], v[36:39], v[76:79]\n\t" \
    "ds_read_b128 v[60:63], v2 offset:28672\n\t" \
    "v_mfma_f32_16x16x32_bf16 v[80:83], a[100:103], v[32:35], v[80:83]\n\t" \
    "v_mfma_f32_16x16x32_bf16 v[84:87], a[100:103], v[36:39], v[84:87]\n\t" \
    "s_waitcnt lgkmcnt(5)\n\t" \
    "v_mfma_f32_16x16x32_bf16 v[72:75], a[32:35], v[40:43], v[72:75]\n\t" \
    "ds_read_b128 v[64:67], v2 offset:11264\n\t" \
    "s_waitcnt lgkmcnt(5)\n\t" \
    "v_mfma_f32_16x16x32_bf16 v[76:79], a[32:35], v[44:47], v[76:79]\n\t" \
    "ds_read_b128 v[68:71], v2 offset:29696\n\t" \
    "v_mfma_f32_16x16x32_bf16 v[80:83], a[104:107], v[40:43], v[80:83]\n\t" \
    "v_mfma_f32_16x16x32_bf16 v[84:87], a[104:107], v[44:47], v[84:87]\n\t" \
    "s_waitcnt lgkmcnt(5)\n\t" \
    "v_mfma_f32_16x16x32_bf16 v[72:75], a[36:39], v[48:51], v[72:75]\n\t" \
    "ds_read_b128 v[24:27], v2 offset:12288\n\t" \
    "s_waitcnt lgkmcnt(5)\n\t" \
    "v_mfma_f32_16x16x32_bf16 v[76:79], a[36:39], v[52:55], v[76:79]\n\t" \
    "ds_read_b128 v[28:31], v2 offset:30720\n\t" \
    "v_mfma_f32_16x16x32_bf16 v[80:83], a[108:111], v[48:51], v[80:83]\n\t" \
    "v_mfma_f32_16x16x32_bf16 v[84:87], a[108:111], v[52:55], v[84:87]\n\t" \
    "s_waitcnt lgkmcnt(5)\n\t" \
    "v_mfma_f32_16x16x32_bf16 v[72:75], a[40:43], v[56:59], v[72:75]\n\t" \
    "ds_read_b128 v[32:35], v2 offset:13312\n\t" \
    "s_waitcnt lgkmcnt(5)\n\t" \
    "v_mfma_f32_16x16x32_bf16 v[76:79], a[40:43], v[60:63], v[76:79]\n\t" \
    "ds_read_b128 v[36:39], v2 offset:31744\n\t" \
    "v_mfma_f32_16x16x32_bf16 v[80:83], a[112:115], v[56:59], v[80:83]\n\t" \
    "v_mfma_f32_16x16x32_bf16 v[84:87], a[112:115], v[60:63], v[84:87]\n\t" \
    "s_waitcnt lgkmcnt(5)\n\t" \
    "v_mfma_f32_16x16x32_bf16 v[72:75], a[44:47], v[64:67], v[72:75]\n\t" \
    "ds_read_b128 v[40:43], v2 offset:14336\n\t" \
    "s_waitcnt lgkmcnt(5)\n\t" \
    "v_mfma_f32_16x16x32_bf16 v[76:79], a[44:47], v[68:71], v[76:79]\n\t" \
    "ds_read_b128 v[44:47], v2 offset:32768\n\t" \
    "v_mfma_f32_16x16x32_bf16 v[80:83], a[116:119], v[64:67], v[80:83]\n\t" \
    "v_mfma_f32_16x16x32_bf16 v[84:87], a[116:119], v[68:71], v[84:87]\n\t" \
    "s_waitcnt lgkmcnt(5)\n\t" \
    "v_mfma_f32_16x16x32_bf16 v[72:75], a[48:51], v[24:27], v[72:75]\n\t" \
    "ds_read_b128 v[48:51], v2 offset:15360\n\t" \
    "s_waitcnt lgkmcnt(5)\n\t" \
    "v_mfma_f32_16x16x32_bf16 v[76:79], a[48:51], v[28:31], v[76:79]\n\t" \
    "ds_read_b128 v[52:55], v2 offset:33792\n\t" \
    "v_mfma_f32_16x16x32_bf16 v[80:83], a[120:123], v[24:27], v[80:83]\n\t" \
    "v_mfma_f32_16x16x32_bf16 v[84:87], a[120:123], v[28:31], v[84:87]\n\t" \
    "s_waitcnt lgkmcnt(5)\n\t" \
    "v_mfma_f32_16x16x32_bf16 v[72:75], a[52:55], v[32:35], v[72:75]\n\t" \
    "ds_read_b128 v[56:59], v2 offset:16384\n\t" \
    "s_waitcnt lgkmcnt(5)\n\t" \
    "v_mfma_f32_16x16x32_bf16 v[76:79], a[52:55], v[36:39], v[76:79]\n\t" \
    "ds_read_b128 v[60:63], v2 offset:34816\n\t" \
    "v_mfma_f32_16x16x32_bf16 v[80:83], a[124:127], v[32:35], v[80:83]\n\t" \
    "v_mfma_f32_16x16x32_bf16 v[84:87], a[124:127], v[36:39], v[84:87]\n\t" \
    "s_cmp_lg_u32 s65, 0\n\t" \
    "s_cbranch_scc1 gx_g110_%=\n\t" \
    "s_add_u32 m0, s91, 0\n\t" \
    "s_nop 0\n\t" \
    "global_load_lds_dwordx4 v1, s[50:51] offset:0\n\t" \
    "s_add_u32 m0, s91, 960\n\t" \
    "s_nop 0\n\t" \
    "global_load_lds_dwordx4 v1, s[50:51] offset:64\n\t" \
    "s_add_u32 m0, s91, 1920\n\t" \
    "s_nop 0\n\t" \
    "global_load_lds_dwordx4 v1, s[50:51] offset:128\n\t" \
    "s_add_u32 m0, s91, 2880\n\t" \
    "s_nop 0\n\t" \
    "global_load_lds_dwordx4 v1, s[50:51] offset:192\n\t" \
    "s_add_u32 m0, s91, 3840\n\t" \
    "s_nop 0\n\t" \
    "global_load_lds_dwordx4 v1, s[50:51] offset:256\n\t" \
    "s_add_u32 m0, s91, 4800\n\t" \
    "s_nop 0\n\t" \
    "global_load_lds_dwordx4 v1, s[50:51] offset:320\n\t" \
    "s_add_u32 m0, s91, 5760\n\t" \
    "s_nop 0\n\t" \
    "global_load_lds_dwordx4 v1, s[50:51] offset:384\n\t" \
    "s_add_u32 m0, s91, 6720\n\t" \
    "s_nop 0\n\t" \
    "global_load_lds_dwordx4 v1, s[50:51] offset:448\n\t" \
    "s_add_u32 m0, s91, 7680\n\t" \
    "s_nop 0\n\t" \
    "global_load_lds_dwordx4 v1, s[50:51] offset:512\n\t" \
    "gx_g110_%=:\n\t" \
    "s_waitcnt lgkmcnt(5)\n\t" \
    "v_mfma_f32_16x16x32_bf16 v[72:75], a[56:59], v[40:43], v[72:75]\n\t" \
    "ds_read_b128 v[64:67], v2 offset:17408\n\t" \
    "s_waitcnt lgkmcnt(5)\n\t" \
    "v_mfma_f32_16x16x32_bf16 v[76:79], a[56:59], v[44:47], v[76:79]\n\t" \
    "ds_read_b128 v[68:71], v2 offset:35840\n\t" \
    "v_mfma_f32_16x16x32_bf16 v[80:83], a[128:131], v[40:43], v[80:83]\n\t" \
    "v_mfma_f32_16x16x32_bf16 v[84:87], a[128:131], v[44:47], v[84:87]\n\t" \
    "s_waitcnt lgkmcnt(5)\n\t" \
    "v_mfma_f32_16x16x32_bf16 v[72:75], a[60:63], v[48:51], v[72:75]\n\t" \
    "ds_read_b128 v[24:27], v17 offset:0\n\t" \
    "s_waitcnt lgkmcnt(5)\n\t" \
    "v_mfma_f32_16x16x32_bf16 v[76:79], a[60:63], v[52:55], v[76:79]\n\t" \
    "ds_read_b128 v[28:31], v17 offset:18432\n\t" \
    "v_mfma_f32_16x16x32_bf16 v[80:83], a[132:135], v[48:51], v[80:83]\n\t" \
    "v_mfma_f32_16x16x32_bf16 v[84:87], a[132:135], v[52:55], v[84:87]\n\t" \
    "s_waitcnt lgkmcnt(5)\n\t" \
    "v_mfma_f32_16x16x32_bf16 v[72:75], a[64:67], v[56:59], v[72:75]\n\t" \
    "ds_read_b128 v[32:35], v17 offset:1024\n\t" \
    "s_waitcnt lgkmcnt(5)\n\t" \
    "v_mfma_f32_16x16x32_bf16 v[76:79], a[64:67], v[60:63], v[76:79]\n\t" \
    "ds_read_b128 v[36:39], v17 offset:19456\n\t" \
    "v_mfma_f32_16x16x32_bf16 v[80:83], a[136:139], v[56:59], v[80:83]\n\t" \
    "v_mfma_f32_16x16x32_bf16 v[84:87], a[136:139], v[60:63], v[84:87]\n\t" \
    "s_waitcnt lgkmcnt(5)\n\t" \
    "v_mfma_f32_16x16x32_bf16 v[72:75], a[68:71], v[64:67], v[72:75]\n\t" \
    "ds_read_b128 v[40:43], v17 offset:2048\n\t" \
    "s_waitcnt lgkmcnt(5)\n\t" \
    "v_mfma_f32_16x16x32_bf16 v[76:79], a[68:71], v[68:71], v[76:79]\n\t" \
    "ds_read_b128 v[44:47], v17 offset:20480\n\t" \
    "v_mfma_f32_16x16x32_bf16 v[80:83], a[140:143], v[64:67], v[80:83]\n\t" \
    "v_mfma_f32_16x16x32_bf16 v[84:87], a[140:143], v[68:71], v[84:87]\n\t" \
    "v_mov_b32 v2, v17\n\t" \
    "s_add_u32 s65, s65, 1\n\t" \
    "s_cmp_lt_u32 s65, 64\n\t" \
    "s_cbranch_scc1 gx_loop6_%=\n\t" \
    "s_nop 7\n\t" \
    "v_cvt_pk_bf16_f32 v104, v72, v73\n\t" \
    "v_cvt_pk_bf16_f32 v105, v74, v75\n\t" \
    "v_cvt_pk_bf16_f32 v106, v80, v81\n\t" \
    "v_cvt_pk_bf16_f32 v107, v82, v83\n\t" \
    "global_store_dwordx4 v8, v[104:107], s[38:39] offset:0 sc1\n\t" \
    "v_cvt_pk_bf16_f32 v108, v76, v77\n\t" \
    "v_cvt_pk_bf16_f32 v109, v78, v79\n\t" \
    "v_cvt_pk_bf16_f32 v110, v84, v85\n\t" \
    "v_cvt_pk_bf16_f32 v111, v86, v87\n\t" \
    "global_store_dwordx4 v8, v[108:111], s[38:39] offset:1024 sc1\n\t" \
    "s_waitcnt vmcnt(0) lgkmcnt(0)\n\t" \
    "s_barrier\n\t" \
    "s_cmp_lg_u32 s66, 0\n\t" \
    "s_cbranch_scc1 gx_SF11_%=\n\t" \
    "s_lshl_b32 s69, s67, 7\n\t" \
    "s_add_u32 s69, s69, 6098944\n\t" \
    "s_add_u32 s69, s69, s52\n\t" \
    "s_add_u32 s70, s65, 0\n\t" \
    "v_mov_b32 v156, s69\n\t" \
    "v_mov_b32 v157, s70\n\t" \
    "s_add_u32 s69, s69, 61440\n\t" \
    "s_max_i32 s70, s70, 1\n\t" \
    "v_writelane_b32 v156, s69, 1\n\t" \
    "v_writelane_b32 v157, s70, 1\n\t" \
    "s_mov_b64 exec, 3\n\t" \
    "global_store_dword v156, v157, s[36:37] sc1\n\t" \
    "s_mov_b64 exec, -1\n\t" \
    "gx_SF11_%=:\n\t" \
    "s_waitcnt vmcnt(0)\n\t" \
    "s_branch ea_done_%=\n\t" \
    "gv_begin_%=:\n\t" \
    "v_mbcnt_lo_u32_b32 v223, -1, 0\n\t" \
    "v_mbcnt_hi_u32_b32 v223, -1, v223\n\t" \
    "s_mov_b32 s68, 262144\n\t" \
    "s_mov_b32 s72, 0\n\t" \
    "s_mov_b32 s83, -1\n\t" \
    "s_mov_b32 s65, 0\n\t" \
    "v_and_b32 v180, 3, v223\n\t" \
    "v_lshlrev_b32 v202, 3, v180\n\t" \
    "v_lshrrev_b32 v181, 5, v223\n\t" \
    "v_lshl_add_u32 v202, v181, 12, v202\n\t" \
    "v_bfe_u32 v182, v223, 2, 2\n\t" \
    "s_mov_b32 s70, 288\n\t" \
    "v_mad_u32_u24 v202, v182, s70, v202\n\t" \
    "v_bfe_u32 v183, v223, 4, 1\n\t" \
    "s_mov_b32 s70, 2176\n\t" \
    "v_mad_u32_u24 v202, v183, s70, v202\n\t" \
    "s_lshl_b32 s69, s66, 6\n\t" \
    "v_xor_b32 v202, s69, v202\n\t" \
    "v_xor_b32 v203, 32, v202\n\t" \
    "s_mul_i32 s69, s66, 9216\n\t" \
    "v_and_b32 v184, 1, v223\n\t" \
    "v_lshl_add_u32 v201, v184, 4, s69\n\t" \
    "v_lshrrev_b32 v185, 3, v223\n\t" \
    "s_mov_b32 s70, 1152\n\t" \
    "v_mad_u32_u24 v201, v185, s70, v201\n\t" \
    "v_bfe_u32 v186, v223, 1, 2\n\t" \
    "v_lshlrev_b32 v204, 5, v186\n\t" \
    "v_lshrrev_b32 v187, 4, v223\n\t" \
    "v_lshlrev_b32 v205, 5, v187\n\t" \
    "v_xor_b32 v204, v204, v205\n\t" \
    "v_add_u32 v201, v201, v204\n\t" \
    "s_mul_i32 s96, s66, 9216\n\t" \
    "v_lshlrev_b32 v207, 3, v180\n\t" \
    "v_bfe_u32 v188, v223, 2, 1\n\t" \
    "v_lshl_add_u32 v207, v188, 7, v207\n\t" \
    "v_bfe_u32 v189, v223, 3, 2\n\t" \
    "s_mov_b32 s70, 288\n\t" \
    "v_mad_u32_u24 v207, v189, s70, v207\n\t" \
    "s_mov_b32 s70, 9216\n\t" \
    "v_mad_u32_u24 v207, v181, s70, v207\n\t" \
    "s_mul_i32 s69, s66, 9\n\t" \
    "s_add_u32 s69, s69, 0\n\t" \
    "s_and_b32 s70, s69, 3\n\t" \
    "s_lshl_b32 s70, s70, 5\n\t" \
    "s_lshr_b32 s69, s69, 2\n\t" \
    "s_lshl_b32 s69, s69, 10\n\t" \
    "v_xor_b32 v204, s70, v207\n\t" \
    "v_add_u32 v204, s69, v204\n\t" \
    "s_mul_i32 s69, s66, 9\n\t" \
    "s_add_u32 s69, s69, 1\n\t" \
    "s_and_b32 s70, s69, 3\n\t" \
    "s_lshl_b32 s70, s70, 5\n\t" \
    "s_lshr_b32 s69, s69, 2\n\t" \
    "s_lshl_b32 s69, s69, 10\n\t" \
    "v_xor_b32 v205, s70, v207\n\t" \
    "v_add_u32 v205, s69, v205\n\t" \
    "s_mul_i32 s69, s66, 9\n\t" \
    "s_add_u32 s69, s69, 2\n\t" \
    "s_and_b32 s70, s69, 3\n\t" \
    "s_lshl_b32 s70, s70, 5\n\t" \
    "s_lshr_b32 s69, s69, 2\n\t" \
    "s_lshl_b32 s69, s69, 10\n\t" \
    "v_xor_b32 v206, s70, v207\n\t" \
    "v_add_u32 v206, s69, v206\n\t" \
    "s_mul_i32 s69, s66, 9\n\t" \
    "s_add_u32 s69, s69, 3\n\t" \
    "s_and_b32 s70, s69, 3\n\t" \
    "s_lshl_b32 s70, s70, 5\n\t" \
    "s_lshr_b32 s69, s69, 2\n\t" \
    "s_lshl_b32 s69, s69, 10\n\t" \
    "v_xor_b32 v207, s70, v207\n\t" \
    "v_add_u32 v207, s69, v207\n\t" \
    "s_waitcnt vmcnt(12) lgkmcnt(0)\n\t" \
    "s_barrier\n\t" \
    "s_add_u32 m0, s82, 65536\n\t" \
    "s_nop 0\n\t" \
    "global_load_lds_dwordx4 v200, s[42:43]\n\t" \
    "v_add_u32 v200, 131072, v200\n\t" \
    "s_add_u32 m0, s82, 69632\n\t" \
    "s_nop 0\n\t" \
    "global_load_lds_dwordx4 v200, s[42:43]\n\t" \
    "v_add_u32 v200, 131072, v200\n\t" \
    "s_add_u32 m0, s82, 73728\n\t" \
    "s_nop 0\n\t" \
    "global_load_lds_dwordx4 v200, s[42:43]\n\t" \
    "v_add_u32 v200, 131072, v200\n\t" \
    "s_add_u32 m0, s82, 77824\n\t" \
    "s_nop 0\n\t" \
    "global_load_lds_dwordx4 v200, s[42:43]\n\t" \
    "v_add_u32 v200, 131072, v200\n\t" \
    "ds_read_b64_tr_b16 a[0:1], v202 offset:36864\n\t" \
    "ds_read_b64_tr_b16 a[2:3], v202 offset:37888\n\t" \
    "ds_read_b64_tr_b16 a[4:5], v202 offset:45056\n\t" \
    "ds_read_b64_tr_b16 a[6:7], v202 offset:46080\n\t" \
    "ds_read_b64_tr_b16 a[72:73], v203 offset:36864\n\t" \
    "ds_read_b64_tr_b16 a[74:75], v203 offset:37888\n\t" \
    "ds_read_b64_tr_b16 a[76:77], v203 offset:45056\n\t" \
    "ds_read_b64_tr_b16 a[78:79], v203 offset:46080\n\t" \
    "v_lshlrev_b32 v3, 4, v180\n\t" \
    "v_lshlrev_b32 v2, 5, v181\n\t" \
    "v_xor_b32 v3, v3, v2\n\t" \
    "s_bfe_u32 s71, s66, 0x10000\n\t" \
    "s_mul_i32 s69, s71, 576\n\t" \
    "s_bfe_u32 s71, s66, 0x10001\n\t" \
    "s_mul_i32 s71, s71, 18432\n\t" \
    "s_add_u32 s69, s69, s71\n\t" \
    "v_lshrrev_b32 v190, 2, v223\n\t" \
    "v_mov_b32 v1, s69\n\t" \
    "s_mov_b32 s70, 1152\n\t" \
    "v_mad_u32_u24 v1, v190, s70, v1\n\t" \
    "v_add_u32 v1, v1, v3\n\t" \
    "v_and_b32 v191, 15, v223\n\t" \
    "v_lshlrev_b32 v3, 6, v191\n\t" \
    "v_lshl_add_u32 v3, v187, 4, v3\n\t" \
    "v_bfe_u32 v192, v223, 3, 1\n\t" \
    "v_lshlrev_b32 v2, 5, v192\n\t" \
    "v_xor_b32 v3, v3, v2\n\t" \
    "v_mov_b32 v2, v3\n\t" \
    "s_lshr_b32 s65, s67, 5\n\t" \
    "s_mul_i32 s69, s65, 36864\n\t" \
    "s_add_u32 s50, s50, s69\n\t" \
    "s_addc_u32 s51, s51, 0\n\t" \
    "s_add_u32 s38, s36, 48234496\n\t" \
    "s_addc_u32 s39, s37, 0\n\t" \
    "s_mul_i32 s89, s66, 9216\n\t" \
    "s_mov_b32 s90, 0\n\t" \
    "s_waitcnt vmcnt(12) lgkmcnt(0)\n\t" \
    "s_barrier\n\t" \
    "s_add_u32 m0, s82, 81920\n\t" \
    "s_nop 0\n\t" \
    "global_load_lds_dwordx4 v200, s[42:43]\n\t" \
    "v_add_u32 v200, 131072, v200\n\t" \
    "s_add_u32 m0, s82, 86016\n\t" \
    "s_nop 0\n\t" \
    "global_load_lds_dwordx4 v200, s[42:43]\n\t" \
    "v_add_u32 v200, 131072, v200\n\t" \
    "s_add_u32 m0, s82, 90112\n\t" \
    "s_nop 0\n\t" \
    "global_load_lds_dwordx4 v200, s[42:43]\n\t" \
    "v_add_u32 v200, 131072, v200\n\t" \
    "s_add_u32 m0, s82, 94208\n\t" \
    "s_nop 0\n\t" \
    "global_load_lds_dwordx4 v200, s[42:43]\n\t" \
    "v_add_u32 v200, 131072, v200\n\t" \
    "ds_read_b64_tr_b16 a[8:9], v202 offset:53248\n\t" \
    "ds_read_b64_tr_b16 a[10:11], v202 offset:54272\n\t" \
    "ds_read_b64_tr_b16 a[12:13], v202 offset:61440\n\t" \
    "ds_read_b64_tr_b16 a[14:15], v202 offset:62464\n\t" \
    "ds_read_b64_tr_b16 a[80:81], v203 offset:53248\n\t" \
    "ds_read_b64_tr_b16 a[82:83], v203 offset:54272\n\t" \
    "ds_read_b64_tr_b16 a[84:85], v203 offset:61440\n\t" \
    "ds_read_b64_tr_b16 a[86:87], v203 offset:62464\n\t" \
    "v_lshlrev_b32 v0, 4, v223\n\t" \
    "s_mul_i32 s69, s66, 2048\n\t" \
    "s_add_u32 s69, s69, 147456\n\t" \
    "v_lshl_add_u32 v4, v223, 4, s69\n\t" \
    "s_mov_b32 s69, 147456\n\t" \
    "v_lshl_add_u32 v5, v223, 4, s69\n\t" \
    "v_lshlrev_b32 v6, 2, v223\n\t" \
    "v_mov_b32 v7, 0\n\t" \
    "s_waitcnt vmcnt(12) lgkmcnt(0)\n\t" \
    "s_barrier\n\t" \
    "s_add_u32 m0, s82, 98304\n\t" \
    "s_nop 0\n\t" \
    "global_load_lds_dwordx4 v200, s[42:43]\n\t" \
    "v_add_u32 v200, 131072, v200\n\t" \
    "s_add_u32 m0, s82, 102400\n\t" \
    "s_nop 0\n\t" \
    "global_load_lds_dwordx4 v200, s[42:43]\n\t" \
    "v_add_u32 v200, 131072, v200\n\t" \
    "s_add_u32 m0, s82, 106496\n\t" \
    "s_nop 0\n\t" \
    "global_load_lds_dwordx4 v200, s[42:43]\n\t" \
    "v_add_u32 v200, 131072, v200\n\t" \
    "s_add_u32 m0, s82, 110592\n\t" \
    "s_nop 0\n\t" \
    "global_load_lds_dwordx4 v200, s[42:43]\n\t" \
    "v_add_u32 v200, 131072, v200\n\t" \
    "v_add_u32 v202, 69632, v202\n\t" \
    "v_add_u32 v203, 69632, v203\n\t" \
    "ds_read_b64_tr_b16 a[16:17], v202 offset:0\n\t" \
    "ds_read_b64_tr_b16 a[18:19], v202 offset:1024\n\t" \
    "ds_read_b64_tr_b16 a[20:21], v202 offset:8192\n\t" \
    "ds_read_b64_tr_b16 a[22:23], v202 offset:9216\n\t" \
    "ds_read_b64_tr_b16 a[88:89], v203 offset:0\n\t" \
    "ds_read_b64_tr_b16 a[90:91], v203 offset:1024\n\t" \
    "ds_read_b64_tr_b16 a[92:93], v203 offset:8192\n\t" \
    "ds_read_b64_tr_b16 a[94:95], v203 offset:9216\n\t" \
    "s_bfe_u32 s71, s67, 0x50000\n\t" \
    "s_mul_i32 s69, s71, 32\n\t" \
    "s_add_u32 s69, s69, 262148\n\t" \
    "v_lshl_add_u32 v19, v180, 16, s69\n\t" \
    "v_lshl_add_u32 v19, v181, 13, v19\n\t" \
    "v_bfe_u32 v193, v223, 2, 3\n\t" \
    "v_lshlrev_b32 v199, 10, v193\n\t" \
    "v_min_u32 v199, 4096, v199\n\t" \
    "v_add_u32 v19, v199, v19\n\t" \
    "s_bfe_u32 s71, s67, 0x50000\n\t" \
    "s_mul_i32 s69, s71, 1024\n\t" \
    "v_lshl_add_u32 v17, v223, 4, s69\n\t" \
    "s_mul_i32 s69, s66, 9\n\t" \
    "s_add_u32 s69, s69, 0\n\t" \
    "s_mul_i32 s70, s69, 7\n\t" \
    "s_and_b32 s70, s70, 63\n\t" \
    "s_lshl_b32 s70, s70, 15\n\t" \
    "s_mul_i32 s69, s69, 2097152\n\t" \
    "s_add_u32 s69, s69, s70\n\t" \
    "v_add_u32 v8, s69, v17\n\t" \
    "s_mul_i32 s69, s66, 9\n\t" \
    "s_add_u32 s69, s69, 1\n\t" \
    "s_mul_i32 s70, s69, 7\n\t" \
    "s_and_b32 s70, s70, 63\n\t" \
    "s_lshl_b32 s70, s70, 15\n\t" \
    "s_mul_i32 s69, s69, 2097152\n\t" \
    "s_add_u32 s69, s69, s70\n\t" \
    "v_add_u32 v9, s69, v17\n\t" \
    "s_mul_i32 s69, s66, 9\n\t" \
    "s_add_u32 s69, s69, 2\n\t" \
    "s_mul_i32 s70, s69, 7\n\t" \
    "s_and_b32 s70, s70, 63\n\t" \
    "s_lshl_b32 s70, s70, 15\n\t" \
    "s_mul_i32 s69, s69, 2097152\n\t" \
    "s_add_u32 s69, s69, s70\n\t" \
    "v_add_u32 v10, s69, v17\n\t" \
    "s_mul_i32 s69, s66, 9\n\t" \
    "s_add_u32 s69, s69, 3\n\t" \
    "s_mul_i32 s70, s69, 7\n\t" \
    "s_and_b32 s70, s70, 63\n\t" \
    "s_lshl_b32 s70, s70, 15\n\t" \
    "s_mul_i32 s69, s69, 2097152\n\t" \
    "s_add_u32 s69, s69, s70\n\t" \
    "v_add_u32 v11, s69, v17\n\t" \
    "s_mul_i32 s69, s66, 9\n\t" \
    "s_add_u32 s69, s69, 4\n\t" \
    "s_mul_i32 s70, s69, 7\n\t" \
    "s_and_b32 s70, s70, 63\n\t" \
    "s_lshl_b32 s70, s70, 15\n\t" \
    "s_mul_i32 s69, s69, 2097152\n\t" \
    "s_add_u32 s69, s69, s70\n\t" \
    "v_add_u32 v12, s69, v17\n\t" \
    "s_mul_i32 s69, s66, 9\n\t" \
    "s_add_u32 s69, s69, 5\n\t" \
    "s_mul_i32 s70, s69, 7\n\t" \
    "s_and_b32 s70, s70, 63\n\t" \
    "s_lshl_b32 s70, s70, 15\n\t" \
    "s_mul_i32 s69, s69, 2097152\n\t" \
    "s_add_u32 s69, s69, s70\n\t" \
    "v_add_u32 v13, s69, v17\n\t" \
    "s_mul_i32 s69, s66, 9\n\t" \
    "s_add_u32 s69, s69, 6\n\t" \
    "s_mul_i32 s70, s69, 7\n\t" \
    "s_and_b32 s70, s70, 63\n\t" \
    "s_lshl_b32 s70, s70, 15\n\t" \
    "s_mul_i32 s69, s69, 2097152\n\t" \
    "s_add_u32 s69, s69, s70\n\t" \
    "v_add_u32 v14, s69, v17\n\t" \
    "s_mul_i32 s69, s66, 9\n\t" \
    "s_add_u32 s69, s69, 7\n\t" \
    "s_mul_i32 s70, s69, 7\n\t" \
    "s_and_b32 s70, s70, 63\n\t" \
    "s_lshl_b32 s70, s70, 15\n\t" \
    "s_mul_i32 s69, s69, 2097152\n\t" \
    "s_add_u32 s69, s69, s70\n\t" \
    "v_add_u32 v15, s69, v17\n\t" \
    "s_mul_i32 s69, s66, 9\n\t" \
    "s_add_u32 s69, s69, 8\n\t" \
    "s_mul_i32 s70, s69, 7\n\t" \
    "s_and_b32 s70, s70, 63\n\t" \
    "s_lshl_b32 s70, s70, 15\n\t" \
    "s_mul_i32 s69, s69, 2097152\n\t" \
    "s_add_u32 s69, s69, s70\n\t" \
    "v_add_u32 v16, s69, v17\n\t" \
    "s_waitcnt vmcnt(12) lgkmcnt(0)\n\t" \
    "s_barrier\n\t" \
    "s_add_u32 m0, s82, 0\n\t" \
    "s_nop 0\n\t" \
    "global_load_lds_dwordx4 v200, s[42:43]\n\t" \
    "v_add_u32 v200, 131072, v200\n\t" \
    "s_add_u32 m0, s82, 4096\n\t" \
    "s_nop 0\n\t" \
    "global_load_lds_dwordx4 v200, s[42:43]\n\t" \
    "v_add_u32 v200, 131072, v200\n\t" \
    "s_add_u32 m0, s82, 8192\n\t" \
    "s_nop 0\n\t" \
    "global_load_lds_dwordx4 v200, s[42:43]\n\t" \
    "v_add_u32 v200, 131072, v200\n\t" \
    "s_add_u32 m0, s82, 12288\n\t" \
    "s_nop 0\n\t" \
    "global_load_lds_dwordx4 v200, s[42:43]\n\t" \
    "v_add_u32 v200, 131072, v200\n\t" \
    "ds_read_b64_tr_b16 a[24:25], v202 offset:16384\n\t" \
    "ds_read_b64_tr_b16 a[26:27], v202 offset:17408\n\t" \
    "ds_read_b64_tr_b16 a[28:29], v202 offset:24576\n\t" \
    "ds_read_b64_tr_b16 a[30:31], v202 offset:25600\n\t" \
    "ds_read_b64_tr_b16 a[96:97], v203 offset:16384\n\t" \
    "ds_read_b64_tr_b16 a[98:99], v203 offset:17408\n\t" \
    "ds_read_b64_tr_b16 a[100:101], v203 offset:24576\n\t" \
    "ds_read_b64_tr_b16 a[102:103], v203 offset:25600\n\t" \
    "s_waitcnt vmcnt(12) lgkmcnt(0)\n\t" \
    "s_barrier\n\t" \
    "s_add_u32 m0, s82, 16384\n\t" \
    "s_nop 0\n\t" \
    "global_load_lds_dwordx4 v200, s[42:43]\n\t" \
    "v_add_u32 v200, 131072, v200\n\t" \
    "s_add_u32 m0, s82, 20480\n\t" \
    "s_nop 0\n\t" \
    "global_load_lds_dwordx4 v200, s[42:43]\n\t" \
    "v_add_u32 v200, 131072, v200\n\t" \
    "s_add_u32 m0, s82, 24576\n\t" \
    "s_nop 0\n\t" \
    "global_load_lds_dwordx4 v200, s[42:43]\n\t" \
    "v_add_u32 v200, 131072, v200\n\t" \
    "s_add_u32 m0, s82, 28672\n\t" \
    "s_nop 0\n\t" \
    "global_load_lds_dwordx4 v200, s[42:43]\n\t" \
    "v_add_u32 v200, 131072, v200\n\t" \
    "s_add_u32 m0, s89, 0\n\t" \
    "s_nop 0\n\t" \
    "global_load_lds_dwordx4 v1, s[50:51] offset:0\n\t" \
    "s_add_u32 m0, s89, 960\n\t" \
    "s_nop 0\n\t" \
    "global_load_lds_dwordx4 v1, s[50:51] offset:64\n\t" \
    "s_add_u32 m0, s89, 1920\n\t" \
    "s_nop 0\n\t" \
    "global_load_lds_dwordx4 v1, s[50:51] offset:128\n\t" \
    "s_add_u32 m0, s89, 2880\n\t" \
    "s_nop 0\n\t" \
    "global_load_lds_dwordx4 v1, s[50:51] offset:192\n\t" \
    "s_add_u32 m0, s89, 3840\n\t" \
    "s_nop 0\n\t" \
    "global_load_lds_dwordx4 v1, s[50:51] offset:256\n\t" \
    "s_add_u32 m0, s89, 4800\n\t" \
    "s_nop 0\n\t" \
    "global_load_lds_dwordx4 v1, s[50:51] offset:320\n\t" \
    "s_add_u32 m0, s89, 5760\n\t" \
    "s_nop 0\n\t" \
    "global_load_lds_dwordx4 v1, s[50:51] offset:384\n\t" \
    "s_add_u32 m0, s89, 6720\n\t" \
    "s_nop 0\n\t" \
    "global_load_lds_dwordx4 v1, s[50:51] offset:448\n\t" \
    "s_add_u32 m0, s89, 7680\n\t" \
    "s_nop 0\n\t" \
    "global_load_lds_dwordx4 v1, s[50:51] offset:512\n\t" \
    "ds_read_b64_tr_b16 a[32:33], v202 offset:32768\n\t" \
    "ds_read_b64_tr_b16 a[34:35], v202 offset:33792\n\t" \
    "ds_read_b64_tr_b16 a[36:37], v202 offset:40960\n\t" \
    "ds_read_b64_tr_b16 a[38:39], v202 offset:41984\n\t" \
    "ds_read_b64_tr_b16 a[104:105], v203 offset:32768\n\t" \
    "ds_read_b64_tr_b16 a[106:107], v203 offset:33792\n\t" \
    "ds_read_b64_tr_b16 a[108:109], v203 offset:40960\n\t" \
    "ds_read_b64_tr_b16 a[110:111], v203 offset:41984\n\t" \
    "s_waitcnt vmcnt(21) lgkmcnt(0)\n\t" \
    "s_barrier\n\t" \
    "s_add_u32 m0, s96, 73728\n\t" \
    "s_nop 0\n\t" \
    "global_load_lds_dwordx4 v201, s[44:45] offset:0\n\t" \
    "s_add_u32 m0, s96, 74624\n\t" \
    "s_nop 0\n\t" \
    "global_load_lds_dwordx4 v201, s[44:45] offset:128\n\t" \
    "s_add_u32 m0, s96, 75520\n\t" \
    "s_nop 0\n\t" \
    "global_load_lds_dwordx4 v201, s[44:45] offset:256\n\t" \
    "s_add_u32 m0, s96, 76416\n\t" \
    "s_nop 0\n\t" \
    "global_load_lds_dwordx4 v201, s[44:45] offset:384\n\t" \
    "s_add_u32 m0, s96, 77312\n\t" \
    "s_nop 0\n\t" \
    "global_load_lds_dwordx4 v201, s[44:45] offset:512\n\t" \
    "s_add_u32 m0, s96, 78208\n\t" \
    "s_nop 0\n\t" \
    "global_load_lds_dwordx4 v201, s[44:45] offset:640\n\t" \
    "s_add_u32 m0, s96, 79104\n\t" \
    "s_nop 0\n\t" \
    "global_load_lds_dwordx4 v201, s[44:45] offset:768\n\t" \
    "s_add_u32 m0, s96, 80000\n\t" \
    "s_nop 0\n\t" \
    "global_load_lds_dwordx4 v201, s[44:45] offset:896\n\t" \
    "s_add_u32 m0, s96, 80896\n\t" \
    "s_nop 0\n\t" \
    "global_load_lds_dwordx4 v201, s[44:45] offset:1024\n\t" \
    "v_add_u32 v201, 36864, v201\n\t" \
    "ds_read_b64_tr_b16 a[40:41], v202 offset:49152\n\t" \
    "ds_read_b64_tr_b16 a[42:43], v202 offset:50176\n\t" \
    "ds_read_b64_tr_b16 a[44:45], v202 offset:57344\n\t" \
    "ds_read_b64_tr_b16 a[46:47], v202 offset:58368\n\t" \
    "ds_read_b64_tr_b16 a[112:113], v203 offset:49152\n\t" \
    "ds_read_b64_tr_b16 a[114:115], v203 offset:50176\n\t" \
    "ds_read_b64_tr_b16 a[116:117], v203 offset:57344\n\t" \
    "ds_read_b64_tr_b16 a[118:119], v203 offset:58368\n\t" \
    "s_waitcnt vmcnt(26) lgkmcnt(0)\n\t" \
    "s_barrier\n\t" \
    "v_add_u32 v202, 65536, v202\n\t" \
    "v_add_u32 v203, 65536, v203\n\t" \
    "ds_read_b64_tr_b16 a[48:49], v202 offset:0\n\t" \
    "ds_read_b64_tr_b16 a[50:51], v202 offset:1024\n\t" \
    "ds_read_b64_tr_b16 a[52:53], v202 offset:8192\n\t" \
    "ds_read_b64_tr_b16 a[54:55], v202 offset:9216\n\t" \
    "ds_read_b64_tr_b16 a[120:121], v203 offset:0\n\t" \
    "ds_read_b64_tr_b16 a[122:123], v203 offset:1024\n\t" \
    "ds_read_b64_tr_b16 a[124:125], v203 offset:8192\n\t" \
    "ds_read_b64_tr_b16 a[126:127], v203 offset:9216\n\t" \
    "s_waitcnt vmcnt(22) lgkmcnt(0)\n\t" \
    "s_barrier\n\t" \
    "s_add_u32 m0, s96, 110592\n\t" \
    "s_nop 0\n\t" \
    "global_load_lds_dwordx4 v201, s[44:45] offset:0\n\t" \
    "s_add_u32 m0, s96, 111488\n\t" \
    "s_nop 0\n\t" \
    "global_load_lds_dwordx4 v201, s[44:45] offset:128\n\t" \
    "s_add_u32 m0, s96, 112384\n\t" \
    "s_nop 0\n\t" \
    "global_load_lds_dwordx4 v201, s[44:45] offset:256\n\t" \
    "s_add_u32 m0, s96, 113280\n\t" \
    "s_nop 0\n\t" \
    "global_load_lds_dwordx4 v201, s[44:45] offset:384\n\t" \
    "s_add_u32 m0, s96, 114176\n\t" \
    "s_nop 0\n\t" \
    "global_load_lds_dwordx4 v201, s[44:45] offset:512\n\t" \
    "s_add_u32 m0, s96, 115072\n\t" \
    "s_nop 0\n\t" \
    "global_load_lds_dwordx4 v201, s[44:45] offset:640\n\t" \
    "s_add_u32 m0, s96, 115968\n\t" \
    "s_nop 0\n\t" \
    "global_load_lds_dwordx4 v201, s[44:45] offset:768\n\t" \
    "s_add_u32 m0, s96, 116864\n\t" \
    "s_nop 0\n\t" \
    "global_load_lds_dwordx4 v201, s[44:45] offset:896\n\t" \
    "s_add_u32 m0, s96, 117760\n\t" \
    "s_nop 0\n\t" \
    "global_load_lds_dwordx4 v201, s[44:45] offset:1024\n\t" \
    "v_add_u32 v201, 36864, v201\n\t" \
    "v_mbcnt_lo_u32_b32 v164, -1, 0\n\t" \
    "v_mbcnt_hi_u32_b32 v164, -1, v164\n\t" \
    "s_cmp_eq_u32 s66, 0\n\t" \
    "s_cselect_b32 s69, 59, 11\n\t" \
    "v_min_u32 v164, s69, v164\n\t" \
    "v_lshlrev_b32 v164, 6, v164\n\t" \
    "v_add_u32 v164, 6225920, v164\n\t" \
    "v_mov_b32 v165, 6234112\n\t" \
    "global_load_dword v165, v165, s[36:37] sc1\n\t" \
    "global_load_dwordx2 v[166:167], v164, s[36:37] sc1\n\t" \
    "v_add_u32 v202, -98304, v202\n\t" \
    "v_add_u32 v203, -98304, v203\n\t" \
    "ds_read_b64_tr_b16 a[56:57], v202 offset:0\n\t" \
    "ds_read_b64_tr_b16 a[58:59], v202 offset:1024\n\t" \
    "ds_read_b64_tr_b16 a[60:61], v202 offset:8192\n\t" \
    "ds_read_b64_tr_b16 a[62:63], v202 offset:9216\n\t" \
    "ds_read_b64_tr_b16 a[128:129], v203 offset:0\n\t" \
    "ds_read_b64_tr_b16 a[130:131], v203 offset:1024\n\t" \
    "ds_read_b64_tr_b16 a[132:133], v203 offset:8192\n\t" \
    "ds_read_b64_tr_b16 a[134:135], v203 offset:9216\n\t" \
    "s_waitcnt vmcnt(29) lgkmcnt(0)\n\t" \
    "s_barrier\n\t" \
    "ds_read_b64_tr_b16 a[64:65], v202 offset:16384\n\t" \
    "ds_read_b64_tr_b16 a[66:67], v202 offset:17408\n\t" \
    "ds_read_b64_tr_b16 a[68:69], v202 offset:24576\n\t" \
    "ds_read_b64_tr_b16 a[70:71], v202 offset:25600\n\t" \
    "ds_read_b64_tr_b16 a[136:137], v203 offset:16384\n\t" \
    "ds_read_b64_tr_b16 a[138:139], v203 offset:17408\n\t" \
    "ds_read_b64_tr_b16 a[140:141], v203 offset:24576\n\t" \
    "ds_read_b64_tr_b16 a[142:143], v203 offset:25600\n\t" \
    "s_waitcnt vmcnt(11) lgkmcnt(0)\n\t" \
    "s_barrier\n\t" \
    "s_add_u32 m0, s96, 36864\n\t" \
    "s_nop 0\n\t" \
    "global_load_lds_dwordx4 v201, s[44:45] offset:0\n\t" \
    "s_add_u32 m0, s96, 37760\n\t" \
    "s_nop 0\n\t" \
    "global_load_lds_dwordx4 v201, s[44:45] offset:128\n\t" \
    "s_add_u32 m0, s96, 38656\n\t" \
    "s_nop 0\n\t" \
    "global_load_lds_dwordx4 v201, s[44:45] offset:256\n\t" \
    "s_add_u32 m0, s96, 39552\n\t" \
    "s_nop 0\n\t" \
    "global_load_lds_dwordx4 v201, s[44:45] offset:384\n\t" \
    "s_add_u32 m0, s96, 40448\n\t" \
    "s_nop 0\n\t" \
    "global_load_lds_dwordx4 v201, s[44:45] offset:512\n\t" \
    "s_add_u32 m0, s96, 41344\n\t" \
    "s_nop 0\n\t" \
    "global_load_lds_dwordx4 v201, s[44:45] offset:640\n\t" \
    "s_add_u32 m0, s96, 42240\n\t" \
    "s_nop 0\n\t" \
    "global_load_lds_dwordx4 v201, s[44:45] offset:768\n\t" \
    "s_add_u32 m0, s96, 43136\n\t" \
    "s_nop 0\n\t" \
    "global_load_lds_dwordx4 v201, s[44:45] offset:896\n\t" \
    "s_add_u32 m0, s96, 44032\n\t" \
    "s_nop 0\n\t" \
    "global_load_lds_dwordx4 v201, s[44:45] offset:1024\n\t" \
    "v_add_u32 v201, 36864, v201\n\t" \
    "v_add_u32 v204, 73728, v204\n\t" \
    "v_add_u32 v205, 73728, v205\n\t" \
    "v_add_u32 v206, 73728, v206\n\t" \
    "v_add_u32 v207, 73728, v207\n\t" \
    "ds_read_b64_tr_b16 a[144:145], v204 offset:0\n\t" \
    "ds_read_b64_tr_b16 a[146:147], v204 offset:18432\n\t" \
    "ds_read_b64_tr_b16 a[160:161], v205 offset:0\n\t" \
    "ds_read_b64_tr_b16 a[162:163], v205 offset:18432\n\t" \
    "ds_read_b64_tr_b16 a[176:177], v206 offset:0\n\t" \
    "ds_read_b64_tr_b16 a[178:179], v206 offset:18432\n\t" \
    "s_waitcnt lgkmcnt(6)\n\t" \
    "ds_read_b64_tr_b16 a[192:193], v207 offset:0\n\t" \
    "ds_read_b64_tr_b16 a[194:195], v207 offset:18432\n\t" \
    "ds_read_b64_tr_b16 a[208:209], v204 offset:1024\n\t" \
    "ds_read_b64_tr_b16 a[210:211], v204 offset:19456\n\t" \
    "ds_read_b64_tr_b16 a[224:225], v205 offset:1024\n\t" \
    "ds_read_b64_tr_b16 a[226:227], v205 offset:19456\n\t" \
    "s_waitcnt lgkmcnt(6)\n\t" \
    "ds_read_b64_tr_b16 a[240:241], v206 offset:1024\n\t" \
    "ds_read_b64_tr_b16 a[242:243], v206 offset:19456\n\t" \
    "ds_read_b64_tr_b16 v[224:225], v207 offset:1024\n\t" \
    "ds_read_b64_tr_b16 v[226:227], v207 offset:19456\n\t" \
    "ds_read_b64_tr_b16 v[240:241], v204 offset:2048\n\t" \
    "ds_read_b64_tr_b16 v[242:243], v204 offset:20480\n\t" \
    "s_waitcnt lgkmcnt(6)\n\t" \
    "s_waitcnt lgkmcnt(6)\n\t" \
    "ds_read_b128 v[24:27], v2 offset:0\n\t" \
    "ds_read_b128 v[28:31], v2 offset:18432\n\t" \
    "ds_read_b128 v[32:35], v2 offset:1024\n\t" \
    "ds_read_b128 v[36:39], v2 offset:19456\n\t" \
    "s_waitcnt lgkmcnt(3)\n\t" \
    "v_mfma_f32_16x16x32_bf16 v[72:75], a[0:3], v[24:27], 0\n\t" \
    "ds_read_b128 v[40:43], v2 offset:2048\n\t" \
    "s_waitcnt lgkmcnt(3)\n\t" \
    "v_mfma_f32_16x16x32_bf16 v[76:79], a[0:3], v[28:31], 0\n\t" \
    "ds_read_b128 v[44:47], v2 offset:20480\n\t" \
    "v_mfma_f32_16x16x32_bf16 v[80:83], a[72:75], v[24:27], 0\n\t" \
    "v_mfma_f32_16x16x32_bf16 v[84:87], a[72:75], v[28:31], 0\n\t" \
    "s_waitcnt lgkmcnt(3)\n\t" \
    "v_mfma_f32_16x16x32_bf16 v[72:75], a[4:7], v[32:35], v[72:75]\n\t" \
    "ds_read_b128 v[48:51], v2 offset:3072\n\t" \
    "s_waitcnt lgkmcnt(3)\n\t" \
    "v_mfma_f32_16x16x32_bf16 v[76:79], a[4:7], v[36:39], v[76:79]\n\t" \
    "ds_read_b128 v[52:55], v2 offset:21504\n\t" \
    "v_mfma_f32_16x16x32_bf16 v[80:83], a[76:79], v[32:35], v[80:83]\n\t" \
    "v_mfma_f32_16x16x32_bf16 v[84:87], a[76:79], v[36:39], v[84:87]\n\t" \
    "s_waitcnt lgkmcnt(3)\n\t" \
    "v_mfma_f32_16x16x32_bf16 v[72:75], a[8:11], v[40:43], v[72:75]\n\t" \
    "ds_read_b128 v[56:59], v2 offset:4096\n\t" \
    "s_waitcnt lgkmcnt(3)\n\t" \
    "v_mfma_f32_16x16x32_bf16 v[76:79], a[8:11], v[44:47], v[76:79]\n\t" \
    "ds_read_b128 v[60:63], v2 offset:22528\n\t" \
    "v_mfma_f32_16x16x32_bf16 v[80:83], a[80:83], v[40:43], v[80:83]\n\t" \
    "v_mfma_f32_16x16x32_bf16 v[84:87], a[80:83], v[44:47], v[84:87]\n\t" \
    "s_waitcnt lgkmcnt(3)\n\t" \
    "v_mfma_f32_16x16x32_bf16 v[72:75], a[12:15], v[48:51], v[72:75]\n\t" \
    "ds_read_b128 v[64:67], v2 offset:5120\n\t" \
    "s_waitcnt lgkmcnt(3)\n\t" \
    "v_mfma_f32_16x16x32_bf16 v[76:79], a[12:15], v[52:55], v[76:79]\n\t" \
    "ds_read_b128 v[68:71], v2 offset:23552\n\t" \
    "v_mfma_f32_16x16x32_bf16 v[80:83], a[84:87], v[48:51], v[80:83]\n\t" \
    "v_mfma_f32_16x16x32_bf16 v[84:87], a[84:87], v[52:55], v[84:87]\n\t" \
    "s_waitcnt lgkmcnt(3)\n\t" \
    "v_mfma_f32_16x16x32_bf16 v[72:75], a[16:19], v[56:59], v[72:75]\n\t" \
    "s_waitcnt lgkmcnt(2)\n\t" \
    "v_mfma_f32_16x16x32_bf16 v[76:79], a[16:19], v[60:63], v[76:79]\n\t" \
    "v_mfma_f32_16x16x32_bf16 v[80:83], a[88:91], v[56:59], v[80:83]\n\t" \
    "v_mfma_f32_16x16x32_bf16 v[84:87], a[88:91], v[60:63], v[84:87]\n\t" \
    "s_waitcnt lgkmcnt(1)\n\t" \
    "v_mfma_f32_16x16x32_bf16 v[72:75], a[20:23], v[64:67], v[72:75]\n\t" \
    "s_waitcnt lgkmcnt(0)\n\t" \
    "v_mfma_f32_16x16x32_bf16 v[76:79], a[20:23], v[68:71], v[76:79]\n\t" \
    "v_mfma_f32_16x16x32_bf16 v[80:83], a[92:95], v[64:67], v[80:83]\n\t" \
    "v_mfma_f32_16x16x32_bf16 v[84:87], a[92:95], v[68:71], v[84:87]\n\t" \
    "s_waitcnt vmcnt(11) lgkmcnt(0)\n\t" \
    "s_barrier\n\t" \
    "s_add_u32 m0, s96, 73728\n\t" \
    "s_nop 0\n\t" \
    "global_load_lds_dwordx4 v201, s[44:45] offset:0\n\t" \
    "s_add_u32 m0, s96, 74624\n\t" \
    "s_nop 0\n\t" \
    "global_load_lds_dwordx4 v201, s[44:45] offset:128\n\t" \
    "s_add_u32 m0, s96, 75520\n\t" \
    "s_nop 0\n\t" \
    "global_load_lds_dwordx4 v201, s[44:45] offset:256\n\t" \
    "s_add_u32 m0, s96, 76416\n\t" \
    "s_nop 0\n\t" \
    "global_load_lds_dwordx4 v201, s[44:45] offset:384\n\t" \
    "s_add_u32 m0, s96, 77312\n\t" \
    "s_nop 0\n\t" \
    "global_load_lds_dwordx4 v201, s[44:45] offset:512\n\t" \
    "s_add_u32 m0, s96, 78208\n\t" \
    "s_nop 0\n\t" \
    "global_load_lds_dwordx4 v201, s[44:45] offset:640\n\t" \
    "s_add_u32 m0, s96, 79104\n\t" \
    "s_nop 0\n\t" \
    "global_load_lds_dwordx4 v201, s[44:45] offset:768\n\t" \
    "s_add_u32 m0, s96, 80000\n\t" \
    "s_nop 0\n\t" \
    "global_load_lds_dwordx4 v201, s[44:45] offset:896\n\t" \
    "s_add_u32 m0, s96, 80896\n\t" \
    "s_nop 0\n\t" \
    "global_load_lds_dwordx4 v201, s[44:45] offset:1024\n\t" \
    "v_add_u32 v201, 36864, v201\n\t" \
    "ds_read_b64_tr_b16 a[148:149], v204 offset:36864\n\t" \
    "ds_read_b64_tr_b16 a[150:151], v204 offset:55296\n\t" \
    "ds_read_b64_tr_b16 a[164:165], v205 offset:36864\n\t" \
    "ds_read_b64_tr_b16 a[166:167], v205 offset:55296\n\t" \
    "ds_read_b64_tr_b16 a[180:181], v206 offset:36864\n\t" \
    "ds_read_b64_tr_b16 a[182:183], v206 offset:55296\n\t" \
    "s_waitcnt lgkmcnt(6)\n\t" \
    "ds_read_b64_tr_b16 a[196:197], v207 offset:36864\n\t" \
    "ds_read_b64_tr_b16 a[198:199], v207 offset:55296\n\t" \
    "ds_read_b64_tr_b16 a[212:213], v204 offset:37888\n\t" \
    "ds_read_b64_tr_b16 a[214:215], v204 offset:56320\n\t" \
    "ds_read_b64_tr_b16 a[228:229], v205 offset:37888\n\t" \
    "ds_read_b64_tr_b16 a[230:231], v205 offset:56320\n\t" \
    "s_waitcnt lgkmcnt(6)\n\t" \
    "ds_read_b64_tr_b16 a[244:245], v206 offset:37888\n\t" \
    "ds_read_b64_tr_b16 a[246:247], v206 offset:56320\n\t" \
    "ds_read_b64_tr_b16 v[228:229], v207 offset:37888\n\t" \
    "ds_read_b64_tr_b16 v[230:231], v207 offset:56320\n\t" \
    "ds_read_b64_tr_b16 v[244:245], v204 offset:38912\n\t" \
    "ds_read_b64_tr_b16 v[246:247], v204 offset:57344\n\t" \
    "s_waitcnt lgkmcnt(6)\n\t" \
    "s_waitcnt lgkmcnt(6)\n\t" \
    "ds_read_b128 v[24:27], v2 offset:6144\n\t" \
    "ds_read_b128 v[28:31], v2 offset:24576\n\t" \
    "ds_read_b128 v[32:35], v2 offset:7168\n\t" \
    "ds_read_b128 v[36:39], v2 offset:25600\n\t" \
    "s_waitcnt lgkmcnt(3)\n\t" \
    "v_mfma_f32_16x16x32_bf16 v[72:75], a[24:27], v[24:27], v[72:75]\n\t" \
    "ds_read_b128 v[40:43], v2 offset:8192\n\t" \
    "s_waitcnt lgkmcnt(3)\n\t" \
    "v_mfma_f32_16x16x32_bf16 v[76:79], a[24:27], v[28:31], v[76:79]\n\t" \
    "ds_read_b128 v[44:47], v2 offset:26624\n\t" \
    "v_mfma_f32_16x16x32_bf16 v[80:83], a[96:99], v[24:27], v[80:83]\n\t" \
    "v_mfma_f32_16x16x32_bf16 v[84:87], a[96:99], v[28:31], v[84:87]\n\t" \
    "s_waitcnt lgkmcnt(3)\n\t" \
    "v_mfma_f32_16x16x32_bf16 v[72:75], a[28:31], v[32:35], v[72:75]\n\t" \
    "ds_read_b128 v[48:51], v2 offset:9216\n\t" \
    "s_waitcnt lgkmcnt(3)\n\t" \
    "v_mfma_f32_16x16x32_bf16 v[76:79], a[28:31], v[36:39], v[76:79]\n\t" \
    "ds_read_b128 v[52:55], v2 offset:27648\n\t" \
    "v_mfma_f32_16x16x32_bf16 v[80:83], a[100:103], v[32:35], v[80:83]\n\t" \
    "v_mfma_f32_16x16x32_bf16 v[84:87], a[100:103], v[36:39], v[84:87]\n\t" \
    "s_waitcnt lgkmcnt(3)\n\t" \
    "v_mfma_f32_16x16x32_bf16 v[72:75], a[32:35], v[40:43], v[72:75]\n\t" \
    "ds_read_b128 v[56:59], v2 offset:10240\n\t" \
    "s_waitcnt lgkmcnt(3)\n\t" \
    "v_mfma_f32_16x16x32_bf16 v[76:79], a[32:35], v[44:47], v[76:79]\n\t" \
    "ds_read_b128 v[60:63], v2 offset:28672\n\t" \
    "v_mfma_f32_16x16x32_bf16 v[80:83], a[104:107], v[40:43], v[80:83]\n\t" \
    "v_mfma_f32_16x16x32_bf16 v[84:87], a[104:107], v[44:47], v[84:87]\n\t" \
    "s_waitcnt lgkmcnt(3)\n\t" \
    "v_mfma_f32_16x16x32_bf16 v[72:75], a[36:39], v[48:51], v[72:75]\n\t" \
    "ds_read_b128 v[64:67], v2 offset:11264\n\t" \
    "s_waitcnt lgkmcnt(3)\n\t" \
    "v_mfma_f32_16x16x32_bf16 v[76:79], a[36:39], v[52:55], v[76:79]\n\t" \
    "ds_read_b128 v[68:71], v2 offset:29696\n\t" \
    "v_mfma_f32_16x16x32_bf16 v[80:83], a[108:111], v[48:51], v[80:83]\n\t" \
    "v_mfma_f32_16x16x32_bf16 v[84:87], a[108:111], v[52:55], v[84:87]\n\t" \
    "s_waitcnt lgkmcnt(3)\n\t" \
    "v_mfma_f32_16x16x32_bf16 v[72:75], a[40:43], v[56:59], v[72:75]\n\t" \
    "s_waitcnt lgkmcnt(2)\n\t" \
    "v_mfma_f32_16x16x32_bf16 v[76:79], a[40:43], v[60:63], v[76:79]\n\t" \
    "v_mfma_f32_16x16x32_bf16 v[80:83], a[112:115], v[56:59], v[80:83]\n\t" \
    "v_mfma_f32_16x16x32_bf16 v[84:87], a[112:115], v[60:63], v[84:87]\n\t" \
    "s_waitcnt lgkmcnt(1)\n\t" \
    "v_mfma_f32_16x16x32_bf16 v[72:75], a[44:47], v[64:67], v[72:75]\n\t" \
    "s_waitcnt lgkmcnt(0)\n\t" \
    "v_mfma_f32_16x16x32_bf16 v[76:79], a[44:47], v[68:71], v[76:79]\n\t" \
    "v_mfma_f32_16x16x32_bf16 v[80:83], a[116:119], v[64:67], v[80:83]\n\t" \
    "v_mfma_f32_16x16x32_bf16 v[84:87], a[116:119], v[68:71], v[84:87]\n\t" \
    "s_waitcnt vmcnt(9) lgkmcnt(0)\n\t" \
    "s_barrier\n\t" \
    "s_add_u32 s50, s50, 73728\n\t" \
    "s_addc_u32 s51, s51, 0\n\t" \
    "s_add_u32 m0, s89, 110592\n\t" \
    "s_nop 0\n\t" \
    "global_load_lds_dwordx4 v1, s[50:51] offset:0\n\t" \
    "s_add_u32 m0, s89, 111552\n\t" \
    "s_nop 0\n\t" \
    "global_load_lds_dwordx4 v1, s[50:51] offset:64\n\t" \
    "s_add_u32 m0, s89, 112512\n\t" \
    "s_nop 0\n\t" \
    "global_load_lds_dwordx4 v1, s[50:51] offset:128\n\t" \
    "s_add_u32 m0, s89, 113472\n\t" \
    "s_nop 0\n\t" \
    "global_load_lds_dwordx4 v1, s[50:51] offset:192\n\t" \
    "s_add_u32 m0, s89, 114432\n\t" \
    "s_nop 0\n\t" \
    "global_load_lds_dwordx4 v1, s[50:51] offset:256\n\t" \
    "s_add_u32 m0, s89, 115392\n\t" \
    "s_nop 0\n\t" \
    "global_load_lds_dwordx4 v1, s[50:51] offset:320\n\t" \
    "s_add_u32 m0, s89, 116352\n\t" \
    "s_nop 0\n\t" \
    "global_load_lds_dwordx4 v1, s[50:51] offset:384\n\t" \
    "s_add_u32 m0, s89, 117312\n\t" \
    "s_nop 0\n\t" \
    "global_load_lds_dwordx4 v1, s[50:51] offset:448\n\t" \
    "s_add_u32 m0, s89, 118272\n\t" \
    "s_nop 0\n\t" \
    "global_load_lds_dwordx4 v1, s[50:51] offset:512\n\t" \
    "v_add_u32 v204, -36864, v204\n\t" \
    "v_add_u32 v205, -36864, v205\n\t" \
    "v_add_u32 v206, -36864, v206\n\t" \
    "v_add_u32 v207, -36864, v207\n\t" \
    "ds_read_b64_tr_b16 a[152:153], v204 offset:0\n\t" \
    "ds_read_b64_tr_b16 a[154:155], v204 offset:18432\n\t" \
    "ds_read_b64_tr_b16 a[168:169], v205 offset:0\n\t" \
    "ds_read_b64_tr_b16 a[170:171], v205 offset:18432\n\t" \
    "ds_read_b64_tr_b16 a[184:185], v206 offset:0\n\t" \
    "ds_read_b64_tr_b16 a[186:187], v206 offset:18432\n\t" \
    "s_waitcnt lgkmcnt(6)\n\t" \
    "ds_read_b64_tr_b16 a[200:201], v207 offset:0\n\t" \
    "ds_read_b64_tr_b16 a[202:203], v207 offset:18432\n\t" \
    "ds_read_b64_tr_b16 a[216:217], v204 offset:1024\n\t" \
    "ds_read_b64_tr_b16 a[218:219], v204 offset:19456\n\t" \
    "ds_read_b64_tr_b16 a[232:233], v205 offset:1024\n\t" \
    "ds_read_b64_tr_b16 a[234:235], v205 offset:19456\n\t" \
    "s_waitcnt lgkmcnt(6)\n\t" \
    "ds_read_b64_tr_b16 a[248:249], v206 offset:1024\n\t" \
    "ds_read_b64_tr_b16 a[250:251], v206 offset:19456\n\t" \
    "ds_read_b64_tr_b16 v[232:233], v207 offset:1024\n\t" \
    "ds_read_b64_tr_b16 v[234:235], v207 offset:19456\n\t" \
    "ds_read_b64_tr_b16 v[248:249], v204 offset:2048\n\t" \
    "ds_read_b64_tr_b16 v[250:251], v204 offset:20480\n\t" \
    "s_waitcnt lgkmcnt(6)\n\t" \
    "s_waitcnt lgkmcnt(6)\n\t" \
    "ds_read_b128 v[24:27], v2 offset:12288\n\t" \
    "ds_read_b128 v[28:31], v2 offset:30720\n\t" \
    "ds_read_b128 v[32:35], v2 offset:13312\n\t" \
    "ds_read_b128 v[36:39], v2 offset:31744\n\t" \
    "s_waitcnt lgkmcnt(3)\n\t" \
    "v_mfma_f32_16x16x32_bf16 v[72:75], a[48:51], v[24:27], v[72:75]\n\t" \
    "ds_read_b128 v[40:43], v2 offset:14336\n\t" \
    "s_waitcnt lgkmcnt(3)\n\t" \
    "v_mfma_f32_16x16x32_bf16 v[76:79], a[48:51], v[28:31], v[76:79]\n\t" \
    "ds_read_b128 v[44:47], v2 offset:32768\n\t" \
    "v_mfma_f32_16x16x32_bf16 v[80:83], a[120:123], v[24:27], v[80:83]\n\t" \
    "v_mfma_f32_16x16x32_bf16 v[84:87], a[120:123], v[28:31], v[84:87]\n\t" \
    "s_waitcnt lgkmcnt(3)\n\t" \
    "v_mfma_f32_16x16x32_bf16 v[72:75], a[52:55], v[32:35], v[72:75]\n\t" \
    "ds_read_b128 v[48:51], v2 offset:15360\n\t" \
    "s_waitcnt lgkmcnt(3)\n\t" \
    "v_mfma_f32_16x16x32_bf16 v[76:79], a[52:55], v[36:39], v[76:79]\n\t" \
    "ds_read_b128 v[52:55], v2 offset:33792\n\t" \
    "v_mfma_f32_16x16x32_bf16 v[80:83], a[124:127], v[32:35], v[80:83]\n\t" \
    "v_mfma_f32_16x16x32_bf16 v[84:87], a[124:127], v[36:39], v[84:87]\n\t" \
    "s_waitcnt lgkmcnt(3)\n\t" \
    "v_mfma_f32_16x16x32_bf16 v[72:75], a[56:59], v[40:43], v[72:75]\n\t" \
    "ds_read_b128 v[56:59], v2 offset:16384\n\t" \
    "s_waitcnt lgkmcnt(3)\n\t" \
    "v_mfma_f32_16x16x32_bf16 v[76:79], a[56:59], v[44:47], v[76:79]\n\t" \
    "ds_read_b128 v[60:63], v2 offset:34816\n\t" \
    "v_mfma_f32_16x16x32_bf16 v[80:83], a[128:131], v[40:43], v[80:83]\n\t" \
    "v_mfma_f32_16x16x32_bf16 v[84:87], a[128:131], v[44:47], v[84:87]\n\t" \
    "s_waitcnt lgkmcnt(3)\n\t" \
    "v_mfma_f32_16x16x32_bf16 v[72:75], a[60:63], v[48:51], v[72:75]\n\t" \
    "ds_read_b128 v[64:67], v2 offset:17408\n\t" \
    "s_waitcnt lgkmcnt(3)\n\t" \
    "v_mfma_f32_16x16x32_bf16 v[76:79], a[60:63], v[52:55], v[76:79]\n\t" \
    "ds_read_b128 v[68:71], v2 offset:35840\n\t" \
    "v_mfma_f32_16x16x32_bf16 v[80:83], a[132:135], v[48:51], v[80:83]\n\t" \
    "v_mfma_f32_16x16x32_bf16 v[84:87], a[132:135], v[52:55], v[84:87]\n\t" \
    "s_waitcnt lgkmcnt(3)\n\t" \
    "v_mfma_f32_16x16x32_bf16 v[72:75], a[64:67], v[56:59], v[72:75]\n\t" \
    "s_waitcnt lgkmcnt(2)\n\t" \
    "v_mfma_f32_16x16x32_bf16 v[76:79], a[64:67], v[60:63], v[76:79]\n\t" \
    "v_mfma_f32_16x16x32_bf16 v[80:83], a[136:139], v[56:59], v[80:83]\n\t" \
    "v_mfma_f32_16x16x32_bf16 v[84:87], a[136:139], v[60:63], v[84:87]\n\t" \
    "s_waitcnt lgkmcnt(1)\n\t" \
    "v_mfma_f32_16x16x32_bf16 v[72:75], a[68:71], v[64:67], v[72:75]\n\t" \
    "s_waitcnt lgkmcnt(0)\n\t" \
    "v_mfma_f32_16x16x32_bf16 v[76:79], a[68:71], v[68:71], v[76:79]\n\t" \
    "v_mfma_f32_16x16x32_bf16 v[80:83], a[140:143], v[64:67], v[80:83]\n\t" \
    "v_mfma_f32_16x16x32_bf16 v[84:87], a[140:143], v[68:71], v[84:87]\n\t" \
    "s_waitcnt vmcnt(9) lgkmcnt(0)\n\t" \
    "s_barrier\n\t" \
    "ds_read_b64_tr_b16 a[156:157], v204 offset:36864\n\t" \
    "ds_read_b64_tr_b16 a[158:159], v204 offset:55296\n\t" \
    "ds_read_b64_tr_b16 a[172:173], v205 offset:36864\n\t" \
    "ds_read_b64_tr_b16 a[174:175], v205 offset:55296\n\t" \
    "ds_read_b64_tr_b16 a[188:189], v206 offset:36864\n\t" \
    "ds_read_b64_tr_b16 a[190:191], v206 offset:55296\n\t" \
    "s_waitcnt lgkmcnt(6)\n\t" \
    "ds_read_b64_tr_b16 a[204:205], v207 offset:36864\n\t" \
    "ds_read_b64_tr_b16 a[206:207], v207 offset:55296\n\t" \
    "ds_read_b64_tr_b16 a[220:221], v204 offset:37888\n\t" \
    "ds_read_b64_tr_b16 a[222:223], v204 offset:56320\n\t" \
    "ds_read_b64_tr_b16 a[236:237], v205 offset:37888\n\t" \
    "ds_read_b64_tr_b16 a[238:239], v205 offset:56320\n\t" \
    "s_waitcnt lgkmcnt(6)\n\t" \
    "ds_read_b64_tr_b16 a[252:253], v206 offset:37888\n\t" \
    "ds_read_b64_tr_b16 a[254:255], v206 offset:56320\n\t" \
    "ds_read_b64_tr_b16 v[236:237], v207 offset:37888\n\t" \
    "ds_read_b64_tr_b16 v[238:239], v207 offset:56320\n\t" \
    "ds_read_b64_tr_b16 v[252:253], v204 offset:38912\n\t" \
    "ds_read_b64_tr_b16 v[254:255], v204 offset:57344\n\t" \
    "s_waitcnt lgkmcnt(6)\n\t" \
    "s_waitcnt lgkmcnt(0)\n\t" \
    "s_barrier\n\t" \
    "v_sub_u32 v166, v166, v165\n\t" \
    "v_cmp_ne_u32 vcc, 1, v166\n\t" \
    "s_cbranch_vccz gv_Cok2_%=\n\t" \
    "gv_C1_%=:\n\t" \
    "global_load_dwordx2 v[166:167], v164, s[36:37] sc1\n\t" \
    "s_waitcnt vmcnt(0)\n\t" \
    "v_sub_u32 v166, v166, v165\n\t" \
    "v_cmp_ne_u32 vcc, 1, v166\n\t" \
    "s_cbranch_vccz gv_Cok2_%=\n\t" \
    "s_cmp_eq_u32 s68, 0\n\t" \
    "s_cbranch_scc1 gv_F3_%=\n\t" \
    "s_sub_u32 s68, s68, 1\n\t" \
    "s_sleep 4\n\t" \
    "s_branch gv_C1_%=\n\t" \
    "gv_F3_%=:\n\t" \
    "v_mov_b32 v168, 6095104\n\t" \
    "v_mov_b32 v169, 4\n\t" \
    "global_store_dword v168, v169, s[36:37] sc1\n\t" \
    "s_waitcnt vmcnt(0)\n\t" \
    "s_branch gv_Cok2_%=\n\t" \
    "gv_Cok2_%=:\n\t" \
    "s_cmp_lg_u32 s66, 0\n\t" \
    "s_cbranch_scc1 gv_Ca4_%=\n\t" \
    "v_mov_b32 v164, 6176768\n\t" \
    "s_mov_b64 exec, 1\n\t" \
    "global_atomic_add v164, v166, s[36:37] sc1\n\t" \
    "s_mov_b64 exec, -1\n\t" \
    "gv_Ca4_%=:\n\t" \
    "v_min_u32_dpp v167, v167, v167 row_shl:6 row_mask:0xf bank_mask:0xf\n\t" \
    "s_nop 0\n\t" \
    "v_readlane_b32 s69, v167, 4\n\t" \
    "v_readlane_b32 s70, v167, 5\n\t" \
    "s_nop 1\n\t" \
    "s_cmp_lt_u32 s70, s69\n\t" \
    "s_cselect_b32 s52, 8192, 0\n\t" \
    "s_xor_b32 s52, s52, 8192\n\t" \
    "s_lshr_b32 s69, s67, 5\n\t" \
    "s_or_b32 s69, s69, s66\n\t" \
    "s_cbranch_scc1 gv_npr5_%=\n\t" \
    "s_memrealtime s[54:55]\n\t" \
    "s_waitcnt lgkmcnt(0)\n\t" \
    "s_lshl_b32 s70, s54, 4\n\t" \
    "s_add_u32 s70, s70, 4160\n\t" \
    "v_mov_b32 v157, s70\n\t" \
    "global_store_dword v19, v157, s[36:37] sc1\n\t" \
    "gv_npr5_%=:\n\t" \
    "s_cmp_lt_u32 s65, 60\n\t" \
    "s_cselect_b32 s69, 73728, 0\n\t" \
    "s_add_u32 s50, s50, s69\n\t" \
    "s_addc_u32 s51, s51, 0\n\t" \
    "s_sub_u32 s90, s90, 36864\n\t" \
    "s_cselect_b32 s90, 110592, s90\n\t" \
    "s_sub_u32 s91, s90, 36864\n\t" \
    "s_cselect_b32 s91, 110592, s91\n\t" \
    "s_add_u32 s91, s91, s89\n\t" \
    "v_add_u32 v17, s90, v3\n\t" \
    "s_lshl_b32 s40, s65, 15\n\t" \
    "s_branch gv_mid8_%=\n\t" \
    ".p2align 6\n\t" \
    "s_nop 0\n\t" \
    "s_nop 0\n\t" \
    "gv_loop6_%=:\n\t" \
    "s_cmp_lt_u32 s65, 60\n\t" \
    "s_cselect_b32 s69, 73728, 0\n\t" \
    "s_add_u32 s50, s50, s69\n\t" \
    "s_addc_u32 s51, s51, 0\n\t" \
    "s_sub_u32 s90, s90, 36864\n\t" \
    "s_cselect_b32 s90, 110592, s90\n\t" \
    "s_sub_u32 s91, s90, 36864\n\t" \
    "s_cselect_b32 s91, 110592, s91\n\t" \
    "s_add_u32 s91, s91, s89\n\t" \
    "v_add_u32 v17, s90, v3\n\t" \
    "s_lshl_b32 s40, s65, 15\n\t" \
    "s_waitcnt lgkmcnt(5)\n\t" \
    "v_mfma_f32_16x16x32_bf16 v[72:75], a[0:3], v[24:27], 0\n\t" \
    "ds_read_b128 v[48:51], v2 offset:3072\n\t" \
    "s_waitcnt lgkmcnt(5)\n\t" \
    "v_mfma_f32_16x16x32_bf16 v[76:79], a[0:3], v[28:31], 0\n\t" \
    "ds_read_b128 v[52:55], v2 offset:21504\n\t" \
    "v_mfma_f32_16x16x32_bf16 v[80:83], a[72:75], v[24:27], 0\n\t" \
    "s_add_u32 m0, s91, 0\n\t" \
    "v_mfma_f32_16x16x32_bf16 v[84:87], a[72:75], v[28:31], 0\n\t" \
    "global_load_lds_dwordx4 v1, s[50:51] offset:0\n\t" \
    "s_waitcnt lgkmcnt(5)\n\t" \
    "v_mfma_f32_16x16x32_bf16 v[72:75], a[4:7], v[32:35], v[72:75]\n\t" \
    "ds_read_b128 v[56:59], v2 offset:4096\n\t" \
    "s_waitcnt lgkmcnt(5)\n\t" \
    "v_mfma_f32_16x16x32_bf16 v[76:79], a[4:7], v[36:39], v[76:79]\n\t" \
    "ds_read_b128 v[60:63], v2 offset:22528\n\t" \
    "v_mfma_f32_16x16x32_bf16 v[80:83], a[76:79], v[32:35], v[80:83]\n\t" \
    "s_add_u32 m0, s91, 960\n\t" \
    "v_mfma_f32_16x16x32_bf16 v[84:87], a[76:79], v[36:39], v[84:87]\n\t" \
    "global_load_lds_dwordx4 v1, s[50:51] offset:64\n\t" \
    "s_waitcnt lgkmcnt(5)\n\t" \
    "v_mfma_f32_16x16x32_bf16 v[72:75], a[8:11], v[40:43], v[72:75]\n\t" \
    "ds_read_b128 v[64:67], v2 offset:5120\n\t" \
    "s_waitcnt lgkmcnt(5)\n\t" \
    "v_mfma_f32_16x16x32_bf16 v[76:79], a[8:11], v[44:47], v[76:79]\n\t" \
    "ds_read_b128 v[68:71], v2 offset:23552\n\t" \
    "v_mfma_f32_16x16x32_bf16 v[80:83], a[80:83], v[40:43], v[80:83]\n\t" \
    "s_add_u32 m0, s91, 1920\n\t" \
    "v_mfma_f32_16x16x32_bf16 v[84:87], a[80:83], v[44:47], v[84:87]\n\t" \
    "global_load_lds_dwordx4 v1, s[50:51] offset:128\n\t" \
    "s_waitcnt lgkmcnt(5)\n\t" \
    "v_mfma_f32_16x16x32_bf16 v[72:75], a[12:15], v[48:51], v[72:75]\n\t" \
    "ds_read_b128 v[24:27], v2 offset:6144\n\t" \
    "s_waitcnt lgkmcnt(5)\n\t" \
    "v_mfma_f32_16x16x32_bf16 v[76:79], a[12:15], v[52:55], v[76:79]\n\t" \
    "ds_read_b128 v[28:31], v2 offset:24576\n\t" \
    "v_mfma_f32_16x16x32_bf16 v[80:83], a[84:87], v[48:51], v[80:83]\n\t" \
    "s_add_u32 m0, s91, 2880\n\t" \
    "v_mfma_f32_16x16x32_bf16 v[84:87], a[84:87], v[52:55], v[84:87]\n\t" \
    "global_load_lds_dwordx4 v1, s[50:51] offset:192\n\t" \
    "s_waitcnt lgkmcnt(5)\n\t" \
    "v_mfma_f32_16x16x32_bf16 v[72:75], a[16:19], v[56:59], v[72:75]\n\t" \
    "ds_read_b128 v[32:35], v2 offset:7168\n\t" \
    "s_waitcnt lgkmcnt(5)\n\t" \
    "v_mfma_f32_16x16x32_bf16 v[76:79], a[16:19], v[60:63], v[76:79]\n\t" \
    "ds_read_b128 v[36:39], v2 offset:25600\n\t" \
    "v_mfma_f32_16x16x32_bf16 v[80:83], a[88:91], v[56:59], v[80:83]\n\t" \
    "s_add_u32 m0, s91, 3840\n\t" \
    "v_mfma_f32_16x16x32_bf16 v[84:87], a[88:91], v[60:63], v[84:87]\n\t" \
    "global_load_lds_dwordx4 v1, s[50:51] offset:256\n\t" \
    "s_waitcnt lgkmcnt(5)\n\t" \
    "v_mfma_f32_16x16x32_bf16 v[72:75], a[20:23], v[64:67], v[72:75]\n\t" \
    "ds_read_b128 v[40:43], v2 offset:8192\n\t" \
    "s_waitcnt lgkmcnt(5)\n\t" \
    "v_mfma_f32_16x16x32_bf16 v[76:79], a[20:23], v[68:71], v[76:79]\n\t" \
    "ds_read_b128 v[44:47], v2 offset:26624\n\t" \
    "v_mfma_f32_16x16x32_bf16 v[80:83], a[92:95], v[64:67], v[80:83]\n\t" \
    "s_add_u32 m0, s91, 4800\n\t" \
    "v_mfma_f32_16x16x32_bf16 v[84:87], a[92:95], v[68:71], v[84:87]\n\t" \
    "global_load_lds_dwordx4 v1, s[50:51] offset:320\n\t" \
    "s_waitcnt lgkmcnt(5)\n\t" \
    "v_mfma_f32_16x16x32_bf16 v[72:75], a[24:27], v[24:27], v[72:75]\n\t" \
    "ds_read_b128 v[48:51], v2 offset:9216\n\t" \
    "s_waitcnt lgkmcnt(5)\n\t" \
    "v_mfma_f32_16x16x32_bf16 v[76:79], a[24:27], v[28:31], v[76:79]\n\t" \
    "ds_read_b128 v[52:55], v2 offset:27648\n\t" \
    "v_mfma_f32_16x16x32_bf16 v[80:83], a[96:99], v[24:27], v[80:83]\n\t" \
    "s_add_u32 m0, s91, 5760\n\t" \
    "v_mfma_f32_16x16x32_bf16 v[84:87], a[96:99], v[28:31], v[84:87]\n\t" \
    "global_load_lds_dwordx4 v1, s[50:51] offset:384\n\t" \
    "s_waitcnt lgkmcnt(5)\n\t" \
    "v_mfma_f32_16x16x32_bf16 v[72:75], a[28:31], v[32:35], v[72:75]\n\t" \
    "ds_read_b128 v[56:59], v2 offset:10240\n\t" \
    "s_waitcnt lgkmcnt(5)\n\t" \
    "v_mfma_f32_16x16x32_bf16 v[76:79], a[28:31], v[36:39], v[76:79]\n\t" \
    "ds_read_b128 v[60:63], v2 offset:28672\n\t" \
    "v_mfma_f32_16x16x32_bf16 v[80:83], a[100:103], v[32:35], v[80:83]\n\t" \
    "s_add_u32 m0, s91, 6720\n\t" \
    "v_mfma_f32_16x16x32_bf16 v[84:87], a[100:103], v[36:39], v[84:87]\n\t" \
    "global_load_lds_dwordx4 v1, s[50:51] offset:448\n\t" \
    "s_waitcnt lgkmcnt(5)\n\t" \
    "v_mfma_f32_16x16x32_bf16 v[72:75], a[32:35], v[40:43], v[72:75]\n\t" \
    "ds_read_b128 v[64:67], v2 offset:11264\n\t" \
    "s_waitcnt lgkmcnt(5)\n\t" \
    "v_mfma_f32_16x16x32_bf16 v[76:79], a[32:35], v[44:47], v[76:79]\n\t" \
    "ds_read_b128 v[68:71], v2 offset:29696\n\t" \
    "v_mfma_f32_16x16x32_bf16 v[80:83], a[104:107], v[40:43], v[80:83]\n\t" \
    "s_add_u32 m0, s91, 7680\n\t" \
    "v_mfma_f32_16x16x32_bf16 v[84:87], a[104:107], v[44:47], v[84:87]\n\t" \
    "global_load_lds_dwordx4 v1, s[50:51] offset:512\n\t" \
    "s_waitcnt lgkmcnt(5)\n\t" \
    "v_mfma_f32_16x16x32_bf16 v[72:75], a[36:39], v[48:51], v[72:75]\n\t" \
    "ds_read_b128 v[24:27], v2 offset:12288\n\t" \
    "s_waitcnt lgkmcnt(5)\n\t" \
    "v_mfma_f32_16x16x32_bf16 v[76:79], a[36:39], v[52:55], v[76:79]\n\t" \
    "ds_read_b128 v[28:31], v2 offset:30720\n\t" \
    "v_mfma_f32_16x16x32_bf16 v[80:83], a[108:111], v[48:51], v[80:83]\n\t" \
    "v_mfma_f32_16x16x32_bf16 v[84:87], a[108:111], v[52:55], v[84:87]\n\t" \
    "s_waitcnt lgkmcnt(5)\n\t" \
    "v_mfma_f32_16x16x32_bf16 v[72:75], a[40:43], v[56:59], v[72:75]\n\t" \
    "ds_read_b128 v[32:35], v2 offset:13312\n\t" \
    "s_waitcnt lgkmcnt(5)\n\t" \
    "v_mfma_f32_16x16x32_bf16 v[76:79], a[40:43], v[60:63], v[76:79]\n\t" \
    "ds_read_b128 v[36:39], v2 offset:31744\n\t" \
    "v_mfma_f32_16x16x32_bf16 v[80:83], a[112:115], v[56:59], v[80:83]\n\t" \
    "v_mfma_f32_16x16x32_bf16 v[84:87], a[112:115], v[60:63], v[84:87]\n\t" \
    "s_waitcnt lgkmcnt(5)\n\t" \
    "v_mfma_f32_16x16x32_bf16 v[72:75], a[44:47], v[64:67], v[72:75]\n\t" \
    "ds_read_b128 v[40:43], v2 offset:14336\n\t" \
    "s_waitcnt lgkmcnt(5)\n\t" \
    "v_mfma_f32_16x16x32_bf16 v[76:79], a[44:47], v[68:71], v[76:79]\n\t" \
    "ds_read_b128 v[44:47], v2 offset:32768\n\t" \
    "v_mfma_f32_16x16x32_bf16 v[80:83], a[116:119], v[64:67], v[80:83]\n\t" \
    "v_mfma_f32_16x16x32_bf16 v[84:87], a[116:119], v[68:71], v[84:87]\n\t" \
    "s_waitcnt lgkmcnt(5)\n\t" \
    "v_mfma_f32_16x16x32_bf16 v[72:75], a[48:51], v[24:27], v[72:75]\n\t" \
    "ds_read_b128 v[48:51], v2 offset:15360\n\t" \
    "s_waitcnt lgkmcnt(5)\n\t" \
    "v_mfma_f32_16x16x32_bf16 v[76:79], a[48:51], v[28:31], v[76:79]\n\t" \
    "ds_read_b128 v[52:55], v2 offset:33792\n\t" \
    "v_mfma_f32_16x16x32_bf16 v[80:83], a[120:123], v[24:27], v[80:83]\n\t" \
    "v_mfma_f32_16x16x32_bf16 v[84:87], a[120:123], v[28:31], v[84:87]\n\t" \
    "s_waitcnt lgkmcnt(5)\n\t" \
    "v_mfma_f32_16x16x32_bf16 v[72:75], a[52:55], v[32:35], v[72:75]\n\t" \
    "ds_read_b128 v[56:59], v2 offset:16384\n\t" \
    "s_waitcnt lgkmcnt(5)\n\t" \
    "v_mfma_f32_16x16x32_bf16 v[76:79], a[52:55], v[36:39], v[76:79]\n\t" \
    "ds_read_b128 v[60:63], v2 offset:34816\n\t" \
    "v_mfma_f32_16x16x32_bf16 v[80:83], a[124:127], v[32:35], v[80:83]\n\t" \
    "v_mfma_f32_16x16x32_bf16 v[84:87], a[124:127], v[36:39], v[84:87]\n\t" \
    "s_waitcnt lgkmcnt(5)\n\t" \
    "v_mfma_f32_16x16x32_bf16 v[72:75], a[56:59], v[40:43], v[72:75]\n\t" \
    "ds_read_b128 v[64:67], v2 offset:17408\n\t" \
    "s_waitcnt lgkmcnt(5)\n\t" \
    "v_mfma_f32_16x16x32_bf16 v[76:79], a[56:59], v[44:47], v[76:79]\n\t" \
    "ds_read_b128 v[68:71], v2 offset:35840\n\t" \
    "v_mfma_f32_16x16x32_bf16 v[80:83], a[128:131], v[40:43], v[80:83]\n\t" \
    "v_mfma_f32_16x16x32_bf16 v[84:87], a[128:131], v[44:47], v[84:87]\n\t" \
    "s_waitcnt lgkmcnt(5)\n\t" \
    "v_mfma_f32_16x16x32_bf16 v[72:75], a[60:63], v[48:51], v[72:75]\n\t" \
    "s_waitcnt lgkmcnt(4)\n\t" \
    "v_mfma_f32_16x16x32_bf16 v[76:79], a[60:63], v[52:55], v[76:79]\n\t" \
    "v_mfma_f32_16x16x32_bf16 v[80:83], a[132:135], v[48:51], v[80:83]\n\t" \
    "v_mfma_f32_16x16x32_bf16 v[84:87], a[132:135], v[52:55], v[84:87]\n\t" \
    "s_waitcnt lgkmcnt(3)\n\t" \
    "v_mfma_f32_16x16x32_bf16 v[72:75], a[64:67], v[56:59], v[72:75]\n\t" \
    "s_waitcnt lgkmcnt(2)\n\t" \
    "v_mfma_f32_16x16x32_bf16 v[76:79], a[64:67], v[60:63], v[76:79]\n\t" \
    "v_mfma_f32_16x16x32_bf16 v[80:83], a[136:139], v[56:59], v[80:83]\n\t" \
    "v_mfma_f32_16x16x32_bf16 v[84:87], a[136:139], v[60:63], v[84:87]\n\t" \
    "s_waitcnt lgkmcnt(1)\n\t" \
    "v_mfma_f32_16x16x32_bf16 v[72:75], a[68:71], v[64:67], v[72:75]\n\t" \
    "s_waitcnt lgkmcnt(0)\n\t" \
    "v_mfma_f32_16x16x32_bf16 v[76:79], a[68:71], v[68:71], v[76:79]\n\t" \
    "v_mfma_f32_16x16x32_bf16 v[80:83], a[140:143], v[64:67], v[80:83]\n\t" \
    "v_mfma_f32_16x16x32_bf16 v[84:87], a[140:143], v[68:71], v[84:87]\n\t" \
    "gv_mid8_%=:\n\t" \
    "v_mov_b32 v2, v17\n\t" \
    "s_nop 2\n\t" \
    "v_cvt_pk_bf16_f32 v104, v72, v73\n\t" \
    "v_cvt_pk_bf16_f32 v105, v74, v75\n\t" \
    "s_nop 0\n\t" \
    "v_cvt_pk_bf16_f32 v106, v80, v81\n\t" \
    "v_cvt_pk_bf16_f32 v107, v82, v83\n\t" \
    "ds_write_b128 v4, v[104:107] offset:0\n\t" \
    "v_cvt_pk_bf16_f32 v108, v76, v77\n\t" \
    "v_cvt_pk_bf16_f32 v109, v78, v79\n\t" \
    "v_cvt_pk_bf16_f32 v110, v84, v85\n\t" \
    "v_cvt_pk_bf16_f32 v111, v86, v87\n\t" \
    "ds_write_b128 v4, v[108:111] offset:1024\n\t" \
    "s_waitcnt vmcnt(18) lgkmcnt(0)\n\t" \
    "s_barrier\n\t" \
    "ds_read_b128 v[164:167], v5 offset:0\n\t" \
    "ds_read_b128 v[168:171], v5 offset:1024\n\t" \
    "ds_read_b128 v[172:175], v5 offset:2048\n\t" \
    "ds_read_b128 v[176:179], v5 offset:3072\n\t" \
    "ds_read_b128 v[180:183], v5 offset:4096\n\t" \
    "ds_read_b128 v[184:187], v5 offset:5120\n\t" \
    "ds_read_b128 v[188:191], v5 offset:6144\n\t" \
    "ds_read_b128 v[192:195], v5 offset:7168\n\t" \
    "s_cmp_lt_u32 s65, 10\n\t" \
    "s_cbranch_scc1 gv_fl9_%=\n\t" \
    "s_cmp_lg_u32 s66, 0\n\t" \
    "s_cbranch_scc1 gv_SF10_%=\n\t" \
    "s_lshl_b32 s69, s67, 7\n\t" \
    "s_add_u32 s69, s69, 6111232\n\t" \
    "s_add_u32 s69, s69, s52\n\t" \
    "s_add_u32 s70, s65, -4\n\t" \
    "v_mov_b32 v156, s69\n\t" \
    "v_mov_b32 v157, s70\n\t" \
    "s_mov_b64 exec, 1\n\t" \
    "global_store_dword v156, v157, s[36:37] sc1\n\t" \
    "s_mov_b64 exec, -1\n\t" \
    "gv_SF10_%=:\n\t" \
    "gv_fl9_%=:\n\t" \
    "v_xor_b32 v4, 0x2000, v4\n\t" \
    "v_xor_b32 v5, 0x2000, v5\n\t" \
    "s_waitcnt lgkmcnt(7)\n\t" \
    "v_mfma_f32_16x16x32_bf16 v[72:75], v[164:167], a[144:147], 0\n\t" \
    "s_waitcnt lgkmcnt(6)\n\t" \
    "v_mfma_f32_16x16x32_bf16 v[76:79], v[168:171], a[144:147], 0\n\t" \
    "s_waitcnt lgkmcnt(5)\n\t" \
    "v_mfma_f32_16x16x32_bf16 v[72:75], v[172:175], a[148:151], v[72:75]\n\t" \
    "s_waitcnt lgkmcnt(4)\n\t" \
    "v_mfma_f32_16x16x32_bf16 v[76:79], v[176:179], a[148:151], v[76:79]\n\t" \
    "s_waitcnt lgkmcnt(3)\n\t" \
    "v_mfma_f32_16x16x32_bf16 v[72:75], v[180:183], a[152:155], v[72:75]\n\t" \
    "s_waitcnt lgkmcnt(2)\n\t" \
    "v_mfma_f32_16x16x32_bf16 v[76:79], v[184:187], a[152:155], v[76:79]\n\t" \
    "s_waitcnt lgkmcnt(1)\n\t" \
    "v_mfma_f32_16x16x32_bf16 v[72:75], v[188:191], a[156:159], v[72:75]\n\t" \
    "s_waitcnt lgkmcnt(0)\n\t" \
    "v_mfma_f32_16x16x32_bf16 v[76:79], v[192:195], a[156:159], v[76:79]\n\t" \
    "v_mfma_f32_16x16x32_bf16 v[80:83], v[164:167], a[160:163], 0\n\t" \
    "v_mfma_f32_16x16x32_bf16 v[84:87], v[168:171], a[160:163], 0\n\t" \
    "v_mfma_f32_16x16x32_bf16 v[80:83], v[172:175], a[164:167], v[80:83]\n\t" \
    "v_mfma_f32_16x16x32_bf16 v[84:87], v[176:179], a[164:167], v[84:87]\n\t" \
    "v_mfma_f32_16x16x32_bf16 v[80:83], v[180:183], a[168:171], v[80:83]\n\t" \
    "v_mfma_f32_16x16x32_bf16 v[84:87], v[184:187], a[168:171], v[84:87]\n\t" \
    "v_cvt_pk_bf16_f32 v104, v72, v73\n\t" \
    "v_mfma_f32_16x16x32_bf16 v[80:83], v[188:191], a[172:175], v[80:83]\n\t" \
    "v_cvt_pk_bf16_f32 v105, v74, v75\n\t" \
    "v_mfma_f32_16x16x32_bf16 v[84:87], v[192:195], a[172:175], v[84:87]\n\t" \
    "v_cvt_pk_bf16_f32 v106, v76, v77\n\t" \
    "v_mfma_f32_16x16x32_bf16 v[88:91], v[164:167], a[176:179], 0\n\t" \
    "v_cvt_pk_bf16_f32 v107, v78, v79\n\t" \
    "v_mfma_f32_16x16x32_bf16 v[92:95], v[168:171], a[176:179], 0\n\t" \
    "v_xor_b32 v18, s40, v8\n\t" \
    "v_mfma_f32_16x16x32_bf16 v[88:91], v[172:175], a[180:183], v[88:91]\n\t" \
    "global_store_dwordx4 v18, v[104:107], s[38:39] sc1\n\t" \
    "v_mfma_f32_16x16x32_bf16 v[92:95], v[176:179], a[180:183], v[92:95]\n\t" \
    "v_mfma_f32_16x16x32_bf16 v[88:91], v[180:183], a[184:187], v[88:91]\n\t" \
    "v_mfma_f32_16x16x32_bf16 v[92:95], v[184:187], a[184:187], v[92:95]\n\t" \
    "v_cvt_pk_bf16_f32 v108, v80, v81\n\t" \
    "v_mfma_f32_16x16x32_bf16 v[88:91], v[188:191], a[188:191], v[88:91]\n\t" \
    "v_cvt_pk_bf16_f32 v109, v82, v83\n\t" \
    "v_mfma_f32_16x16x32_bf16 v[92:95], v[192:195], a[188:191], v[92:95]\n\t" \
    "v_cvt_pk_bf16_f32 v110, v84, v85\n\t" \
    "v_mfma_f32_16x16x32_bf16 v[72:75], v[164:167], a[192:195], 0\n\t" \
    "v_cvt_pk_bf16_f32 v111, v86, v87\n\t" \
    "v_mfma_f32_16x16x32_bf16 v[76:79], v[168:171], a[192:195], 0\n\t" \
    "v_xor_b32 v18, s40, v9\n\t" \
    "v_mfma_f32_16x16x32_bf16 v[72:75], v[172:175], a[196:199], v[72:75]\n\t" \
    "global_store_dwordx4 v18, v[108:111], s[38:39] sc1\n\t" \
    "v_mfma_f32_16x16x32_bf16 v[76:79], v[176:179], a[196:199], v[76:79]\n\t" \
    "v_mfma_f32_16x16x32_bf16 v[72:75], v[180:183], a[200:203], v[72:75]\n\t" \
    "v_mfma_f32_16x16x32_bf16 v[76:79], v[184:187], a[200:203], v[76:79]\n\t" \
    "v_cvt_pk_bf16_f32 v104, v88, v89\n\t" \
    "v_mfma_f32_16x16x32_bf16 v[72:75], v[188:191], a[204:207], v[72:75]\n\t" \
    "v_cvt_pk_bf16_f32 v105, v90, v91\n\t" \
    "v_mfma_f32_16x16x32_bf16 v[76:79], v[192:195], a[204:207], v[76:79]\n\t" \
    "v_cvt_pk_bf16_f32 v106, v92, v93\n\t" \
    "v_mfma_f32_16x16x32_bf16 v[80:83], v[164:167], a[208:211], 0\n\t" \
    "v_cvt_pk_bf16_f32 v107, v94, v95\n\t" \
    "v_mfma_f32_16x16x32_bf16 v[84:87], v[168:171], a[208:211], 0\n\t" \
    "v_xor_b32 v18, s40, v10\n\t" \
    "v_mfma_f32_16x16x32_bf16 v[80:83], v[172:175], a[212:215], v[80:83]\n\t" \
    "global_store_dwordx4 v18, v[104:107], s[38:39] sc1\n\t" \
    "v_mfma_f32_16x16x32_bf16 v[84:87], v[176:179], a[212:215], v[84:87]\n\t" \
    "v_mfma_f32_16x16x32_bf16 v[80:83], v[180:183], a[216:219], v[80:83]\n\t" \
    "v_mfma_f32_16x16x32_bf16 v[84:87], v[184:187], a[216:219], v[84:87]\n\t" \
    "v_cvt_pk_bf16_f32 v108, v72, v73\n\t" \
    "v_mfma_f32_16x16x32_bf16 v[80:83], v[188:191], a[220:223], v[80:83]\n\t" \
    "v_cvt_pk_bf16_f32 v109, v74, v75\n\t" \
    "v_mfma_f32_16x16x32_bf16 v[84:87], v[192:195], a[220:223], v[84:87]\n\t" \
    "v_cvt_pk_bf16_f32 v110, v76, v77\n\t" \
    "v_mfma_f32_16x16x32_bf16 v[88:91], v[164:167], a[224:227], 0\n\t" \
    "v_cvt_pk_bf16_f32 v111, v78, v79\n\t" \
    "v_mfma_f32_16x16x32_bf16 v[92:95], v[168:171], a[224:227], 0\n\t" \
    "v_xor_b32 v18, s40, v11\n\t" \
    "v_mfma_f32_16x16x32_bf16 v[88:91], v[172:175], a[228:231], v[88:91]\n\t" \
    "global_store_dwordx4 v18, v[108:111], s[38:39] sc1\n\t" \
    "v_mfma_f32_16x16x32_bf16 v[92:95], v[176:179], a[228:231], v[92:95]\n\t" \
    "v_mfma_f32_16x16x32_bf16 v[88:91], v[180:183], a[232:235], v[88:91]\n\t" \
    "v_mfma_f32_16x16x32_bf16 v[92:95], v[184:187], a[232:235], v[92:95]\n\t" \
    "v_cvt_pk_bf16_f32 v104, v80, v81\n\t" \
    "v_mfma_f32_16x16x32_bf16 v[88:91], v[188:191], a[236:239], v[88:91]\n\t" \
    "v_cvt_pk_bf16_f32 v105, v82, v83\n\t" \
    "v_mfma_f32_16x16x32_bf16 v[92:95], v[192:195], a[236:239], v[92:95]\n\t" \
    "v_cvt_pk_bf16_f32 v106, v84, v85\n\t" \
    "v_mfma_f32_16x16x32_bf16 v[72:75], v[164:167], a[240:243], 0\n\t" \
    "v_cvt_pk_bf16_f32 v107, v86, v87\n\t" \
    "v_mfma_f32_16x16x32_bf16 v[76:79], v[168:171], a[240:243], 0\n\t" \
    "v_xor_b32 v18, s40, v12\n\t" \
    "v_mfma_f32_16x16x32_bf16 v[72:75], v[172:175], a[244:247], v[72:75]\n\t" \
    "global_store_dwordx4 v18, v[104:107], s[38:39] sc1\n\t" \
    "v_mfma_f32_16x16x32_bf16 v[76:79], v[176:179], a[244:247], v[76:79]\n\t" \
    "v_mfma_f32_16x16x32_bf16 v[72:75], v[180:183], a[248:251], v[72:75]\n\t" \
    "v_mfma_f32_16x16x32_bf16 v[76:79], v[184:187], a[248:251], v[76:79]\n\t" \
    "v_cvt_pk_bf16_f32 v108, v88, v89\n\t" \
    "v_mfma_f32_16x16x32_bf16 v[72:75], v[188:191], a[252:255], v[72:75]\n\t" \
    "v_cvt_pk_bf16_f32 v109, v90, v91\n\t" \
    "v_mfma_f32_16x16x32_bf16 v[76:79], v[192:195], a[252:255], v[76:79]\n\t" \
    "v_cvt_pk_bf16_f32 v110, v92, v93\n\t" \
    "v_mfma_f32_16x16x32_bf16 v[80:83], v[164:167], v[224:227], 0\n\t" \
    "v_cvt_pk_bf16_f32 v111, v94, v95\n\t" \
    "v_mfma_f32_16x16x32_bf16 v[84:87], v[168:171], v[224:227], 0\n\t" \
    "v_xor_b32 v18, s40, v13\n\t" \
    "v_mfma_f32_16x16x32_bf16 v[80:83], v[172:175], v[228:231], v[80:83]\n\t" \
    "global_store_dwordx4 v18, v[108:111], s[38:39] sc1\n\t" \
    "v_mfma_f32_16x16x32_bf16 v[84:87], v[176:179], v[228:231], v[84:87]\n\t" \
    "v_mfma_f32_16x16x32_bf16 v[80:83], v[180:183], v[232:235], v[80:83]\n\t" \
    "v_mfma_f32_16x16x32_bf16 v[84:87], v[184:187], v[232:235], v[84:87]\n\t" \
    "v_cvt_pk_bf16_f32 v104, v72, v73\n\t" \
    "v_mfma_f32_16x16x32_bf16 v[80:83], v[188:191], v[236:239], v[80:83]\n\t" \
    "v_cvt_pk_bf16_f32 v105, v74, v75\n\t" \
    "v_mfma_f32_16x16x32_bf16 v[84:87], v[192:195], v[236:239], v[84:87]\n\t" \
    "v_cvt_pk_bf16_f32 v106, v76, v77\n\t" \
    "s_cmp_ge_u32 s65, 4\n\t" \
    "s_cbranch_scc1 gv_f1st11_%=\n\t" \
    "s_waitcnt vmcnt(6)\n\t" \
    "s_barrier\n\t" \
    "gv_f1st11_%=:\n\t" \
    "v_mfma_f32_16x16x32_bf16 v[88:91], v[164:167], v[240:243], 0\n\t" \
    "v_cvt_pk_bf16_f32 v107, v78, v79\n\t" \
    "ds_read_b128 v[24:27], v17 offset:0\n\t" \
    "v_mfma_f32_16x16x32_bf16 v[92:95], v[168:171], v[240:243], 0\n\t" \
    "v_xor_b32 v18, s40, v14\n\t" \
    "ds_read_b128 v[28:31], v17 offset:18432\n\t" \
    "v_mfma_f32_16x16x32_bf16 v[88:91], v[172:175], v[244:247], v[88:91]\n\t" \
    "global_store_dwordx4 v18, v[104:107], s[38:39] sc1\n\t" \
    "ds_read_b128 v[32:35], v17 offset:1024\n\t" \
    "v_mfma_f32_16x16x32_bf16 v[92:95], v[176:179], v[244:247], v[92:95]\n\t" \
    "ds_read_b128 v[36:39], v17 offset:19456\n\t" \
    "v_mfma_f32_16x16x32_bf16 v[88:91], v[180:183], v[248:251], v[88:91]\n\t" \
    "ds_read_b128 v[40:43], v17 offset:2048\n\t" \
    "v_mfma_f32_16x16x32_bf16 v[92:95], v[184:187], v[248:251], v[92:95]\n\t" \
    "v_cvt_pk_bf16_f32 v108, v80, v81\n\t" \
    "ds_read_b128 v[44:47], v17 offset:20480\n\t" \
    "v_mfma_f32_16x16x32_bf16 v[88:91], v[188:191], v[252:255], v[88:91]\n\t" \
    "v_cvt_pk_bf16_f32 v109, v82, v83\n\t" \
    "v_mfma_f32_16x16x32_bf16 v[92:95], v[192:195], v[252:255], v[92:95]\n\t" \
    "v_cvt_pk_bf16_f32 v110, v84, v85\n\t" \
    "v_cvt_pk_bf16_f32 v111, v86, v87\n\t" \
    "v_xor_b32 v18, s40, v15\n\t" \
    "global_store_dwordx4 v18, v[108:111], s[38:39] sc1\n\t" \
    "s_nop 1\n\t" \
    "v_cvt_pk_bf16_f32 v104, v88, v89\n\t" \
    "v_cvt_pk_bf16_f32 v105, v90, v91\n\t" \
    "v_cvt_pk_bf16_f32 v106, v92, v93\n\t" \
    "v_cvt_pk_bf16_f32 v107, v94, v95\n\t" \
    "v_xor_b32 v18, s40, v16\n\t" \
    "global_store_dwordx4 v18, v[104:107], s[38:39] sc1\n\t" \
    "s_sub_u32 s69, s65, 6\n\t" \
    "s_cmp_lt_u32 s69, 56\n\t" \
    "s_cbranch_scc1 gv_nofast7_%=\n\t" \
    "s_waitcnt vmcnt(0)\n\t" \
    "s_barrier\n\t" \
    "s_cmp_lg_u32 s66, 0\n\t" \
    "s_cbranch_scc1 gv_SF12_%=\n\t" \
    "s_lshl_b32 s69, s67, 7\n\t" \
    "s_add_u32 s69, s69, 6111232\n\t" \
    "s_add_u32 s69, s69, s52\n\t" \
    "s_add_u32 s70, s65, 0\n\t" \
    "v_mov_b32 v156, s69\n\t" \
    "v_mov_b32 v157, s70\n\t" \
    "s_mov_b64 exec, 1\n\t" \
    "global_store_dword v156, v157, s[36:37] sc1\n\t" \
    "s_mov_b64 exec, -1\n\t" \
    "gv_SF12_%=:\n\t" \
    "s_cmp_ge_u32 s65, 2\n\t" \
    "s_cbranch_scc1 gv_nofast7_%=\n\t" \
    "s_add_u32 m0, s91, 0\n\t" \
    "s_nop 0\n\t" \
    "global_load_lds_dwordx4 v1, s[50:51] offset:0\n\t" \
    "s_add_u32 m0, s91, 960\n\t" \
    "s_nop 0\n\t" \
    "global_load_lds_dwordx4 v1, s[50:51] offset:64\n\t" \
    "s_add_u32 m0, s91, 1920\n\t" \
    "s_nop 0\n\t" \
    "global_load_lds_dwordx4 v1, s[50:51] offset:128\n\t" \
    "s_add_u32 m0, s91, 2880\n\t" \
    "s_nop 0\n\t" \
    "global_load_lds_dwordx4 v1, s[50:51] offset:192\n\t" \
    "s_add_u32 m0, s91, 3840\n\t" \
    "s_nop 0\n\t" \
    "global_load_lds_dwordx4 v1, s[50:51] offset:256\n\t" \
    "s_add_u32 m0, s91, 4800\n\t" \
    "s_nop 0\n\t" \
    "global_load_lds_dwordx4 v1, s[50:51] offset:320\n\t" \
    "s_add_u32 m0, s91, 5760\n\t" \
    "s_nop 0\n\t" \
    "global_load_lds_dwordx4 v1, s[50:51] offset:384\n\t" \
    "s_add_u32 m0, s91, 6720\n\t" \
    "s_nop 0\n\t" \
    "global_load_lds_dwordx4 v1, s[50:51] offset:448\n\t" \
    "s_add_u32 m0, s91, 7680\n\t" \
    "s_nop 0\n\t" \
    "global_load_lds_dwordx4 v1, s[50:51] offset:512\n\t" \
    "gv_nofast7_%=:\n\t" \
    "s_add_u32 s65, s65, 2\n\t" \
    "s_cmp_lt_u32 s65, 64\n\t" \
    "s_cbranch_scc1 gv_loop6_%=\n\t" \
    "s_waitcnt vmcnt(0) lgkmcnt(0)\n\t" \
    "s_barrier\n\t" \
    "ea_done_%=:\n\t" \
    "s_waitcnt lgkmcnt(0)\n\t" \
    "ea_last_%=:\n\t" \
    ""
#define ASM_R \
    "s_mov_b64 s[36:37], %0\n\t" \
    "s_mov_b64 s[42:43], %1\n\t" \
    "s_mov_b64 s[44:45], %2\n\t" \
    "s_mov_b32 s66, %3\n\t" \
    "s_mov_b32 s67, %4\n\t" \
    "s_mov_b64 s[50:51], %5\n\t" \
    "s_mov_b32 s82, %7\n\t" \
    "s_load_dwordx8 s[84:91], %6, 0\n\t" \
    "v_mbcnt_lo_u32_b32 v223, -1, 0\n\t" \
    "v_mbcnt_hi_u32_b32 v223, -1, v223\n\t" \
    "s_getpc_b64 s[46:47]\n\t" \
    "rr_pc_%=:\n\t" \
    "s_lshl_b32 s69, s66, 13\n\t" \
    "v_lshl_add_u32 v222, v223, 7, s69\n\t" \
    "v_min_u32 v222, rr_last_%=-rr_pc_%=-4, v222\n\t" \
    "global_load_dword v222, v222, s[46:47]\n\t" \
    "v_lshlrev_b32 v223, 2, v223\n\t" \
    "v_add_u32 v223, s82, v223\n\t" \
    "ds_read_b32 v0, v223 offset:0\n\t" \
    "ds_read_b32 v1, v223 offset:256\n\t" \
    "ds_read_b32 v2, v223 offset:512\n\t" \
    "ds_read_b32 v3, v223 offset:768\n\t" \
    "ds_read_b32 v4, v223 offset:1024\n\t" \
    "ds_read_b32 v5, v223 offset:1280\n\t" \
    "ds_read_b32 v6, v223 offset:1536\n\t" \
    "ds_read_b32 v7, v223 offset:1792\n\t" \
    "ds_read_b32 v8, v223 offset:2048\n\t" \
    "ds_read_b32 v9, v223 offset:2304\n\t" \
    "ds_read_b32 v10, v223 offset:2560\n\t" \
    "ds_read_b32 v11, v223 offset:2816\n\t" \
    "s_waitcnt lgkmcnt(0)\n\t" \
    "ds_read_b32 v12, v223 offset:3072\n\t" \
    "ds_read_b32 v13, v223 offset:3328\n\t" \
    "ds_read_b32 v14, v223 offset:3584\n\t" \
    "ds_read_b32 v15, v223 offset:3840\n\t" \
    "ds_read_b32 v16, v223 offset:4096\n\t" \
    "ds_read_b32 v17, v223 offset:4352\n\t" \
    "ds_read_b32 v18, v223 offset:4608\n\t" \
    "ds_read_b32 v19, v223 offset:4864\n\t" \
    "ds_read_b32 v20, v223 offset:5120\n\t" \
    "ds_read_b32 v21, v223 offset:5376\n\t" \
    "ds_read_b32 v22, v223 offset:5632\n\t" \
    "ds_read_b32 v23, v223 offset:5888\n\t" \
    "s_waitcnt lgkmcnt(0)\n\t" \
    "ds_read_b32 v24, v223 offset:6144\n\t" \
    "ds_read_b32 v25, v223 offset:6400\n\t" \
    "ds_read_b32 v26, v223 offset:6656\n\t" \
    "ds_read_b32 v27, v223 offset:6912\n\t" \
    "ds_read_b32 v28, v223 offset:7168\n\t" \
    "ds_read_b32 v29, v223 offset:7424\n\t" \
    "ds_read_b32 v30, v223 offset:7680\n\t" \
    "ds_read_b32 v31, v223 offset:7936\n\t" \
    "ds_read_b32 v32, v223 offset:8192\n\t" \
    "ds_read_b32 v33, v223 offset:8448\n\t" \
    "ds_read_b32 v34, v223 offset:8704\n\t" \
    "ds_read_b32 v35, v223 offset:8960\n\t" \
    "s_waitcnt lgkmcnt(0)\n\t" \
    "ds_read_b32 v36, v223 offset:9216\n\t" \
    "ds_read_b32 v37, v223 offset:9472\n\t" \
    "ds_read_b32 v38, v223 offset:9728\n\t" \
    "ds_read_b32 v39, v223 offset:9984\n\t" \
    "ds_read_b32 v40, v223 offset:10240\n\t" \
    "ds_read_b32 v41, v223 offset:10496\n\t" \
    "ds_read_b32 v42, v223 offset:10752\n\t" \
    "ds_read_b32 v43, v223 offset:11008\n\t" \
    "ds_read_b32 v44, v223 offset:11264\n\t" \
    "ds_read_b32 v45, v223 offset:11520\n\t" \
    "ds_read_b32 v46, v223 offset:11776\n\t" \
    "ds_read_b32 v164, v223 offset:12032\n\t" \
    "s_waitcnt lgkmcnt(0)\n\t" \
    "ds_read_b32 v165, v223 offset:12288\n\t" \
    "ds_read_b32 v166, v223 offset:12544\n\t" \
    "s_waitcnt lgkmcnt(0)\n\t" \
    "s_mov_b32 s68, 262144\n\t" \
    "s_mov_b32 s72, 0\n\t" \
    "s_mov_b32 s83, -1\n\t" \
    "s_mov_b32 s65, 0\n\t" \
    "v_add_u32 v181, 65536, v2\n\t" \
    "v_add_u32 v182, 131072, v2\n\t" \
    "v_add_u32 v183, 196608, v2\n\t" \
    "s_mov_b32 s77, 3019898880\n\t" \
    "s_mov_b32 s78, 3054198966\n\t" \
    "s_mov_b32 s71, 3\n\t" \
    "rr_E1_%=:\n\t" \
    "s_memtime s[54:55]\n\t" \
    "s_nop 0\n\t" \
    "s_memrealtime s[4:5]\n\t" \
    "s_nop 0\n\t" \
    "s_memrealtime s[6:7]\n\t" \
    "s_nop 0\n\t" \
    "s_memrealtime s[8:9]\n\t" \
    "s_nop 0\n\t" \
    "s_memrealtime s[10:11]\n\t" \
    "s_nop 0\n\t" \
    "s_memrealtime s[12:13]\n\t" \
    "s_nop 0\n\t" \
    "s_memrealtime s[14:15]\n\t" \
    "s_nop 0\n\t" \
    "s_memrealtime s[16:17]\n\t" \
    "s_nop 0\n\t" \
    "s_memrealtime s[18:19]\n\t" \
    "s_nop 0\n\t" \
    "s_memrealtime s[20:21]\n\t" \
    "s_nop 0\n\t" \
    "s_memrealtime s[22:23]\n\t" \
    "s_nop 0\n\t" \
    "s_memrealtime s[24:25]\n\t" \
    "s_nop 0\n\t" \
    "s_memrealtime s[26:27]\n\t" \
    "s_nop 0\n\t" \
    "s_memrealtime s[28:29]\n\t" \
    "s_nop 0\n\t" \
    "s_memtime s[56:57]\n\t" \
    "s_mov_b32 s70, 1\n\t" \
    "s_sub_u32 s71, s71, 1\n\t" \
    "s_cselect_b32 s69, 0, -1\n\t" \
    "s_waitcnt lgkmcnt(0)\n\t" \
    "s_sub_u32 s54, s56, s54\n\t" \
    "s_and_b32 s54, s54, s69\n\t" \
    "s_cmp_gt_u32 s54, 124\n\t" \
    "s_cbranch_scc1 rr_E1_%=\n\t" \
    "s_cmp_eq_u32 s4, s28\n\t" \
    "s_addc_u32 s70, s70, 0\n\t" \
    "s_cmp_eq_u32 s6, s28\n\t" \
    "s_addc_u32 s70, s70, 0\n\t" \
    "s_cmp_eq_u32 s8, s28\n\t" \
    "s_addc_u32 s70, s70, 0\n\t" \
    "s_cmp_eq_u32 s10, s28\n\t" \
    "s_addc_u32 s70, s70, 0\n\t" \
    "s_cmp_eq_u32 s12, s28\n\t" \
    "s_addc_u32 s70, s70, 0\n\t" \
    "s_cmp_eq_u32 s14, s28\n\t" \
    "s_addc_u32 s70, s70, 0\n\t" \
    "s_cmp_eq_u32 s16, s28\n\t" \
    "s_addc_u32 s70, s70, 0\n\t" \
    "s_cmp_eq_u32 s18, s28\n\t" \
    "s_addc_u32 s70, s70, 0\n\t" \
    "s_cmp_eq_u32 s20, s28\n\t" \
    "s_addc_u32 s70, s70, 0\n\t" \
    "s_cmp_eq_u32 s22, s28\n\t" \
    "s_addc_u32 s70, s70, 0\n\t" \
    "s_cmp_eq_u32 s24, s28\n\t" \
    "s_addc_u32 s70, s70, 0\n\t" \
    "s_cmp_eq_u32 s26, s28\n\t" \
    "s_addc_u32 s70, s70, 0\n\t" \
    "s_mov_b32 s75, s73\n\t" \
    "s_mov_b32 s76, s74\n\t" \
    "s_lshl_b32 s70, s70, 3\n\t" \
    "s_sub_u32 s73, s56, s70\n\t" \
    "s_lshl_b32 s74, s28, 4\n\t" \
    "v_mov_b32 v172, 0x1f0000\n\t" \
    "v_mov_b32 v173, 0x3f0000\n\t" \
    "global_load_dword v172, v172, s[36:37]\n\t" \
    "global_load_dword v173, v173, s[36:37]\n\t" \
    "s_mov_b32 s65, 63\n\t" \
    "s_mul_i32 s70, s67, 7\n\t" \
    "s_add_u32 s69, s65, 0\n\t" \
    "s_xor_b32 s69, s69, s70\n\t" \
    "s_and_b32 s69, s69, 63\n\t" \
    "s_lshl_b32 s69, s69, 15\n\t" \
    "s_mul_i32 s70, s67, 2097152\n\t" \
    "s_add_u32 s69, s69, s70\n\t" \
    "s_add_u32 s69, s69, 48234496\n\t" \
    "s_add_u32 s38, s36, s69\n\t" \
    "s_addc_u32 s39, s37, 0\n\t" \
    "v_mov_b32 v174, 0\n\t" \
    "global_load_dword v174, v174, s[38:39]\n\t" \
    "v_mbcnt_lo_u32_b32 v184, -1, 0\n\t" \
    "v_mbcnt_hi_u32_b32 v184, -1, v184\n\t" \
    "v_min_u32 v184, 59, v184\n\t" \
    "v_lshlrev_b32 v184, 6, v184\n\t" \
    "v_add_u32 v184, 6225920, v184\n\t" \
    "v_mov_b32 v185, 6234112\n\t" \
    "global_load_dword v185, v185, s[36:37] sc1\n\t" \
    "global_load_dwordx2 v[186:187], v184, s[36:37] sc1\n\t" \
    "s_waitcnt vmcnt(0)\n\t" \
    "v_sub_u32 v186, v186, v185\n\t" \
    "v_cmp_ne_u32 vcc, 1, v186\n\t" \
    "s_cbranch_vccz rr_Cok4_%=\n\t" \
    "rr_C3_%=:\n\t" \
    "global_load_dwordx2 v[186:187], v184, s[36:37] sc1\n\t" \
    "s_waitcnt vmcnt(0)\n\t" \
    "v_sub_u32 v186, v186, v185\n\t" \
    "v_cmp_ne_u32 vcc, 1, v186\n\t" \
    "s_cbranch_vccz rr_Cok4_%=\n\t" \
    "s_cmp_eq_u32 s68, 0\n\t" \
    "s_cbranch_scc1 rr_F5_%=\n\t" \
    "s_sub_u32 s68, s68, 1\n\t" \
    "s_sleep 4\n\t" \
    "s_branch rr_C3_%=\n\t" \
    "rr_F5_%=:\n\t" \
    "v_mov_b32 v188, 6095104\n\t" \
    "v_mov_b32 v189, 4\n\t" \
    "global_store_dword v188, v189, s[36:37] sc1\n\t" \
    "s_waitcnt vmcnt(0)\n\t" \
    "s_branch rr_Cok4_%=\n\t" \
    "rr_Cok4_%=:\n\t" \
    "v_min_u32_dpp v187, v187, v187 row_shl:6 row_mask:0xf bank_mask:0xf\n\t" \
    "s_nop 0\n\t" \
    "v_readlane_b32 s69, v187, 0\n\t" \
    "v_readlane_b32 s70, v187, 1\n\t" \
    "s_nop 1\n\t" \
    "s_cmp_lt_u32 s70, s69\n\t" \
    "s_cselect_b32 s62, 8192, 0\n\t" \
    "s_cselect_b32 s69, s70, s69\n\t" \
    "s_mov_b32 s71, s69\n\t" \
    "v_readlane_b32 s69, v187, 2\n\t" \
    "v_readlane_b32 s70, v187, 3\n\t" \
    "s_nop 1\n\t" \
    "s_cmp_lt_u32 s70, s69\n\t" \
    "s_cselect_b32 s63, 8192, 0\n\t" \
    "s_cselect_b32 s69, s70, s69\n\t" \
    "s_max_u32 s71, s69, s71\n\t" \
    "v_readlane_b32 s69, v187, 4\n\t" \
    "v_readlane_b32 s70, v187, 5\n\t" \
    "s_nop 1\n\t" \
    "s_cmp_lt_u32 s70, s69\n\t" \
    "s_cselect_b32 s64, 8192, 0\n\t" \
    "s_cselect_b32 s69, s70, s69\n\t" \
    "s_max_u32 s71, s69, s71\n\t" \
    "s_add_u32 s63, s63, 0x200000\n\t" \
    "s_xor_b32 s52, s64, 8192\n\t" \
    "s_cmp_gt_u32 s71, 625\n\t" \
    "s_cselect_b32 s69, 1, 0\n\t" \
    "s_mul_i32 s70, s69, 160\n\t" \
    "s_add_u32 s86, s86, s70\n\t" \
    "s_mul_i32 s70, s69, 536\n\t" \
    "s_add_u32 s87, s87, s70\n\t" \
    "s_mul_i32 s70, s69, 256\n\t" \
    "s_add_u32 s88, s88, s70\n\t" \
    "v_add_u32 v6, s52, v6\n\t" \
    "v_mov_b32 v172, -1\n\t" \
    "v_mov_b32 v173, -1\n\t" \
    "v_mov_b32 v174, -1\n\t" \
    "v_mov_b32 v175, -1\n\t" \
    "v_mov_b32 v170, 0\n\t" \
    "s_mov_b32 s65, s66\n\t" \
    "s_sub_u32 s80, s74, 32000\n\t" \
    "v_mov_b32 v177, -1\n\t" \
    "s_cmp_lg_u32 s51, 0\n\t" \
    "s_cselect_b32 s42, 0, 0\n\t" \
    "s_cselect_b32 s43, 32, 0\n\t" \
    "s_cmp_gt_u32 s65, 61\n\t" \
    "s_cbranch_scc1 rr_arm16_%=\n\t" \
    "s_add_u32 s69, s65, 2\n\t" \
    "s_bitcmp1_b32 s69, 0\n\t" \
    "s_cselect_b32 s70, s63, s62\n\t" \
    "s_bfe_u32 s69, s69, 0x20001\n\t" \
    "s_lshl_b32 s69, s69, 19\n\t" \
    "s_add_u32 s69, s69, s70\n\t" \
    "s_add_u32 s46, s36, s69\n\t" \
    "s_addc_u32 s47, s37, 0\n\t" \
    "global_store_dwordx4 v164, v[172:175], s[46:47] sc1\n\t" \
    "global_store_dwordx4 v165, v[172:175], s[46:47] sc1\n\t" \
    "global_store_dwordx4 v166, v[172:175], s[46:47] sc1\n\t" \
    "rr_arm16_%=:\n\t" \
    "s_waitcnt vmcnt(0)\n\t" \
    "s_add_u32 s52, s36, s62\n\t" \
    "s_addc_u32 s53, s37, 0\n\t" \
    "v_lshrrev_b32 v187, 9, v0\n\t" \
    "v_lshl_add_u32 v187, v187, 2, v7\n\t" \
    "v_add_u32 v187, 3072, v187\n\t" \
    "v_mov_b32 v189, -1\n\t" \
    "s_sleep 40\n\t" \
    "s_mov_b32 s71, 3\n\t" \
    "rr_E17_%=:\n\t" \
    "s_memtime s[54:55]\n\t" \
    "s_nop 0\n\t" \
    "s_memrealtime s[4:5]\n\t" \
    "s_nop 0\n\t" \
    "s_memrealtime s[6:7]\n\t" \
    "s_nop 0\n\t" \
    "s_memrealtime s[8:9]\n\t" \
    "s_nop 0\n\t" \
    "s_memrealtime s[10:11]\n\t" \
    "s_nop 0\n\t" \
    "s_memrealtime s[12:13]\n\t" \
    "s_nop 0\n\t" \
    "s_memrealtime s[14:15]\n\t" \
    "s_nop 0\n\t" \
    "s_memrealtime s[16:17]\n\t" \
    "s_nop 0\n\t" \
    "s_memrealtime s[18:19]\n\t" \
    "s_nop 0\n\t" \
    "s_memrealtime s[20:21]\n\t" \
    "s_nop 0\n\t" \
    "s_memrealtime s[22:23]\n\t" \
    "s_nop 0\n\t" \
    "s_memrealtime s[24:25]\n\t" \
    "s_nop 0\n\t" \
    "s_memrealtime s[26:27]\n\t" \
    "s_nop 0\n\t" \
    "s_memrealtime s[28:29]\n\t" \
    "s_nop 0\n\t" \
    "s_memtime s[56:57]\n\t" \
    "s_mov_b32 s70, 1\n\t" \
    "s_sub_u32 s71, s71, 1\n\t" \
    "s_cselect_b32 s69, 0, -1\n\t" \
    "s_waitcnt lgkmcnt(0)\n\t" \
    "s_sub_u32 s54, s56, s54\n\t" \
    "s_and_b32 s54, s54, s69\n\t" \
    "s_cmp_gt_u32 s54, 124\n\t" \
    "s_cbranch_scc1 rr_E17_%=\n\t" \
    "s_cmp_eq_u32 s4, s28\n\t" \
    "s_addc_u32 s70, s70, 0\n\t" \
    "s_cmp_eq_u32 s6, s28\n\t" \
    "s_addc_u32 s70, s70, 0\n\t" \
    "s_cmp_eq_u32 s8, s28\n\t" \
    "s_addc_u32 s70, s70, 0\n\t" \
    "s_cmp_eq_u32 s10, s28\n\t" \
    "s_addc_u32 s70, s70, 0\n\t" \
    "s_cmp_eq_u32 s12, s28\n\t" \
    "s_addc_u32 s70, s70, 0\n\t" \
    "s_cmp_eq_u32 s14, s28\n\t" \
    "s_addc_u32 s70, s70, 0\n\t" \
    "s_cmp_eq_u32 s16, s28\n\t" \
    "s_addc_u32 s70, s70, 0\n\t" \
    "s_cmp_eq_u32 s18, s28\n\t" \
    "s_addc_u32 s70, s70, 0\n\t" \
    "s_cmp_eq_u32 s20, s28\n\t" \
    "s_addc_u32 s70, s70, 0\n\t" \
    "s_cmp_eq_u32 s22, s28\n\t" \
    "s_addc_u32 s70, s70, 0\n\t" \
    "s_cmp_eq_u32 s24, s28\n\t" \
    "s_addc_u32 s70, s70, 0\n\t" \
    "s_cmp_eq_u32 s26, s28\n\t" \
    "s_addc_u32 s70, s70, 0\n\t" \
    "s_mov_b32 s75, s73\n\t" \
    "s_mov_b32 s76, s74\n\t" \
    "s_lshl_b32 s70, s70, 3\n\t" \
    "s_sub_u32 s73, s56, s70\n\t" \
    "s_lshl_b32 s74, s28, 4\n\t" \
    "s_sub_u32 s69, s73, s75\n\t" \
    "s_sub_u32 s70, s74, s76\n\t" \
    "s_sub_u32 s71, s70, 1600\n\t" \
    "s_cmp_lt_u32 s71, 400000\n\t" \
    "s_cbranch_scc0 rr_K19_%=\n\t" \
    "v_cvt_f32_u32 v190, s69\n\t" \
    "v_cvt_f32_u32 v191, s70\n\t" \
    "v_rcp_f32 v192, v190\n\t" \
    "v_rcp_f32 v193, v191\n\t" \
    "v_mul_f32 v192, v192, v191\n\t" \
    "v_mul_f32 v193, v193, v190\n\t" \
    "v_mul_f32 v192, 0x4f800000, v192\n\t" \
    "v_mul_f32 v193, 0x4f000000, v193\n\t" \
    "v_cvt_u32_f32 v192, v192\n\t" \
    "v_cvt_u32_f32 v193, v193\n\t" \
    "v_readfirstlane_b32 s69, v192\n\t" \
    "v_readfirstlane_b32 s70, v193\n\t" \
    "s_nop 1\n\t" \
    "s_mov_b32 s78, s69\n\t" \
    "s_mov_b32 s77, s70\n\t" \
    "rr_K19_%=:\n\t" \
    "s_add_u32 s69, s65, 0\n\t" \
    "s_bitcmp1_b32 s69, 0\n\t" \
    "s_cselect_b32 s70, s63, s62\n\t" \
    "s_bfe_u32 s69, s69, 0x20001\n\t" \
    "s_lshl_b32 s69, s69, 19\n\t" \
    "s_add_u32 s69, s69, s70\n\t" \
    "s_add_u32 s46, s36, s69\n\t" \
    "s_addc_u32 s47, s37, 0\n\t" \
    "s_add_u32 s69, s65, 1\n\t" \
    "s_bitcmp1_b32 s69, 0\n\t" \
    "s_cselect_b32 s70, s63, s62\n\t" \
    "s_bfe_u32 s69, s69, 0x20001\n\t" \
    "s_lshl_b32 s69, s69, 19\n\t" \
    "s_add_u32 s69, s69, s70\n\t" \
    "s_add_u32 s48, s36, s69\n\t" \
    "s_addc_u32 s49, s37, 0\n\t" \
    "s_mul_i32 s70, s67, 7\n\t" \
    "s_add_u32 s69, s65, 0\n\t" \
    "s_xor_b32 s69, s69, s70\n\t" \
    "s_and_b32 s69, s69, 63\n\t" \
    "s_lshl_b32 s69, s69, 15\n\t" \
    "s_mul_i32 s70, s67, 2097152\n\t" \
    "s_add_u32 s69, s69, s70\n\t" \
    "s_add_u32 s69, s69, 48234496\n\t" \
    "s_add_u32 s38, s36, s69\n\t" \
    "s_addc_u32 s39, s37, 0\n\t" \
    "s_add_u32 s69, s65, 0\n\t" \
    "s_mul_i32 s70, s69, 171\n\t" \
    "s_lshr_b32 s70, s70, 9\n\t" \
    "s_mul_i32 s70, s70, 3\n\t" \
    "s_sub_u32 s69, s69, s70\n\t" \
    "s_mul_i32 s69, s69, 36\n\t" \
    "s_add_u32 s69, s69, s67\n\t" \
    "s_lshl_b32 s69, s69, 14\n\t" \
    "s_add_u32 s69, s69, s64\n\t" \
    "s_add_u32 s69, s69, 4194304\n\t" \
    "s_add_u32 s40, s36, s69\n\t" \
    "s_addc_u32 s41, s37, 0\n\t" \
    "s_mov_b32 s91, 0\n\t" \
    "s_mov_b32 s96, 0x55555555\n\t" \
    "s_mov_b32 s58, 0\n\t" \
    "s_mov_b32 s59, -1\n\t" \
    "v_mov_b32 v160, 0\n\t" \
    "v_mov_b32 v161, 0\n\t" \
    "v_mov_b32 v162, 0\n\t" \
    "v_mov_b32 v163, 0\n\t" \
    "rr_pg20_%=:\n\t" \
    "v_cmp_eq_u32 vcc, -1, v189\n\t" \
    "s_cbranch_vccz rr_ph24_%=\n\t" \
    "s_sleep 10\n\t" \
    "global_load_dword v189, v187, s[52:53] sc1\n\t" \
    "s_mov_b32 s71, 3\n\t" \
    "rr_E25_%=:\n\t" \
    "s_memtime s[54:55]\n\t" \
    "s_nop 0\n\t" \
    "s_memrealtime s[4:5]\n\t" \
    "s_nop 0\n\t" \
    "s_memrealtime s[6:7]\n\t" \
    "s_nop 0\n\t" \
    "s_memrealtime s[8:9]\n\t" \
    "s_nop 0\n\t" \
    "s_memrealtime s[10:11]\n\t" \
    "s_nop 0\n\t" \
    "s_memrealtime s[12:13]\n\t" \
    "s_nop 0\n\t" \
    "s_memrealtime s[14:15]\n\t" \
    "s_nop 0\n\t" \
    "s_memrealtime s[16:17]\n\t" \
    "s_nop 0\n\t" \
    "s_memrealtime s[18:19]\n\t" \
    "s_nop 0\n\t" \
    "s_memrealtime s[20:21]\n\t" \
    "s_nop 0\n\t" \
    "s_memrealtime s[22:23]\n\t" \
    "s_nop 0\n\t" \
    "s_memrealtime s[24:25]\n\t" \
    "s_nop 0\n\t" \
    "s_memrealtime s[26:27]\n\t" \
    "s_nop 0\n\t" \
    "s_memrealtime s[28:29]\n\t" \
    "s_nop 0\n\t" \
    "s_memtime s[56:57]\n\t" \
    "s_mov_b32 s70, 1\n\t" \
    "s_sub_u32 s71, s71, 1\n\t" \
    "s_cselect_b32 s69, 0, -1\n\t" \
    "s_waitcnt lgkmcnt(0)\n\t" \
    "s_sub_u32 s54, s56, s54\n\t" \
    "s_and_b32 s54, s54, s69\n\t" \
    "s_cmp_gt_u32 s54, 124\n\t" \
    "s_cbranch_scc1 rr_E25_%=\n\t" \
    "s_cmp_eq_u32 s4, s28\n\t" \
    "s_addc_u32 s70, s70, 0\n\t" \
    "s_cmp_eq_u32 s6, s28\n\t" \
    "s_addc_u32 s70, s70, 0\n\t" \
    "s_cmp_eq_u32 s8, s28\n\t" \
    "s_addc_u32 s70, s70, 0\n\t" \
    "s_cmp_eq_u32 s10, s28\n\t" \
    "s_addc_u32 s70, s70, 0\n\t" \
    "s_cmp_eq_u32 s12, s28\n\t" \
    "s_addc_u32 s70, s70, 0\n\t" \
    "s_cmp_eq_u32 s14, s28\n\t" \
    "s_addc_u32 s70, s70, 0\n\t" \
    "s_cmp_eq_u32 s16, s28\n\t" \
    "s_addc_u32 s70, s70, 0\n\t" \
    "s_cmp_eq_u32 s18, s28\n\t" \
    "s_addc_u32 s70, s70, 0\n\t" \
    "s_cmp_eq_u32 s20, s28\n\t" \
    "s_addc_u32 s70, s70, 0\n\t" \
    "s_cmp_eq_u32 s22, s28\n\t" \
    "s_addc_u32 s70, s70, 0\n\t" \
    "s_cmp_eq_u32 s24, s28\n\t" \
    "s_addc_u32 s70, s70, 0\n\t" \
    "s_cmp_eq_u32 s26, s28\n\t" \
    "s_addc_u32 s70, s70, 0\n\t" \
    "s_mov_b32 s75, s73\n\t" \
    "s_mov_b32 s76, s74\n\t" \
    "s_lshl_b32 s70, s70, 3\n\t" \
    "s_sub_u32 s73, s56, s70\n\t" \
    "s_lshl_b32 s74, s28, 4\n\t" \
    "s_waitcnt vmcnt(0)\n\t" \
    "rr_ph24_%=:\n\t" \
    "v_cmp_ne_u32 s[34:35], -1, v189\n\t" \
    "s_memtime s[56:57]\n\t" \
    "s_waitcnt lgkmcnt(0)\n\t" \
    "s_sub_u32 s69, s56, s73\n\t" \
    "s_mul_hi_u32 s69, s69, s78\n\t" \
    "s_add_u32 s69, s69, s74\n\t" \
    "s_add_u32 s69, s69, 1280\n\t" \
    "v_sub_u32 v184, s69, v189\n\t" \
    "v_cmp_le_i32 vcc, 0, v184\n\t" \
    "s_nop 1\n\t" \
    "s_and_b64 s[54:55], vcc, s[34:35]\n\t" \
    "s_and_b64 s[54:55], s[54:55], s[58:59]\n\t" \
    "s_cbranch_scc0 rr_pn21_%=\n\t" \
    "s_mov_b64 exec, s[54:55]\n\t" \
    "global_load_dword v177, v6, s[36:37] sc1\n\t" \
    "s_mov_b64 exec, -1\n\t" \
    "s_waitcnt vmcnt(0)\n\t" \
    "v_cmp_le_i32 vcc, s65, v177\n\t" \
    "s_nop 1\n\t" \
    "s_andn2_b32 s59, s59, vcc_hi\n\t" \
    "s_lshr_b32 s69, vcc_hi, 1\n\t" \
    "s_and_b32 s69, s69, vcc_hi\n\t" \
    "s_and_b32 s54, s69, s96\n\t" \
    "s_andn2_b32 s96, s96, s54\n\t" \
    "s_mov_b32 s55, s54\n\t" \
    "s_lshr_b64 s[54:55], s[54:55], s50\n\t" \
    "s_bitcmp1_b32 s54, 0\n\t" \
    "s_cbranch_scc0 rr_pk27_%=\n\t" \
    "global_load_dwordx4 a[0:3], v26, s[38:39] offset:0 sc1\n\t" \
    "global_load_dwordx4 a[4:7], v26, s[38:39] offset:1024 sc1\n\t" \
    "rr_pk27_%=:\n\t" \
    "s_bitcmp1_b32 s54, 2\n\t" \
    "s_cbranch_scc0 rr_pk28_%=\n\t" \
    "global_load_dwordx4 a[8:11], v27, s[38:39] offset:0 sc1\n\t" \
    "global_load_dwordx4 a[12:15], v27, s[38:39] offset:1024 sc1\n\t" \
    "rr_pk28_%=:\n\t" \
    "s_bitcmp1_b32 s54, 4\n\t" \
    "s_cbranch_scc0 rr_pk29_%=\n\t" \
    "global_load_dwordx4 a[16:19], v28, s[38:39] offset:0 sc1\n\t" \
    "global_load_dwordx4 a[20:23], v28, s[38:39] offset:1024 sc1\n\t" \
    "rr_pk29_%=:\n\t" \
    "s_bitcmp1_b32 s54, 6\n\t" \
    "s_cbranch_scc0 rr_pk30_%=\n\t" \
    "global_load_dwordx4 a[24:27], v29, s[38:39] offset:0 sc1\n\t" \
    "global_load_dwordx4 a[28:31], v29, s[38:39] offset:1024 sc1\n\t" \
    "rr_pk30_%=:\n\t" \
    "s_bitcmp1_b32 s54, 8\n\t" \
    "s_cbranch_scc0 rr_pk31_%=\n\t" \
    "global_load_dwordx4 a[32:35], v30, s[38:39] offset:0 sc1\n\t" \
    "global_load_dwordx4 a[36:39], v30, s[38:39] offset:1024 sc1\n\t" \
    "rr_pk31_%=:\n\t" \
    "s_bitcmp1_b32 s54, 10\n\t" \
    "s_cbranch_scc0 rr_pk32_%=\n\t" \
    "global_load_dwordx4 a[40:43], v31, s[38:39] offset:0 sc1\n\t" \
    "global_load_dwordx4 a[44:47], v31, s[38:39] offset:1024 sc1\n\t" \
    "rr_pk32_%=:\n\t" \
    "s_bitcmp1_b32 s54, 12\n\t" \
    "s_cbranch_scc0 rr_pk33_%=\n\t" \
    "global_load_dwordx4 a[48:51], v32, s[38:39] offset:0 sc1\n\t" \
    "global_load_dwordx4 a[52:55], v32, s[38:39] offset:1024 sc1\n\t" \
    "rr_pk33_%=:\n\t" \
    "s_bitcmp1_b32 s54, 14\n\t" \
    "s_cbranch_scc0 rr_pk34_%=\n\t" \
    "global_load_dwordx4 a[56:59], v33, s[38:39] offset:0 sc1\n\t" \
    "global_load_dwordx4 a[60:63], v33, s[38:39] offset:1024 sc1\n\t" \
    "rr_pk34_%=:\n\t" \
    "s_bitcmp1_b32 s54, 16\n\t" \
    "s_cbranch_scc0 rr_pk35_%=\n\t" \
    "global_load_dwordx4 a[64:67], v34, s[38:39] offset:0 sc1\n\t" \
    "global_load_dwordx4 a[68:71], v34, s[38:39] offset:1024 sc1\n\t" \
    "rr_pk35_%=:\n\t" \
    "s_bitcmp1_b32 s54, 18\n\t" \
    "s_cbranch_scc0 rr_pk36_%=\n\t" \
    "global_load_dwordx4 a[72:75], v35, s[38:39] offset:0 sc1\n\t" \
    "global_load_dwordx4 a[76:79], v35, s[38:39] offset:1024 sc1\n\t" \
    "rr_pk36_%=:\n\t" \
    "s_bitcmp1_b32 s54, 20\n\t" \
    "s_cbranch_scc0 rr_pk37_%=\n\t" \
    "global_load_dwordx4 a[80:83], v36, s[38:39] offset:0 sc1\n\t" \
    "global_load_dwordx4 a[84:87], v36, s[38:39] offset:1024 sc1\n\t" \
    "rr_pk37_%=:\n\t" \
    "s_bitcmp1_b32 s54, 22\n\t" \
    "s_cbranch_scc0 rr_pk38_%=\n\t" \
    "global_load_dwordx4 a[88:91], v37, s[38:39] offset:0 sc1\n\t" \
    "global_load_dwordx4 a[92:95], v37, s[38:39] offset:1024 sc1\n\t" \
    "rr_pk38_%=:\n\t" \
    "s_bitcmp1_b32 s54, 24\n\t" \
    "s_cbranch_scc0 rr_pk39_%=\n\t" \
    "global_load_dwordx4 a[96:99], v38, s[38:39] offset:0 sc1\n\t" \
    "global_load_dwordx4 a[100:103], v38, s[38:39] offset:1024 sc1\n\t" \
    "rr_pk39_%=:\n\t" \
    "s_bitcmp1_b32 s54, 26\n\t" \
    "s_cbranch_scc0 rr_pk40_%=\n\t" \
    "global_load_dwordx4 a[104:107], v39, s[38:39] offset:0 sc1\n\t" \
    "global_load_dwordx4 a[108:111], v39, s[38:39] offset:1024 sc1\n\t" \
    "rr_pk40_%=:\n\t" \
    "s_bitcmp1_b32 s54, 28\n\t" \
    "s_cbranch_scc0 rr_pk41_%=\n\t" \
    "global_load_dwordx4 a[112:115], v40, s[38:39] offset:0 sc1\n\t" \
    "global_load_dwordx4 a[116:119], v40, s[38:39] offset:1024 sc1\n\t" \
    "rr_pk41_%=:\n\t" \
    "s_bitcmp1_b32 s54, 30\n\t" \
    "s_cbranch_scc0 rr_pk42_%=\n\t" \
    "global_load_dwordx4 a[120:123], v41, s[38:39] offset:0 sc1\n\t" \
    "global_load_dwordx4 a[124:127], v41, s[38:39] offset:1024 sc1\n\t" \
    "rr_pk42_%=:\n\t" \
    "rr_pn21_%=:\n\t" \
    "s_orn2_b32 s69, s96, s34\n\t" \
    "s_cbranch_scc0 rr_pe22_%=\n\t" \
    "s_cmp_eq_u32 s68, 0\n\t" \
    "s_cbranch_scc1 rr_F43_%=\n\t" \
    "s_sub_u32 s68, s68, 1\n\t" \
    "s_branch rr_pg20_%=\n\t" \
    "rr_F43_%=:\n\t" \
    "v_mov_b32 v184, 6095104\n\t" \
    "v_mov_b32 v185, 10\n\t" \
    "global_store_dword v184, v185, s[36:37] sc1\n\t" \
    "s_waitcnt vmcnt(0)\n\t" \
    "s_branch rr_pe22_%=\n\t" \
    "rr_pe22_%=:\n\t" \
    "s_mov_b32 s83, s65\n\t" \
    "v_mov_b32 v184, v189\n\t" \
    "v_subrev_u32 v184, s80, v184\n\t" \
    "s_nop 1\n\t" \
    "v_max_i32_dpp v184, v184, v184 row_ror:1 row_mask:0xf bank_mask:0xf\n\t" \
    "s_nop 1\n\t" \
    "v_max_i32_dpp v184, v184, v184 row_ror:2 row_mask:0xf bank_mask:0xf\n\t" \
    "s_nop 1\n\t" \
    "v_max_i32_dpp v184, v184, v184 row_ror:4 row_mask:0xf bank_mask:0xf\n\t" \
    "s_nop 1\n\t" \
    "v_max_i32_dpp v184, v184, v184 row_ror:8 row_mask:0xf bank_mask:0xf\n\t" \
    "s_nop 0\n\t" \
    "v_readlane_b32 s69, v184, 0\n\t" \
    "v_readlane_b32 s71, v184, 16\n\t" \
    "v_readlane_b32 s70, v184, 32\n\t" \
    "v_readlane_b32 s81, v184, 48\n\t" \
    "s_max_i32 s69, s69, s71\n\t" \
    "s_nop 0\n\t" \
    "s_max_i32 s71, s70, s81\n\t" \
    "s_max_i32 s69, s69, s71\n\t" \
    "s_add_u32 s81, s69, s80\n\t" \
    "s_cmp_lg_u32 s65, 0\n\t" \
    "s_cbranch_scc1 rr_u245_%=\n\t" \
    "s_waitcnt vmcnt(0)\n\t" \
    "global_load_dword v177, v6, s[36:37] sc1\n\t" \
    "v_mov_b32 v184, v189\n\t" \
    "v_subrev_u32 v184, s80, v184\n\t" \
    "s_nop 1\n\t" \
    "v_max_i32_dpp v184, v184, v184 row_ror:1 row_mask:0xf bank_mask:0xf\n\t" \
    "s_nop 1\n\t" \
    "v_max_i32_dpp v184, v184, v184 row_ror:2 row_mask:0xf bank_mask:0xf\n\t" \
    "s_nop 1\n\t" \
    "v_max_i32_dpp v184, v184, v184 row_ror:4 row_mask:0xf bank_mask:0xf\n\t" \
    "s_nop 1\n\t" \
    "v_max_i32_dpp v184, v184, v184 row_ror:8 row_mask:0xf bank_mask:0xf\n\t" \
    "s_nop 0\n\t" \
    "v_readlane_b32 s69, v184, 0\n\t" \
    "v_readlane_b32 s71, v184, 16\n\t" \
    "s_nop 1\n\t" \
    "s_max_i32 s69, s69, s71\n\t" \
    "s_add_u32 s81, s69, s80\n\t" \
    "s_memtime s[56:57]\n\t" \
    "s_waitcnt lgkmcnt(0)\n\t" \
    "s_sub_u32 s69, s56, s73\n\t" \
    "s_mul_hi_u32 s69, s69, s78\n\t" \
    "s_add_u32 s69, s69, s74\n\t" \
    "s_add_u32 s69, s69, 480\n\t" \
    "s_sub_u32 s69, s69, s81\n\t" \
    "s_max_i32 s69, s69, 0\n\t" \
    "s_add_u32 s69, s69, s81\n\t" \
    "v_and_b32_e64 v184, -2, s69\n\t" \
    "global_store_dword v3, v184, s[46:47] sc1\n\t" \
    "s_branch rr_h044_%=\n\t" \
    "rr_pf13_%=:\n\t" \
    "s_cmp_gt_u32 s65, 62\n\t" \
    "s_cbranch_scc1 rr_end8_%=\n\t" \
    "v_min_i32_dpp v177, v177, v177 row_ror:1 row_mask:0xf bank_mask:0xf\n\t" \
    "s_nop 1\n\t" \
    "v_min_i32_dpp v177, v177, v177 row_ror:2 row_mask:0xf bank_mask:0xf\n\t" \
    "s_nop 1\n\t" \
    "v_min_i32_dpp v177, v177, v177 row_ror:4 row_mask:0xf bank_mask:0xf\n\t" \
    "s_nop 1\n\t" \
    "v_min_i32_dpp v177, v177, v177 row_ror:8 row_mask:0xf bank_mask:0xf\n\t" \
    "s_nop 0\n\t" \
    "v_readlane_b32 s69, v177, 0\n\t" \
    "v_readlane_b32 s70, v177, 16\n\t" \
    "s_nop 1\n\t" \
    "s_min_i32 s69, s69, s70\n\t" \
    "s_max_i32 s83, s83, s69\n\t" \
    "rr_WF46_%=:\n\t" \
    "s_cmp_ge_i32 s83, s65\n\t" \
    "s_cbranch_scc1 rr_WFok47_%=\n\t" \
    "global_load_dword v184, v6, s[36:37] sc1\n\t" \
    "s_waitcnt vmcnt(0)\n\t" \
    "v_min_i32_dpp v184, v184, v184 row_ror:1 row_mask:0xf bank_mask:0xf\n\t" \
    "s_nop 1\n\t" \
    "v_min_i32_dpp v184, v184, v184 row_ror:2 row_mask:0xf bank_mask:0xf\n\t" \
    "s_nop 1\n\t" \
    "v_min_i32_dpp v184, v184, v184 row_ror:4 row_mask:0xf bank_mask:0xf\n\t" \
    "s_nop 1\n\t" \
    "v_min_i32_dpp v184, v184, v184 row_ror:8 row_mask:0xf bank_mask:0xf\n\t" \
    "s_nop 0\n\t" \
    "v_readlane_b32 s69, v184, 0\n\t" \
    "v_readlane_b32 s70, v184, 16\n\t" \
    "s_nop 1\n\t" \
    "s_min_i32 s83, s69, s70\n\t" \
    "s_cmp_ge_i32 s83, s65\n\t" \
    "s_cbranch_scc1 rr_WFok47_%=\n\t" \
    "s_cmp_eq_u32 s68, 0\n\t" \
    "s_cbranch_scc1 rr_F48_%=\n\t" \
    "s_sub_u32 s68, s68, 1\n\t" \
    "s_sleep 2\n\t" \
    "s_branch rr_WF46_%=\n\t" \
    "rr_F48_%=:\n\t" \
    "v_mov_b32 v184, 6095104\n\t" \
    "v_mov_b32 v185, 10\n\t" \
    "global_store_dword v184, v185, s[36:37] sc1\n\t" \
    "s_waitcnt vmcnt(0)\n\t" \
    "s_branch rr_WFok47_%=\n\t" \
    "rr_WFok47_%=:\n\t" \
    "s_mul_i32 s70, s67, 7\n\t" \
    "s_add_u32 s69, s65, 0\n\t" \
    "s_xor_b32 s69, s69, s70\n\t" \
    "s_and_b32 s69, s69, 63\n\t" \
    "s_lshl_b32 s69, s69, 15\n\t" \
    "s_mul_i32 s70, s67, 2097152\n\t" \
    "s_add_u32 s69, s69, s70\n\t" \
    "s_add_u32 s69, s69, 48234496\n\t" \
    "s_add_u32 s38, s36, s69\n\t" \
    "s_addc_u32 s39, s37, 0\n\t" \
    "s_mul_hi_u32 s69, s85, s78\n\t" \
    "s_add_u32 s79, s81, s69\n\t" \
    "s_add_u32 s79, s79, s87\n\t" \
    "s_cmp_ge_u32 s65, 36\n\t" \
    "s_cselect_b32 s69, 28, 0\n\t" \
    "s_sub_u32 s79, s79, s69\n\t" \
    "s_add_u32 s80, s79, s88\n\t" \
    "s_add_u32 s79, s79, -96\n\t" \
    "s_add_u32 s79, s79, s42\n\t" \
    "s_sub_u32 s70, s79, s74\n\t" \
    "s_max_i32 s70, s70, 0\n\t" \
    "s_min_i32 s70, s70, 16000\n\t" \
    "s_lshl_b32 s70, s70, 1\n\t" \
    "s_mul_hi_u32 s70, s70, s77\n\t" \
    "s_add_u32 s70, s70, s73\n\t" \
    "rr_W49_%=:\n\t" \
    "s_memtime s[56:57]\n\t" \
    "s_waitcnt lgkmcnt(0)\n\t" \
    "s_sub_u32 s69, s70, s56\n\t" \
    "s_cmp_gt_i32 s69, 24\n\t" \
    "s_cbranch_scc1 rr_W49_%=\n\t" \
    "global_load_dwordx4 a[0:3], v26, s[38:39] offset:0 sc1\n\t" \
    "global_load_dwordx4 a[4:7], v26, s[38:39] offset:1024 sc1\n\t" \
    "global_load_dwordx4 a[8:11], v27, s[38:39] offset:0 sc1\n\t" \
    "global_load_dwordx4 a[12:15], v27, s[38:39] offset:1024 sc1\n\t" \
    "global_load_dwordx4 a[16:19], v28, s[38:39] offset:0 sc1\n\t" \
    "global_load_dwordx4 a[20:23], v28, s[38:39] offset:1024 sc1\n\t" \
    "global_load_dwordx4 a[24:27], v29, s[38:39] offset:0 sc1\n\t" \
    "global_load_dwordx4 a[28:31], v29, s[38:39] offset:1024 sc1\n\t" \
    "global_load_dwordx4 a[32:35], v30, s[38:39] offset:0 sc1\n\t" \
    "global_load_dwordx4 a[36:39], v30, s[38:39] offset:1024 sc1\n\t" \
    "global_load_dwordx4 a[40:43], v31, s[38:39] offset:0 sc1\n\t" \
    "global_load_dwordx4 a[44:47], v31, s[38:39] offset:1024 sc1\n\t" \
    "global_load_dwordx4 a[48:51], v32, s[38:39] offset:0 sc1\n\t" \
    "global_load_dwordx4 a[52:55], v32, s[38:39] offset:1024 sc1\n\t" \
    "global_load_dwordx4 a[56:59], v33, s[38:39] offset:0 sc1\n\t" \
    "global_load_dwordx4 a[60:63], v33, s[38:39] offset:1024 sc1\n\t" \
    "global_load_dwordx4 a[64:67], v34, s[38:39] offset:0 sc1\n\t" \
    "global_load_dwordx4 a[68:71], v34, s[38:39] offset:1024 sc1\n\t" \
    "global_load_dwordx4 a[72:75], v35, s[38:39] offset:0 sc1\n\t" \
    "global_load_dwordx4 a[76:79], v35, s[38:39] offset:1024 sc1\n\t" \
    "global_load_dwordx4 a[80:83], v36, s[38:39] offset:0 sc1\n\t" \
    "global_load_dwordx4 a[84:87], v36, s[38:39] offset:1024 sc1\n\t" \
    "global_load_dwordx4 a[88:91], v37, s[38:39] offset:0 sc1\n\t" \
    "global_load_dwordx4 a[92:95], v37, s[38:39] offset:1024 sc1\n\t" \
    "global_load_dwordx4 a[96:99], v38, s[38:39] offset:0 sc1\n\t" \
    "global_load_dwordx4 a[100:103], v38, s[38:39] offset:1024 sc1\n\t" \
    "global_load_dwordx4 a[104:107], v39, s[38:39] offset:0 sc1\n\t" \
    "global_load_dwordx4 a[108:111], v39, s[38:39] offset:1024 sc1\n\t" \
    "global_load_dwordx4 a[112:115], v40, s[38:39] offset:0 sc1\n\t" \
    "global_load_dwordx4 a[116:119], v40, s[38:39] offset:1024 sc1\n\t" \
    "global_load_dwordx4 a[120:123], v41, s[38:39] offset:0 sc1\n\t" \
    "global_load_dwordx4 a[124:127], v41, s[38:39] offset:1024 sc1\n\t" \
    "s_add_u32 s69, s65, 4\n\t" \
    "s_min_i32 s69, s69, 62\n\t" \
    "s_cmp_ge_i32 s83, s69\n\t" \
    "s_cbranch_scc1 rr_nfl50_%=\n\t" \
    "global_load_dword v177, v6, s[36:37] sc1\n\t" \
    "rr_nfl50_%=:\n\t" \
    "s_mul_hi_u32 s69, s85, s78\n\t" \
    "s_add_u32 s79, s81, s69\n\t" \
    "s_add_u32 s79, s79, s87\n\t" \
    "s_cmp_ge_u32 s65, 36\n\t" \
    "s_cselect_b32 s69, 28, 0\n\t" \
    "s_sub_u32 s79, s79, s69\n\t" \
    "s_add_u32 s80, s79, s88\n\t" \
    "s_add_u32 s79, s79, 320\n\t" \
    "s_add_u32 s79, s79, s43\n\t" \
    "s_sub_u32 s70, s79, s74\n\t" \
    "s_max_i32 s70, s70, 0\n\t" \
    "s_min_i32 s70, s70, 16000\n\t" \
    "s_lshl_b32 s70, s70, 1\n\t" \
    "s_mul_hi_u32 s70, s70, s77\n\t" \
    "s_add_u32 s70, s70, s73\n\t" \
    "rr_W51_%=:\n\t" \
    "s_memtime s[56:57]\n\t" \
    "s_waitcnt lgkmcnt(0)\n\t" \
    "s_sub_u32 s69, s70, s56\n\t" \
    "s_cmp_gt_i32 s69, 24\n\t" \
    "s_cbranch_scc1 rr_W51_%=\n\t" \
    "s_cmp_gt_u32 s65, 61\n\t" \
    "s_cbranch_scc1 rr_arm52_%=\n\t" \
    "s_add_u32 s69, s65, 2\n\t" \
    "s_bitcmp1_b32 s69, 0\n\t" \
    "s_cselect_b32 s70, s63, s62\n\t" \
    "s_bfe_u32 s69, s69, 0x20001\n\t" \
    "s_lshl_b32 s69, s69, 19\n\t" \
    "s_add_u32 s69, s69, s70\n\t" \
    "s_add_u32 s46, s36, s69\n\t" \
    "s_addc_u32 s47, s37, 0\n\t" \
    "global_store_dwordx4 v164, v[172:175], s[46:47] sc1\n\t" \
    "global_store_dwordx4 v165, v[172:175], s[46:47] sc1\n\t" \
    "global_store_dwordx4 v166, v[172:175], s[46:47] sc1\n\t" \
    "rr_arm52_%=:\n\t" \
    "s_waitcnt vmcnt(0)\n\t" \
    "v_min_i32_dpp v177, v177, v177 row_ror:1 row_mask:0xf bank_mask:0xf\n\t" \
    "s_nop 1\n\t" \
    "v_min_i32_dpp v177, v177, v177 row_ror:2 row_mask:0xf bank_mask:0xf\n\t" \
    "s_nop 1\n\t" \
    "v_min_i32_dpp v177, v177, v177 row_ror:4 row_mask:0xf bank_mask:0xf\n\t" \
    "s_nop 1\n\t" \
    "v_min_i32_dpp v177, v177, v177 row_ror:8 row_mask:0xf bank_mask:0xf\n\t" \
    "s_nop 0\n\t" \
    "v_readlane_b32 s69, v177, 0\n\t" \
    "v_readlane_b32 s70, v177, 16\n\t" \
    "s_nop 1\n\t" \
    "s_min_i32 s69, s69, s70\n\t" \
    "s_max_i32 s83, s83, s69\n\t" \
    "s_cmp_lg_u32 s65, 40\n\t" \
    "s_cbranch_scc1 rr_f253_%=\n\t" \
    "s_cmp_lg_u32 s67, 35\n\t" \
    "s_cbranch_scc1 rr_f253_%=\n\t" \
    "v_add_u32 v185, 36864, v6\n\t" \
    "rr_f2l54_%=:\n\t" \
    "global_load_dword v186, v185, s[36:37] sc1\n\t" \
    "s_waitcnt vmcnt(0)\n\t" \
    "v_cmp_ne_u32 vcc, 1, v186\n\t" \
    "s_cbranch_vccz rr_f253_%=\n\t" \
    "s_cmp_eq_u32 s68, 0\n\t" \
    "s_cbranch_scc1 rr_F55_%=\n\t" \
    "s_sub_u32 s68, s68, 1\n\t" \
    "s_sleep 2\n\t" \
    "s_branch rr_f2l54_%=\n\t" \
    "rr_F55_%=:\n\t" \
    "v_mov_b32 v187, 6095104\n\t" \
    "v_mov_b32 v188, 11\n\t" \
    "global_store_dword v187, v188, s[36:37] sc1\n\t" \
    "s_waitcnt vmcnt(0)\n\t" \
    "s_branch rr_f253_%=\n\t" \
    "rr_f253_%=:\n\t" \
    "s_cmp_eq_u32 s51, 0\n\t" \
    "s_cbranch_scc1 rr_sl23_%=\n\t" \
    "s_add_u32 s69, s65, -1\n\t" \
    "s_bitcmp1_b32 s69, 0\n\t" \
    "s_cselect_b32 s70, s63, s62\n\t" \
    "s_bfe_u32 s69, s69, 0x20001\n\t" \
    "s_lshl_b32 s69, s69, 19\n\t" \
    "s_add_u32 s69, s69, s70\n\t" \
    "s_add_u32 s46, s36, s69\n\t" \
    "s_addc_u32 s47, s37, 0\n\t" \
    "s_add_u32 s79, s80, 480\n\t" \
    "s_sub_u32 s70, s79, s74\n\t" \
    "s_max_i32 s70, s70, 0\n\t" \
    "s_min_i32 s70, s70, 16000\n\t" \
    "s_lshl_b32 s70, s70, 1\n\t" \
    "s_mul_hi_u32 s70, s70, s77\n\t" \
    "s_add_u32 s70, s70, s73\n\t" \
    "rr_W56_%=:\n\t" \
    "s_memtime s[56:57]\n\t" \
    "s_waitcnt lgkmcnt(0)\n\t" \
    "s_sub_u32 s69, s70, s56\n\t" \
    "s_cmp_gt_i32 s69, 24\n\t" \
    "s_cbranch_scc1 rr_W56_%=\n\t" \
    "v_add_u32 v187, 4096, v7\n\t" \
    "global_load_dword v184, v187, s[46:47] sc1\n\t" \
    "s_waitcnt vmcnt(0)\n\t" \
    "v_cmp_eq_u32 vcc, -1, v184\n\t" \
    "s_cbranch_vccz rr_Hok58_%=\n\t" \
    "rr_H57_%=:\n\t" \
    "global_load_dword v184, v187, s[46:47] sc1\n\t" \
    "s_waitcnt vmcnt(0)\n\t" \
    "v_cmp_eq_u32 vcc, -1, v184\n\t" \
    "s_cbranch_vccz rr_Hc59_%=\n\t" \
    "s_cmp_eq_u32 s68, 0\n\t" \
    "s_cbranch_scc1 rr_F60_%=\n\t" \
    "s_sub_u32 s68, s68, 1\n\t" \
    "s_sleep 4\n\t" \
    "s_branch rr_H57_%=\n\t" \
    "rr_F60_%=:\n\t" \
    "v_mov_b32 v185, 6095104\n\t" \
    "v_mov_b32 v186, 19\n\t" \
    "global_store_dword v185, v186, s[36:37] sc1\n\t" \
    "s_waitcnt vmcnt(0)\n\t" \
    "s_branch rr_Hc59_%=\n\t" \
    "rr_Hc59_%=:\n\t" \
    "rr_Hok58_%=:\n\t" \
    "v_subrev_u32 v184, s80, v184\n\t" \
    "s_nop 1\n\t" \
    "v_max_i32_dpp v184, v184, v184 row_ror:1 row_mask:0xf bank_mask:0xf\n\t" \
    "s_nop 1\n\t" \
    "v_max_i32_dpp v184, v184, v184 row_ror:2 row_mask:0xf bank_mask:0xf\n\t" \
    "s_nop 1\n\t" \
    "v_max_i32_dpp v184, v184, v184 row_ror:4 row_mask:0xf bank_mask:0xf\n\t" \
    "s_nop 1\n\t" \
    "v_max_i32_dpp v184, v184, v184 row_ror:8 row_mask:0xf bank_mask:0xf\n\t" \
    "s_nop 0\n\t" \
    "v_readlane_b32 s69, v184, 0\n\t" \
    "v_readlane_b32 s71, v184, 16\n\t" \
    "s_nop 1\n\t" \
    "s_max_i32 s69, s69, s71\n\t" \
    "s_add_u32 s81, s69, s80\n\t" \
    "s_branch rr_u245_%=\n\t" \
    "rr_sl23_%=:\n\t" \
    "ds_read_b64 v[184:185], v9\n\t" \
    "s_sub_u32 s70, s65, 1\n\t" \
    "s_waitcnt lgkmcnt(0)\n\t" \
    "v_readfirstlane_b32 s69, v185\n\t" \
    "v_readfirstlane_b32 s81, v184\n\t" \
    "s_nop 0\n\t" \
    "s_cmp_eq_u32 s69, s70\n\t" \
    "s_cbranch_scc1 rr_u245_%=\n\t" \
    "s_cmp_eq_u32 s68, 0\n\t" \
    "s_cbranch_scc1 rr_F61_%=\n\t" \
    "s_sub_u32 s68, s68, 1\n\t" \
    "s_sleep 2\n\t" \
    "s_branch rr_sl23_%=\n\t" \
    "rr_F61_%=:\n\t" \
    "v_mov_b32 v184, 6095104\n\t" \
    "v_mov_b32 v185, 14\n\t" \
    "global_store_dword v184, v185, s[36:37] sc1\n\t" \
    "s_waitcnt vmcnt(0)\n\t" \
    "s_branch rr_u245_%=\n\t" \
    "rr_u245_%=:\n\t" \
    "s_add_u32 s69, s65, -1\n\t" \
    "s_bitcmp1_b32 s69, 0\n\t" \
    "s_cselect_b32 s70, s63, s62\n\t" \
    "s_bfe_u32 s69, s69, 0x20001\n\t" \
    "s_lshl_b32 s69, s69, 19\n\t" \
    "s_add_u32 s69, s69, s70\n\t" \
    "s_add_u32 s46, s36, s69\n\t" \
    "s_addc_u32 s47, s37, 0\n\t" \
    "s_cmp_lg_u32 s65, 1\n\t" \
    "s_cbranch_scc1 rr_Oc64_%=\n\t" \
    "s_add_u32 s79, s81, 480\n\t" \
    "s_sub_u32 s70, s79, s74\n\t" \
    "s_max_i32 s70, s70, 0\n\t" \
    "s_min_i32 s70, s70, 16000\n\t" \
    "s_lshl_b32 s70, s70, 1\n\t" \
    "s_mul_hi_u32 s70, s70, s77\n\t" \
    "s_add_u32 s70, s70, s73\n\t" \
    "rr_W65_%=:\n\t" \
    "s_memtime s[56:57]\n\t" \
    "s_waitcnt lgkmcnt(0)\n\t" \
    "s_sub_u32 s69, s70, s56\n\t" \
    "s_cmp_gt_i32 s69, 24\n\t" \
    "s_cbranch_scc1 rr_W65_%=\n\t" \
    "global_load_dword v184, v46, s[46:47] sc1\n\t" \
    "s_waitcnt vmcnt(0)\n\t" \
    "v_cmp_eq_u32 vcc, -1, v184\n\t" \
    "s_cbranch_vccz rr_Ook63_%=\n\t" \
    "rr_O62_%=:\n\t" \
    "global_load_dword v184, v46, s[46:47] offset:1536 sc1\n\t" \
    "s_waitcnt vmcnt(0)\n\t" \
    "v_cmp_eq_u32 vcc, -1, v184\n\t" \
    "s_cbranch_vccz rr_Ook63_%=\n\t" \
    "s_cmp_eq_u32 s68, 0\n\t" \
    "s_cbranch_scc1 rr_F66_%=\n\t" \
    "s_sub_u32 s68, s68, 1\n\t" \
    "s_sleep 3\n\t" \
    "s_branch rr_O62_%=\n\t" \
    "rr_F66_%=:\n\t" \
    "v_mov_b32 v185, 6095104\n\t" \
    "v_mov_b32 v186, 18\n\t" \
    "global_store_dword v185, v186, s[36:37] sc1\n\t" \
    "s_waitcnt vmcnt(0)\n\t" \
    "s_branch rr_Ook63_%=\n\t" \
    "rr_Ook63_%=:\n\t" \
    "v_subrev_u32 v184, s80, v184\n\t" \
    "s_nop 1\n\t" \
    "v_max_i32_dpp v184, v184, v184 row_ror:1 row_mask:0xf bank_mask:0xf\n\t" \
    "s_nop 1\n\t" \
    "v_max_i32_dpp v184, v184, v184 row_ror:2 row_mask:0xf bank_mask:0xf\n\t" \
    "s_nop 1\n\t" \
    "v_max_i32_dpp v184, v184, v184 row_ror:4 row_mask:0xf bank_mask:0xf\n\t" \
    "s_nop 1\n\t" \
    "v_max_i32_dpp v184, v184, v184 row_ror:8 row_mask:0xf bank_mask:0xf\n\t" \
    "s_nop 0\n\t" \
    "v_readlane_b32 s69, v184, 0\n\t" \
    "v_readlane_b32 s71, v184, 16\n\t" \
    "v_readlane_b32 s70, v184, 32\n\t" \
    "v_readlane_b32 s81, v184, 48\n\t" \
    "s_max_i32 s69, s69, s71\n\t" \
    "s_nop 0\n\t" \
    "s_max_i32 s71, s70, s81\n\t" \
    "s_max_i32 s69, s69, s71\n\t" \
    "s_add_u32 s81, s69, s80\n\t" \
    "rr_Oc64_%=:\n\t" \
    "s_mul_hi_u32 s69, s85, s78\n\t" \
    "s_add_u32 s79, s81, s69\n\t" \
    "s_add_u32 s79, s79, s87\n\t" \
    "s_cmp_ge_u32 s65, 36\n\t" \
    "s_cselect_b32 s69, 28, 0\n\t" \
    "s_sub_u32 s79, s79, s69\n\t" \
    "s_add_u32 s80, s79, s88\n\t" \
    "s_sub_u32 s70, s79, s74\n\t" \
    "s_max_i32 s70, s70, 0\n\t" \
    "s_min_i32 s70, s70, 16000\n\t" \
    "s_lshl_b32 s70, s70, 1\n\t" \
    "s_mul_hi_u32 s70, s70, s77\n\t" \
    "s_add_u32 s70, s70, s73\n\t" \
    "rr_W67_%=:\n\t" \
    "s_memtime s[56:57]\n\t" \
    "s_waitcnt lgkmcnt(0)\n\t" \
    "s_sub_u32 s69, s70, s56\n\t" \
    "s_cmp_gt_i32 s69, 24\n\t" \
    "s_cbranch_scc1 rr_W67_%=\n\t" \
    "rr_act7_%=:\n\t" \
    "s_add_u32 s69, s65, 0\n\t" \
    "s_bitcmp1_b32 s69, 0\n\t" \
    "s_cselect_b32 s70, s63, s62\n\t" \
    "s_bfe_u32 s69, s69, 0x20001\n\t" \
    "s_lshl_b32 s69, s69, 19\n\t" \
    "s_add_u32 s69, s69, s70\n\t" \
    "s_add_u32 s46, s36, s69\n\t" \
    "s_addc_u32 s47, s37, 0\n\t" \
    "s_add_u32 s69, s65, 1\n\t" \
    "s_bitcmp1_b32 s69, 0\n\t" \
    "s_cselect_b32 s70, s63, s62\n\t" \
    "s_bfe_u32 s69, s69, 0x20001\n\t" \
    "s_lshl_b32 s69, s69, 19\n\t" \
    "s_add_u32 s69, s69, s70\n\t" \
    "s_add_u32 s48, s36, s69\n\t" \
    "s_addc_u32 s49, s37, 0\n\t" \
    "s_add_u32 s69, s65, 0\n\t" \
    "s_mul_i32 s70, s69, 171\n\t" \
    "s_lshr_b32 s70, s70, 9\n\t" \
    "s_mul_i32 s70, s70, 3\n\t" \
    "s_sub_u32 s69, s69, s70\n\t" \
    "s_mul_i32 s69, s69, 36\n\t" \
    "s_add_u32 s69, s69, s67\n\t" \
    "s_lshl_b32 s69, s69, 14\n\t" \
    "s_add_u32 s69, s69, s64\n\t" \
    "s_add_u32 s69, s69, 4194304\n\t" \
    "s_add_u32 s40, s36, s69\n\t" \
    "s_addc_u32 s41, s37, 0\n\t" \
    "s_add_u32 s69, s65, 2\n\t" \
    "s_cmp_ge_i32 s83, s69\n\t" \
    "s_cbranch_scc1 rr_nf68_%=\n\t" \
    "global_load_dword v177, v6, s[36:37] sc1\n\t" \
    "rr_nf68_%=:\n\t" \
    "s_mov_b32 s71, 3\n\t" \
    "rr_E69_%=:\n\t" \
    "s_memtime s[54:55]\n\t" \
    "s_nop 0\n\t" \
    "s_memrealtime s[4:5]\n\t" \
    "s_nop 0\n\t" \
    "s_memrealtime s[6:7]\n\t" \
    "s_nop 0\n\t" \
    "s_memrealtime s[8:9]\n\t" \
    "s_nop 0\n\t" \
    "s_memrealtime s[10:11]\n\t" \
    "s_nop 0\n\t" \
    "s_memrealtime s[12:13]\n\t" \
    "s_nop 0\n\t" \
    "s_memrealtime s[14:15]\n\t" \
    "s_nop 0\n\t" \
    "s_memrealtime s[16:17]\n\t" \
    "s_nop 0\n\t" \
    "s_memrealtime s[18:19]\n\t" \
    "s_nop 0\n\t" \
    "s_memrealtime s[20:21]\n\t" \
    "s_nop 0\n\t" \
    "s_memrealtime s[22:23]\n\t" \
    "s_nop 0\n\t" \
    "s_memrealtime s[24:25]\n\t" \
    "s_nop 0\n\t" \
    "s_memrealtime s[26:27]\n\t" \
    "s_nop 0\n\t" \
    "s_memrealtime s[28:29]\n\t" \
    "s_nop 0\n\t" \
    "s_memtime s[56:57]\n\t" \
    "s_mov_b32 s70, 1\n\t" \
    "s_sub_u32 s71, s71, 1\n\t" \
    "s_cselect_b32 s69, 0, -1\n\t" \
    "s_waitcnt lgkmcnt(0)\n\t" \
    "s_sub_u32 s54, s56, s54\n\t" \
    "s_and_b32 s54, s54, s69\n\t" \
    "s_cmp_gt_u32 s54, 124\n\t" \
    "s_cbranch_scc1 rr_E69_%=\n\t" \
    "s_cmp_eq_u32 s4, s28\n\t" \
    "s_addc_u32 s70, s70, 0\n\t" \
    "s_cmp_eq_u32 s6, s28\n\t" \
    "s_addc_u32 s70, s70, 0\n\t" \
    "s_cmp_eq_u32 s8, s28\n\t" \
    "s_addc_u32 s70, s70, 0\n\t" \
    "s_cmp_eq_u32 s10, s28\n\t" \
    "s_addc_u32 s70, s70, 0\n\t" \
    "s_cmp_eq_u32 s12, s28\n\t" \
    "s_addc_u32 s70, s70, 0\n\t" \
    "s_cmp_eq_u32 s14, s28\n\t" \
    "s_addc_u32 s70, s70, 0\n\t" \
    "s_cmp_eq_u32 s16, s28\n\t" \
    "s_addc_u32 s70, s70, 0\n\t" \
    "s_cmp_eq_u32 s18, s28\n\t" \
    "s_addc_u32 s70, s70, 0\n\t" \
    "s_cmp_eq_u32 s20, s28\n\t" \
    "s_addc_u32 s70, s70, 0\n\t" \
    "s_cmp_eq_u32 s22, s28\n\t" \
    "s_addc_u32 s70, s70, 0\n\t" \
    "s_cmp_eq_u32 s24, s28\n\t" \
    "s_addc_u32 s70, s70, 0\n\t" \
    "s_cmp_eq_u32 s26, s28\n\t" \
    "s_addc_u32 s70, s70, 0\n\t" \
    "s_mov_b32 s75, s73\n\t" \
    "s_mov_b32 s76, s74\n\t" \
    "s_lshl_b32 s70, s70, 3\n\t" \
    "s_sub_u32 s73, s56, s70\n\t" \
    "s_lshl_b32 s74, s28, 4\n\t" \
    "s_sub_u32 s69, s73, s75\n\t" \
    "s_sub_u32 s70, s74, s76\n\t" \
    "s_sub_u32 s71, s70, 1600\n\t" \
    "s_cmp_lt_u32 s71, 400000\n\t" \
    "s_cbranch_scc0 rr_K71_%=\n\t" \
    "v_cvt_f32_u32 v184, s69\n\t" \
    "v_cvt_f32_u32 v185, s70\n\t" \
    "v_rcp_f32 v186, v184\n\t" \
    "v_rcp_f32 v187, v185\n\t" \
    "v_mul_f32 v186, v186, v185\n\t" \
    "v_mul_f32 v187, v187, v184\n\t" \
    "v_mul_f32 v186, 0x4f800000, v186\n\t" \
    "v_mul_f32 v187, 0x4f000000, v187\n\t" \
    "v_cvt_u32_f32 v186, v186\n\t" \
    "v_cvt_u32_f32 v187, v187\n\t" \
    "v_readfirstlane_b32 s69, v186\n\t" \
    "v_readfirstlane_b32 s70, v187\n\t" \
    "s_nop 1\n\t" \
    "s_sub_u32 s69, s69, s78\n\t" \
    "s_ashr_i32 s69, s69, 2\n\t" \
    "s_add_u32 s78, s78, s69\n\t" \
    "s_sub_u32 s70, s70, s77\n\t" \
    "s_ashr_i32 s70, s70, 2\n\t" \
    "s_add_u32 s77, s77, s70\n\t" \
    "rr_K71_%=:\n\t" \
    "s_mov_b32 s91, 0\n\t" \
    "v_mov_b32 v160, 0\n\t" \
    "v_mov_b32 v161, 0\n\t" \
    "v_mov_b32 v162, 0\n\t" \
    "v_mov_b32 v163, 0\n\t" \
    "s_sub_u32 s70, s80, s74\n\t" \
    "s_max_i32 s70, s70, 0\n\t" \
    "s_min_i32 s70, s70, 16000\n\t" \
    "s_lshl_b32 s70, s70, 1\n\t" \
    "s_mul_hi_u32 s70, s70, s77\n\t" \
    "s_add_u32 s70, s70, s73\n\t" \
    "s_sub_u32 s70, s70, 100\n\t" \
    "s_getpc_b64 s[30:31]\n\t" \
    "rr_Wp78_%=:\n\t" \
    "s_add_u32 s30, s30, rr_We79_%=-rr_Wp78_%=\n\t" \
    "s_addc_u32 s31, s31, 0\n\t" \
    "rr_W77_%=:\n\t" \
    "s_memtime s[56:57]\n\t" \
    "s_waitcnt lgkmcnt(0)\n\t" \
    "s_sub_u32 s69, s70, s56\n\t" \
    "s_cmp_gt_i32 s69, 111\n\t" \
    "s_cbranch_scc1 rr_W77_%=\n\t" \
    "s_max_i32 s69, s69, 0\n\t" \
    "s_lshr_b32 s69, s69, 1\n\t" \
    "s_and_b32 s69, s69, 0x3c\n\t" \
    "s_sub_u32 s30, s30, s69\n\t" \
    "s_subb_u32 s31, s31, 0\n\t" \
    "s_setpc_b64 s[30:31]\n\t" \
    ".p2align 6\n\t" \
    "s_nop 0\n\t" \
    "s_nop 0\n\t" \
    "s_nop 0\n\t" \
    "s_nop 1\n\t" \
    "s_nop 1\n\t" \
    "s_nop 1\n\t" \
    "s_nop 1\n\t" \
    "s_nop 1\n\t" \
    "s_nop 1\n\t" \
    "s_nop 1\n\t" \
    "s_nop 1\n\t" \
    "s_nop 1\n\t" \
    "s_nop 1\n\t" \
    "s_nop 1\n\t" \
    "s_nop 1\n\t" \
    "s_nop 1\n\t" \
    "rr_We79_%=:\n\t" \
    "global_load_dword v184, v7, s[46:47] sc1\n\t" \
    "s_sub_u32 s97, s74, s86\n\t" \
    "s_sub_u32 s71, s73, 190\n\t" \
    "s_getpc_b64 s[30:31]\n\t" \
    "rr_wp80_%=:\n\t" \
    "s_add_u32 s30, s30, rr_again9_%=-rr_wp80_%=\n\t" \
    "s_addc_u32 s31, s31, (rr_again9_%=-rr_wp80_%=)>>32\n\t" \
    "s_waitcnt vmcnt(0) lgkmcnt(0)\n\t" \
    "rr_hok72_%=:\n\t" \
    "s_memtime s[56:57]\n\t" \
    "v_cmp_eq_u32 vcc, -1, v184\n\t" \
    "v_subrev_u32 v185, s97, v184\n\t" \
    "s_nop 1\n\t" \
    "v_max_i32_dpp v185, v185, v185 row_ror:1 row_mask:0xf bank_mask:0xf\n\t" \
    "s_cbranch_vccnz rr_hlate73_%=\n\t" \
    "s_nop 0\n\t" \
    "v_max_i32_dpp v185, v185, v185 row_ror:2 row_mask:0xf bank_mask:0xf\n\t" \
    "s_nop 1\n\t" \
    "v_max_i32_dpp v185, v185, v185 row_ror:4 row_mask:0xf bank_mask:0xf\n\t" \
    "s_nop 1\n\t" \
    "v_max_i32_dpp v185, v185, v185 row_ror:8 row_mask:0xf bank_mask:0xf\n\t" \
    "s_nop 0\n\t" \
    "v_readlane_b32 s69, v185, 0\n\t" \
    "v_readlane_b32 s70, v185, 16\n\t" \
    "s_waitcnt lgkmcnt(0)\n\t" \
    "s_sub_u32 s71, s71, s56\n\t" \
    "s_max_i32 s81, s69, s70\n\t" \
    "s_max_i32 s69, s81, 0\n\t" \
    "s_lshl_b32 s69, s69, 1\n\t" \
    "s_mul_hi_u32 s69, s69, s77\n\t" \
    "s_add_u32 s69, s69, s71\n\t" \
    "s_cmp_gt_i32 s69, 111\n\t" \
    "s_cbranch_scc1 rr_wl74_%=\n\t" \
    "rr_wf76_%=:\n\t" \
    "s_max_i32 s69, s69, 0\n\t" \
    "s_lshr_b32 s69, s69, 1\n\t" \
    "s_and_b32 s69, s69, 0x3c\n\t" \
    "s_sub_u32 s30, s30, s69\n\t" \
    "s_subb_u32 s31, s31, 0\n\t" \
    "s_setpc_b64 s[30:31]\n\t" \
    "rr_wl74_%=:\n\t" \
    "s_min_i32 s69, s69, 24000\n\t" \
    "s_add_u32 s70, s69, s56\n\t" \
    "s_add_u32 s70, s70, 62\n\t" \
    "rr_wp75_%=:\n\t" \
    "s_memtime s[56:57]\n\t" \
    "s_waitcnt lgkmcnt(0)\n\t" \
    "s_sub_u32 s69, s70, s56\n\t" \
    "s_cmp_gt_i32 s69, 111\n\t" \
    "s_cbranch_scc1 rr_wp75_%=\n\t" \
    "s_branch rr_wf76_%=\n\t" \
    ".p2align 6\n\t" \
    "s_nop 0\n\t" \
    "s_nop 0\n\t" \
    "s_nop 0\n\t" \
    "s_nop 1\n\t" \
    "s_nop 1\n\t" \
    "s_nop 1\n\t" \
    "s_nop 1\n\t" \
    "s_nop 1\n\t" \
    "s_nop 1\n\t" \
    "s_nop 1\n\t" \
    "s_nop 1\n\t" \
    "s_nop 1\n\t" \
    "s_nop 1\n\t" \
    "s_nop 1\n\t" \
    "s_nop 1\n\t" \
    "s_nop 1\n\t" \
    "rr_again9_%=:\n\t" \
    "global_load_dwordx4 v[48:51], v10, s[46:47] sc1\n\t" \
    "global_load_dwordx4 v[52:55], v11, s[46:47] sc1\n\t" \
    "global_load_dwordx4 v[56:59], v12, s[46:47] sc1\n\t" \
    "global_load_dwordx4 v[60:63], v13, s[46:47] sc1\n\t" \
    "global_load_dwordx4 v[64:67], v14, s[46:47] sc1\n\t" \
    "global_load_dwordx4 v[68:71], v15, s[46:47] sc1\n\t" \
    "global_load_dwordx4 v[72:75], v16, s[46:47] sc1\n\t" \
    "global_load_dwordx4 v[76:79], v17, s[46:47] sc1\n\t" \
    "global_load_dwordx4 v[80:83], v18, s[46:47] sc1\n\t" \
    "global_load_dwordx4 v[84:87], v19, s[46:47] sc1\n\t" \
    "global_load_dwordx4 v[88:91], v20, s[46:47] sc1\n\t" \
    "global_load_dwordx4 v[92:95], v21, s[46:47] sc1\n\t" \
    "global_load_dwordx4 v[96:99], v22, s[46:47] sc1\n\t" \
    "global_load_dwordx4 v[100:103], v23, s[46:47] sc1\n\t" \
    "global_load_dwordx4 v[104:107], v24, s[46:47] sc1\n\t" \
    "global_load_dwordx4 v[108:111], v25, s[46:47] sc1\n\t" \
    "global_load_dwordx4 v[112:115], v42, s[46:47] sc1\n\t" \
    "global_load_dwordx4 v[116:119], v43, s[46:47] sc1\n\t" \
    "global_load_dwordx4 v[120:123], v44, s[46:47] sc1\n\t" \
    "global_load_dwordx4 v[124:127], v45, s[46:47] sc1\n\t" \
    "global_load_dwordx4 v[128:131], v0, s[40:41] offset:0 sc1\n\t" \
    "global_load_dwordx4 v[132:135], v0, s[40:41] offset:1024 sc1\n\t" \
    "global_load_dwordx4 v[136:139], v0, s[40:41] offset:2048 sc1\n\t" \
    "global_load_dwordx4 v[140:143], v0, s[40:41] offset:3072 sc1\n\t" \
    "global_load_dwordx4 v[144:147], v1, s[40:41] offset:0 sc1\n\t" \
    "global_load_dwordx4 v[148:151], v1, s[40:41] offset:1024 sc1\n\t" \
    "global_load_dwordx4 v[152:155], v1, s[40:41] offset:2048 sc1\n\t" \
    "global_load_dwordx4 v[156:159], v1, s[40:41] offset:3072 sc1\n\t" \
    "s_cmp_lg_u32 s91, 0\n\t" \
    "s_cbranch_scc1 rr_nob12_%=\n\t" \
    "s_add_u32 s81, s81, s97\n\t" \
    "v_mov_b32 v184, s81\n\t" \
    "v_mov_b32 v185, s65\n\t" \
    "ds_write_b64 v9, v[184:185]\n\t" \
    "rr_nob12_%=:\n\t" \
    "s_waitcnt vmcnt(27)\n\t" \
    "v_smfmac_f32_16x16x64_bf16 v[160:163], v[48:51], a[0:7], v4\n\t" \
    "s_waitcnt vmcnt(26)\n\t" \
    "v_smfmac_f32_16x16x64_bf16 v[160:163], v[52:55], a[8:15], v4\n\t" \
    "s_waitcnt vmcnt(25)\n\t" \
    "v_smfmac_f32_16x16x64_bf16 v[160:163], v[56:59], a[16:23], v4\n\t" \
    "s_waitcnt vmcnt(24)\n\t" \
    "v_smfmac_f32_16x16x64_bf16 v[160:163], v[60:63], a[24:31], v4\n\t" \
    "s_waitcnt vmcnt(23)\n\t" \
    "v_smfmac_f32_16x16x64_bf16 v[160:163], v[64:67], a[32:39], v4\n\t" \
    "s_waitcnt vmcnt(22)\n\t" \
    "v_smfmac_f32_16x16x64_bf16 v[160:163], v[68:71], a[40:47], v4\n\t" \
    "s_waitcnt vmcnt(21)\n\t" \
    "v_smfmac_f32_16x16x64_bf16 v[160:163], v[72:75], a[48:55], v4\n\t" \
    "s_waitcnt vmcnt(20)\n\t" \
    "v_smfmac_f32_16x16x64_bf16 v[160:163], v[76:79], a[56:63], v4\n\t" \
    "s_waitcnt vmcnt(19)\n\t" \
    "v_smfmac_f32_16x16x64_bf16 v[160:163], v[80:83], a[64:71], v4\n\t" \
    "s_waitcnt vmcnt(18)\n\t" \
    "v_smfmac_f32_16x16x64_bf16 v[160:163], v[84:87], a[72:79], v4\n\t" \
    "s_waitcnt vmcnt(17)\n\t" \
    "v_smfmac_f32_16x16x64_bf16 v[160:163], v[88:91], a[80:87], v4\n\t" \
    "s_waitcnt vmcnt(16)\n\t" \
    "v_smfmac_f32_16x16x64_bf16 v[160:163], v[92:95], a[88:95], v4\n\t" \
    "s_waitcnt vmcnt(15)\n\t" \
    "v_smfmac_f32_16x16x64_bf16 v[160:163], v[96:99], a[96:103], v4\n\t" \
    "s_waitcnt vmcnt(14)\n\t" \
    "v_smfmac_f32_16x16x64_bf16 v[160:163], v[100:103], a[104:111], v4\n\t" \
    "s_waitcnt vmcnt(13)\n\t" \
    "v_smfmac_f32_16x16x64_bf16 v[160:163], v[104:107], a[112:119], v4\n\t" \
    "s_waitcnt vmcnt(12)\n\t" \
    "v_smfmac_f32_16x16x64_bf16 v[160:163], v[108:111], a[120:127], v4\n\t" \
    "s_waitcnt vmcnt(6)\n\t" \
    "v_smfmac_f32_16x16x64_bf16 v[160:163], v[112:115], v[128:135], v4\n\t" \
    "s_waitcnt vmcnt(4)\n\t" \
    "v_smfmac_f32_16x16x64_bf16 v[160:163], v[116:119], v[136:143], v4\n\t" \
    "s_waitcnt vmcnt(2)\n\t" \
    "v_smfmac_f32_16x16x64_bf16 v[160:163], v[120:123], v[144:151], v4\n\t" \
    "s_waitcnt vmcnt(0)\n\t" \
    "v_smfmac_f32_16x16x64_bf16 v[160:163], v[124:127], v[152:159], v4\n\t" \
    "s_nop 7\n\t" \
    "v_pk_add_f32 v[160:161], v[160:161], v[162:163]\n\t" \
    "v_cvt_pk_bf16_f32 v184, v160, v161\n\t" \
    "v_cmp_u_f32 vcc, v160, v161\n\t" \
    "s_cbranch_vccnz rr_bad10_%=\n\t" \
    "rr_cont11_%=:\n\t" \
    "v_mov_b32_dpp v185, v184 quad_perm:[1,0,3,2] row_mask:0xf bank_mask:0xf\n\t" \
    "v_perm_b32 v184, v185, v184, v5\n\t" \
    "global_store_dword v2, v184, s[48:49] sc1\n\t" \
    "global_store_dword v181, v184, s[48:49] sc1\n\t" \
    "global_store_dword v182, v184, s[48:49] sc1\n\t" \
    "global_store_dword v183, v184, s[48:49] sc1\n\t" \
    "s_cmp_eq_u32 s65, 62\n\t" \
    "s_cbranch_scc1 rr_end8_%=\n\t" \
    "s_add_u32 s65, s65, 2\n\t" \
    "s_branch rr_pf13_%=\n\t" \
    "rr_bad10_%=:\n\t" \
    "s_waitcnt lgkmcnt(0)\n\t" \
    "v_max_u32 v185, v48, v49\n\t" \
    "v_max3_u32 v185, v185, v50, v51\n\t" \
    "v_max3_u32 v185, v185, v52, v53\n\t" \
    "v_max3_u32 v185, v185, v54, v55\n\t" \
    "v_max3_u32 v185, v185, v56, v57\n\t" \
    "v_max3_u32 v185, v185, v58, v59\n\t" \
    "v_max3_u32 v185, v185, v60, v61\n\t" \
    "v_max3_u32 v185, v185, v62, v63\n\t" \
    "v_max3_u32 v185, v185, v64, v65\n\t" \
    "v_max3_u32 v185, v185, v66, v67\n\t" \
    "v_max3_u32 v185, v185, v68, v69\n\t" \
    "v_max3_u32 v185, v185, v70, v71\n\t" \
    "v_max3_u32 v185, v185, v72, v73\n\t" \
    "v_max3_u32 v185, v185, v74, v75\n\t" \
    "v_max3_u32 v185, v185, v76, v77\n\t" \
    "v_max3_u32 v185, v185, v78, v79\n\t" \
    "v_max3_u32 v185, v185, v80, v81\n\t" \
    "v_max3_u32 v185, v185, v82, v83\n\t" \
    "v_max3_u32 v185, v185, v84, v85\n\t" \
    "v_max3_u32 v185, v185, v86, v87\n\t" \
    "v_max3_u32 v185, v185, v88, v89\n\t" \
    "v_max3_u32 v185, v185, v90, v91\n\t" \
    "v_max3_u32 v185, v185, v92, v93\n\t" \
    "v_max3_u32 v185, v185, v94, v95\n\t" \
    "v_max3_u32 v185, v185, v96, v97\n\t" \
    "v_max3_u32 v185, v185, v98, v99\n\t" \
    "v_max3_u32 v185, v185, v100, v101\n\t" \
    "v_max3_u32 v185, v185, v102, v103\n\t" \
    "v_max3_u32 v185, v185, v104, v105\n\t" \
    "v_max3_u32 v185, v185, v106, v107\n\t" \
    "v_max3_u32 v185, v185, v108, v109\n\t" \
    "v_max3_u32 v185, v185, v110, v111\n\t" \
    "v_max3_u32 v185, v185, v112, v113\n\t" \
    "v_max3_u32 v185, v185, v114, v115\n\t" \
    "v_max3_u32 v185, v185, v116, v117\n\t" \
    "v_max3_u32 v185, v185, v118, v119\n\t" \
    "v_max3_u32 v185, v185, v120, v121\n\t" \
    "v_max3_u32 v185, v185, v122, v123\n\t" \
    "v_max3_u32 v185, v185, v124, v125\n\t" \
    "v_max3_u32 v185, v185, v126, v127\n\t" \
    "v_max_u32 v186, v128, v129\n\t" \
    "v_max3_u32 v186, v186, v130, v131\n\t" \
    "v_max3_u32 v186, v186, v132, v133\n\t" \
    "v_max3_u32 v186, v186, v134, v135\n\t" \
    "v_max3_u32 v186, v186, v136, v137\n\t" \
    "v_max3_u32 v186, v186, v138, v139\n\t" \
    "v_max3_u32 v186, v186, v140, v141\n\t" \
    "v_max3_u32 v186, v186, v142, v143\n\t" \
    "v_max3_u32 v186, v186, v144, v145\n\t" \
    "v_max3_u32 v186, v186, v146, v147\n\t" \
    "v_max3_u32 v186, v186, v148, v149\n\t" \
    "v_max3_u32 v186, v186, v150, v151\n\t" \
    "v_max3_u32 v186, v186, v152, v153\n\t" \
    "v_max3_u32 v186, v186, v154, v155\n\t" \
    "v_max3_u32 v186, v186, v156, v157\n\t" \
    "v_max3_u32 v186, v186, v158, v159\n\t" \
    "v_max_u32 v185, v185, v186\n\t" \
    "v_cmp_eq_u32 vcc, -1, v185\n\t" \
    "s_cbranch_vccz rr_cont11_%=\n\t" \
    "s_mov_b32 s91, 1\n\t" \
    "v_mov_b32 v160, 0\n\t" \
    "v_mov_b32 v161, 0\n\t" \
    "v_mov_b32 v162, 0\n\t" \
    "v_mov_b32 v163, 0\n\t" \
    "s_cmp_eq_u32 s68, 0\n\t" \
    "s_cbranch_scc1 rr_F81_%=\n\t" \
    "s_sub_u32 s68, s68, 1\n\t" \
    "s_branch rr_again9_%=\n\t" \
    "rr_F81_%=:\n\t" \
    "v_mov_b32 v186, 6095104\n\t" \
    "v_mov_b32 v187, 3\n\t" \
    "global_store_dword v186, v187, s[36:37] sc1\n\t" \
    "s_waitcnt vmcnt(0)\n\t" \
    "s_branch rr_cont11_%=\n\t" \
    "rr_hlate73_%=:\n\t" \
    "s_waitcnt lgkmcnt(0)\n\t" \
    "v_add_u32 v189, 1024, v7\n\t" \
    "global_load_dword v186, v189, s[46:47] sc1\n\t" \
    "s_sleep 4\n\t" \
    "v_add_u32 v189, 2048, v7\n\t" \
    "global_load_dword v187, v189, s[46:47] sc1\n\t" \
    "s_waitcnt vmcnt(1)\n\t" \
    "v_cmp_eq_u32 vcc, -1, v186\n\t" \
    "s_cbranch_vccz rr_hg82_%=\n\t" \
    "v_add_u32 v189, 3072, v7\n\t" \
    "global_load_dword v186, v189, s[46:47] sc1\n\t" \
    "s_waitcnt vmcnt(1)\n\t" \
    "v_cmp_eq_u32 vcc, -1, v187\n\t" \
    "s_cbranch_vccz rr_hg83_%=\n\t" \
    "s_waitcnt vmcnt(0)\n\t" \
    "v_cmp_eq_u32 vcc, -1, v186\n\t" \
    "s_cbranch_vccz rr_hg82_%=\n\t" \
    "rr_hp84_%=:\n\t" \
    "global_load_dword v184, v189, s[46:47] sc1\n\t" \
    "s_waitcnt vmcnt(0)\n\t" \
    "v_cmp_eq_u32 vcc, -1, v184\n\t" \
    "s_cbranch_vccz rr_hpd85_%=\n\t" \
    "s_cmp_eq_u32 s68, 0\n\t" \
    "s_cbranch_scc1 rr_F86_%=\n\t" \
    "s_sub_u32 s68, s68, 1\n\t" \
    "s_sleep 1\n\t" \
    "s_branch rr_hp84_%=\n\t" \
    "rr_F86_%=:\n\t" \
    "v_mov_b32 v186, 6095104\n\t" \
    "v_mov_b32 v187, 7\n\t" \
    "global_store_dword v186, v187, s[36:37] sc1\n\t" \
    "s_waitcnt vmcnt(0)\n\t" \
    "s_branch rr_hpd85_%=\n\t" \
    "rr_hpd85_%=:\n\t" \
    "rr_h044_%=:\n\t" \
    "v_and_b32 v184, -2, v184\n\t" \
    "s_mov_b32 s71, 3\n\t" \
    "rr_E87_%=:\n\t" \
    "s_memtime s[54:55]\n\t" \
    "s_nop 0\n\t" \
    "s_memrealtime s[4:5]\n\t" \
    "s_nop 0\n\t" \
    "s_memrealtime s[6:7]\n\t" \
    "s_nop 0\n\t" \
    "s_memrealtime s[8:9]\n\t" \
    "s_nop 0\n\t" \
    "s_memrealtime s[10:11]\n\t" \
    "s_nop 0\n\t" \
    "s_memrealtime s[12:13]\n\t" \
    "s_nop 0\n\t" \
    "s_memrealtime s[14:15]\n\t" \
    "s_nop 0\n\t" \
    "s_memrealtime s[16:17]\n\t" \
    "s_nop 0\n\t" \
    "s_memrealtime s[18:19]\n\t" \
    "s_nop 0\n\t" \
    "s_memrealtime s[20:21]\n\t" \
    "s_nop 0\n\t" \
    "s_memrealtime s[22:23]\n\t" \
    "s_nop 0\n\t" \
    "s_memrealtime s[24:25]\n\t" \
    "s_nop 0\n\t" \
    "s_memrealtime s[26:27]\n\t" \
    "s_nop 0\n\t" \
    "s_memrealtime s[28:29]\n\t" \
    "s_nop 0\n\t" \
    "s_memtime s[56:57]\n\t" \
    "s_mov_b32 s70, 1\n\t" \
    "s_sub_u32 s71, s71, 1\n\t" \
    "s_cselect_b32 s69, 0, -1\n\t" \
    "s_waitcnt lgkmcnt(0)\n\t" \
    "s_sub_u32 s54, s56, s54\n\t" \
    "s_and_b32 s54, s54, s69\n\t" \
    "s_cmp_gt_u32 s54, 124\n\t" \
    "s_cbranch_scc1 rr_E87_%=\n\t" \
    "s_cmp_eq_u32 s4, s28\n\t" \
    "s_addc_u32 s70, s70, 0\n\t" \
    "s_cmp_eq_u32 s6, s28\n\t" \
    "s_addc_u32 s70, s70, 0\n\t" \
    "s_cmp_eq_u32 s8, s28\n\t" \
    "s_addc_u32 s70, s70, 0\n\t" \
    "s_cmp_eq_u32 s10, s28\n\t" \
    "s_addc_u32 s70, s70, 0\n\t" \
    "s_cmp_eq_u32 s12, s28\n\t" \
    "s_addc_u32 s70, s70, 0\n\t" \
    "s_cmp_eq_u32 s14, s28\n\t" \
    "s_addc_u32 s70, s70, 0\n\t" \
    "s_cmp_eq_u32 s16, s28\n\t" \
    "s_addc_u32 s70, s70, 0\n\t" \
    "s_cmp_eq_u32 s18, s28\n\t" \
    "s_addc_u32 s70, s70, 0\n\t" \
    "s_cmp_eq_u32 s20, s28\n\t" \
    "s_addc_u32 s70, s70, 0\n\t" \
    "s_cmp_eq_u32 s22, s28\n\t" \
    "s_addc_u32 s70, s70, 0\n\t" \
    "s_cmp_eq_u32 s24, s28\n\t" \
    "s_addc_u32 s70, s70, 0\n\t" \
    "s_cmp_eq_u32 s26, s28\n\t" \
    "s_addc_u32 s70, s70, 0\n\t" \
    "s_mov_b32 s75, s73\n\t" \
    "s_mov_b32 s76, s74\n\t" \
    "s_lshl_b32 s70, s70, 3\n\t" \
    "s_sub_u32 s73, s56, s70\n\t" \
    "s_lshl_b32 s74, s28, 4\n\t" \
    "s_sub_u32 s97, s74, s86\n\t" \
    "s_sub_u32 s71, s73, 190\n\t" \
    "s_getpc_b64 s[30:31]\n\t" \
    "rr_wp89_%=:\n\t" \
    "s_add_u32 s30, s30, rr_again9_%=-rr_wp89_%=\n\t" \
    "s_addc_u32 s31, s31, (rr_again9_%=-rr_wp89_%=)>>32\n\t" \
    "s_branch rr_hok72_%=\n\t" \
    "rr_hg82_%=:\n\t" \
    "v_mov_b32 v184, v186\n\t" \
    "s_branch rr_hok72_%=\n\t" \
    "rr_hg83_%=:\n\t" \
    "v_mov_b32 v184, v187\n\t" \
    "s_branch rr_hok72_%=\n\t" \
    "rr_end8_%=:\n\t" \
    "s_waitcnt vmcnt(0) lgkmcnt(0)\n\t" \
    "s_endpgm\n\t" \
    "rr_last_%=:\n\t" \
    ""
#define ASM_O \
    "s_mov_b64 s[36:37], %0\n\t" \
    "s_mov_b64 s[42:43], %1\n\t" \
    "s_mov_b64 s[44:45], %2\n\t" \
    "s_mov_b32 s66, %3\n\t" \
    "s_mov_b32 s67, %4\n\t" \
    "s_mov_b64 s[50:51], %5\n\t" \
    "s_mov_b32 s82, %7\n\t" \
    "v_mbcnt_lo_u32_b32 v223, -1, 0\n\t" \
    "v_mbcnt_hi_u32_b32 v223, -1, v223\n\t" \
    "s_getpc_b64 s[46:47]\n\t" \
    "oo_pc_%=:\n\t" \
    "s_lshl_b32 s69, s66, 13\n\t" \
    "v_lshl_add_u32 v222, v223, 7, s69\n\t" \
    "v_min_u32 v222, oo_last_%=-oo_pc_%=-4, v222\n\t" \
    "global_load_dword v222, v222, s[46:47]\n\t" \
    "v_lshlrev_b32 v223, 2, v223\n\t" \
    "v_add_u32 v223, s82, v223\n\t" \
    "ds_read_b32 v0, v223 offset:0\n\t" \
    "ds_read_b32 v1, v223 offset:256\n\t" \
    "ds_read_b32 v2, v223 offset:512\n\t" \
    "ds_read_b32 v3, v223 offset:768\n\t" \
    "ds_read_b32 v4, v223 offset:1024\n\t" \
    "ds_read_b32 v5, v223 offset:1280\n\t" \
    "ds_read_b32 v6, v223 offset:1536\n\t" \
    "ds_read_b32 v7, v223 offset:1792\n\t" \
    "ds_read_b32 v8, v223 offset:2048\n\t" \
    "ds_read_b32 v9, v223 offset:2304\n\t" \
    "ds_read_b32 v10, v223 offset:2560\n\t" \
    "ds_read_b32 v11, v223 offset:2816\n\t" \
    "s_waitcnt lgkmcnt(0)\n\t" \
    "ds_read_b32 v12, v223 offset:3072\n\t" \
    "ds_read_b32 v13, v223 offset:3328\n\t" \
    "ds_read_b32 v14, v223 offset:3584\n\t" \
    "ds_read_b32 v15, v223 offset:3840\n\t" \
    "ds_read_b32 v16, v223 offset:4096\n\t" \
    "ds_read_b32 v17, v223 offset:4352\n\t" \
    "s_waitcnt lgkmcnt(0)\n\t" \
    "s_mov_b32 s68, 262144\n\t" \
    "s_mov_b32 s72, 0\n\t" \
    "s_mov_b32 s83, -1\n\t" \
    "s_mov_b32 s65, 0\n\t" \
    "s_barrier\n\t" \
    "s_mov_b32 s65, s67\n\t" \
    "s_mov_b32 s96, s67\n\t" \
    "s_add_u32 s38, s36, 48234496\n\t" \
    "s_addc_u32 s39, s37, 0\n\t" \
    "oo_poll1_%=:\n\t" \
    "s_cmp_gt_u32 s96, 28\n\t" \
    "s_cselect_b64 vcc, -1, 0\n\t" \
    "v_cndmask_b32 v25, v12, v13, vcc\n\t" \
    "global_load_dword v24, v25, s[36:37] sc1\n\t" \
    "s_waitcnt vmcnt(0)\n\t" \
    "v_cmp_gt_i32 vcc, s96, v24\n\t" \
    "s_cbranch_vccz oo_go2_%=\n\t" \
    "s_cmp_eq_u32 s68, 0\n\t" \
    "s_cbranch_scc1 oo_F7_%=\n\t" \
    "s_sub_u32 s68, s68, 1\n\t" \
    "s_sleep 10\n\t" \
    "s_branch oo_poll1_%=\n\t" \
    "oo_F7_%=:\n\t" \
    "v_mov_b32 v25, 6095104\n\t" \
    "v_mov_b32 v26, 40\n\t" \
    "global_store_dword v25, v26, s[36:37] sc1\n\t" \
    "s_waitcnt vmcnt(0)\n\t" \
    "s_branch oo_go2_%=\n\t" \
    "oo_go2_%=:\n\t" \
    "s_mul_i32 s69, s96, 65536\n\t" \
    "s_add_u32 s69, s69, 146800640\n\t" \
    "s_add_u32 s40, s36, s69\n\t" \
    "s_addc_u32 s41, s37, 0\n\t" \
    "s_lshl_b32 s69, s96, 15\n\t" \
    "v_xor_b32 v18, s69, v0\n\t" \
    "v_xor_b32 v19, s69, v1\n\t" \
    "v_xor_b32 v20, s69, v2\n\t" \
    "v_xor_b32 v21, s69, v3\n\t" \
    "v_xor_b32 v22, s69, v4\n\t" \
    "v_xor_b32 v23, s69, v5\n\t" \
    "global_load_dwordx4 v[32:35], v6, s[40:41] offset:-4096 sc1\n\t" \
    "global_load_dwordx4 v[36:39], v6, s[40:41] offset:-3072 sc1\n\t" \
    "global_load_dwordx4 a[0:3], v18, s[38:39] offset:-4096 sc1\n\t" \
    "global_load_dwordx4 a[4:7], v19, s[38:39] offset:-4096 sc1\n\t" \
    "global_load_dwordx4 a[8:11], v20, s[38:39] offset:-4096 sc1\n\t" \
    "global_load_dwordx4 a[12:15], v21, s[38:39] offset:-4096 sc1\n\t" \
    "global_load_dwordx4 a[16:19], v22, s[38:39] offset:-4096 sc1\n\t" \
    "global_load_dwordx4 a[20:23], v23, s[38:39] offset:-4096 sc1\n\t" \
    "global_load_dwordx4 v[40:43], v6, s[40:41] offset:-2048 sc1\n\t" \
    "global_load_dwordx4 v[44:47], v6, s[40:41] offset:-1024 sc1\n\t" \
    "global_load_dwordx4 a[24:27], v18, s[38:39] offset:-3072 sc1\n\t" \
    "global_load_dwordx4 a[28:31], v19, s[38:39] offset:-3072 sc1\n\t" \
    "global_load_dwordx4 a[32:35], v20, s[38:39] offset:-3072 sc1\n\t" \
    "global_load_dwordx4 a[36:39], v21, s[38:39] offset:-3072 sc1\n\t" \
    "global_load_dwordx4 a[40:43], v22, s[38:39] offset:-3072 sc1\n\t" \
    "global_load_dwordx4 a[44:47], v23, s[38:39] offset:-3072 sc1\n\t" \
    "global_load_dwordx4 v[48:51], v6, s[40:41] offset:0 sc1\n\t" \
    "global_load_dwordx4 v[52:55], v6, s[40:41] offset:1024 sc1\n\t" \
    "global_load_dwordx4 a[48:51], v18, s[38:39] offset:-2048 sc1\n\t" \
    "global_load_dwordx4 a[52:55], v19, s[38:39] offset:-2048 sc1\n\t" \
    "global_load_dwordx4 a[56:59], v20, s[38:39] offset:-2048 sc1\n\t" \
    "global_load_dwordx4 a[60:63], v21, s[38:39] offset:-2048 sc1\n\t" \
    "global_load_dwordx4 a[64:67], v22, s[38:39] offset:-2048 sc1\n\t" \
    "global_load_dwordx4 a[68:71], v23, s[38:39] offset:-2048 sc1\n\t" \
    "global_load_dwordx4 v[56:59], v6, s[40:41] offset:2048 sc1\n\t" \
    "global_load_dwordx4 v[60:63], v6, s[40:41] offset:3072 sc1\n\t" \
    "global_load_dwordx4 a[72:75], v18, s[38:39] offset:-1024 sc1\n\t" \
    "global_load_dwordx4 a[76:79], v19, s[38:39] offset:-1024 sc1\n\t" \
    "global_load_dwordx4 a[80:83], v20, s[38:39] offset:-1024 sc1\n\t" \
    "global_load_dwordx4 a[84:87], v21, s[38:39] offset:-1024 sc1\n\t" \
    "global_load_dwordx4 a[88:91], v22, s[38:39] offset:-1024 sc1\n\t" \
    "global_load_dwordx4 a[92:95], v23, s[38:39] offset:-1024 sc1\n\t" \
    "oo_loop3_%=:\n\t" \
    "s_mul_i32 s69, s96, 16384\n\t" \
    "s_add_u32 s69, s69, 150994944\n\t" \
    "s_add_u32 s46, s36, s69\n\t" \
    "s_addc_u32 s47, s37, 0\n\t" \
    "s_mul_i32 s69, s96, 9216\n\t" \
    "s_add_u32 s69, s69, 0\n\t" \
    "s_add_u32 s48, s36, s69\n\t" \
    "s_addc_u32 s49, s37, 0\n\t" \
    "s_waitcnt vmcnt(29)\n\t" \
    "v_mfma_f32_16x16x32_bf16 v[72:75], a[0:3], v[32:35], 0\n\t" \
    "global_load_dwordx4 v[64:67], v7, s[40:41] offset:-4096 sc1\n\t" \
    "v_mfma_f32_16x16x32_bf16 v[76:79], a[0:3], v[36:39], 0\n\t" \
    "global_load_dwordx4 v[68:71], v7, s[40:41] offset:-3072 sc1\n\t" \
    "s_waitcnt vmcnt(30)\n\t" \
    "v_mfma_f32_16x16x32_bf16 v[80:83], a[4:7], v[32:35], 0\n\t" \
    "global_load_dwordx4 a[96:99], v18, s[38:39] offset:0 sc1\n\t" \
    "v_mfma_f32_16x16x32_bf16 v[84:87], a[4:7], v[36:39], 0\n\t" \
    "global_load_dwordx4 a[100:103], v19, s[38:39] offset:0 sc1\n\t" \
    "s_waitcnt vmcnt(31)\n\t" \
    "v_mfma_f32_16x16x32_bf16 v[88:91], a[8:11], v[32:35], 0\n\t" \
    "global_load_dwordx4 a[104:107], v20, s[38:39] offset:0 sc1\n\t" \
    "v_mfma_f32_16x16x32_bf16 v[92:95], a[8:11], v[36:39], 0\n\t" \
    "global_load_dwordx4 a[108:111], v21, s[38:39] offset:0 sc1\n\t" \
    "s_waitcnt vmcnt(32)\n\t" \
    "v_mfma_f32_16x16x32_bf16 v[96:99], a[12:15], v[32:35], 0\n\t" \
    "global_load_dwordx4 a[112:115], v22, s[38:39] offset:0 sc1\n\t" \
    "v_mfma_f32_16x16x32_bf16 v[100:103], a[12:15], v[36:39], 0\n\t" \
    "global_load_dwordx4 a[116:119], v23, s[38:39] offset:0 sc1\n\t" \
    "s_waitcnt vmcnt(33)\n\t" \
    "v_mfma_f32_16x16x32_bf16 v[104:107], a[16:19], v[32:35], 0\n\t" \
    "v_mfma_f32_16x16x32_bf16 v[108:111], a[16:19], v[36:39], 0\n\t" \
    "s_waitcnt vmcnt(32)\n\t" \
    "v_mfma_f32_16x16x32_bf16 v[112:115], a[20:23], v[32:35], 0\n\t" \
    "v_mfma_f32_16x16x32_bf16 v[116:119], a[20:23], v[36:39], 0\n\t" \
    "s_add_u32 s96, s96, 4\n\t" \
    "s_cmp_gt_u32 s96, 28\n\t" \
    "s_cselect_b64 vcc, -1, 0\n\t" \
    "v_cndmask_b32 v25, v12, v13, vcc\n\t" \
    "global_load_dword v24, v25, s[36:37] sc1\n\t" \
    "s_waitcnt vmcnt(30)\n\t" \
    "v_mfma_f32_16x16x32_bf16 v[72:75], a[24:27], v[40:43], v[72:75]\n\t" \
    "global_load_dwordx4 v[32:35], v7, s[40:41] offset:-2048 sc1\n\t" \
    "v_mfma_f32_16x16x32_bf16 v[76:79], a[24:27], v[44:47], v[76:79]\n\t" \
    "global_load_dwordx4 v[36:39], v7, s[40:41] offset:-1024 sc1\n\t" \
    "s_waitcnt vmcnt(31)\n\t" \
    "v_mfma_f32_16x16x32_bf16 v[80:83], a[28:31], v[40:43], v[80:83]\n\t" \
    "global_load_dwordx4 a[0:3], v18, s[38:39] offset:1024 sc1\n\t" \
    "v_mfma_f32_16x16x32_bf16 v[84:87], a[28:31], v[44:47], v[84:87]\n\t" \
    "global_load_dwordx4 a[4:7], v19, s[38:39] offset:1024 sc1\n\t" \
    "s_waitcnt vmcnt(32)\n\t" \
    "v_mfma_f32_16x16x32_bf16 v[88:91], a[32:35], v[40:43], v[88:91]\n\t" \
    "global_load_dwordx4 a[8:11], v20, s[38:39] offset:1024 sc1\n\t" \
    "v_mfma_f32_16x16x32_bf16 v[92:95], a[32:35], v[44:47], v[92:95]\n\t" \
    "global_load_dwordx4 a[12:15], v21, s[38:39] offset:1024 sc1\n\t" \
    "s_waitcnt vmcnt(33)\n\t" \
    "v_mfma_f32_16x16x32_bf16 v[96:99], a[36:39], v[40:43], v[96:99]\n\t" \
    "global_load_dwordx4 a[16:19], v22, s[38:39] offset:1024 sc1\n\t" \
    "v_mfma_f32_16x16x32_bf16 v[100:103], a[36:39], v[44:47], v[100:103]\n\t" \
    "global_load_dwordx4 a[20:23], v23, s[38:39] offset:1024 sc1\n\t" \
    "s_waitcnt vmcnt(34)\n\t" \
    "v_mfma_f32_16x16x32_bf16 v[104:107], a[40:43], v[40:43], v[104:107]\n\t" \
    "v_mfma_f32_16x16x32_bf16 v[108:111], a[40:43], v[44:47], v[108:111]\n\t" \
    "s_waitcnt vmcnt(33)\n\t" \
    "v_mfma_f32_16x16x32_bf16 v[112:115], a[44:47], v[40:43], v[112:115]\n\t" \
    "v_mfma_f32_16x16x32_bf16 v[116:119], a[44:47], v[44:47], v[116:119]\n\t" \
    "s_waitcnt vmcnt(30)\n\t" \
    "v_mfma_f32_16x16x32_bf16 v[72:75], a[48:51], v[48:51], v[72:75]\n\t" \
    "global_load_dwordx4 v[40:43], v7, s[40:41] offset:0 sc1\n\t" \
    "v_mfma_f32_16x16x32_bf16 v[76:79], a[48:51], v[52:55], v[76:79]\n\t" \
    "global_load_dwordx4 v[44:47], v7, s[40:41] offset:1024 sc1\n\t" \
    "s_waitcnt vmcnt(31)\n\t" \
    "v_mfma_f32_16x16x32_bf16 v[80:83], a[52:55], v[48:51], v[80:83]\n\t" \
    "global_load_dwordx4 a[24:27], v18, s[38:39] offset:2048 sc1\n\t" \
    "v_mfma_f32_16x16x32_bf16 v[84:87], a[52:55], v[52:55], v[84:87]\n\t" \
    "global_load_dwordx4 a[28:31], v19, s[38:39] offset:2048 sc1\n\t" \
    "s_waitcnt vmcnt(32)\n\t" \
    "v_mfma_f32_16x16x32_bf16 v[88:91], a[56:59], v[48:51], v[88:91]\n\t" \
    "global_load_dwordx4 a[32:35], v20, s[38:39] offset:2048 sc1\n\t" \
    "v_mfma_f32_16x16x32_bf16 v[92:95], a[56:59], v[52:55], v[92:95]\n\t" \
    "global_load_dwordx4 a[36:39], v21, s[38:39] offset:2048 sc1\n\t" \
    "s_waitcnt vmcnt(33)\n\t" \
    "v_mfma_f32_16x16x32_bf16 v[96:99], a[60:63], v[48:51], v[96:99]\n\t" \
    "global_load_dwordx4 a[40:43], v22, s[38:39] offset:2048 sc1\n\t" \
    "v_mfma_f32_16x16x32_bf16 v[100:103], a[60:63], v[52:55], v[100:103]\n\t" \
    "global_load_dwordx4 a[44:47], v23, s[38:39] offset:2048 sc1\n\t" \
    "s_waitcnt vmcnt(34)\n\t" \
    "v_mfma_f32_16x16x32_bf16 v[104:107], a[64:67], v[48:51], v[104:107]\n\t" \
    "v_mfma_f32_16x16x32_bf16 v[108:111], a[64:67], v[52:55], v[108:111]\n\t" \
    "s_waitcnt vmcnt(33)\n\t" \
    "v_mfma_f32_16x16x32_bf16 v[112:115], a[68:71], v[48:51], v[112:115]\n\t" \
    "v_mfma_f32_16x16x32_bf16 v[116:119], a[68:71], v[52:55], v[116:119]\n\t" \
    "s_waitcnt vmcnt(30)\n\t" \
    "v_mfma_f32_16x16x32_bf16 v[72:75], a[72:75], v[56:59], v[72:75]\n\t" \
    "global_load_dwordx4 v[48:51], v7, s[40:41] offset:2048 sc1\n\t" \
    "v_mfma_f32_16x16x32_bf16 v[76:79], a[72:75], v[60:63], v[76:79]\n\t" \
    "global_load_dwordx4 v[52:55], v7, s[40:41] offset:3072 sc1\n\t" \
    "s_waitcnt vmcnt(31)\n\t" \
    "v_mfma_f32_16x16x32_bf16 v[80:83], a[76:79], v[56:59], v[80:83]\n\t" \
    "global_load_dwordx4 a[48:51], v18, s[38:39] offset:3072 sc1\n\t" \
    "v_mfma_f32_16x16x32_bf16 v[84:87], a[76:79], v[60:63], v[84:87]\n\t" \
    "global_load_dwordx4 a[52:55], v19, s[38:39] offset:3072 sc1\n\t" \
    "s_waitcnt vmcnt(32)\n\t" \
    "v_mfma_f32_16x16x32_bf16 v[88:91], a[80:83], v[56:59], v[88:91]\n\t" \
    "global_load_dwordx4 a[56:59], v20, s[38:39] offset:3072 sc1\n\t" \
    "v_mfma_f32_16x16x32_bf16 v[92:95], a[80:83], v[60:63], v[92:95]\n\t" \
    "global_load_dwordx4 a[60:63], v21, s[38:39] offset:3072 sc1\n\t" \
    "s_waitcnt vmcnt(33)\n\t" \
    "v_mfma_f32_16x16x32_bf16 v[96:99], a[84:87], v[56:59], v[96:99]\n\t" \
    "global_load_dwordx4 a[64:67], v22, s[38:39] offset:3072 sc1\n\t" \
    "v_mfma_f32_16x16x32_bf16 v[100:103], a[84:87], v[60:63], v[100:103]\n\t" \
    "global_load_dwordx4 a[68:71], v23, s[38:39] offset:3072 sc1\n\t" \
    "s_waitcnt vmcnt(34)\n\t" \
    "v_mfma_f32_16x16x32_bf16 v[104:107], a[88:91], v[56:59], v[104:107]\n\t" \
    "v_mfma_f32_16x16x32_bf16 v[108:111], a[88:91], v[60:63], v[108:111]\n\t" \
    "s_waitcnt vmcnt(33)\n\t" \
    "v_mfma_f32_16x16x32_bf16 v[112:115], a[92:95], v[56:59], v[112:115]\n\t" \
    "v_mfma_f32_16x16x32_bf16 v[116:119], a[92:95], v[60:63], v[116:119]\n\t" \
    "s_waitcnt vmcnt(30)\n\t" \
    "v_mfma_f32_16x16x32_bf16 v[72:75], a[96:99], v[64:67], v[72:75]\n\t" \
    "global_load_dwordx4 v[56:59], v8, s[46:47] offset:0 sc1\n\t" \
    "v_mfma_f32_16x16x32_bf16 v[76:79], a[96:99], v[68:71], v[76:79]\n\t" \
    "global_load_dwordx4 v[60:63], v9, s[46:47] offset:0 sc1\n\t" \
    "s_waitcnt vmcnt(31)\n\t" \
    "v_mfma_f32_16x16x32_bf16 v[80:83], a[100:103], v[64:67], v[80:83]\n\t" \
    "global_load_dwordx4 a[72:75], v10, s[48:49] offset:0 sc1\n\t" \
    "v_mfma_f32_16x16x32_bf16 v[84:87], a[100:103], v[68:71], v[84:87]\n\t" \
    "global_load_dwordx4 a[76:79], v10, s[48:49] offset:256 sc1\n\t" \
    "s_waitcnt vmcnt(32)\n\t" \
    "v_mfma_f32_16x16x32_bf16 v[88:91], a[104:107], v[64:67], v[88:91]\n\t" \
    "global_load_dwordx4 a[80:83], v10, s[48:49] offset:512 sc1\n\t" \
    "v_mfma_f32_16x16x32_bf16 v[92:95], a[104:107], v[68:71], v[92:95]\n\t" \
    "global_load_dwordx4 a[84:87], v10, s[48:49] offset:768 sc1\n\t" \
    "s_waitcnt vmcnt(33)\n\t" \
    "v_mfma_f32_16x16x32_bf16 v[96:99], a[108:111], v[64:67], v[96:99]\n\t" \
    "global_load_dwordx4 a[88:91], v10, s[48:49] offset:1024 sc1\n\t" \
    "v_mfma_f32_16x16x32_bf16 v[100:103], a[108:111], v[68:71], v[100:103]\n\t" \
    "global_load_dwordx4 a[92:95], v10, s[48:49] offset:1280 sc1\n\t" \
    "s_waitcnt vmcnt(34)\n\t" \
    "v_mfma_f32_16x16x32_bf16 v[104:107], a[112:115], v[64:67], v[104:107]\n\t" \
    "v_mfma_f32_16x16x32_bf16 v[108:111], a[112:115], v[68:71], v[108:111]\n\t" \
    "s_waitcnt vmcnt(33)\n\t" \
    "v_mfma_f32_16x16x32_bf16 v[112:115], a[116:119], v[64:67], v[112:115]\n\t" \
    "v_mfma_f32_16x16x32_bf16 v[116:119], a[116:119], v[68:71], v[116:119]\n\t" \
    "s_waitcnt vmcnt(29)\n\t" \
    "v_mfma_f32_16x16x32_bf16 v[72:75], a[0:3], v[32:35], v[72:75]\n\t" \
    "global_load_dwordx4 v[64:67], v8, s[46:47] offset:1024 sc1\n\t" \
    "v_mfma_f32_16x16x32_bf16 v[76:79], a[0:3], v[36:39], v[76:79]\n\t" \
    "global_load_dwordx4 v[68:71], v9, s[46:47] offset:1024 sc1\n\t" \
    "s_waitcnt vmcnt(30)\n\t" \
    "v_mfma_f32_16x16x32_bf16 v[80:83], a[4:7], v[32:35], v[80:83]\n\t" \
    "global_load_dwordx4 a[96:99], v11, s[48:49] offset:0 sc1\n\t" \
    "v_mfma_f32_16x16x32_bf16 v[84:87], a[4:7], v[36:39], v[84:87]\n\t" \
    "global_load_dwordx4 a[100:103], v11, s[48:49] offset:256 sc1\n\t" \
    "s_waitcnt vmcnt(31)\n\t" \
    "v_mfma_f32_16x16x32_bf16 v[88:91], a[8:11], v[32:35], v[88:91]\n\t" \
    "global_load_dwordx4 a[104:107], v11, s[48:49] offset:512 sc1\n\t" \
    "v_mfma_f32_16x16x32_bf16 v[92:95], a[8:11], v[36:39], v[92:95]\n\t" \
    "global_load_dwordx4 a[108:111], v11, s[48:49] offset:768 sc1\n\t" \
    "s_waitcnt vmcnt(32)\n\t" \
    "v_mfma_f32_16x16x32_bf16 v[96:99], a[12:15], v[32:35], v[96:99]\n\t" \
    "global_load_dwordx4 a[112:115], v11, s[48:49] offset:1024 sc1\n\t" \
    "v_mfma_f32_16x16x32_bf16 v[100:103], a[12:15], v[36:39], v[100:103]\n\t" \
    "global_load_dwordx4 a[116:119], v11, s[48:49] offset:1280 sc1\n\t" \
    "s_waitcnt vmcnt(33)\n\t" \
    "v_mfma_f32_16x16x32_bf16 v[104:107], a[16:19], v[32:35], v[104:107]\n\t" \
    "v_mfma_f32_16x16x32_bf16 v[108:111], a[16:19], v[36:39], v[108:111]\n\t" \
    "s_waitcnt vmcnt(32)\n\t" \
    "v_mfma_f32_16x16x32_bf16 v[112:115], a[20:23], v[32:35], v[112:115]\n\t" \
    "v_mfma_f32_16x16x32_bf16 v[116:119], a[20:23], v[36:39], v[116:119]\n\t" \
    "v_cmp_gt_i32 vcc, s96, v24\n\t" \
    "s_cmp_ge_u32 s96, 58\n\t" \
    "s_cbranch_scc1 oo_stop4_%=\n\t" \
    "s_cbranch_vccnz oo_stop4_%=\n\t" \
    "s_mul_i32 s69, s96, 65536\n\t" \
    "s_add_u32 s69, s69, 146800640\n\t" \
    "s_add_u32 s40, s36, s69\n\t" \
    "s_addc_u32 s41, s37, 0\n\t" \
    "s_lshl_b32 s69, s96, 15\n\t" \
    "v_xor_b32 v18, s69, v0\n\t" \
    "v_xor_b32 v19, s69, v1\n\t" \
    "v_xor_b32 v20, s69, v2\n\t" \
    "v_xor_b32 v21, s69, v3\n\t" \
    "v_xor_b32 v22, s69, v4\n\t" \
    "v_xor_b32 v23, s69, v5\n\t" \
    "s_waitcnt vmcnt(29)\n\t" \
    "v_mfma_f32_16x16x32_bf16 v[72:75], a[24:27], v[40:43], v[72:75]\n\t" \
    "global_load_dwordx4 v[32:35], v6, s[40:41] offset:-4096 sc1\n\t" \
    "v_mfma_f32_16x16x32_bf16 v[76:79], a[24:27], v[44:47], v[76:79]\n\t" \
    "global_load_dwordx4 v[36:39], v6, s[40:41] offset:-3072 sc1\n\t" \
    "s_waitcnt vmcnt(30)\n\t" \
    "v_mfma_f32_16x16x32_bf16 v[80:83], a[28:31], v[40:43], v[80:83]\n\t" \
    "global_load_dwordx4 a[0:3], v18, s[38:39] offset:-4096 sc1\n\t" \
    "v_mfma_f32_16x16x32_bf16 v[84:87], a[28:31], v[44:47], v[84:87]\n\t" \
    "global_load_dwordx4 a[4:7], v19, s[38:39] offset:-4096 sc1\n\t" \
    "s_waitcnt vmcnt(31)\n\t" \
    "v_mfma_f32_16x16x32_bf16 v[88:91], a[32:35], v[40:43], v[88:91]\n\t" \
    "global_load_dwordx4 a[8:11], v20, s[38:39] offset:-4096 sc1\n\t" \
    "v_mfma_f32_16x16x32_bf16 v[92:95], a[32:35], v[44:47], v[92:95]\n\t" \
    "global_load_dwordx4 a[12:15], v21, s[38:39] offset:-4096 sc1\n\t" \
    "s_waitcnt vmcnt(32)\n\t" \
    "v_mfma_f32_16x16x32_bf16 v[96:99], a[36:39], v[40:43], v[96:99]\n\t" \
    "global_load_dwordx4 a[16:19], v22, s[38:39] offset:-4096 sc1\n\t" \
    "v_mfma_f32_16x16x32_bf16 v[100:103], a[36:39], v[44:47], v[100:103]\n\t" \
    "global_load_dwordx4 a[20:23], v23, s[38:39] offset:-4096 sc1\n\t" \
    "s_waitcnt vmcnt(33)\n\t" \
    "v_mfma_f32_16x16x32_bf16 v[104:107], a[40:43], v[40:43], v[104:107]\n\t" \
    "v_mfma_f32_16x16x32_bf16 v[108:111], a[40:43], v[44:47], v[108:111]\n\t" \
    "s_waitcnt vmcnt(32)\n\t" \
    "v_mfma_f32_16x16x32_bf16 v[112:115], a[44:47], v[40:43], v[112:115]\n\t" \
    "v_mfma_f32_16x16x32_bf16 v[116:119], a[44:47], v[44:47], v[116:119]\n\t" \
    "s_waitcnt vmcnt(29)\n\t" \
    "v_mfma_f32_16x16x32_bf16 v[72:75], a[48:51], v[48:51], v[72:75]\n\t" \
    "global_load_dwordx4 v[40:43], v6, s[40:41] offset:-2048 sc1\n\t" \
    "v_mfma_f32_16x16x32_bf16 v[76:79], a[48:51], v[52:55], v[76:79]\n\t" \
    "global_load_dwordx4 v[44:47], v6, s[40:41] offset:-1024 sc1\n\t" \
    "s_waitcnt vmcnt(30)\n\t" \
    "v_mfma_f32_16x16x32_bf16 v[80:83], a[52:55], v[48:51], v[80:83]\n\t" \
    "global_load_dwordx4 a[24:27], v18, s[38:39] offset:-3072 sc1\n\t" \
    "v_mfma_f32_16x16x32_bf16 v[84:87], a[52:55], v[52:55], v[84:87]\n\t" \
    "global_load_dwordx4 a[28:31], v19, s[38:39] offset:-3072 sc1\n\t" \
    "s_waitcnt vmcnt(31)\n\t" \
    "v_mfma_f32_16x16x32_bf16 v[88:91], a[56:59], v[48:51], v[88:91]\n\t" \
    "global_load_dwordx4 a[32:35], v20, s[38:39] offset:-3072 sc1\n\t" \
    "v_mfma_f32_16x16x32_bf16 v[92:95], a[56:59], v[52:55], v[92:95]\n\t" \
    "global_load_dwordx4 a[36:39], v21, s[38:39] offset:-3072 sc1\n\t" \
    "s_waitcnt vmcnt(32)\n\t" \
    "v_mfma_f32_16x16x32_bf16 v[96:99], a[60:63], v[48:51], v[96:99]\n\t" \
    "global_load_dwordx4 a[40:43], v22, s[38:39] offset:-3072 sc1\n\t" \
    "v_mfma_f32_16x16x32_bf16 v[100:103], a[60:63], v[52:55], v[100:103]\n\t" \
    "global_load_dwordx4 a[44:47], v23, s[38:39] offset:-3072 sc1\n\t" \
    "s_waitcnt vmcnt(33)\n\t" \
    "v_mfma_f32_16x16x32_bf16 v[104:107], a[64:67], v[48:51], v[104:107]\n\t" \
    "v_mfma_f32_16x16x32_bf16 v[108:111], a[64:67], v[52:55], v[108:111]\n\t" \
    "s_waitcnt vmcnt(32)\n\t" \
    "v_mfma_f32_16x16x32_bf16 v[112:115], a[68:71], v[48:51], v[112:115]\n\t" \
    "v_mfma_f32_16x16x32_bf16 v[116:119], a[68:71], v[52:55], v[116:119]\n\t" \
    "s_waitcnt vmcnt(29)\n\t" \
    "v_mfma_f32_16x16x32_bf16 v[72:75], a[72:75], v[56:59], v[72:75]\n\t" \
    "global_load_dwordx4 v[48:51], v6, s[40:41] offset:0 sc1\n\t" \
    "v_mfma_f32_16x16x32_bf16 v[76:79], a[72:75], v[60:63], v[76:79]\n\t" \
    "global_load_dwordx4 v[52:55], v6, s[40:41] offset:1024 sc1\n\t" \
    "s_waitcnt vmcnt(30)\n\t" \
    "v_mfma_f32_16x16x32_bf16 v[80:83], a[76:79], v[56:59], v[80:83]\n\t" \
    "global_load_dwordx4 a[48:51], v18, s[38:39] offset:-2048 sc1\n\t" \
    "v_mfma_f32_16x16x32_bf16 v[84:87], a[76:79], v[60:63], v[84:87]\n\t" \
    "global_load_dwordx4 a[52:55], v19, s[38:39] offset:-2048 sc1\n\t" \
    "s_waitcnt vmcnt(31)\n\t" \
    "v_mfma_f32_16x16x32_bf16 v[88:91], a[80:83], v[56:59], v[88:91]\n\t" \
    "global_load_dwordx4 a[56:59], v20, s[38:39] offset:-2048 sc1\n\t" \
    "v_mfma_f32_16x16x32_bf16 v[92:95], a[80:83], v[60:63], v[92:95]\n\t" \
    "global_load_dwordx4 a[60:63], v21, s[38:39] offset:-2048 sc1\n\t" \
    "s_waitcnt vmcnt(32)\n\t" \
    "v_mfma_f32_16x16x32_bf16 v[96:99], a[84:87], v[56:59], v[96:99]\n\t" \
    "global_load_dwordx4 a[64:67], v22, s[38:39] offset:-2048 sc1\n\t" \
    "v_mfma_f32_16x16x32_bf16 v[100:103], a[84:87], v[60:63], v[100:103]\n\t" \
    "global_load_dwordx4 a[68:71], v23, s[38:39] offset:-2048 sc1\n\t" \
    "s_waitcnt vmcnt(33)\n\t" \
    "v_mfma_f32_16x16x32_bf16 v[104:107], a[88:91], v[56:59], v[104:107]\n\t" \
    "v_mfma_f32_16x16x32_bf16 v[108:111], a[88:91], v[60:63], v[108:111]\n\t" \
    "s_waitcnt vmcnt(32)\n\t" \
    "v_mfma_f32_16x16x32_bf16 v[112:115], a[92:95], v[56:59], v[112:115]\n\t" \
    "v_mfma_f32_16x16x32_bf16 v[116:119], a[92:95], v[60:63], v[116:119]\n\t" \
    "s_waitcnt vmcnt(29)\n\t" \
    "v_mfma_f32_16x16x32_bf16 v[72:75], a[96:99], v[64:67], v[72:75]\n\t" \
    "global_load_dwordx4 v[56:59], v6, s[40:41] offset:2048 sc1\n\t" \
    "v_mfma_f32_16x16x32_bf16 v[76:79], a[96:99], v[68:71], v[76:79]\n\t" \
    "global_load_dwordx4 v[60:63], v6, s[40:41] offset:3072 sc1\n\t" \
    "s_waitcnt vmcnt(30)\n\t" \
    "v_mfma_f32_16x16x32_bf16 v[80:83], a[100:103], v[64:67], v[80:83]\n\t" \
    "global_load_dwordx4 a[72:75], v18, s[38:39] offset:-1024 sc1\n\t" \
    "v_mfma_f32_16x16x32_bf16 v[84:87], a[100:103], v[68:71], v[84:87]\n\t" \
    "global_load_dwordx4 a[76:79], v19, s[38:39] offset:-1024 sc1\n\t" \
    "s_waitcnt vmcnt(31)\n\t" \
    "v_mfma_f32_16x16x32_bf16 v[88:91], a[104:107], v[64:67], v[88:91]\n\t" \
    "global_load_dwordx4 a[80:83], v20, s[38:39] offset:-1024 sc1\n\t" \
    "v_mfma_f32_16x16x32_bf16 v[92:95], a[104:107], v[68:71], v[92:95]\n\t" \
    "global_load_dwordx4 a[84:87], v21, s[38:39] offset:-1024 sc1\n\t" \
    "s_waitcnt vmcnt(32)\n\t" \
    "v_mfma_f32_16x16x32_bf16 v[96:99], a[108:111], v[64:67], v[96:99]\n\t" \
    "global_load_dwordx4 a[88:91], v22, s[38:39] offset:-1024 sc1\n\t" \
    "v_mfma_f32_16x16x32_bf16 v[100:103], a[108:111], v[68:71], v[100:103]\n\t" \
    "global_load_dwordx4 a[92:95], v23, s[38:39] offset:-1024 sc1\n\t" \
    "s_waitcnt vmcnt(33)\n\t" \
    "v_mfma_f32_16x16x32_bf16 v[104:107], a[112:115], v[64:67], v[104:107]\n\t" \
    "v_mfma_f32_16x16x32_bf16 v[108:111], a[112:115], v[68:71], v[108:111]\n\t" \
    "s_waitcnt vmcnt(32)\n\t" \
    "v_mfma_f32_16x16x32_bf16 v[112:115], a[116:119], v[64:67], v[112:115]\n\t" \
    "v_mfma_f32_16x16x32_bf16 v[116:119], a[116:119], v[68:71], v[116:119]\n\t" \
    "s_mov_b32 s97, 1\n\t" \
    "s_branch oo_epi5_%=\n\t" \
    "oo_stop4_%=:\n\t" \
    "s_waitcnt vmcnt(29)\n\t" \
    "v_mfma_f32_16x16x32_bf16 v[72:75], a[24:27], v[40:43], v[72:75]\n\t" \
    "v_mfma_f32_16x16x32_bf16 v[76:79], a[24:27], v[44:47], v[76:79]\n\t" \
    "s_waitcnt vmcnt(28)\n\t" \
    "v_mfma_f32_16x16x32_bf16 v[80:83], a[28:31], v[40:43], v[80:83]\n\t" \
    "v_mfma_f32_16x16x32_bf16 v[84:87], a[28:31], v[44:47], v[84:87]\n\t" \
    "s_waitcnt vmcnt(27)\n\t" \
    "v_mfma_f32_16x16x32_bf16 v[88:91], a[32:35], v[40:43], v[88:91]\n\t" \
    "v_mfma_f32_16x16x32_bf16 v[92:95], a[32:35], v[44:47], v[92:95]\n\t" \
    "s_waitcnt vmcnt(26)\n\t" \
    "v_mfma_f32_16x16x32_bf16 v[96:99], a[36:39], v[40:43], v[96:99]\n\t" \
    "v_mfma_f32_16x16x32_bf16 v[100:103], a[36:39], v[44:47], v[100:103]\n\t" \
    "s_waitcnt vmcnt(25)\n\t" \
    "v_mfma_f32_16x16x32_bf16 v[104:107], a[40:43], v[40:43], v[104:107]\n\t" \
    "v_mfma_f32_16x16x32_bf16 v[108:111], a[40:43], v[44:47], v[108:111]\n\t" \
    "s_waitcnt vmcnt(24)\n\t" \
    "v_mfma_f32_16x16x32_bf16 v[112:115], a[44:47], v[40:43], v[112:115]\n\t" \
    "v_mfma_f32_16x16x32_bf16 v[116:119], a[44:47], v[44:47], v[116:119]\n\t" \
    "s_waitcnt vmcnt(21)\n\t" \
    "v_mfma_f32_16x16x32_bf16 v[72:75], a[48:51], v[48:51], v[72:75]\n\t" \
    "v_mfma_f32_16x16x32_bf16 v[76:79], a[48:51], v[52:55], v[76:79]\n\t" \
    "s_waitcnt vmcnt(20)\n\t" \
    "v_mfma_f32_16x16x32_bf16 v[80:83], a[52:55], v[48:51], v[80:83]\n\t" \
    "v_mfma_f32_16x16x32_bf16 v[84:87], a[52:55], v[52:55], v[84:87]\n\t" \
    "s_waitcnt vmcnt(19)\n\t" \
    "v_mfma_f32_16x16x32_bf16 v[88:91], a[56:59], v[48:51], v[88:91]\n\t" \
    "v_mfma_f32_16x16x32_bf16 v[92:95], a[56:59], v[52:55], v[92:95]\n\t" \
    "s_waitcnt vmcnt(18)\n\t" \
    "v_mfma_f32_16x16x32_bf16 v[96:99], a[60:63], v[48:51], v[96:99]\n\t" \
    "v_mfma_f32_16x16x32_bf16 v[100:103], a[60:63], v[52:55], v[100:103]\n\t" \
    "s_waitcnt vmcnt(17)\n\t" \
    "v_mfma_f32_16x16x32_bf16 v[104:107], a[64:67], v[48:51], v[104:107]\n\t" \
    "v_mfma_f32_16x16x32_bf16 v[108:111], a[64:67], v[52:55], v[108:111]\n\t" \
    "s_waitcnt vmcnt(16)\n\t" \
    "v_mfma_f32_16x16x32_bf16 v[112:115], a[68:71], v[48:51], v[112:115]\n\t" \
    "v_mfma_f32_16x16x32_bf16 v[116:119], a[68:71], v[52:55], v[116:119]\n\t" \
    "s_waitcnt vmcnt(13)\n\t" \
    "v_mfma_f32_16x16x32_bf16 v[72:75], a[72:75], v[56:59], v[72:75]\n\t" \
    "v_mfma_f32_16x16x32_bf16 v[76:79], a[72:75], v[60:63], v[76:79]\n\t" \
    "s_waitcnt vmcnt(12)\n\t" \
    "v_mfma_f32_16x16x32_bf16 v[80:83], a[76:79], v[56:59], v[80:83]\n\t" \
    "v_mfma_f32_16x16x32_bf16 v[84:87], a[76:79], v[60:63], v[84:87]\n\t" \
    "s_waitcnt vmcnt(11)\n\t" \
    "v_mfma_f32_16x16x32_bf16 v[88:91], a[80:83], v[56:59], v[88:91]\n\t" \
    "v_mfma_f32_16x16x32_bf16 v[92:95], a[80:83], v[60:63], v[92:95]\n\t" \
    "s_waitcnt vmcnt(10)\n\t" \
    "v_mfma_f32_16x16x32_bf16 v[96:99], a[84:87], v[56:59], v[96:99]\n\t" \
    "v_mfma_f32_16x16x32_bf16 v[100:103], a[84:87], v[60:63], v[100:103]\n\t" \
    "s_waitcnt vmcnt(9)\n\t" \
    "v_mfma_f32_16x16x32_bf16 v[104:107], a[88:91], v[56:59], v[104:107]\n\t" \
    "v_mfma_f32_16x16x32_bf16 v[108:111], a[88:91], v[60:63], v[108:111]\n\t" \
    "s_waitcnt vmcnt(8)\n\t" \
    "v_mfma_f32_16x16x32_bf16 v[112:115], a[92:95], v[56:59], v[112:115]\n\t" \
    "v_mfma_f32_16x16x32_bf16 v[116:119], a[92:95], v[60:63], v[116:119]\n\t" \
    "s_waitcnt vmcnt(5)\n\t" \
    "v_mfma_f32_16x16x32_bf16 v[72:75], a[96:99], v[64:67], v[72:75]\n\t" \
    "v_mfma_f32_16x16x32_bf16 v[76:79], a[96:99], v[68:71], v[76:79]\n\t" \
    "s_waitcnt vmcnt(4)\n\t" \
    "v_mfma_f32_16x16x32_bf16 v[80:83], a[100:103], v[64:67], v[80:83]\n\t" \
    "v_mfma_f32_16x16x32_bf16 v[84:87], a[100:103], v[68:71], v[84:87]\n\t" \
    "s_waitcnt vmcnt(3)\n\t" \
    "v_mfma_f32_16x16x32_bf16 v[88:91], a[104:107], v[64:67], v[88:91]\n\t" \
    "v_mfma_f32_16x16x32_bf16 v[92:95], a[104:107], v[68:71], v[92:95]\n\t" \
    "s_waitcnt vmcnt(2)\n\t" \
    "v_mfma_f32_16x16x32_bf16 v[96:99], a[108:111], v[64:67], v[96:99]\n\t" \
    "v_mfma_f32_16x16x32_bf16 v[100:103], a[108:111], v[68:71], v[100:103]\n\t" \
    "s_waitcnt vmcnt(1)\n\t" \
    "v_mfma_f32_16x16x32_bf16 v[104:107], a[112:115], v[64:67], v[104:107]\n\t" \
    "v_mfma_f32_16x16x32_bf16 v[108:111], a[112:115], v[68:71], v[108:111]\n\t" \
    "s_waitcnt vmcnt(0)\n\t" \
    "v_mfma_f32_16x16x32_bf16 v[112:115], a[116:119], v[64:67], v[112:115]\n\t" \
    "v_mfma_f32_16x16x32_bf16 v[116:119], a[116:119], v[68:71], v[116:119]\n\t" \
    "s_mov_b32 s97, 0\n\t" \
    "oo_epi5_%=:\n\t" \
    "s_mul_i32 s69, s65, 73728\n\t" \
    "s_add_u32 s44, s42, s69\n\t" \
    "s_addc_u32 s45, s43, 0\n\t" \
    "s_nop 7\n\t" \
    "ds_write_b128 v14, v[72:75] offset:0\n\t" \
    "ds_write_b128 v14, v[76:79] offset:1024\n\t" \
    "ds_write_b128 v14, v[80:83] offset:2048\n\t" \
    "ds_write_b128 v14, v[84:87] offset:3072\n\t" \
    "ds_write_b128 v14, v[88:91] offset:4096\n\t" \
    "ds_write_b128 v14, v[92:95] offset:5120\n\t" \
    "ds_write_b128 v14, v[96:99] offset:6144\n\t" \
    "ds_write_b128 v14, v[100:103] offset:7168\n\t" \
    "ds_write_b128 v14, v[104:107] offset:8192\n\t" \
    "ds_write_b128 v14, v[108:111] offset:9216\n\t" \
    "ds_write_b128 v14, v[112:115] offset:10240\n\t" \
    "ds_write_b128 v14, v[116:119] offset:11264\n\t" \
    "s_waitcnt lgkmcnt(0)\n\t" \
    "s_barrier\n\t" \
    "ds_read_b128 v[120:123], v15 offset:0\n\t" \
    "ds_read_b128 v[124:127], v15 offset:12288\n\t" \
    "ds_read_b128 v[128:131], v15 offset:24576\n\t" \
    "ds_read_b128 v[132:135], v15 offset:36864\n\t" \
    "ds_read_b128 v[136:139], v15 offset:4096\n\t" \
    "ds_read_b128 v[140:143], v15 offset:16384\n\t" \
    "ds_read_b128 v[144:147], v15 offset:28672\n\t" \
    "ds_read_b128 v[148:151], v15 offset:40960\n\t" \
    "ds_read_b128 v[152:155], v15 offset:8192\n\t" \
    "ds_read_b128 v[156:159], v15 offset:20480\n\t" \
    "ds_read_b128 v[160:163], v15 offset:32768\n\t" \
    "ds_read_b128 v[164:167], v15 offset:45056\n\t" \
    "v_xor_b32 v14, 0x10000, v14\n\t" \
    "v_xor_b32 v15, 0x10000, v15\n\t" \
    "s_waitcnt lgkmcnt(10)\n\t" \
    "v_pk_add_f32 v[120:121], v[120:121], v[124:125]\n\t" \
    "v_pk_add_f32 v[122:123], v[122:123], v[126:127]\n\t" \
    "s_waitcnt lgkmcnt(8)\n\t" \
    "v_pk_add_f32 v[128:129], v[128:129], v[132:133]\n\t" \
    "v_pk_add_f32 v[130:131], v[130:131], v[134:135]\n\t" \
    "v_pk_add_f32 v[120:121], v[120:121], v[128:129]\n\t" \
    "v_pk_add_f32 v[122:123], v[122:123], v[130:131]\n\t" \
    "v_cvt_pk_bf16_f32 v120, v120, v121\n\t" \
    "v_cvt_pk_bf16_f32 v121, v122, v123\n\t" \
    "global_store_dwordx2 v16, v[120:121], s[44:45] offset:0\n\t" \
    "s_waitcnt lgkmcnt(6)\n\t" \
    "v_pk_add_f32 v[136:137], v[136:137], v[140:141]\n\t" \
    "v_pk_add_f32 v[138:139], v[138:139], v[142:143]\n\t" \
    "s_waitcnt lgkmcnt(4)\n\t" \
    "v_pk_add_f32 v[144:145], v[144:145], v[148:149]\n\t" \
    "v_pk_add_f32 v[146:147], v[146:147], v[150:151]\n\t" \
    "v_pk_add_f32 v[136:137], v[136:137], v[144:145]\n\t" \
    "v_pk_add_f32 v[138:139], v[138:139], v[146:147]\n\t" \
    "v_cvt_pk_bf16_f32 v136, v136, v137\n\t" \
    "v_cvt_pk_bf16_f32 v137, v138, v139\n\t" \
    "global_store_dwordx2 v16, v[136:137], s[44:45] offset:64\n\t" \
    "s_waitcnt lgkmcnt(2)\n\t" \
    "v_pk_add_f32 v[152:153], v[152:153], v[156:157]\n\t" \
    "v_pk_add_f32 v[154:155], v[154:155], v[158:159]\n\t" \
    "s_waitcnt lgkmcnt(0)\n\t" \
    "v_pk_add_f32 v[160:161], v[160:161], v[164:165]\n\t" \
    "v_pk_add_f32 v[162:163], v[162:163], v[166:167]\n\t" \
    "v_pk_add_f32 v[152:153], v[152:153], v[160:161]\n\t" \
    "v_pk_add_f32 v[154:155], v[154:155], v[162:163]\n\t" \
    "v_cvt_pk_bf16_f32 v152, v152, v153\n\t" \
    "v_cvt_pk_bf16_f32 v153, v154, v155\n\t" \
    "global_store_dwordx2 v16, v[152:153], s[44:45] offset:128\n\t" \
    "s_mov_b32 s65, s96\n\t" \
    "s_cmp_lg_u32 s97, 0\n\t" \
    "s_cbranch_scc1 oo_loop3_%=\n\t" \
    "s_cmp_lt_u32 s65, 58\n\t" \
    "s_cbranch_scc1 oo_poll1_%=\n\t" \
    "s_waitcnt vmcnt(0) lgkmcnt(0)\n\t" \
    "s_barrier\n\t" \
    "oo_last_%=:\n\t" \
    ""
#define ASM_CV \
    "v_mov_b32 v0, %0\n\t" \
    "v_mov_b32 v2, %2\n\t" \
    "v_subrev_u32 v1, 768, %1\n\t" \
    "v_mov_b32 v4, 0\n\t" \
    "v_mov_b32 v5, 0\n\t" \
    "v_mov_b32 v6, 0\n\t" \
    "v_mov_b32 v7, 0\n\t" \
    "v_mov_b32 v8, 0\n\t" \
    "v_mov_b32 v9, 0\n\t" \
    "v_mov_b32 v10, 0\n\t" \
    "v_mov_b32 v11, 0\n\t" \
    "v_mov_b32 v12, 0\n\t" \
    "v_mov_b32 v13, 0\n\t" \
    "v_mov_b32 v14, 0\n\t" \
    "v_mov_b32 v15, 0\n\t" \
    "v_mov_b32 v16, 0\n\t" \
    "v_mov_b32 v17, 0\n\t" \
    "v_mov_b32 v18, 0\n\t" \
    "v_mov_b32 v19, 0\n\t" \
    "v_mov_b32 v20, 0\n\t" \
    "v_mov_b32 v21, 0\n\t" \
    "v_mov_b32 v22, 0\n\t" \
    "v_mov_b32 v23, 0\n\t" \
    "v_mov_b32 v24, 0\n\t" \
    "v_mov_b32 v25, 0\n\t" \
    "v_mov_b32 v26, 0\n\t" \
    "v_mov_b32 v27, 0\n\t" \
    "v_mov_b32 v28, 0\n\t" \
    "v_mov_b32 v29, 0\n\t" \
    "v_mov_b32 v30, 0\n\t" \
    "v_mov_b32 v31, 0\n\t" \
    "v_mov_b32 v32, 0\n\t" \
    "v_mov_b32 v33, 0\n\t" \
    "v_mov_b32 v34, 0\n\t" \
    "v_mov_b32 v35, 0\n\t" \
    "s_waitcnt lgkmcnt(0)\n\t" \
    "ds_read_b64 v[36:37], v0 offset:0\n\t" \
    "ds_read_b64 v[38:39], v0 offset:8\n\t" \
    "ds_read_b64 v[40:41], v0 offset:32\n\t" \
    "ds_read_b64 v[42:43], v0 offset:40\n\t" \
    "ds_read_b128 v[44:47], v1 offset:768\n\t" \
    "ds_read_b128 v[48:51], v1 offset:2304\n\t" \
    "ds_read_b128 v[52:55], v1 offset:3840\n\t" \
    "ds_read_b128 v[56:59], v1 offset:5376\n\t" \
    "ds_read_b128 v[60:63], v1 offset:8448\n\t" \
    "ds_read_b128 v[64:67], v1 offset:9984\n\t" \
    "ds_read_b128 v[68:71], v1 offset:11520\n\t" \
    "ds_read_b128 v[72:75], v1 offset:13056\n\t" \
    "s_mov_b32 s69, 8\n\t" \
    "cv_c1_%=:\n\t" \
    "s_waitcnt lgkmcnt(7)\n\t" \
    "v_mfma_f32_16x16x32_bf16 v[4:7], v[36:39], v[44:47], v[4:7]\n\t" \
    "ds_read_b64 v[76:77], v0 offset:64\n\t" \
    "ds_read_b64 v[78:79], v0 offset:72\n\t" \
    "v_mfma_f32_16x16x32_bf16 v[20:23], v[40:43], v[44:47], v[20:23]\n\t" \
    "ds_read_b64 v[80:81], v0 offset:96\n\t" \
    "ds_read_b64 v[82:83], v0 offset:104\n\t" \
    "s_waitcnt lgkmcnt(10)\n\t" \
    "v_mfma_f32_16x16x32_bf16 v[8:11], v[36:39], v[48:51], v[8:11]\n\t" \
    "ds_read_b128 v[84:87], v1 offset:672\n\t" \
    "ds_read_b128 v[88:91], v1 offset:2208\n\t" \
    "v_mfma_f32_16x16x32_bf16 v[24:27], v[40:43], v[48:51], v[24:27]\n\t" \
    "ds_read_b128 v[92:95], v1 offset:3744\n\t" \
    "ds_read_b128 v[96:99], v1 offset:5280\n\t" \
    "s_waitcnt lgkmcnt(13)\n\t" \
    "v_mfma_f32_16x16x32_bf16 v[12:15], v[36:39], v[52:55], v[12:15]\n\t" \
    "ds_read_b128 v[100:103], v1 offset:8352\n\t" \
    "ds_read_b128 v[104:107], v1 offset:9888\n\t" \
    "v_mfma_f32_16x16x32_bf16 v[28:31], v[40:43], v[52:55], v[28:31]\n\t" \
    "s_waitcnt lgkmcnt(14)\n\t" \
    "v_mfma_f32_16x16x32_bf16 v[16:19], v[36:39], v[56:59], v[16:19]\n\t" \
    "ds_read_b128 v[108:111], v1 offset:11424\n\t" \
    "v_mfma_f32_16x16x32_bf16 v[32:35], v[40:43], v[56:59], v[32:35]\n\t" \
    "s_waitcnt lgkmcnt(14)\n\t" \
    "v_mfma_f32_16x16x32_bf16 v[4:7], v[36:39], v[60:63], v[4:7]\n\t" \
    "ds_read_b128 v[112:115], v1 offset:12960\n\t" \
    "v_mfma_f32_16x16x32_bf16 v[20:23], v[40:43], v[60:63], v[20:23]\n\t" \
    "s_waitcnt lgkmcnt(14)\n\t" \
    "v_mfma_f32_16x16x32_bf16 v[8:11], v[36:39], v[64:67], v[8:11]\n\t" \
    "v_mfma_f32_16x16x32_bf16 v[24:27], v[40:43], v[64:67], v[24:27]\n\t" \
    "s_waitcnt lgkmcnt(13)\n\t" \
    "v_mfma_f32_16x16x32_bf16 v[12:15], v[36:39], v[68:71], v[12:15]\n\t" \
    "v_mfma_f32_16x16x32_bf16 v[28:31], v[40:43], v[68:71], v[28:31]\n\t" \
    "s_waitcnt lgkmcnt(12)\n\t" \
    "v_mfma_f32_16x16x32_bf16 v[16:19], v[36:39], v[72:75], v[16:19]\n\t" \
    "v_mfma_f32_16x16x32_bf16 v[32:35], v[40:43], v[72:75], v[32:35]\n\t" \
    "s_waitcnt lgkmcnt(7)\n\t" \
    "v_mfma_f32_16x16x32_bf16 v[4:7], v[76:79], v[84:87], v[4:7]\n\t" \
    "ds_read_b64 v[36:37], v0 offset:128\n\t" \
    "ds_read_b64 v[38:39], v0 offset:136\n\t" \
    "v_mfma_f32_16x16x32_bf16 v[20:23], v[80:83], v[84:87], v[20:23]\n\t" \
    "ds_read_b64 v[40:41], v0 offset:160\n\t" \
    "ds_read_b64 v[42:43], v0 offset:168\n\t" \
    "s_waitcnt lgkmcnt(10)\n\t" \
    "v_mfma_f32_16x16x32_bf16 v[8:11], v[76:79], v[88:91], v[8:11]\n\t" \
    "ds_read_b128 v[44:47], v1 offset:576\n\t" \
    "ds_read_b128 v[48:51], v1 offset:2112\n\t" \
    "v_mfma_f32_16x16x32_bf16 v[24:27], v[80:83], v[88:91], v[24:27]\n\t" \
    "ds_read_b128 v[52:55], v1 offset:3648\n\t" \
    "ds_read_b128 v[56:59], v1 offset:5184\n\t" \
    "s_waitcnt lgkmcnt(13)\n\t" \
    "v_mfma_f32_16x16x32_bf16 v[12:15], v[76:79], v[92:95], v[12:15]\n\t" \
    "ds_read_b128 v[60:63], v1 offset:8256\n\t" \
    "ds_read_b128 v[64:67], v1 offset:9792\n\t" \
    "v_mfma_f32_16x16x32_bf16 v[28:31], v[80:83], v[92:95], v[28:31]\n\t" \
    "s_waitcnt lgkmcnt(14)\n\t" \
    "v_mfma_f32_16x16x32_bf16 v[16:19], v[76:79], v[96:99], v[16:19]\n\t" \
    "ds_read_b128 v[68:71], v1 offset:11328\n\t" \
    "v_mfma_f32_16x16x32_bf16 v[32:35], v[80:83], v[96:99], v[32:35]\n\t" \
    "s_waitcnt lgkmcnt(14)\n\t" \
    "v_mfma_f32_16x16x32_bf16 v[4:7], v[76:79], v[100:103], v[4:7]\n\t" \
    "ds_read_b128 v[72:75], v1 offset:12864\n\t" \
    "v_mfma_f32_16x16x32_bf16 v[20:23], v[80:83], v[100:103], v[20:23]\n\t" \
    "s_waitcnt lgkmcnt(14)\n\t" \
    "v_mfma_f32_16x16x32_bf16 v[8:11], v[76:79], v[104:107], v[8:11]\n\t" \
    "v_mfma_f32_16x16x32_bf16 v[24:27], v[80:83], v[104:107], v[24:27]\n\t" \
    "s_waitcnt lgkmcnt(13)\n\t" \
    "v_mfma_f32_16x16x32_bf16 v[12:15], v[76:79], v[108:111], v[12:15]\n\t" \
    "v_mfma_f32_16x16x32_bf16 v[28:31], v[80:83], v[108:111], v[28:31]\n\t" \
    "s_waitcnt lgkmcnt(12)\n\t" \
    "v_mfma_f32_16x16x32_bf16 v[16:19], v[76:79], v[112:115], v[16:19]\n\t" \
    "v_mfma_f32_16x16x32_bf16 v[32:35], v[80:83], v[112:115], v[32:35]\n\t" \
    "v_add_u32 v0, 128, v0\n\t" \
    "v_subrev_u32 v1, 192, v1\n\t" \
    "s_sub_u32 s69, s69, 1\n\t" \
    "s_cmp_lg_u32 s69, 0\n\t" \
    "s_cbranch_scc1 cv_c1_%=\n\t" \
    "s_waitcnt lgkmcnt(0)\n\t" \
    "s_mov_b32 s69, 8\n\t" \
    "cv_c2_%=:\n\t" \
    "s_waitcnt lgkmcnt(5)\n\t" \
    "v_mfma_f32_16x16x32_bf16 v[8:11], v[36:39], v[48:51], v[8:11]\n\t" \
    "ds_read_b64 v[76:77], v0 offset:64\n\t" \
    "ds_read_b64 v[78:79], v0 offset:72\n\t" \
    "v_mfma_f32_16x16x32_bf16 v[24:27], v[40:43], v[48:51], v[24:27]\n\t" \
    "ds_read_b64 v[80:81], v0 offset:96\n\t" \
    "ds_read_b64 v[82:83], v0 offset:104\n\t" \
    "s_waitcnt lgkmcnt(8)\n\t" \
    "v_mfma_f32_16x16x32_bf16 v[12:15], v[36:39], v[52:55], v[12:15]\n\t" \
    "ds_read_b128 v[88:91], v1 offset:2208\n\t" \
    "ds_read_b128 v[92:95], v1 offset:3744\n\t" \
    "v_mfma_f32_16x16x32_bf16 v[28:31], v[40:43], v[52:55], v[28:31]\n\t" \
    "ds_read_b128 v[96:99], v1 offset:5280\n\t" \
    "ds_read_b128 v[104:107], v1 offset:9888\n\t" \
    "s_waitcnt lgkmcnt(11)\n\t" \
    "v_mfma_f32_16x16x32_bf16 v[16:19], v[36:39], v[56:59], v[16:19]\n\t" \
    "ds_read_b128 v[108:111], v1 offset:11424\n\t" \
    "ds_read_b128 v[112:115], v1 offset:12960\n\t" \
    "v_mfma_f32_16x16x32_bf16 v[32:35], v[40:43], v[56:59], v[32:35]\n\t" \
    "s_waitcnt lgkmcnt(12)\n\t" \
    "v_mfma_f32_16x16x32_bf16 v[8:11], v[36:39], v[64:67], v[8:11]\n\t" \
    "v_mfma_f32_16x16x32_bf16 v[24:27], v[40:43], v[64:67], v[24:27]\n\t" \
    "s_waitcnt lgkmcnt(11)\n\t" \
    "v_mfma_f32_16x16x32_bf16 v[12:15], v[36:39], v[68:71], v[12:15]\n\t" \
    "v_mfma_f32_16x16x32_bf16 v[28:31], v[40:43], v[68:71], v[28:31]\n\t" \
    "s_waitcnt lgkmcnt(10)\n\t" \
    "v_mfma_f32_16x16x32_bf16 v[16:19], v[36:39], v[72:75], v[16:19]\n\t" \
    "v_mfma_f32_16x16x32_bf16 v[32:35], v[40:43], v[72:75], v[32:35]\n\t" \
    "s_waitcnt lgkmcnt(5)\n\t" \
    "v_mfma_f32_16x16x32_bf16 v[8:11], v[76:79], v[88:91], v[8:11]\n\t" \
    "ds_read_b64 v[36:37], v0 offset:128\n\t" \
    "ds_read_b64 v[38:39], v0 offset:136\n\t" \
    "v_mfma_f32_16x16x32_bf16 v[24:27], v[80:83], v[88:91], v[24:27]\n\t" \
    "ds_read_b64 v[40:41], v0 offset:160\n\t" \
    "ds_read_b64 v[42:43], v0 offset:168\n\t" \
    "s_waitcnt lgkmcnt(8)\n\t" \
    "v_mfma_f32_16x16x32_bf16 v[12:15], v[76:79], v[92:95], v[12:15]\n\t" \
    "ds_read_b128 v[48:51], v1 offset:2112\n\t" \
    "ds_read_b128 v[52:55], v1 offset:3648\n\t" \
    "v_mfma_f32_16x16x32_bf16 v[28:31], v[80:83], v[92:95], v[28:31]\n\t" \
    "ds_read_b128 v[56:59], v1 offset:5184\n\t" \
    "ds_read_b128 v[64:67], v1 offset:9792\n\t" \
    "s_waitcnt lgkmcnt(11)\n\t" \
    "v_mfma_f32_16x16x32_bf16 v[16:19], v[76:79], v[96:99], v[16:19]\n\t" \
    "ds_read_b128 v[68:71], v1 offset:11328\n\t" \
    "ds_read_b128 v[72:75], v1 offset:12864\n\t" \
    "v_mfma_f32_16x16x32_bf16 v[32:35], v[80:83], v[96:99], v[32:35]\n\t" \
    "s_waitcnt lgkmcnt(12)\n\t" \
    "v_mfma_f32_16x16x32_bf16 v[8:11], v[76:79], v[104:107], v[8:11]\n\t" \
    "v_mfma_f32_16x16x32_bf16 v[24:27], v[80:83], v[104:107], v[24:27]\n\t" \
    "s_waitcnt lgkmcnt(11)\n\t" \
    "v_mfma_f32_16x16x32_bf16 v[12:15], v[76:79], v[108:111], v[12:15]\n\t" \
    "v_mfma_f32_16x16x32_bf16 v[28:31], v[80:83], v[108:111], v[28:31]\n\t" \
    "s_waitcnt lgkmcnt(10)\n\t" \
    "v_mfma_f32_16x16x32_bf16 v[16:19], v[76:79], v[112:115], v[16:19]\n\t" \
    "v_mfma_f32_16x16x32_bf16 v[32:35], v[80:83], v[112:115], v[32:35]\n\t" \
    "v_add_u32 v0, 128, v0\n\t" \
    "v_subrev_u32 v1, 192, v1\n\t" \
    "s_sub_u32 s69, s69, 1\n\t" \
    "s_cmp_lg_u32 s69, 0\n\t" \
    "s_cbranch_scc1 cv_c2_%=\n\t" \
    "s_waitcnt lgkmcnt(0)\n\t" \
    "s_mov_b32 s69, 8\n\t" \
    "cv_c3_%=:\n\t" \
    "s_waitcnt lgkmcnt(3)\n\t" \
    "v_mfma_f32_16x16x32_bf16 v[12:15], v[36:39], v[52:55], v[12:15]\n\t" \
    "ds_read_b64 v[76:77], v0 offset:64\n\t" \
    "ds_read_b64 v[78:79], v0 offset:72\n\t" \
    "v_mfma_f32_16x16x32_bf16 v[28:31], v[40:43], v[52:55], v[28:31]\n\t" \
    "ds_read_b64 v[80:81], v0 offset:96\n\t" \
    "ds_read_b64 v[82:83], v0 offset:104\n\t" \
    "s_waitcnt lgkmcnt(6)\n\t" \
    "v_mfma_f32_16x16x32_bf16 v[16:19], v[36:39], v[56:59], v[16:19]\n\t" \
    "ds_read_b128 v[92:95], v1 offset:3744\n\t" \
    "ds_read_b128 v[96:99], v1 offset:5280\n\t" \
    "v_mfma_f32_16x16x32_bf16 v[32:35], v[40:43], v[56:59], v[32:35]\n\t" \
    "ds_read_b128 v[108:111], v1 offset:11424\n\t" \
    "ds_read_b128 v[112:115], v1 offset:12960\n\t" \
    "s_waitcnt lgkmcnt(9)\n\t" \
    "v_mfma_f32_16x16x32_bf16 v[12:15], v[36:39], v[68:71], v[12:15]\n\t" \
    "v_mfma_f32_16x16x32_bf16 v[28:31], v[40:43], v[68:71], v[28:31]\n\t" \
    "s_waitcnt lgkmcnt(8)\n\t" \
    "v_mfma_f32_16x16x32_bf16 v[16:19], v[36:39], v[72:75], v[16:19]\n\t" \
    "v_mfma_f32_16x16x32_bf16 v[32:35], v[40:43], v[72:75], v[32:35]\n\t" \
    "s_waitcnt lgkmcnt(3)\n\t" \
    "v_mfma_f32_16x16x32_bf16 v[12:15], v[76:79], v[92:95], v[12:15]\n\t" \
    "ds_read_b64 v[36:37], v0 offset:128\n\t" \
    "ds_read_b64 v[38:39], v0 offset:136\n\t" \
    "v_mfma_f32_16x16x32_bf16 v[28:31], v[80:83], v[92:95], v[28:31]\n\t" \
    "ds_read_b64 v[40:41], v0 offset:160\n\t" \
    "ds_read_b64 v[42:43], v0 offset:168\n\t" \
    "s_waitcnt lgkmcnt(6)\n\t" \
    "v_mfma_f32_16x16x32_bf16 v[16:19], v[76:79], v[96:99], v[16:19]\n\t" \
    "ds_read_b128 v[52:55], v1 offset:3648\n\t" \
    "ds_read_b128 v[56:59], v1 offset:5184\n\t" \
    "v_mfma_f32_16x16x32_bf16 v[32:35], v[80:83], v[96:99], v[32:35]\n\t" \
    "ds_read_b128 v[68:71], v1 offset:11328\n\t" \
    "ds_read_b128 v[72:75], v1 offset:12864\n\t" \
    "s_waitcnt lgkmcnt(9)\n\t" \
    "v_mfma_f32_16x16x32_bf16 v[12:15], v[76:79], v[108:111], v[12:15]\n\t" \
    "v_mfma_f32_16x16x32_bf16 v[28:31], v[80:83], v[108:111], v[28:31]\n\t" \
    "s_waitcnt lgkmcnt(8)\n\t" \
    "v_mfma_f32_16x16x32_bf16 v[16:19], v[76:79], v[112:115], v[16:19]\n\t" \
    "v_mfma_f32_16x16x32_bf16 v[32:35], v[80:83], v[112:115], v[32:35]\n\t" \
    "v_add_u32 v0, 128, v0\n\t" \
    "v_subrev_u32 v1, 192, v1\n\t" \
    "s_sub_u32 s69, s69, 1\n\t" \
    "s_cmp_lg_u32 s69, 0\n\t" \
    "s_cbranch_scc1 cv_c3_%=\n\t" \
    "s_waitcnt lgkmcnt(0)\n\t" \
    "ds_read_b64 v[36:37], v0 offset:0\n\t" \
    "ds_read_b64 v[38:39], v0 offset:8\n\t" \
    "ds_read_b64 v[40:41], v0 offset:32\n\t" \
    "ds_read_b64 v[42:43], v0 offset:40\n\t" \
    "ds_read_b128 v[56:59], v1 offset:5376\n\t" \
    "ds_read_b128 v[72:75], v1 offset:13056\n\t" \
    "ds_read_b64 v[76:77], v0 offset:64\n\t" \
    "ds_read_b64 v[78:79], v0 offset:72\n\t" \
    "ds_read_b64 v[80:81], v0 offset:96\n\t" \
    "ds_read_b64 v[82:83], v0 offset:104\n\t" \
    "ds_read_b128 v[96:99], v1 offset:5280\n\t" \
    "ds_read_b128 v[112:115], v1 offset:12960\n\t" \
    "s_mov_b32 s69, 4\n\t" \
    "cv_c4_%=:\n\t" \
    "s_waitcnt lgkmcnt(7)\n\t" \
    "v_mfma_f32_16x16x32_bf16 v[16:19], v[36:39], v[56:59], v[16:19]\n\t" \
    "ds_read_b64 v[116:117], v0 offset:128\n\t" \
    "ds_read_b64 v[118:119], v0 offset:136\n\t" \
    "v_mfma_f32_16x16x32_bf16 v[32:35], v[40:43], v[56:59], v[32:35]\n\t" \
    "ds_read_b64 v[120:121], v0 offset:160\n\t" \
    "ds_read_b64 v[122:123], v0 offset:168\n\t" \
    "s_waitcnt lgkmcnt(10)\n\t" \
    "v_mfma_f32_16x16x32_bf16 v[16:19], v[36:39], v[72:75], v[16:19]\n\t" \
    "ds_read_b128 v[136:139], v1 offset:5184\n\t" \
    "ds_read_b128 v[152:155], v1 offset:12864\n\t" \
    "v_mfma_f32_16x16x32_bf16 v[32:35], v[40:43], v[72:75], v[32:35]\n\t" \
    "ds_read_b64 v[156:157], v0 offset:192\n\t" \
    "ds_read_b64 v[158:159], v0 offset:200\n\t" \
    "s_waitcnt lgkmcnt(9)\n\t" \
    "v_mfma_f32_16x16x32_bf16 v[16:19], v[76:79], v[96:99], v[16:19]\n\t" \
    "ds_read_b64 v[160:161], v0 offset:224\n\t" \
    "ds_read_b64 v[162:163], v0 offset:232\n\t" \
    "v_mfma_f32_16x16x32_bf16 v[32:35], v[80:83], v[96:99], v[32:35]\n\t" \
    "ds_read_b128 v[176:179], v1 offset:5088\n\t" \
    "ds_read_b128 v[192:195], v1 offset:12768\n\t" \
    "s_waitcnt lgkmcnt(12)\n\t" \
    "v_mfma_f32_16x16x32_bf16 v[16:19], v[76:79], v[112:115], v[16:19]\n\t" \
    "v_mfma_f32_16x16x32_bf16 v[32:35], v[80:83], v[112:115], v[32:35]\n\t" \
    "s_waitcnt lgkmcnt(7)\n\t" \
    "v_mfma_f32_16x16x32_bf16 v[16:19], v[116:119], v[136:139], v[16:19]\n\t" \
    "ds_read_b64 v[36:37], v0 offset:256\n\t" \
    "ds_read_b64 v[38:39], v0 offset:264\n\t" \
    "v_mfma_f32_16x16x32_bf16 v[32:35], v[120:123], v[136:139], v[32:35]\n\t" \
    "ds_read_b64 v[40:41], v0 offset:288\n\t" \
    "ds_read_b64 v[42:43], v0 offset:296\n\t" \
    "s_waitcnt lgkmcnt(10)\n\t" \
    "v_mfma_f32_16x16x32_bf16 v[16:19], v[116:119], v[152:155], v[16:19]\n\t" \
    "ds_read_b128 v[56:59], v1 offset:4992\n\t" \
    "ds_read_b128 v[72:75], v1 offset:12672\n\t" \
    "v_mfma_f32_16x16x32_bf16 v[32:35], v[120:123], v[152:155], v[32:35]\n\t" \
    "ds_read_b64 v[76:77], v0 offset:320\n\t" \
    "ds_read_b64 v[78:79], v0 offset:328\n\t" \
    "s_waitcnt lgkmcnt(9)\n\t" \
    "v_mfma_f32_16x16x32_bf16 v[16:19], v[156:159], v[176:179], v[16:19]\n\t" \
    "ds_read_b64 v[80:81], v0 offset:352\n\t" \
    "ds_read_b64 v[82:83], v0 offset:360\n\t" \
    "v_mfma_f32_16x16x32_bf16 v[32:35], v[160:163], v[176:179], v[32:35]\n\t" \
    "ds_read_b128 v[96:99], v1 offset:4896\n\t" \
    "ds_read_b128 v[112:115], v1 offset:12576\n\t" \
    "s_waitcnt lgkmcnt(12)\n\t" \
    "v_mfma_f32_16x16x32_bf16 v[16:19], v[156:159], v[192:195], v[16:19]\n\t" \
    "v_mfma_f32_16x16x32_bf16 v[32:35], v[160:163], v[192:195], v[32:35]\n\t" \
    "v_add_u32 v0, 256, v0\n\t" \
    "v_subrev_u32 v1, 384, v1\n\t" \
    "s_sub_u32 s69, s69, 1\n\t" \
    "s_cmp_lg_u32 s69, 0\n\t" \
    "s_cbranch_scc1 cv_c4_%=\n\t" \
    "s_waitcnt lgkmcnt(0)\n\t" \
    "s_nop 7\n\t" \
    "ds_write_b128 v2, v[4:7] offset:0\n\t" \
    "ds_write_b128 v2, v[8:11] offset:1024\n\t" \
    "ds_write_b128 v2, v[12:15] offset:2048\n\t" \
    "ds_write_b128 v2, v[16:19] offset:3072\n\t" \
    "ds_write_b128 v2, v[20:23] offset:4096\n\t" \
    "ds_write_b128 v2, v[24:27] offset:5120\n\t" \
    "ds_write_b128 v2, v[28:31] offset:6144\n\t" \
    "ds_write_b128 v2, v[32:35] offset:7168\n\t" \
    "s_waitcnt lgkmcnt(0)\n\t" \
    "cv_last_%=:\n\t" \
    ""
#define ASM_CLOB "v0", "v1", "v2", "v3", "v4", "v5", "v6", "v7", "v8", "v9", "v10", "v11", "v12", "v13", "v14", "v15", "v16", "v17", "v18", "v19", "v20", "v21", "v22", "v23", "v24", "v25", "v26", "v27", "v28", "v29", "v30", "v31", "v32", "v33", "v34", "v35", "v36", "v37", "v38", "v39", "v40", "v41", "v42", "v43", "v44", "v45", "v46", "v47", "v48", "v49", "v50", "v51", "v52", "v53", "v54", "v55", "v56", "v57", "v58", "v59", "v60", "v61", "v62", "v63", "v64", "v65", "v66", "v67", "v68", "v69", "v70", "v71", "v72", "v73", "v74", "v75", "v76", "v77", "v78", "v79", "v80", "v81", "v82", "v83", "v84", "v85", "v86", "v87", "v88", "v89", "v90", "v91", "v92", "v93", "v94", "v95", "v96", "v97", "v98", "v99", "v100", "v101", "v102", "v103", "v104", "v105", "v106", "v107", "v108", "v109", "v110", "v111", "v112", "v113", "v114", "v115", "v116", "v117", "v118", "v119", "v120", "v121", "v122", "v123", "v124", "v125", "v126", "v127", "v128", "v129", "v130", "v131", "v132", "v133", "v134", "v135", "v136", "v137", "v138", "v139", "v140", "v141", "v142", "v143", "v144", "v145", "v146", "v147", "v148", "v149", "v150", "v151", "v152", "v153", "v154", "v155", "v156", "v157", "v158", "v159", "v160", "v161", "v162", "v163", "v164", "v165", "v166", "v167", "v168", "v169", "v170", "v171", "v172", "v173", "v174", "v175", "v176", "v177", "v178", "v179", "v180", "v181", "v182", "v183", "v184", "v185", "v186", "v187", "v188", "v189", "v190", "v191", "v192", "v193", "v194", "v195", "v196", "v197", "v198", "v199", "v200", "v201", "v202", "v203", "v204", "v205", "v206", "v207", "v208", "v209", "v210", "v211", "v212", "v213", "v214", "v215", "v222", "v223", "v224", "v225", "v226", "v227", "v228", "v229", "v230", "v231", "v232", "v233", "v234", "v235", "v236", "v237", "v238", "v239", "v240", "v241", "v242", "v243", "v244", "v245", "v246", "v247", "v248", "v249", "v250", "v251", "v252", "v253", "v254", "v255", "a0", "a1", "a2", "a3", "a4", "a5", "a6", "a7", "a8", "a9", "a10", "a11", "a12", "a13", "a14", "a15", "a16", "a17", "a18", "a19", "a20", "a21", "a22", "a23", "a24", "a25", "a26", "a27", "a28", "a29", "a30", "a31", "a32", "a33", "a34", "a35", "a36", "a37", "a38", "a39", "a40", "a41", "a42", "a43", "a44", "a45", "a46", "a47", "a48", "a49", "a50", "a51", "a52", "a53", "a54", "a55", "a56", "a57", "a58", "a59", "a60", "a61", "a62", "a63", "a64", "a65", "a66", "a67", "a68", "a69", "a70", "a71", "a72", "a73", "a74", "a75", "a76", "a77", "a78", "a79", "a80", "a81", "a82", "a83", "a84", "a85", "a86", "a87", "a88", "a89", "a90", "a91", "a92", "a93", "a94", "a95", "a96", "a97", "a98", "a99", "a100", "a101", "a102", "a103", "a104", "a105", "a106", "a107", "a108", "a109", "a110", "a111", "a112", "a113", "a114", "a115", "a116", "a117", "a118", "a119", "a120", "a121", "a122", "a123", "a124", "a125", "a126", "a127", "a128", "a129", "a130", "a131", "a132", "a133", "a134", "a135", "a136", "a137", "a138", "a139", "a140", "a141", "a142", "a143", "a144", "a145", "a146", "a147", "a148", "a149", "a150", "a151", "a152", "a153", "a154", "a155", "a156", "a157", "a158", "a159", "a160", "a161", "a162", "a163", "a164", "a165", "a166", "a167", "a168", "a169", "a170", "a171", "a172", "a173", "a174", "a175", "a176", "a177", "a178", "a179", "a180", "a181", "a182", "a183", "a184", "a185", "a186", "a187", "a188", "a189", "a190", "a191", "a192", "a193", "a194", "a195", "a196", "a197", "a198", "a199", "a200", "a201", "a202", "a203", "a204", "a205", "a206", "a207", "a208", "a209", "a210", "a211", "a212", "a213", "a214", "a215", "a216", "a217", "a218", "a219", "a220", "a221", "a222", "a223", "a224", "a225", "a226", "a227", "a228", "a229", "a230", "a231", "a232", "a233", "a234", "a235", "a236", "a237", "a238", "a239", "a240", "a241", "a242", "a243", "a244", "a245", "a246", "a247", "a248", "a249", "a250", "a251", "a252", "a253", "a254", "a255", "s33", "s34", "s35", "s36", "s37", "s38", "s39", "s40", "s41", "s42", "s43", "s44", "s45", "s46", "s47", "s48", "s49", "s50", "s51", "s52", "s53", "s54", "s55", "s56", "s57", "s58", "s59", "s60", "s61", "s62", "s63", "s64", "s65", "s66", "s67", "s68", "s69", "s70", "s71", "s72", "s73", "s74", "s75", "s76", "s77", "s78", "s79", "s80", "s81", "s82", "s83", "s84", "s85", "s86", "s87", "s88", "s89", "s90", "s91", "s92", "s93", "s94", "s95", "s96", "s97", "s98", "s99", "vcc", "scc", "memory"
#define CV_CLOB "v0", "v1", "v2", "v3", "v4", "v5", "v6", "v7", "v8", "v9", "v10", "v11", "v12", "v13", "v14", "v15", "v16", "v17", "v18", "v19", "v20", "v21", "v22", "v23", "v24", "v25", "v26", "v27", "v28", "v29", "v30", "v31", "v32", "v33", "v34", "v35", "v36", "v37", "v38", "v39", "v40", "v41", "v42", "v43", "v44", "v45", "v46", "v47", "v48", "v49", "v50", "v51", "v52", "v53", "v54", "v55", "v56", "v57", "v58", "v59", "v60", "v61", "v62", "v63", "v64", "v65", "v66", "v67", "v68", "v69", "v70", "v71", "v72", "v73", "v74", "v75", "v76", "v77", "v78", "v79", "v80", "v81", "v82", "v83", "v84", "v85", "v86", "v87", "v88", "v89", "v90", "v91", "v92", "v93", "v94", "v95", "v96", "v97", "v98", "v99", "v100", "v101", "v102", "v103", "v104", "v105", "v106", "v107", "v108", "v109", "v110", "v111", "v112", "v113", "v114", "v115", "v116", "v117", "v118", "v119", "v120", "v121", "v122", "v123", "v124", "v125", "v126", "v127", "v128", "v129", "v130", "v131", "v132", "v133", "v134", "v135", "v136", "v137", "v138", "v139", "v140", "v141", "v142", "v143", "v144", "v145", "v146", "v147", "v148", "v149", "v150", "v151", "v152", "v153", "v154", "v155", "v156", "v157", "v158", "v159", "v160", "v161", "v162", "v163", "v164", "v165", "v166", "v167", "v168", "v169", "v170", "v171", "v172", "v173", "v174", "v175", "v176", "v177", "v178", "v179", "v180", "v181", "v182", "v183", "v184", "v185", "v186", "v187", "v188", "v189", "v190", "v191", "v192", "v193", "v194", "v195", "s69", "scc", "memory"
static constexpr unsigned G_STEP = 0x80000;
static constexpr unsigned G_CPY = 0x10000;
static constexpr unsigned G_HOFF = 0x40000;
static constexpr unsigned G_RM = 0x4;
static constexpr unsigned G_RP = 0x2;
static constexpr unsigned G_HP = 0x20;
static constexpr unsigned G_HKB = 0x8;
static constexpr unsigned G_RHO = 0x1400;
static constexpr unsigned G_RHS = 0x600;
static constexpr unsigned G_NARM = 0x3;
static constexpr unsigned G_WS_UM = 0x400000;
static constexpr unsigned G_WS_CTL = 0x5d0000;
static constexpr unsigned G_WS_DONE = 0x5f0000;
static constexpr unsigned G_WS_EP = 0x5f2000;
static constexpr unsigned G_WS_STAMPS = 0xc000000;
static constexpr unsigned G_C_TASK = 0x200;
static constexpr unsigned G_C_GX = 0x1000;
static constexpr unsigned G_C_GV = 0x4000;
static constexpr unsigned G_C_KM = 0x8000;
static constexpr unsigned G_C_UM = 0x9000;
static constexpr unsigned G_C_PC = 0xc000;
static constexpr unsigned G_C_UT = 0xd000;
static constexpr unsigned G_C_GK = 0x10000;
static constexpr unsigned G_C_YC = 0x11000;
static constexpr unsigned G_C_ERR = 0x100;
static constexpr unsigned G_C_ARR = 0x14000;
static constexpr unsigned G_C_KG = 0x14100;
static constexpr unsigned G_C_END = 0x1a000;
static constexpr unsigned G_CTF = 0x2000;
static constexpr unsigned G_HS8 = 0x80000;
static constexpr unsigned G_HS4 = 0x40000;
static constexpr unsigned G_UXS = 0x200000;
static constexpr unsigned G_UMH = 0xa0000;
static constexpr unsigned G_D_QX = 0x600000;
static constexpr unsigned G_D_KX = 0x1600000;
static constexpr unsigned G_D_KM = 0x2600000;
static constexpr unsigned G_D_UX = 0x2e00000;
static constexpr unsigned G_D_UMX = 0x7600000;
static constexpr unsigned G_D_PX = 0x8c00000;
static constexpr unsigned G_D_PM = 0x9000000;
static constexpr unsigned G_D_KT = 0x9100000;
static constexpr unsigned G_D_UT = 0x9600000;
static constexpr unsigned G_D_YB = 0x9900000;
static constexpr unsigned G_D_END = 0x9c00000;
static constexpr unsigned G_SKM = 0x7;
static constexpr unsigned G_GVN = 0x2;
static constexpr unsigned G_STG = 0x9000;
static constexpr unsigned G_NA_ = 0x1c;
static constexpr unsigned G_KROW = 0x30;
#define FLG(x_) (kDefault[5])
#define FLG2(x_) (kDefault[7])
static constexpr int kDefault[8] = {PRM_DEFAULT};
#define MFMA32(a_, b_, c_) __builtin_amdgcn_mfma_f32_16x16x32_bf16(__builtin_bit_cast(bf16x8, a_), __builtin_bit_cast(bf16x8, b_), c_, 0, 0, 0)
#define MFMA16(a_, b_, c_) __builtin_amdgcn_mfma_f32_16x16x16bf16_1k(__builtin_bit_cast(s16x4, a_), __builtin_bit_cast(s16x4, b_), c_, 0, 0, 0)
#define WLD8(d_, voff_, base_, off_) asm volatile("s_nop 4\n\tglobal_load_dwordx2 %0, %1, %2 offset:%3 sc1" : "=v"(d_) : "v"(voff_), "s"(base_), "i"(off_) : "memory")
#define WLD16(d_, voff_, base_, off_) asm volatile("s_nop 4\n\tglobal_load_dwordx4 %0, %1, %2 offset:%3 sc1" : "=v"(d_) : "v"(voff_), "s"(base_), "i"(off_) : "memory")
#define WST16(voff_, d_, base_, off_) asm volatile("s_nop 4\n\tglobal_store_dwordx4 %0, %1, %2 offset:%3 sc1\n\ts_nop 1" :: "v"(voff_), "v"(d_), "s"(base_), "i"(off_) : "memory")
#define WLD16X4(d0, d1, d2, d3, voff_, base_) asm volatile("s_nop 4\n\tglobal_load_dwordx4 %0, %4, %5 sc1\n\tglobal_load_dwordx4 %1, %4, %5 offset:1024 sc1\n\t" \
    "global_load_dwordx4 %2, %4, %5 offset:2048 sc1\n\tglobal_load_dwordx4 %3, %4, %5 offset:3072 sc1" \
    : "=&v"(d0), "=&v"(d1), "=&v"(d2), "=&v"(d3) : "v"(voff_), "s"(base_) : "memory")

__device__ __forceinline__ u32x4 pack8(const f32x4& a, const f32x4& b) {
    return u32x4{pk2bf(a[0], a[1]), pk2bf(a[2], a[3]), pk2bf(b[0], b[1]), pk2bf(b[2], b[3])};
}
template <class T> __device__ __forceinline__ T* uni(T* q) {
    const u64 v = (u64)q;
    unsigned lo = __builtin_amdgcn_readfirstlane((unsigned)v), hi = __builtin_amdgcn_readfirstlane((unsigned)(v >> 32));
    asm volatile("s_nop 4" : "+s"(lo), "+s"(hi));
    return (T*)(((u64)hi << 32) | lo);
}
#define CTLO (G_WS_CTL + (unsigned)p.prm[0])
template <bool FAST = false>
__device__ __forceinline__ void wait_word(const MainP& p, const char* f, int want, unsigned code) {
    unsigned tries = 0;
    while (__any((int)__hip_atomic_load((const gu32*)f, RLX_AGENT) < want)) {
        if (++tries > kWaitCap) { __hip_atomic_store((gu32*)(p.ws + G_WS_CTL + G_C_ERR), code, RLX_AGENT); break; }
        if ((tries & 1023u) == 0u && __hip_atomic_load((const gu32*)(p.ws + G_WS_CTL + G_C_ERR), RLX_AGENT) != 0xffffffffu) break;
        if (FAST) __builtin_amdgcn_s_sleep(1); else __builtin_amdgcn_s_sleep(10);
    }
}

__device__ __forceinline__ int skew(int s, int id) { return (s ^ (G_SKM * id)) & 63; }
__device__ __forceinline__ unsigned umx_base(int h) { return G_D_UMX + (h / 3) * 0x200000 + (h % 3) * G_UMH; }
typedef GAS unsigned long long gu64;
__device__ __forceinline__ void init_done(char* ws, int slot, unsigned val) {
    const unsigned e = __hip_atomic_load((const gu32*)(ws + G_WS_EP), RLX_AGENT);
    __hip_atomic_store((gu64*)(ws + G_WS_DONE + slot * 64), ((u64)val << 32) | (e + 1u), RLX_AGENT);
}
__device__ __forceinline__ void init_seen(char* ws, int lane, unsigned e) {
    for (unsigned tries = 0; tries < kWaitCap; ++tries) {
        if (!__any(__hip_atomic_load((const gu32*)(ws + G_WS_DONE + min(lane, 59) * 64), RLX_AGENT) != e + 1u)) break;
        __builtin_amdgcn_s_sleep(8);
    }
}
__device__ __forceinline__ void init_count(char* ws, int lane) { if (lane == 0) __hip_atomic_fetch_add((gu32*)(ws + G_WS_CTL + G_C_ARR), 1u, RLX_AGENT); }
__device__ __forceinline__ void fill_block(char* ws, int b, int tid, const u32x4& v0, const u32x4& v1) {
    const unsigned o = b * 8192 + tid * 16, o2 = o + 4096;
    asm volatile("global_store_dwordx4 %0, %1, %2 sc1\n\ts_nop 1" :: "v"(o), "v"(v0), "s"(ws) : "memory");
    asm volatile("global_store_dwordx4 %0, %1, %2 sc1\n\ts_nop 1" :: "v"(o2), "v"(v1), "s"(ws) : "memory");
}
__device__ __forceinline__ void init_fill(char* ws, const u16* mem0, int f, int tid) {
    const u32x4 sent = {0xffffffffu, 0xffffffffu, 0xffffffffu, 0xffffffffu};
    u32x4 v0 = sent, v1 = sent;
    if (f < 16) {
#pragma unroll
        for (int k = 0; k < 2; ++k) {
            const int q = tid + 256 * k + ((f & 2) ? 256 : 0), m = q & 15;
            if (!(f & 2) || (q >= 512 && q < 576)) {
                const unsigned* src = (const unsigned*)(mem0 + (2 * (m >> 2) + (m & 1)) * DM + 16 * (q >> 4) + (m & 2));
                const u32x4 o = {min(src[0], 0xfffffffeu), min(src[2], 0xfffffffeu), min(src[4], 0xfffffffeu), min(src[6], 0xfffffffeu)};
                if (k) v1 = o; else v0 = o;
            }
        }
    }
    constexpr int HB2 = 2 * ((G_HKB + 7) / 8), NH = 32 + 8 * G_RP, NP = NH + 8 * HB2;
#pragma unroll 1
    for (int q = f; q < NP + 144 + (int)(G_C_END >> 13); q += 48) {
        const int u = q - 32, y = q - NH;
        fill_block(ws, q < 32 ? (q >> 4) * 256 + ((q >> 2) & 3) * 8 + 4 + (q & 3) : q < NH ? ((u >> 2) & 1) * 256 + (u >> 3) * 8 + (u & 3) : q < NP ? (y & 1) * 256 + 32 + ((y >> 1) & 3) * 8 + (y >> 3) : q < NP + 144 ? q - NP + 512 : q - NP - 144 + (int)(G_WS_CTL >> 13), tid, v0, v1);
        v0 = sent; v1 = sent;
    }
    const unsigned e = __hip_atomic_load((const gu32*)(ws + G_WS_EP), RLX_AGENT);
    asm volatile("s_waitcnt vmcnt(0)" :: "v"(e) : "memory");
    __syncthreads();
    if (tid == 0) __hip_atomic_store((gu64*)(ws + G_WS_DONE + (12 + f) * 64), (u64)(e + 1u), RLX_AGENT);
}
__device__ __forceinline__ void init_cal(char* ws, char* smem, int id, int tid, int n) {
    const int sl = id % 6, w = tid >> 6;
    char* a = ws + (size_t)(sl >> 1) * 0x200000 + 0x1B0000 + (id / 6) * 16384 + (sl & 1) * 8192 + w * 2048;
    volatile unsigned* lds = (volatile unsigned*)smem;
    const u32x4 z = {0u, 0u, 0u, 0u};
    if (tid == 0) lds[4] = 0u;
    asm volatile("global_store_dwordx4 %0, %1, off sc1\n\tglobal_store_dwordx4 %0, %1, off offset:1024 sc1\n\ts_waitcnt vmcnt(0)" :: "v"(a + (tid & 63) * 16), "v"(z) : "memory");
    __syncthreads();
    if ((tid & 63) == 0) {
        unsigned best = 0xfffffffeu;
#pragma unroll 1
        for (int it = 0; it < 2 * n; ++it) {
            const unsigned c0 = (unsigned)__builtin_amdgcn_s_memtime();
            const unsigned v = __hip_atomic_fetch_or((gu32*)(a + it * 128), 0u, RLX_AGENT);
            asm volatile("s_waitcnt vmcnt(0)" :: "v"(v) : "memory");
            best = min(best, (unsigned)__builtin_amdgcn_s_memtime() - c0);
            if (best < 500u) lds[4] = 1u;
            if (it + 1 >= n && lds[4]) break;
        }
        lds[w] = best;
    }
    __syncthreads();
    if (tid == 0) init_done(ws, id, min(min(lds[0], lds[1]), min(lds[2], lds[3])));
    __syncthreads();
}

#define TST(slot_, i_)
#define PIN8(a) asm volatile("" : "+v"(a[0]), "+v"(a[1]), "+v"(a[2]), "+v"(a[3]), "+v"(a[4]), "+v"(a[5]), "+v"(a[6]), "+v"(a[7]))
struct AOps { u32x4 q[4], k[8]; int fl; };
struct APtr { const char* q; const char* k; const char* m; unsigned voff, vkm, voffm, vfl; };
__device__ __forceinline__ void attn_batch(const MainP& p, AOps& o, const APtr& a) {
    const char* q2 = a.q + 4096; const char* k2 = a.k + 4096;
    WLD16(o.q[0], a.voff, a.q, 0); WLD16(o.q[1], a.voff, a.q, 2048); WLD16(o.q[2], a.voff, q2, 0); WLD16(o.q[3], a.voff, q2, 2048);
    WLD16X4(o.k[0], o.k[1], o.k[2], o.k[3], a.voff, a.k); WLD16X4(o.k[4], o.k[5], o.k[6], o.k[7], a.voff, k2);
    asm volatile("global_load_dword %0, %1, %2 sc1" : "=v"(o.fl) : "v"(a.vfl), "s"(p.ws) : "memory");
}
__device__ __forceinline__ void attn_pin(AOps& o) {
    asm volatile("" : "+v"(o.fl), "+v"(o.q[0]), "+v"(o.q[1]), "+v"(o.q[2]), "+v"(o.q[3])); PIN8(o.k);
}
static constexpr int NFINE = 6;
static constexpr int NG1 = 64 - NFINE;
__device__ __forceinline__ void score_round4(float* r, const f32x4& x) {
    asm volatile("v_cvt_pk_bf16_f32 %0, 0, %4\n\tv_cvt_pk_bf16_f32 %1, 0, %5\n\tv_cvt_pk_bf16_f32 %2, 0, %6\n\tv_cvt_pk_bf16_f32 %3, 0, %7\n\t"
                 "v_mul_f32 %0, 0x3db504f3, %0\n\tv_mul_f32 %1, 0x3db504f3, %1\n\tv_mul_f32 %2, 0x3db504f3, %2\n\tv_mul_f32 %3, 0x3db504f3, %3\n\t"
                 "v_cvt_pk_bf16_f32 %0, 0, %0\n\tv_cvt_pk_bf16_f32 %1, 0, %1\n\tv_cvt_pk_bf16_f32 %2, 0, %2\n\tv_cvt_pk_bf16_f32 %3, 0, %3"
                 : "=&v"(r[0]), "=&v"(r[1]), "=&v"(r[2]), "=&v"(r[3]) : "v"(x[0]), "v"(x[1]), "v"(x[2]), "v"(x[3]));
}
__device__ __forceinline__ float vmax3(float a, float b, float c) { float r; asm("v_max3_f32 %0, %1, %2, %3" : "=v"(r) : "v"(a), "v"(b), "v"(c)); return r; }
template <int N>
__device__ __forceinline__ void attn_soft(float (&q)[12], u32x2& Pm, u32x4& Px) {
    float m = vmax3(q[0], q[1], q[2]);
#pragma unroll
    for (int i = 3; i < N; i += 2) m = vmax3(m, q[i], q[i + 1 < N ? i + 1 : i]);
    { auto a = __builtin_amdgcn_permlane16_swap(__float_as_uint(m), __float_as_uint(m), false, false); m = vmax3(m, __uint_as_float(a[0]), __uint_as_float(a[1])); }
    { auto a = __builtin_amdgcn_permlane32_swap(__float_as_uint(m), __float_as_uint(m), false, false); m = vmax3(m, __uint_as_float(a[0]), __uint_as_float(a[1])); }
    const float ml = -m * 1.4426950408889634f;
#pragma unroll
    for (int i = 0; i < N; ++i) q[i] = __builtin_amdgcn_exp2f(fmaf(q[i], 1.4426950408889634f, ml));
    float sum = 0.5f * ((q[0] + q[1]) + (q[2] + q[3])) + ((q[4] + q[5]) + (q[6] + q[7]));
    if (N > 8) sum += (q[8] + q[9]) + (q[10] + q[11]);
    sum = rowsum4(sum);
    const float inv = __builtin_amdgcn_rcpf(sum);
    Pm = u32x2{pk2bf(q[0] * inv, q[1] * inv), pk2bf(q[2] * inv, q[3] * inv)};
    Px = u32x4{pk2bf(q[4] * inv, q[5] * inv), pk2bf(q[6] * inv, q[7] * inv), N > 8 ? pk2bf(q[8] * inv, q[9] * inv) : 0u, N > 8 ? pk2bf(q[10] * inv, q[11] * inv) : 0u};
}
static constexpr int NA = G_NA_;
__device__ __forceinline__ int attn_next(int s) { return s < NA ? s + 1 : s + 2; }
__device__ __forceinline__ unsigned attn_flag(const MainP& p, int s, int hq) { return CTLO + G_C_PC + ((s > NA && (s & 1)) ? 1024 : 0) + hq * 128; }
struct BOps { u32x4 q[8], k[8]; };
#define WLD16X4A(d0, d1, d2, d3, voff_, base_) asm volatile("s_nop 4\n\tglobal_load_dwordx4 %0, %4, %5 sc1\n\tglobal_load_dwordx4 %1, %4, %5 offset:1024 sc1\n\t" \
    "global_load_dwordx4 %2, %4, %5 offset:2048 sc1\n\tglobal_load_dwordx4 %3, %4, %5 offset:3072 sc1" \
    : "=&a"(d0), "=&a"(d1), "=&a"(d2), "=&a"(d3) : "v"(voff_), "s"(base_) : "memory")
#define PIN8A(a) asm volatile("" : "+a"(a[0]), "+a"(a[1]), "+a"(a[2]), "+a"(a[3]), "+a"(a[4]), "+a"(a[5]), "+a"(a[6]), "+a"(a[7]))
#define MFMA_AV0(acc, a_, b_) asm volatile("v_mfma_f32_16x16x32_bf16 %0, %1, %2, 0" : "=&v"(acc) : "a"(a_), "v"(b_))
#define MFMA_AV(acc, a_, b_) asm volatile("v_mfma_f32_16x16x32_bf16 %0, %1, %2, %0" : "+v"(acc) : "a"(a_), "v"(b_))
__device__ __forceinline__ void attn_batch8(const MainP& p, BOps& o, const APtr& a) {
    const char* q2 = a.q + 4096; const char* k2 = a.k + 4096;
    WLD16X4(o.q[0], o.q[1], o.q[2], o.q[3], a.voff, a.q); WLD16X4(o.q[4], o.q[5], o.q[6], o.q[7], a.voff, q2);
    WLD16X4A(o.k[0], o.k[1], o.k[2], o.k[3], a.voff, a.k); WLD16X4A(o.k[4], o.k[5], o.k[6], o.k[7], a.voff, k2);
}
__device__ __forceinline__ void attn_step(const MainP& p, BOps& o, BOps& n1, BOps& n2, APtr& a, int& kg, int& kk, int s, int h, int w, int lane) {
    const int g = lane >> 4, c = lane & 15;
    const float scale = 0.08838834764831845f;
    TST(s * 64 + h * 2, 0);
    const int s2 = attn_next(attn_next(s));
    if (kg <= s2 || kk < s) {
        VMC0();
        wait_word<true>(p, p.ws + a.vfl, (lane & 1) ? s : s2 + 1, 13u);
    }
    TST(s * 64 + h * 2, 1);
    a.q = p.ws + G_D_QX + (size_t)h * G_HS8 + skew(s2, h) * 8192; a.k = a.q + (G_D_KX - G_D_QX); a.m = p.ws + G_D_KM + (size_t)h * G_HS4 + skew(s, h) * 4096;
    __builtin_amdgcn_sched_barrier(0);
    u32x4 km[4];
    int fl;
    WLD16X4A(km[0], km[1], km[2], km[3], a.vkm, a.m);
    asm volatile("global_load_dword %0, %1, %2 sc1" : "=v"(fl) : "v"(a.vfl), "s"(p.ws) : "memory");
    attn_batch8(p, n2, a);
    f32x4 y[5];
#pragma unroll
    for (int wc = 0; wc < 4; ++wc)
#pragma unroll
        for (int i = 0; i < 3; ++i) { if (wc == 0) MFMA_AV0(y[i], o.k[i >> 1], o.q[(i + 1) >> 1]); else MFMA_AV(y[i], o.k[wc * 2 + (i >> 1)], o.q[wc * 2 + ((i + 1) >> 1)]); }
    asm volatile("s_nop 7" : "+v"(y[0]), "+v"(y[1]), "+v"(y[2]));
    float q0[12], q1[12];
    score_round4(q0 + 4, y[0]); score_round4(q1 + 4, y[1]); score_round4(q1 + 8, y[2]);
#pragma unroll
    for (int i = 0; i < 4; ++i) {
        const bool vis = 4 * g + i <= c;
        q0[4 + i] = vis ? q0[4 + i] : -INFINITY; q1[8 + i] = vis ? q1[8 + i] : -INFINITY;
    }
    __builtin_amdgcn_sched_barrier(0);
    asm volatile("s_waitcnt vmcnt(16)\n\ts_barrier" : "+a"(km[0]), "+a"(km[1]), "+a"(km[2]), "+a"(km[3]), "+v"(fl) :: "memory");
    PIN8(n1.q); PIN8A(n1.k);
    { const unsigned fo = attn_flag(p, s, h >> 2); const int fv = s - 1;
      asm volatile("s_cmp_lg_u32 %3, 0\n\ts_cbranch_scc1 1f\n\tglobal_store_dword %0, %1, %2 sc1\n1:" :: "v"(fo), "v"(fv), "s"(p.ws), "s"(w) : "scc", "memory"); }
#pragma unroll
    for (int wc = 0; wc < 4; ++wc)
#pragma unroll
        for (int i = 0; i < 2; ++i) { if (wc == 0) MFMA_AV0(y[3 + i], km[0], o.q[i]); else MFMA_AV(y[3 + i], km[wc], o.q[wc * 2 + i]); }
    asm volatile("s_nop 7" : "+v"(y[3]), "+v"(y[4]));
    kg = __builtin_amdgcn_readlane(fl, 0); kk = __builtin_amdgcn_readlane(fl, 1);
    score_round4(q0, y[3]); score_round4(q1, y[4]);
    u32x2 Pm0, Pm1; u32x4 Px0, Px1;
    attn_soft<8>(q0, Pm0, Px0); attn_soft<12>(q1, Pm1, Px1);
    char* pxb = p.ws + G_D_PX + (size_t)(s * 32 + h) * 2048;
    char* pmb = p.ws + G_D_PM + (size_t)s * 16384 + h * 256; char* pm2 = pmb + 8192;
    WST16(a.voff, Px0, pxb, 0); WST16(a.voff, Px1, pxb, 1024);
    asm volatile("global_store_dwordx2 %0, %1, %3 sc1\n\tglobal_store_dwordx2 %0, %2, %4 sc1" :: "v"(a.voffm), "v"(Pm0), "v"(Pm1), "s"(pmb), "s"(pm2) : "memory");
    { const unsigned fo = attn_flag(p, s, h >> 2);
      asm volatile("s_cmp_lt_u32 %4, 46\n\ts_cbranch_scc1 1f\n\ts_waitcnt vmcnt(0)\n\ts_barrier\n\ts_cmp_lg_u32 %3, 0\n\ts_cbranch_scc1 1f\n\tglobal_store_dword %0, %1, %2 sc1\n1:"
                   :: "v"(fo), "v"(s), "s"(p.ws), "s"(w), "s"(s) : "scc", "memory"); }
    TST(s * 64 + h * 2, 2);
}
__device__ __forceinline__ void attn_role(const MainP& p, int s, int h, int w, int lane) {
    APtr a;
    a.q = p.ws + G_D_QX + (size_t)h * G_HS8 + skew(s, h) * 8192; a.k = a.q + (G_D_KX - G_D_QX);
    a.voff = lane * 16; a.vkm = (lane & 55) * 16; a.voffm = (lane & 15) * 16 + ((lane >> 4) & 1) * 8;
    a.vfl = CTLO + ((lane & 1) ? G_C_KM : G_C_GX) + h * 128;
    BOps o0, o1, o2;
    wait_word(p, p.ws + CTLO + G_C_GX + h * 128, attn_next(s) + 1, 10u);
    attn_batch8(p, o0, a);
    a.q = p.ws + G_D_QX + (size_t)h * G_HS8 + skew(attn_next(s), h) * 8192; a.k = a.q + (G_D_KX - G_D_QX);
    attn_batch8(p, o1, a);
    VMC0();
    PIN8(o0.q); PIN8A(o0.k); PIN8(o1.q); PIN8A(o1.k);
    int kg = -1, kk = -1;
    static_assert(NA % 2 == 0 && NG1 + 3 < NSEG, "ranges");
#pragma unroll 1
    for (;;) {
        attn_step(p, o0, o1, o2, a, kg, kk, s, h, w, lane);
        if ((s = attn_next(s)) >= NG1) break;
        attn_step(p, o1, o2, o0, a, kg, kk, s, h, w, lane);
        if ((s = attn_next(s)) >= NG1) break;
        attn_step(p, o2, o0, o1, a, kg, kk, s, h, w, lane);
        if ((s = attn_next(s)) >= NG1) break;
    }
    VMC0();
}
__device__ __forceinline__ unsigned umax4(const u32x4& v) { return max(max(v[0], v[1]), max(v[2], v[3])); }
__device__ __forceinline__ bool give_up(const MainP& p, unsigned& tries, unsigned code) {
    if (++tries <= kWaitCap / 4) return false;
    __hip_atomic_store((gu32*)(p.ws + G_WS_CTL + G_C_ERR), code, RLX_AGENT);
    return true;
}
__device__ __forceinline__ void task_attn_tail(const MainP& p, int s, int h, int w, int lane) {
    const int g = lane >> 4, c = lane & 15, qt = w & 1;
    const float scale = 0.08838834764831845f;
    APtr a;
    a.q = p.ws + G_D_QX + (size_t)h * G_HS8 + skew(s, h) * 8192 + qt * 1024; a.k = p.ws + G_D_KX + (size_t)h * G_HS8 + skew(s, h) * 8192; a.m = p.ws + G_D_KM + (size_t)h * G_HS4 + skew(s, h) * 4096;
    a.voff = lane * 16; a.vkm = (lane & 55) * 16; a.voffm = c * 16 + (g & 1) * 8; a.vfl = G_WS_CTL;
    AOps o;
    wait_word(p, p.ws + CTLO + G_C_GX + h * 128, s + 1, 14u);
    attn_batch(p, o, a);
    VMC0();
    attn_pin(o);
    float q[12];
    u32x4 km[4];
#pragma unroll
    for (int kt = 1; kt >= -1; --kt) {
        if (kt < 0) {
            TST(s * 64 + h * 2 + qt, 0);
            wait_word(p, p.ws + CTLO + G_C_KM + h * 128, s - 2, 15u);
            if (FLG(p.prm[5]) & 2) wait_word<true>(p, p.ws + CTLO + G_C_KM + h * 128, s - 1, 15u);
            for (unsigned tries = 0;;) {
                WLD16X4(km[0], km[1], km[2], km[3], a.vkm, a.m);
                asm volatile("s_waitcnt vmcnt(0)" : "+v"(km[0]), "+v"(km[1]), "+v"(km[2]), "+v"(km[3]) :: "memory");
                if (!__any(max(max(umax4(km[0]), umax4(km[1])), max(umax4(km[2]), umax4(km[3]))) == 0xffffffffu) || give_up(p, tries, 16u)) break;
            }
            TST(s * 64 + h * 2 + qt, 1);
        }
        f32x4 y = {0.f, 0.f, 0.f, 0.f};
#pragma unroll
        for (int wc = 0; wc < 4; ++wc) y = MFMA32(kt < 0 ? km[wc] : o.k[wc * 2 + (kt < 0 ? 0 : kt)], o.q[wc], y);
#pragma unroll
        for (int i = 0; i < 4; ++i) q[4 + 4 * kt + i] = (16 * kt + 4 * g + i <= 16 * qt + c) ? bfr(bfr(y[i]) * scale) : -INFINITY;
    }
    u32x2 Pm; u32x4 Px;
    attn_soft<12>(q, Pm, Px);
    char* pxb = p.ws + G_D_PX + (size_t)(s * 32 + h) * 2048 + qt * 1024;
    char* pmb = p.ws + G_D_PM + (size_t)s * 16384 + h * 256 + qt * 8192;
    WST16(a.voff, Px, pxb, 0);
    asm volatile("global_store_dwordx2 %0, %1, %2 sc1" :: "v"(a.voffm), "v"(Pm), "s"(pmb) : "memory");
    TST(s * 64 + h * 2 + qt, 2);
}

#define RLD(d_, voff_, base_, off_) asm volatile("global_load_dwordx4 %0, %1, %2 offset:%3 sc1" : "=v"(d_) : "v"(voff_), "s"(base_), "i"(off_) : "memory")
#define RLDA(d_, voff_, base_, off_) asm volatile("global_load_dwordx4 %0, %1, %2 offset:%3 sc1" : "=a"(d_) : "v"(voff_), "s"(base_), "i"(off_) : "memory")
#define MFMA_ACC_A(acc, a_, b_) asm volatile("v_mfma_f32_16x16x32_bf16 %0, %1, %2, %0" : "+v"(acc) : "a"(a_), "v"(b_))
#define VMW(n_) asm volatile("s_waitcnt vmcnt(%0)" :: "i"(n_) : "memory")
template <int NT, int DEPTH, bool TAIL>
__device__ __forceinline__ void task_out(const MainP& p, char* smem, int s, int ct0, int w, int lane) {
    const int g = lane >> 4, c = lane & 15;
    const unsigned voff = lane * 16;
    f32x4 acc[NT][2];
#pragma unroll
    for (int t = 0; t < NT; ++t) { acc[t][0] = f32x4{0.f, 0.f, 0.f, 0.f}; acc[t][1] = acc[t][0]; asm volatile("" : "+v"(acc[t][0]), "+v"(acc[t][1])); }
    const char* ub[NT];
#pragma unroll
    for (int t = 0; t < NT; ++t) ub[t] = uni(p.ws + G_D_UX + (size_t)(ct0 + t) * G_UXS + skew(s, ct0 + t) * 32768 + w * 8192);
    const char* pb = uni(p.ws + G_D_PX + (size_t)(s * 32 + w * 8) * 2048);
    const char* um = uni(p.ws + s * 9216 + ct0 * 256);
    const unsigned vu0 = umx_base(w * 8 + g) + c * 16, vu1 = umx_base(w * 8 + 4 + g) + c * 16;
    const char* pm = uni(p.ws + G_D_PM + (size_t)s * 16384 + w * 2048);
    constexpr int NR = DEPTH + 1 > 10 ? 10 : DEPTH + 1;
    u32x4 a[NR][NT], b[10][2];
    unsigned vb = voff, va = voff;
    TST(4096 + (s * 36 + ct0) * 4 + w, 0);
    const int f0 = (int)__hip_atomic_load((const gu32*)(p.ws + CTLO + (lane < 8 ? G_C_UM + (w * 8 + lane) * 128 : lane < 16 ? G_C_GV + ((s & (G_GVN - 1)) * 32 + w * 8 + lane - 8) * 128
                                                                                                                                : attn_flag(p, s, w * 2 + (lane & 1)) - CTLO)), RLX_AGENT);
    const bool early = !TAIL && !__any(f0 < s);
    if (!early) wait_word(p, p.ws + CTLO + (lane < 8 ? G_C_GV + ((s & (G_GVN - 1)) * 32 + w * 8 + lane) * 128 : G_C_UM + w * 8 * 128), lane < 8 ? s : s - 3, 21u);
#define ALOAD(q) do { if ((q) < 8) { _Pragma("unroll") for (int t = 0; t < NT; ++t) RLDA(a[(q) % NR][t], va, ub[t], 0);  \
                                     asm volatile("v_add_u32 %0, 0x400, %0" : "+v"(va)); }                                            \
                      else { _Pragma("unroll") for (int t = 0; t < NT; ++t) RLDA(a[(q) % NR][t], ((q) & 1) ? vu1 : vu0, um, t * 256); } } while (0)
    constexpr int D0 = DEPTH < 8 ? DEPTH : 8;
    asm volatile("s_nop 4");
#pragma unroll
    for (int q = 0; q < D0; ++q) ALOAD(q);
    asm volatile("s_cmp_lg_u32 %0, 0\n\ts_cbranch_scc1 1f\n\ts_waitcnt vmcnt(0)\n1:\n\ts_waitcnt vmcnt(30)" :: "s"(__builtin_amdgcn_readfirstlane((int)early)) : "scc", "memory");
    unsigned tries = 0;
    if (TAIL) {
        const char* sp = pm + (lane & 7) * 256 + ((lane >> 3) & 1) * 8192;
        const int dl = (FLG(p.prm[5]) >> 26) & 31;
        if (dl) {
            wait_word<true>(p, p.ws + CTLO + G_C_KM + ((ct0 >> 1) + w) * 128, s - 1, 25u);
            for (int i = 0; i < dl + (s < 63 ? 5 : 0); ++i) __builtin_amdgcn_s_sleep(2);
        } else
        while (__any(__hip_atomic_load((const gu32*)sp, RLX_AGENT) == 0xffffffffu) && !give_up(p, tries, 23u)) __builtin_amdgcn_s_sleep(1);
    } else if (!early) wait_word(p, p.ws + CTLO + (lane < 8 ? G_C_UM + (w * 8 + lane) * 128 : attn_flag(p, s, w * 2 + (lane & 1)) - CTLO), s, 22u);
    if (!early) TST(4096 + (s * 36 + ct0) * 4 + w, 1);
    for (;;) {
    asm volatile("s_nop 4");
#pragma unroll
    for (int q = 0; q < 8; ++q) { RLD(b[q][0], vb, pb, 0); RLD(b[q][1], vb, pb, 1024); asm volatile("v_add_u32 %0, 0x800, %0" : "+v"(vb)); }
#pragma unroll
    for (int q = 8; q < 10; ++q) { RLD(b[q][0], voff, pm, (q & 1) * 1024); RLD(b[q][1], voff, pm + 8192, (q & 1) * 1024); }
#pragma unroll
    for (int q = D0; q < NR; ++q) ALOAD(q);
#pragma unroll
    for (int q = 0; q < 10; ++q) {
        const int issuedA = (NR + q < 10 ? NR + q : 10) - D0;
        if (q < D0) VMW(2 * (9 - q) + issuedA * NT); else VMW((issuedA - (q - D0 + 1)) * NT);
#pragma unroll
        for (int t = 0; t < NT; ++t) { MFMA_ACC_A(acc[t][0], a[q % NR][t], b[q][0]); MFMA_ACC_A(acc[t][1], a[q % NR][t], b[q][1]); }
        if (NR + q < 10) ALOAD(NR + q);
    }
    asm volatile("s_nop 7" ::: "memory");
#pragma unroll
    for (int t = 0; t < NT; ++t) asm volatile("" : "+v"(acc[t][0]), "+v"(acc[t][1]));
    if (!TAIL) break;
    f32x4 chk = acc[0][0];
#pragma unroll
    for (int t = 0; t < NT; ++t) chk += acc[t][1] + acc[t][0];
    const float ck = (chk[0] + chk[1]) + (chk[2] + chk[3]);
    if (!__any(ck != ck)) break;
    unsigned mx = 0;
#pragma unroll
    for (int q = 0; q < 10; ++q) { asm volatile("" : "+v"(b[q][0]), "+v"(b[q][1])); mx = max(mx, max(umax4(b[q][0]), umax4(b[q][1]))); }
#pragma unroll
    for (int t = 0; t < NT; ++t) { asm volatile("" : "+a"(a[8 % NR][t]), "+a"(a[9 % NR][t])); mx = max(mx, max(umax4(a[8 % NR][t]), umax4(a[9 % NR][t]))); }
    if (!__any(mx == 0xffffffffu) || give_up(p, tries, 24u)) break;
    vb = voff;
#pragma unroll
    for (int t = 0; t < NT; ++t) { acc[t][0] = f32x4{0.f, 0.f, 0.f, 0.f}; acc[t][1] = acc[t][0]; asm volatile("" : "+v"(acc[t][0]), "+v"(acc[t][1])); }
    }
    TST(4096 + (s * 36 + ct0) * 4 + w, 2);
    f32x4* red = (f32x4*)smem;
#pragma unroll
    for (int t = 0; t < NT; ++t) { red[((w * NT + t) * 2 + 0) * 64 + lane] = acc[t][0]; red[((w * NT + t) * 2 + 1) * 64 + lane] = acc[t][1]; }
    __syncthreads();
    u16* orow = p.out + (size_t)(32 * s + 16 * (w & 1) + c) * OUTW + 16 * ct0 + 4 * g;
#pragma unroll
    for (int t = 0; t < NT; ++t)
        if ((t & 1) == (w >> 1)) {
            const int m = w & 1;
            const f32x4 v = (red[((0 * NT + t) * 2 + m) * 64 + lane] + red[((1 * NT + t) * 2 + m) * 64 + lane]) +
                            (red[((2 * NT + t) * 2 + m) * 64 + lane] + red[((3 * NT + t) * 2 + m) * 64 + lane]);
            *(u32x2*)(orow + 16 * t) = u32x2{pk2bf(v[0], v[1]), pk2bf(v[2], v[3])};
        }
}

static constexpr int KROW = G_KROW;
static constexpr int KARR = 80 * KROW;
static constexpr int UCS = 2208;
struct BCh {
    u16 uc[4][UCS];
    f32x4 cst[NST];
    u16 Kh[KARR];
    u16 Kl[KARR];
};
static_assert(4 * sizeof(BCh) <= LDS_TOTAL - 32, "LDS");
__device__ __forceinline__ unsigned prm(unsigned hi, unsigned lo, unsigned sel) { return __builtin_amdgcn_perm(hi, lo, sel); }
#define BST(i)
#define BST2(i)
__device__ __forceinline__ void task_ut(const MainP& p, char* smem, int rb, int w, int lane) {
    const int tid = w * 64 + lane, g = lane >> 4, c = lane & 15;
    const u32x4* src = (const u32x4*)(p.U + (size_t)rb * 64 * DM);
    u32x4 v[18];
#pragma unroll
    for (int i = 0; i < 18; ++i) {
        v[i] = src[tid + 256 * i];
    }
    BST2(0);
#pragma unroll
    for (int i = 0; i < 18; ++i) { const int ch = tid + 256 * i, row = ch / 72; *(u32x4*)(smem + row * 1184 + (ch - 72 * row) * 16) = v[i]; }
    __syncthreads();
    const unsigned ad = (unsigned)(uintptr_t)smem + (16 * g + (c >> 2)) * 1184 + (w * 144 + 4 * (c & 3)) * 2;
#pragma unroll
    for (int i = 0; i < 9; ++i) {
        u32x2 x0, x1, x2, x3;
        asm volatile("ds_read_b64_tr_b16 %0, %4 offset:%5\n\tds_read_b64_tr_b16 %1, %4 offset:%6\n\tds_read_b64_tr_b16 %2, %4 offset:%7\n\tds_read_b64_tr_b16 %3, %4 offset:%8\n\ts_waitcnt lgkmcnt(0)"
                     : "=&v"(x0), "=&v"(x1), "=&v"(x2), "=&v"(x3) : "v"(ad), "i"(i * 32), "i"(4 * 1184 + i * 32), "i"(8 * 1184 + i * 32), "i"(12 * 1184 + i * 32) : "memory");
        v[2 * i] = u32x4{x0[0], x0[1], x1[0], x1[1]}; v[2 * i + 1] = u32x4{x2[0], x2[1], x3[0], x3[1]};
    }
    __syncthreads();
    BST2(1);
    char* tw = smem + w * (144 * 144);
#pragma unroll
    for (int i = 0; i < 9; ++i) { *(u32x4*)(tw + (16 * i + c) * 144 + g * 32) = v[2 * i]; *(u32x4*)(tw + (16 * i + c) * 144 + g * 32 + 16) = v[2 * i + 1]; }
    const char* dst = uni(p.ws + G_D_UT + (size_t)rb * 73728 + w * 18432);
#pragma unroll
    for (int i = 0; i < 18; ++i) {
        const u32x4 o = *(const u32x4*)(tw + (8 * i + (lane >> 3)) * 144 + (lane & 7) * 16);
        const unsigned vi = lane * 16 + i * 1024;
        WST16(vi, o, dst, 0);
    }
    BST2(2);
    const u32x4 sent = {0xffffffffu, 0xffffffffu, 0xffffffffu, 0xffffffffu};
#pragma unroll 1
    for (int sg = NG1; sg < 64; ++sg) {
        const unsigned ov = umx_base(rb) + sg * 9216 + tid * 16, ok = G_D_KM + rb * G_HS4 + skew(sg, rb) * 4096 + tid * 16;
        const unsigned op = tid < 128 ? G_D_PX + (sg * 32 + rb) * 2048 + tid * 16
                          : tid < 192 ? ov + 6144 : G_D_PM + sg * 16384 + ((tid >> 4) & 1) * 8192 + rb * 256 + (tid & 15) * 16;
        asm volatile("global_store_dwordx4 %0, %4, %5 sc1\n\tglobal_store_dwordx4 %1, %4, %5 sc1\n\tglobal_store_dwordx4 %2, %4, %5 sc1\n\t"
                     "global_store_dwordx4 %3, %4, %5 sc1\n\ts_nop 1" :: "v"(ov), "v"(ov + 4096), "v"(ok), "v"(op), "v"(sent), "s"(p.ws) : "memory");
    }
    BST2(3);
    VMC0();
    BST2(4);
    __syncthreads();
    if (tid == 0) __hip_atomic_store((gu32*)(p.ws + CTLO + G_C_UT + rb * 128), 1u, RLX_AGENT);
}
struct KPar { u16 ldt, pa[2][4]; };
__device__ __forceinline__ KPar kgen_par(const MainP& p, int d, int lane) {
    KPar k;
    k.ldt = p.logdt[d];
#pragma unroll
    for (int q = 0; q < 2; ++q) { const int i = d * NST + lane + 64 * q; k.pa[q][0] = p.logA[i]; k.pa[q][1] = p.Aim[i]; k.pa[q][2] = p.Cre[i]; k.pa[q][3] = p.Cim[i]; }
    return k;
}
struct Pull { const char* a; const char* b; const char* c; unsigned at; };
__device__ __forceinline__ void pull_inputs(Pull& q, int w, bool now) {
    if (q.a && (now || (int)((unsigned)__builtin_amdgcn_s_memrealtime() - q.at) >= 0)) {
#pragma unroll
        for (int i = 0; i < 3; ++i)
            if (i < 2 || w == 0) asm volatile("s_mov_b32 m0, %3\n\ts_nop 0\n\tglobal_load_lds_dword %0, %1\n\tglobal_load_lds_dword %0, %2" :: "v"(threadIdx.x * 128 + i * 32768), "s"(q.a), "s"(q.b), "s"(155648) : "memory");
        asm volatile("global_load_lds_dword %0, %1" :: "v"(min(threadIdx.x, 143u) * 128), "s"(q.c) : "memory");
        q.a = nullptr;
    }
}
__device__ __forceinline__ void task_kgen(const MainP& p, char* smem, int d, int w, int lane, const KPar& kp, bool first, unsigned ep, Pull& pl) {
    BCh& L = reinterpret_cast<BCh*>(smem)[w];
    const int g = lane >> 4, c = lane & 15, d0 = d;
    const int tid = (d & 3) * 64 + lane;
    unsigned* Kd = (unsigned*)L.uc;
    BST(10);
    const auto& pa = kp.pa;
    const float dt = bfr(expf(bf2f(kp.ldt)));
#pragma unroll
    for (int q = 0; q < 2; ++q) {
        const float ar = -bfr(expf(bf2f(pa[q][0])));
        const float cr = bf2f(pa[q][2]) * dt, ci = bf2f(pa[q][3]) * dt;
        L.cst[lane + 64 * q] = f32x4{ar * dt * 1.4426950408889634f, bf2f(pa[q][1]) * dt * 0.15915494309189535f,
                                     __log2f(hypotf(cr, ci)), atan2f(ci, cr) * 0.15915494309189535f};
    }
    BST(11);
    {
        f32x4 acc[3];
#pragma unroll
        for (int v = 0; v < 3; ++v) acc[v] = f32x4{0.f, 0.f, 0.f, 0.f};
        auto base_of = [](int v, int r) { return v == 0 ? (r & 7) + 128 * (r >> 3) : v == 1 ? (r < 8 ? 256 + 2 * (r & 3) + 128 * (r >> 2) : 512 + 4 * (r & 1) + 128 * ((r - 8) >> 1))
                                                                                           : 1024 + 128 * min(r, 8); };
        const float b0 = (float)base_of(0, c), b1 = (float)base_of(1, c), b2 = (float)base_of(2, c), off = (float)(8 * c);
#define KG_POLAR(h_, l_, lg_, f_, a_, b_) do { const float m_ = __builtin_amdgcn_exp2f(fmaf(cc[0], b_, lg_)), p_ = __builtin_amdgcn_fractf(fmaf(a_, b_, f_)); \
            const float re_ = m_ * __builtin_amdgcn_cosf(p_), im_ = m_ * __builtin_amdgcn_sinf(p_);                                                            \
            h_ = pk2bf(re_, im_); l_ = pk2bf(re_ - lo2f(h_), im_ - hi2f(h_)); } while (0)
#pragma unroll 1
        for (int t = 0; t < NST / 16; ++t) {
            u32x4 fh, fl, ah[3], al[3];
            pull_inputs(pl, w, false);
#pragma unroll
            for (int j = 0; j < 4; ++j) {
                const f32x4 cc = L.cst[16 * t + 4 * g + j];
                KG_POLAR(fh[j], fl[j], 0.f, 0.f, -cc[1], off);
                KG_POLAR(ah[0][j], al[0][j], cc[2], cc[3], cc[1], b0); KG_POLAR(ah[1][j], al[1][j], cc[2], cc[3], cc[1], b1); KG_POLAR(ah[2][j], al[2][j], cc[2], cc[3], cc[1], b2);
            }
#pragma unroll
            for (int v = 0; v < 3; ++v) { acc[v] = MFMA32(al[v], fh, acc[v]); acc[v] = MFMA32(ah[v], fl, acc[v]); acc[v] = MFMA32(ah[v], fh, acc[v]); }
        }
        BST(12);
        auto split = [](float k) { const unsigned hi = pk2bf(k, 0.f) & 0xffffu; return hi | (pk2bf(0.f, k - __uint_as_float(hi << 16)) & 0xffff0000u); };
#pragma unroll
        for (int v = 0; v < 3; ++v)
#pragma unroll
            for (int i = 0; i < 4; ++i) {
                const int pos = base_of(v, 4 * g + i) + 8 * c;
                if (pos <= 2048) Kd[pos] = split(2.0f * acc[v][i]);
            }
    }
    char* kt = p.ws + G_D_KT + (size_t)d * 8192;
    char* kt2 = kt + 4096;
    const unsigned voff = lane * 16;
#pragma unroll
    for (int q = 0; q < 4; ++q) {
        const int e = lane + 64 * q;
        unsigned t[8], k[8];
        const unsigned k0 = Kd[8 * e], k8 = Kd[8 * e + 8];
        if (q == 0) {
            const u32x4 a = *(const u32x4*)&Kd[8 * e], b = *(const u32x4*)&Kd[8 * e + 4];
            const bool two = lane >= 32;
            t[0] = a[0]; t[1] = two ? a[0] : a[1]; t[2] = a[2]; t[3] = two ? b[0] : a[3]; t[4] = b[0]; t[5] = two ? b[0] : b[1]; t[6] = b[2]; t[7] = two ? k8 : b[3];
        } else if (q == 1) {
            const unsigned k4 = Kd[8 * e + 4];
            t[0] = t[1] = t[2] = k0; t[3] = t[4] = t[5] = k4; t[6] = t[7] = k8;
        } else {
            t[0] = t[1] = t[2] = t[3] = k0; t[4] = (lane & 1) ? k8 : k0; t[5] = t[6] = t[7] = k8;
        }
#pragma unroll
        for (int j = 0; j < 8; ++j) k[j] = t[7 - j];
        u32x4 kh, kl;
#pragma unroll
        for (int j = 0; j < 4; ++j) { kh[j] = prm(k[2 * j + 1], k[2 * j], 0x05040100u); kl[j] = prm(k[2 * j + 1], k[2 * j], 0x07060302u); }
        WST16(voff, kh, kt, q * 1024); WST16(voff, kl, kt2, q * 1024);
    }
    BST(13);
    VMC0();
    BST(14);
    if (first) init_seen(p.ws, lane, ep);
    BST(15);
    if (lane == 0) __hip_atomic_fetch_add((gu32*)(p.ws + G_WS_CTL + G_C_KG + (d >> 2) * 128), 1u, RLX_AGENT);
}
__device__ __forceinline__ void task_s4d(const MainP& p, char* smem, int d0, int w, int lane, int& nb, bool more) {
    BCh* LL = reinterpret_cast<BCh*>(smem);
    BCh& L = LL[w];
    const int tid = w * 64 + lane, g = lane >> 4, c = lane & 15, d = d0 + w;
    BST(0);
    wait_word(p, p.ws + (lane < 32 ? CTLO + G_C_UT + lane * 128 : G_WS_CTL + G_C_KG + (d0 >> 2) * 128), lane < 32 ? 1 : 3, 30u);
    const float dsk = bf2f(p.Dskip[d]);
    const char* kt = p.ws + G_D_KT + (size_t)d * 8192;
    const char* kt2 = kt + 4096;
    const char* ut = p.ws + G_D_UT + d * 128;
    const unsigned voff = lane * 16;
    u32x4 kh[4], kl[4], uu[4];
#pragma unroll
    for (int q = 0; q < 4; ++q) { const unsigned vu = (8 * q + (lane >> 3)) * 73728 + (lane & 7) * 16; WLD16(uu[q], vu, ut, 0); }
    WLD16X4(kh[0], kh[1], kh[2], kh[3], voff, kt); WLD16X4(kl[0], kl[1], kl[2], kl[3], voff, kt2);
    const u32x4 z4 = {0u, 0u, 0u, 0u};
    *(u32x4*)&L.Kh[8 * lane] = z4; *(u32x4*)&L.Kl[8 * lane] = z4;
    *(u32x4*)&L.Kh[512 + 8 * (lane & 31)] = z4; *(u32x4*)&L.Kl[512 + 8 * (lane & 31)] = z4;
    *(u32x4*)&L.uc[0][64 + SL] = z4;
    *(u32x4*)&L.uc[(lane >> 3) & 3][8 * (lane & 7)] = z4;
    asm volatile("s_waitcnt vmcnt(0)" : "+v"(kh[0]), "+v"(kh[1]), "+v"(kh[2]), "+v"(kh[3]), "+v"(kl[0]), "+v"(kl[1]), "+v"(kl[2]), "+v"(kl[3]),
                 "+v"(uu[0]), "+v"(uu[1]), "+v"(uu[2]), "+v"(uu[3]) :: "memory");
    BST(1);
#pragma unroll
    for (int q = 0; q < 4; ++q) {
        const int e = lane + 64 * q, at = (16 + (e >> 2)) * KROW + 24 - 8 * (e & 3);
        *(u32x4*)&L.uc[0][64 + 8 * e] = uu[q];
        *(u32x4*)&L.Kh[at] = kh[q]; *(u32x4*)&L.Kl[at] = kl[q];
    }
    BST(2);
    BST(3);
#pragma unroll
    for (int q = 0; q < 4; ++q) {
        const int e = lane + 64 * q;
        const int ue = 56 + 8 * e;
        const u32x4 a = *(const u32x4*)&L.uc[0][ue];
        const u32x2 b = *(const u32x2*)&L.uc[0][ue + 8];
        const unsigned s0 = __builtin_amdgcn_alignbit(a[1], a[0], 16), s1 = __builtin_amdgcn_alignbit(a[2], a[1], 16), s2 = __builtin_amdgcn_alignbit(a[3], a[2], 16),
                       s3 = __builtin_amdgcn_alignbit(b[0], a[3], 16), s4 = __builtin_amdgcn_alignbit(b[1], b[0], 16);
        *(u32x4*)&L.uc[1][ue] = u32x4{s0, s1, s2, s3};
        *(u32x4*)&L.uc[2][ue] = u32x4{a[1], a[2], a[3], b[0]};
        *(u32x4*)&L.uc[3][ue] = u32x4{s1, s2, s3, s4};
    }
    if (lane == 0) {
        const u32x4 a = *(const u32x4*)&L.uc[0][56 + SL];
        const unsigned s0 = __builtin_amdgcn_alignbit(a[1], a[0], 16), s1 = __builtin_amdgcn_alignbit(a[2], a[1], 16), s2 = __builtin_amdgcn_alignbit(a[3], a[2], 16), s3 = a[3] >> 16;
        *(u32x4*)&L.uc[1][56 + SL] = u32x4{s0, s1, s2, s3};
        *(u32x4*)&L.uc[2][56 + SL] = u32x4{a[1], a[2], a[3], 0u};
        *(u32x4*)&L.uc[3][56 + SL] = u32x4{s1, s2, s3, 0u};
    }
    BST(4);
    {
        const int o = 33 + c + 8 * g;
        const unsigned ua = (unsigned)(uintptr_t)&L.uc[o & 3][o & ~3], ka = (unsigned)(uintptr_t)&L.Kh[(16 + c) * KROW + 8 * g];
        const f32x4* res = (const f32x4*)L.Kh + lane;
        asm volatile(ASM_CV :: "v"(ua), "v"(ka), "v"((unsigned)(uintptr_t)res) : CV_CLOB);
        BST(5);
#pragma unroll
        for (int par = 0; par < 2; ++par)
#pragma unroll
            for (int t = 0; t < 4; ++t) {
                const int n = 16 * (par + 2 * (16 * t + c)) + 4 * g;
                const u32x2 uu = *(const u32x2*)&L.uc[0][64 + n];
                const f32x4 y4 = res[(par * 4 + t) * 64];
                const float r0 = bfr(y4[0]) + bfr(lo2f(uu[0]) * dsk), r1 = bfr(y4[1]) + bfr(hi2f(uu[0]) * dsk);
                const float r2 = bfr(y4[2]) + bfr(lo2f(uu[1]) * dsk), r3 = bfr(y4[3]) + bfr(hi2f(uu[1]) * dsk);
                *(u32x2*)&L.uc[1][n] = u32x2{pk2bf(r0, r1), pk2bf(r2, r3)};
            }
    }
    __syncthreads();
    BST(6);
    u32x4 y[4];
#pragma unroll
    for (int ch = 0; ch < 4; ++ch) y[ch] = *(const u32x4*)&LL[ch].uc[1][8 * tid];
    const unsigned vt = tid * 16;
    char* yb = p.ws + G_D_YB + (size_t)(d0 >> 2) * 16384;
#pragma unroll
    for (int i = 0; i < 4; ++i) {
        const u32x4 o = {prm(y[1][i], y[0][i], 0x05040100u), prm(y[3][i], y[2][i], 0x05040100u), prm(y[1][i], y[0][i], 0x07060302u), prm(y[3][i], y[2][i], 0x07060302u)};
        char* yi = yb + i * 4096;
        WST16(vt, o, yi, 0);
    }
    BST(7);
    if (tid == 0 && more) nb = (int)(__hip_atomic_fetch_add((gu32*)(p.ws + CTLO + G_C_TASK + 128), 1u, RLX_AGENT) + 1u);
    VMC0();
    __syncthreads();
    if (tid == 0) __hip_atomic_fetch_add((gu32*)(p.ws + CTLO + G_C_YC + (d0 >> 6) * 128), 1u, RLX_AGENT);
    BST(8);
}
__device__ __forceinline__ void task_merge(const MainP& p, char* smem, int d0, int i, int w, int lane) {
    const unsigned vt = (w * 64 + lane) * 16;
    wait_word(p, p.ws + CTLO + G_C_YC + (d0 >> 6) * 128, 15, 31u);
    {
        const char* sb = p.ws + G_D_YB + (size_t)(d0 >> 6) * 262144 + i * 4096;
        u32x4 z[16];
#pragma unroll
        for (int j = 0; j < 16; ++j) { const char* sj = sb + j * 16384; WLD16(z[j], vt, sj, 0); }
        asm volatile("s_waitcnt vmcnt(0)" : "+v"(z[0]), "+v"(z[1]), "+v"(z[2]), "+v"(z[3]), "+v"(z[4]), "+v"(z[5]), "+v"(z[6]), "+v"(z[7]),
                     "+v"(z[8]), "+v"(z[9]), "+v"(z[10]), "+v"(z[11]), "+v"(z[12]), "+v"(z[13]), "+v"(z[14]), "+v"(z[15]) :: "memory");
        char* tw = smem + w * 18432;
#pragma unroll
        for (int j = 0; j < 8; ++j) {
            *(u32x4*)(tw + lane * 144 + j * 16) = u32x4{z[2 * j][0], z[2 * j][1], z[2 * j + 1][0], z[2 * j + 1][1]};
            *(u32x4*)(tw + (64 + lane) * 144 + j * 16) = u32x4{z[2 * j][2], z[2 * j][3], z[2 * j + 1][2], z[2 * j + 1][3]};
        }
#pragma unroll
        for (int k = 0; k < 16; ++k) {
            const int q = 8 * k + (lane >> 3);
            const int row = 8 * (64 * w + (q & 63)) + 2 * i + (q >> 6);
            *(u32x4*)(p.out + (size_t)row * OUTW + DM + (d0 & ~63) + 8 * (lane & 7)) = *(const u32x4*)(tw + q * 144 + (lane & 7) * 16);
        }
    }
}

static constexpr int NKG = 144, E_B = NKG + 36;
static constexpr int Q1 = 0, Q2 = Q1 + (NFINE - 2) * 25;
static constexpr int NTASK = E_B + Q2 + 68;

__device__ __forceinline__ void worker_body(const MainP& p, char* smem, const int w, const int lane, int quota) {
    const int tid = w * 64 + lane;
    int* slot = (int*)(smem + LDS_TOTAL - 16);
    int nb = -1;
    int nst = 0;
    gu32* qa = (gu32*)(p.ws + CTLO + G_C_TASK);
    for (;;) {
        if (tid == 0) {
            int t = E_B;
            if (quota > 0) {
                const int k = nb >= 0 ? nb : (int)(__hip_atomic_fetch_add(qa + 32, 1u, RLX_AGENT) + 1u);
                if (k < E_B) t = k;
                nb = -1;
            }
            if (t >= E_B) t = E_B + (int)(__hip_atomic_fetch_add(qa, 1u, RLX_AGENT) + 1u);
            *slot = t;
        }
        __syncthreads();
        const int t = __builtin_amdgcn_readfirstlane(*slot);
        if (t >= NTASK) break;
        quota = t < E_B ? quota - 1 : 0;
        if (t < E_B) {
            if (t < 128 || (t >= 144 && t < 160)) task_s4d(p, smem, 4 * (t < 128 ? t : t - 16), w, lane, nb, quota > 0);
            else task_merge(p, smem, 64 * ((t < 144 ? t - 128 : t - 144) >> 2), t & 3, w, lane);
        } else {
            const int u = t - E_B;
            const bool last2 = u >= Q2;
            const int sg = last2 ? 62 + (u - Q2) / 34 : NG1 + (u - Q1) / 25, r = last2 ? (u - Q2) % 34 : (u - Q1) % 25;
            if (r < 16) task_attn_tail(p, sg, 2 * r + (w >> 1), w, lane);
            else if (last2) task_out<2, 9, true>(p, smem, sg, 2 * (r - 16), w, lane);
            else task_out<4, 9, true>(p, smem, sg, 4 * (r - 16), w, lane);
            if (last2) break;
        }
        __syncthreads();
    }
}

__host__ __device__ constexpr unsigned phys(unsigned x) { return (x >> 13) * 16384 + (x & 8191); }
__host__ __device__ constexpr unsigned aMB(int f, int b) { return phys(f < 8 ? 16384 + f * 1024 + b : 28672 + b); }
__host__ __device__ constexpr unsigned aPX(int pr, int b) { return phys(pr * 1024 + b); }
__host__ __device__ constexpr unsigned aPM(int u, int b) { return phys(24576 + u * 1024 + b); }
#define IST(k)

static constexpr int L_RW = 160000;
__device__ __forceinline__ int lane_id() { int l; asm volatile("v_mbcnt_lo_u32_b32 %0, -1, 0\n\tv_mbcnt_hi_u32_b32 %0, -1, %0" : "=v"(l)); return l; }
typedef const __attribute__((address_space(4))) MainP* KArg;

__global__ __launch_bounds__(256, 1) void main_kernel(MainP p_) {
    __shared__ __attribute__((aligned(16))) char smem[LDS_TOTAL];
    KArg ka = (KArg)__builtin_amdgcn_kernarg_segment_ptr();
    static_assert(offsetof(MainP, W) == 0 && offsetof(MainP, ws) == 0x20 && offsetof(MainP, X) == 0x28 && offsetof(MainP, prm) == 0x40, "argument layout");
    asm volatile(ASM_EA :: "s"(ka), "s"(blockIdx.x), "v"(threadIdx.x) : ASM_CLOB);
    asm volatile("" : "+s"(ka));
    const unsigned t00 = (unsigned)__builtin_amdgcn_s_memrealtime();
    const int b = blockIdx.x, x = b & 7, ix = b >> 3;
    const int w = __builtin_amdgcn_readfirstlane(threadIdx.x >> 6);
    const bool isr = x < 4 && ix >= 16;
    if (x < 4 && ix >= 29) init_cal(ka->ws, smem, x * 3 + ix - 29, threadIdx.x, (FLG(ka->prm[5]) >> 16) & 7);
    if (x >= 4 ? ix >= 24 : (ix >= 25 && ix < 29)) init_fill(ka->ws, ka->mem0, x >= 4 ? (ix - 24) * 4 + (x & 3) : 32 + (ix - 25) * 4 + x, threadIdx.x);
    IST(0);
    const int NS = (FLG(ka->prm[5]) >> 8) & 3, NF = (FLG(ka->prm[5]) >> 21) & 7, RK = (FLG(ka->prm[5]) >> 10) & 3;
    const int NSP = FLG2(ka->prm[7]) & 7;
    const int NA1 = 18 + 4 * NSP, NB1 = NA1 - 3 * max(NSP - 4, 0), N1 = RK ? NA1 + (RK - 1) * NB1 : 0, N2 = N1 + (28 - 4 * NSP) * NS, NKE = min(N2 + 24 * NF, NKG);
    if (ix >= (x < 4 ? 16 : 24)) {
        MainP p;
        p.ws = ka->ws; p.logA = ka->logA; p.Aim = ka->Aim; p.Cre = ka->Cre; p.Cim = ka->Cim; p.logdt = ka->logdt;
        Pull pl = {nullptr, nullptr, nullptr, t00 + 400 + 50 * ((FLG(ka->prm[5]) >> 4) & 15)};
        const bool own = x < 4 && ix < 25 + NSP;
        int d0, nr, dd;
        if (own) {
            const int jt = ix < 25 ? ix - 16 : ix - 25, solo = jt < NSP;
            const int k = ix >= 25 ? 18 + NSP + 3 * jt + w - 1 : solo ? 3 * jt + w - 1 : NSP + 2 * jt + w - 2;
            d0 = k < NB1 ? x * NB1 + k : 4 * NB1 * RK + x * (NA1 - NB1) + k - NB1; nr = w < 2 - solo ? 0 : k < NB1 ? RK : min(RK, 1); dd = 4 * NB1;
            if (threadIdx.x < 8) ((unsigned*)(smem + L_RW))[threadIdx.x] = 0xffffffffu;
        } else if (x < 4) { d0 = 4 * (N1 + (ix - 25 - NSP) * 4 + x) + w; nr = NS; dd = 4 * (28 - 4 * NSP); }
        else {
            d0 = 4 * (N2 + (ix - 24) * 4 + (x & 3)) + w; nr = ix < 30 ? NF : 0; dd = 96;
            const size_t o = (size_t)((ix - 24) * 4 + (x & 3)) * 73728; pl.a = (const char*)ka->X + o; pl.b = (const char*)ka->U + o;
            pl.c = (const char*)((x & 2) ? ((x & 1) ? ka->Cim : ka->Cre) : ((x & 1) ? ka->Aim : ka->logA)) + (ix - 24) * 18432;
        }
        while (nr > 0 && d0 + dd * (nr - 1) >= 4 * NKG) --nr;
        KPar kn;
        if (nr > 0) kn = kgen_par(p, d0, threadIdx.x & 63);
        __builtin_amdgcn_sched_barrier(0);
        const unsigned ep = __hip_atomic_load((const gu32*)(p.ws + G_WS_EP), RLX_AGENT);
        asm volatile("s_waitcnt vmcnt(0)" :: "v"(ep) : "memory");
        __syncthreads();
        if (x >= 4 && ix == 24) {
            const char* xs = (const char*)ka->X + threadIdx.x * 128;
#pragma unroll
            for (int i = 0; i < 7; ++i) asm volatile("s_mov_b32 m0, %1\n\ts_nop 0\n\tglobal_load_lds_dword %0, off" :: "v"(xs + i * 32768), "s"(155648) : "memory");
        }
        {
            const int q = x < 4 ? ix - 25 : ix - 26;
            if (q >= 0 && q < 4 && (FLG(ka->prm[5]) & 1)) {
                const int hs = (x & 3) * 8 + 4 * (q & 1) + ((1 - (int)((u64)ka->W[q & 2] >> 8)) & 3);
                const char* src = (q & 2) ? (const char*)ka->W[3] + (size_t)hs * 147456 + threadIdx.x * 128 : (const char*)ka->W[x < 4] + hs * 256 + (threadIdx.x >> 1) * 8192 + (threadIdx.x & 1) * 128;
                const unsigned st = (q & 2) ? 32768u : 1048576u;
#pragma unroll
                for (int i = 0; i < 5; ++i)
                    if (i < 4 || w < 2) {
                        if (FLG(ka->prm[5]) & 4) asm volatile("s_mov_b32 m0, %1\n\ts_nop 0\n\tglobal_load_lds_dword %0, off nt" :: "v"(src + (size_t)i * st), "s"(155648) : "memory");
                        else asm volatile("s_mov_b32 m0, %1\n\ts_nop 0\n\tglobal_load_lds_dword %0, off" :: "v"(src + (size_t)i * st), "s"(155648) : "memory");
                    }
            }
        }
        if (nr == 0) init_seen(p.ws, threadIdx.x & 63, ep);
#pragma unroll 1
        for (int r = 0; r < nr; ++r) {
            const KPar kp = kn;
            kn = kgen_par(p, d0 + dd * min(r + 1, nr - 1), threadIdx.x & 63);
            __builtin_amdgcn_sched_barrier(0);
            task_kgen(p, smem, d0 + dd * r, w, threadIdx.x & 63, kp, r == 0, ep, pl);
        }
        if (w == 0) init_count(p.ws, threadIdx.x & 63);
        while (pl.a) { pull_inputs(pl, w, false); __builtin_amdgcn_s_sleep(8); }
        if (x < 4 && !own) return;
        asm volatile("" : "+s"(ka));
    }
    if (isr) {
        const int lane = threadIdx.x & 63, g = lane >> 4, c = lane & 15;
        const int jt = ix < 25 ? ix - 16 : ix - 25, solo = jt < NSP, par = solo ? ix >= 25 : w;
        const unsigned lb = w * G_STG;
        unsigned* ct = (unsigned*)(smem + lb) + lane;
        asm volatile("" :: "v"(ct) : "memory");
#define CTW(k, val) ct[(k) * 64] = (unsigned)(val)
        const u64 ws = (u64)ka->ws, prm = (u64)ka + 0x40;
        const u64 stb = 0;
        IST(1);
        CTW(0, lane * 16);
        const int j = x * 9 + jt;
        const int rot = (jt * 16) / 9;
        if (w >= 2 - solo) return;
        CTW(1, lane * 16 + 4096);
        CTW(2, aMB(j >> 2, (j & 3) * 256 + (4 * g + 2 * ((c >> 1) & 1) + (c & 1)) * 16 + (c >> 2) * 4));
        for (int k = 0; k < (int)G_NARM; ++k) {
            constexpr int N0 = G_HKB, N1 = N0 + (9 * G_RM + 3) / 4, N2 = N1 + 5 * G_RP;
            static_assert(N2 <= 9 * G_NARM, "shares");
            const int q = min(jt + 9 * k, N2 - 1), qm = min(x * (N1 - N0) + q - N0, (int)(9 * G_RM) - 1), qp = x * 5 * G_RP + q - N1;
            const int cp = q < N0 ? 4 + x : q < N1 ? qm / 9 : qp / 20, kb = q < N0 ? q : q < N1 ? (qm % 9 == 8 ? 28 : 16 + qm % 9) : (qp % 20 < 16 ? qp % 20 : 8 + qp % 20);
            CTW(47 + k, cp * G_CPY + phys(kb * 1024 + lane * 16));
        }
        const unsigned pc = x * G_RP / 4 * G_CPY;
        CTW(3, (lane & 3) * G_CPY + G_HOFF + G_RHO + ((lane >> 2) & 1) * G_RHS + j * 32); CTW(46, x * G_CPY + G_HOFF + G_RHO + min(lane, 35) * 32);
        CTW(4, (c & 2) ? 0xEEEE : 0x4444); CTW(5, (c & 1) ? 0x03020706u : 0x05040100u);
        CTW(6, G_WS_CTL + G_C_GV + (par * 32 + (lane & 31)) * 128); CTW(7, x * G_CPY + G_HOFF + (lane & 31) * G_HP); CTW(8, lane * 4); CTW(9, L_RW);
        for (int k = 0; k < 16; ++k) { const int i = (k + rot) & 15; CTW(10 + k, pc + aPX(i, lane * 16)); CTW(26 + k, i * 2048 + lane * 16); }
        for (int k = 0; k < 4; ++k) CTW(42 + k, pc + aPM(k, lane * 16));
        IST(2);
        asm volatile(ASM_R :: "s"(ws), "s"(ws), "s"(ws), "s"(__builtin_amdgcn_readfirstlane(par)), "s"(__builtin_amdgcn_readfirstlane(j)), "s"((u64)__builtin_amdgcn_readfirstlane(2 * rot) + ((u64)__builtin_amdgcn_readfirstlane(solo) << 32)), "s"(prm), "s"(lb), "s"(stb) : ASM_CLOB);
    }
    asm volatile("" : "+s"(ka));
    MainP p;
    p.prm[5] = ka->prm[5];
    p.U = ka->U; p.ws = ka->ws; p.logA = ka->logA; p.Aim = ka->Aim; p.Cre = ka->Cre; p.Cim = ka->Cim; p.logdt = ka->logdt; p.Dskip = ka->Dskip; p.out = ka->out;
    {
        auto cal = [&](int k) { return __hip_atomic_load((const gu32*)(p.ws + G_WS_DONE + k * 64 + 4), RLX_AGENT); };
        p.prm[0] = __builtin_amdgcn_readfirstlane((min(cal(5), cal(11)) < min(cal(4), cal(10)) ? 8192 : 0) ^ G_CTF);
    }
    const bool s4 = ix < 22 - ((FLG(p.prm[5]) >> 12) & 15);
    if (s4 && ix >= 8 && ix < 24) for (int i = 0; i < (int)(((ix & 7) * 4 + (x & 3)) * ((FLG(p.prm[5]) >> 24) & 3)); ++i) __builtin_amdgcn_s_sleep(3);
    if (x >= 4 && ix >= 8 && ix < 16) {
#pragma unroll 1
        for (int q = NKE + (ix - 8) * 4 + (x & 3); q < NKG; q += 32) {
            Pull pl = {nullptr, nullptr, nullptr, 0u};
            task_kgen(p, smem, 4 * q + w, w, lane_id(), kgen_par(p, 4 * q + w, lane_id()), false, 0u, pl);
        }
    }
    if (ix >= 24 && ix < 30) {
#pragma unroll 1
        for (int rb = (ix - 24) * 4 + (x & 3); rb < 32; rb += 24) {
            __syncthreads();
            task_ut(p, smem, rb, w, lane_id());
        }
        __syncthreads();
        const int lane = lane_id(), g = lane >> 4, c = lane & 15, ct0 = 6 * (ix - 24), s0 = x & 3;
        const unsigned lb = w * G_STG;
        unsigned* ct = (unsigned*)(smem + lb) + lane;
        asm volatile("" :: "v"(ct) : "memory");
        for (int t = 0; t < 6; ++t) CTW(t, (ct0 + t) * G_UXS + skew(0, ct0 + t) * 32768 + w * 8192 + 4096 + lane * 16);
        CTW(6, w * 16384 + 4096 + lane * 16); CTW(7, w * 16384 + 12288 + lane * 16); CTW(8, w * 2048 + lane * 16); CTW(9, w * 2048 + 8192 + lane * 16);
        CTW(10, umx_base(w * 8 + g) + ct0 * 256 + c * 16); CTW(11, umx_base(w * 8 + 4 + g) + ct0 * 256 + c * 16);
        const unsigned fa = lane < 8 ? CTLO + G_C_UM + (w * 8 + lane) * 128 : CTLO + G_C_GV + ((s0 & 1) * 32 + w * 8 + (lane & 7)) * 128;
        CTW(12, lane < 16 ? fa : attn_flag(p, s0, w * 2 + (lane & 1))); CTW(13, lane < 16 ? fa : attn_flag(p, s0 + 60, w * 2 + (lane & 1)));
        CTW(14, w * 12288 + lane * 16); CTW(15, w * 1024 + lane * 16);
        CTW(16, (16 * (w & 1) + c) * (OUTW * 2) + (16 * ct0 + 16 * (w >> 1) + 4 * g) * 2); CTW(17, lane * 4);
        static_assert(NG1 == 58, "as in the generated block");
        const u64 ws = (u64)p.ws;
        const u64 stb = 0;
        asm volatile(ASM_O :: "s"(ws), "s"((u64)p.out), "s"(ws), "s"(w), "s"(s0), "s"(ws), "s"(ws), "s"(lb), "s"(stb) : ASM_CLOB);
        asm volatile("" : "+s"(ka));
        p.prm[5] = ka->prm[5];
        p.U = ka->U; p.ws = ka->ws; p.logA = ka->logA; p.Aim = ka->Aim; p.Cre = ka->Cre; p.Cim = ka->Cim; p.logdt = ka->logdt; p.Dskip = ka->Dskip; p.out = ka->out;
    }
    if ((ix & 22) == 22 && x >= 4) { attn_role(p, ix >= 30 ? 0 : NA + 1, (x & 3) * 8 + (ix & 1) * 4 + w, w, lane_id());
        if (b == 255) {
            wait_word(p, p.ws + G_WS_CTL + G_C_ARR, 255, 50u);
            if (w == 0 && lane_id() == 0) __hip_atomic_fetch_add((gu32*)(p.ws + G_WS_EP), 1u, RLX_AGENT);
        }
        __syncthreads();
    }
    worker_body(p, smem, w, lane_id(), s4 ? 1000 : 0);
}

extern "C" __global__ void code_end_marker() {}

static constexpr int g_prm[8] = {PRM_DEFAULT};

extern "C" void kernel_launch(void* const* d_in, const int* in_sizes, int n_in, void* d_out, int out_size,
                              void* d_ws, size_t ws_size, hipStream_t stream) {
    char* ws = (char*)(((uintptr_t)d_ws + 0x1FFFFF) & ~(uintptr_t)0x1FFFFF);
    MainP p;
    for (int i = 0; i < 8; ++i) p.prm[i] = g_prm[i];
    p.mem0 = (const u16*)d_in[1];
    for (int i = 0; i < 4; ++i) p.W[i] = (const u16*)d_in[2 + i];
    p.X = (const u16*)d_in[0]; p.U = (const u16*)d_in[7]; p.ws = ws;
    p.logA = (const u16*)d_in[8]; p.Aim = (const u16*)d_in[9]; p.Cre = (const u16*)d_in[10]; p.Cim = (const u16*)d_in[11];
    p.logdt = (const u16*)d_in[12]; p.Dskip = (const u16*)d_in[13]; p.out = (u16*)d_out;
    main_kernel<<<256, 256, 0, stream>>>(p);
}
```

```cpp
#include <hip/hip_runtime.h>
#include <hip/hip_bf16.h>
#include <stdint.h>
#include <stddef.h>

typedef __attribute__((ext_vector_type(8))) __bf16 bf16x8;
typedef __attribute__((ext_vector_type(4))) short s16x4;
typedef __attribute__((ext_vector_type(4))) float f32x4;
typedef __attribute__((ext_vector_type(4))) unsigned u32x4;
typedef __attribute__((ext_vector_type(2))) unsigned u32x2;
typedef __attribute__((ext_vector_type(2))) float f32x2;
typedef unsigned short u16;
typedef unsigned long long u64;

#define GAS __attribute__((address_space(1)))
typedef GAS unsigned gu32;
#define RLX_AGENT __ATOMIC_RELAXED, __HIP_MEMORY_SCOPE_AGENT

static constexpr int DM = 576, NH = 32, DH = 128, MM = 8, SEG = 32, NSEG = 64, SL = 2048;
static constexpr int HD = NH * DH;
static constexpr int OUTW = 2 * DM;
static constexpr int NST = 128;

__device__ __forceinline__ float bf2f(u16 h) { return __uint_as_float(((unsigned)h) << 16); }
typedef __attribute__((ext_vector_type(2))) __bf16 bf16x2_t;
__device__ __forceinline__ unsigned pk2bf(float a, float b) {
    bf16x2_t v; v[0] = (__bf16)a; v[1] = (__bf16)b;
    return __builtin_bit_cast(unsigned, v);
}
__device__ __forceinline__ float bfr(float f) { return __uint_as_float(pk2bf(f, f) << 16); }
__device__ __forceinline__ float lo2f(unsigned u) { return __uint_as_float(u << 16); }
__device__ __forceinline__ float hi2f(unsigned u) { return __uint_as_float(u & 0xffff0000u); }
__device__ __forceinline__ float rowmax4(float x) {
    auto a = __builtin_amdgcn_permlane16_swap(__float_as_uint(x), __float_as_uint(x), false, false);
    x = fmaxf(__uint_as_float(a[0]), __uint_as_float(a[1]));
    auto b = __builtin_amdgcn_permlane32_swap(__float_as_uint(x), __float_as_uint(x), false, false);
    return fmaxf(__uint_as_float(b[0]), __uint_as_float(b[1]));
}
__device__ __forceinline__ float rowsum4(float x) {
    auto a = __builtin_amdgcn_permlane16_swap(__float_as_uint(x), __float_as_uint(x), false, false);
    x = __uint_as_float(a[0]) + __uint_as_float(a[1]);
    auto b = __builtin_amdgcn_permlane32_swap(__float_as_uint(x), __float_as_uint(x), false, false);
    return __uint_as_float(b[0]) + __uint_as_float(b[1]);
}

struct MainP {
    const u16* W[4];
    char* ws; const u16* X; const u16* mem0; const u16* U;
    int prm[8];
    const u16* logA; const u16* Aim; const u16* Cre; const u16* Cim; const u16* logdt; const u16* Dskip;
    u16* out;
};
static constexpr int LDS_TOTAL = 163840;
static constexpr unsigned kWaitCap = 1u << 18;
#define VMC0() asm volatile("s_waitcnt vmcnt(0)" ::: "memory")
#define PRM_DEFAULT 1090, 600, 96, 680, 824, 1615005555, 15728816, 7
#define ASM_EA \
    "s_load_dwordx8 s[40:47], %0, 0\n\t" \
    "s_load_dwordx4 s[36:39], %0, 0x20\n\t" \
    "s_load_dwordx8 s[84:91], %0, 0x40\n\t" \
    "s_getpc_b64 s[54:55]\n\t" \
    "ea_pc_%=:\n\t" \
    "s_bfe_u32 s69, %1, 0x20003\n\t" \
    "s_lshl_b32 s69, s69, 15\n\t" \
    "v_lshl_add_u32 v1, %2, 7, s69\n\t" \
    "v_min_u32 v1, code_end_marker-ea_pc_%=, v1\n\t" \
    "s_mov_b32 m0, 155648\n\t" \
    "v_readfirstlane_b32 s66, %2\n\t" \
    "global_load_lds_dword v1, s[54:55]\n\t" \
    "s_bitcmp1_b32 %1, 2\n\t" \
    "s_cselect_b32 s69, 24, 16\n\t" \
    "s_lshr_b32 s70, %1, 3\n\t" \
    "s_cmp_ge_u32 s70, s69\n\t" \
    "s_cbranch_scc1 ea_done_%=\n\t" \
    "s_lshr_b32 s66, s66, 6\n\t" \
    "s_and_b32 s69, %1, 0xc4\n\t" \
    "s_cmp_eq_u32 s69, 4\n\t" \
    "s_cselect_b32 s69, 72, 36\n\t" \
    "s_add_u32 s82, s66, s69\n\t" \
    "s_lshl_b32 s82, s82, 10\n\t" \
    "s_mov_b32 m0, s82\n\t" \
    "s_and_b32 s67, %1, 3\n\t" \
    "s_lshl_b32 s67, s67, 3\n\t" \
    "s_and_b32 s69, s70, 7\n\t" \
    "s_add_u32 s67, s67, s69\n\t" \
    "s_lshl_b32 s69, s67, 8\n\t" \
    "v_lshrrev_b32 v1, 4, %2\n\t" \
    "v_lshrrev_b32 v2, 3, %2\n\t" \
    "v_and_b32 v2, 6, v2\n\t" \
    "v_and_or_b32 v2, v1, 8, v2\n\t" \
    "v_xor_b32 v2, v2, %2\n\t" \
    "v_and_b32 v2, 15, v2\n\t" \
    "v_lshl_add_u32 v2, v2, 4, s69\n\t" \
    "v_lshl_add_u32 v200, v1, 13, v2\n\t" \
    "s_mov_b32 s71, 16\n\t" \
    "s_bitcmp1_b32 %1, 2\n\t" \
    "s_waitcnt lgkmcnt(0)\n\t" \
    "s_cselect_b64 s[48:49], s[42:43], s[40:41]\n\t" \
    "s_cselect_b64 s[40:41], s[40:41], s[42:43]\n\t" \
    "s_cmp_ge_u32 s70, 8\n\t" \
    "s_cselect_b64 s[42:43], s[44:45], s[48:49]\n\t" \
    "s_cselect_b64 s[44:45], s[46:47], s[40:41]\n\t" \
    "ea_lp1_%=:\n\t" \
    "global_load_lds_dwordx4 v200, s[42:43] nt\n\t" \
    "v_add_u32 v200, 131072, v200\n\t" \
    "s_add_u32 m0, m0, 4096\n\t" \
    "s_sub_u32 s71, s71, 1\n\t" \
    "s_cmp_lg_u32 s71, 0\n\t" \
    "s_cbranch_scc1 ea_lp1_%=\n\t" \
    "s_mov_b64 s[50:51], s[38:39]\n\t" \
    "s_cmp_lt_u32 s70, 8\n\t" \
    "s_cbranch_scc1 ea_w_%=\n\t" \
    "s_mul_i32 s69, s67, 147456\n\t" \
    "s_add_u32 s44, s44, s69\n\t" \
    "s_addc_u32 s45, s45, 0\n\t" \
    "s_and_b32 s69, s70, 16\n\t" \
    "s_lshl_b32 s69, s69, 1\n\t" \
    "s_add_u32 s67, s67, s69\n\t" \
    "s_bitcmp1_b32 %1, 2\n\t" \
    "s_cbranch_scc1 gv_begin_%=\n\t" \
    "s_branch vv_begin_%=\n\t" \
    "ea_w_%=:\n\t" \
    "s_bitcmp1_b32 %1, 2\n\t" \
    "s_cbranch_scc1 gx_begin_%=\n\t" \
    "qk_begin_%=:\n\t" \
    "v_mbcnt_lo_u32_b32 v223, -1, 0\n\t" \
    "v_mbcnt_hi_u32_b32 v223, -1, v223\n\t" \
    "s_mov_b32 s68, 262144\n\t" \
    "s_mov_b32 s72, 0\n\t" \
    "s_mov_b32 s83, -1\n\t" \
    "s_mov_b32 s65, 0\n\t" \
    "v_and_b32 v180, 3, v223\n\t" \
    "v_lshlrev_b32 v202, 3, v180\n\t" \
    "v_lshrrev_b32 v181, 5, v223\n\t" \
    "v_lshl_add_u32 v202, v181, 12, v202\n\t" \
    "v_bfe_u32 v182, v223, 2, 2\n\t" \
    "s_mov_b32 s70, 288\n\t" \
    "v_mad_u32_u24 v202, v182, s70, v202\n\t" \
    "v_bfe_u32 v183, v223, 4, 1\n\t" \
    "s_mov_b32 s70, 2176\n\t" \
    "v_mad_u32_u24 v202, v183, s70, v202\n\t" \
    "s_lshl_b32 s69, s66, 6\n\t" \
    "v_xor_b32 v202, s69, v202\n\t" \
    "v_xor_b32 v203, 32, v202\n\t" \
    "v_lshlrev_b32 v0, 4, v223\n\t" \
    "s_mul_i32 s69, s66, 2048\n\t" \
    "s_bfe_u32 s71, s67, 0x20003\n\t" \
    "s_mul_i32 s71, s71, 65536\n\t" \
    "s_add_u32 s69, s69, s71\n\t" \
    "s_add_u32 s69, s69, 32768\n\t" \
    "v_lshl_add_u32 v1, v223, 4, s69\n\t" \
    "s_bfe_u32 s71, s67, 0x20003\n\t" \
    "s_mul_i32 s69, s71, 65536\n\t" \
    "s_add_u32 s69, s69, 53248\n\t" \
    "v_lshl_add_u32 v2, v223, 4, s69\n\t" \
    "s_bfe_u32 s71, s67, 0x20003\n\t" \
    "s_mul_i32 s69, s71, 65536\n\t" \
    "s_add_u32 s69, s69, 32768\n\t" \
    "v_lshl_add_u32 v34, v223, 4, s69\n\t" \
    "s_mov_b32 s69, 17476\n\t" \
    "v_bfe_u32 v184, v223, 1, 1\n\t" \
    "v_mov_b32 v10, s69\n\t" \
    "s_mov_b32 s70, 43690\n\t" \
    "v_mad_u32_u24 v10, v184, s70, v10\n\t" \
    "v_mov_b32 v11, 0\n\t" \
    "s_waitcnt vmcnt(12) lgkmcnt(0)\n\t" \
    "s_barrier\n\t" \
    "s_add_u32 m0, s82, 65536\n\t" \
    "s_nop 0\n\t" \
    "global_load_lds_dwordx4 v200, s[42:43] nt\n\t" \
    "v_add_u32 v200, 131072, v200\n\t" \
    "s_add_u32 m0, s82, 69632\n\t" \
    "s_nop 0\n\t" \
    "global_load_lds_dwordx4 v200, s[42:43] nt\n\t" \
    "v_add_u32 v200, 131072, v200\n\t" \
    "s_add_u32 m0, s82, 73728\n\t" \
    "s_nop 0\n\t" \
    "global_load_lds_dwordx4 v200, s[42:43] nt\n\t" \
    "v_add_u32 v200, 131072, v200\n\t" \
    "s_add_u32 m0, s82, 77824\n\t" \
    "s_nop 0\n\t" \
    "global_load_lds_dwordx4 v200, s[42:43] nt\n\t" \
    "v_add_u32 v200, 131072, v200\n\t" \
    "ds_read_b64_tr_b16 a[0:1], v202 offset:36864\n\t" \
    "ds_read_b64_tr_b16 a[2:3], v202 offset:37888\n\t" \
    "ds_read_b64_tr_b16 a[4:5], v202 offset:45056\n\t" \
    "ds_read_b64_tr_b16 a[6:7], v202 offset:46080\n\t" \
    "ds_read_b64_tr_b16 a[72:73], v203 offset:36864\n\t" \
    "ds_read_b64_tr_b16 a[74:75], v203 offset:37888\n\t" \
    "ds_read_b64_tr_b16 a[76:77], v203 offset:45056\n\t" \
    "ds_read_b64_tr_b16 a[78:79], v203 offset:46080\n\t" \
    "s_mul_i32 s69, s66, 512\n\t" \
    "s_add_u32 s69, s69, 16384\n\t" \
    "v_and_b32 v185, 13, v223\n\t" \
    "v_lshl_add_u32 v12, v185, 5, s69\n\t" \
    "v_lshl_add_u32 v12, v184, 2, v12\n\t" \
    "v_lshrrev_b32 v186, 4, v223\n\t" \
    "v_lshl_add_u32 v12, v186, 3, v12\n\t" \
    "s_mov_b32 s69, 16384\n\t" \
    "v_and_b32 v187, 15, v223\n\t" \
    "v_lshl_add_u32 v13, v187, 3, s69\n\t" \
    "v_lshl_add_u32 v13, v186, 8, v13\n\t" \
    "s_bfe_u32 s71, s67, 0x20003\n\t" \
    "s_mul_i32 s69, s71, 65536\n\t" \
    "s_add_u32 s69, s69, 266240\n\t" \
    "v_and_b32 v188, 31, v223\n\t" \
    "v_lshl_add_u32 v20, v188, 5, s69\n\t" \
    "v_lshl_add_u32 v20, v181, 2, v20\n\t" \
    "s_mul_i32 s69, s67, 32\n\t" \
    "s_add_u32 s69, s69, 262144\n\t" \
    "v_lshl_add_u32 v21, v180, 16, s69\n\t" \
    "v_bfe_u32 v189, v223, 2, 3\n\t" \
    "v_lshlrev_b32 v199, 10, v189\n\t" \
    "v_min_u32 v199, 4096, v199\n\t" \
    "v_add_u32 v21, v199, v21\n\t" \
    "s_bfe_u32 s71, s67, 0x20003\n\t" \
    "s_mul_i32 s69, s71, 65536\n\t" \
    "s_add_u32 s69, s69, 267264\n\t" \
    "v_min_u32 v35, 35, v223\n\t" \
    "v_lshl_add_u32 v35, v35, 5, s69\n\t" \
    "v_lshlrev_b32 v22, 2, v223\n\t" \
    "s_waitcnt vmcnt(12) lgkmcnt(0)\n\t" \
    "s_barrier\n\t" \
    "s_add_u32 m0, s82, 81920\n\t" \
    "s_nop 0\n\t" \
    "global_load_lds_dwordx4 v200, s[42:43] nt\n\t" \
    "v_add_u32 v200, 131072, v200\n\t" \
    "s_add_u32 m0, s82, 86016\n\t" \
    "s_nop 0\n\t" \
    "global_load_lds_dwordx4 v200, s[42:43] nt\n\t" \
    "v_add_u32 v200, 131072, v200\n\t" \
    "s_add_u32 m0, s82, 90112\n\t" \
    "s_nop 0\n\t" \
    "global_load_lds_dwordx4 v200, s[42:43] nt\n\t" \
    "v_add_u32 v200, 131072, v200\n\t" \
    "s_add_u32 m0, s82, 94208\n\t" \
    "s_nop 0\n\t" \
    "global_load_lds_dwordx4 v200, s[42:43] nt\n\t" \
    "v_add_u32 v200, 131072, v200\n\t" \
    "ds_read_b64_tr_b16 a[8:9], v202 offset:53248\n\t" \
    "ds_read_b64_tr_b16 a[10:11], v202 offset:54272\n\t" \
    "ds_read_b64_tr_b16 a[12:13], v202 offset:61440\n\t" \
    "ds_read_b64_tr_b16 a[14:15], v202 offset:62464\n\t" \
    "ds_read_b64_tr_b16 a[80:81], v203 offset:53248\n\t" \
    "ds_read_b64_tr_b16 a[82:83], v203 offset:54272\n\t" \
    "ds_read_b64_tr_b16 a[84:85], v203 offset:61440\n\t" \
    "ds_read_b64_tr_b16 a[86:87], v203 offset:62464\n\t" \
    "s_mul_i32 s69, s67, 128\n\t" \
    "s_add_u32 s69, s69, 6127616\n\t" \
    "v_mov_b32 v14, s69\n\t" \
    "v_and_b32 v190, 57, v223\n\t" \
    "v_lshlrev_b32 v23, 4, v190\n\t" \
    "v_lshl_add_u32 v23, v184, 6, v23\n\t" \
    "v_bfe_u32 v191, v223, 2, 1\n\t" \
    "v_lshl_add_u32 v23, v191, 5, v23\n\t" \
    "v_add_u32 v15, 4096, v23\n\t" \
    "s_mul_i32 s69, s66, 512\n\t" \
    "s_add_u32 s69, s69, 8192\n\t" \
    "v_lshl_add_u32 v16, v187, 4, s69\n\t" \
    "v_lshl_add_u32 v16, v186, 2, v16\n\t" \
    "s_mov_b32 s69, 8192\n\t" \
    "v_and_b32 v192, 1, v223\n\t" \
    "v_lshl_add_u32 v17, v192, 3, s69\n\t" \
    "v_lshrrev_b32 v193, 2, v223\n\t" \
    "v_lshl_add_u32 v17, v193, 4, v17\n\t" \
    "s_mul_i32 s69, s67, 128\n\t" \
    "s_add_u32 s69, s69, 6160384\n\t" \
    "v_mov_b32 v24, s69\n\t" \
    "s_waitcnt vmcnt(12) lgkmcnt(0)\n\t" \
    "s_barrier\n\t" \
    "s_add_u32 m0, s82, 98304\n\t" \
    "s_nop 0\n\t" \
    "global_load_lds_dwordx4 v200, s[42:43] nt\n\t" \
    "v_add_u32 v200, 131072, v200\n\t" \
    "s_add_u32 m0, s82, 102400\n\t" \
    "s_nop 0\n\t" \
    "global_load_lds_dwordx4 v200, s[42:43] nt\n\t" \
    "v_add_u32 v200, 131072, v200\n\t" \
    "s_add_u32 m0, s82, 106496\n\t" \
    "s_nop 0\n\t" \
    "global_load_lds_dwordx4 v200, s[42:43] nt\n\t" \
    "v_add_u32 v200, 131072, v200\n\t" \
    "s_add_u32 m0, s82, 110592\n\t" \
    "s_nop 0\n\t" \
    "global_load_lds_dwordx4 v200, s[42:43] nt\n\t" \
    "v_add_u32 v200, 131072, v200\n\t" \
    "v_add_u32 v202, 69632, v202\n\t" \
    "v_add_u32 v203, 69632, v203\n\t" \
    "ds_read_b64_tr_b16 a[16:17], v202 offset:0\n\t" \
    "ds_read_b64_tr_b16 a[18:19], v202 offset:1024\n\t" \
    "ds_read_b64_tr_b16 a[20:21], v202 offset:8192\n\t" \
    "ds_read_b64_tr_b16 a[22:23], v202 offset:9216\n\t" \
    "ds_read_b64_tr_b16 a[88:89], v203 offset:0\n\t" \
    "ds_read_b64_tr_b16 a[90:91], v203 offset:1024\n\t" \
    "ds_read_b64_tr_b16 a[92:93], v203 offset:8192\n\t" \
    "ds_read_b64_tr_b16 a[94:95], v203 offset:9216\n\t" \
    "s_bfe_u32 s71, s66, 0x10000\n\t" \
    "s_mul_i32 s69, s71, 128\n\t" \
    "s_bfe_u32 s71, s67, 0x40000\n\t" \
    "s_mul_i32 s71, s71, 512\n\t" \
    "s_add_u32 s69, s69, s71\n\t" \
    "s_bfe_u32 s71, s67, 0x10004\n\t" \
    "s_mul_i32 s71, s71, 16384\n\t" \
    "s_add_u32 s69, s69, s71\n\t" \
    "v_lshl_add_u32 v18, v180, 2, s69\n\t" \
    "v_lshl_add_u32 v18, v191, 5, v18\n\t" \
    "v_bfe_u32 v194, v223, 3, 1\n\t" \
    "v_lshl_add_u32 v18, v194, 8, v18\n\t" \
    "v_lshl_add_u32 v18, v183, 4, v18\n\t" \
    "v_lshl_add_u32 v18, v181, 6, v18\n\t" \
    "s_bfe_u32 s71, s66, 0x10000\n\t" \
    "s_mul_i32 s69, s71, 128\n\t" \
    "s_bfe_u32 s71, s67, 0x10000\n\t" \
    "s_mul_i32 s71, s71, 8\n\t" \
    "s_add_u32 s69, s69, s71\n\t" \
    "s_bfe_u32 s71, s67, 0x40001\n\t" \
    "s_mul_i32 s71, s71, 256\n\t" \
    "s_add_u32 s69, s69, s71\n\t" \
    "s_add_u32 s69, s69, 49152\n\t" \
    "v_lshl_add_u32 v19, v184, 5, s69\n\t" \
    "v_and_b32 v195, 5, v193\n\t" \
    "v_lshl_add_u32 v19, v195, 2, v19\n\t" \
    "v_lshl_add_u32 v19, v181, 6, v19\n\t" \
    "v_add_u32 v3, 65536, v18\n\t" \
    "v_add_u32 v4, 65536, v19\n\t" \
    "s_waitcnt vmcnt(12) lgkmcnt(0)\n\t" \
    "s_barrier\n\t" \
    "s_add_u32 m0, s82, 0\n\t" \
    "s_nop 0\n\t" \
    "global_load_lds_dwordx4 v200, s[42:43] nt\n\t" \
    "v_add_u32 v200, 131072, v200\n\t" \
    "s_add_u32 m0, s82, 4096\n\t" \
    "s_nop 0\n\t" \
    "global_load_lds_dwordx4 v200, s[42:43] nt\n\t" \
    "v_add_u32 v200, 131072, v200\n\t" \
    "s_add_u32 m0, s82, 8192\n\t" \
    "s_nop 0\n\t" \
    "global_load_lds_dwordx4 v200, s[42:43] nt\n\t" \
    "v_add_u32 v200, 131072, v200\n\t" \
    "s_add_u32 m0, s82, 12288\n\t" \
    "s_nop 0\n\t" \
    "global_load_lds_dwordx4 v200, s[42:43] nt\n\t" \
    "v_add_u32 v200, 131072, v200\n\t" \
    "ds_read_b64_tr_b16 a[24:25], v202 offset:16384\n\t" \
    "ds_read_b64_tr_b16 a[26:27], v202 offset:17408\n\t" \
    "ds_read_b64_tr_b16 a[28:29], v202 offset:24576\n\t" \
    "ds_read_b64_tr_b16 a[30:31], v202 offset:25600\n\t" \
    "ds_read_b64_tr_b16 a[96:97], v203 offset:16384\n\t" \
    "ds_read_b64_tr_b16 a[98:99], v203 offset:17408\n\t" \
    "ds_read_b64_tr_b16 a[100:101], v203 offset:24576\n\t" \
    "ds_read_b64_tr_b16 a[102:103], v203 offset:25600\n\t" \
    "s_mov_b32 s77, 3019898880\n\t" \
    "s_mov_b32 s78, 3054198966\n\t" \
    "s_mov_b32 s71, 3\n\t" \
    "qk_E1_%=:\n\t" \
    "s_memtime s[54:55]\n\t" \
    "s_nop 0\n\t" \
    "s_memrealtime s[4:5]\n\t" \
    "s_nop 0\n\t" \
    "s_memrealtime s[6:7]\n\t" \
    "s_nop 0\n\t" \
    "s_memrealtime s[8:9]\n\t" \
    "s_nop 0\n\t" \
    "s_memrealtime s[10:11]\n\t" \
    "s_nop 0\n\t" \
    "s_memrealtime s[12:13]\n\t" \
    "s_nop 0\n\t" \
    "s_memrealtime s[14:15]\n\t" \
    "s_nop 0\n\t" \
    "s_memrealtime s[16:17]\n\t" \
    "s_nop 0\n\t" \
    "s_memrealtime s[18:19]\n\t" \
    "s_nop 0\n\t" \
    "s_memrealtime s[20:21]\n\t" \
    "s_nop 0\n\t" \
    "s_memrealtime s[22:23]\n\t" \
    "s_nop 0\n\t" \
    "s_memrealtime s[24:25]\n\t" \
    "s_nop 0\n\t" \
    "s_memrealtime s[26:27]\n\t" \
    "s_nop 0\n\t" \
    "s_memrealtime s[28:29]\n\t" \
    "s_nop 0\n\t" \
    "s_memtime s[56:57]\n\t" \
    "s_mov_b32 s70, 1\n\t" \
    "s_sub_u32 s71, s71, 1\n\t" \
    "s_cselect_b32 s69, 0, -1\n\t" \
    "s_waitcnt lgkmcnt(0)\n\t" \
    "s_sub_u32 s54, s56, s54\n\t" \
    "s_and_b32 s54, s54, s69\n\t" \
    "s_cmp_gt_u32 s54, 124\n\t" \
    "s_cbranch_scc1 qk_E1_%=\n\t" \
    "s_cmp_eq_u32 s4, s28\n\t" \
    "s_addc_u32 s70, s70, 0\n\t" \
    "s_cmp_eq_u32 s6, s28\n\t" \
    "s_addc_u32 s70, s70, 0\n\t" \
    "s_cmp_eq_u32 s8, s28\n\t" \
    "s_addc_u32 s70, s70, 0\n\t" \
    "s_cmp_eq_u32 s10, s28\n\t" \
    "s_addc_u32 s70, s70, 0\n\t" \
    "s_cmp_eq_u32 s12, s28\n\t" \
    "s_addc_u32 s70, s70, 0\n\t" \
    "s_cmp_eq_u32 s14, s28\n\t" \
    "s_addc_u32 s70, s70, 0\n\t" \
    "s_cmp_eq_u32 s16, s28\n\t" \
    "s_addc_u32 s70, s70, 0\n\t" \
    "s_cmp_eq_u32 s18, s28\n\t" \
    "s_addc_u32 s70, s70, 0\n\t" \
    "s_cmp_eq_u32 s20, s28\n\t" \
    "s_addc_u32 s70, s70, 0\n\t" \
    "s_cmp_eq_u32 s22, s28\n\t" \
    "s_addc_u32 s70, s70, 0\n\t" \
    "s_cmp_eq_u32 s24, s28\n\t" \
    "s_addc_u32 s70, s70, 0\n\t" \
    "s_cmp_eq_u32 s26, s28\n\t" \
    "s_addc_u32 s70, s70, 0\n\t" \
    "s_mov_b32 s75, s73\n\t" \
    "s_mov_b32 s76, s74\n\t" \
    "s_lshl_b32 s70, s70, 3\n\t" \
    "s_sub_u32 s73, s56, s70\n\t" \
    "s_lshl_b32 s74, s28, 4\n\t" \
    "s_waitcnt vmcnt(12) lgkmcnt(0)\n\t" \
    "s_barrier\n\t" \
    "s_add_u32 m0, s82, 16384\n\t" \
    "s_nop 0\n\t" \
    "global_load_lds_dwordx4 v200, s[42:43] nt\n\t" \
    "v_add_u32 v200, 131072, v200\n\t" \
    "s_add_u32 m0, s82, 20480\n\t" \
    "s_nop 0\n\t" \
    "global_load_lds_dwordx4 v200, s[42:43] nt\n\t" \
    "v_add_u32 v200, 131072, v200\n\t" \
    "s_add_u32 m0, s82, 24576\n\t" \
    "s_nop 0\n\t" \
    "global_load_lds_dwordx4 v200, s[42:43] nt\n\t" \
    "v_add_u32 v200, 131072, v200\n\t" \
    "s_add_u32 m0, s82, 28672\n\t" \
    "s_nop 0\n\t" \
    "global_load_lds_dwordx4 v200, s[42:43] nt\n\t" \
    "v_add_u32 v200, 131072, v200\n\t" \
    "ds_read_b64_tr_b16 a[32:33], v202 offset:32768\n\t" \
    "ds_read_b64_tr_b16 a[34:35], v202 offset:33792\n\t" \
    "ds_read_b64_tr_b16 a[36:37], v202 offset:40960\n\t" \
    "ds_read_b64_tr_b16 a[38:39], v202 offset:41984\n\t" \
    "ds_read_b64_tr_b16 a[104:105], v203 offset:32768\n\t" \
    "ds_read_b64_tr_b16 a[106:107], v203 offset:33792\n\t" \
    "ds_read_b64_tr_b16 a[108:109], v203 offset:40960\n\t" \
    "ds_read_b64_tr_b16 a[110:111], v203 offset:41984\n\t" \
    "s_waitcnt vmcnt(12) lgkmcnt(0)\n\t" \
    "s_barrier\n\t" \
    "v_subrev_u32 v200, 4718592, v200\n\t" \
    "s_add_u32 m0, s82, 32768\n\t" \
    "s_nop 0\n\t" \
    "global_load_lds_dwordx4 v200, s[44:45] nt\n\t" \
    "v_add_u32 v200, 131072, v200\n\t" \
    "s_add_u32 m0, s82, 36864\n\t" \
    "s_nop 0\n\t" \
    "global_load_lds_dwordx4 v200, s[44:45] nt\n\t" \
    "v_add_u32 v200, 131072, v200\n\t" \
    "s_add_u32 m0, s82, 40960\n\t" \
    "s_nop 0\n\t" \
    "global_load_lds_dwordx4 v200, s[44:45] nt\n\t" \
    "v_add_u32 v200, 131072, v200\n\t" \
    "s_add_u32 m0, s82, 45056\n\t" \
    "s_nop 0\n\t" \
    "global_load_lds_dwordx4 v200, s[44:45] nt\n\t" \
    "v_add_u32 v200, 131072, v200\n\t" \
    "ds_read_b64_tr_b16 a[40:41], v202 offset:49152\n\t" \
    "ds_read_b64_tr_b16 a[42:43], v202 offset:50176\n\t" \
    "ds_read_b64_tr_b16 a[44:45], v202 offset:57344\n\t" \
    "ds_read_b64_tr_b16 a[46:47], v202 offset:58368\n\t" \
    "ds_read_b64_tr_b16 a[112:113], v203 offset:49152\n\t" \
    "ds_read_b64_tr_b16 a[114:115], v203 offset:50176\n\t" \
    "ds_read_b64_tr_b16 a[116:117], v203 offset:57344\n\t" \
    "ds_read_b64_tr_b16 a[118:119], v203 offset:58368\n\t" \
    "s_waitcnt vmcnt(12) lgkmcnt(0)\n\t" \
    "s_barrier\n\t" \
    "s_add_u32 m0, s82, 49152\n\t" \
    "s_nop 0\n\t" \
    "global_load_lds_dwordx4 v200, s[44:45] nt\n\t" \
    "v_add_u32 v200, 131072, v200\n\t" \
    "s_add_u32 m0, s82, 53248\n\t" \
    "s_nop 0\n\t" \
    "global_load_lds_dwordx4 v200, s[44:45] nt\n\t" \
    "v_add_u32 v200, 131072, v200\n\t" \
    "s_add_u32 m0, s82, 57344\n\t" \
    "s_nop 0\n\t" \
    "global_load_lds_dwordx4 v200, s[44:45] nt\n\t" \
    "v_add_u32 v200, 131072, v200\n\t" \
    "s_add_u32 m0, s82, 61440\n\t" \
    "s_nop 0\n\t" \
    "global_load_lds_dwordx4 v200, s[44:45] nt\n\t" \
    "v_add_u32 v200, 131072, v200\n\t" \
    "v_add_u32 v202, 65536, v202\n\t" \
    "v_add_u32 v203, 65536, v203\n\t" \
    "ds_read_b64_tr_b16 a[48:49], v202 offset:0\n\t" \
    "ds_read_b64_tr_b16 a[50:51], v202 offset:1024\n\t" \
    "ds_read_b64_tr_b16 a[52:53], v202 offset:8192\n\t" \
    "ds_read_b64_tr_b16 a[54:55], v202 offset:9216\n\t" \
    "ds_read_b64_tr_b16 a[120:121], v203 offset:0\n\t" \
    "ds_read_b64_tr_b16 a[122:123], v203 offset:1024\n\t" \
    "ds_read_b64_tr_b16 a[124:125], v203 offset:8192\n\t" \
    "ds_read_b64_tr_b16 a[126:127], v203 offset:9216\n\t" \
    "s_waitcnt vmcnt(12) lgkmcnt(0)\n\t" \
    "s_barrier\n\t" \
    "s_add_u32 m0, s82, 65536\n\t" \
    "s_nop 0\n\t" \
    "global_load_lds_dwordx4 v200, s[44:45] nt\n\t" \
    "v_add_u32 v200, 131072, v200\n\t" \
    "s_add_u32 m0, s82, 69632\n\t" \
    "s_nop 0\n\t" \
    "global_load_lds_dwordx4 v200, s[44:45] nt\n\t" \
    "v_add_u32 v200, 131072, v200\n\t" \
    "s_add_u32 m0, s82, 73728\n\t" \
    "s_nop 0\n\t" \
    "global_load_lds_dwordx4 v200, s[44:45] nt\n\t" \
    "v_add_u32 v200, 131072, v200\n\t" \
    "s_add_u32 m0, s82, 77824\n\t" \
    "s_nop 0\n\t" \
    "global_load_lds_dwordx4 v200, s[44:45] nt\n\t" \
    "v_add_u32 v200, 131072, v200\n\t" \
    "v_add_u32 v202, -98304, v202\n\t" \
    "v_add_u32 v203, -98304, v203\n\t" \
    "ds_read_b64_tr_b16 a[56:57], v202 offset:0\n\t" \
    "ds_read_b64_tr_b16 a[58:59], v202 offset:1024\n\t" \
    "ds_read_b64_tr_b16 a[60:61], v202 offset:8192\n\t" \
    "ds_read_b64_tr_b16 a[62:63], v202 offset:9216\n\t" \
    "ds_read_b64_tr_b16 a[128:129], v203 offset:0\n\t" \
    "ds_read_b64_tr_b16 a[130:131], v203 offset:1024\n\t" \
    "ds_read_b64_tr_b16 a[132:133], v203 offset:8192\n\t" \
    "ds_read_b64_tr_b16 a[134:135], v203 offset:9216\n\t" \
    "s_waitcnt vmcnt(12) lgkmcnt(0)\n\t" \
    "s_barrier\n\t" \
    "s_add_u32 m0, s82, 81920\n\t" \
    "s_nop 0\n\t" \
    "global_load_lds_dwordx4 v200, s[44:45] nt\n\t" \
    "v_add_u32 v200, 131072, v200\n\t" \
    "s_add_u32 m0, s82, 86016\n\t" \
    "s_nop 0\n\t" \
    "global_load_lds_dwordx4 v200, s[44:45] nt\n\t" \
    "v_add_u32 v200, 131072, v200\n\t" \
    "s_add_u32 m0, s82, 90112\n\t" \
    "s_nop 0\n\t" \
    "global_load_lds_dwordx4 v200, s[44:45] nt\n\t" \
    "v_add_u32 v200, 131072, v200\n\t" \
    "s_add_u32 m0, s82, 94208\n\t" \
    "s_nop 0\n\t" \
    "global_load_lds_dwordx4 v200, s[44:45] nt\n\t" \
    "v_add_u32 v200, 131072, v200\n\t" \
    "ds_read_b64_tr_b16 a[64:65], v202 offset:16384\n\t" \
    "ds_read_b64_tr_b16 a[66:67], v202 offset:17408\n\t" \
    "ds_read_b64_tr_b16 a[68:69], v202 offset:24576\n\t" \
    "ds_read_b64_tr_b16 a[70:71], v202 offset:25600\n\t" \
    "ds_read_b64_tr_b16 a[136:137], v203 offset:16384\n\t" \
    "ds_read_b64_tr_b16 a[138:139], v203 offset:17408\n\t" \
    "ds_read_b64_tr_b16 a[140:141], v203 offset:24576\n\t" \
    "ds_read_b64_tr_b16 a[142:143], v203 offset:25600\n\t" \
    "s_waitcnt vmcnt(12) lgkmcnt(0)\n\t" \
    "s_barrier\n\t" \
    "s_add_u32 m0, s82, 98304\n\t" \
    "s_nop 0\n\t" \
    "global_load_lds_dwordx4 v200, s[44:45] nt\n\t" \
    "v_add_u32 v200, 131072, v200\n\t" \
    "s_add_u32 m0, s82, 102400\n\t" \
    "s_nop 0\n\t" \
    "global_load_lds_dwordx4 v200, s[44:45] nt\n\t" \
    "v_add_u32 v200, 131072, v200\n\t" \
    "s_add_u32 m0, s82, 106496\n\t" \
    "s_nop 0\n\t" \
    "global_load_lds_dwordx4 v200, s[44:45] nt\n\t" \
    "v_add_u32 v200, 131072, v200\n\t" \
    "s_add_u32 m0, s82, 110592\n\t" \
    "s_nop 0\n\t" \
    "global_load_lds_dwordx4 v200, s[44:45] nt\n\t" \
    "v_add_u32 v200, 131072, v200\n\t" \
    "ds_read_b64_tr_b16 a[144:145], v202 offset:32768\n\t" \
    "ds_read_b64_tr_b16 a[146:147], v202 offset:33792\n\t" \
    "ds_read_b64_tr_b16 a[148:149], v202 offset:40960\n\t" \
    "ds_read_b64_tr_b16 a[150:151], v202 offset:41984\n\t" \
    "ds_read_b64_tr_b16 a[216:217], v203 offset:32768\n\t" \
    "ds_read_b64_tr_b16 a[218:219], v203 offset:33792\n\t" \
    "ds_read_b64_tr_b16 a[220:221], v203 offset:40960\n\t" \
    "ds_read_b64_tr_b16 a[222:223], v203 offset:41984\n\t" \
    "s_waitcnt vmcnt(12) lgkmcnt(0)\n\t" \
    "s_barrier\n\t" \
    "s_add_u32 m0, s82, 0\n\t" \
    "s_nop 0\n\t" \
    "global_load_lds_dwordx4 v200, s[44:45] nt\n\t" \
    "v_add_u32 v200, 131072, v200\n\t" \
    "s_add_u32 m0, s82, 4096\n\t" \
    "s_nop 0\n\t" \
    "global_load_lds_dwordx4 v200, s[44:45] nt\n\t" \
    "v_add_u32 v200, 131072, v200\n\t" \
    "s_add_u32 m0, s82, 8192\n\t" \
    "s_nop 0\n\t" \
    "global_load_lds_dwordx4 v200, s[44:45] nt\n\t" \
    "v_add_u32 v200, 131072, v200\n\t" \
    "s_add_u32 m0, s82, 12288\n\t" \
    "s_nop 0\n\t" \
    "global_load_lds_dwordx4 v200, s[44:45] nt\n\t" \
    "v_add_u32 v200, 131072, v200\n\t" \
    "ds_read_b64_tr_b16 a[152:153], v202 offset:49152\n\t" \
    "ds_read_b64_tr_b16 a[154:155], v202 offset:50176\n\t" \
    "ds_read_b64_tr_b16 a[156:157], v202 offset:57344\n\t" \
    "ds_read_b64_tr_b16 a[158:159], v202 offset:58368\n\t" \
    "ds_read_b64_tr_b16 a[224:225], v203 offset:49152\n\t" \
    "ds_read_b64_tr_b16 a[226:227], v203 offset:50176\n\t" \
    "ds_read_b64_tr_b16 a[228:229], v203 offset:57344\n\t" \
    "ds_read_b64_tr_b16 a[230:231], v203 offset:58368\n\t" \
    "s_waitcnt vmcnt(12) lgkmcnt(0)\n\t" \
    "s_barrier\n\t" \
    "s_add_u32 m0, s82, 16384\n\t" \
    "s_nop 0\n\t" \
    "global_load_lds_dwordx4 v200, s[44:45] nt\n\t" \
    "v_add_u32 v200, 131072, v200\n\t" \
    "s_add_u32 m0, s82, 20480\n\t" \
    "s_nop 0\n\t" \
    "global_load_lds_dwordx4 v200, s[44:45] nt\n\t" \
    "v_add_u32 v200, 131072, v200\n\t" \
    "s_add_u32 m0, s82, 24576\n\t" \
    "s_nop 0\n\t" \
    "global_load_lds_dwordx4 v200, s[44:45] nt\n\t" \
    "v_add_u32 v200, 131072, v200\n\t" \
    "s_add_u32 m0, s82, 28672\n\t" \
    "s_nop 0\n\t" \
    "global_load_lds_dwordx4 v200, s[44:45] nt\n\t" \
    "v_add_u32 v200, 131072, v200\n\t" \
    "v_add_u32 v202, 65536, v202\n\t" \
    "v_add_u32 v203, 65536, v203\n\t" \
    "ds_read_b64_tr_b16 a[160:161], v202 offset:0\n\t" \
    "ds_read_b64_tr_b16 a[162:163], v202 offset:1024\n\t" \
    "ds_read_b64_tr_b16 a[164:165], v202 offset:8192\n\t" \
    "ds_read_b64_tr_b16 a[166:167], v202 offset:9216\n\t" \
    "ds_read_b64_tr_b16 a[232:233], v203 offset:0\n\t" \
    "ds_read_b64_tr_b16 a[234:235], v203 offset:1024\n\t" \
    "ds_read_b64_tr_b16 a[236:237], v203 offset:8192\n\t" \
    "ds_read_b64_tr_b16 a[238:239], v203 offset:9216\n\t" \
    "s_waitcnt vmcnt(12) lgkmcnt(0)\n\t" \
    "s_barrier\n\t" \
    "s_add_u32 m0, s82, 32768\n\t" \
    "s_nop 0\n\t" \
    "global_load_lds_dwordx4 v200, s[44:45] nt\n\t" \
    "v_add_u32 v200, 131072, v200\n\t" \
    "s_add_u32 m0, s82, 36864\n\t" \
    "s_nop 0\n\t" \
    "global_load_lds_dwordx4 v200, s[44:45] nt\n\t" \
    "v_add_u32 v200, 131072, v200\n\t" \
    "s_add_u32 m0, s82, 40960\n\t" \
    "s_nop 0\n\t" \
    "global_load_lds_dwordx4 v200, s[44:45] nt\n\t" \
    "v_add_u32 v200, 131072, v200\n\t" \
    "s_add_u32 m0, s82, 45056\n\t" \
    "s_nop 0\n\t" \
    "global_load_lds_dwordx4 v200, s[44:45] nt\n\t" \
    "v_add_u32 v200, 131072, v200\n\t" \
    "ds_read_b64_tr_b16 a[168:169], v202 offset:16384\n\t" \
    "ds_read_b64_tr_b16 a[170:171], v202 offset:17408\n\t" \
    "ds_read_b64_tr_b16 a[172:173], v202 offset:24576\n\t" \
    "ds_read_b64_tr_b16 a[174:175], v202 offset:25600\n\t" \
    "ds_read_b64_tr_b16 a[240:241], v203 offset:16384\n\t" \
    "ds_read_b64_tr_b16 a[242:243], v203 offset:17408\n\t" \
    "ds_read_b64_tr_b16 a[244:245], v203 offset:24576\n\t" \
    "ds_read_b64_tr_b16 a[246:247], v203 offset:25600\n\t" \
    "s_waitcnt vmcnt(12) lgkmcnt(0)\n\t" \
    "s_barrier\n\t" \
    "s_add_u32 m0, s82, 49152\n\t" \
    "s_nop 0\n\t" \
    "global_load_lds_dwordx4 v200, s[44:45] nt\n\t" \
    "v_add_u32 v200, 131072, v200\n\t" \
    "s_add_u32 m0, s82, 53248\n\t" \
    "s_nop 0\n\t" \
    "global_load_lds_dwordx4 v200, s[44:45] nt\n\t" \
    "v_add_u32 v200, 131072, v200\n\t" \
    "s_add_u32 m0, s82, 57344\n\t" \
    "s_nop 0\n\t" \
    "global_load_lds_dwordx4 v200, s[44:45] nt\n\t" \
    "v_add_u32 v200, 131072, v200\n\t" \
    "s_add_u32 m0, s82, 61440\n\t" \
    "s_nop 0\n\t" \
    "global_load_lds_dwordx4 v200, s[44:45] nt\n\t" \
    "v_add_u32 v200, 131072, v200\n\t" \
    "ds_read_b64_tr_b16 a[176:177], v202 offset:32768\n\t" \
    "ds_read_b64_tr_b16 a[178:179], v202 offset:33792\n\t" \
    "ds_read_b64_tr_b16 a[180:181], v202 offset:40960\n\t" \
    "ds_read_b64_tr_b16 a[182:183], v202 offset:41984\n\t" \
    "ds_read_b64_tr_b16 a[248:249], v203 offset:32768\n\t" \
    "ds_read_b64_tr_b16 a[250:251], v203 offset:33792\n\t" \
    "ds_read_b64_tr_b16 a[252:253], v203 offset:40960\n\t" \
    "ds_read_b64_tr_b16 a[254:255], v203 offset:41984\n\t" \
    "s_waitcnt vmcnt(12) lgkmcnt(0)\n\t" \
    "s_barrier\n\t" \
    "v_mbcnt_lo_u32_b32 v160, -1, 0\n\t" \
    "v_mbcnt_hi_u32_b32 v160, -1, v160\n\t" \
    "v_min_u32 v160, 59, v160\n\t" \
    "v_lshlrev_b32 v160, 6, v160\n\t" \
    "v_add_u32 v160, 6258688, v160\n\t" \
    "v_mov_b32 v161, 6266880\n\t" \
    "global_load_dword v161, v161, s[36:37] sc1\n\t" \
    "global_load_dwordx2 v[162:163], v160, s[36:37] sc1\n\t" \
    "v_add_u32 v202, -65536, v202\n\t" \
    "v_add_u32 v203, -65536, v203\n\t" \
    "ds_read_b64_tr_b16 a[184:185], v202 offset:0\n\t" \
    "ds_read_b64_tr_b16 a[186:187], v202 offset:1024\n\t" \
    "ds_read_b64_tr_b16 a[188:189], v202 offset:8192\n\t" \
    "ds_read_b64_tr_b16 a[190:191], v202 offset:9216\n\t" \
    "ds_read_b64_tr_b16 v[224:225], v203 offset:0\n\t" \
    "ds_read_b64_tr_b16 v[226:227], v203 offset:1024\n\t" \
    "ds_read_b64_tr_b16 v[228:229], v203 offset:8192\n\t" \
    "ds_read_b64_tr_b16 v[230:231], v203 offset:9216\n\t" \
    "s_waitcnt vmcnt(10) lgkmcnt(0)\n\t" \
    "s_barrier\n\t" \
    "ds_read_b64_tr_b16 a[192:193], v202 offset:16384\n\t" \
    "ds_read_b64_tr_b16 a[194:195], v202 offset:17408\n\t" \
    "ds_read_b64_tr_b16 a[196:197], v202 offset:24576\n\t" \
    "ds_read_b64_tr_b16 a[198:199], v202 offset:25600\n\t" \
    "ds_read_b64_tr_b16 v[232:233], v203 offset:16384\n\t" \
    "ds_read_b64_tr_b16 v[234:235], v203 offset:17408\n\t" \
    "ds_read_b64_tr_b16 v[236:237], v203 offset:24576\n\t" \
    "ds_read_b64_tr_b16 v[238:239], v203 offset:25600\n\t" \
    "s_waitcnt vmcnt(6) lgkmcnt(0)\n\t" \
    "s_barrier\n\t" \
    "ds_read_b64_tr_b16 a[200:201], v202 offset:32768\n\t" \
    "ds_read_b64_tr_b16 a[202:203], v202 offset:33792\n\t" \
    "ds_read_b64_tr_b16 a[204:205], v202 offset:40960\n\t" \
    "ds_read_b64_tr_b16 a[206:207], v202 offset:41984\n\t" \
    "ds_read_b64_tr_b16 v[240:241], v203 offset:32768\n\t" \
    "ds_read_b64_tr_b16 v[242:243], v203 offset:33792\n\t" \
    "ds_read_b64_tr_b16 v[244:245], v203 offset:40960\n\t" \
    "ds_read_b64_tr_b16 v[246:247], v203 offset:41984\n\t" \
    "s_waitcnt vmcnt(2) lgkmcnt(0)\n\t" \
    "s_barrier\n\t" \
    "ds_read_b64_tr_b16 a[208:209], v202 offset:49152\n\t" \
    "ds_read_b64_tr_b16 a[210:211], v202 offset:50176\n\t" \
    "ds_read_b64_tr_b16 a[212:213], v202 offset:57344\n\t" \
    "ds_read_b64_tr_b16 a[214:215], v202 offset:58368\n\t" \
    "ds_read_b64_tr_b16 v[248:249], v203 offset:49152\n\t" \
    "ds_read_b64_tr_b16 v[250:251], v203 offset:50176\n\t" \
    "ds_read_b64_tr_b16 v[252:253], v203 offset:57344\n\t" \
    "ds_read_b64_tr_b16 v[254:255], v203 offset:58368\n\t" \
    "s_waitcnt vmcnt(0) lgkmcnt(0)\n\t" \
    "s_barrier\n\t" \
    "s_mul_i32 s70, s67, 7\n\t" \
    "s_add_u32 s69, s65, 0\n\t" \
    "s_xor_b32 s69, s69, s70\n\t" \
    "s_and_b32 s69, s69, 63\n\t" \
    "s_lshl_b32 s69, s69, 13\n\t" \
    "s_mul_i32 s70, s67, 524288\n\t" \
    "s_add_u32 s69, s69, s70\n\t" \
    "s_add_u32 s69, s69, 23068672\n\t" \
    "s_add_u32 s50, s36, s69\n\t" \
    "s_addc_u32 s51, s37, 0\n\t" \
    "v_sub_u32 v162, v162, v161\n\t" \
    "v_cmp_ne_u32 vcc, 1, v162\n\t" \
    "s_cbranch_vccz qk_Cok4_%=\n\t" \
    "qk_C3_%=:\n\t" \
    "global_load_dwordx2 v[162:163], v160, s[36:37] sc1\n\t" \
    "s_waitcnt vmcnt(0)\n\t" \
    "v_sub_u32 v162, v162, v161\n\t" \
    "v_cmp_ne_u32 vcc, 1, v162\n\t" \
    "s_cbranch_vccz qk_Cok4_%=\n\t" \
    "s_cmp_eq_u32 s68, 0\n\t" \
    "s_cbranch_scc1 qk_F5_%=\n\t" \
    "s_sub_u32 s68, s68, 1\n\t" \
    "s_sleep 4\n\t" \
    "s_branch qk_C3_%=\n\t" \
    "qk_F5_%=:\n\t" \
    "v_mov_b32 v164, 6095104\n\t" \
    "v_mov_b32 v165, 4\n\t" \
    "global_store_dword v164, v165, s[36:37] sc1\n\t" \
    "s_waitcnt vmcnt(0)\n\t" \
    "s_branch qk_Cok4_%=\n\t" \
    "qk_Cok4_%=:\n\t" \
    "s_cmp_lg_u32 s66, 0\n\t" \
    "s_cbranch_scc1 qk_Ca6_%=\n\t" \
    "v_mov_b32 v160, 6176768\n\t" \
    "s_mov_b64 exec, 1\n\t" \
    "global_atomic_add v160, v162, s[36:37] sc1\n\t" \
    "s_mov_b64 exec, -1\n\t" \
    "qk_Ca6_%=:\n\t" \
    "v_min_u32_dpp v163, v163, v163 row_shl:6 row_mask:0xf bank_mask:0xf\n\t" \
    "s_nop 0\n\t" \
    "v_readlane_b32 s69, v163, 0\n\t" \
    "v_readlane_b32 s70, v163, 1\n\t" \
    "s_nop 1\n\t" \
    "s_cmp_lt_u32 s70, s69\n\t" \
    "s_cselect_b32 s62, 8192, 0\n\t" \
    "s_cselect_b32 s69, s70, s69\n\t" \
    "s_mov_b32 s71, s69\n\t" \
    "v_readlane_b32 s69, v163, 2\n\t" \
    "v_readlane_b32 s70, v163, 3\n\t" \
    "s_nop 1\n\t" \
    "s_cmp_lt_u32 s70, s69\n\t" \
    "s_cselect_b32 s63, 8192, 0\n\t" \
    "s_cselect_b32 s69, s70, s69\n\t" \
    "s_max_u32 s71, s69, s71\n\t" \
    "v_readlane_b32 s69, v163, 4\n\t" \
    "v_readlane_b32 s70, v163, 5\n\t" \
    "s_nop 1\n\t" \
    "s_cmp_lt_u32 s70, s69\n\t" \
    "s_cselect_b32 s64, 8192, 0\n\t" \
    "s_cselect_b32 s69, s70, s69\n\t" \
    "s_max_u32 s71, s69, s71\n\t" \
    "s_add_u32 s63, s63, 0x200000\n\t" \
    "s_xor_b32 s52, s64, 8192\n\t" \
    "s_cmp_gt_u32 s71, 625\n\t" \
    "s_cselect_b32 s69, 1, 0\n\t" \
    "s_mul_i32 s70, s69, 160\n\t" \
    "s_add_u32 s86, s86, s70\n\t" \
    "s_mul_i32 s70, s69, 536\n\t" \
    "s_add_u32 s87, s87, s70\n\t" \
    "s_mul_i32 s70, s69, 256\n\t" \
    "s_add_u32 s88, s88, s70\n\t" \
    "v_add_u32 v14, s52, v14\n\t" \
    "v_add_u32 v24, s52, v24\n\t" \
    "v_mov_b32 v26, 0x3db504f3\n\t" \
    "v_mov_b32 v28, 0x3fb8aa3b\n\t" \
    "v_mov_b32 v27, 0x3db504f3\n\t" \
    "v_mov_b32 v29, 0x3fb8aa3b\n\t" \
    "v_mov_b32 v29, 0x3e0293ee\n\t" \
    "s_mov_b32 s48, 0xaaaaaaaa\n\t" \
    "s_mov_b32 s49, 0xaaaaaaaa\n\t" \
    "s_mov_b32 s58, 0x00ff00ff\n\t" \
    "s_mov_b32 s59, 0x00ff00ff\n\t" \
    "s_lshl_b32 s69, s66, 2\n\t" \
    "v_mov_b32 v30, s69\n\t" \
    "v_mov_b32 v31, 0\n\t" \
    "v_and_b32 v33, 12, v22\n\t" \
    "ds_write_b32 v30, v31 offset:20496\n\t" \
    "s_waitcnt lgkmcnt(0)\n\t" \
    "s_mov_b64 s[40:41], s[36:37]\n\t" \
    "s_mov_b64 s[44:45], 0\n\t" \
    "s_barrier\n\t" \
    "s_mov_b32 s71, 3\n\t" \
    "qk_E7_%=:\n\t" \
    "s_memtime s[54:55]\n\t" \
    "s_nop 0\n\t" \
    "s_memrealtime s[4:5]\n\t" \
    "s_nop 0\n\t" \
    "s_memrealtime s[6:7]\n\t" \
    "s_nop 0\n\t" \
    "s_memrealtime s[8:9]\n\t" \
    "s_nop 0\n\t" \
    "s_memrealtime s[10:11]\n\t" \
    "s_nop 0\n\t" \
    "s_memrealtime s[12:13]\n\t" \
    "s_nop 0\n\t" \
    "s_memrealtime s[14:15]\n\t" \
    "s_nop 0\n\t" \
    "s_memrealtime s[16:17]\n\t" \
    "s_nop 0\n\t" \
    "s_memrealtime s[18:19]\n\t" \
    "s_nop 0\n\t" \
    "s_memrealtime s[20:21]\n\t" \
    "s_nop 0\n\t" \
    "s_memrealtime s[22:23]\n\t" \
    "s_nop 0\n\t" \
    "s_memrealtime s[24:25]\n\t" \
    "s_nop 0\n\t" \
    "s_memrealtime s[26:27]\n\t" \
    "s_nop 0\n\t" \
    "s_memrealtime s[28:29]\n\t" \
    "s_nop 0\n\t" \
    "s_memtime s[56:57]\n\t" \
    "s_mov_b32 s70, 1\n\t" \
    "s_sub_u32 s71, s71, 1\n\t" \
    "s_cselect_b32 s69, 0, -1\n\t" \
    "s_waitcnt lgkmcnt(0)\n\t" \
    "s_sub_u32 s54, s56, s54\n\t" \
    "s_and_b32 s54, s54, s69\n\t" \
    "s_cmp_gt_u32 s54, 124\n\t" \
    "s_cbranch_scc1 qk_E7_%=\n\t" \
    "s_cmp_eq_u32 s4, s28\n\t" \
    "s_addc_u32 s70, s70, 0\n\t" \
    "s_cmp_eq_u32 s6, s28\n\t" \
    "s_addc_u32 s70, s70, 0\n\t" \
    "s_cmp_eq_u32 s8, s28\n\t" \
    "s_addc_u32 s70, s70, 0\n\t" \
    "s_cmp_eq_u32 s10, s28\n\t" \
    "s_addc_u32 s70, s70, 0\n\t" \
    "s_cmp_eq_u32 s12, s28\n\t" \
    "s_addc_u32 s70, s70, 0\n\t" \
    "s_cmp_eq_u32 s14, s28\n\t" \
    "s_addc_u32 s70, s70, 0\n\t" \
    "s_cmp_eq_u32 s16, s28\n\t" \
    "s_addc_u32 s70, s70, 0\n\t" \
    "s_cmp_eq_u32 s18, s28\n\t" \
    "s_addc_u32 s70, s70, 0\n\t" \
    "s_cmp_eq_u32 s20, s28\n\t" \
    "s_addc_u32 s70, s70, 0\n\t" \
    "s_cmp_eq_u32 s22, s28\n\t" \
    "s_addc_u32 s70, s70, 0\n\t" \
    "s_cmp_eq_u32 s24, s28\n\t" \
    "s_addc_u32 s70, s70, 0\n\t" \
    "s_cmp_eq_u32 s26, s28\n\t" \
    "s_addc_u32 s70, s70, 0\n\t" \
    "s_mov_b32 s75, s73\n\t" \
    "s_mov_b32 s76, s74\n\t" \
    "s_lshl_b32 s70, s70, 3\n\t" \
    "s_sub_u32 s73, s56, s70\n\t" \
    "s_lshl_b32 s74, s28, 4\n\t" \
    "s_sub_u32 s69, s73, s75\n\t" \
    "s_sub_u32 s70, s74, s76\n\t" \
    "s_sub_u32 s71, s70, 1600\n\t" \
    "s_cmp_lt_u32 s71, 400000\n\t" \
    "s_cbranch_scc0 qk_K9_%=\n\t" \
    "v_cvt_f32_u32 v160, s69\n\t" \
    "v_cvt_f32_u32 v161, s70\n\t" \
    "v_rcp_f32 v162, v160\n\t" \
    "v_rcp_f32 v163, v161\n\t" \
    "v_mul_f32 v162, v162, v161\n\t" \
    "v_mul_f32 v163, v163, v160\n\t" \
    "v_mul_f32 v162, 0x4f800000, v162\n\t" \
    "v_mul_f32 v163, 0x4f000000, v163\n\t" \
    "v_cvt_u32_f32 v162, v162\n\t" \
    "v_cvt_u32_f32 v163, v163\n\t" \
    "v_readfirstlane_b32 s69, v162\n\t" \
    "v_readfirstlane_b32 s70, v163\n\t" \
    "s_nop 1\n\t" \
    "s_mov_b32 s78, s69\n\t" \
    "s_mov_b32 s77, s70\n\t" \
    "qk_K9_%=:\n\t" \
    "s_mov_b32 s79, s74\n\t" \
    "s_add_u32 s80, s74, 2400\n\t" \
    "s_cmp_eq_u32 s66, 1\n\t" \
    "s_cbranch_scc1 qk_loop11_%=\n\t" \
    "s_cmp_eq_u32 s66, 2\n\t" \
    "s_cbranch_scc1 qk_loop12_%=\n\t" \
    "s_cmp_eq_u32 s66, 3\n\t" \
    "s_cbranch_scc1 qk_loop13_%=\n\t" \
    "qk_loop10_%=:\n\t" \
    "v_mov_b32 v72, 0\n\t" \
    "v_mov_b32 v73, 0\n\t" \
    "v_mov_b32 v74, 0\n\t" \
    "v_mov_b32 v75, 0\n\t" \
    "v_mov_b32 v76, 0\n\t" \
    "v_mov_b32 v77, 0\n\t" \
    "v_mov_b32 v78, 0\n\t" \
    "v_mov_b32 v79, 0\n\t" \
    "v_mov_b32 v80, 0\n\t" \
    "v_mov_b32 v81, 0\n\t" \
    "v_mov_b32 v82, 0\n\t" \
    "v_mov_b32 v83, 0\n\t" \
    "v_mov_b32 v84, 0\n\t" \
    "v_mov_b32 v85, 0\n\t" \
    "v_mov_b32 v86, 0\n\t" \
    "v_mov_b32 v87, 0\n\t" \
    "v_mov_b32 v88, 0\n\t" \
    "v_mov_b32 v89, 0\n\t" \
    "v_mov_b32 v90, 0\n\t" \
    "v_mov_b32 v91, 0\n\t" \
    "v_mov_b32 v92, 0\n\t" \
    "v_mov_b32 v93, 0\n\t" \
    "v_mov_b32 v94, 0\n\t" \
    "v_mov_b32 v95, 0\n\t" \
    "v_mov_b32 v96, 0\n\t" \
    "v_mov_b32 v97, 0\n\t" \
    "v_mov_b32 v98, 0\n\t" \
    "v_mov_b32 v99, 0\n\t" \
    "v_add_u32 v31, 1, v31\n\t" \
    "s_add_u32 s69, s65, 0\n\t" \
    "s_bitcmp1_b32 s69, 0\n\t" \
    "s_cselect_b32 s70, s63, s62\n\t" \
    "s_bfe_u32 s69, s69, 0x20001\n\t" \
    "s_lshl_b32 s69, s69, 19\n\t" \
    "s_add_u32 s69, s69, s70\n\t" \
    "s_add_u32 s46, s36, s69\n\t" \
    "s_addc_u32 s47, s37, 0\n\t" \
    "s_add_u32 s69, s65, 3\n\t" \
    "s_cmp_ge_i32 s83, s69\n\t" \
    "s_cselect_b64 vcc, -1, 0\n\t" \
    "v_mov_b32 v9, 6233984\n\t" \
    "v_cndmask_b32 v9, v24, v9, vcc\n\t" \
    "s_mul_i32 s70, s67, 7\n\t" \
    "s_add_u32 s69, s65, 1\n\t" \
    "s_xor_b32 s69, s69, s70\n\t" \
    "s_and_b32 s69, s69, 63\n\t" \
    "s_lshl_b32 s69, s69, 13\n\t" \
    "s_mul_i32 s70, s67, 524288\n\t" \
    "s_add_u32 s69, s69, s70\n\t" \
    "s_add_u32 s69, s69, 23068672\n\t" \
    "s_add_u32 s38, s36, s69\n\t" \
    "s_addc_u32 s39, s37, 0\n\t" \
    "qk_go19_%=:\n\t" \
    "s_sub_u32 s70, s79, s74\n\t" \
    "s_max_i32 s70, s70, 0\n\t" \
    "s_min_i32 s70, s70, 16000\n\t" \
    "s_lshl_b32 s70, s70, 1\n\t" \
    "s_mul_hi_u32 s70, s70, s77\n\t" \
    "s_add_u32 s70, s70, s73\n\t" \
    "s_sub_u32 s70, s70, 100\n\t" \
    "s_getpc_b64 s[30:31]\n\t" \
    "qk_Wp21_%=:\n\t" \
    "s_add_u32 s30, s30, qk_We22_%=-qk_Wp21_%=\n\t" \
    "s_addc_u32 s31, s31, 0\n\t" \
    "qk_W20_%=:\n\t" \
    "s_memtime s[56:57]\n\t" \
    "s_waitcnt lgkmcnt(0)\n\t" \
    "s_sub_u32 s69, s70, s56\n\t" \
    "s_cmp_gt_i32 s69, 111\n\t" \
    "s_cbranch_scc1 qk_W20_%=\n\t" \
    "s_max_i32 s69, s69, 0\n\t" \
    "s_lshr_b32 s69, s69, 1\n\t" \
    "s_and_b32 s69, s69, 0x3c\n\t" \
    "s_sub_u32 s30, s30, s69\n\t" \
    "s_subb_u32 s31, s31, 0\n\t" \
    "s_setpc_b64 s[30:31]\n\t" \
    ".p2align 6\n\t" \
    "s_nop 0\n\t" \
    "s_nop 0\n\t" \
    "s_nop 0\n\t" \
    "s_nop 1\n\t" \
    "s_nop 1\n\t" \
    "s_nop 1\n\t" \
    "s_nop 1\n\t" \
    "s_nop 1\n\t" \
    "s_nop 1\n\t" \
    "s_nop 1\n\t" \
    "s_nop 1\n\t" \
    "s_nop 1\n\t" \
    "s_nop 1\n\t" \
    "s_nop 1\n\t" \
    "s_nop 1\n\t" \
    "s_nop 1\n\t" \
    "qk_We22_%=:\n\t" \
    "qk_G23_%=:\n\t" \
    "global_load_dwordx4 v[36:39], v1, s[46:47] sc1\n\t" \
    "s_nop 9\n\t" \
    "global_load_dwordx4 v[40:43], v1, s[46:47] offset:1024 sc1\n\t" \
    "s_nop 9\n\t" \
    "global_load_dwordx4 v[44:47], v2, s[46:47] sc1\n\t" \
    "s_waitcnt vmcnt(2)\n\t" \
    "v_smfmac_f32_16x16x64_bf16 v[72:75], v[36:39], a[0:7], v10\n\t" \
    "ds_write_b128 v0, v[36:39] offset:0\n\t" \
    "v_smfmac_f32_16x16x64_bf16 v[76:79], v[36:39], a[72:79], v10\n\t" \
    "v_max3_u32 v160, v36, v37, v38\n\t" \
    "v_smfmac_f32_16x16x64_bf16 v[80:83], v[36:39], a[144:151], v10\n\t" \
    "v_smfmac_f32_16x16x64_bf16 v[84:87], v[36:39], a[216:223], v10\n\t" \
    "s_waitcnt vmcnt(1)\n\t" \
    "ds_write_b128 v0, v[40:43] offset:1024\n\t" \
    "v_smfmac_f32_16x16x64_bf16 v[72:75], v[40:43], a[8:15], v10\n\t" \
    "v_max3_u32 v160, v160, v39, v40\n\t" \
    "v_max3_u32 v160, v160, v41, v42\n\t" \
    "v_smfmac_f32_16x16x64_bf16 v[76:79], v[40:43], a[80:87], v10\n\t" \
    "v_max_u32 v160, v160, v43\n\t" \
    "v_cmp_eq_u32 vcc, -1, v160\n\t" \
    "v_smfmac_f32_16x16x64_bf16 v[80:83], v[40:43], a[152:159], v10\n\t" \
    "v_smfmac_f32_16x16x64_bf16 v[84:87], v[40:43], a[224:231], v10\n\t" \
    "s_cbranch_vccnz qk_Gbad24_%=\n\t" \
    "qk_Gc26_%=:\n\t" \
    "s_waitcnt lgkmcnt(0)\n\t" \
    "s_barrier\n\t" \
    "ds_read_b128 v[48:51], v0 offset:2048\n\t" \
    "ds_read_b128 v[52:55], v0 offset:3072\n\t" \
    "s_waitcnt vmcnt(0)\n\t" \
    "v_smfmac_f32_16x16x64_bf16 v[72:75], v[44:47], a[64:71], v10\n\t" \
    "ds_read_b128 v[56:59], v0 offset:4096\n\t" \
    "v_max3_u32 v160, v44, v45, v46\n\t" \
    "v_smfmac_f32_16x16x64_bf16 v[76:79], v[44:47], a[136:143], v10\n\t" \
    "ds_read_b128 v[60:63], v0 offset:5120\n\t" \
    "v_max_u32 v160, v160, v47\n\t" \
    "v_smfmac_f32_16x16x64_bf16 v[80:83], v[44:47], a[208:215], v10\n\t" \
    "ds_read_b128 v[64:67], v0 offset:6144\n\t" \
    "v_cmp_eq_u32 vcc, -1, v160\n\t" \
    "v_smfmac_f32_16x16x64_bf16 v[84:87], v[44:47], v[248:255], v10\n\t" \
    "ds_read_b128 v[68:71], v0 offset:7168\n\t" \
    "s_cbranch_vccnz qk_Gbad225_%=\n\t" \
    "qk_Gc227_%=:\n\t" \
    "s_waitcnt lgkmcnt(5)\n\t" \
    "v_smfmac_f32_16x16x64_bf16 v[72:75], v[48:51], a[16:23], v10\n\t" \
    "s_waitcnt lgkmcnt(4)\n\t" \
    "v_smfmac_f32_16x16x64_bf16 v[72:75], v[52:55], a[24:31], v10\n\t" \
    "s_waitcnt lgkmcnt(3)\n\t" \
    "v_smfmac_f32_16x16x64_bf16 v[72:75], v[56:59], a[32:39], v10\n\t" \
    "global_load_dword v25, v9, s[36:37] sc1\n\t" \
    "s_waitcnt lgkmcnt(2)\n\t" \
    "v_smfmac_f32_16x16x64_bf16 v[72:75], v[60:63], a[40:47], v10\n\t" \
    "s_waitcnt lgkmcnt(1)\n\t" \
    "v_smfmac_f32_16x16x64_bf16 v[72:75], v[64:67], a[48:55], v10\n\t" \
    "s_waitcnt lgkmcnt(0)\n\t" \
    "v_smfmac_f32_16x16x64_bf16 v[72:75], v[68:71], a[56:63], v10\n\t" \
    "v_smfmac_f32_16x16x64_bf16 v[76:79], v[48:51], a[88:95], v10\n\t" \
    "s_nop 1\n\t" \
    "s_nop 0\n\t" \
    "v_smfmac_f32_16x16x64_bf16 v[76:79], v[52:55], a[96:103], v10\n\t" \
    "s_nop 1\n\t" \
    "s_nop 0\n\t" \
    "v_smfmac_f32_16x16x64_bf16 v[76:79], v[56:59], a[104:111], v10\n\t" \
    "v_add_f32 v72, v72, v74\n\t" \
    "v_smfmac_f32_16x16x64_bf16 v[76:79], v[60:63], a[112:119], v10\n\t" \
    "v_add_f32 v73, v73, v75\n\t" \
    "v_smfmac_f32_16x16x64_bf16 v[76:79], v[64:67], a[120:127], v10\n\t" \
    "v_cvt_pk_bf16_f32 v156, v72, v73\n\t" \
    "v_smfmac_f32_16x16x64_bf16 v[76:79], v[68:71], a[128:135], v10\n\t" \
    "ds_write_b32 v12, v156 offset:0\n\t" \
    "v_smfmac_f32_16x16x64_bf16 v[80:83], v[48:51], a[160:167], v10\n\t" \
    "s_nop 1\n\t" \
    "s_nop 0\n\t" \
    "v_smfmac_f32_16x16x64_bf16 v[80:83], v[52:55], a[168:175], v10\n\t" \
    "s_nop 1\n\t" \
    "s_nop 0\n\t" \
    "v_smfmac_f32_16x16x64_bf16 v[80:83], v[56:59], a[176:183], v10\n\t" \
    "v_add_f32 v76, v76, v78\n\t" \
    "v_smfmac_f32_16x16x64_bf16 v[80:83], v[60:63], a[184:191], v10\n\t" \
    "v_add_f32 v77, v77, v79\n\t" \
    "v_smfmac_f32_16x16x64_bf16 v[80:83], v[64:67], a[192:199], v10\n\t" \
    "v_cvt_pk_bf16_f32 v157, v76, v77\n\t" \
    "v_smfmac_f32_16x16x64_bf16 v[80:83], v[68:71], a[200:207], v10\n\t" \
    "ds_write_b32 v12, v157 offset:64\n\t" \
    "v_smfmac_f32_16x16x64_bf16 v[84:87], v[48:51], a[232:239], v10\n\t" \
    "s_nop 1\n\t" \
    "s_nop 0\n\t" \
    "v_smfmac_f32_16x16x64_bf16 v[84:87], v[52:55], a[240:247], v10\n\t" \
    "s_nop 1\n\t" \
    "s_nop 0\n\t" \
    "v_smfmac_f32_16x16x64_bf16 v[84:87], v[56:59], a[248:255], v10\n\t" \
    "v_add_f32 v80, v80, v82\n\t" \
    "v_smfmac_f32_16x16x64_bf16 v[84:87], v[60:63], v[224:231], v10\n\t" \
    "v_add_f32 v81, v81, v83\n\t" \
    "v_smfmac_f32_16x16x64_bf16 v[84:87], v[64:67], v[232:239], v10\n\t" \
    "v_cvt_pk_bf16_f32 v158, v80, v81\n\t" \
    "v_smfmac_f32_16x16x64_bf16 v[84:87], v[68:71], v[240:247], v10\n\t" \
    "ds_write_b32 v16, v158 offset:0\n\t" \
    "s_nop 6\n\t" \
    "v_pk_add_f32 v[84:85], v[84:85], v[86:87]\n\t" \
    "v_cvt_pk_bf16_f32 v159, v84, v85\n\t" \
    "ds_write_b32 v16, v159 offset:256\n\t" \
    "s_cmp_eq_u32 s65, 0\n\t" \
    "s_cbranch_scc1 qk_k017_%=\n\t" \
    "qk_k0b18_%=:\n\t" \
    "s_waitcnt lgkmcnt(0)\n\t" \
    "s_barrier\n\t" \
    "ds_read_b64_tr_b16 v[132:133], v13 offset:0\n\t" \
    "ds_read_b64_tr_b16 v[134:135], v13 offset:128\n\t" \
    "ds_read_b64_tr_b16 v[136:137], v13 offset:1024\n\t" \
    "ds_read_b64_tr_b16 v[138:139], v13 offset:1152\n\t" \
    "ds_read_b64_tr_b16 v[140:141], v17 offset:0\n\t" \
    "ds_read_b64_tr_b16 v[142:143], v17 offset:256\n\t" \
    "ds_read_b64_tr_b16 v[144:145], v17 offset:512\n\t" \
    "ds_read_b64_tr_b16 v[146:147], v17 offset:768\n\t" \
    "ds_read_b64_tr_b16 v[148:149], v17 offset:1024\n\t" \
    "ds_read_b64_tr_b16 v[150:151], v17 offset:1280\n\t" \
    "ds_read_b64_tr_b16 v[152:153], v17 offset:1536\n\t" \
    "ds_read_b64_tr_b16 v[154:155], v17 offset:1792\n\t" \
    "s_waitcnt lgkmcnt(10)\n\t" \
    "v_smfmac_f32_16x16x64_bf16 v[88:91], v[132:135], v[100:107], v10\n\t" \
    "v_smfmac_f32_16x16x64_bf16 v[92:95], v[132:135], v[116:123], v10\n\t" \
    "s_waitcnt lgkmcnt(8)\n\t" \
    "v_smfmac_f32_16x16x64_bf16 v[88:91], v[136:139], v[108:115], v10\n\t" \
    "v_smfmac_f32_16x16x64_bf16 v[92:95], v[136:139], v[124:131], v10\n\t" \
    "s_waitcnt lgkmcnt(4)\n\t" \
    "v_smfmac_f32_16x16x64_bf16 v[96:99], v[132:135], v[140:147], v10\n\t" \
    "s_waitcnt lgkmcnt(0)\n\t" \
    "s_nop 1\n\t" \
    "v_smfmac_f32_16x16x64_bf16 v[96:99], v[136:139], v[148:155], v10\n\t" \
    "s_nop 0\n\t" \
    "v_add_f32 v168, v88, v90\n\t" \
    "v_add_f32 v169, v92, v94\n\t" \
    "v_max_f32 v178, v168, v169\n\t" \
    "v_mul_f32 v168, v168, v29\n\t" \
    "v_mul_f32 v169, v169, v29\n\t" \
    "v_max_f32_dpp v178, v178, v178 row_mirror row_mask:0xf bank_mask:0xf\n\t" \
    "s_nop 0\n\t" \
    "v_add_f32 v172, v96, v98\n\t" \
    "v_max_f32 v173, v178, v172\n\t" \
    "v_mul_f32 v172, v172, v29\n\t" \
    "s_nop 0\n\t" \
    "v_max_f32_dpp v173, v173, v173 quad_perm:[1,0,3,2] row_mask:0xf bank_mask:0xf\n\t" \
    "s_nop 1\n\t" \
    "v_max_f32_dpp v173, v173, v173 quad_perm:[2,3,0,1] row_mask:0xf bank_mask:0xf\n\t" \
    "s_nop 1\n\t" \
    "v_max_f32_dpp v173, v173, v173 row_half_mirror row_mask:0xf bank_mask:0xf\n\t" \
    "v_fma_f32 v172, -v173, v29, v172\n\t" \
    "v_fma_f32 v168, -v173, v29, v168\n\t" \
    "v_fma_f32 v169, -v173, v29, v169\n\t" \
    "v_exp_f32 v172, v172\n\t" \
    "v_exp_f32 v168, v168\n\t" \
    "v_exp_f32 v169, v169\n\t" \
    "v_fma_f32 v173, v172, 0.5, v168\n\t" \
    "v_add_f32 v173, v173, v169\n\t" \
    "v_mov_b32_dpp v174, v172 quad_perm:[0,0,2,2] row_mask:0xf bank_mask:0xf\n\t" \
    "v_mov_b32_dpp v175, v172 quad_perm:[1,1,3,3] row_mask:0xf bank_mask:0xf\n\t" \
    "v_add_f32_dpp v173, v173, v173 quad_perm:[1,0,3,2] row_mask:0xf bank_mask:0xf\n\t" \
    "v_mov_b32_dpp v176, v168 quad_perm:[1,0,3,2] row_mask:0xf bank_mask:0xf\n\t" \
    "v_mov_b32_dpp v177, v169 quad_perm:[1,0,3,2] row_mask:0xf bank_mask:0xf\n\t" \
    "v_add_f32_dpp v173, v173, v173 quad_perm:[2,3,0,1] row_mask:0xf bank_mask:0xf\n\t" \
    "v_cndmask_b32 v168, v168, v177, s[48:49]\n\t" \
    "v_cndmask_b32 v169, v176, v169, s[48:49]\n\t" \
    "v_add_f32_dpp v173, v173, v173 row_half_mirror row_mask:0xf bank_mask:0xf\n\t" \
    "s_nop 1\n\t" \
    "v_add_f32_dpp v173, v173, v173 row_mirror row_mask:0xf bank_mask:0xf\n\t" \
    "v_rcp_f32 v173, v173\n\t" \
    "s_nop 0\n\t" \
    "v_pk_mul_f32 v[168:169], v[168:169], v[172:173] op_sel:[0,1] op_sel_hi:[1,1]\n\t" \
    "v_pk_mul_f32 v[174:175], v[174:175], v[172:173] op_sel:[0,1] op_sel_hi:[1,1]\n\t" \
    "v_cvt_pk_bf16_f32 v176, v168, v169\n\t" \
    "global_store_dword v18, v176, s[46:47] sc1\n\t" \
    "global_store_dword v3, v176, s[46:47] sc1\n\t" \
    "v_cvt_pk_bf16_f32 v177, v174, v175\n\t" \
    "global_store_dword v19, v177, s[46:47] sc1\n\t" \
    "global_store_dword v4, v177, s[46:47] sc1\n\t" \
    "s_cmp_eq_u32 s65, 63\n\t" \
    "s_cbranch_scc1 qk_last14_%=\n\t" \
    "s_mov_b32 s71, 3\n\t" \
    "qk_E29_%=:\n\t" \
    "s_memtime s[54:55]\n\t" \
    "s_nop 0\n\t" \
    "s_memrealtime s[4:5]\n\t" \
    "s_nop 0\n\t" \
    "s_memrealtime s[6:7]\n\t" \
    "s_nop 0\n\t" \
    "s_memrealtime s[8:9]\n\t" \
    "s_nop 0\n\t" \
    "s_memrealtime s[10:11]\n\t" \
    "s_nop 0\n\t" \
    "s_memrealtime s[12:13]\n\t" \
    "s_nop 0\n\t" \
    "s_memrealtime s[14:15]\n\t" \
    "s_nop 0\n\t" \
    "s_memrealtime s[16:17]\n\t" \
    "s_nop 0\n\t" \
    "s_memrealtime s[18:19]\n\t" \
    "s_nop 0\n\t" \
    "s_memrealtime s[20:21]\n\t" \
    "s_nop 0\n\t" \
    "s_memrealtime s[22:23]\n\t" \
    "s_nop 0\n\t" \
    "s_memrealtime s[24:25]\n\t" \
    "s_nop 0\n\t" \
    "s_memrealtime s[26:27]\n\t" \
    "s_nop 0\n\t" \
    "s_memrealtime s[28:29]\n\t" \
    "s_nop 0\n\t" \
    "s_memtime s[56:57]\n\t" \
    "s_mov_b32 s70, 1\n\t" \
    "s_sub_u32 s71, s71, 1\n\t" \
    "s_cselect_b32 s69, 0, -1\n\t" \
    "s_waitcnt lgkmcnt(0)\n\t" \
    "s_sub_u32 s54, s56, s54\n\t" \
    "s_and_b32 s54, s54, s69\n\t" \
    "s_cmp_gt_u32 s54, 124\n\t" \
    "s_cbranch_scc1 qk_E29_%=\n\t" \
    "s_cmp_eq_u32 s4, s28\n\t" \
    "s_addc_u32 s70, s70, 0\n\t" \
    "s_cmp_eq_u32 s6, s28\n\t" \
    "s_addc_u32 s70, s70, 0\n\t" \
    "s_cmp_eq_u32 s8, s28\n\t" \
    "s_addc_u32 s70, s70, 0\n\t" \
    "s_cmp_eq_u32 s10, s28\n\t" \
    "s_addc_u32 s70, s70, 0\n\t" \
    "s_cmp_eq_u32 s12, s28\n\t" \
    "s_addc_u32 s70, s70, 0\n\t" \
    "s_cmp_eq_u32 s14, s28\n\t" \
    "s_addc_u32 s70, s70, 0\n\t" \
    "s_cmp_eq_u32 s16, s28\n\t" \
    "s_addc_u32 s70, s70, 0\n\t" \
    "s_cmp_eq_u32 s18, s28\n\t" \
    "s_addc_u32 s70, s70, 0\n\t" \
    "s_cmp_eq_u32 s20, s28\n\t" \
    "s_addc_u32 s70, s70, 0\n\t" \
    "s_cmp_eq_u32 s22, s28\n\t" \
    "s_addc_u32 s70, s70, 0\n\t" \
    "s_cmp_eq_u32 s24, s28\n\t" \
    "s_addc_u32 s70, s70, 0\n\t" \
    "s_cmp_eq_u32 s26, s28\n\t" \
    "s_addc_u32 s70, s70, 0\n\t" \
    "s_mov_b32 s75, s73\n\t" \
    "s_mov_b32 s76, s74\n\t" \
    "s_lshl_b32 s70, s70, 3\n\t" \
    "s_sub_u32 s73, s56, s70\n\t" \
    "s_lshl_b32 s74, s28, 4\n\t" \
    "s_sub_u32 s69, s73, s75\n\t" \
    "s_sub_u32 s70, s74, s76\n\t" \
    "s_sub_u32 s71, s70, 1600\n\t" \
    "s_cmp_lt_u32 s71, 400000\n\t" \
    "s_cbranch_scc0 qk_K31_%=\n\t" \
    "v_cvt_f32_u32 v160, s69\n\t" \
    "v_cvt_f32_u32 v161, s70\n\t" \
    "v_rcp_f32 v162, v160\n\t" \
    "v_rcp_f32 v163, v161\n\t" \
    "v_mul_f32 v162, v162, v161\n\t" \
    "v_mul_f32 v163, v163, v160\n\t" \
    "v_mul_f32 v162, 0x4f800000, v162\n\t" \
    "v_mul_f32 v163, 0x4f000000, v163\n\t" \
    "v_cvt_u32_f32 v162, v162\n\t" \
    "v_cvt_u32_f32 v163, v163\n\t" \
    "v_readfirstlane_b32 s69, v162\n\t" \
    "v_readfirstlane_b32 s70, v163\n\t" \
    "s_nop 1\n\t" \
    "s_sub_u32 s69, s69, s78\n\t" \
    "s_ashr_i32 s69, s69, 2\n\t" \
    "s_add_u32 s78, s78, s69\n\t" \
    "s_sub_u32 s70, s70, s77\n\t" \
    "s_ashr_i32 s70, s70, 2\n\t" \
    "s_add_u32 s77, s77, s70\n\t" \
    "qk_K31_%=:\n\t" \
    "s_waitcnt vmcnt(4)\n\t" \
    "v_readfirstlane_b32 s69, v25\n\t" \
    "s_add_u32 s71, s65, 1\n\t" \
    "s_max_i32 s83, s83, s69\n\t" \
    "qk_WF32_%=:\n\t" \
    "s_cmp_ge_i32 s83, s71\n\t" \
    "s_cbranch_scc1 qk_WFok33_%=\n\t" \
    "global_load_dword v160, v24, s[36:37] sc1\n\t" \
    "s_waitcnt vmcnt(0)\n\t" \
    "v_readfirstlane_b32 s83, v160\n\t" \
    "s_nop 1\n\t" \
    "s_cmp_ge_i32 s83, s71\n\t" \
    "s_cbranch_scc1 qk_WFok33_%=\n\t" \
    "s_cmp_eq_u32 s68, 0\n\t" \
    "s_cbranch_scc1 qk_F34_%=\n\t" \
    "s_sub_u32 s68, s68, 1\n\t" \
    "s_sleep 2\n\t" \
    "s_branch qk_WF32_%=\n\t" \
    "qk_F34_%=:\n\t" \
    "v_mov_b32 v160, 6095104\n\t" \
    "v_mov_b32 v161, 9\n\t" \
    "global_store_dword v160, v161, s[36:37] sc1\n\t" \
    "s_waitcnt vmcnt(0)\n\t" \
    "s_branch qk_WFok33_%=\n\t" \
    "qk_WFok33_%=:\n\t" \
    "v_mov_b32 v72, 0\n\t" \
    "v_mov_b32 v73, 0\n\t" \
    "v_mov_b32 v74, 0\n\t" \
    "v_mov_b32 v75, 0\n\t" \
    "v_mov_b32 v76, 0\n\t" \
    "v_mov_b32 v77, 0\n\t" \
    "v_mov_b32 v78, 0\n\t" \
    "v_mov_b32 v79, 0\n\t" \
    "v_mov_b32 v80, 0\n\t" \
    "v_mov_b32 v81, 0\n\t" \
    "v_mov_b32 v82, 0\n\t" \
    "v_mov_b32 v83, 0\n\t" \
    "v_mov_b32 v84, 0\n\t" \
    "v_mov_b32 v85, 0\n\t" \
    "v_mov_b32 v86, 0\n\t" \
    "v_mov_b32 v87, 0\n\t" \
    "v_mov_b32 v88, 0\n\t" \
    "v_mov_b32 v89, 0\n\t" \
    "v_mov_b32 v90, 0\n\t" \
    "v_mov_b32 v91, 0\n\t" \
    "v_mov_b32 v92, 0\n\t" \
    "v_mov_b32 v93, 0\n\t" \
    "v_mov_b32 v94, 0\n\t" \
    "v_mov_b32 v95, 0\n\t" \
    "v_mov_b32 v96, 0\n\t" \
    "v_mov_b32 v97, 0\n\t" \
    "v_mov_b32 v98, 0\n\t" \
    "v_mov_b32 v99, 0\n\t" \
    "s_add_u32 s79, s74, 416\n\t" \
    "s_sub_u32 s70, s79, s74\n\t" \
    "s_max_i32 s70, s70, 0\n\t" \
    "s_min_i32 s70, s70, 16000\n\t" \
    "s_lshl_b32 s70, s70, 1\n\t" \
    "s_mul_hi_u32 s70, s70, s77\n\t" \
    "s_add_u32 s70, s70, s73\n\t" \
    "qk_W35_%=:\n\t" \
    "s_memtime s[56:57]\n\t" \
    "s_waitcnt lgkmcnt(0)\n\t" \
    "s_sub_u32 s69, s70, s56\n\t" \
    "s_cmp_gt_i32 s69, 24\n\t" \
    "s_cbranch_scc1 qk_W35_%=\n\t" \
    "global_load_dwordx4 v[100:103], v23, s[38:39] offset:0 sc1\n\t" \
    "global_load_dwordx4 v[104:107], v23, s[38:39] offset:2048 sc1\n\t" \
    "global_load_dwordx4 v[108:111], v15, s[38:39] offset:0 sc1\n\t" \
    "global_load_dwordx4 v[112:115], v15, s[38:39] offset:2048 sc1\n\t" \
    "global_load_dwordx4 v[116:119], v23, s[38:39] offset:1024 sc1\n\t" \
    "global_load_dwordx4 v[120:123], v23, s[38:39] offset:3072 sc1\n\t" \
    "global_load_dwordx4 v[124:127], v15, s[38:39] offset:1024 sc1\n\t" \
    "global_load_dwordx4 v[128:131], v15, s[38:39] offset:3072 sc1\n\t" \
    "s_add_u32 s65, s65, 1\n\t" \
    "v_add_u32 v31, 1, v31\n\t" \
    "s_add_u32 s69, s65, 0\n\t" \
    "s_bitcmp1_b32 s69, 0\n\t" \
    "s_cselect_b32 s70, s63, s62\n\t" \
    "s_bfe_u32 s69, s69, 0x20001\n\t" \
    "s_lshl_b32 s69, s69, 19\n\t" \
    "s_add_u32 s69, s69, s70\n\t" \
    "s_add_u32 s46, s36, s69\n\t" \
    "s_addc_u32 s47, s37, 0\n\t" \
    "s_add_u32 s69, s65, 3\n\t" \
    "s_cmp_ge_i32 s83, s69\n\t" \
    "s_cselect_b64 vcc, -1, 0\n\t" \
    "v_mov_b32 v9, 6233984\n\t" \
    "v_cndmask_b32 v9, v24, v9, vcc\n\t" \
    "s_mul_i32 s70, s67, 7\n\t" \
    "s_add_u32 s69, s65, 1\n\t" \
    "s_xor_b32 s69, s69, s70\n\t" \
    "s_and_b32 s69, s69, 63\n\t" \
    "s_lshl_b32 s69, s69, 13\n\t" \
    "s_mul_i32 s70, s67, 524288\n\t" \
    "s_add_u32 s69, s69, s70\n\t" \
    "s_add_u32 s69, s69, 23068672\n\t" \
    "s_add_u32 s38, s36, s69\n\t" \
    "s_addc_u32 s39, s37, 0\n\t" \
    "s_waitcnt lgkmcnt(0)\n\t" \
    "s_barrier\n\t" \
    "ds_read_b32 v160, v11 offset:20480\n\t" \
    "s_waitcnt lgkmcnt(0)\n\t" \
    "v_readfirstlane_b32 s81, v160\n\t" \
    "s_nop 1\n\t" \
    "s_mul_hi_u32 s69, s85, s78\n\t" \
    "s_add_u32 s79, s81, s69\n\t" \
    "s_add_u32 s79, s79, s87\n\t" \
    "s_cmp_ge_u32 s65, 37\n\t" \
    "s_cselect_b32 s69, 28, 0\n\t" \
    "s_sub_u32 s79, s79, s69\n\t" \
    "s_cmp_lt_u32 s65, 9\n\t" \
    "s_cselect_b32 s69, 96, 0\n\t" \
    "s_add_u32 s79, s79, s69\n\t" \
    "s_add_u32 s80, s79, s88\n\t" \
    "s_branch qk_go19_%=\n\t" \
    "qk_Gbad24_%=:\n\t" \
    "s_waitcnt vmcnt(0) lgkmcnt(0)\n\t" \
    "s_nop 7\n\t" \
    "v_mov_b32 v72, 0\n\t" \
    "v_mov_b32 v73, 0\n\t" \
    "v_mov_b32 v74, 0\n\t" \
    "v_mov_b32 v75, 0\n\t" \
    "v_mov_b32 v76, 0\n\t" \
    "v_mov_b32 v77, 0\n\t" \
    "v_mov_b32 v78, 0\n\t" \
    "v_mov_b32 v79, 0\n\t" \
    "v_mov_b32 v80, 0\n\t" \
    "v_mov_b32 v81, 0\n\t" \
    "v_mov_b32 v82, 0\n\t" \
    "v_mov_b32 v83, 0\n\t" \
    "v_mov_b32 v84, 0\n\t" \
    "v_mov_b32 v85, 0\n\t" \
    "v_mov_b32 v86, 0\n\t" \
    "v_mov_b32 v87, 0\n\t" \
    "s_cmp_eq_u32 s68, 0\n\t" \
    "s_cbranch_scc1 qk_F36_%=\n\t" \
    "s_sub_u32 s68, s68, 1\n\t" \
    "s_branch qk_G23_%=\n\t" \
    "qk_F36_%=:\n\t" \
    "v_mov_b32 v160, 6095104\n\t" \
    "v_mov_b32 v161, 1\n\t" \
    "global_store_dword v160, v161, s[36:37] sc1\n\t" \
    "s_waitcnt vmcnt(0)\n\t" \
    "s_branch qk_Gc26_%=\n\t" \
    "qk_Gbad225_%=:\n\t" \
    "global_load_dwordx4 v[44:47], v2, s[46:47] sc1\n\t" \
    "s_waitcnt vmcnt(0)\n\t" \
    "v_max3_u32 v160, v44, v45, v46\n\t" \
    "v_max_u32 v160, v160, v47\n\t" \
    "v_cmp_eq_u32 vcc, -1, v160\n\t" \
    "s_cbranch_vccz qk_Gre228_%=\n\t" \
    "s_cmp_eq_u32 s68, 0\n\t" \
    "s_cbranch_scc1 qk_F37_%=\n\t" \
    "s_sub_u32 s68, s68, 1\n\t" \
    "s_sleep 1\n\t" \
    "s_branch qk_Gbad225_%=\n\t" \
    "qk_F37_%=:\n\t" \
    "v_mov_b32 v160, 6095104\n\t" \
    "v_mov_b32 v161, 2\n\t" \
    "global_store_dword v160, v161, s[36:37] sc1\n\t" \
    "s_waitcnt vmcnt(0)\n\t" \
    "s_branch qk_Gre228_%=\n\t" \
    "qk_Gre228_%=:\n\t" \
    "v_mov_b32 v72, 0\n\t" \
    "v_mov_b32 v73, 0\n\t" \
    "v_mov_b32 v74, 0\n\t" \
    "v_mov_b32 v75, 0\n\t" \
    "v_mov_b32 v76, 0\n\t" \
    "v_mov_b32 v77, 0\n\t" \
    "v_mov_b32 v78, 0\n\t" \
    "v_mov_b32 v79, 0\n\t" \
    "v_mov_b32 v80, 0\n\t" \
    "v_mov_b32 v81, 0\n\t" \
    "v_mov_b32 v82, 0\n\t" \
    "v_mov_b32 v83, 0\n\t" \
    "v_mov_b32 v84, 0\n\t" \
    "v_mov_b32 v85, 0\n\t" \
    "v_mov_b32 v86, 0\n\t" \
    "v_mov_b32 v87, 0\n\t" \
    "v_smfmac_f32_16x16x64_bf16 v[72:75], v[36:39], a[0:7], v10\n\t" \
    "v_smfmac_f32_16x16x64_bf16 v[76:79], v[36:39], a[72:79], v10\n\t" \
    "v_smfmac_f32_16x16x64_bf16 v[80:83], v[36:39], a[144:151], v10\n\t" \
    "v_smfmac_f32_16x16x64_bf16 v[84:87], v[36:39], a[216:223], v10\n\t" \
    "v_smfmac_f32_16x16x64_bf16 v[72:75], v[40:43], a[8:15], v10\n\t" \
    "v_smfmac_f32_16x16x64_bf16 v[76:79], v[40:43], a[80:87], v10\n\t" \
    "v_smfmac_f32_16x16x64_bf16 v[80:83], v[40:43], a[152:159], v10\n\t" \
    "v_smfmac_f32_16x16x64_bf16 v[84:87], v[40:43], a[224:231], v10\n\t" \
    "v_smfmac_f32_16x16x64_bf16 v[72:75], v[44:47], a[64:71], v10\n\t" \
    "v_smfmac_f32_16x16x64_bf16 v[76:79], v[44:47], a[136:143], v10\n\t" \
    "v_smfmac_f32_16x16x64_bf16 v[80:83], v[44:47], a[208:215], v10\n\t" \
    "v_smfmac_f32_16x16x64_bf16 v[84:87], v[44:47], v[248:255], v10\n\t" \
    "s_branch qk_Gc227_%=\n\t" \
    "qk_k017_%=:\n\t" \
    "qk_WF38_%=:\n\t" \
    "s_cmp_ge_i32 s83, s65\n\t" \
    "s_cbranch_scc1 qk_WFok39_%=\n\t" \
    "global_load_dword v160, v24, s[36:37] sc1\n\t" \
    "s_waitcnt vmcnt(0)\n\t" \
    "v_readfirstlane_b32 s83, v160\n\t" \
    "s_nop 1\n\t" \
    "s_cmp_ge_i32 s83, s65\n\t" \
    "s_cbranch_scc1 qk_WFok39_%=\n\t" \
    "s_cmp_eq_u32 s68, 0\n\t" \
    "s_cbranch_scc1 qk_F40_%=\n\t" \
    "s_sub_u32 s68, s68, 1\n\t" \
    "s_sleep 2\n\t" \
    "s_branch qk_WF38_%=\n\t" \
    "qk_F40_%=:\n\t" \
    "v_mov_b32 v160, 6095104\n\t" \
    "v_mov_b32 v161, 8\n\t" \
    "global_store_dword v160, v161, s[36:37] sc1\n\t" \
    "s_waitcnt vmcnt(0)\n\t" \
    "s_branch qk_WFok39_%=\n\t" \
    "qk_WFok39_%=:\n\t" \
    "s_memrealtime s[54:55]\n\t" \
    "s_waitcnt lgkmcnt(0)\n\t" \
    "s_lshl_b32 s69, s54, 4\n\t" \
    "s_add_u32 s69, s69, 1280\n\t" \
    "v_mov_b32 v160, s69\n\t" \
    "global_load_dwordx4 v[100:103], v23, s[50:51] offset:0 sc1\n\t" \
    "global_load_dwordx4 v[104:107], v23, s[50:51] offset:2048 sc1\n\t" \
    "global_load_dwordx4 v[108:111], v15, s[50:51] offset:0 sc1\n\t" \
    "global_load_dwordx4 v[112:115], v15, s[50:51] offset:2048 sc1\n\t" \
    "global_load_dwordx4 v[116:119], v23, s[50:51] offset:1024 sc1\n\t" \
    "global_load_dwordx4 v[120:123], v23, s[50:51] offset:3072 sc1\n\t" \
    "global_load_dwordx4 v[124:127], v15, s[50:51] offset:1024 sc1\n\t" \
    "global_load_dwordx4 v[128:131], v15, s[50:51] offset:3072 sc1\n\t" \
    "global_store_dword v21, v160, s[46:47] sc1\n\t" \
    "s_waitcnt vmcnt(1)\n\t" \
    "s_branch qk_k0b18_%=\n\t" \
    "qk_last14_%=:\n\t" \
    "s_endpgm\n\t" \
    "qk_loop11_%=:\n\t" \
    "v_mov_b32 v72, 0\n\t" \
    "v_mov_b32 v73, 0\n\t" \
    "v_mov_b32 v74, 0\n\t" \
    "v_mov_b32 v75, 0\n\t" \
    "v_mov_b32 v76, 0\n\t" \
    "v_mov_b32 v77, 0\n\t" \
    "v_mov_b32 v78, 0\n\t" \
    "v_mov_b32 v79, 0\n\t" \
    "v_mov_b32 v80, 0\n\t" \
    "v_mov_b32 v81, 0\n\t" \
    "v_mov_b32 v82, 0\n\t" \
    "v_mov_b32 v83, 0\n\t" \
    "v_mov_b32 v84, 0\n\t" \
    "v_mov_b32 v85, 0\n\t" \
    "v_mov_b32 v86, 0\n\t" \
    "v_mov_b32 v87, 0\n\t" \
    "v_mov_b32 v88, 0\n\t" \
    "v_mov_b32 v89, 0\n\t" \
    "v_mov_b32 v90, 0\n\t" \
    "v_mov_b32 v91, 0\n\t" \
    "v_mov_b32 v92, 0\n\t" \
    "v_mov_b32 v93, 0\n\t" \
    "v_mov_b32 v94, 0\n\t" \
    "v_mov_b32 v95, 0\n\t" \
    "v_mov_b32 v96, 0\n\t" \
    "v_mov_b32 v97, 0\n\t" \
    "v_mov_b32 v98, 0\n\t" \
    "v_mov_b32 v99, 0\n\t" \
    "v_add_u32 v31, 1, v31\n\t" \
    "s_add_u32 s69, s65, 0\n\t" \
    "s_bitcmp1_b32 s69, 0\n\t" \
    "s_cselect_b32 s70, s63, s62\n\t" \
    "s_bfe_u32 s69, s69, 0x20001\n\t" \
    "s_lshl_b32 s69, s69, 19\n\t" \
    "s_add_u32 s69, s69, s70\n\t" \
    "s_add_u32 s46, s36, s69\n\t" \
    "s_addc_u32 s47, s37, 0\n\t" \
    "s_add_u32 s69, s65, 3\n\t" \
    "s_cmp_ge_i32 s83, s69\n\t" \
    "s_cselect_b64 vcc, -1, 0\n\t" \
    "v_mov_b32 v9, 6233984\n\t" \
    "v_cndmask_b32 v9, v24, v9, vcc\n\t" \
    "s_mul_i32 s70, s67, 7\n\t" \
    "s_add_u32 s69, s65, 1\n\t" \
    "s_xor_b32 s69, s69, s70\n\t" \
    "s_and_b32 s69, s69, 63\n\t" \
    "s_lshl_b32 s69, s69, 13\n\t" \
    "s_mul_i32 s70, s67, 524288\n\t" \
    "s_add_u32 s69, s69, s70\n\t" \
    "s_add_u32 s69, s69, 23068672\n\t" \
    "s_add_u32 s38, s36, s69\n\t" \
    "s_addc_u32 s39, s37, 0\n\t" \
    "qk_go46_%=:\n\t" \
    "s_sub_u32 s70, s79, s74\n\t" \
    "s_max_i32 s70, s70, 0\n\t" \
    "s_min_i32 s70, s70, 16000\n\t" \
    "s_lshl_b32 s70, s70, 1\n\t" \
    "s_mul_hi_u32 s70, s70, s77\n\t" \
    "s_add_u32 s70, s70, s73\n\t" \
    "s_sub_u32 s70, s70, 100\n\t" \
    "s_getpc_b64 s[30:31]\n\t" \
    "qk_Wp48_%=:\n\t" \
    "s_add_u32 s30, s30, qk_We49_%=-qk_Wp48_%=\n\t" \
    "s_addc_u32 s31, s31, 0\n\t" \
    "qk_W47_%=:\n\t" \
    "s_memtime s[56:57]\n\t" \
    "s_waitcnt lgkmcnt(0)\n\t" \
    "s_sub_u32 s69, s70, s56\n\t" \
    "s_cmp_gt_i32 s69, 111\n\t" \
    "s_cbranch_scc1 qk_W47_%=\n\t" \
    "s_max_i32 s69, s69, 0\n\t" \
    "s_lshr_b32 s69, s69, 1\n\t" \
    "s_and_b32 s69, s69, 0x3c\n\t" \
    "s_sub_u32 s30, s30, s69\n\t" \
    "s_subb_u32 s31, s31, 0\n\t" \
    "s_setpc_b64 s[30:31]\n\t" \
    ".p2align 6\n\t" \
    "s_nop 0\n\t" \
    "s_nop 0\n\t" \
    "s_nop 0\n\t" \
    "s_nop 1\n\t" \
    "s_nop 1\n\t" \
    "s_nop 1\n\t" \
    "s_nop 1\n\t" \
    "s_nop 1\n\t" \
    "s_nop 1\n\t" \
    "s_nop 1\n\t" \
    "s_nop 1\n\t" \
    "s_nop 1\n\t" \
    "s_nop 1\n\t" \
    "s_nop 1\n\t" \
    "s_nop 1\n\t" \
    "s_nop 1\n\t" \
    "qk_We49_%=:\n\t" \
    "qk_G50_%=:\n\t" \
    "global_load_dwordx4 v[36:39], v1, s[46:47] sc1\n\t" \
    "s_nop 9\n\t" \
    "global_load_dwordx4 v[40:43], v1, s[46:47] offset:1024 sc1\n\t" \
    "s_nop 9\n\t" \
    "global_load_dwordx4 v[44:47], v2, s[46:47] sc1\n\t" \
    "s_waitcnt vmcnt(2)\n\t" \
    "v_smfmac_f32_16x16x64_bf16 v[72:75], v[36:39], a[16:23], v10\n\t" \
    "ds_write_b128 v0, v[36:39] offset:2048\n\t" \
    "v_smfmac_f32_16x16x64_bf16 v[76:79], v[36:39], a[88:95], v10\n\t" \
    "v_max3_u32 v160, v36, v37, v38\n\t" \
    "v_smfmac_f32_16x16x64_bf16 v[80:83], v[36:39], a[160:167], v10\n\t" \
    "v_smfmac_f32_16x16x64_bf16 v[84:87], v[36:39], a[232:239], v10\n\t" \
    "s_waitcnt vmcnt(1)\n\t" \
    "ds_write_b128 v0, v[40:43] offset:3072\n\t" \
    "v_smfmac_f32_16x16x64_bf16 v[72:75], v[40:43], a[24:31], v10\n\t" \
    "v_max3_u32 v160, v160, v39, v40\n\t" \
    "v_max3_u32 v160, v160, v41, v42\n\t" \
    "v_smfmac_f32_16x16x64_bf16 v[76:79], v[40:43], a[96:103], v10\n\t" \
    "v_max_u32 v160, v160, v43\n\t" \
    "v_cmp_eq_u32 vcc, -1, v160\n\t" \
    "v_smfmac_f32_16x16x64_bf16 v[80:83], v[40:43], a[168:175], v10\n\t" \
    "v_smfmac_f32_16x16x64_bf16 v[84:87], v[40:43], a[240:247], v10\n\t" \
    "s_cbranch_vccnz qk_Gbad51_%=\n\t" \
    "qk_Gc53_%=:\n\t" \
    "s_waitcnt lgkmcnt(0)\n\t" \
    "s_barrier\n\t" \
    "ds_read_b128 v[48:51], v0 offset:4096\n\t" \
    "ds_read_b128 v[52:55], v0 offset:5120\n\t" \
    "s_waitcnt vmcnt(0)\n\t" \
    "v_smfmac_f32_16x16x64_bf16 v[72:75], v[44:47], a[64:71], v10\n\t" \
    "ds_read_b128 v[56:59], v0 offset:6144\n\t" \
    "v_max3_u32 v160, v44, v45, v46\n\t" \
    "v_smfmac_f32_16x16x64_bf16 v[76:79], v[44:47], a[136:143], v10\n\t" \
    "ds_read_b128 v[60:63], v0 offset:7168\n\t" \
    "v_max_u32 v160, v160, v47\n\t" \
    "v_smfmac_f32_16x16x64_bf16 v[80:83], v[44:47], a[208:215], v10\n\t" \
    "ds_read_b128 v[64:67], v0 offset:0\n\t" \
    "v_cmp_eq_u32 vcc, -1, v160\n\t" \
    "v_smfmac_f32_16x16x64_bf16 v[84:87], v[44:47], v[248:255], v10\n\t" \
    "ds_read_b128 v[68:71], v0 offset:1024\n\t" \
    "s_cbranch_vccnz qk_Gbad252_%=\n\t" \
    "qk_Gc254_%=:\n\t" \
    "s_waitcnt lgkmcnt(5)\n\t" \
    "v_smfmac_f32_16x16x64_bf16 v[72:75], v[48:51], a[32:39], v10\n\t" \
    "s_waitcnt lgkmcnt(4)\n\t" \
    "v_smfmac_f32_16x16x64_bf16 v[72:75], v[52:55], a[40:47], v10\n\t" \
    "s_waitcnt lgkmcnt(3)\n\t" \
    "v_smfmac_f32_16x16x64_bf16 v[72:75], v[56:59], a[48:55], v10\n\t" \
    "global_load_dword v25, v9, s[36:37] sc1\n\t" \
    "s_waitcnt lgkmcnt(2)\n\t" \
    "v_smfmac_f32_16x16x64_bf16 v[72:75], v[60:63], a[56:63], v10\n\t" \
    "s_waitcnt lgkmcnt(1)\n\t" \
    "v_smfmac_f32_16x16x64_bf16 v[72:75], v[64:67], a[0:7], v10\n\t" \
    "s_waitcnt lgkmcnt(0)\n\t" \
    "v_smfmac_f32_16x16x64_bf16 v[72:75], v[68:71], a[8:15], v10\n\t" \
    "v_smfmac_f32_16x16x64_bf16 v[76:79], v[48:51], a[104:111], v10\n\t" \
    "s_nop 1\n\t" \
    "s_nop 0\n\t" \
    "v_smfmac_f32_16x16x64_bf16 v[76:79], v[52:55], a[112:119], v10\n\t" \
    "s_nop 1\n\t" \
    "s_nop 0\n\t" \
    "v_smfmac_f32_16x16x64_bf16 v[76:79], v[56:59], a[120:127], v10\n\t" \
    "v_add_f32 v72, v72, v74\n\t" \
    "v_smfmac_f32_16x16x64_bf16 v[76:79], v[60:63], a[128:135], v10\n\t" \
    "v_add_f32 v73, v73, v75\n\t" \
    "v_smfmac_f32_16x16x64_bf16 v[76:79], v[64:67], a[72:79], v10\n\t" \
    "v_cvt_pk_bf16_f32 v156, v72, v73\n\t" \
    "v_smfmac_f32_16x16x64_bf16 v[76:79], v[68:71], a[80:87], v10\n\t" \
    "ds_write_b32 v12, v156 offset:0\n\t" \
    "v_smfmac_f32_16x16x64_bf16 v[80:83], v[48:51], a[176:183], v10\n\t" \
    "s_nop 1\n\t" \
    "s_nop 0\n\t" \
    "v_smfmac_f32_16x16x64_bf16 v[80:83], v[52:55], a[184:191], v10\n\t" \
    "s_nop 1\n\t" \
    "s_nop 0\n\t" \
    "v_smfmac_f32_16x16x64_bf16 v[80:83], v[56:59], a[192:199], v10\n\t" \
    "v_add_f32 v76, v76, v78\n\t" \
    "v_smfmac_f32_16x16x64_bf16 v[80:83], v[60:63], a[200:207], v10\n\t" \
    "v_add_f32 v77, v77, v79\n\t" \
    "v_smfmac_f32_16x16x64_bf16 v[80:83], v[64:67], a[144:151], v10\n\t" \
    "v_cvt_pk_bf16_f32 v157, v76, v77\n\t" \
    "v_smfmac_f32_16x16x64_bf16 v[80:83], v[68:71], a[152:159], v10\n\t" \
    "ds_write_b32 v12, v157 offset:64\n\t" \
    "v_smfmac_f32_16x16x64_bf16 v[84:87], v[48:51], a[248:255], v10\n\t" \
    "s_nop 1\n\t" \
    "s_nop 0\n\t" \
    "v_smfmac_f32_16x16x64_bf16 v[84:87], v[52:55], v[224:231], v10\n\t" \
    "s_nop 1\n\t" \
    "s_nop 0\n\t" \
    "v_smfmac_f32_16x16x64_bf16 v[84:87], v[56:59], v[232:239], v10\n\t" \
    "v_add_f32 v80, v80, v82\n\t" \
    "v_smfmac_f32_16x16x64_bf16 v[84:87], v[60:63], v[240:247], v10\n\t" \
    "v_add_f32 v81, v81, v83\n\t" \
    "v_smfmac_f32_16x16x64_bf16 v[84:87], v[64:67], a[216:223], v10\n\t" \
    "v_cvt_pk_bf16_f32 v158, v80, v81\n\t" \
    "v_smfmac_f32_16x16x64_bf16 v[84:87], v[68:71], a[224:231], v10\n\t" \
    "ds_write_b32 v16, v158 offset:0\n\t" \
    "s_nop 6\n\t" \
    "v_pk_add_f32 v[84:85], v[84:85], v[86:87]\n\t" \
    "v_cvt_pk_bf16_f32 v159, v84, v85\n\t" \
    "ds_write_b32 v16, v159 offset:256\n\t" \
    "s_cmp_eq_u32 s65, 0\n\t" \
    "s_cbranch_scc1 qk_k044_%=\n\t" \
    "qk_k0b45_%=:\n\t" \
    "s_waitcnt lgkmcnt(0)\n\t" \
    "s_barrier\n\t" \
    "ds_read_b64_tr_b16 v[132:133], v13 offset:0\n\t" \
    "ds_read_b64_tr_b16 v[134:135], v13 offset:128\n\t" \
    "ds_read_b64_tr_b16 v[136:137], v13 offset:1024\n\t" \
    "ds_read_b64_tr_b16 v[138:139], v13 offset:1152\n\t" \
    "ds_read_b64_tr_b16 v[140:141], v17 offset:0\n\t" \
    "ds_read_b64_tr_b16 v[142:143], v17 offset:256\n\t" \
    "ds_read_b64_tr_b16 v[144:145], v17 offset:512\n\t" \
    "ds_read_b64_tr_b16 v[146:147], v17 offset:768\n\t" \
    "ds_read_b64_tr_b16 v[148:149], v17 offset:1024\n\t" \
    "ds_read_b64_tr_b16 v[150:151], v17 offset:1280\n\t" \
    "ds_read_b64_tr_b16 v[152:153], v17 offset:1536\n\t" \
    "ds_read_b64_tr_b16 v[154:155], v17 offset:1792\n\t" \
    "s_waitcnt lgkmcnt(10)\n\t" \
    "v_smfmac_f32_16x16x64_bf16 v[88:91], v[132:135], v[100:107], v10\n\t" \
    "v_smfmac_f32_16x16x64_bf16 v[92:95], v[132:135], v[116:123], v10\n\t" \
    "s_waitcnt lgkmcnt(8)\n\t" \
    "v_smfmac_f32_16x16x64_bf16 v[88:91], v[136:139], v[108:115], v10\n\t" \
    "v_smfmac_f32_16x16x64_bf16 v[92:95], v[136:139], v[124:131], v10\n\t" \
    "s_waitcnt lgkmcnt(4)\n\t" \
    "v_smfmac_f32_16x16x64_bf16 v[96:99], v[132:135], v[140:147], v10\n\t" \
    "s_waitcnt lgkmcnt(0)\n\t" \
    "s_nop 1\n\t" \
    "v_smfmac_f32_16x16x64_bf16 v[96:99], v[136:139], v[148:155], v10\n\t" \
    "s_nop 0\n\t" \
    "v_add_f32 v168, v89, v91\n\t" \
    "v_add_f32 v169, v93, v95\n\t" \
    "v_max_f32 v178, v168, v169\n\t" \
    "v_mul_f32 v168, v168, v29\n\t" \
    "v_mul_f32 v169, v169, v29\n\t" \
    "v_max_f32_dpp v178, v178, v178 row_mirror row_mask:0xf bank_mask:0xf\n\t" \
    "s_nop 0\n\t" \
    "v_add_f32 v172, v97, v99\n\t" \
    "v_max_f32 v173, v178, v172\n\t" \
    "v_mul_f32 v172, v172, v29\n\t" \
    "s_nop 0\n\t" \
    "v_max_f32_dpp v173, v173, v173 quad_perm:[1,0,3,2] row_mask:0xf bank_mask:0xf\n\t" \
    "s_nop 1\n\t" \
    "v_max_f32_dpp v173, v173, v173 quad_perm:[2,3,0,1] row_mask:0xf bank_mask:0xf\n\t" \
    "s_nop 1\n\t" \
    "v_max_f32_dpp v173, v173, v173 row_half_mirror row_mask:0xf bank_mask:0xf\n\t" \
    "v_fma_f32 v172, -v173, v29, v172\n\t" \
    "v_fma_f32 v168, -v173, v29, v168\n\t" \
    "v_fma_f32 v169, -v173, v29, v169\n\t" \
    "v_exp_f32 v172, v172\n\t" \
    "v_exp_f32 v168, v168\n\t" \
    "v_exp_f32 v169, v169\n\t" \
    "v_fma_f32 v173, v172, 0.5, v168\n\t" \
    "v_add_f32 v173, v173, v169\n\t" \
    "v_mov_b32_dpp v174, v172 quad_perm:[0,0,2,2] row_mask:0xf bank_mask:0xf\n\t" \
    "v_mov_b32_dpp v175, v172 quad_perm:[1,1,3,3] row_mask:0xf bank_mask:0xf\n\t" \
    "v_add_f32_dpp v173, v173, v173 quad_perm:[1,0,3,2] row_mask:0xf bank_mask:0xf\n\t" \
    "v_mov_b32_dpp v176, v168 quad_perm:[1,0,3,2] row_mask:0xf bank_mask:0xf\n\t" \
    "v_mov_b32_dpp v177, v169 quad_perm:[1,0,3,2] row_mask:0xf bank_mask:0xf\n\t" \
    "v_add_f32_dpp v173, v173, v173 quad_perm:[2,3,0,1] row_mask:0xf bank_mask:0xf\n\t" \
    "v_cndmask_b32 v168, v168, v177, s[48:49]\n\t" \
    "v_cndmask_b32 v169, v176, v169, s[48:49]\n\t" \
    "v_add_f32_dpp v173, v173, v173 row_half_mirror row_mask:0xf bank_mask:0xf\n\t" \
    "s_nop 1\n\t" \
    "v_add_f32_dpp v173, v173, v173 row_mirror row_mask:0xf bank_mask:0xf\n\t" \
    "v_rcp_f32 v173, v173\n\t" \
    "s_nop 0\n\t" \
    "v_pk_mul_f32 v[168:169], v[168:169], v[172:173] op_sel:[0,1] op_sel_hi:[1,1]\n\t" \
    "v_pk_mul_f32 v[174:175], v[174:175], v[172:173] op_sel:[0,1] op_sel_hi:[1,1]\n\t" \
    "v_cvt_pk_bf16_f32 v176, v168, v169\n\t" \
    "global_store_dword v18, v176, s[46:47] sc1\n\t" \
    "global_store_dword v3, v176, s[46:47] sc1\n\t" \
    "v_cvt_pk_bf16_f32 v177, v174, v175\n\t" \
    "global_store_dword v19, v177, s[46:47] sc1\n\t" \
    "global_store_dword v4, v177, s[46:47] sc1\n\t" \
    "s_cmp_eq_u32 s65, 63\n\t" \
    "s_cbranch_scc1 qk_last41_%=\n\t" \
    "s_mov_b32 s71, 3\n\t" \
    "qk_E56_%=:\n\t" \
    "s_memtime s[54:55]\n\t" \
    "s_nop 0\n\t" \
    "s_memrealtime s[4:5]\n\t" \
    "s_nop 0\n\t" \
    "s_memrealtime s[6:7]\n\t" \
    "s_nop 0\n\t" \
    "s_memrealtime s[8:9]\n\t" \
    "s_nop 0\n\t" \
    "s_memrealtime s[10:11]\n\t" \
    "s_nop 0\n\t" \
    "s_memrealtime s[12:13]\n\t" \
    "s_nop 0\n\t" \
    "s_memrealtime s[14:15]\n\t" \
    "s_nop 0\n\t" \
    "s_memrealtime s[16:17]\n\t" \
    "s_nop 0\n\t" \
    "s_memrealtime s[18:19]\n\t" \
    "s_nop 0\n\t" \
    "s_memrealtime s[20:21]\n\t" \
    "s_nop 0\n\t" \
    "s_memrealtime s[22:23]\n\t" \
    "s_nop 0\n\t" \
    "s_memrealtime s[24:25]\n\t" \
    "s_nop 0\n\t" \
    "s_memrealtime s[26:27]\n\t" \
    "s_nop 0\n\t" \
    "s_memrealtime s[28:29]\n\t" \
    "s_nop 0\n\t" \
    "s_memtime s[56:57]\n\t" \
    "s_mov_b32 s70, 1\n\t" \
    "s_sub_u32 s71, s71, 1\n\t" \
    "s_cselect_b32 s69, 0, -1\n\t" \
    "s_waitcnt lgkmcnt(0)\n\t" \
    "s_sub_u32 s54, s56, s54\n\t" \
    "s_and_b32 s54, s54, s69\n\t" \
    "s_cmp_gt_u32 s54, 124\n\t" \
    "s_cbranch_scc1 qk_E56_%=\n\t" \
    "s_cmp_eq_u32 s4, s28\n\t" \
    "s_addc_u32 s70, s70, 0\n\t" \
    "s_cmp_eq_u32 s6, s28\n\t" \
    "s_addc_u32 s70, s70, 0\n\t" \
    "s_cmp_eq_u32 s8, s28\n\t" \
    "s_addc_u32 s70, s70, 0\n\t" \
    "s_cmp_eq_u32 s10, s28\n\t" \
    "s_addc_u32 s70, s70, 0\n\t" \
    "s_cmp_eq_u32 s12, s28\n\t" \
    "s_addc_u32 s70, s70, 0\n\t" \
    "s_cmp_eq_u32 s14, s28\n\t" \
    "s_addc_u32 s70, s70, 0\n\t" \
    "s_cmp_eq_u32 s16, s28\n\t" \
    "s_addc_u32 s70, s70, 0\n\t" \
    "s_cmp_eq_u32 s18, s28\n\t" \
    "s_addc_u32 s70, s70, 0\n\t" \
    "s_cmp_eq_u32 s20, s28\n\t" \
    "s_addc_u32 s70, s70, 0\n\t" \
    "s_cmp_eq_u32 s22, s28\n\t" \
    "s_addc_u32 s70, s70, 0\n\t" \
    "s_cmp_eq_u32 s24, s28\n\t" \
    "s_addc_u32 s70, s70, 0\n\t" \
    "s_cmp_eq_u32 s26, s28\n\t" \
    "s_addc_u32 s70, s70, 0\n\t" \
    "s_mov_b32 s75, s73\n\t" \
    "s_mov_b32 s76, s74\n\t" \
    "s_lshl_b32 s70, s70, 3\n\t" \
    "s_sub_u32 s73, s56, s70\n\t" \
    "s_lshl_b32 s74, s28, 4\n\t" \
    "s_sub_u32 s69, s73, s75\n\t" \
    "s_sub_u32 s70, s74, s76\n\t" \
    "s_sub_u32 s71, s70, 1600\n\t" \
    "s_cmp_lt_u32 s71, 400000\n\t" \
    "s_cbranch_scc0 qk_K58_%=\n\t" \
    "v_cvt_f32_u32 v160, s69\n\t" \
    "v_cvt_f32_u32 v161, s70\n\t" \
    "v_rcp_f32 v162, v160\n\t" \
    "v_rcp_f32 v163, v161\n\t" \
    "v_mul_f32 v162, v162, v161\n\t" \
    "v_mul_f32 v163, v163, v160\n\t" \
    "v_mul_f32 v162, 0x4f800000, v162\n\t" \
    "v_mul_f32 v163, 0x4f000000, v163\n\t" \
    "v_cvt_u32_f32 v162, v162\n\t" \
    "v_cvt_u32_f32 v163, v163\n\t" \
    "v_readfirstlane_b32 s69, v162\n\t" \
    "v_readfirstlane_b32 s70, v163\n\t" \
    "s_nop 1\n\t" \
    "s_sub_u32 s69, s69, s78\n\t" \
    "s_ashr_i32 s69, s69, 2\n\t" \
    "s_add_u32 s78, s78, s69\n\t" \
    "s_sub_u32 s70, s70, s77\n\t" \
    "s_ashr_i32 s70, s70, 2\n\t" \
    "s_add_u32 s77, s77, s70\n\t" \
    "qk_K58_%=:\n\t" \
    "s_waitcnt vmcnt(4)\n\t" \
    "v_readfirstlane_b32 s69, v25\n\t" \
    "s_add_u32 s71, s65, 1\n\t" \
    "s_max_i32 s83, s83, s69\n\t" \
    "qk_WF59_%=:\n\t" \
    "s_cmp_ge_i32 s83, s71\n\t" \
    "s_cbranch_scc1 qk_WFok60_%=\n\t" \
    "global_load_dword v160, v24, s[36:37] sc1\n\t" \
    "s_waitcnt vmcnt(0)\n\t" \
    "v_readfirstlane_b32 s83, v160\n\t" \
    "s_nop 1\n\t" \
    "s_cmp_ge_i32 s83, s71\n\t" \
    "s_cbranch_scc1 qk_WFok60_%=\n\t" \
    "s_cmp_eq_u32 s68, 0\n\t" \
    "s_cbranch_scc1 qk_F61_%=\n\t" \
    "s_sub_u32 s68, s68, 1\n\t" \
    "s_sleep 2\n\t" \
    "s_branch qk_WF59_%=\n\t" \
    "qk_F61_%=:\n\t" \
    "v_mov_b32 v160, 6095104\n\t" \
    "v_mov_b32 v161, 9\n\t" \
    "global_store_dword v160, v161, s[36:37] sc1\n\t" \
    "s_waitcnt vmcnt(0)\n\t" \
    "s_branch qk_WFok60_%=\n\t" \
    "qk_WFok60_%=:\n\t" \
    "v_mov_b32 v72, 0\n\t" \
    "v_mov_b32 v73, 0\n\t" \
    "v_mov_b32 v74, 0\n\t" \
    "v_mov_b32 v75, 0\n\t" \
    "v_mov_b32 v76, 0\n\t" \
    "v_mov_b32 v77, 0\n\t" \
    "v_mov_b32 v78, 0\n\t" \
    "v_mov_b32 v79, 0\n\t" \
    "v_mov_b32 v80, 0\n\t" \
    "v_mov_b32 v81, 0\n\t" \
    "v_mov_b32 v82, 0\n\t" \
    "v_mov_b32 v83, 0\n\t" \
    "v_mov_b32 v84, 0\n\t" \
    "v_mov_b32 v85, 0\n\t" \
    "v_mov_b32 v86, 0\n\t" \
    "v_mov_b32 v87, 0\n\t" \
    "v_mov_b32 v88, 0\n\t" \
    "v_mov_b32 v89, 0\n\t" \
    "v_mov_b32 v90, 0\n\t" \
    "v_mov_b32 v91, 0\n\t" \
    "v_mov_b32 v92, 0\n\t" \
    "v_mov_b32 v93, 0\n\t" \
    "v_mov_b32 v94, 0\n\t" \
    "v_mov_b32 v95, 0\n\t" \
    "v_mov_b32 v96, 0\n\t" \
    "v_mov_b32 v97, 0\n\t" \
    "v_mov_b32 v98, 0\n\t" \
    "v_mov_b32 v99, 0\n\t" \
    "s_add_u32 s79, s74, 416\n\t" \
    "s_sub_u32 s70, s79, s74\n\t" \
    "s_max_i32 s70, s70, 0\n\t" \
    "s_min_i32 s70, s70, 16000\n\t" \
    "s_lshl_b32 s70, s70, 1\n\t" \
    "s_mul_hi_u32 s70, s70, s77\n\t" \
    "s_add_u32 s70, s70, s73\n\t" \
    "qk_W62_%=:\n\t" \
    "s_memtime s[56:57]\n\t" \
    "s_waitcnt lgkmcnt(0)\n\t" \
    "s_sub_u32 s69, s70, s56\n\t" \
    "s_cmp_gt_i32 s69, 24\n\t" \
    "s_cbranch_scc1 qk_W62_%=\n\t" \
    "global_load_dwordx4 v[100:103], v23, s[38:39] offset:0 sc1\n\t" \
    "global_load_dwordx4 v[104:107], v23, s[38:39] offset:2048 sc1\n\t" \
    "global_load_dwordx4 v[108:111], v15, s[38:39] offset:0 sc1\n\t" \
    "global_load_dwordx4 v[112:115], v15, s[38:39] offset:2048 sc1\n\t" \
    "global_load_dwordx4 v[116:119], v23, s[38:39] offset:1024 sc1\n\t" \
    "global_load_dwordx4 v[120:123], v23, s[38:39] offset:3072 sc1\n\t" \
    "global_load_dwordx4 v[124:127], v15, s[38:39] offset:1024 sc1\n\t" \
    "global_load_dwordx4 v[128:131], v15, s[38:39] offset:3072 sc1\n\t" \
    "s_add_u32 s65, s65, 1\n\t" \
    "v_add_u32 v31, 1, v31\n\t" \
    "s_add_u32 s69, s65, 0\n\t" \
    "s_bitcmp1_b32 s69, 0\n\t" \
    "s_cselect_b32 s70, s63, s62\n\t" \
    "s_bfe_u32 s69, s69, 0x20001\n\t" \
    "s_lshl_b32 s69, s69, 19\n\t" \
    "s_add_u32 s69, s69, s70\n\t" \
    "s_add_u32 s46, s36, s69\n\t" \
    "s_addc_u32 s47, s37, 0\n\t" \
    "s_add_u32 s69, s65, 3\n\t" \
    "s_cmp_ge_i32 s83, s69\n\t" \
    "s_cselect_b64 vcc, -1, 0\n\t" \
    "v_mov_b32 v9, 6233984\n\t" \
    "v_cndmask_b32 v9, v24, v9, vcc\n\t" \
    "s_mul_i32 s70, s67, 7\n\t" \
    "s_add_u32 s69, s65, 1\n\t" \
    "s_xor_b32 s69, s69, s70\n\t" \
    "s_and_b32 s69, s69, 63\n\t" \
    "s_lshl_b32 s69, s69, 13\n\t" \
    "s_mul_i32 s70, s67, 524288\n\t" \
    "s_add_u32 s69, s69, s70\n\t" \
    "s_add_u32 s69, s69, 23068672\n\t" \
    "s_add_u32 s38, s36, s69\n\t" \
    "s_addc_u32 s39, s37, 0\n\t" \
    "s_waitcnt lgkmcnt(0)\n\t" \
    "s_barrier\n\t" \
    "ds_read_b32 v160, v11 offset:20480\n\t" \
    "s_waitcnt lgkmcnt(0)\n\t" \
    "v_readfirstlane_b32 s81, v160\n\t" \
    "s_nop 1\n\t" \
    "s_mul_hi_u32 s69, s85, s78\n\t" \
    "s_add_u32 s79, s81, s69\n\t" \
    "s_add_u32 s79, s79, s87\n\t" \
    "s_cmp_ge_u32 s65, 37\n\t" \
    "s_cselect_b32 s69, 28, 0\n\t" \
    "s_sub_u32 s79, s79, s69\n\t" \
    "s_cmp_lt_u32 s65, 9\n\t" \
    "s_cselect_b32 s69, 96, 0\n\t" \
    "s_add_u32 s79, s79, s69\n\t" \
    "s_add_u32 s80, s79, s88\n\t" \
    "s_branch qk_go46_%=\n\t" \
    "qk_Gbad51_%=:\n\t" \
    "s_waitcnt vmcnt(0) lgkmcnt(0)\n\t" \
    "s_nop 7\n\t" \
    "v_mov_b32 v72, 0\n\t" \
    "v_mov_b32 v73, 0\n\t" \
    "v_mov_b32 v74, 0\n\t" \
    "v_mov_b32 v75, 0\n\t" \
    "v_mov_b32 v76, 0\n\t" \
    "v_mov_b32 v77, 0\n\t" \
    "v_mov_b32 v78, 0\n\t" \
    "v_mov_b32 v79, 0\n\t" \
    "v_mov_b32 v80, 0\n\t" \
    "v_mov_b32 v81, 0\n\t" \
    "v_mov_b32 v82, 0\n\t" \
    "v_mov_b32 v83, 0\n\t" \
    "v_mov_b32 v84, 0\n\t" \
    "v_mov_b32 v85, 0\n\t" \
    "v_mov_b32 v86, 0\n\t" \
    "v_mov_b32 v87, 0\n\t" \
    "s_cmp_eq_u32 s68, 0\n\t" \
    "s_cbranch_scc1 qk_F63_%=\n\t" \
    "s_sub_u32 s68, s68, 1\n\t" \
    "s_branch qk_G50_%=\n\t" \
    "qk_F63_%=:\n\t" \
    "v_mov_b32 v160, 6095104\n\t" \
    "v_mov_b32 v161, 1\n\t" \
    "global_store_dword v160, v161, s[36:37] sc1\n\t" \
    "s_waitcnt vmcnt(0)\n\t" \
    "s_branch qk_Gc53_%=\n\t" \
    "qk_Gbad252_%=:\n\t" \
    "global_load_dwordx4 v[44:47], v2, s[46:47] sc1\n\t" \
    "s_waitcnt vmcnt(0)\n\t" \
    "v_max3_u32 v160, v44, v45, v46\n\t" \
    "v_max_u32 v160, v160, v47\n\t" \
    "v_cmp_eq_u32 vcc, -1, v160\n\t" \
    "s_cbranch_vccz qk_Gre255_%=\n\t" \
    "s_cmp_eq_u32 s68, 0\n\t" \
    "s_cbranch_scc1 qk_F64_%=\n\t" \
    "s_sub_u32 s68, s68, 1\n\t" \
    "s_sleep 1\n\t" \
    "s_branch qk_Gbad252_%=\n\t" \
    "qk_F64_%=:\n\t" \
    "v_mov_b32 v160, 6095104\n\t" \
    "v_mov_b32 v161, 2\n\t" \
    "global_store_dword v160, v161, s[36:37] sc1\n\t" \
    "s_waitcnt vmcnt(0)\n\t" \
    "s_branch qk_Gre255_%=\n\t" \
    "qk_Gre255_%=:\n\t" \
    "v_mov_b32 v72, 0\n\t" \
    "v_mov_b32 v73, 0\n\t" \
    "v_mov_b32 v74, 0\n\t" \
    "v_mov_b32 v75, 0\n\t" \
    "v_mov_b32 v76, 0\n\t" \
    "v_mov_b32 v77, 0\n\t" \
    "v_mov_b32 v78, 0\n\t" \
    "v_mov_b32 v79, 0\n\t" \
    "v_mov_b32 v80, 0\n\t" \
    "v_mov_b32 v81, 0\n\t" \
    "v_mov_b32 v82, 0\n\t" \
    "v_mov_b32 v83, 0\n\t" \
    "v_mov_b32 v84, 0\n\t" \
    "v_mov_b32 v85, 0\n\t" \
    "v_mov_b32 v86, 0\n\t" \
    "v_mov_b32 v87, 0\n\t" \
    "v_smfmac_f32_16x16x64_bf16 v[72:75], v[36:39], a[16:23], v10\n\t" \
    "v_smfmac_f32_16x16x64_bf16 v[76:79], v[36:39], a[88:95], v10\n\t" \
    "v_smfmac_f32_16x16x64_bf16 v[80:83], v[36:39], a[160:167], v10\n\t" \
    "v_smfmac_f32_16x16x64_bf16 v[84:87], v[36:39], a[232:239], v10\n\t" \
    "v_smfmac_f32_16x16x64_bf16 v[72:75], v[40:43], a[24:31], v10\n\t" \
    "v_smfmac_f32_16x16x64_bf16 v[76:79], v[40:43], a[96:103], v10\n\t" \
    "v_smfmac_f32_16x16x64_bf16 v[80:83], v[40:43], a[168:175], v10\n\t" \
    "v_smfmac_f32_16x16x64_bf16 v[84:87], v[40:43], a[240:247], v10\n\t" \
    "v_smfmac_f32_16x16x64_bf16 v[72:75], v[44:47], a[64:71], v10\n\t" \
    "v_smfmac_f32_16x16x64_bf16 v[76:79], v[44:47], a[136:143], v10\n\t" \
    "v_smfmac_f32_16x16x64_bf16 v[80:83], v[44:47], a[208:215], v10\n\t" \
    "v_smfmac_f32_16x16x64_bf16 v[84:87], v[44:47], v[248:255], v10\n\t" \
    "s_branch qk_Gc254_%=\n\t" \
    "qk_k044_%=:\n\t" \
    "qk_WF65_%=:\n\t" \
    "s_cmp_ge_i32 s83, s65\n\t" \
    "s_cbranch_scc1 qk_WFok66_%=\n\t" \
    "global_load_dword v160, v24, s[36:37] sc1\n\t" \
    "s_waitcnt vmcnt(0)\n\t" \
    "v_readfirstlane_b32 s83, v160\n\t" \
    "s_nop 1\n\t" \
    "s_cmp_ge_i32 s83, s65\n\t" \
    "s_cbranch_scc1 qk_WFok66_%=\n\t" \
    "s_cmp_eq_u32 s68, 0\n\t" \
    "s_cbranch_scc1 qk_F67_%=\n\t" \
    "s_sub_u32 s68, s68, 1\n\t" \
    "s_sleep 2\n\t" \
    "s_branch qk_WF65_%=\n\t" \
    "qk_F67_%=:\n\t" \
    "v_mov_b32 v160, 6095104\n\t" \
    "v_mov_b32 v161, 8\n\t" \
    "global_store_dword v160, v161, s[36:37] sc1\n\t" \
    "s_waitcnt vmcnt(0)\n\t" \
    "s_branch qk_WFok66_%=\n\t" \
    "qk_WFok66_%=:\n\t" \
    "global_load_dwordx4 v[100:103], v23, s[50:51] offset:0 sc1\n\t" \
    "global_load_dwordx4 v[104:107], v23, s[50:51] offset:2048 sc1\n\t" \
    "global_load_dwordx4 v[108:111], v15, s[50:51] offset:0 sc1\n\t" \
    "global_load_dwordx4 v[112:115], v15, s[50:51] offset:2048 sc1\n\t" \
    "global_load_dwordx4 v[116:119], v23, s[50:51] offset:1024 sc1\n\t" \
    "global_load_dwordx4 v[120:123], v23, s[50:51] offset:3072 sc1\n\t" \
    "global_load_dwordx4 v[124:127], v15, s[50:51] offset:1024 sc1\n\t" \
    "global_load_dwordx4 v[128:131], v15, s[50:51] offset:3072 sc1\n\t" \
    "s_waitcnt vmcnt(0)\n\t" \
    "s_branch qk_k0b45_%=\n\t" \
    "qk_last41_%=:\n\t" \
    "s_endpgm\n\t" \
    "qk_loop12_%=:\n\t" \
    "v_mov_b32 v72, 0\n\t" \
    "v_mov_b32 v73, 0\n\t" \
    "v_mov_b32 v74, 0\n\t" \
    "v_mov_b32 v75, 0\n\t" \
    "v_mov_b32 v76, 0\n\t" \
    "v_mov_b32 v77, 0\n\t" \
    "v_mov_b32 v78, 0\n\t" \
    "v_mov_b32 v79, 0\n\t" \
    "v_mov_b32 v80, 0\n\t" \
    "v_mov_b32 v81, 0\n\t" \
    "v_mov_b32 v82, 0\n\t" \
    "v_mov_b32 v83, 0\n\t" \
    "v_mov_b32 v84, 0\n\t" \
    "v_mov_b32 v85, 0\n\t" \
    "v_mov_b32 v86, 0\n\t" \
    "v_mov_b32 v87, 0\n\t" \
    "v_add_u32 v31, 1, v31\n\t" \
    "s_add_u32 s69, s65, 0\n\t" \
    "s_bitcmp1_b32 s69, 0\n\t" \
    "s_cselect_b32 s70, s63, s62\n\t" \
    "s_bfe_u32 s69, s69, 0x20001\n\t" \
    "s_lshl_b32 s69, s69, 19\n\t" \
    "s_add_u32 s69, s69, s70\n\t" \
    "s_add_u32 s46, s36, s69\n\t" \
    "s_addc_u32 s47, s37, 0\n\t" \
    "s_mul_i32 s70, s67, 7\n\t" \
    "s_add_u32 s69, s65, 0\n\t" \
    "s_xor_b32 s69, s69, s70\n\t" \
    "s_and_b32 s69, s69, 63\n\t" \
    "s_lshl_b32 s69, s69, 12\n\t" \
    "s_mul_i32 s70, s67, 262144\n\t" \
    "s_add_u32 s69, s69, s70\n\t" \
    "s_add_u32 s69, s69, 39845888\n\t" \
    "s_add_u32 s40, s36, s69\n\t" \
    "s_addc_u32 s41, s37, 0\n\t" \
    "s_sub_u32 s69, s65, 1\n\t" \
    "v_mov_b32 v25, s69\n\t" \
    "qk_go73_%=:\n\t" \
    "s_sub_u32 s70, s79, s74\n\t" \
    "s_max_i32 s70, s70, 0\n\t" \
    "s_min_i32 s70, s70, 16000\n\t" \
    "s_lshl_b32 s70, s70, 1\n\t" \
    "s_mul_hi_u32 s70, s70, s77\n\t" \
    "s_add_u32 s70, s70, s73\n\t" \
    "s_sub_u32 s70, s70, 100\n\t" \
    "s_getpc_b64 s[30:31]\n\t" \
    "qk_Wp75_%=:\n\t" \
    "s_add_u32 s30, s30, qk_We76_%=-qk_Wp75_%=\n\t" \
    "s_addc_u32 s31, s31, 0\n\t" \
    "qk_W74_%=:\n\t" \
    "s_memtime s[56:57]\n\t" \
    "s_waitcnt lgkmcnt(0)\n\t" \
    "s_sub_u32 s69, s70, s56\n\t" \
    "s_cmp_gt_i32 s69, 111\n\t" \
    "s_cbranch_scc1 qk_W74_%=\n\t" \
    "s_max_i32 s69, s69, 0\n\t" \
    "s_lshr_b32 s69, s69, 1\n\t" \
    "s_and_b32 s69, s69, 0x3c\n\t" \
    "s_sub_u32 s30, s30, s69\n\t" \
    "s_subb_u32 s31, s31, 0\n\t" \
    "s_setpc_b64 s[30:31]\n\t" \
    ".p2align 6\n\t" \
    "s_nop 0\n\t" \
    "s_nop 0\n\t" \
    "s_nop 0\n\t" \
    "s_nop 1\n\t" \
    "s_nop 1\n\t" \
    "s_nop 1\n\t" \
    "s_nop 1\n\t" \
    "s_nop 1\n\t" \
    "s_nop 1\n\t" \
    "s_nop 1\n\t" \
    "s_nop 1\n\t" \
    "s_nop 1\n\t" \
    "s_nop 1\n\t" \
    "s_nop 1\n\t" \
    "s_nop 1\n\t" \
    "s_nop 1\n\t" \
    "qk_We76_%=:\n\t" \
    "qk_G77_%=:\n\t" \
    "global_load_dwordx4 v[36:39], v1, s[46:47] sc1\n\t" \
    "s_nop 9\n\t" \
    "global_load_dwordx4 v[40:43], v1, s[46:47] offset:1024 sc1\n\t" \
    "s_nop 9\n\t" \
    "global_load_dwordx4 v[44:47], v2, s[46:47] sc1\n\t" \
    "s_waitcnt vmcnt(2)\n\t" \
    "v_smfmac_f32_16x16x64_bf16 v[72:75], v[36:39], a[32:39], v10\n\t" \
    "ds_write_b128 v0, v[36:39] offset:4096\n\t" \
    "v_smfmac_f32_16x16x64_bf16 v[76:79], v[36:39], a[104:111], v10\n\t" \
    "v_max3_u32 v160, v36, v37, v38\n\t" \
    "v_smfmac_f32_16x16x64_bf16 v[80:83], v[36:39], a[176:183], v10\n\t" \
    "v_smfmac_f32_16x16x64_bf16 v[84:87], v[36:39], a[248:255], v10\n\t" \
    "s_waitcnt vmcnt(1)\n\t" \
    "ds_write_b128 v0, v[40:43] offset:5120\n\t" \
    "v_smfmac_f32_16x16x64_bf16 v[72:75], v[40:43], a[40:47], v10\n\t" \
    "v_max3_u32 v160, v160, v39, v40\n\t" \
    "v_max3_u32 v160, v160, v41, v42\n\t" \
    "v_smfmac_f32_16x16x64_bf16 v[76:79], v[40:43], a[112:119], v10\n\t" \
    "v_max_u32 v160, v160, v43\n\t" \
    "v_cmp_eq_u32 vcc, -1, v160\n\t" \
    "v_smfmac_f32_16x16x64_bf16 v[80:83], v[40:43], a[184:191], v10\n\t" \
    "v_smfmac_f32_16x16x64_bf16 v[84:87], v[40:43], v[224:231], v10\n\t" \
    "s_cbranch_vccnz qk_Gbad78_%=\n\t" \
    "qk_Gc80_%=:\n\t" \
    "s_waitcnt lgkmcnt(0)\n\t" \
    "s_barrier\n\t" \
    "ds_read_b128 v[48:51], v0 offset:6144\n\t" \
    "ds_read_b128 v[52:55], v0 offset:7168\n\t" \
    "s_waitcnt vmcnt(0)\n\t" \
    "v_smfmac_f32_16x16x64_bf16 v[72:75], v[44:47], a[64:71], v10\n\t" \
    "ds_read_b128 v[56:59], v0 offset:0\n\t" \
    "v_max3_u32 v160, v44, v45, v46\n\t" \
    "v_smfmac_f32_16x16x64_bf16 v[76:79], v[44:47], a[136:143], v10\n\t" \
    "ds_read_b128 v[60:63], v0 offset:1024\n\t" \
    "v_max_u32 v160, v160, v47\n\t" \
    "v_smfmac_f32_16x16x64_bf16 v[80:83], v[44:47], a[208:215], v10\n\t" \
    "ds_read_b128 v[64:67], v0 offset:2048\n\t" \
    "v_cmp_eq_u32 vcc, -1, v160\n\t" \
    "v_smfmac_f32_16x16x64_bf16 v[84:87], v[44:47], v[248:255], v10\n\t" \
    "ds_read_b128 v[68:71], v0 offset:3072\n\t" \
    "s_cbranch_vccnz qk_Gbad279_%=\n\t" \
    "qk_Gc281_%=:\n\t" \
    "s_waitcnt lgkmcnt(5)\n\t" \
    "v_smfmac_f32_16x16x64_bf16 v[72:75], v[48:51], a[48:55], v10\n\t" \
    "global_store_dword v14, v25, s[36:37] sc1\n\t" \
    "s_waitcnt lgkmcnt(4)\n\t" \
    "v_smfmac_f32_16x16x64_bf16 v[72:75], v[52:55], a[56:63], v10\n\t" \
    "s_waitcnt lgkmcnt(3)\n\t" \
    "v_smfmac_f32_16x16x64_bf16 v[72:75], v[56:59], a[0:7], v10\n\t" \
    "s_waitcnt lgkmcnt(2)\n\t" \
    "v_smfmac_f32_16x16x64_bf16 v[72:75], v[60:63], a[8:15], v10\n\t" \
    "s_waitcnt lgkmcnt(1)\n\t" \
    "v_smfmac_f32_16x16x64_bf16 v[72:75], v[64:67], a[16:23], v10\n\t" \
    "s_waitcnt lgkmcnt(0)\n\t" \
    "v_smfmac_f32_16x16x64_bf16 v[72:75], v[68:71], a[24:31], v10\n\t" \
    "v_smfmac_f32_16x16x64_bf16 v[76:79], v[48:51], a[120:127], v10\n\t" \
    "s_nop 1\n\t" \
    "s_nop 0\n\t" \
    "v_smfmac_f32_16x16x64_bf16 v[76:79], v[52:55], a[128:135], v10\n\t" \
    "s_nop 1\n\t" \
    "s_nop 0\n\t" \
    "v_smfmac_f32_16x16x64_bf16 v[76:79], v[56:59], a[72:79], v10\n\t" \
    "v_add_f32 v72, v72, v74\n\t" \
    "v_smfmac_f32_16x16x64_bf16 v[76:79], v[60:63], a[80:87], v10\n\t" \
    "v_add_f32 v73, v73, v75\n\t" \
    "v_smfmac_f32_16x16x64_bf16 v[76:79], v[64:67], a[88:95], v10\n\t" \
    "v_cvt_pk_bf16_f32 v156, v72, v73\n\t" \
    "v_smfmac_f32_16x16x64_bf16 v[76:79], v[68:71], a[96:103], v10\n\t" \
    "ds_write_b32 v12, v156 offset:0\n\t" \
    "v_smfmac_f32_16x16x64_bf16 v[80:83], v[48:51], a[192:199], v10\n\t" \
    "s_nop 1\n\t" \
    "s_nop 0\n\t" \
    "v_smfmac_f32_16x16x64_bf16 v[80:83], v[52:55], a[200:207], v10\n\t" \
    "s_nop 1\n\t" \
    "s_nop 0\n\t" \
    "v_smfmac_f32_16x16x64_bf16 v[80:83], v[56:59], a[144:151], v10\n\t" \
    "v_add_f32 v76, v76, v78\n\t" \
    "v_smfmac_f32_16x16x64_bf16 v[80:83], v[60:63], a[152:159], v10\n\t" \
    "v_add_f32 v77, v77, v79\n\t" \
    "v_smfmac_f32_16x16x64_bf16 v[80:83], v[64:67], a[160:167], v10\n\t" \
    "v_cvt_pk_bf16_f32 v157, v76, v77\n\t" \
    "v_smfmac_f32_16x16x64_bf16 v[80:83], v[68:71], a[168:175], v10\n\t" \
    "ds_write_b32 v12, v157 offset:64\n\t" \
    "v_smfmac_f32_16x16x64_bf16 v[84:87], v[48:51], v[232:239], v10\n\t" \
    "s_nop 1\n\t" \
    "s_nop 0\n\t" \
    "v_smfmac_f32_16x16x64_bf16 v[84:87], v[52:55], v[240:247], v10\n\t" \
    "s_nop 1\n\t" \
    "s_nop 0\n\t" \
    "v_smfmac_f32_16x16x64_bf16 v[84:87], v[56:59], a[216:223], v10\n\t" \
    "v_add_f32 v80, v80, v82\n\t" \
    "v_smfmac_f32_16x16x64_bf16 v[84:87], v[60:63], a[224:231], v10\n\t" \
    "v_add_f32 v81, v81, v83\n\t" \
    "v_smfmac_f32_16x16x64_bf16 v[84:87], v[64:67], a[232:239], v10\n\t" \
    "v_cvt_pk_bf16_f32 v158, v80, v81\n\t" \
    "v_smfmac_f32_16x16x64_bf16 v[84:87], v[68:71], a[240:247], v10\n\t" \
    "ds_write_b32 v16, v158 offset:0\n\t" \
    "s_nop 6\n\t" \
    "v_pk_add_f32 v[84:85], v[84:85], v[86:87]\n\t" \
    "v_cvt_pk_bf16_f32 v159, v84, v85\n\t" \
    "ds_write_b32 v16, v159 offset:256\n\t" \
    "s_waitcnt lgkmcnt(0)\n\t" \
    "s_barrier\n\t" \
    "s_sleep 1\n\t" \
    "ds_read_b64_tr_b16 v[140:141], v17 offset:0\n\t" \
    "ds_read_b64_tr_b16 v[142:143], v17 offset:256\n\t" \
    "ds_read_b64_tr_b16 v[144:145], v17 offset:512\n\t" \
    "ds_read_b64_tr_b16 v[146:147], v17 offset:768\n\t" \
    "ds_read_b64_tr_b16 v[148:149], v17 offset:1024\n\t" \
    "ds_read_b64_tr_b16 v[150:151], v17 offset:1280\n\t" \
    "ds_read_b64_tr_b16 v[152:153], v17 offset:1536\n\t" \
    "ds_read_b64_tr_b16 v[154:155], v17 offset:1792\n\t" \
    "s_waitcnt lgkmcnt(0)\n\t" \
    "s_cmp_eq_u32 s65, 63\n\t" \
    "s_cbranch_scc1 qk_last68_%=\n\t" \
    "s_mov_b32 s71, 3\n\t" \
    "qk_E83_%=:\n\t" \
    "s_memtime s[54:55]\n\t" \
    "s_nop 0\n\t" \
    "s_memrealtime s[4:5]\n\t" \
    "s_nop 0\n\t" \
    "s_memrealtime s[6:7]\n\t" \
    "s_nop 0\n\t" \
    "s_memrealtime s[8:9]\n\t" \
    "s_nop 0\n\t" \
    "s_memrealtime s[10:11]\n\t" \
    "s_nop 0\n\t" \
    "s_memrealtime s[12:13]\n\t" \
    "s_nop 0\n\t" \
    "s_memrealtime s[14:15]\n\t" \
    "s_nop 0\n\t" \
    "s_memrealtime s[16:17]\n\t" \
    "s_nop 0\n\t" \
    "s_memrealtime s[18:19]\n\t" \
    "s_nop 0\n\t" \
    "s_memrealtime s[20:21]\n\t" \
    "s_nop 0\n\t" \
    "s_memrealtime s[22:23]\n\t" \
    "s_nop 0\n\t" \
    "s_memrealtime s[24:25]\n\t" \
    "s_nop 0\n\t" \
    "s_memrealtime s[26:27]\n\t" \
    "s_nop 0\n\t" \
    "s_memrealtime s[28:29]\n\t" \
    "s_nop 0\n\t" \
    "s_memtime s[56:57]\n\t" \
    "s_mov_b32 s70, 1\n\t" \
    "s_sub_u32 s71, s71, 1\n\t" \
    "s_cselect_b32 s69, 0, -1\n\t" \
    "s_waitcnt lgkmcnt(0)\n\t" \
    "s_sub_u32 s54, s56, s54\n\t" \
    "s_and_b32 s54, s54, s69\n\t" \
    "s_cmp_gt_u32 s54, 124\n\t" \
    "s_cbranch_scc1 qk_E83_%=\n\t" \
    "s_cmp_eq_u32 s4, s28\n\t" \
    "s_addc_u32 s70, s70, 0\n\t" \
    "s_cmp_eq_u32 s6, s28\n\t" \
    "s_addc_u32 s70, s70, 0\n\t" \
    "s_cmp_eq_u32 s8, s28\n\t" \
    "s_addc_u32 s70, s70, 0\n\t" \
    "s_cmp_eq_u32 s10, s28\n\t" \
    "s_addc_u32 s70, s70, 0\n\t" \
    "s_cmp_eq_u32 s12, s28\n\t" \
    "s_addc_u32 s70, s70, 0\n\t" \
    "s_cmp_eq_u32 s14, s28\n\t" \
    "s_addc_u32 s70, s70, 0\n\t" \
    "s_cmp_eq_u32 s16, s28\n\t" \
    "s_addc_u32 s70, s70, 0\n\t" \
    "s_cmp_eq_u32 s18, s28\n\t" \
    "s_addc_u32 s70, s70, 0\n\t" \
    "s_cmp_eq_u32 s20, s28\n\t" \
    "s_addc_u32 s70, s70, 0\n\t" \
    "s_cmp_eq_u32 s22, s28\n\t" \
    "s_addc_u32 s70, s70, 0\n\t" \
    "s_cmp_eq_u32 s24, s28\n\t" \
    "s_addc_u32 s70, s70, 0\n\t" \
    "s_cmp_eq_u32 s26, s28\n\t" \
    "s_addc_u32 s70, s70, 0\n\t" \
    "s_mov_b32 s75, s73\n\t" \
    "s_mov_b32 s76, s74\n\t" \
    "s_lshl_b32 s70, s70, 3\n\t" \
    "s_sub_u32 s73, s56, s70\n\t" \
    "s_lshl_b32 s74, s28, 4\n\t" \
    "s_sub_u32 s69, s73, s75\n\t" \
    "s_sub_u32 s70, s74, s76\n\t" \
    "s_sub_u32 s71, s70, 1600\n\t" \
    "s_cmp_lt_u32 s71, 400000\n\t" \
    "s_cbranch_scc0 qk_K85_%=\n\t" \
    "v_cvt_f32_u32 v160, s69\n\t" \
    "v_cvt_f32_u32 v161, s70\n\t" \
    "v_rcp_f32 v162, v160\n\t" \
    "v_rcp_f32 v163, v161\n\t" \
    "v_mul_f32 v162, v162, v161\n\t" \
    "v_mul_f32 v163, v163, v160\n\t" \
    "v_mul_f32 v162, 0x4f800000, v162\n\t" \
    "v_mul_f32 v163, 0x4f000000, v163\n\t" \
    "v_cvt_u32_f32 v162, v162\n\t" \
    "v_cvt_u32_f32 v163, v163\n\t" \
    "v_readfirstlane_b32 s69, v162\n\t" \
    "v_readfirstlane_b32 s70, v163\n\t" \
    "s_nop 1\n\t" \
    "s_sub_u32 s69, s69, s78\n\t" \
    "s_ashr_i32 s69, s69, 2\n\t" \
    "s_add_u32 s78, s78, s69\n\t" \
    "s_sub_u32 s70, s70, s77\n\t" \
    "s_ashr_i32 s70, s70, 2\n\t" \
    "s_add_u32 s77, s77, s70\n\t" \
    "qk_K85_%=:\n\t" \
    "v_mov_b32 v72, 0\n\t" \
    "v_mov_b32 v73, 0\n\t" \
    "v_mov_b32 v74, 0\n\t" \
    "v_mov_b32 v75, 0\n\t" \
    "v_mov_b32 v76, 0\n\t" \
    "v_mov_b32 v77, 0\n\t" \
    "v_mov_b32 v78, 0\n\t" \
    "v_mov_b32 v79, 0\n\t" \
    "v_mov_b32 v80, 0\n\t" \
    "v_mov_b32 v81, 0\n\t" \
    "v_mov_b32 v82, 0\n\t" \
    "v_mov_b32 v83, 0\n\t" \
    "v_mov_b32 v84, 0\n\t" \
    "v_mov_b32 v85, 0\n\t" \
    "v_mov_b32 v86, 0\n\t" \
    "v_mov_b32 v87, 0\n\t" \
    "s_add_u32 s79, s74, 560\n\t" \
    "s_cmp_ge_u32 s65, 58\n\t" \
    "s_cselect_b32 s79, s74, s79\n\t" \
    "s_sub_u32 s70, s79, s74\n\t" \
    "s_max_i32 s70, s70, 0\n\t" \
    "s_min_i32 s70, s70, 16000\n\t" \
    "s_lshl_b32 s70, s70, 1\n\t" \
    "s_mul_hi_u32 s70, s70, s77\n\t" \
    "s_add_u32 s70, s70, s73\n\t" \
    "qk_W86_%=:\n\t" \
    "s_memtime s[56:57]\n\t" \
    "s_waitcnt lgkmcnt(0)\n\t" \
    "s_sub_u32 s69, s70, s56\n\t" \
    "s_cmp_gt_i32 s69, 24\n\t" \
    "s_cbranch_scc1 qk_W86_%=\n\t" \
    "s_mov_b64 exec, s[58:59]\n\t" \
    "global_store_dwordx4 v0, v[140:143], s[40:41] offset:0 sc1\n\t" \
    "global_store_dwordx4 v0, v[144:147], s[40:41] offset:1024 sc1\n\t" \
    "global_store_dwordx4 v0, v[148:151], s[40:41] offset:2048 sc1\n\t" \
    "global_store_dwordx4 v0, v[152:155], s[40:41] offset:3072 sc1\n\t" \
    "s_mov_b64 exec, -1\n\t" \
    "s_add_u32 s65, s65, 1\n\t" \
    "v_add_u32 v31, 1, v31\n\t" \
    "s_add_u32 s69, s65, 0\n\t" \
    "s_bitcmp1_b32 s69, 0\n\t" \
    "s_cselect_b32 s70, s63, s62\n\t" \
    "s_bfe_u32 s69, s69, 0x20001\n\t" \
    "s_lshl_b32 s69, s69, 19\n\t" \
    "s_add_u32 s69, s69, s70\n\t" \
    "s_add_u32 s46, s36, s69\n\t" \
    "s_addc_u32 s47, s37, 0\n\t" \
    "s_mul_i32 s70, s67, 7\n\t" \
    "s_add_u32 s69, s65, 0\n\t" \
    "s_xor_b32 s69, s69, s70\n\t" \
    "s_and_b32 s69, s69, 63\n\t" \
    "s_lshl_b32 s69, s69, 12\n\t" \
    "s_mul_i32 s70, s67, 262144\n\t" \
    "s_add_u32 s69, s69, s70\n\t" \
    "s_add_u32 s69, s69, 39845888\n\t" \
    "s_add_u32 s40, s36, s69\n\t" \
    "s_addc_u32 s41, s37, 0\n\t" \
    "s_sub_u32 s69, s65, 1\n\t" \
    "v_mov_b32 v25, s69\n\t" \
    "s_waitcnt lgkmcnt(0)\n\t" \
    "s_barrier\n\t" \
    "ds_read_b32 v160, v11 offset:20480\n\t" \
    "s_waitcnt lgkmcnt(0)\n\t" \
    "v_readfirstlane_b32 s81, v160\n\t" \
    "s_nop 1\n\t" \
    "s_mul_hi_u32 s69, s85, s78\n\t" \
    "s_add_u32 s79, s81, s69\n\t" \
    "s_add_u32 s79, s79, s87\n\t" \
    "s_cmp_ge_u32 s65, 37\n\t" \
    "s_cselect_b32 s69, 28, 0\n\t" \
    "s_sub_u32 s79, s79, s69\n\t" \
    "s_cmp_lt_u32 s65, 9\n\t" \
    "s_cselect_b32 s69, 96, 0\n\t" \
    "s_add_u32 s79, s79, s69\n\t" \
    "s_add_u32 s80, s79, s88\n\t" \
    "s_branch qk_go73_%=\n\t" \
    "qk_Gbad78_%=:\n\t" \
    "s_waitcnt vmcnt(0) lgkmcnt(0)\n\t" \
    "s_nop 7\n\t" \
    "v_mov_b32 v72, 0\n\t" \
    "v_mov_b32 v73, 0\n\t" \
    "v_mov_b32 v74, 0\n\t" \
    "v_mov_b32 v75, 0\n\t" \
    "v_mov_b32 v76, 0\n\t" \
    "v_mov_b32 v77, 0\n\t" \
    "v_mov_b32 v78, 0\n\t" \
    "v_mov_b32 v79, 0\n\t" \
    "v_mov_b32 v80, 0\n\t" \
    "v_mov_b32 v81, 0\n\t" \
    "v_mov_b32 v82, 0\n\t" \
    "v_mov_b32 v83, 0\n\t" \
    "v_mov_b32 v84, 0\n\t" \
    "v_mov_b32 v85, 0\n\t" \
    "v_mov_b32 v86, 0\n\t" \
    "v_mov_b32 v87, 0\n\t" \
    "s_cmp_eq_u32 s68, 0\n\t" \
    "s_cbranch_scc1 qk_F87_%=\n\t" \
    "s_sub_u32 s68, s68, 1\n\t" \
    "s_branch qk_G77_%=\n\t" \
    "qk_F87_%=:\n\t" \
    "v_mov_b32 v160, 6095104\n\t" \
    "v_mov_b32 v161, 1\n\t" \
    "global_store_dword v160, v161, s[36:37] sc1\n\t" \
    "s_waitcnt vmcnt(0)\n\t" \
    "s_branch qk_Gc80_%=\n\t" \
    "qk_Gbad279_%=:\n\t" \
    "global_load_dwordx4 v[44:47], v2, s[46:47] sc1\n\t" \
    "s_waitcnt vmcnt(0)\n\t" \
    "v_max3_u32 v160, v44, v45, v46\n\t" \
    "v_max_u32 v160, v160, v47\n\t" \
    "v_cmp_eq_u32 vcc, -1, v160\n\t" \
    "s_cbranch_vccz qk_Gre282_%=\n\t" \
    "s_cmp_eq_u32 s68, 0\n\t" \
    "s_cbranch_scc1 qk_F88_%=\n\t" \
    "s_sub_u32 s68, s68, 1\n\t" \
    "s_sleep 1\n\t" \
    "s_branch qk_Gbad279_%=\n\t" \
    "qk_F88_%=:\n\t" \
    "v_mov_b32 v160, 6095104\n\t" \
    "v_mov_b32 v161, 2\n\t" \
    "global_store_dword v160, v161, s[36:37] sc1\n\t" \
    "s_waitcnt vmcnt(0)\n\t" \
    "s_branch qk_Gre282_%=\n\t" \
    "qk_Gre282_%=:\n\t" \
    "v_mov_b32 v72, 0\n\t" \
    "v_mov_b32 v73, 0\n\t" \
    "v_mov_b32 v74, 0\n\t" \
    "v_mov_b32 v75, 0\n\t" \
    "v_mov_b32 v76, 0\n\t" \
    "v_mov_b32 v77, 0\n\t" \
    "v_mov_b32 v78, 0\n\t" \
    "v_mov_b32 v79, 0\n\t" \
    "v_mov_b32 v80, 0\n\t" \
    "v_mov_b32 v81, 0\n\t" \
    "v_mov_b32 v82, 0\n\t" \
    "v_mov_b32 v83, 0\n\t" \
    "v_mov_b32 v84, 0\n\t" \
    "v_mov_b32 v85, 0\n\t" \
    "v_mov_b32 v86, 0\n\t" \
    "v_mov_b32 v87, 0\n\t" \
    "v_smfmac_f32_16x16x64_bf16 v[72:75], v[36:39], a[32:39], v10\n\t" \
    "v_smfmac_f32_16x16x64_bf16 v[76:79], v[36:39], a[104:111], v10\n\t" \
    "v_smfmac_f32_16x16x64_bf16 v[80:83], v[36:39], a[176:183], v10\n\t" \
    "v_smfmac_f32_16x16x64_bf16 v[84:87], v[36:39], a[248:255], v10\n\t" \
    "v_smfmac_f32_16x16x64_bf16 v[72:75], v[40:43], a[40:47], v10\n\t" \
    "v_smfmac_f32_16x16x64_bf16 v[76:79], v[40:43], a[112:119], v10\n\t" \
    "v_smfmac_f32_16x16x64_bf16 v[80:83], v[40:43], a[184:191], v10\n\t" \
    "v_smfmac_f32_16x16x64_bf16 v[84:87], v[40:43], v[224:231], v10\n\t" \
    "v_smfmac_f32_16x16x64_bf16 v[72:75], v[44:47], a[64:71], v10\n\t" \
    "v_smfmac_f32_16x16x64_bf16 v[76:79], v[44:47], a[136:143], v10\n\t" \
    "v_smfmac_f32_16x16x64_bf16 v[80:83], v[44:47], a[208:215], v10\n\t" \
    "v_smfmac_f32_16x16x64_bf16 v[84:87], v[44:47], v[248:255], v10\n\t" \
    "s_branch qk_Gc281_%=\n\t" \
    "qk_last68_%=:\n\t" \
    "s_mov_b64 exec, s[58:59]\n\t" \
    "global_store_dwordx4 v0, v[140:143], s[40:41] offset:0 sc1\n\t" \
    "global_store_dwordx4 v0, v[144:147], s[40:41] offset:1024 sc1\n\t" \
    "global_store_dwordx4 v0, v[148:151], s[40:41] offset:2048 sc1\n\t" \
    "global_store_dwordx4 v0, v[152:155], s[40:41] offset:3072 sc1\n\t" \
    "s_mov_b64 exec, -1\n\t" \
    "s_endpgm\n\t" \
    "qk_loop13_%=:\n\t" \
    "v_mov_b32 v72, 0\n\t" \
    "v_mov_b32 v73, 0\n\t" \
    "v_mov_b32 v74, 0\n\t" \
    "v_mov_b32 v75, 0\n\t" \
    "v_mov_b32 v76, 0\n\t" \
    "v_mov_b32 v77, 0\n\t" \
    "v_mov_b32 v78, 0\n\t" \
    "v_mov_b32 v79, 0\n\t" \
    "v_mov_b32 v80, 0\n\t" \
    "v_mov_b32 v81, 0\n\t" \
    "v_mov_b32 v82, 0\n\t" \
    "v_mov_b32 v83, 0\n\t" \
    "v_mov_b32 v84, 0\n\t" \
    "v_mov_b32 v85, 0\n\t" \
    "v_mov_b32 v86, 0\n\t" \
    "v_mov_b32 v87, 0\n\t" \
    "v_add_u32 v31, 1, v31\n\t" \
    "s_add_u32 s69, s65, 0\n\t" \
    "s_bitcmp1_b32 s69, 0\n\t" \
    "s_cselect_b32 s70, s63, s62\n\t" \
    "s_bfe_u32 s69, s69, 0x20001\n\t" \
    "s_lshl_b32 s69, s69, 19\n\t" \
    "s_add_u32 s69, s69, s70\n\t" \
    "s_add_u32 s46, s36, s69\n\t" \
    "s_addc_u32 s47, s37, 0\n\t" \
    "qk_go94_%=:\n\t" \
    "s_sub_u32 s70, s79, s74\n\t" \
    "s_max_i32 s70, s70, 0\n\t" \
    "s_min_i32 s70, s70, 16000\n\t" \
    "s_lshl_b32 s70, s70, 1\n\t" \
    "s_mul_hi_u32 s70, s70, s77\n\t" \
    "s_add_u32 s70, s70, s73\n\t" \
    "s_sub_u32 s70, s70, 100\n\t" \
    "s_getpc_b64 s[30:31]\n\t" \
    "qk_Wp96_%=:\n\t" \
    "s_add_u32 s30, s30, qk_We97_%=-qk_Wp96_%=\n\t" \
    "s_addc_u32 s31, s31, 0\n\t" \
    "qk_W95_%=:\n\t" \
    "s_memtime s[56:57]\n\t" \
    "s_waitcnt lgkmcnt(0)\n\t" \
    "s_sub_u32 s69, s70, s56\n\t" \
    "s_cmp_gt_i32 s69, 111\n\t" \
    "s_cbranch_scc1 qk_W95_%=\n\t" \
    "s_max_i32 s69, s69, 0\n\t" \
    "s_lshr_b32 s69, s69, 1\n\t" \
    "s_and_b32 s69, s69, 0x3c\n\t" \
    "s_sub_u32 s30, s30, s69\n\t" \
    "s_subb_u32 s31, s31, 0\n\t" \
    "s_setpc_b64 s[30:31]\n\t" \
    ".p2align 6\n\t" \
    "s_nop 0\n\t" \
    "s_nop 0\n\t" \
    "s_nop 0\n\t" \
    "s_nop 1\n\t" \
    "s_nop 1\n\t" \
    "s_nop 1\n\t" \
    "s_nop 1\n\t" \
    "s_nop 1\n\t" \
    "s_nop 1\n\t" \
    "s_nop 1\n\t" \
    "s_nop 1\n\t" \
    "s_nop 1\n\t" \
    "s_nop 1\n\t" \
    "s_nop 1\n\t" \
    "s_nop 1\n\t" \
    "s_nop 1\n\t" \
    "qk_We97_%=:\n\t" \
    "qk_G98_%=:\n\t" \
    "global_load_dwordx4 v[36:39], v1, s[46:47] sc1\n\t" \
    "s_nop 9\n\t" \
    "global_load_dwordx4 v[40:43], v1, s[46:47] offset:1024 sc1\n\t" \
    "s_nop 9\n\t" \
    "global_load_dwordx4 v[44:47], v2, s[46:47] sc1\n\t" \
    "s_mul_hi_u32 s69, s84, s78\n\t" \
    "s_add_u32 s33, s74, s69\n\t" \
    "s_lshr_b32 s69, s90, 16\n\t" \
    "s_add_u32 s34, s33, s69\n\t" \
    "s_and_b32 s69, s90, 0xffff\n\t" \
    "s_sub_u32 s35, s80, s74\n\t" \
    "s_sub_u32 s35, s35, s69\n\t" \
    "s_add_u32 s99, s35, 1280\n\t" \
    "s_add_u32 s98, s34, 600\n\t" \
    "s_waitcnt vmcnt(2)\n\t" \
    "v_smfmac_f32_16x16x64_bf16 v[72:75], v[36:39], a[48:55], v10\n\t" \
    "ds_write_b128 v0, v[36:39] offset:6144\n\t" \
    "v_smfmac_f32_16x16x64_bf16 v[76:79], v[36:39], a[120:127], v10\n\t" \
    "v_max3_u32 v160, v36, v37, v38\n\t" \
    "v_smfmac_f32_16x16x64_bf16 v[80:83], v[36:39], a[192:199], v10\n\t" \
    "v_smfmac_f32_16x16x64_bf16 v[84:87], v[36:39], v[232:239], v10\n\t" \
    "s_waitcnt vmcnt(1)\n\t" \
    "ds_write_b128 v0, v[40:43] offset:7168\n\t" \
    "v_smfmac_f32_16x16x64_bf16 v[72:75], v[40:43], a[56:63], v10\n\t" \
    "v_max3_u32 v160, v160, v39, v40\n\t" \
    "v_max3_u32 v160, v160, v41, v42\n\t" \
    "v_smfmac_f32_16x16x64_bf16 v[76:79], v[40:43], a[128:135], v10\n\t" \
    "v_max_u32 v160, v160, v43\n\t" \
    "v_cmp_eq_u32 vcc, -1, v160\n\t" \
    "v_smfmac_f32_16x16x64_bf16 v[80:83], v[40:43], a[200:207], v10\n\t" \
    "v_smfmac_f32_16x16x64_bf16 v[84:87], v[40:43], v[240:247], v10\n\t" \
    "s_cbranch_vccnz qk_Gbad99_%=\n\t" \
    "qk_Gc101_%=:\n\t" \
    "s_waitcnt lgkmcnt(0)\n\t" \
    "s_barrier\n\t" \
    "s_memtime s[56:57]\n\t" \
    "ds_read_b128 v[48:51], v0 offset:0\n\t" \
    "ds_read_b128 v[52:55], v0 offset:1024\n\t" \
    "s_waitcnt vmcnt(0)\n\t" \
    "v_smfmac_f32_16x16x64_bf16 v[72:75], v[44:47], a[64:71], v10\n\t" \
    "ds_read_b128 v[56:59], v0 offset:2048\n\t" \
    "v_max3_u32 v160, v44, v45, v46\n\t" \
    "v_smfmac_f32_16x16x64_bf16 v[76:79], v[44:47], a[136:143], v10\n\t" \
    "ds_read_b128 v[60:63], v0 offset:3072\n\t" \
    "v_max_u32 v160, v160, v47\n\t" \
    "v_smfmac_f32_16x16x64_bf16 v[80:83], v[44:47], a[208:215], v10\n\t" \
    "ds_read_b128 v[64:67], v0 offset:4096\n\t" \
    "v_cmp_eq_u32 vcc, -1, v160\n\t" \
    "v_smfmac_f32_16x16x64_bf16 v[84:87], v[44:47], v[248:255], v10\n\t" \
    "ds_read_b128 v[68:71], v0 offset:5120\n\t" \
    "s_cbranch_vccnz qk_Gbad2100_%=\n\t" \
    "qk_Gc2102_%=:\n\t" \
    "s_sub_u32 s69, s56, s73\n\t" \
    "s_mul_hi_u32 s69, s69, s78\n\t" \
    "s_cmp_gt_i32 s69, s35\n\t" \
    "s_waitcnt lgkmcnt(5)\n\t" \
    "v_smfmac_f32_16x16x64_bf16 v[72:75], v[48:51], a[0:7], v10\n\t" \
    "s_cselect_b32 s70, s34, s33\n\t" \
    "s_cmp_gt_i32 s69, s99\n\t" \
    "s_cselect_b32 s70, s98, s70\n\t" \
    "s_waitcnt lgkmcnt(4)\n\t" \
    "v_smfmac_f32_16x16x64_bf16 v[72:75], v[52:55], a[8:15], v10\n\t" \
    "s_add_u32 s70, s69, s70\n\t" \
    "v_and_b32_e64 v162, -2, s70\n\t" \
    "s_waitcnt lgkmcnt(3)\n\t" \
    "v_smfmac_f32_16x16x64_bf16 v[72:75], v[56:59], a[16:23], v10\n\t" \
    "s_mov_b64 exec, s[44:45]\n\t" \
    "global_store_dword v21, v162, s[46:47] sc1\n\t" \
    "s_mov_b64 exec, -1\n\t" \
    "s_waitcnt lgkmcnt(2)\n\t" \
    "v_smfmac_f32_16x16x64_bf16 v[72:75], v[60:63], a[24:31], v10\n\t" \
    "s_waitcnt lgkmcnt(1)\n\t" \
    "v_smfmac_f32_16x16x64_bf16 v[72:75], v[64:67], a[32:39], v10\n\t" \
    "s_waitcnt lgkmcnt(0)\n\t" \
    "v_smfmac_f32_16x16x64_bf16 v[72:75], v[68:71], a[40:47], v10\n\t" \
    "v_smfmac_f32_16x16x64_bf16 v[76:79], v[48:51], a[72:79], v10\n\t" \
    "s_nop 1\n\t" \
    "s_nop 0\n\t" \
    "v_smfmac_f32_16x16x64_bf16 v[76:79], v[52:55], a[80:87], v10\n\t" \
    "s_nop 1\n\t" \
    "s_nop 0\n\t" \
    "v_smfmac_f32_16x16x64_bf16 v[76:79], v[56:59], a[88:95], v10\n\t" \
    "v_add_f32 v72, v72, v74\n\t" \
    "v_smfmac_f32_16x16x64_bf16 v[76:79], v[60:63], a[96:103], v10\n\t" \
    "v_add_f32 v73, v73, v75\n\t" \
    "v_smfmac_f32_16x16x64_bf16 v[76:79], v[64:67], a[104:111], v10\n\t" \
    "v_cvt_pk_bf16_f32 v156, v72, v73\n\t" \
    "v_smfmac_f32_16x16x64_bf16 v[76:79], v[68:71], a[112:119], v10\n\t" \
    "ds_write_b32 v12, v156 offset:0\n\t" \
    "v_smfmac_f32_16x16x64_bf16 v[80:83], v[48:51], a[144:151], v10\n\t" \
    "s_nop 1\n\t" \
    "s_nop 0\n\t" \
    "v_smfmac_f32_16x16x64_bf16 v[80:83], v[52:55], a[152:159], v10\n\t" \
    "s_nop 1\n\t" \
    "s_nop 0\n\t" \
    "v_smfmac_f32_16x16x64_bf16 v[80:83], v[56:59], a[160:167], v10\n\t" \
    "v_add_f32 v76, v76, v78\n\t" \
    "v_smfmac_f32_16x16x64_bf16 v[80:83], v[60:63], a[168:175], v10\n\t" \
    "v_add_f32 v77, v77, v79\n\t" \
    "v_smfmac_f32_16x16x64_bf16 v[80:83], v[64:67], a[176:183], v10\n\t" \
    "v_cvt_pk_bf16_f32 v157, v76, v77\n\t" \
    "v_smfmac_f32_16x16x64_bf16 v[80:83], v[68:71], a[184:191], v10\n\t" \
    "ds_write_b32 v12, v157 offset:64\n\t" \
    "v_smfmac_f32_16x16x64_bf16 v[84:87], v[48:51], a[216:223], v10\n\t" \
    "s_nop 1\n\t" \
    "s_nop 0\n\t" \
    "v_smfmac_f32_16x16x64_bf16 v[84:87], v[52:55], a[224:231], v10\n\t" \
    "s_nop 1\n\t" \
    "s_nop 0\n\t" \
    "v_smfmac_f32_16x16x64_bf16 v[84:87], v[56:59], a[232:239], v10\n\t" \
    "v_add_f32 v80, v80, v82\n\t" \
    "v_smfmac_f32_16x16x64_bf16 v[84:87], v[60:63], a[240:247], v10\n\t" \
    "v_add_f32 v81, v81, v83\n\t" \
    "v_smfmac_f32_16x16x64_bf16 v[84:87], v[64:67], a[248:255], v10\n\t" \
    "v_cvt_pk_bf16_f32 v158, v80, v81\n\t" \
    "v_smfmac_f32_16x16x64_bf16 v[84:87], v[68:71], v[224:231], v10\n\t" \
    "ds_write_b32 v16, v158 offset:0\n\t" \
    "s_nop 6\n\t" \
    "v_pk_add_f32 v[84:85], v[84:85], v[86:87]\n\t" \
    "v_cvt_pk_bf16_f32 v159, v84, v85\n\t" \
    "ds_write_b32 v16, v159 offset:256\n\t" \
    "s_waitcnt lgkmcnt(0)\n\t" \
    "s_barrier\n\t" \
    "s_cmp_eq_u32 s65, 63\n\t" \
    "s_cbranch_scc1 qk_last89_%=\n\t" \
    "s_mov_b64 s[44:45], 0xfffff\n\t" \
    "s_sub_u32 s70, s80, s74\n\t" \
    "s_max_i32 s70, s70, 0\n\t" \
    "s_min_i32 s70, s70, 16000\n\t" \
    "s_lshl_b32 s70, s70, 1\n\t" \
    "s_mul_hi_u32 s70, s70, s77\n\t" \
    "s_add_u32 s70, s70, s73\n\t" \
    "qk_W104_%=:\n\t" \
    "s_memtime s[56:57]\n\t" \
    "s_waitcnt lgkmcnt(0)\n\t" \
    "s_sub_u32 s69, s70, s56\n\t" \
    "s_cmp_gt_i32 s69, 24\n\t" \
    "s_cbranch_scc1 qk_W104_%=\n\t" \
    "global_load_dword v160, v20, s[46:47] sc1\n\t" \
    "s_mov_b32 s71, 3\n\t" \
    "qk_E108_%=:\n\t" \
    "s_memtime s[54:55]\n\t" \
    "s_nop 0\n\t" \
    "s_memrealtime s[4:5]\n\t" \
    "s_nop 0\n\t" \
    "s_memrealtime s[6:7]\n\t" \
    "s_nop 0\n\t" \
    "s_memrealtime s[8:9]\n\t" \
    "s_nop 0\n\t" \
    "s_memrealtime s[10:11]\n\t" \
    "s_nop 0\n\t" \
    "s_memrealtime s[12:13]\n\t" \
    "s_nop 0\n\t" \
    "s_memrealtime s[14:15]\n\t" \
    "s_nop 0\n\t" \
    "s_memrealtime s[16:17]\n\t" \
    "s_nop 0\n\t" \
    "s_memrealtime s[18:19]\n\t" \
    "s_nop 0\n\t" \
    "s_memrealtime s[20:21]\n\t" \
    "s_nop 0\n\t" \
    "s_memrealtime s[22:23]\n\t" \
    "s_nop 0\n\t" \
    "s_memrealtime s[24:25]\n\t" \
    "s_nop 0\n\t" \
    "s_memrealtime s[26:27]\n\t" \
    "s_nop 0\n\t" \
    "s_memrealtime s[28:29]\n\t" \
    "s_nop 0\n\t" \
    "s_memtime s[56:57]\n\t" \
    "s_mov_b32 s70, 1\n\t" \
    "s_sub_u32 s71, s71, 1\n\t" \
    "s_cselect_b32 s69, 0, -1\n\t" \
    "s_waitcnt lgkmcnt(0)\n\t" \
    "s_sub_u32 s54, s56, s54\n\t" \
    "s_and_b32 s54, s54, s69\n\t" \
    "s_cmp_gt_u32 s54, 124\n\t" \
    "s_cbranch_scc1 qk_E108_%=\n\t" \
    "s_cmp_eq_u32 s4, s28\n\t" \
    "s_addc_u32 s70, s70, 0\n\t" \
    "s_cmp_eq_u32 s6, s28\n\t" \
    "s_addc_u32 s70, s70, 0\n\t" \
    "s_cmp_eq_u32 s8, s28\n\t" \
    "s_addc_u32 s70, s70, 0\n\t" \
    "s_cmp_eq_u32 s10, s28\n\t" \
    "s_addc_u32 s70, s70, 0\n\t" \
    "s_cmp_eq_u32 s12, s28\n\t" \
    "s_addc_u32 s70, s70, 0\n\t" \
    "s_cmp_eq_u32 s14, s28\n\t" \
    "s_addc_u32 s70, s70, 0\n\t" \
    "s_cmp_eq_u32 s16, s28\n\t" \
    "s_addc_u32 s70, s70, 0\n\t" \
    "s_cmp_eq_u32 s18, s28\n\t" \
    "s_addc_u32 s70, s70, 0\n\t" \
    "s_cmp_eq_u32 s20, s28\n\t" \
    "s_addc_u32 s70, s70, 0\n\t" \
    "s_cmp_eq_u32 s22, s28\n\t" \
    "s_addc_u32 s70, s70, 0\n\t" \
    "s_cmp_eq_u32 s24, s28\n\t" \
    "s_addc_u32 s70, s70, 0\n\t" \
    "s_cmp_eq_u32 s26, s28\n\t" \
    "s_addc_u32 s70, s70, 0\n\t" \
    "s_mov_b32 s75, s73\n\t" \
    "s_mov_b32 s76, s74\n\t" \
    "s_lshl_b32 s70, s70, 3\n\t" \
    "s_sub_u32 s73, s56, s70\n\t" \
    "s_lshl_b32 s74, s28, 4\n\t" \
    "s_sub_u32 s69, s73, s75\n\t" \
    "s_sub_u32 s70, s74, s76\n\t" \
    "s_sub_u32 s71, s70, 1600\n\t" \
    "s_cmp_lt_u32 s71, 400000\n\t" \
    "s_cbranch_scc0 qk_K110_%=\n\t" \
    "v_cvt_f32_u32 v161, s69\n\t" \
    "v_cvt_f32_u32 v162, s70\n\t" \
    "v_rcp_f32 v163, v161\n\t" \
    "v_rcp_f32 v164, v162\n\t" \
    "v_mul_f32 v163, v163, v162\n\t" \
    "v_mul_f32 v164, v164, v161\n\t" \
    "v_mul_f32 v163, 0x4f800000, v163\n\t" \
    "v_mul_f32 v164, 0x4f000000, v164\n\t" \
    "v_cvt_u32_f32 v163, v163\n\t" \
    "v_cvt_u32_f32 v164, v164\n\t" \
    "v_readfirstlane_b32 s69, v163\n\t" \
    "v_readfirstlane_b32 s70, v164\n\t" \
    "s_nop 1\n\t" \
    "s_sub_u32 s69, s69, s78\n\t" \
    "s_ashr_i32 s69, s69, 2\n\t" \
    "s_add_u32 s78, s78, s69\n\t" \
    "s_sub_u32 s70, s70, s77\n\t" \
    "s_ashr_i32 s70, s70, 2\n\t" \
    "s_add_u32 s77, s77, s70\n\t" \
    "qk_K110_%=:\n\t" \
    "s_waitcnt vmcnt(0)\n\t" \
    "v_cmp_eq_u32 vcc, -1, v160\n\t" \
    "s_cbranch_vccz qk_Hok106_%=\n\t" \
    "qk_H105_%=:\n\t" \
    "global_load_dword v160, v20, s[46:47] sc1\n\t" \
    "s_waitcnt vmcnt(0)\n\t" \
    "v_cmp_eq_u32 vcc, -1, v160\n\t" \
    "s_cbranch_vccz qk_Hc107_%=\n\t" \
    "s_cmp_eq_u32 s68, 0\n\t" \
    "s_cbranch_scc1 qk_F111_%=\n\t" \
    "s_sub_u32 s68, s68, 1\n\t" \
    "s_sleep 1\n\t" \
    "s_branch qk_H105_%=\n\t" \
    "qk_F111_%=:\n\t" \
    "v_mov_b32 v161, 6095104\n\t" \
    "v_mov_b32 v162, 5\n\t" \
    "global_store_dword v161, v162, s[36:37] sc1\n\t" \
    "s_waitcnt vmcnt(0)\n\t" \
    "s_branch qk_Hc107_%=\n\t" \
    "qk_Hc107_%=:\n\t" \
    "qk_Hok106_%=:\n\t" \
    "v_subrev_u32 v160, s80, v160\n\t" \
    "v_and_b32 v20, -5, v20\n\t" \
    "s_nop 0\n\t" \
    "v_max_i32_dpp v160, v160, v160 row_ror:1 row_mask:0xf bank_mask:0xf\n\t" \
    "s_nop 1\n\t" \
    "v_max_i32_dpp v160, v160, v160 row_ror:2 row_mask:0xf bank_mask:0xf\n\t" \
    "s_nop 1\n\t" \
    "v_max_i32_dpp v160, v160, v160 row_ror:4 row_mask:0xf bank_mask:0xf\n\t" \
    "s_nop 1\n\t" \
    "v_max_i32_dpp v160, v160, v160 row_ror:8 row_mask:0xf bank_mask:0xf\n\t" \
    "s_nop 0\n\t" \
    "v_readlane_b32 s69, v160, 0\n\t" \
    "v_readlane_b32 s71, v160, 16\n\t" \
    "v_readlane_b32 s70, v160, 32\n\t" \
    "v_readlane_b32 s81, v160, 48\n\t" \
    "s_max_i32 s69, s69, s71\n\t" \
    "s_nop 0\n\t" \
    "s_max_i32 s71, s70, s81\n\t" \
    "s_max_i32 s69, s69, s71\n\t" \
    "s_add_u32 s81, s69, s80\n\t" \
    "v_mov_b32 v72, 0\n\t" \
    "v_mov_b32 v73, 0\n\t" \
    "v_mov_b32 v74, 0\n\t" \
    "v_mov_b32 v75, 0\n\t" \
    "v_mov_b32 v76, 0\n\t" \
    "v_mov_b32 v77, 0\n\t" \
    "v_mov_b32 v78, 0\n\t" \
    "v_mov_b32 v79, 0\n\t" \
    "v_mov_b32 v80, 0\n\t" \
    "v_mov_b32 v81, 0\n\t" \
    "v_mov_b32 v82, 0\n\t" \
    "v_mov_b32 v83, 0\n\t" \
    "v_mov_b32 v84, 0\n\t" \
    "v_mov_b32 v85, 0\n\t" \
    "v_mov_b32 v86, 0\n\t" \
    "v_mov_b32 v87, 0\n\t" \
    "s_cmp_lg_u32 s65, 0\n\t" \
    "s_cbranch_scc1 qk_Oc114_%=\n\t" \
    "s_add_u32 s79, s81, 240\n\t" \
    "s_sub_u32 s70, s79, s74\n\t" \
    "s_max_i32 s70, s70, 0\n\t" \
    "s_min_i32 s70, s70, 16000\n\t" \
    "s_lshl_b32 s70, s70, 1\n\t" \
    "s_mul_hi_u32 s70, s70, s77\n\t" \
    "s_add_u32 s70, s70, s73\n\t" \
    "qk_W115_%=:\n\t" \
    "s_memtime s[56:57]\n\t" \
    "s_waitcnt lgkmcnt(0)\n\t" \
    "s_sub_u32 s69, s70, s56\n\t" \
    "s_cmp_gt_i32 s69, 24\n\t" \
    "s_cbranch_scc1 qk_W115_%=\n\t" \
    "global_load_dword v160, v35, s[46:47] sc1\n\t" \
    "s_waitcnt vmcnt(0)\n\t" \
    "v_cmp_eq_u32 vcc, -1, v160\n\t" \
    "s_cbranch_vccz qk_Ook113_%=\n\t" \
    "qk_O112_%=:\n\t" \
    "global_load_dword v160, v35, s[46:47] offset:1536 sc1\n\t" \
    "s_waitcnt vmcnt(0)\n\t" \
    "v_cmp_eq_u32 vcc, -1, v160\n\t" \
    "s_cbranch_vccz qk_Ook113_%=\n\t" \
    "s_cmp_eq_u32 s68, 0\n\t" \
    "s_cbranch_scc1 qk_F116_%=\n\t" \
    "s_sub_u32 s68, s68, 1\n\t" \
    "s_sleep 3\n\t" \
    "s_branch qk_O112_%=\n\t" \
    "qk_F116_%=:\n\t" \
    "v_mov_b32 v161, 6095104\n\t" \
    "v_mov_b32 v162, 16\n\t" \
    "global_store_dword v161, v162, s[36:37] sc1\n\t" \
    "s_waitcnt vmcnt(0)\n\t" \
    "s_branch qk_Ook113_%=\n\t" \
    "qk_Ook113_%=:\n\t" \
    "v_subrev_u32 v160, s80, v160\n\t" \
    "s_nop 1\n\t" \
    "v_max_i32_dpp v160, v160, v160 row_ror:1 row_mask:0xf bank_mask:0xf\n\t" \
    "s_nop 1\n\t" \
    "v_max_i32_dpp v160, v160, v160 row_ror:2 row_mask:0xf bank_mask:0xf\n\t" \
    "s_nop 1\n\t" \
    "v_max_i32_dpp v160, v160, v160 row_ror:4 row_mask:0xf bank_mask:0xf\n\t" \
    "s_nop 1\n\t" \
    "v_max_i32_dpp v160, v160, v160 row_ror:8 row_mask:0xf bank_mask:0xf\n\t" \
    "s_nop 0\n\t" \
    "v_readlane_b32 s69, v160, 0\n\t" \
    "v_readlane_b32 s71, v160, 16\n\t" \
    "v_readlane_b32 s70, v160, 32\n\t" \
    "v_readlane_b32 s81, v160, 48\n\t" \
    "s_max_i32 s69, s69, s71\n\t" \
    "s_nop 0\n\t" \
    "s_max_i32 s71, s70, s81\n\t" \
    "s_max_i32 s69, s69, s71\n\t" \
    "s_add_u32 s81, s69, s80\n\t" \
    "qk_Oc114_%=:\n\t" \
    "v_mov_b32 v160, s81\n\t" \
    "ds_write_b32 v11, v160 offset:20480\n\t" \
    "s_add_u32 s65, s65, 1\n\t" \
    "v_add_u32 v31, 1, v31\n\t" \
    "s_add_u32 s69, s65, 0\n\t" \
    "s_bitcmp1_b32 s69, 0\n\t" \
    "s_cselect_b32 s70, s63, s62\n\t" \
    "s_bfe_u32 s69, s69, 0x20001\n\t" \
    "s_lshl_b32 s69, s69, 19\n\t" \
    "s_add_u32 s69, s69, s70\n\t" \
    "s_add_u32 s46, s36, s69\n\t" \
    "s_addc_u32 s47, s37, 0\n\t" \
    "s_waitcnt lgkmcnt(0)\n\t" \
    "s_barrier\n\t" \
    "ds_read_b32 v160, v11 offset:20480\n\t" \
    "s_waitcnt lgkmcnt(0)\n\t" \
    "v_readfirstlane_b32 s81, v160\n\t" \
    "s_nop 1\n\t" \
    "s_mul_hi_u32 s69, s85, s78\n\t" \
    "s_add_u32 s79, s81, s69\n\t" \
    "s_add_u32 s79, s79, s87\n\t" \
    "s_cmp_ge_u32 s65, 37\n\t" \
    "s_cselect_b32 s69, 28, 0\n\t" \
    "s_sub_u32 s79, s79, s69\n\t" \
    "s_cmp_lt_u32 s65, 9\n\t" \
    "s_cselect_b32 s69, 96, 0\n\t" \
    "s_add_u32 s79, s79, s69\n\t" \
    "s_add_u32 s80, s79, s88\n\t" \
    "s_branch qk_go94_%=\n\t" \
    "qk_Gbad99_%=:\n\t" \
    "s_waitcnt vmcnt(0) lgkmcnt(0)\n\t" \
    "s_nop 7\n\t" \
    "v_mov_b32 v72, 0\n\t" \
    "v_mov_b32 v73, 0\n\t" \
    "v_mov_b32 v74, 0\n\t" \
    "v_mov_b32 v75, 0\n\t" \
    "v_mov_b32 v76, 0\n\t" \
    "v_mov_b32 v77, 0\n\t" \
    "v_mov_b32 v78, 0\n\t" \
    "v_mov_b32 v79, 0\n\t" \
    "v_mov_b32 v80, 0\n\t" \
    "v_mov_b32 v81, 0\n\t" \
    "v_mov_b32 v82, 0\n\t" \
    "v_mov_b32 v83, 0\n\t" \
    "v_mov_b32 v84, 0\n\t" \
    "v_mov_b32 v85, 0\n\t" \
    "v_mov_b32 v86, 0\n\t" \
    "v_mov_b32 v87, 0\n\t" \
    "s_cmp_eq_u32 s68, 0\n\t" \
    "s_cbranch_scc1 qk_F117_%=\n\t" \
    "s_sub_u32 s68, s68, 1\n\t" \
    "s_branch qk_G98_%=\n\t" \
    "qk_F117_%=:\n\t" \
    "v_mov_b32 v160, 6095104\n\t" \
    "v_mov_b32 v161, 1\n\t" \
    "global_store_dword v160, v161, s[36:37] sc1\n\t" \
    "s_waitcnt vmcnt(0)\n\t" \
    "s_branch qk_Gc101_%=\n\t" \
    "qk_Gbad2100_%=:\n\t" \
    "global_load_dwordx4 v[44:47], v2, s[46:47] sc1\n\t" \
    "s_waitcnt vmcnt(0)\n\t" \
    "v_max3_u32 v160, v44, v45, v46\n\t" \
    "v_max_u32 v160, v160, v47\n\t" \
    "v_cmp_eq_u32 vcc, -1, v160\n\t" \
    "s_cbranch_vccz qk_Gre2103_%=\n\t" \
    "s_cmp_eq_u32 s68, 0\n\t" \
    "s_cbranch_scc1 qk_F118_%=\n\t" \
    "s_sub_u32 s68, s68, 1\n\t" \
    "s_sleep 1\n\t" \
    "s_branch qk_Gbad2100_%=\n\t" \
    "qk_F118_%=:\n\t" \
    "v_mov_b32 v160, 6095104\n\t" \
    "v_mov_b32 v161, 2\n\t" \
    "global_store_dword v160, v161, s[36:37] sc1\n\t" \
    "s_waitcnt vmcnt(0)\n\t" \
    "s_branch qk_Gre2103_%=\n\t" \
    "qk_Gre2103_%=:\n\t" \
    "v_mov_b32 v72, 0\n\t" \
    "v_mov_b32 v73, 0\n\t" \
    "v_mov_b32 v74, 0\n\t" \
    "v_mov_b32 v75, 0\n\t" \
    "v_mov_b32 v76, 0\n\t" \
    "v_mov_b32 v77, 0\n\t" \
    "v_mov_b32 v78, 0\n\t" \
    "v_mov_b32 v79, 0\n\t" \
    "v_mov_b32 v80, 0\n\t" \
    "v_mov_b32 v81, 0\n\t" \
    "v_mov_b32 v82, 0\n\t" \
    "v_mov_b32 v83, 0\n\t" \
    "v_mov_b32 v84, 0\n\t" \
    "v_mov_b32 v85, 0\n\t" \
    "v_mov_b32 v86, 0\n\t" \
    "v_mov_b32 v87, 0\n\t" \
    "v_smfmac_f32_16x16x64_bf16 v[72:75], v[36:39], a[48:55], v10\n\t" \
    "v_smfmac_f32_16x16x64_bf16 v[76:79], v[36:39], a[120:127], v10\n\t" \
    "v_smfmac_f32_16x16x64_bf16 v[80:83], v[36:39], a[192:199], v10\n\t" \
    "v_smfmac_f32_16x16x64_bf16 v[84:87], v[36:39], v[232:239], v10\n\t" \
    "v_smfmac_f32_16x16x64_bf16 v[72:75], v[40:43], a[56:63], v10\n\t" \
    "v_smfmac_f32_16x16x64_bf16 v[76:79], v[40:43], a[128:135], v10\n\t" \
    "v_smfmac_f32_16x16x64_bf16 v[80:83], v[40:43], a[200:207], v10\n\t" \
    "v_smfmac_f32_16x16x64_bf16 v[84:87], v[40:43], v[240:247], v10\n\t" \
    "v_smfmac_f32_16x16x64_bf16 v[72:75], v[44:47], a[64:71], v10\n\t" \
    "v_smfmac_f32_16x16x64_bf16 v[76:79], v[44:47], a[136:143], v10\n\t" \
    "v_smfmac_f32_16x16x64_bf16 v[80:83], v[44:47], a[208:215], v10\n\t" \
    "v_smfmac_f32_16x16x64_bf16 v[84:87], v[44:47], v[248:255], v10\n\t" \
    "s_waitcnt lgkmcnt(0)\n\t" \
    "s_memtime s[56:57]\n\t" \
    "s_waitcnt lgkmcnt(0)\n\t" \
    "s_branch qk_Gc2102_%=\n\t" \
    "qk_last89_%=:\n\t" \
    "s_endpgm\n\t" \
    "vv_begin_%=:\n\t" \
    "v_mbcnt_lo_u32_b32 v223, -1, 0\n\t" \
    "v_mbcnt_hi_u32_b32 v223, -1, v223\n\t" \
    "s_mov_b32 s68, 262144\n\t" \
    "s_mov_b32 s72, 0\n\t" \
    "s_mov_b32 s83, -1\n\t" \
    "s_mov_b32 s65, 0\n\t" \
    "v_and_b32 v180, 3, v223\n\t" \
    "v_lshlrev_b32 v202, 3, v180\n\t" \
    "v_lshrrev_b32 v181, 5, v223\n\t" \
    "v_lshl_add_u32 v202, v181, 12, v202\n\t" \
    "v_bfe_u32 v182, v223, 2, 2\n\t" \
    "s_mov_b32 s70, 288\n\t" \
    "v_mad_u32_u24 v202, v182, s70, v202\n\t" \
    "v_bfe_u32 v183, v223, 4, 1\n\t" \
    "s_mov_b32 s70, 2176\n\t" \
    "v_mad_u32_u24 v202, v183, s70, v202\n\t" \
    "s_lshl_b32 s69, s66, 6\n\t" \
    "v_xor_b32 v202, s69, v202\n\t" \
    "v_xor_b32 v203, 32, v202\n\t" \
    "s_mul_i32 s69, s66, 9216\n\t" \
    "v_and_b32 v184, 1, v223\n\t" \
    "v_lshl_add_u32 v201, v184, 4, s69\n\t" \
    "v_lshrrev_b32 v185, 3, v223\n\t" \
    "s_mov_b32 s70, 1152\n\t" \
    "v_mad_u32_u24 v201, v185, s70, v201\n\t" \
    "v_bfe_u32 v186, v223, 1, 2\n\t" \
    "v_lshlrev_b32 v204, 5, v186\n\t" \
    "v_lshrrev_b32 v187, 4, v223\n\t" \
    "v_lshlrev_b32 v205, 5, v187\n\t" \
    "v_xor_b32 v204, v204, v205\n\t" \
    "v_add_u32 v201, v201, v204\n\t" \
    "s_mul_i32 s96, s66, 9216\n\t" \
    "v_lshlrev_b32 v207, 3, v180\n\t" \
    "v_bfe_u32 v188, v223, 2, 1\n\t" \
    "v_lshl_add_u32 v207, v188, 7, v207\n\t" \
    "v_bfe_u32 v189, v223, 3, 2\n\t" \
    "s_mov_b32 s70, 288\n\t" \
    "v_mad_u32_u24 v207, v189, s70, v207\n\t" \
    "s_mov_b32 s70, 9216\n\t" \
    "v_mad_u32_u24 v207, v181, s70, v207\n\t" \
    "s_mul_i32 s69, s66, 9\n\t" \
    "s_add_u32 s69, s69, 0\n\t" \
    "s_and_b32 s70, s69, 3\n\t" \
    "s_lshl_b32 s70, s70, 5\n\t" \
    "s_lshr_b32 s69, s69, 2\n\t" \
    "s_lshl_b32 s69, s69, 10\n\t" \
    "v_xor_b32 v204, s70, v207\n\t" \
    "v_add_u32 v204, s69, v204\n\t" \
    "s_mul_i32 s69, s66, 9\n\t" \
    "s_add_u32 s69, s69, 1\n\t" \
    "s_and_b32 s70, s69, 3\n\t" \
    "s_lshl_b32 s70, s70, 5\n\t" \
    "s_lshr_b32 s69, s69, 2\n\t" \
    "s_lshl_b32 s69, s69, 10\n\t" \
    "v_xor_b32 v205, s70, v207\n\t" \
    "v_add_u32 v205, s69, v205\n\t" \
    "s_mul_i32 s69, s66, 9\n\t" \
    "s_add_u32 s69, s69, 2\n\t" \
    "s_and_b32 s70, s69, 3\n\t" \
    "s_lshl_b32 s70, s70, 5\n\t" \
    "s_lshr_b32 s69, s69, 2\n\t" \
    "s_lshl_b32 s69, s69, 10\n\t" \
    "v_xor_b32 v206, s70, v207\n\t" \
    "v_add_u32 v206, s69, v206\n\t" \
    "s_mul_i32 s69, s66, 9\n\t" \
    "s_add_u32 s69, s69, 3\n\t" \
    "s_and_b32 s70, s69, 3\n\t" \
    "s_lshl_b32 s70, s70, 5\n\t" \
    "s_lshr_b32 s69, s69, 2\n\t" \
    "s_lshl_b32 s69, s69, 10\n\t" \
    "v_xor_b32 v207, s70, v207\n\t" \
    "v_add_u32 v207, s69, v207\n\t" \
    "s_waitcnt vmcnt(12) lgkmcnt(0)\n\t" \
    "s_barrier\n\t" \
    "s_add_u32 m0, s82, 65536\n\t" \
    "s_nop 0\n\t" \
    "global_load_lds_dwordx4 v200, s[42:43] nt\n\t" \
    "v_add_u32 v200, 131072, v200\n\t" \
    "s_add_u32 m0, s82, 69632\n\t" \
    "s_nop 0\n\t" \
    "global_load_lds_dwordx4 v200, s[42:43] nt\n\t" \
    "v_add_u32 v200, 131072, v200\n\t" \
    "s_add_u32 m0, s82, 73728\n\t" \
    "s_nop 0\n\t" \
    "global_load_lds_dwordx4 v200, s[42:43] nt\n\t" \
    "v_add_u32 v200, 131072, v200\n\t" \
    "s_add_u32 m0, s82, 77824\n\t" \
    "s_nop 0\n\t" \
    "global_load_lds_dwordx4 v200, s[42:43] nt\n\t" \
    "v_add_u32 v200, 131072, v200\n\t" \
    "ds_read_b64_tr_b16 a[0:1], v202 offset:36864\n\t" \
    "ds_read_b64_tr_b16 a[2:3], v202 offset:37888\n\t" \
    "ds_read_b64_tr_b16 a[4:5], v202 offset:45056\n\t" \
    "ds_read_b64_tr_b16 a[6:7], v202 offset:46080\n\t" \
    "ds_read_b64_tr_b16 a[72:73], v203 offset:36864\n\t" \
    "ds_read_b64_tr_b16 a[74:75], v203 offset:37888\n\t" \
    "ds_read_b64_tr_b16 a[76:77], v203 offset:45056\n\t" \
    "ds_read_b64_tr_b16 a[78:79], v203 offset:46080\n\t" \
    "v_lshlrev_b32 v0, 4, v223\n\t" \
    "s_mul_i32 s69, s66, 2048\n\t" \
    "s_bfe_u32 s71, s67, 0x20003\n\t" \
    "s_mul_i32 s71, s71, 65536\n\t" \
    "s_add_u32 s69, s69, s71\n\t" \
    "s_add_u32 s69, s69, 32768\n\t" \
    "v_lshl_add_u32 v1, v223, 4, s69\n\t" \
    "s_bfe_u32 s71, s67, 0x20003\n\t" \
    "s_mul_i32 s69, s71, 65536\n\t" \
    "s_add_u32 s69, s69, 53248\n\t" \
    "v_lshl_add_u32 v2, v223, 4, s69\n\t" \
    "s_bfe_u32 s71, s67, 0x20003\n\t" \
    "s_mul_i32 s69, s71, 65536\n\t" \
    "s_add_u32 s69, s69, 32768\n\t" \
    "v_lshl_add_u32 v34, v223, 4, s69\n\t" \
    "s_mov_b32 s69, 17476\n\t" \
    "v_bfe_u32 v190, v223, 1, 1\n\t" \
    "v_mov_b32 v10, s69\n\t" \
    "s_mov_b32 s70, 43690\n\t" \
    "v_mad_u32_u24 v10, v190, s70, v10\n\t" \
    "v_mov_b32 v11, 0\n\t" \
    "s_waitcnt vmcnt(12) lgkmcnt(0)\n\t" \
    "s_barrier\n\t" \
    "s_add_u32 m0, s82, 81920\n\t" \
    "s_nop 0\n\t" \
    "global_load_lds_dwordx4 v200, s[42:43] nt\n\t" \
    "v_add_u32 v200, 131072, v200\n\t" \
    "s_add_u32 m0, s82, 86016\n\t" \
    "s_nop 0\n\t" \
    "global_load_lds_dwordx4 v200, s[42:43] nt\n\t" \
    "v_add_u32 v200, 131072, v200\n\t" \
    "s_add_u32 m0, s82, 90112\n\t" \
    "s_nop 0\n\t" \
    "global_load_lds_dwordx4 v200, s[42:43] nt\n\t" \
    "v_add_u32 v200, 131072, v200\n\t" \
    "s_add_u32 m0, s82, 94208\n\t" \
    "s_nop 0\n\t" \
    "global_load_lds_dwordx4 v200, s[42:43] nt\n\t" \
    "v_add_u32 v200, 131072, v200\n\t" \
    "ds_read_b64_tr_b16 a[8:9], v202 offset:53248\n\t" \
    "ds_read_b64_tr_b16 a[10:11], v202 offset:54272\n\t" \
    "ds_read_b64_tr_b16 a[12:13], v202 offset:61440\n\t" \
    "ds_read_b64_tr_b16 a[14:15], v202 offset:62464\n\t" \
    "ds_read_b64_tr_b16 a[80:81], v203 offset:53248\n\t" \
    "ds_read_b64_tr_b16 a[82:83], v203 offset:54272\n\t" \
    "ds_read_b64_tr_b16 a[84:85], v203 offset:61440\n\t" \
    "ds_read_b64_tr_b16 a[86:87], v203 offset:62464\n\t" \
    "s_mul_i32 s69, s66, 512\n\t" \
    "s_add_u32 s69, s69, 16384\n\t" \
    "v_and_b32 v191, 13, v223\n\t" \
    "v_lshl_add_u32 v12, v191, 5, s69\n\t" \
    "v_lshl_add_u32 v12, v190, 2, v12\n\t" \
    "v_lshl_add_u32 v12, v187, 3, v12\n\t" \
    "s_mov_b32 s69, 16384\n\t" \
    "v_and_b32 v192, 15, v223\n\t" \
    "v_lshl_add_u32 v13, v192, 3, s69\n\t" \
    "v_lshl_add_u32 v13, v187, 8, v13\n\t" \
    "s_bfe_u32 s71, s67, 0x20003\n\t" \
    "s_mul_i32 s69, s71, 65536\n\t" \
    "s_add_u32 s69, s69, 266240\n\t" \
    "v_and_b32 v193, 31, v223\n\t" \
    "v_lshl_add_u32 v20, v193, 5, s69\n\t" \
    "v_lshl_add_u32 v20, v181, 2, v20\n\t" \
    "s_mul_i32 s69, s67, 32\n\t" \
    "s_add_u32 s69, s69, 262144\n\t" \
    "v_lshl_add_u32 v21, v180, 16, s69\n\t" \
    "v_bfe_u32 v194, v223, 2, 3\n\t" \
    "v_lshlrev_b32 v199, 10, v194\n\t" \
    "v_min_u32 v199, 4096, v199\n\t" \
    "v_add_u32 v21, v199, v21\n\t" \
    "s_bfe_u32 s71, s67, 0x20003\n\t" \
    "s_mul_i32 s69, s71, 65536\n\t" \
    "s_add_u32 s69, s69, 267264\n\t" \
    "v_min_u32 v35, 35, v223\n\t" \
    "v_lshl_add_u32 v35, v35, 5, s69\n\t" \
    "v_lshlrev_b32 v22, 2, v223\n\t" \
    "s_waitcnt vmcnt(12) lgkmcnt(0)\n\t" \
    "s_barrier\n\t" \
    "s_add_u32 m0, s82, 98304\n\t" \
    "s_nop 0\n\t" \
    "global_load_lds_dwordx4 v200, s[42:43] nt\n\t" \
    "v_add_u32 v200, 131072, v200\n\t" \
    "s_add_u32 m0, s82, 102400\n\t" \
    "s_nop 0\n\t" \
    "global_load_lds_dwordx4 v200, s[42:43] nt\n\t" \
    "v_add_u32 v200, 131072, v200\n\t" \
    "s_add_u32 m0, s82, 106496\n\t" \
    "s_nop 0\n\t" \
    "global_load_lds_dwordx4 v200, s[42:43] nt\n\t" \
    "v_add_u32 v200, 131072, v200\n\t" \
    "s_add_u32 m0, s82, 110592\n\t" \
    "s_nop 0\n\t" \
    "global_load_lds_dwordx4 v200, s[42:43] nt\n\t" \
    "v_add_u32 v200, 131072, v200\n\t" \
    "v_add_u32 v202, 69632, v202\n\t" \
    "v_add_u32 v203, 69632, v203\n\t" \
    "ds_read_b64_tr_b16 a[16:17], v202 offset:0\n\t" \
    "ds_read_b64_tr_b16 a[18:19], v202 offset:1024\n\t" \
    "ds_read_b64_tr_b16 a[20:21], v202 offset:8192\n\t" \
    "ds_read_b64_tr_b16 a[22:23], v202 offset:9216\n\t" \
    "ds_read_b64_tr_b16 a[88:89], v203 offset:0\n\t" \
    "ds_read_b64_tr_b16 a[90:91], v203 offset:1024\n\t" \
    "ds_read_b64_tr_b16 a[92:93], v203 offset:8192\n\t" \
    "ds_read_b64_tr_b16 a[94:95], v203 offset:9216\n\t" \
    "s_mul_i32 s69, s66, 147456\n\t" \
    "s_mul_i32 s71, s67, 256\n\t" \
    "s_add_u32 s69, s69, s71\n\t" \
    "v_lshl_add_u32 v14, v192, 4, s69\n\t" \
    "v_lshl_add_u32 v14, v187, 14, v14\n\t" \
    "s_mul_i32 s69, s66, 147456\n\t" \
    "s_mul_i32 s71, s67, 256\n\t" \
    "s_add_u32 s69, s69, s71\n\t" \
    "s_add_u32 s69, s69, 65536\n\t" \
    "v_lshl_add_u32 v15, v192, 4, s69\n\t" \
    "v_lshl_add_u32 v15, v187, 14, v15\n\t" \
    "s_mul_i32 s69, s66, 147456\n\t" \
    "s_mul_i32 s71, s67, 256\n\t" \
    "s_add_u32 s69, s69, s71\n\t" \
    "s_add_u32 s69, s69, 131072\n\t" \
    "v_lshl_add_u32 v16, v192, 4, s69\n\t" \
    "v_lshl_add_u32 v16, v187, 2, v16\n\t" \
    "s_mul_i32 s97, s67, 171\n\t" \
    "s_lshr_b32 s97, s97, 9\n\t" \
    "s_mul_i32 s97, s97, 131072\n\t" \
    "s_mul_i32 s71, s66, 2304\n\t" \
    "s_add_u32 s69, s97, s71\n\t" \
    "s_mul_i32 s71, s67, 655360\n\t" \
    "s_add_u32 s69, s69, s71\n\t" \
    "v_lshl_add_u32 v17, v223, 4, s69\n\t" \
    "s_mul_i32 s71, s66, 2304\n\t" \
    "s_add_u32 s69, s97, s71\n\t" \
    "s_mul_i32 s71, s67, 655360\n\t" \
    "s_add_u32 s69, s69, s71\n\t" \
    "s_add_u32 s69, s69, 2048\n\t" \
    "v_lshl_add_u32 v18, v192, 4, s69\n\t" \
    "v_lshl_add_u32 v18, v187, 2, v18\n\t" \
    "s_waitcnt vmcnt(12) lgkmcnt(0)\n\t" \
    "s_barrier\n\t" \
    "s_add_u32 m0, s82, 0\n\t" \
    "s_nop 0\n\t" \
    "global_load_lds_dwordx4 v200, s[42:43] nt\n\t" \
    "v_add_u32 v200, 131072, v200\n\t" \
    "s_add_u32 m0, s82, 4096\n\t" \
    "s_nop 0\n\t" \
    "global_load_lds_dwordx4 v200, s[42:43] nt\n\t" \
    "v_add_u32 v200, 131072, v200\n\t" \
    "s_add_u32 m0, s82, 8192\n\t" \
    "s_nop 0\n\t" \
    "global_load_lds_dwordx4 v200, s[42:43] nt\n\t" \
    "v_add_u32 v200, 131072, v200\n\t" \
    "s_add_u32 m0, s82, 12288\n\t" \
    "s_nop 0\n\t" \
    "global_load_lds_dwordx4 v200, s[42:43] nt\n\t" \
    "v_add_u32 v200, 131072, v200\n\t" \
    "ds_read_b64_tr_b16 a[24:25], v202 offset:16384\n\t" \
    "ds_read_b64_tr_b16 a[26:27], v202 offset:17408\n\t" \
    "ds_read_b64_tr_b16 a[28:29], v202 offset:24576\n\t" \
    "ds_read_b64_tr_b16 a[30:31], v202 offset:25600\n\t" \
    "ds_read_b64_tr_b16 a[96:97], v203 offset:16384\n\t" \
    "ds_read_b64_tr_b16 a[98:99], v203 offset:17408\n\t" \
    "ds_read_b64_tr_b16 a[100:101], v203 offset:24576\n\t" \
    "ds_read_b64_tr_b16 a[102:103], v203 offset:25600\n\t" \
    "s_mov_b32 s77, 3019898880\n\t" \
    "s_mov_b32 s78, 3054198966\n\t" \
    "s_mov_b32 s71, 3\n\t" \
    "vv_E1_%=:\n\t" \
    "s_memtime s[54:55]\n\t" \
    "s_nop 0\n\t" \
    "s_memrealtime s[4:5]\n\t" \
    "s_nop 0\n\t" \
    "s_memrealtime s[6:7]\n\t" \
    "s_nop 0\n\t" \
    "s_memrealtime s[8:9]\n\t" \
    "s_nop 0\n\t" \
    "s_memrealtime s[10:11]\n\t" \
    "s_nop 0\n\t" \
    "s_memrealtime s[12:13]\n\t" \
    "s_nop 0\n\t" \
    "s_memrealtime s[14:15]\n\t" \
    "s_nop 0\n\t" \
    "s_memrealtime s[16:17]\n\t" \
    "s_nop 0\n\t" \
    "s_memrealtime s[18:19]\n\t" \
    "s_nop 0\n\t" \
    "s_memrealtime s[20:21]\n\t" \
    "s_nop 0\n\t" \
    "s_memrealtime s[22:23]\n\t" \
    "s_nop 0\n\t" \
    "s_memrealtime s[24:25]\n\t" \
    "s_nop 0\n\t" \
    "s_memrealtime s[26:27]\n\t" \
    "s_nop 0\n\t" \
    "s_memrealtime s[28:29]\n\t" \
    "s_nop 0\n\t" \
    "s_memtime s[56:57]\n\t" \
    "s_mov_b32 s70, 1\n\t" \
    "s_sub_u32 s71, s71, 1\n\t" \
    "s_cselect_b32 s69, 0, -1\n\t" \
    "s_waitcnt lgkmcnt(0)\n\t" \
    "s_sub_u32 s54, s56, s54\n\t" \
    "s_and_b32 s54, s54, s69\n\t" \
    "s_cmp_gt_u32 s54, 124\n\t" \
    "s_cbranch_scc1 vv_E1_%=\n\t" \
    "s_cmp_eq_u32 s4, s28\n\t" \
    "s_addc_u32 s70, s70, 0\n\t" \
    "s_cmp_eq_u32 s6, s28\n\t" \
    "s_addc_u32 s70, s70, 0\n\t" \
    "s_cmp_eq_u32 s8, s28\n\t" \
    "s_addc_u32 s70, s70, 0\n\t" \
    "s_cmp_eq_u32 s10, s28\n\t" \
    "s_addc_u32 s70, s70, 0\n\t" \
    "s_cmp_eq_u32 s12, s28\n\t" \
    "s_addc_u32 s70, s70, 0\n\t" \
    "s_cmp_eq_u32 s14, s28\n\t" \
    "s_addc_u32 s70, s70, 0\n\t" \
    "s_cmp_eq_u32 s16, s28\n\t" \
    "s_addc_u32 s70, s70, 0\n\t" \
    "s_cmp_eq_u32 s18, s28\n\t" \
    "s_addc_u32 s70, s70, 0\n\t" \
    "s_cmp_eq_u32 s20, s28\n\t" \
    "s_addc_u32 s70, s70, 0\n\t" \
    "s_cmp_eq_u32 s22, s28\n\t" \
    "s_addc_u32 s70, s70, 0\n\t" \
    "s_cmp_eq_u32 s24, s28\n\t" \
    "s_addc_u32 s70, s70, 0\n\t" \
    "s_cmp_eq_u32 s26, s28\n\t" \
    "s_addc_u32 s70, s70, 0\n\t" \
    "s_mov_b32 s75, s73\n\t" \
    "s_mov_b32 s76, s74\n\t" \
    "s_lshl_b32 s70, s70, 3\n\t" \
    "s_sub_u32 s73, s56, s70\n\t" \
    "s_lshl_b32 s74, s28, 4\n\t" \
    "s_waitcnt vmcnt(12) lgkmcnt(0)\n\t" \
    "s_barrier\n\t" \
    "s_add_u32 m0, s82, 16384\n\t" \
    "s_nop 0\n\t" \
    "global_load_lds_dwordx4 v200, s[42:43] nt\n\t" \
    "v_add_u32 v200, 131072, v200\n\t" \
    "s_add_u32 m0, s82, 20480\n\t" \
    "s_nop 0\n\t" \
    "global_load_lds_dwordx4 v200, s[42:43] nt\n\t" \
    "v_add_u32 v200, 131072, v200\n\t" \
    "s_add_u32 m0, s82, 24576\n\t" \
    "s_nop 0\n\t" \
    "global_load_lds_dwordx4 v200, s[42:43] nt\n\t" \
    "v_add_u32 v200, 131072, v200\n\t" \
    "s_add_u32 m0, s82, 28672\n\t" \
    "s_nop 0\n\t" \
    "global_load_lds_dwordx4 v200, s[42:43] nt\n\t" \
    "v_add_u32 v200, 131072, v200\n\t" \
    "ds_read_b64_tr_b16 a[32:33], v202 offset:32768\n\t" \
    "ds_read_b64_tr_b16 a[34:35], v202 offset:33792\n\t" \
    "ds_read_b64_tr_b16 a[36:37], v202 offset:40960\n\t" \
    "ds_read_b64_tr_b16 a[38:39], v202 offset:41984\n\t" \
    "ds_read_b64_tr_b16 a[104:105], v203 offset:32768\n\t" \
    "ds_read_b64_tr_b16 a[106:107], v203 offset:33792\n\t" \
    "ds_read_b64_tr_b16 a[108:109], v203 offset:40960\n\t" \
    "ds_read_b64_tr_b16 a[110:111], v203 offset:41984\n\t" \
    "s_waitcnt vmcnt(12) lgkmcnt(0)\n\t" \
    "s_barrier\n\t" \
    "s_add_u32 m0, s96, 0\n\t" \
    "s_nop 0\n\t" \
    "global_load_lds_dwordx4 v201, s[44:45] offset:0 nt\n\t" \
    "s_add_u32 m0, s96, 896\n\t" \
    "s_nop 0\n\t" \
    "global_load_lds_dwordx4 v201, s[44:45] offset:128 nt\n\t" \
    "s_add_u32 m0, s96, 1792\n\t" \
    "s_nop 0\n\t" \
    "global_load_lds_dwordx4 v201, s[44:45] offset:256 nt\n\t" \
    "s_add_u32 m0, s96, 2688\n\t" \
    "s_nop 0\n\t" \
    "global_load_lds_dwordx4 v201, s[44:45] offset:384 nt\n\t" \
    "s_add_u32 m0, s96, 3584\n\t" \
    "s_nop 0\n\t" \
    "global_load_lds_dwordx4 v201, s[44:45] offset:512 nt\n\t" \
    "s_add_u32 m0, s96, 4480\n\t" \
    "s_nop 0\n\t" \
    "global_load_lds_dwordx4 v201, s[44:45] offset:640 nt\n\t" \
    "s_add_u32 m0, s96, 5376\n\t" \
    "s_nop 0\n\t" \
    "global_load_lds_dwordx4 v201, s[44:45] offset:768 nt\n\t" \
    "s_add_u32 m0, s96, 6272\n\t" \
    "s_nop 0\n\t" \
    "global_load_lds_dwordx4 v201, s[44:45] offset:896 nt\n\t" \
    "s_add_u32 m0, s96, 7168\n\t" \
    "s_nop 0\n\t" \
    "global_load_lds_dwordx4 v201, s[44:45] offset:1024 nt\n\t" \
    "v_add_u32 v201, 36864, v201\n\t" \
    "ds_read_b64_tr_b16 a[40:41], v202 offset:49152\n\t" \
    "ds_read_b64_tr_b16 a[42:43], v202 offset:50176\n\t" \
    "ds_read_b64_tr_b16 a[44:45], v202 offset:57344\n\t" \
    "ds_read_b64_tr_b16 a[46:47], v202 offset:58368\n\t" \
    "ds_read_b64_tr_b16 a[112:113], v203 offset:49152\n\t" \
    "ds_read_b64_tr_b16 a[114:115], v203 offset:50176\n\t" \
    "ds_read_b64_tr_b16 a[116:117], v203 offset:57344\n\t" \
    "ds_read_b64_tr_b16 a[118:119], v203 offset:58368\n\t" \
    "s_waitcnt vmcnt(17) lgkmcnt(0)\n\t" \
    "s_barrier\n\t" \
    "v_add_u32 v202, 65536, v202\n\t" \
    "v_add_u32 v203, 65536, v203\n\t" \
    "ds_read_b64_tr_b16 a[48:49], v202 offset:0\n\t" \
    "ds_read_b64_tr_b16 a[50:51], v202 offset:1024\n\t" \
    "ds_read_b64_tr_b16 a[52:53], v202 offset:8192\n\t" \
    "ds_read_b64_tr_b16 a[54:55], v202 offset:9216\n\t" \
    "ds_read_b64_tr_b16 a[120:121], v203 offset:0\n\t" \
    "ds_read_b64_tr_b16 a[122:123], v203 offset:1024\n\t" \
    "ds_read_b64_tr_b16 a[124:125], v203 offset:8192\n\t" \
    "ds_read_b64_tr_b16 a[126:127], v203 offset:9216\n\t" \
    "s_waitcnt vmcnt(13) lgkmcnt(0)\n\t" \
    "s_barrier\n\t" \
    "s_add_u32 m0, s96, 73728\n\t" \
    "s_nop 0\n\t" \
    "global_load_lds_dwordx4 v201, s[44:45] offset:0 nt\n\t" \
    "s_add_u32 m0, s96, 74624\n\t" \
    "s_nop 0\n\t" \
    "global_load_lds_dwordx4 v201, s[44:45] offset:128 nt\n\t" \
    "s_add_u32 m0, s96, 75520\n\t" \
    "s_nop 0\n\t" \
    "global_load_lds_dwordx4 v201, s[44:45] offset:256 nt\n\t" \
    "s_add_u32 m0, s96, 76416\n\t" \
    "s_nop 0\n\t" \
    "global_load_lds_dwordx4 v201, s[44:45] offset:384 nt\n\t" \
    "s_add_u32 m0, s96, 77312\n\t" \
    "s_nop 0\n\t" \
    "global_load_lds_dwordx4 v201, s[44:45] offset:512 nt\n\t" \
    "s_add_u32 m0, s96, 78208\n\t" \
    "s_nop 0\n\t" \
    "global_load_lds_dwordx4 v201, s[44:45] offset:640 nt\n\t" \
    "s_add_u32 m0, s96, 79104\n\t" \
    "s_nop 0\n\t" \
    "global_load_lds_dwordx4 v201, s[44:45] offset:768 nt\n\t" \
    "s_add_u32 m0, s96, 80000\n\t" \
    "s_nop 0\n\t" \
    "global_load_lds_dwordx4 v201, s[44:45] offset:896 nt\n\t" \
    "s_add_u32 m0, s96, 80896\n\t" \
    "s_nop 0\n\t" \
    "global_load_lds_dwordx4 v201, s[44:45] offset:1024 nt\n\t" \
    "v_add_u32 v201, 36864, v201\n\t" \
    "v_add_u32 v202, -98304, v202\n\t" \
    "v_add_u32 v203, -98304, v203\n\t" \
    "ds_read_b64_tr_b16 a[56:57], v202 offset:0\n\t" \
    "ds_read_b64_tr_b16 a[58:59], v202 offset:1024\n\t" \
    "ds_read_b64_tr_b16 a[60:61], v202 offset:8192\n\t" \
    "ds_read_b64_tr_b16 a[62:63], v202 offset:9216\n\t" \
    "ds_read_b64_tr_b16 a[128:129], v203 offset:0\n\t" \
    "ds_read_b64_tr_b16 a[130:131], v203 offset:1024\n\t" \
    "ds_read_b64_tr_b16 a[132:133], v203 offset:8192\n\t" \
    "ds_read_b64_tr_b16 a[134:135], v203 offset:9216\n\t" \
    "s_waitcnt vmcnt(18) lgkmcnt(0)\n\t" \
    "s_barrier\n\t" \
    "s_add_u32 m0, s96, 110592\n\t" \
    "s_nop 0\n\t" \
    "global_load_lds_dwordx4 v201, s[44:45] offset:0 nt\n\t" \
    "s_add_u32 m0, s96, 111488\n\t" \
    "s_nop 0\n\t" \
    "global_load_lds_dwordx4 v201, s[44:45] offset:128 nt\n\t" \
    "s_add_u32 m0, s96, 112384\n\t" \
    "s_nop 0\n\t" \
    "global_load_lds_dwordx4 v201, s[44:45] offset:256 nt\n\t" \
    "s_add_u32 m0, s96, 113280\n\t" \
    "s_nop 0\n\t" \
    "global_load_lds_dwordx4 v201, s[44:45] offset:384 nt\n\t" \
    "s_add_u32 m0, s96, 114176\n\t" \
    "s_nop 0\n\t" \
    "global_load_lds_dwordx4 v201, s[44:45] offset:512 nt\n\t" \
    "s_add_u32 m0, s96, 115072\n\t" \
    "s_nop 0\n\t" \
    "global_load_lds_dwordx4 v201, s[44:45] offset:640 nt\n\t" \
    "s_add_u32 m0, s96, 115968\n\t" \
    "s_nop 0\n\t" \
    "global_load_lds_dwordx4 v201, s[44:45] offset:768 nt\n\t" \
    "s_add_u32 m0, s96, 116864\n\t" \
    "s_nop 0\n\t" \
    "global_load_lds_dwordx4 v201, s[44:45] offset:896 nt\n\t" \
    "s_add_u32 m0, s96, 117760\n\t" \
    "s_nop 0\n\t" \
    "global_load_lds_dwordx4 v201, s[44:45] offset:1024 nt\n\t" \
    "v_add_u32 v201, 36864, v201\n\t" \
    "ds_read_b64_tr_b16 a[64:65], v202 offset:16384\n\t" \
    "ds_read_b64_tr_b16 a[66:67], v202 offset:17408\n\t" \
    "ds_read_b64_tr_b16 a[68:69], v202 offset:24576\n\t" \
    "ds_read_b64_tr_b16 a[70:71], v202 offset:25600\n\t" \
    "ds_read_b64_tr_b16 a[136:137], v203 offset:16384\n\t" \
    "ds_read_b64_tr_b16 a[138:139], v203 offset:17408\n\t" \
    "ds_read_b64_tr_b16 a[140:141], v203 offset:24576\n\t" \
    "ds_read_b64_tr_b16 a[142:143], v203 offset:25600\n\t" \
    "s_waitcnt vmcnt(18) lgkmcnt(0)\n\t" \
    "s_barrier\n\t" \
    "s_add_u32 m0, s96, 36864\n\t" \
    "s_nop 0\n\t" \
    "global_load_lds_dwordx4 v201, s[44:45] offset:0 nt\n\t" \
    "s_add_u32 m0, s96, 37760\n\t" \
    "s_nop 0\n\t" \
    "global_load_lds_dwordx4 v201, s[44:45] offset:128 nt\n\t" \
    "s_add_u32 m0, s96, 38656\n\t" \
    "s_nop 0\n\t" \
    "global_load_lds_dwordx4 v201, s[44:45] offset:256 nt\n\t" \
    "s_add_u32 m0, s96, 39552\n\t" \
    "s_nop 0\n\t" \
    "global_load_lds_dwordx4 v201, s[44:45] offset:384 nt\n\t" \
    "s_add_u32 m0, s96, 40448\n\t" \
    "s_nop 0\n\t" \
    "global_load_lds_dwordx4 v201, s[44:45] offset:512 nt\n\t" \
    "s_add_u32 m0, s96, 41344\n\t" \
    "s_nop 0\n\t" \
    "global_load_lds_dwordx4 v201, s[44:45] offset:640 nt\n\t" \
    "s_add_u32 m0, s96, 42240\n\t" \
    "s_nop 0\n\t" \
    "global_load_lds_dwordx4 v201, s[44:45] offset:768 nt\n\t" \
    "s_add_u32 m0, s96, 43136\n\t" \
    "s_nop 0\n\t" \
    "global_load_lds_dwordx4 v201, s[44:45] offset:896 nt\n\t" \
    "s_add_u32 m0, s96, 44032\n\t" \
    "s_nop 0\n\t" \
    "global_load_lds_dwordx4 v201, s[44:45] offset:1024 nt\n\t" \
    "v_add_u32 v201, 36864, v201\n\t" \
    "ds_read_b64_tr_b16 a[144:145], v204 offset:0\n\t" \
    "ds_read_b64_tr_b16 a[146:147], v204 offset:18432\n\t" \
    "ds_read_b64_tr_b16 a[160:161], v205 offset:0\n\t" \
    "ds_read_b64_tr_b16 a[162:163], v205 offset:18432\n\t" \
    "ds_read_b64_tr_b16 a[176:177], v206 offset:0\n\t" \
    "ds_read_b64_tr_b16 a[178:179], v206 offset:18432\n\t" \
    "s_waitcnt lgkmcnt(6)\n\t" \
    "ds_read_b64_tr_b16 a[192:193], v207 offset:0\n\t" \
    "ds_read_b64_tr_b16 a[194:195], v207 offset:18432\n\t" \
    "ds_read_b64_tr_b16 a[208:209], v204 offset:1024\n\t" \
    "ds_read_b64_tr_b16 a[210:211], v204 offset:19456\n\t" \
    "ds_read_b64_tr_b16 a[224:225], v205 offset:1024\n\t" \
    "ds_read_b64_tr_b16 a[226:227], v205 offset:19456\n\t" \
    "s_waitcnt lgkmcnt(6)\n\t" \
    "ds_read_b64_tr_b16 a[240:241], v206 offset:1024\n\t" \
    "ds_read_b64_tr_b16 a[242:243], v206 offset:19456\n\t" \
    "ds_read_b64_tr_b16 v[224:225], v207 offset:1024\n\t" \
    "ds_read_b64_tr_b16 v[226:227], v207 offset:19456\n\t" \
    "ds_read_b64_tr_b16 v[240:241], v204 offset:2048\n\t" \
    "ds_read_b64_tr_b16 v[242:243], v204 offset:20480\n\t" \
    "s_waitcnt lgkmcnt(6)\n\t" \
    "s_waitcnt vmcnt(18) lgkmcnt(0)\n\t" \
    "s_barrier\n\t" \
    "v_mbcnt_lo_u32_b32 v140, -1, 0\n\t" \
    "v_mbcnt_hi_u32_b32 v140, -1, v140\n\t" \
    "v_min_u32 v140, 59, v140\n\t" \
    "v_lshlrev_b32 v140, 6, v140\n\t" \
    "v_add_u32 v140, 6258688, v140\n\t" \
    "v_mov_b32 v141, 6266880\n\t" \
    "global_load_dword v141, v141, s[36:37] sc1\n\t" \
    "global_load_dwordx2 v[142:143], v140, s[36:37] sc1\n\t" \
    "v_add_u32 v204, 73728, v204\n\t" \
    "v_add_u32 v205, 73728, v205\n\t" \
    "v_add_u32 v206, 73728, v206\n\t" \
    "v_add_u32 v207, 73728, v207\n\t" \
    "ds_read_b64_tr_b16 a[148:149], v204 offset:0\n\t" \
    "ds_read_b64_tr_b16 a[150:151], v204 offset:18432\n\t" \
    "ds_read_b64_tr_b16 a[164:165], v205 offset:0\n\t" \
    "ds_read_b64_tr_b16 a[166:167], v205 offset:18432\n\t" \
    "ds_read_b64_tr_b16 a[180:181], v206 offset:0\n\t" \
    "ds_read_b64_tr_b16 a[182:183], v206 offset:18432\n\t" \
    "s_waitcnt lgkmcnt(6)\n\t" \
    "ds_read_b64_tr_b16 a[196:197], v207 offset:0\n\t" \
    "ds_read_b64_tr_b16 a[198:199], v207 offset:18432\n\t" \
    "ds_read_b64_tr_b16 a[212:213], v204 offset:1024\n\t" \
    "ds_read_b64_tr_b16 a[214:215], v204 offset:19456\n\t" \
    "ds_read_b64_tr_b16 a[228:229], v205 offset:1024\n\t" \
    "ds_read_b64_tr_b16 a[230:231], v205 offset:19456\n\t" \
    "s_waitcnt lgkmcnt(6)\n\t" \
    "ds_read_b64_tr_b16 a[244:245], v206 offset:1024\n\t" \
    "ds_read_b64_tr_b16 a[246:247], v206 offset:19456\n\t" \
    "ds_read_b64_tr_b16 v[228:229], v207 offset:1024\n\t" \
    "ds_read_b64_tr_b16 v[230:231], v207 offset:19456\n\t" \
    "ds_read_b64_tr_b16 v[244:245], v204 offset:2048\n\t" \
    "ds_read_b64_tr_b16 v[246:247], v204 offset:20480\n\t" \
    "s_waitcnt lgkmcnt(6)\n\t" \
    "s_waitcnt vmcnt(11) lgkmcnt(0)\n\t" \
    "s_barrier\n\t" \
    "ds_read_b64_tr_b16 a[152:153], v204 offset:36864\n\t" \
    "ds_read_b64_tr_b16 a[154:155], v204 offset:55296\n\t" \
    "ds_read_b64_tr_b16 a[168:169], v205 offset:36864\n\t" \
    "ds_read_b64_tr_b16 a[170:171], v205 offset:55296\n\t" \
    "ds_read_b64_tr_b16 a[184:185], v206 offset:36864\n\t" \
    "ds_read_b64_tr_b16 a[186:187], v206 offset:55296\n\t" \
    "s_waitcnt lgkmcnt(6)\n\t" \
    "ds_read_b64_tr_b16 a[200:201], v207 offset:36864\n\t" \
    "ds_read_b64_tr_b16 a[202:203], v207 offset:55296\n\t" \
    "ds_read_b64_tr_b16 a[216:217], v204 offset:37888\n\t" \
    "ds_read_b64_tr_b16 a[218:219], v204 offset:56320\n\t" \
    "ds_read_b64_tr_b16 a[232:233], v205 offset:37888\n\t" \
    "ds_read_b64_tr_b16 a[234:235], v205 offset:56320\n\t" \
    "s_waitcnt lgkmcnt(6)\n\t" \
    "ds_read_b64_tr_b16 a[248:249], v206 offset:37888\n\t" \
    "ds_read_b64_tr_b16 a[250:251], v206 offset:56320\n\t" \
    "ds_read_b64_tr_b16 v[232:233], v207 offset:37888\n\t" \
    "ds_read_b64_tr_b16 v[234:235], v207 offset:56320\n\t" \
    "ds_read_b64_tr_b16 v[248:249], v204 offset:38912\n\t" \
    "ds_read_b64_tr_b16 v[250:251], v204 offset:57344\n\t" \
    "s_waitcnt lgkmcnt(6)\n\t" \
    "s_waitcnt vmcnt(2) lgkmcnt(0)\n\t" \
    "s_barrier\n\t" \
    "v_add_u32 v204, -36864, v204\n\t" \
    "v_add_u32 v205, -36864, v205\n\t" \
    "v_add_u32 v206, -36864, v206\n\t" \
    "v_add_u32 v207, -36864, v207\n\t" \
    "ds_read_b64_tr_b16 a[156:157], v204 offset:0\n\t" \
    "ds_read_b64_tr_b16 a[158:159], v204 offset:18432\n\t" \
    "ds_read_b64_tr_b16 a[172:173], v205 offset:0\n\t" \
    "ds_read_b64_tr_b16 a[174:175], v205 offset:18432\n\t" \
    "ds_read_b64_tr_b16 a[188:189], v206 offset:0\n\t" \
    "ds_read_b64_tr_b16 a[190:191], v206 offset:18432\n\t" \
    "s_waitcnt lgkmcnt(6)\n\t" \
    "ds_read_b64_tr_b16 a[204:205], v207 offset:0\n\t" \
    "ds_read_b64_tr_b16 a[206:207], v207 offset:18432\n\t" \
    "ds_read_b64_tr_b16 a[220:221], v204 offset:1024\n\t" \
    "ds_read_b64_tr_b16 a[222:223], v204 offset:19456\n\t" \
    "ds_read_b64_tr_b16 a[236:237], v205 offset:1024\n\t" \
    "ds_read_b64_tr_b16 a[238:239], v205 offset:19456\n\t" \
    "s_waitcnt lgkmcnt(6)\n\t" \
    "ds_read_b64_tr_b16 a[252:253], v206 offset:1024\n\t" \
    "ds_read_b64_tr_b16 a[254:255], v206 offset:19456\n\t" \
    "ds_read_b64_tr_b16 v[236:237], v207 offset:1024\n\t" \
    "ds_read_b64_tr_b16 v[238:239], v207 offset:19456\n\t" \
    "ds_read_b64_tr_b16 v[252:253], v204 offset:2048\n\t" \
    "ds_read_b64_tr_b16 v[254:255], v204 offset:20480\n\t" \
    "s_waitcnt lgkmcnt(6)\n\t" \
    "s_waitcnt vmcnt(0) lgkmcnt(0)\n\t" \
    "s_barrier\n\t" \
    "v_sub_u32 v142, v142, v141\n\t" \
    "v_cmp_ne_u32 vcc, 1, v142\n\t" \
    "s_cbranch_vccz vv_Cok4_%=\n\t" \
    "vv_C3_%=:\n\t" \
    "global_load_dwordx2 v[142:143], v140, s[36:37] sc1\n\t" \
    "s_waitcnt vmcnt(0)\n\t" \
    "v_sub_u32 v142, v142, v141\n\t" \
    "v_cmp_ne_u32 vcc, 1, v142\n\t" \
    "s_cbranch_vccz vv_Cok4_%=\n\t" \
    "s_cmp_eq_u32 s68, 0\n\t" \
    "s_cbranch_scc1 vv_F5_%=\n\t" \
    "s_sub_u32 s68, s68, 1\n\t" \
    "s_sleep 4\n\t" \
    "s_branch vv_C3_%=\n\t" \
    "vv_F5_%=:\n\t" \
    "v_mov_b32 v144, 6095104\n\t" \
    "v_mov_b32 v145, 4\n\t" \
    "global_store_dword v144, v145, s[36:37] sc1\n\t" \
    "s_waitcnt vmcnt(0)\n\t" \
    "s_branch vv_Cok4_%=\n\t" \
    "vv_Cok4_%=:\n\t" \
    "s_cmp_lg_u32 s66, 0\n\t" \
    "s_cbranch_scc1 vv_Ca6_%=\n\t" \
    "v_mov_b32 v140, 6176768\n\t" \
    "s_mov_b64 exec, 1\n\t" \
    "global_atomic_add v140, v142, s[36:37] sc1\n\t" \
    "s_mov_b64 exec, -1\n\t" \
    "vv_Ca6_%=:\n\t" \
    "v_min_u32_dpp v143, v143, v143 row_shl:6 row_mask:0xf bank_mask:0xf\n\t" \
    "s_nop 0\n\t" \
    "v_readlane_b32 s69, v143, 0\n\t" \
    "v_readlane_b32 s70, v143, 1\n\t" \
    "s_nop 1\n\t" \
    "s_cmp_lt_u32 s70, s69\n\t" \
    "s_cselect_b32 s62, 8192, 0\n\t" \
    "s_cselect_b32 s69, s70, s69\n\t" \
    "s_mov_b32 s71, s69\n\t" \
    "v_readlane_b32 s69, v143, 2\n\t" \
    "v_readlane_b32 s70, v143, 3\n\t" \
    "s_nop 1\n\t" \
    "s_cmp_lt_u32 s70, s69\n\t" \
    "s_cselect_b32 s63, 8192, 0\n\t" \
    "s_cselect_b32 s69, s70, s69\n\t" \
    "s_max_u32 s71, s69, s71\n\t" \
    "v_readlane_b32 s69, v143, 4\n\t" \
    "v_readlane_b32 s70, v143, 5\n\t" \
    "s_nop 1\n\t" \
    "s_cmp_lt_u32 s70, s69\n\t" \
    "s_cselect_b32 s64, 8192, 0\n\t" \
    "s_cselect_b32 s69, s70, s69\n\t" \
    "s_max_u32 s71, s69, s71\n\t" \
    "s_add_u32 s63, s63, 0x200000\n\t" \
    "s_xor_b32 s52, s64, 8192\n\t" \
    "s_cmp_gt_u32 s71, 625\n\t" \
    "s_cselect_b32 s69, 1, 0\n\t" \
    "s_mul_i32 s70, s69, 160\n\t" \
    "s_add_u32 s86, s86, s70\n\t" \
    "s_mul_i32 s70, s69, 536\n\t" \
    "s_add_u32 s87, s87, s70\n\t" \
    "s_mul_i32 s70, s69, 256\n\t" \
    "s_add_u32 s88, s88, s70\n\t" \
    "v_mov_b32 v148, -1\n\t" \
    "v_mov_b32 v149, -1\n\t" \
    "v_mov_b32 v150, -1\n\t" \
    "v_mov_b32 v151, -1\n\t" \
    "s_mov_b32 s71, 3\n\t" \
    "vv_E7_%=:\n\t" \
    "s_memtime s[54:55]\n\t" \
    "s_nop 0\n\t" \
    "s_memrealtime s[4:5]\n\t" \
    "s_nop 0\n\t" \
    "s_memrealtime s[6:7]\n\t" \
    "s_nop 0\n\t" \
    "s_memrealtime s[8:9]\n\t" \
    "s_nop 0\n\t" \
    "s_memrealtime s[10:11]\n\t" \
    "s_nop 0\n\t" \
    "s_memrealtime s[12:13]\n\t" \
    "s_nop 0\n\t" \
    "s_memrealtime s[14:15]\n\t" \
    "s_nop 0\n\t" \
    "s_memrealtime s[16:17]\n\t" \
    "s_nop 0\n\t" \
    "s_memrealtime s[18:19]\n\t" \
    "s_nop 0\n\t" \
    "s_memrealtime s[20:21]\n\t" \
    "s_nop 0\n\t" \
    "s_memrealtime s[22:23]\n\t" \
    "s_nop 0\n\t" \
    "s_memrealtime s[24:25]\n\t" \
    "s_nop 0\n\t" \
    "s_memrealtime s[26:27]\n\t" \
    "s_nop 0\n\t" \
    "s_memrealtime s[28:29]\n\t" \
    "s_nop 0\n\t" \
    "s_memtime s[56:57]\n\t" \
    "s_mov_b32 s70, 1\n\t" \
    "s_sub_u32 s71, s71, 1\n\t" \
    "s_cselect_b32 s69, 0, -1\n\t" \
    "s_waitcnt lgkmcnt(0)\n\t" \
    "s_sub_u32 s54, s56, s54\n\t" \
    "s_and_b32 s54, s54, s69\n\t" \
    "s_cmp_gt_u32 s54, 124\n\t" \
    "s_cbranch_scc1 vv_E7_%=\n\t" \
    "s_cmp_eq_u32 s4, s28\n\t" \
    "s_addc_u32 s70, s70, 0\n\t" \
    "s_cmp_eq_u32 s6, s28\n\t" \
    "s_addc_u32 s70, s70, 0\n\t" \
    "s_cmp_eq_u32 s8, s28\n\t" \
    "s_addc_u32 s70, s70, 0\n\t" \
    "s_cmp_eq_u32 s10, s28\n\t" \
    "s_addc_u32 s70, s70, 0\n\t" \
    "s_cmp_eq_u32 s12, s28\n\t" \
    "s_addc_u32 s70, s70, 0\n\t" \
    "s_cmp_eq_u32 s14, s28\n\t" \
    "s_addc_u32 s70, s70, 0\n\t" \
    "s_cmp_eq_u32 s16, s28\n\t" \
    "s_addc_u32 s70, s70, 0\n\t" \
    "s_cmp_eq_u32 s18, s28\n\t" \
    "s_addc_u32 s70, s70, 0\n\t" \
    "s_cmp_eq_u32 s20, s28\n\t" \
    "s_addc_u32 s70, s70, 0\n\t" \
    "s_cmp_eq_u32 s22, s28\n\t" \
    "s_addc_u32 s70, s70, 0\n\t" \
    "s_cmp_eq_u32 s24, s28\n\t" \
    "s_addc_u32 s70, s70, 0\n\t" \
    "s_cmp_eq_u32 s26, s28\n\t" \
    "s_addc_u32 s70, s70, 0\n\t" \
    "s_mov_b32 s75, s73\n\t" \
    "s_mov_b32 s76, s74\n\t" \
    "s_lshl_b32 s70, s70, 3\n\t" \
    "s_sub_u32 s73, s56, s70\n\t" \
    "s_lshl_b32 s74, s28, 4\n\t" \
    "s_sub_u32 s69, s73, s75\n\t" \
    "s_sub_u32 s70, s74, s76\n\t" \
    "s_sub_u32 s71, s70, 1600\n\t" \
    "s_cmp_lt_u32 s71, 400000\n\t" \
    "s_cbranch_scc0 vv_K9_%=\n\t" \
    "v_cvt_f32_u32 v140, s69\n\t" \
    "v_cvt_f32_u32 v141, s70\n\t" \
    "v_rcp_f32 v142, v140\n\t" \
    "v_rcp_f32 v143, v141\n\t" \
    "v_mul_f32 v142, v142, v141\n\t" \
    "v_mul_f32 v143, v143, v140\n\t" \
    "v_mul_f32 v142, 0x4f800000, v142\n\t" \
    "v_mul_f32 v143, 0x4f000000, v143\n\t" \
    "v_cvt_u32_f32 v142, v142\n\t" \
    "v_cvt_u32_f32 v143, v143\n\t" \
    "v_readfirstlane_b32 s69, v142\n\t" \
    "v_readfirstlane_b32 s70, v143\n\t" \
    "s_nop 1\n\t" \
    "s_mov_b32 s78, s69\n\t" \
    "s_mov_b32 s77, s70\n\t" \
    "vv_K9_%=:\n\t" \
    "s_mov_b32 s79, s74\n\t" \
    "s_sub_u32 s80, s74, 32000\n\t" \
    "s_cmp_eq_u32 s66, 1\n\t" \
    "s_cbranch_scc1 vv_loop11_%=\n\t" \
    "s_cmp_eq_u32 s66, 2\n\t" \
    "s_cbranch_scc1 vv_loop12_%=\n\t" \
    "s_cmp_eq_u32 s66, 3\n\t" \
    "s_cbranch_scc1 vv_loop13_%=\n\t" \
    "vv_loop10_%=:\n\t" \
    "v_mov_b32 v72, 0\n\t" \
    "v_mov_b32 v73, 0\n\t" \
    "v_mov_b32 v74, 0\n\t" \
    "v_mov_b32 v75, 0\n\t" \
    "v_mov_b32 v76, 0\n\t" \
    "v_mov_b32 v77, 0\n\t" \
    "v_mov_b32 v78, 0\n\t" \
    "v_mov_b32 v79, 0\n\t" \
    "v_mov_b32 v92, 0\n\t" \
    "v_mov_b32 v93, 0\n\t" \
    "v_mov_b32 v94, 0\n\t" \
    "v_mov_b32 v95, 0\n\t" \
    "v_mov_b32 v96, 0\n\t" \
    "v_mov_b32 v97, 0\n\t" \
    "v_mov_b32 v98, 0\n\t" \
    "v_mov_b32 v99, 0\n\t" \
    "v_mov_b32 v100, 0\n\t" \
    "v_mov_b32 v101, 0\n\t" \
    "v_mov_b32 v102, 0\n\t" \
    "v_mov_b32 v103, 0\n\t" \
    "v_mov_b32 v104, 0\n\t" \
    "v_mov_b32 v105, 0\n\t" \
    "v_mov_b32 v106, 0\n\t" \
    "v_mov_b32 v107, 0\n\t" \
    "v_mov_b32 v108, 0\n\t" \
    "v_mov_b32 v109, 0\n\t" \
    "v_mov_b32 v110, 0\n\t" \
    "v_mov_b32 v111, 0\n\t" \
    "v_mov_b32 v112, 0\n\t" \
    "v_mov_b32 v113, 0\n\t" \
    "v_mov_b32 v114, 0\n\t" \
    "v_mov_b32 v115, 0\n\t" \
    "v_mov_b32 v116, 0\n\t" \
    "v_mov_b32 v117, 0\n\t" \
    "v_mov_b32 v118, 0\n\t" \
    "v_mov_b32 v119, 0\n\t" \
    "v_mov_b32 v120, 0\n\t" \
    "v_mov_b32 v121, 0\n\t" \
    "v_mov_b32 v122, 0\n\t" \
    "v_mov_b32 v123, 0\n\t" \
    "v_mov_b32 v124, 0\n\t" \
    "v_mov_b32 v125, 0\n\t" \
    "v_mov_b32 v126, 0\n\t" \
    "v_mov_b32 v127, 0\n\t" \
    "vv_go15_%=:\n\t" \
    "s_add_u32 s69, s65, 0\n\t" \
    "s_bitcmp1_b32 s69, 0\n\t" \
    "s_cselect_b32 s70, s63, s62\n\t" \
    "s_bfe_u32 s69, s69, 0x20001\n\t" \
    "s_lshl_b32 s69, s69, 19\n\t" \
    "s_add_u32 s69, s69, s70\n\t" \
    "s_add_u32 s46, s36, s69\n\t" \
    "s_addc_u32 s47, s37, 0\n\t" \
    "s_add_u32 s69, s65, 0\n\t" \
    "s_mul_i32 s70, s69, 171\n\t" \
    "s_lshr_b32 s70, s70, 9\n\t" \
    "s_mul_i32 s70, s70, 3\n\t" \
    "s_sub_u32 s69, s69, s70\n\t" \
    "s_mul_i32 s69, s69, 36\n\t" \
    "s_lshl_b32 s69, s69, 14\n\t" \
    "s_add_u32 s69, s69, s64\n\t" \
    "s_add_u32 s69, s69, 4194304\n\t" \
    "s_add_u32 s38, s36, s69\n\t" \
    "s_addc_u32 s39, s37, 0\n\t" \
    "s_mul_i32 s69, s65, 9216\n\t" \
    "s_add_u32 s69, s69, 123731968\n\t" \
    "s_add_u32 s40, s36, s69\n\t" \
    "s_addc_u32 s41, s37, 0\n\t" \
    "s_sub_u32 s70, s79, s74\n\t" \
    "s_max_i32 s70, s70, 0\n\t" \
    "s_min_i32 s70, s70, 16000\n\t" \
    "s_lshl_b32 s70, s70, 1\n\t" \
    "s_mul_hi_u32 s70, s70, s77\n\t" \
    "s_add_u32 s70, s70, s73\n\t" \
    "s_sub_u32 s70, s70, 100\n\t" \
    "s_getpc_b64 s[30:31]\n\t" \
    "vv_Wp17_%=:\n\t" \
    "s_add_u32 s30, s30, vv_We18_%=-vv_Wp17_%=\n\t" \
    "s_addc_u32 s31, s31, 0\n\t" \
    "vv_W16_%=:\n\t" \
    "s_memtime s[56:57]\n\t" \
    "s_waitcnt lgkmcnt(0)\n\t" \
    "s_sub_u32 s69, s70, s56\n\t" \
    "s_cmp_gt_i32 s69, 111\n\t" \
    "s_cbranch_scc1 vv_W16_%=\n\t" \
    "s_max_i32 s69, s69, 0\n\t" \
    "s_lshr_b32 s69, s69, 1\n\t" \
    "s_and_b32 s69, s69, 0x3c\n\t" \
    "s_sub_u32 s30, s30, s69\n\t" \
    "s_subb_u32 s31, s31, 0\n\t" \
    "s_setpc_b64 s[30:31]\n\t" \
    ".p2align 6\n\t" \
    "s_nop 0\n\t" \
    "s_nop 0\n\t" \
    "s_nop 0\n\t" \
    "s_nop 1\n\t" \
    "s_nop 1\n\t" \
    "s_nop 1\n\t" \
    "s_nop 1\n\t" \
    "s_nop 1\n\t" \
    "s_nop 1\n\t" \
    "s_nop 1\n\t" \
    "s_nop 1\n\t" \
    "s_nop 1\n\t" \
    "s_nop 1\n\t" \
    "s_nop 1\n\t" \
    "s_nop 1\n\t" \
    "s_nop 1\n\t" \
    "vv_We18_%=:\n\t" \
    "vv_G19_%=:\n\t" \
    "global_load_dwordx4 v[36:39], v1, s[46:47] sc1\n\t" \
    "s_nop 9\n\t" \
    "global_load_dwordx4 v[40:43], v1, s[46:47] offset:1024 sc1\n\t" \
    "s_nop 9\n\t" \
    "global_load_dwordx4 v[44:47], v2, s[46:47] sc1\n\t" \
    "s_waitcnt vmcnt(2)\n\t" \
    "v_smfmac_f32_16x16x64_bf16 v[72:75], v[36:39], a[0:7], v10\n\t" \
    "ds_write_b128 v0, v[36:39] offset:0\n\t" \
    "v_smfmac_f32_16x16x64_bf16 v[76:79], v[36:39], a[72:79], v10\n\t" \
    "v_max3_u32 v140, v36, v37, v38\n\t" \
    "s_waitcnt vmcnt(1)\n\t" \
    "ds_write_b128 v0, v[40:43] offset:1024\n\t" \
    "v_smfmac_f32_16x16x64_bf16 v[72:75], v[40:43], a[8:15], v10\n\t" \
    "v_max3_u32 v140, v140, v39, v40\n\t" \
    "v_max3_u32 v140, v140, v41, v42\n\t" \
    "v_max_u32 v140, v140, v43\n\t" \
    "v_cmp_eq_u32 vcc, -1, v140\n\t" \
    "v_smfmac_f32_16x16x64_bf16 v[76:79], v[40:43], a[80:87], v10\n\t" \
    "s_cbranch_vccnz vv_Gbad20_%=\n\t" \
    "vv_Gc22_%=:\n\t" \
    "s_waitcnt lgkmcnt(0)\n\t" \
    "s_barrier\n\t" \
    "ds_read_b128 v[48:51], v0 offset:2048\n\t" \
    "ds_read_b128 v[52:55], v0 offset:3072\n\t" \
    "s_waitcnt vmcnt(0)\n\t" \
    "v_smfmac_f32_16x16x64_bf16 v[72:75], v[44:47], a[64:71], v10\n\t" \
    "ds_read_b128 v[56:59], v0 offset:4096\n\t" \
    "ds_read_b128 v[60:63], v0 offset:5120\n\t" \
    "v_max3_u32 v140, v44, v45, v46\n\t" \
    "v_max_u32 v140, v140, v47\n\t" \
    "v_smfmac_f32_16x16x64_bf16 v[76:79], v[44:47], a[136:143], v10\n\t" \
    "ds_read_b128 v[64:67], v0 offset:6144\n\t" \
    "ds_read_b128 v[68:71], v0 offset:7168\n\t" \
    "v_cmp_eq_u32 vcc, -1, v140\n\t" \
    "s_cbranch_vccnz vv_Gbad221_%=\n\t" \
    "vv_Gc223_%=:\n\t" \
    "s_waitcnt lgkmcnt(5)\n\t" \
    "v_smfmac_f32_16x16x64_bf16 v[72:75], v[48:51], a[16:23], v10\n\t" \
    "s_waitcnt lgkmcnt(4)\n\t" \
    "v_smfmac_f32_16x16x64_bf16 v[72:75], v[52:55], a[24:31], v10\n\t" \
    "s_waitcnt lgkmcnt(3)\n\t" \
    "v_smfmac_f32_16x16x64_bf16 v[72:75], v[56:59], a[32:39], v10\n\t" \
    "s_waitcnt lgkmcnt(2)\n\t" \
    "v_smfmac_f32_16x16x64_bf16 v[72:75], v[60:63], a[40:47], v10\n\t" \
    "s_waitcnt lgkmcnt(1)\n\t" \
    "v_smfmac_f32_16x16x64_bf16 v[72:75], v[64:67], a[48:55], v10\n\t" \
    "s_waitcnt lgkmcnt(0)\n\t" \
    "v_smfmac_f32_16x16x64_bf16 v[72:75], v[68:71], a[56:63], v10\n\t" \
    "v_smfmac_f32_16x16x64_bf16 v[76:79], v[48:51], a[88:95], v10\n\t" \
    "s_nop 1\n\t" \
    "s_nop 0\n\t" \
    "v_smfmac_f32_16x16x64_bf16 v[76:79], v[52:55], a[96:103], v10\n\t" \
    "s_nop 1\n\t" \
    "s_nop 0\n\t" \
    "v_smfmac_f32_16x16x64_bf16 v[76:79], v[56:59], a[104:111], v10\n\t" \
    "v_add_f32 v72, v72, v74\n\t" \
    "v_smfmac_f32_16x16x64_bf16 v[76:79], v[60:63], a[112:119], v10\n\t" \
    "v_add_f32 v73, v73, v75\n\t" \
    "v_smfmac_f32_16x16x64_bf16 v[76:79], v[64:67], a[120:127], v10\n\t" \
    "v_cvt_pk_bf16_f32 v80, v72, v73\n\t" \
    "v_smfmac_f32_16x16x64_bf16 v[76:79], v[68:71], a[128:135], v10\n\t" \
    "ds_write_b32 v12, v80 offset:0\n\t" \
    "s_nop 6\n\t" \
    "v_add_f32 v76, v76, v78\n\t" \
    "v_add_f32 v77, v77, v79\n\t" \
    "v_cvt_pk_bf16_f32 v81, v76, v77\n\t" \
    "ds_write_b32 v12, v81 offset:64\n\t" \
    "s_waitcnt lgkmcnt(0)\n\t" \
    "s_barrier\n\t" \
    "ds_read_b64_tr_b16 v[84:85], v13 offset:0\n\t" \
    "ds_read_b64_tr_b16 v[86:87], v13 offset:128\n\t" \
    "ds_read_b64_tr_b16 v[88:89], v13 offset:1024\n\t" \
    "ds_read_b64_tr_b16 v[90:91], v13 offset:1152\n\t" \
    "s_waitcnt lgkmcnt(2)\n\t" \
    "v_smfmac_f32_16x16x64_bf16 v[92:95], v[84:87], a[144:151], v10\n\t" \
    "s_nop 2\n\t" \
    "s_waitcnt lgkmcnt(0)\n\t" \
    "v_smfmac_f32_16x16x64_bf16 v[92:95], v[88:91], a[152:159], v10\n\t" \
    "s_nop 2\n\t" \
    "v_smfmac_f32_16x16x64_bf16 v[96:99], v[84:87], a[160:167], v10\n\t" \
    "s_nop 2\n\t" \
    "v_smfmac_f32_16x16x64_bf16 v[96:99], v[88:91], a[168:175], v10\n\t" \
    "s_nop 2\n\t" \
    "v_smfmac_f32_16x16x64_bf16 v[100:103], v[84:87], a[176:183], v10\n\t" \
    "s_nop 0\n\t" \
    "v_add_f32 v92, v92, v94\n\t" \
    "v_add_f32 v93, v93, v95\n\t" \
    "v_smfmac_f32_16x16x64_bf16 v[100:103], v[88:91], a[184:191], v10\n\t" \
    "s_nop 1\n\t" \
    "v_cvt_pk_bf16_f32 v128, v92, v93\n\t" \
    "v_smfmac_f32_16x16x64_bf16 v[104:107], v[84:87], a[192:199], v10\n\t" \
    "s_nop 0\n\t" \
    "v_add_f32 v96, v96, v98\n\t" \
    "v_add_f32 v97, v97, v99\n\t" \
    "v_smfmac_f32_16x16x64_bf16 v[104:107], v[88:91], a[200:207], v10\n\t" \
    "s_nop 1\n\t" \
    "v_cvt_pk_bf16_f32 v129, v96, v97\n\t" \
    "v_smfmac_f32_16x16x64_bf16 v[124:127], v[84:87], v[240:247], v10\n\t" \
    "s_nop 0\n\t" \
    "v_add_f32 v100, v100, v102\n\t" \
    "v_add_f32 v101, v101, v103\n\t" \
    "v_smfmac_f32_16x16x64_bf16 v[124:127], v[88:91], v[248:255], v10\n\t" \
    "s_nop 1\n\t" \
    "global_load_dword v140, v20, s[46:47] sc1\n\t" \
    "v_cvt_pk_bf16_f32 v130, v100, v101\n\t" \
    "v_smfmac_f32_16x16x64_bf16 v[108:111], v[84:87], a[208:215], v10\n\t" \
    "s_nop 0\n\t" \
    "v_add_f32 v104, v104, v106\n\t" \
    "v_add_f32 v105, v105, v107\n\t" \
    "v_smfmac_f32_16x16x64_bf16 v[108:111], v[88:91], a[216:223], v10\n\t" \
    "s_nop 0\n\t" \
    "v_cvt_pk_bf16_f32 v131, v104, v105\n\t" \
    "v_permlane32_swap_b32 v128, v130\n\t" \
    "v_smfmac_f32_16x16x64_bf16 v[112:115], v[84:87], a[224:231], v10\n\t" \
    "s_nop 0\n\t" \
    "v_permlane32_swap_b32 v129, v131\n\t" \
    "s_nop 1\n\t" \
    "v_permlane16_swap_b32 v128, v129\n\t" \
    "v_smfmac_f32_16x16x64_bf16 v[112:115], v[88:91], a[232:239], v10\n\t" \
    "v_permlane16_swap_b32 v130, v131\n\t" \
    "global_store_dwordx4 v14, v[128:131], s[38:39] sc1\n\t" \
    "v_add_f32 v124, v124, v126\n\t" \
    "v_smfmac_f32_16x16x64_bf16 v[116:119], v[84:87], a[240:247], v10\n\t" \
    "s_nop 0\n\t" \
    "v_add_f32 v125, v125, v127\n\t" \
    "v_cvt_pk_bf16_f32 v136, v124, v125\n\t" \
    "v_smfmac_f32_16x16x64_bf16 v[116:119], v[88:91], a[248:255], v10\n\t" \
    "global_store_dword v16, v136, s[38:39] sc1\n\t" \
    "v_add_f32 v108, v108, v110\n\t" \
    "v_add_f32 v109, v109, v111\n\t" \
    "v_smfmac_f32_16x16x64_bf16 v[120:123], v[84:87], v[224:231], v10\n\t" \
    "s_nop 0\n\t" \
    "v_cvt_pk_bf16_f32 v132, v108, v109\n\t" \
    "v_add_f32 v112, v112, v114\n\t" \
    "v_smfmac_f32_16x16x64_bf16 v[120:123], v[88:91], v[232:239], v10\n\t" \
    "v_add_f32 v113, v113, v115\n\t" \
    "v_cvt_pk_bf16_f32 v133, v112, v113\n\t" \
    "v_add_f32 v116, v116, v118\n\t" \
    "v_add_f32 v117, v117, v119\n\t" \
    "v_cvt_pk_bf16_f32 v134, v116, v117\n\t" \
    "s_nop 2\n\t" \
    "v_pk_add_f32 v[120:121], v[120:121], v[122:123]\n\t" \
    "v_cvt_pk_bf16_f32 v135, v120, v121\n\t" \
    "v_permlane32_swap_b32 v132, v134\n\t" \
    "s_nop 0\n\t" \
    "v_permlane32_swap_b32 v133, v135\n\t" \
    "s_nop 1\n\t" \
    "v_permlane16_swap_b32 v132, v133\n\t" \
    "v_permlane16_swap_b32 v134, v135\n\t" \
    "global_store_dwordx4 v15, v[132:135], s[38:39] sc1\n\t" \
    "s_nop 1\n\t" \
    "global_store_dwordx4 v17, v[128:131], s[40:41] sc1\n\t" \
    "global_store_dwordx4 v17, v[132:135], s[40:41] offset:1024 sc1\n\t" \
    "global_store_dword v18, v136, s[40:41] sc1\n\t" \
    "s_cmp_eq_u32 s65, 63\n\t" \
    "s_cbranch_scc1 vv_end14_%=\n\t" \
    "s_mov_b32 s71, 3\n\t" \
    "vv_E25_%=:\n\t" \
    "s_memtime s[54:55]\n\t" \
    "s_nop 0\n\t" \
    "s_memrealtime s[4:5]\n\t" \
    "s_nop 0\n\t" \
    "s_memrealtime s[6:7]\n\t" \
    "s_nop 0\n\t" \
    "s_memrealtime s[8:9]\n\t" \
    "s_nop 0\n\t" \
    "s_memrealtime s[10:11]\n\t" \
    "s_nop 0\n\t" \
    "s_memrealtime s[12:13]\n\t" \
    "s_nop 0\n\t" \
    "s_memrealtime s[14:15]\n\t" \
    "s_nop 0\n\t" \
    "s_memrealtime s[16:17]\n\t" \
    "s_nop 0\n\t" \
    "s_memrealtime s[18:19]\n\t" \
    "s_nop 0\n\t" \
    "s_memrealtime s[20:21]\n\t" \
    "s_nop 0\n\t" \
    "s_memrealtime s[22:23]\n\t" \
    "s_nop 0\n\t" \
    "s_memrealtime s[24:25]\n\t" \
    "s_nop 0\n\t" \
    "s_memrealtime s[26:27]\n\t" \
    "s_nop 0\n\t" \
    "s_memrealtime s[28:29]\n\t" \
    "s_nop 0\n\t" \
    "s_memtime s[56:57]\n\t" \
    "s_mov_b32 s70, 1\n\t" \
    "s_sub_u32 s71, s71, 1\n\t" \
    "s_cselect_b32 s69, 0, -1\n\t" \
    "s_waitcnt lgkmcnt(0)\n\t" \
    "s_sub_u32 s54, s56, s54\n\t" \
    "s_and_b32 s54, s54, s69\n\t" \
    "s_cmp_gt_u32 s54, 124\n\t" \
    "s_cbranch_scc1 vv_E25_%=\n\t" \
    "s_cmp_eq_u32 s4, s28\n\t" \
    "s_addc_u32 s70, s70, 0\n\t" \
    "s_cmp_eq_u32 s6, s28\n\t" \
    "s_addc_u32 s70, s70, 0\n\t" \
    "s_cmp_eq_u32 s8, s28\n\t" \
    "s_addc_u32 s70, s70, 0\n\t" \
    "s_cmp_eq_u32 s10, s28\n\t" \
    "s_addc_u32 s70, s70, 0\n\t" \
    "s_cmp_eq_u32 s12, s28\n\t" \
    "s_addc_u32 s70, s70, 0\n\t" \
    "s_cmp_eq_u32 s14, s28\n\t" \
    "s_addc_u32 s70, s70, 0\n\t" \
    "s_cmp_eq_u32 s16, s28\n\t" \
    "s_addc_u32 s70, s70, 0\n\t" \
    "s_cmp_eq_u32 s18, s28\n\t" \
    "s_addc_u32 s70, s70, 0\n\t" \
    "s_cmp_eq_u32 s20, s28\n\t" \
    "s_addc_u32 s70, s70, 0\n\t" \
    "s_cmp_eq_u32 s22, s28\n\t" \
    "s_addc_u32 s70, s70, 0\n\t" \
    "s_cmp_eq_u32 s24, s28\n\t" \
    "s_addc_u32 s70, s70, 0\n\t" \
    "s_cmp_eq_u32 s26, s28\n\t" \
    "s_addc_u32 s70, s70, 0\n\t" \
    "s_mov_b32 s75, s73\n\t" \
    "s_mov_b32 s76, s74\n\t" \
    "s_lshl_b32 s70, s70, 3\n\t" \
    "s_sub_u32 s73, s56, s70\n\t" \
    "s_lshl_b32 s74, s28, 4\n\t" \
    "s_sub_u32 s69, s73, s75\n\t" \
    "s_sub_u32 s70, s74, s76\n\t" \
    "s_sub_u32 s71, s70, 1600\n\t" \
    "s_cmp_lt_u32 s71, 400000\n\t" \
    "s_cbranch_scc0 vv_K27_%=\n\t" \
    "v_cvt_f32_u32 v141, s69\n\t" \
    "v_cvt_f32_u32 v142, s70\n\t" \
    "v_rcp_f32 v143, v141\n\t" \
    "v_rcp_f32 v144, v142\n\t" \
    "v_mul_f32 v143, v143, v142\n\t" \
    "v_mul_f32 v144, v144, v141\n\t" \
    "v_mul_f32 v143, 0x4f800000, v143\n\t" \
    "v_mul_f32 v144, 0x4f000000, v144\n\t" \
    "v_cvt_u32_f32 v143, v143\n\t" \
    "v_cvt_u32_f32 v144, v144\n\t" \
    "v_readfirstlane_b32 s69, v143\n\t" \
    "v_readfirstlane_b32 s70, v144\n\t" \
    "s_nop 1\n\t" \
    "s_sub_u32 s69, s69, s78\n\t" \
    "s_ashr_i32 s69, s69, 2\n\t" \
    "s_add_u32 s78, s78, s69\n\t" \
    "s_sub_u32 s70, s70, s77\n\t" \
    "s_ashr_i32 s70, s70, 2\n\t" \
    "s_add_u32 s77, s77, s70\n\t" \
    "vv_K27_%=:\n\t" \
    "s_waitcnt vmcnt(6)\n\t" \
    "v_cmp_eq_u32 vcc, -1, v140\n\t" \
    "s_cbranch_vccz vv_Hok29_%=\n\t" \
    "vv_H28_%=:\n\t" \
    "global_load_dword v140, v20, s[46:47] sc1\n\t" \
    "s_waitcnt vmcnt(0)\n\t" \
    "v_cmp_eq_u32 vcc, -1, v140\n\t" \
    "s_cbranch_vccz vv_Hc30_%=\n\t" \
    "s_cmp_eq_u32 s68, 0\n\t" \
    "s_cbranch_scc1 vv_F31_%=\n\t" \
    "s_sub_u32 s68, s68, 1\n\t" \
    "s_sleep 5\n\t" \
    "s_branch vv_H28_%=\n\t" \
    "vv_F31_%=:\n\t" \
    "v_mov_b32 v141, 6095104\n\t" \
    "v_mov_b32 v142, 6\n\t" \
    "global_store_dword v141, v142, s[36:37] sc1\n\t" \
    "s_waitcnt vmcnt(0)\n\t" \
    "s_branch vv_Hc30_%=\n\t" \
    "vv_Hc30_%=:\n\t" \
    "vv_Hok29_%=:\n\t" \
    "v_subrev_u32 v140, s80, v140\n\t" \
    "v_and_b32 v20, -5, v20\n\t" \
    "s_nop 0\n\t" \
    "v_max_i32_dpp v140, v140, v140 row_ror:1 row_mask:0xf bank_mask:0xf\n\t" \
    "s_nop 1\n\t" \
    "v_max_i32_dpp v140, v140, v140 row_ror:2 row_mask:0xf bank_mask:0xf\n\t" \
    "s_nop 1\n\t" \
    "v_max_i32_dpp v140, v140, v140 row_ror:4 row_mask:0xf bank_mask:0xf\n\t" \
    "s_nop 1\n\t" \
    "v_max_i32_dpp v140, v140, v140 row_ror:8 row_mask:0xf bank_mask:0xf\n\t" \
    "s_nop 0\n\t" \
    "v_readlane_b32 s69, v140, 0\n\t" \
    "v_readlane_b32 s71, v140, 16\n\t" \
    "v_readlane_b32 s70, v140, 32\n\t" \
    "v_readlane_b32 s81, v140, 48\n\t" \
    "s_max_i32 s69, s69, s71\n\t" \
    "s_nop 0\n\t" \
    "s_max_i32 s71, s70, s81\n\t" \
    "s_max_i32 s69, s69, s71\n\t" \
    "s_add_u32 s81, s69, s80\n\t" \
    "s_cmp_lg_u32 s66, 0\n\t" \
    "s_cbranch_scc1 vv_SF32_%=\n\t" \
    "s_lshl_b32 s69, s67, 7\n\t" \
    "s_add_u32 s69, s69, 6131712\n\t" \
    "s_add_u32 s69, s69, s52\n\t" \
    "s_add_u32 s70, s65, -1\n\t" \
    "v_mov_b32 v140, s69\n\t" \
    "v_mov_b32 v141, s70\n\t" \
    "s_mov_b64 exec, 1\n\t" \
    "global_store_dword v140, v141, s[36:37] sc1\n\t" \
    "s_mov_b64 exec, -1\n\t" \
    "vv_SF32_%=:\n\t" \
    "v_mov_b32 v72, 0\n\t" \
    "v_mov_b32 v73, 0\n\t" \
    "v_mov_b32 v74, 0\n\t" \
    "v_mov_b32 v75, 0\n\t" \
    "v_mov_b32 v76, 0\n\t" \
    "v_mov_b32 v77, 0\n\t" \
    "v_mov_b32 v78, 0\n\t" \
    "v_mov_b32 v79, 0\n\t" \
    "v_mov_b32 v92, 0\n\t" \
    "v_mov_b32 v93, 0\n\t" \
    "v_mov_b32 v94, 0\n\t" \
    "v_mov_b32 v95, 0\n\t" \
    "v_mov_b32 v96, 0\n\t" \
    "v_mov_b32 v97, 0\n\t" \
    "v_mov_b32 v98, 0\n\t" \
    "v_mov_b32 v99, 0\n\t" \
    "v_mov_b32 v100, 0\n\t" \
    "v_mov_b32 v101, 0\n\t" \
    "v_mov_b32 v102, 0\n\t" \
    "v_mov_b32 v103, 0\n\t" \
    "v_mov_b32 v104, 0\n\t" \
    "v_mov_b32 v105, 0\n\t" \
    "v_mov_b32 v106, 0\n\t" \
    "v_mov_b32 v107, 0\n\t" \
    "v_mov_b32 v108, 0\n\t" \
    "v_mov_b32 v109, 0\n\t" \
    "v_mov_b32 v110, 0\n\t" \
    "v_mov_b32 v111, 0\n\t" \
    "v_mov_b32 v112, 0\n\t" \
    "v_mov_b32 v113, 0\n\t" \
    "v_mov_b32 v114, 0\n\t" \
    "v_mov_b32 v115, 0\n\t" \
    "v_mov_b32 v116, 0\n\t" \
    "v_mov_b32 v117, 0\n\t" \
    "v_mov_b32 v118, 0\n\t" \
    "v_mov_b32 v119, 0\n\t" \
    "v_mov_b32 v120, 0\n\t" \
    "v_mov_b32 v121, 0\n\t" \
    "v_mov_b32 v122, 0\n\t" \
    "v_mov_b32 v123, 0\n\t" \
    "v_mov_b32 v124, 0\n\t" \
    "v_mov_b32 v125, 0\n\t" \
    "v_mov_b32 v126, 0\n\t" \
    "v_mov_b32 v127, 0\n\t" \
    "s_cmp_lg_u32 s65, 0\n\t" \
    "s_cbranch_scc1 vv_Oc35_%=\n\t" \
    "s_add_u32 s79, s81, 240\n\t" \
    "s_sub_u32 s70, s79, s74\n\t" \
    "s_max_i32 s70, s70, 0\n\t" \
    "s_min_i32 s70, s70, 16000\n\t" \
    "s_lshl_b32 s70, s70, 1\n\t" \
    "s_mul_hi_u32 s70, s70, s77\n\t" \
    "s_add_u32 s70, s70, s73\n\t" \
    "vv_W36_%=:\n\t" \
    "s_memtime s[56:57]\n\t" \
    "s_waitcnt lgkmcnt(0)\n\t" \
    "s_sub_u32 s69, s70, s56\n\t" \
    "s_cmp_gt_i32 s69, 24\n\t" \
    "s_cbranch_scc1 vv_W36_%=\n\t" \
    "global_load_dword v140, v35, s[46:47] sc1\n\t" \
    "s_waitcnt vmcnt(0)\n\t" \
    "v_cmp_eq_u32 vcc, -1, v140\n\t" \
    "s_cbranch_vccz vv_Ook34_%=\n\t" \
    "vv_O33_%=:\n\t" \
    "global_load_dword v140, v35, s[46:47] offset:1536 sc1\n\t" \
    "s_waitcnt vmcnt(0)\n\t" \
    "v_cmp_eq_u32 vcc, -1, v140\n\t" \
    "s_cbranch_vccz vv_Ook34_%=\n\t" \
    "s_cmp_eq_u32 s68, 0\n\t" \
    "s_cbranch_scc1 vv_F37_%=\n\t" \
    "s_sub_u32 s68, s68, 1\n\t" \
    "s_sleep 3\n\t" \
    "s_branch vv_O33_%=\n\t" \
    "vv_F37_%=:\n\t" \
    "v_mov_b32 v141, 6095104\n\t" \
    "v_mov_b32 v142, 17\n\t" \
    "global_store_dword v141, v142, s[36:37] sc1\n\t" \
    "s_waitcnt vmcnt(0)\n\t" \
    "s_branch vv_Ook34_%=\n\t" \
    "vv_Ook34_%=:\n\t" \
    "v_subrev_u32 v140, s80, v140\n\t" \
    "s_nop 1\n\t" \
    "v_max_i32_dpp v140, v140, v140 row_ror:1 row_mask:0xf bank_mask:0xf\n\t" \
    "s_nop 1\n\t" \
    "v_max_i32_dpp v140, v140, v140 row_ror:2 row_mask:0xf bank_mask:0xf\n\t" \
    "s_nop 1\n\t" \
    "v_max_i32_dpp v140, v140, v140 row_ror:4 row_mask:0xf bank_mask:0xf\n\t" \
    "s_nop 1\n\t" \
    "v_max_i32_dpp v140, v140, v140 row_ror:8 row_mask:0xf bank_mask:0xf\n\t" \
    "s_nop 0\n\t" \
    "v_readlane_b32 s69, v140, 0\n\t" \
    "v_readlane_b32 s71, v140, 16\n\t" \
    "v_readlane_b32 s70, v140, 32\n\t" \
    "v_readlane_b32 s81, v140, 48\n\t" \
    "s_max_i32 s69, s69, s71\n\t" \
    "s_nop 0\n\t" \
    "s_max_i32 s71, s70, s81\n\t" \
    "s_max_i32 s69, s69, s71\n\t" \
    "s_add_u32 s81, s69, s80\n\t" \
    "vv_Oc35_%=:\n\t" \
    "s_mul_hi_u32 s69, s85, s78\n\t" \
    "s_add_u32 s79, s81, s69\n\t" \
    "s_add_u32 s79, s79, s87\n\t" \
    "s_cmp_ge_u32 s65, 36\n\t" \
    "s_cselect_b32 s69, 28, 0\n\t" \
    "s_sub_u32 s79, s79, s69\n\t" \
    "s_cmp_lt_u32 s65, 8\n\t" \
    "s_cselect_b32 s69, 96, 0\n\t" \
    "s_add_u32 s79, s79, s69\n\t" \
    "s_add_u32 s80, s79, s88\n\t" \
    "s_add_u32 s65, s65, 1\n\t" \
    "s_branch vv_go15_%=\n\t" \
    "vv_Gbad20_%=:\n\t" \
    "s_waitcnt vmcnt(0) lgkmcnt(0)\n\t" \
    "s_nop 7\n\t" \
    "v_mov_b32 v72, 0\n\t" \
    "v_mov_b32 v73, 0\n\t" \
    "v_mov_b32 v74, 0\n\t" \
    "v_mov_b32 v75, 0\n\t" \
    "v_mov_b32 v76, 0\n\t" \
    "v_mov_b32 v77, 0\n\t" \
    "v_mov_b32 v78, 0\n\t" \
    "v_mov_b32 v79, 0\n\t" \
    "s_cmp_eq_u32 s68, 0\n\t" \
    "s_cbranch_scc1 vv_F38_%=\n\t" \
    "s_sub_u32 s68, s68, 1\n\t" \
    "s_branch vv_G19_%=\n\t" \
    "vv_F38_%=:\n\t" \
    "v_mov_b32 v140, 6095104\n\t" \
    "v_mov_b32 v141, 1\n\t" \
    "global_store_dword v140, v141, s[36:37] sc1\n\t" \
    "s_waitcnt vmcnt(0)\n\t" \
    "s_branch vv_Gc22_%=\n\t" \
    "vv_Gbad221_%=:\n\t" \
    "global_load_dwordx4 v[44:47], v2, s[46:47] sc1\n\t" \
    "s_waitcnt vmcnt(0)\n\t" \
    "v_max3_u32 v140, v44, v45, v46\n\t" \
    "v_max_u32 v140, v140, v47\n\t" \
    "v_cmp_eq_u32 vcc, -1, v140\n\t" \
    "s_cbranch_vccz vv_Gre224_%=\n\t" \
    "s_cmp_eq_u32 s68, 0\n\t" \
    "s_cbranch_scc1 vv_F39_%=\n\t" \
    "s_sub_u32 s68, s68, 1\n\t" \
    "s_sleep 1\n\t" \
    "s_branch vv_Gbad221_%=\n\t" \
    "vv_F39_%=:\n\t" \
    "v_mov_b32 v140, 6095104\n\t" \
    "v_mov_b32 v141, 2\n\t" \
    "global_store_dword v140, v141, s[36:37] sc1\n\t" \
    "s_waitcnt vmcnt(0)\n\t" \
    "s_branch vv_Gre224_%=\n\t" \
    "vv_Gre224_%=:\n\t" \
    "v_mov_b32 v72, 0\n\t" \
    "v_mov_b32 v73, 0\n\t" \
    "v_mov_b32 v74, 0\n\t" \
    "v_mov_b32 v75, 0\n\t" \
    "v_mov_b32 v76, 0\n\t" \
    "v_mov_b32 v77, 0\n\t" \
    "v_mov_b32 v78, 0\n\t" \
    "v_mov_b32 v79, 0\n\t" \
    "v_smfmac_f32_16x16x64_bf16 v[72:75], v[36:39], a[0:7], v10\n\t" \
    "s_nop 0\n\t" \
    "v_smfmac_f32_16x16x64_bf16 v[76:79], v[36:39], a[72:79], v10\n\t" \
    "v_smfmac_f32_16x16x64_bf16 v[72:75], v[40:43], a[8:15], v10\n\t" \
    "v_smfmac_f32_16x16x64_bf16 v[76:79], v[40:43], a[80:87], v10\n\t" \
    "v_smfmac_f32_16x16x64_bf16 v[72:75], v[44:47], a[64:71], v10\n\t" \
    "v_smfmac_f32_16x16x64_bf16 v[76:79], v[44:47], a[136:143], v10\n\t" \
    "s_branch vv_Gc223_%=\n\t" \
    "vv_end14_%=:\n\t" \
    "s_endpgm\n\t" \
    "vv_loop11_%=:\n\t" \
    "v_mov_b32 v72, 0\n\t" \
    "v_mov_b32 v73, 0\n\t" \
    "v_mov_b32 v74, 0\n\t" \
    "v_mov_b32 v75, 0\n\t" \
    "v_mov_b32 v76, 0\n\t" \
    "v_mov_b32 v77, 0\n\t" \
    "v_mov_b32 v78, 0\n\t" \
    "v_mov_b32 v79, 0\n\t" \
    "v_mov_b32 v92, 0\n\t" \
    "v_mov_b32 v93, 0\n\t" \
    "v_mov_b32 v94, 0\n\t" \
    "v_mov_b32 v95, 0\n\t" \
    "v_mov_b32 v96, 0\n\t" \
    "v_mov_b32 v97, 0\n\t" \
    "v_mov_b32 v98, 0\n\t" \
    "v_mov_b32 v99, 0\n\t" \
    "v_mov_b32 v100, 0\n\t" \
    "v_mov_b32 v101, 0\n\t" \
    "v_mov_b32 v102, 0\n\t" \
    "v_mov_b32 v103, 0\n\t" \
    "v_mov_b32 v104, 0\n\t" \
    "v_mov_b32 v105, 0\n\t" \
    "v_mov_b32 v106, 0\n\t" \
    "v_mov_b32 v107, 0\n\t" \
    "v_mov_b32 v108, 0\n\t" \
    "v_mov_b32 v109, 0\n\t" \
    "v_mov_b32 v110, 0\n\t" \
    "v_mov_b32 v111, 0\n\t" \
    "v_mov_b32 v112, 0\n\t" \
    "v_mov_b32 v113, 0\n\t" \
    "v_mov_b32 v114, 0\n\t" \
    "v_mov_b32 v115, 0\n\t" \
    "v_mov_b32 v116, 0\n\t" \
    "v_mov_b32 v117, 0\n\t" \
    "v_mov_b32 v118, 0\n\t" \
    "v_mov_b32 v119, 0\n\t" \
    "v_mov_b32 v120, 0\n\t" \
    "v_mov_b32 v121, 0\n\t" \
    "v_mov_b32 v122, 0\n\t" \
    "v_mov_b32 v123, 0\n\t" \
    "v_mov_b32 v124, 0\n\t" \
    "v_mov_b32 v125, 0\n\t" \
    "v_mov_b32 v126, 0\n\t" \
    "v_mov_b32 v127, 0\n\t" \
    "vv_go41_%=:\n\t" \
    "s_add_u32 s69, s65, 0\n\t" \
    "s_bitcmp1_b32 s69, 0\n\t" \
    "s_cselect_b32 s70, s63, s62\n\t" \
    "s_bfe_u32 s69, s69, 0x20001\n\t" \
    "s_lshl_b32 s69, s69, 19\n\t" \
    "s_add_u32 s69, s69, s70\n\t" \
    "s_add_u32 s46, s36, s69\n\t" \
    "s_addc_u32 s47, s37, 0\n\t" \
    "s_add_u32 s69, s65, 0\n\t" \
    "s_mul_i32 s70, s69, 171\n\t" \
    "s_lshr_b32 s70, s70, 9\n\t" \
    "s_mul_i32 s70, s70, 3\n\t" \
    "s_sub_u32 s69, s69, s70\n\t" \
    "s_mul_i32 s69, s69, 36\n\t" \
    "s_lshl_b32 s69, s69, 14\n\t" \
    "s_add_u32 s69, s69, s64\n\t" \
    "s_add_u32 s69, s69, 4194304\n\t" \
    "s_add_u32 s38, s36, s69\n\t" \
    "s_addc_u32 s39, s37, 0\n\t" \
    "s_mul_i32 s69, s65, 9216\n\t" \
    "s_add_u32 s69, s69, 123731968\n\t" \
    "s_add_u32 s40, s36, s69\n\t" \
    "s_addc_u32 s41, s37, 0\n\t" \
    "s_sub_u32 s70, s79, s74\n\t" \
    "s_max_i32 s70, s70, 0\n\t" \
    "s_min_i32 s70, s70, 16000\n\t" \
    "s_lshl_b32 s70, s70, 1\n\t" \
    "s_mul_hi_u32 s70, s70, s77\n\t" \
    "s_add_u32 s70, s70, s73\n\t" \
    "s_sub_u32 s70, s70, 100\n\t" \
    "s_getpc_b64 s[30:31]\n\t" \
    "vv_Wp43_%=:\n\t" \
    "s_add_u32 s30, s30, vv_We44_%=-vv_Wp43_%=\n\t" \
    "s_addc_u32 s31, s31, 0\n\t" \
    "vv_W42_%=:\n\t" \
    "s_memtime s[56:57]\n\t" \
    "s_waitcnt lgkmcnt(0)\n\t" \
    "s_sub_u32 s69, s70, s56\n\t" \
    "s_cmp_gt_i32 s69, 111\n\t" \
    "s_cbranch_scc1 vv_W42_%=\n\t" \
    "s_max_i32 s69, s69, 0\n\t" \
    "s_lshr_b32 s69, s69, 1\n\t" \
    "s_and_b32 s69, s69, 0x3c\n\t" \
    "s_sub_u32 s30, s30, s69\n\t" \
    "s_subb_u32 s31, s31, 0\n\t" \
    "s_setpc_b64 s[30:31]\n\t" \
    ".p2align 6\n\t" \
    "s_nop 0\n\t" \
    "s_nop 0\n\t" \
    "s_nop 0\n\t" \
    "s_nop 1\n\t" \
    "s_nop 1\n\t" \
    "s_nop 1\n\t" \
    "s_nop 1\n\t" \
    "s_nop 1\n\t" \
    "s_nop 1\n\t" \
    "s_nop 1\n\t" \
    "s_nop 1\n\t" \
    "s_nop 1\n\t" \
    "s_nop 1\n\t" \
    "s_nop 1\n\t" \
    "s_nop 1\n\t" \
    "s_nop 1\n\t" \
    "vv_We44_%=:\n\t" \
    "vv_G45_%=:\n\t" \
    "global_load_dwordx4 v[36:39], v1, s[46:47] sc1\n\t" \
    "s_nop 9\n\t" \
    "global_load_dwordx4 v[40:43], v1, s[46:47] offset:1024 sc1\n\t" \
    "s_nop 9\n\t" \
    "global_load_dwordx4 v[44:47], v2, s[46:47] sc1\n\t" \
    "s_waitcnt vmcnt(2)\n\t" \
    "v_smfmac_f32_16x16x64_bf16 v[72:75], v[36:39], a[16:23], v10\n\t" \
    "ds_write_b128 v0, v[36:39] offset:2048\n\t" \
    "v_smfmac_f32_16x16x64_bf16 v[76:79], v[36:39], a[88:95], v10\n\t" \
    "v_max3_u32 v140, v36, v37, v38\n\t" \
    "s_waitcnt vmcnt(1)\n\t" \
    "ds_write_b128 v0, v[40:43] offset:3072\n\t" \
    "v_smfmac_f32_16x16x64_bf16 v[72:75], v[40:43], a[24:31], v10\n\t" \
    "v_max3_u32 v140, v140, v39, v40\n\t" \
    "v_max3_u32 v140, v140, v41, v42\n\t" \
    "v_max_u32 v140, v140, v43\n\t" \
    "v_cmp_eq_u32 vcc, -1, v140\n\t" \
    "v_smfmac_f32_16x16x64_bf16 v[76:79], v[40:43], a[96:103], v10\n\t" \
    "s_cbranch_vccnz vv_Gbad46_%=\n\t" \
    "vv_Gc48_%=:\n\t" \
    "s_waitcnt lgkmcnt(0)\n\t" \
    "s_barrier\n\t" \
    "ds_read_b128 v[48:51], v0 offset:4096\n\t" \
    "ds_read_b128 v[52:55], v0 offset:5120\n\t" \
    "s_waitcnt vmcnt(0)\n\t" \
    "v_smfmac_f32_16x16x64_bf16 v[72:75], v[44:47], a[64:71], v10\n\t" \
    "ds_read_b128 v[56:59], v0 offset:6144\n\t" \
    "ds_read_b128 v[60:63], v0 offset:7168\n\t" \
    "v_max3_u32 v140, v44, v45, v46\n\t" \
    "v_max_u32 v140, v140, v47\n\t" \
    "v_smfmac_f32_16x16x64_bf16 v[76:79], v[44:47], a[136:143], v10\n\t" \
    "ds_read_b128 v[64:67], v0 offset:0\n\t" \
    "ds_read_b128 v[68:71], v0 offset:1024\n\t" \
    "v_cmp_eq_u32 vcc, -1, v140\n\t" \
    "s_cbranch_vccnz vv_Gbad247_%=\n\t" \
    "vv_Gc249_%=:\n\t" \
    "s_waitcnt lgkmcnt(5)\n\t" \
    "v_smfmac_f32_16x16x64_bf16 v[72:75], v[48:51], a[32:39], v10\n\t" \
    "s_waitcnt lgkmcnt(4)\n\t" \
    "v_smfmac_f32_16x16x64_bf16 v[72:75], v[52:55], a[40:47], v10\n\t" \
    "s_waitcnt lgkmcnt(3)\n\t" \
    "v_smfmac_f32_16x16x64_bf16 v[72:75], v[56:59], a[48:55], v10\n\t" \
    "s_waitcnt lgkmcnt(2)\n\t" \
    "v_smfmac_f32_16x16x64_bf16 v[72:75], v[60:63], a[56:63], v10\n\t" \
    "s_waitcnt lgkmcnt(1)\n\t" \
    "v_smfmac_f32_16x16x64_bf16 v[72:75], v[64:67], a[0:7], v10\n\t" \
    "s_waitcnt lgkmcnt(0)\n\t" \
    "v_smfmac_f32_16x16x64_bf16 v[72:75], v[68:71], a[8:15], v10\n\t" \
    "v_smfmac_f32_16x16x64_bf16 v[76:79], v[48:51], a[104:111], v10\n\t" \
    "s_nop 1\n\t" \
    "s_nop 0\n\t" \
    "v_smfmac_f32_16x16x64_bf16 v[76:79], v[52:55], a[112:119], v10\n\t" \
    "s_nop 1\n\t" \
    "s_nop 0\n\t" \
    "v_smfmac_f32_16x16x64_bf16 v[76:79], v[56:59], a[120:127], v10\n\t" \
    "v_add_f32 v72, v72, v74\n\t" \
    "v_smfmac_f32_16x16x64_bf16 v[76:79], v[60:63], a[128:135], v10\n\t" \
    "v_add_f32 v73, v73, v75\n\t" \
    "v_smfmac_f32_16x16x64_bf16 v[76:79], v[64:67], a[72:79], v10\n\t" \
    "v_cvt_pk_bf16_f32 v80, v72, v73\n\t" \
    "v_smfmac_f32_16x16x64_bf16 v[76:79], v[68:71], a[80:87], v10\n\t" \
    "ds_write_b32 v12, v80 offset:0\n\t" \
    "s_nop 6\n\t" \
    "v_add_f32 v76, v76, v78\n\t" \
    "v_add_f32 v77, v77, v79\n\t" \
    "v_cvt_pk_bf16_f32 v81, v76, v77\n\t" \
    "ds_write_b32 v12, v81 offset:64\n\t" \
    "s_waitcnt lgkmcnt(0)\n\t" \
    "s_barrier\n\t" \
    "ds_read_b64_tr_b16 v[84:85], v13 offset:0\n\t" \
    "ds_read_b64_tr_b16 v[86:87], v13 offset:128\n\t" \
    "ds_read_b64_tr_b16 v[88:89], v13 offset:1024\n\t" \
    "ds_read_b64_tr_b16 v[90:91], v13 offset:1152\n\t" \
    "s_waitcnt lgkmcnt(2)\n\t" \
    "v_smfmac_f32_16x16x64_bf16 v[92:95], v[84:87], a[144:151], v10\n\t" \
    "s_nop 2\n\t" \
    "s_waitcnt lgkmcnt(0)\n\t" \
    "v_smfmac_f32_16x16x64_bf16 v[92:95], v[88:91], a[152:159], v10\n\t" \
    "s_nop 2\n\t" \
    "v_smfmac_f32_16x16x64_bf16 v[96:99], v[84:87], a[160:167], v10\n\t" \
    "s_nop 2\n\t" \
    "v_smfmac_f32_16x16x64_bf16 v[96:99], v[88:91], a[168:175], v10\n\t" \
    "s_nop 2\n\t" \
    "v_smfmac_f32_16x16x64_bf16 v[100:103], v[84:87], a[176:183], v10\n\t" \
    "s_nop 0\n\t" \
    "v_add_f32 v92, v92, v94\n\t" \
    "v_add_f32 v93, v93, v95\n\t" \
    "v_smfmac_f32_16x16x64_bf16 v[100:103], v[88:91], a[184:191], v10\n\t" \
    "s_nop 1\n\t" \
    "v_cvt_pk_bf16_f32 v128, v92, v93\n\t" \
    "v_smfmac_f32_16x16x64_bf16 v[104:107], v[84:87], a[192:199], v10\n\t" \
    "s_nop 0\n\t" \
    "v_add_f32 v96, v96, v98\n\t" \
    "v_add_f32 v97, v97, v99\n\t" \
    "v_smfmac_f32_16x16x64_bf16 v[104:107], v[88:91], a[200:207], v10\n\t" \
    "s_nop 1\n\t" \
    "v_cvt_pk_bf16_f32 v129, v96, v97\n\t" \
    "v_smfmac_f32_16x16x64_bf16 v[124:127], v[84:87], v[240:247], v10\n\t" \
    "s_nop 0\n\t" \
    "v_add_f32 v100, v100, v102\n\t" \
    "v_add_f32 v101, v101, v103\n\t" \
    "v_smfmac_f32_16x16x64_bf16 v[124:127], v[88:91], v[248:255], v10\n\t" \
    "s_nop 1\n\t" \
    "global_load_dword v140, v20, s[46:47] sc1\n\t" \
    "v_cvt_pk_bf16_f32 v130, v100, v101\n\t" \
    "v_smfmac_f32_16x16x64_bf16 v[108:111], v[84:87], a[208:215], v10\n\t" \
    "s_nop 0\n\t" \
    "v_add_f32 v104, v104, v106\n\t" \
    "v_add_f32 v105, v105, v107\n\t" \
    "v_smfmac_f32_16x16x64_bf16 v[108:111], v[88:91], a[216:223], v10\n\t" \
    "s_nop 0\n\t" \
    "v_cvt_pk_bf16_f32 v131, v104, v105\n\t" \
    "v_permlane32_swap_b32 v128, v130\n\t" \
    "v_smfmac_f32_16x16x64_bf16 v[112:115], v[84:87], a[224:231], v10\n\t" \
    "s_nop 0\n\t" \
    "v_permlane32_swap_b32 v129, v131\n\t" \
    "s_nop 1\n\t" \
    "v_permlane16_swap_b32 v128, v129\n\t" \
    "v_smfmac_f32_16x16x64_bf16 v[112:115], v[88:91], a[232:239], v10\n\t" \
    "v_permlane16_swap_b32 v130, v131\n\t" \
    "global_store_dwordx4 v14, v[128:131], s[38:39] sc1\n\t" \
    "v_add_f32 v124, v124, v126\n\t" \
    "v_smfmac_f32_16x16x64_bf16 v[116:119], v[84:87], a[240:247], v10\n\t" \
    "s_nop 0\n\t" \
    "v_add_f32 v125, v125, v127\n\t" \
    "v_cvt_pk_bf16_f32 v136, v124, v125\n\t" \
    "v_smfmac_f32_16x16x64_bf16 v[116:119], v[88:91], a[248:255], v10\n\t" \
    "global_store_dword v16, v136, s[38:39] sc1\n\t" \
    "v_add_f32 v108, v108, v110\n\t" \
    "v_add_f32 v109, v109, v111\n\t" \
    "v_smfmac_f32_16x16x64_bf16 v[120:123], v[84:87], v[224:231], v10\n\t" \
    "s_nop 0\n\t" \
    "v_cvt_pk_bf16_f32 v132, v108, v109\n\t" \
    "v_add_f32 v112, v112, v114\n\t" \
    "v_smfmac_f32_16x16x64_bf16 v[120:123], v[88:91], v[232:239], v10\n\t" \
    "v_add_f32 v113, v113, v115\n\t" \
    "v_cvt_pk_bf16_f32 v133, v112, v113\n\t" \
    "v_add_f32 v116, v116, v118\n\t" \
    "v_add_f32 v117, v117, v119\n\t" \
    "v_cvt_pk_bf16_f32 v134, v116, v117\n\t" \
    "s_nop 2\n\t" \
    "v_pk_add_f32 v[120:121], v[120:121], v[122:123]\n\t" \
    "v_cvt_pk_bf16_f32 v135, v120, v121\n\t" \
    "v_permlane32_swap_b32 v132, v134\n\t" \
    "s_nop 0\n\t" \
    "v_permlane32_swap_b32 v133, v135\n\t" \
    "s_nop 1\n\t" \
    "v_permlane16_swap_b32 v132, v133\n\t" \
    "v_permlane16_swap_b32 v134, v135\n\t" \
    "global_store_dwordx4 v15, v[132:135], s[38:39] sc1\n\t" \
    "s_nop 1\n\t" \
    "global_store_dwordx4 v17, v[128:131], s[40:41] sc1\n\t" \
    "global_store_dwordx4 v17, v[132:135], s[40:41] offset:1024 sc1\n\t" \
    "global_store_dword v18, v136, s[40:41] sc1\n\t" \
    "s_cmp_eq_u32 s65, 63\n\t" \
    "s_cbranch_scc1 vv_end40_%=\n\t" \
    "s_mov_b32 s71, 3\n\t" \
    "vv_E51_%=:\n\t" \
    "s_memtime s[54:55]\n\t" \
    "s_nop 0\n\t" \
    "s_memrealtime s[4:5]\n\t" \
    "s_nop 0\n\t" \
    "s_memrealtime s[6:7]\n\t" \
    "s_nop 0\n\t" \
    "s_memrealtime s[8:9]\n\t" \
    "s_nop 0\n\t" \
    "s_memrealtime s[10:11]\n\t" \
    "s_nop 0\n\t" \
    "s_memrealtime s[12:13]\n\t" \
    "s_nop 0\n\t" \
    "s_memrealtime s[14:15]\n\t" \
    "s_nop 0\n\t" \
    "s_memrealtime s[16:17]\n\t" \
    "s_nop 0\n\t" \
    "s_memrealtime s[18:19]\n\t" \
    "s_nop 0\n\t" \
    "s_memrealtime s[20:21]\n\t" \
    "s_nop 0\n\t" \
    "s_memrealtime s[22:23]\n\t" \
    "s_nop 0\n\t" \
    "s_memrealtime s[24:25]\n\t" \
    "s_nop 0\n\t" \
    "s_memrealtime s[26:27]\n\t" \
    "s_nop 0\n\t" \
    "s_memrealtime s[28:29]\n\t" \
    "s_nop 0\n\t" \
    "s_memtime s[56:57]\n\t" \
    "s_mov_b32 s70, 1\n\t" \
    "s_sub_u32 s71, s71, 1\n\t" \
    "s_cselect_b32 s69, 0, -1\n\t" \
    "s_waitcnt lgkmcnt(0)\n\t" \
    "s_sub_u32 s54, s56, s54\n\t" \
    "s_and_b32 s54, s54, s69\n\t" \
    "s_cmp_gt_u32 s54, 124\n\t" \
    "s_cbranch_scc1 vv_E51_%=\n\t" \
    "s_cmp_eq_u32 s4, s28\n\t" \
    "s_addc_u32 s70, s70, 0\n\t" \
    "s_cmp_eq_u32 s6, s28\n\t" \
    "s_addc_u32 s70, s70, 0\n\t" \
    "s_cmp_eq_u32 s8, s28\n\t" \
    "s_addc_u32 s70, s70, 0\n\t" \
    "s_cmp_eq_u32 s10, s28\n\t" \
    "s_addc_u32 s70, s70, 0\n\t" \
    "s_cmp_eq_u32 s12, s28\n\t" \
    "s_addc_u32 s70, s70, 0\n\t" \
    "s_cmp_eq_u32 s14, s28\n\t" \
    "s_addc_u32 s70, s70, 0\n\t" \
    "s_cmp_eq_u32 s16, s28\n\t" \
    "s_addc_u32 s70, s70, 0\n\t" \
    "s_cmp_eq_u32 s18, s28\n\t" \
    "s_addc_u32 s70, s70, 0\n\t" \
    "s_cmp_eq_u32 s20, s28\n\t" \
    "s_addc_u32 s70, s70, 0\n\t" \
    "s_cmp_eq_u32 s22, s28\n\t" \
    "s_addc_u32 s70, s70, 0\n\t" \
    "s_cmp_eq_u32 s24, s28\n\t" \
    "s_addc_u32 s70, s70, 0\n\t" \
    "s_cmp_eq_u32 s26, s28\n\t" \
    "s_addc_u32 s70, s70, 0\n\t" \
    "s_mov_b32 s75, s73\n\t" \
    "s_mov_b32 s76, s74\n\t" \
    "s_lshl_b32 s70, s70, 3\n\t" \
    "s_sub_u32 s73, s56, s70\n\t" \
    "s_lshl_b32 s74, s28, 4\n\t" \
    "s_sub_u32 s69, s73, s75\n\t" \
    "s_sub_u32 s70, s74, s76\n\t" \
    "s_sub_u32 s71, s70, 1600\n\t" \
    "s_cmp_lt_u32 s71, 400000\n\t" \
    "s_cbranch_scc0 vv_K53_%=\n\t" \
    "v_cvt_f32_u32 v141, s69\n\t" \
    "v_cvt_f32_u32 v142, s70\n\t" \
    "v_rcp_f32 v143, v141\n\t" \
    "v_rcp_f32 v144, v142\n\t" \
    "v_mul_f32 v143, v143, v142\n\t" \
    "v_mul_f32 v144, v144, v141\n\t" \
    "v_mul_f32 v143, 0x4f800000, v143\n\t" \
    "v_mul_f32 v144, 0x4f000000, v144\n\t" \
    "v_cvt_u32_f32 v143, v143\n\t" \
    "v_cvt_u32_f32 v144, v144\n\t" \
    "v_readfirstlane_b32 s69, v143\n\t" \
    "v_readfirstlane_b32 s70, v144\n\t" \
    "s_nop 1\n\t" \
    "s_sub_u32 s69, s69, s78\n\t" \
    "s_ashr_i32 s69, s69, 2\n\t" \
    "s_add_u32 s78, s78, s69\n\t" \
    "s_sub_u32 s70, s70, s77\n\t" \
    "s_ashr_i32 s70, s70, 2\n\t" \
    "s_add_u32 s77, s77, s70\n\t" \
    "vv_K53_%=:\n\t" \
    "s_waitcnt vmcnt(6)\n\t" \
    "v_cmp_eq_u32 vcc, -1, v140\n\t" \
    "s_cbranch_vccz vv_Hok55_%=\n\t" \
    "vv_H54_%=:\n\t" \
    "global_load_dword v140, v20, s[46:47] sc1\n\t" \
    "s_waitcnt vmcnt(0)\n\t" \
    "v_cmp_eq_u32 vcc, -1, v140\n\t" \
    "s_cbranch_vccz vv_Hc56_%=\n\t" \
    "s_cmp_eq_u32 s68, 0\n\t" \
    "s_cbranch_scc1 vv_F57_%=\n\t" \
    "s_sub_u32 s68, s68, 1\n\t" \
    "s_sleep 5\n\t" \
    "s_branch vv_H54_%=\n\t" \
    "vv_F57_%=:\n\t" \
    "v_mov_b32 v141, 6095104\n\t" \
    "v_mov_b32 v142, 6\n\t" \
    "global_store_dword v141, v142, s[36:37] sc1\n\t" \
    "s_waitcnt vmcnt(0)\n\t" \
    "s_branch vv_Hc56_%=\n\t" \
    "vv_Hc56_%=:\n\t" \
    "vv_Hok55_%=:\n\t" \
    "v_subrev_u32 v140, s80, v140\n\t" \
    "v_and_b32 v20, -5, v20\n\t" \
    "s_nop 0\n\t" \
    "v_max_i32_dpp v140, v140, v140 row_ror:1 row_mask:0xf bank_mask:0xf\n\t" \
    "s_nop 1\n\t" \
    "v_max_i32_dpp v140, v140, v140 row_ror:2 row_mask:0xf bank_mask:0xf\n\t" \
    "s_nop 1\n\t" \
    "v_max_i32_dpp v140, v140, v140 row_ror:4 row_mask:0xf bank_mask:0xf\n\t" \
    "s_nop 1\n\t" \
    "v_max_i32_dpp v140, v140, v140 row_ror:8 row_mask:0xf bank_mask:0xf\n\t" \
    "s_nop 0\n\t" \
    "v_readlane_b32 s69, v140, 0\n\t" \
    "v_readlane_b32 s71, v140, 16\n\t" \
    "v_readlane_b32 s70, v140, 32\n\t" \
    "v_readlane_b32 s81, v140, 48\n\t" \
    "s_max_i32 s69, s69, s71\n\t" \
    "s_nop 0\n\t" \
    "s_max_i32 s71, s70, s81\n\t" \
    "s_max_i32 s69, s69, s71\n\t" \
    "s_add_u32 s81, s69, s80\n\t" \
    "v_mov_b32 v72, 0\n\t" \
    "v_mov_b32 v73, 0\n\t" \
    "v_mov_b32 v74, 0\n\t" \
    "v_mov_b32 v75, 0\n\t" \
    "v_mov_b32 v76, 0\n\t" \
    "v_mov_b32 v77, 0\n\t" \
    "v_mov_b32 v78, 0\n\t" \
    "v_mov_b32 v79, 0\n\t" \
    "v_mov_b32 v92, 0\n\t" \
    "v_mov_b32 v93, 0\n\t" \
    "v_mov_b32 v94, 0\n\t" \
    "v_mov_b32 v95, 0\n\t" \
    "v_mov_b32 v96, 0\n\t" \
    "v_mov_b32 v97, 0\n\t" \
    "v_mov_b32 v98, 0\n\t" \
    "v_mov_b32 v99, 0\n\t" \
    "v_mov_b32 v100, 0\n\t" \
    "v_mov_b32 v101, 0\n\t" \
    "v_mov_b32 v102, 0\n\t" \
    "v_mov_b32 v103, 0\n\t" \
    "v_mov_b32 v104, 0\n\t" \
    "v_mov_b32 v105, 0\n\t" \
    "v_mov_b32 v106, 0\n\t" \
    "v_mov_b32 v107, 0\n\t" \
    "v_mov_b32 v108, 0\n\t" \
    "v_mov_b32 v109, 0\n\t" \
    "v_mov_b32 v110, 0\n\t" \
    "v_mov_b32 v111, 0\n\t" \
    "v_mov_b32 v112, 0\n\t" \
    "v_mov_b32 v113, 0\n\t" \
    "v_mov_b32 v114, 0\n\t" \
    "v_mov_b32 v115, 0\n\t" \
    "v_mov_b32 v116, 0\n\t" \
    "v_mov_b32 v117, 0\n\t" \
    "v_mov_b32 v118, 0\n\t" \
    "v_mov_b32 v119, 0\n\t" \
    "v_mov_b32 v120, 0\n\t" \
    "v_mov_b32 v121, 0\n\t" \
    "v_mov_b32 v122, 0\n\t" \
    "v_mov_b32 v123, 0\n\t" \
    "v_mov_b32 v124, 0\n\t" \
    "v_mov_b32 v125, 0\n\t" \
    "v_mov_b32 v126, 0\n\t" \
    "v_mov_b32 v127, 0\n\t" \
    "s_cmp_lg_u32 s65, 0\n\t" \
    "s_cbranch_scc1 vv_Oc60_%=\n\t" \
    "s_add_u32 s79, s81, 240\n\t" \
    "s_sub_u32 s70, s79, s74\n\t" \
    "s_max_i32 s70, s70, 0\n\t" \
    "s_min_i32 s70, s70, 16000\n\t" \
    "s_lshl_b32 s70, s70, 1\n\t" \
    "s_mul_hi_u32 s70, s70, s77\n\t" \
    "s_add_u32 s70, s70, s73\n\t" \
    "vv_W61_%=:\n\t" \
    "s_memtime s[56:57]\n\t" \
    "s_waitcnt lgkmcnt(0)\n\t" \
    "s_sub_u32 s69, s70, s56\n\t" \
    "s_cmp_gt_i32 s69, 24\n\t" \
    "s_cbranch_scc1 vv_W61_%=\n\t" \
    "global_load_dword v140, v35, s[46:47] sc1\n\t" \
    "s_waitcnt vmcnt(0)\n\t" \
    "v_cmp_eq_u32 vcc, -1, v140\n\t" \
    "s_cbranch_vccz vv_Ook59_%=\n\t" \
    "vv_O58_%=:\n\t" \
    "global_load_dword v140, v35, s[46:47] offset:1536 sc1\n\t" \
    "s_waitcnt vmcnt(0)\n\t" \
    "v_cmp_eq_u32 vcc, -1, v140\n\t" \
    "s_cbranch_vccz vv_Ook59_%=\n\t" \
    "s_cmp_eq_u32 s68, 0\n\t" \
    "s_cbranch_scc1 vv_F62_%=\n\t" \
    "s_sub_u32 s68, s68, 1\n\t" \
    "s_sleep 3\n\t" \
    "s_branch vv_O58_%=\n\t" \
    "vv_F62_%=:\n\t" \
    "v_mov_b32 v141, 6095104\n\t" \
    "v_mov_b32 v142, 17\n\t" \
    "global_store_dword v141, v142, s[36:37] sc1\n\t" \
    "s_waitcnt vmcnt(0)\n\t" \
    "s_branch vv_Ook59_%=\n\t" \
    "vv_Ook59_%=:\n\t" \
    "v_subrev_u32 v140, s80, v140\n\t" \
    "s_nop 1\n\t" \
    "v_max_i32_dpp v140, v140, v140 row_ror:1 row_mask:0xf bank_mask:0xf\n\t" \
    "s_nop 1\n\t" \
    "v_max_i32_dpp v140, v140, v140 row_ror:2 row_mask:0xf bank_mask:0xf\n\t" \
    "s_nop 1\n\t" \
    "v_max_i32_dpp v140, v140, v140 row_ror:4 row_mask:0xf bank_mask:0xf\n\t" \
    "s_nop 1\n\t" \
    "v_max_i32_dpp v140, v140, v140 row_ror:8 row_mask:0xf bank_mask:0xf\n\t" \
    "s_nop 0\n\t" \
    "v_readlane_b32 s69, v140, 0\n\t" \
    "v_readlane_b32 s71, v140, 16\n\t" \
    "v_readlane_b32 s70, v140, 32\n\t" \
    "v_readlane_b32 s81, v140, 48\n\t" \
    "s_max_i32 s69, s69, s71\n\t" \
    "s_nop 0\n\t" \
    "s_max_i32 s71, s70, s81\n\t" \
    "s_max_i32 s69, s69, s71\n\t" \
    "s_add_u32 s81, s69, s80\n\t" \
    "vv_Oc60_%=:\n\t" \
    "s_mul_hi_u32 s69, s85, s78\n\t" \
    "s_add_u32 s79, s81, s69\n\t" \
    "s_add_u32 s79, s79, s87\n\t" \
    "s_cmp_ge_u32 s65, 36\n\t" \
    "s_cselect_b32 s69, 28, 0\n\t" \
    "s_sub_u32 s79, s79, s69\n\t" \
    "s_cmp_lt_u32 s65, 8\n\t" \
    "s_cselect_b32 s69, 96, 0\n\t" \
    "s_add_u32 s79, s79, s69\n\t" \
    "s_add_u32 s80, s79, s88\n\t" \
    "s_add_u32 s65, s65, 1\n\t" \
    "s_branch vv_go41_%=\n\t" \
    "vv_Gbad46_%=:\n\t" \
    "s_waitcnt vmcnt(0) lgkmcnt(0)\n\t" \
    "s_nop 7\n\t" \
    "v_mov_b32 v72, 0\n\t" \
    "v_mov_b32 v73, 0\n\t" \
    "v_mov_b32 v74, 0\n\t" \
    "v_mov_b32 v75, 0\n\t" \
    "v_mov_b32 v76, 0\n\t" \
    "v_mov_b32 v77, 0\n\t" \
    "v_mov_b32 v78, 0\n\t" \
    "v_mov_b32 v79, 0\n\t" \
    "s_cmp_eq_u32 s68, 0\n\t" \
    "s_cbranch_scc1 vv_F63_%=\n\t" \
    "s_sub_u32 s68, s68, 1\n\t" \
    "s_branch vv_G45_%=\n\t" \
    "vv_F63_%=:\n\t" \
    "v_mov_b32 v140, 6095104\n\t" \
    "v_mov_b32 v141, 1\n\t" \
    "global_store_dword v140, v141, s[36:37] sc1\n\t" \
    "s_waitcnt vmcnt(0)\n\t" \
    "s_branch vv_Gc48_%=\n\t" \
    "vv_Gbad247_%=:\n\t" \
    "global_load_dwordx4 v[44:47], v2, s[46:47] sc1\n\t" \
    "s_waitcnt vmcnt(0)\n\t" \
    "v_max3_u32 v140, v44, v45, v46\n\t" \
    "v_max_u32 v140, v140, v47\n\t" \
    "v_cmp_eq_u32 vcc, -1, v140\n\t" \
    "s_cbranch_vccz vv_Gre250_%=\n\t" \
    "s_cmp_eq_u32 s68, 0\n\t" \
    "s_cbranch_scc1 vv_F64_%=\n\t" \
    "s_sub_u32 s68, s68, 1\n\t" \
    "s_sleep 1\n\t" \
    "s_branch vv_Gbad247_%=\n\t" \
    "vv_F64_%=:\n\t" \
    "v_mov_b32 v140, 6095104\n\t" \
    "v_mov_b32 v141, 2\n\t" \
    "global_store_dword v140, v141, s[36:37] sc1\n\t" \
    "s_waitcnt vmcnt(0)\n\t" \
    "s_branch vv_Gre250_%=\n\t" \
    "vv_Gre250_%=:\n\t" \
    "v_mov_b32 v72, 0\n\t" \
    "v_mov_b32 v73, 0\n\t" \
    "v_mov_b32 v74, 0\n\t" \
    "v_mov_b32 v75, 0\n\t" \
    "v_mov_b32 v76, 0\n\t" \
    "v_mov_b32 v77, 0\n\t" \
    "v_mov_b32 v78, 0\n\t" \
    "v_mov_b32 v79, 0\n\t" \
    "v_smfmac_f32_16x16x64_bf16 v[72:75], v[36:39], a[16:23], v10\n\t" \
    "s_nop 0\n\t" \
    "v_smfmac_f32_16x16x64_bf16 v[76:79], v[36:39], a[88:95], v10\n\t" \
    "v_smfmac_f32_16x16x64_bf16 v[72:75], v[40:43], a[24:31], v10\n\t" \
    "v_smfmac_f32_16x16x64_bf16 v[76:79], v[40:43], a[96:103], v10\n\t" \
    "v_smfmac_f32_16x16x64_bf16 v[72:75], v[44:47], a[64:71], v10\n\t" \
    "v_smfmac_f32_16x16x64_bf16 v[76:79], v[44:47], a[136:143], v10\n\t" \
    "s_branch vv_Gc249_%=\n\t" \
    "vv_end40_%=:\n\t" \
    "s_endpgm\n\t" \
    "vv_loop12_%=:\n\t" \
    "v_mov_b32 v72, 0\n\t" \
    "v_mov_b32 v73, 0\n\t" \
    "v_mov_b32 v74, 0\n\t" \
    "v_mov_b32 v75, 0\n\t" \
    "v_mov_b32 v76, 0\n\t" \
    "v_mov_b32 v77, 0\n\t" \
    "v_mov_b32 v78, 0\n\t" \
    "v_mov_b32 v79, 0\n\t" \
    "v_mov_b32 v92, 0\n\t" \
    "v_mov_b32 v93, 0\n\t" \
    "v_mov_b32 v94, 0\n\t" \
    "v_mov_b32 v95, 0\n\t" \
    "v_mov_b32 v96, 0\n\t" \
    "v_mov_b32 v97, 0\n\t" \
    "v_mov_b32 v98, 0\n\t" \
    "v_mov_b32 v99, 0\n\t" \
    "v_mov_b32 v100, 0\n\t" \
    "v_mov_b32 v101, 0\n\t" \
    "v_mov_b32 v102, 0\n\t" \
    "v_mov_b32 v103, 0\n\t" \
    "v_mov_b32 v104, 0\n\t" \
    "v_mov_b32 v105, 0\n\t" \
    "v_mov_b32 v106, 0\n\t" \
    "v_mov_b32 v107, 0\n\t" \
    "v_mov_b32 v108, 0\n\t" \
    "v_mov_b32 v109, 0\n\t" \
    "v_mov_b32 v110, 0\n\t" \
    "v_mov_b32 v111, 0\n\t" \
    "v_mov_b32 v112, 0\n\t" \
    "v_mov_b32 v113, 0\n\t" \
    "v_mov_b32 v114, 0\n\t" \
    "v_mov_b32 v115, 0\n\t" \
    "v_mov_b32 v116, 0\n\t" \
    "v_mov_b32 v117, 0\n\t" \
    "v_mov_b32 v118, 0\n\t" \
    "v_mov_b32 v119, 0\n\t" \
    "v_mov_b32 v120, 0\n\t" \
    "v_mov_b32 v121, 0\n\t" \
    "v_mov_b32 v122, 0\n\t" \
    "v_mov_b32 v123, 0\n\t" \
    "v_mov_b32 v124, 0\n\t" \
    "v_mov_b32 v125, 0\n\t" \
    "v_mov_b32 v126, 0\n\t" \
    "v_mov_b32 v127, 0\n\t" \
    "vv_go66_%=:\n\t" \
    "s_add_u32 s69, s65, 0\n\t" \
    "s_bitcmp1_b32 s69, 0\n\t" \
    "s_cselect_b32 s70, s63, s62\n\t" \
    "s_bfe_u32 s69, s69, 0x20001\n\t" \
    "s_lshl_b32 s69, s69, 19\n\t" \
    "s_add_u32 s69, s69, s70\n\t" \
    "s_add_u32 s46, s36, s69\n\t" \
    "s_addc_u32 s47, s37, 0\n\t" \
    "s_add_u32 s69, s65, 0\n\t" \
    "s_mul_i32 s70, s69, 171\n\t" \
    "s_lshr_b32 s70, s70, 9\n\t" \
    "s_mul_i32 s70, s70, 3\n\t" \
    "s_sub_u32 s69, s69, s70\n\t" \
    "s_mul_i32 s69, s69, 36\n\t" \
    "s_lshl_b32 s69, s69, 14\n\t" \
    "s_add_u32 s69, s69, s64\n\t" \
    "s_add_u32 s69, s69, 4194304\n\t" \
    "s_add_u32 s38, s36, s69\n\t" \
    "s_addc_u32 s39, s37, 0\n\t" \
    "s_mul_i32 s69, s65, 9216\n\t" \
    "s_add_u32 s69, s69, 123731968\n\t" \
    "s_add_u32 s40, s36, s69\n\t" \
    "s_addc_u32 s41, s37, 0\n\t" \
    "s_sub_u32 s70, s79, s74\n\t" \
    "s_max_i32 s70, s70, 0\n\t" \
    "s_min_i32 s70, s70, 16000\n\t" \
    "s_lshl_b32 s70, s70, 1\n\t" \
    "s_mul_hi_u32 s70, s70, s77\n\t" \
    "s_add_u32 s70, s70, s73\n\t" \
    "s_sub_u32 s70, s70, 100\n\t" \
    "s_getpc_b64 s[30:31]\n\t" \
    "vv_Wp68_%=:\n\t" \
    "s_add_u32 s30, s30, vv_We69_%=-vv_Wp68_%=\n\t" \
    "s_addc_u32 s31, s31, 0\n\t" \
    "vv_W67_%=:\n\t" \
    "s_memtime s[56:57]\n\t" \
    "s_waitcnt lgkmcnt(0)\n\t" \
    "s_sub_u32 s69, s70, s56\n\t" \
    "s_cmp_gt_i32 s69, 111\n\t" \
    "s_cbranch_scc1 vv_W67_%=\n\t" \
    "s_max_i32 s69, s69, 0\n\t" \
    "s_lshr_b32 s69, s69, 1\n\t" \
    "s_and_b32 s69, s69, 0x3c\n\t" \
    "s_sub_u32 s30, s30, s69\n\t" \
    "s_subb_u32 s31, s31, 0\n\t" \
    "s_setpc_b64 s[30:31]\n\t" \
    ".p2align 6\n\t" \
    "s_nop 0\n\t" \
    "s_nop 0\n\t" \
    "s_nop 0\n\t" \
    "s_nop 1\n\t" \
    "s_nop 1\n\t" \
    "s_nop 1\n\t" \
    "s_nop 1\n\t" \
    "s_nop 1\n\t" \
    "s_nop 1\n\t" \
    "s_nop 1\n\t" \
    "s_nop 1\n\t" \
    "s_nop 1\n\t" \
    "s_nop 1\n\t" \
    "s_nop 1\n\t" \
    "s_nop 1\n\t" \
    "s_nop 1\n\t" \
    "vv_We69_%=:\n\t" \
    "vv_G70_%=:\n\t" \
    "global_load_dwordx4 v[36:39], v1, s[46:47] sc1\n\t" \
    "s_nop 9\n\t" \
    "global_load_dwordx4 v[40:43], v1, s[46:47] offset:1024 sc1\n\t" \
    "s_nop 9\n\t" \
    "global_load_dwordx4 v[44:47], v2, s[46:47] sc1\n\t" \
    "s_waitcnt vmcnt(2)\n\t" \
    "v_smfmac_f32_16x16x64_bf16 v[72:75], v[36:39], a[32:39], v10\n\t" \
    "ds_write_b128 v0, v[36:39] offset:4096\n\t" \
    "v_smfmac_f32_16x16x64_bf16 v[76:79], v[36:39], a[104:111], v10\n\t" \
    "v_max3_u32 v140, v36, v37, v38\n\t" \
    "s_waitcnt vmcnt(1)\n\t" \
    "ds_write_b128 v0, v[40:43] offset:5120\n\t" \
    "v_smfmac_f32_16x16x64_bf16 v[72:75], v[40:43], a[40:47], v10\n\t" \
    "v_max3_u32 v140, v140, v39, v40\n\t" \
    "v_max3_u32 v140, v140, v41, v42\n\t" \
    "v_max_u32 v140, v140, v43\n\t" \
    "v_cmp_eq_u32 vcc, -1, v140\n\t" \
    "v_smfmac_f32_16x16x64_bf16 v[76:79], v[40:43], a[112:119], v10\n\t" \
    "s_cbranch_vccnz vv_Gbad71_%=\n\t" \
    "vv_Gc73_%=:\n\t" \
    "s_waitcnt lgkmcnt(0)\n\t" \
    "s_barrier\n\t" \
    "ds_read_b128 v[48:51], v0 offset:6144\n\t" \
    "ds_read_b128 v[52:55], v0 offset:7168\n\t" \
    "s_waitcnt vmcnt(0)\n\t" \
    "v_smfmac_f32_16x16x64_bf16 v[72:75], v[44:47], a[64:71], v10\n\t" \
    "ds_read_b128 v[56:59], v0 offset:0\n\t" \
    "ds_read_b128 v[60:63], v0 offset:1024\n\t" \
    "v_max3_u32 v140, v44, v45, v46\n\t" \
    "v_max_u32 v140, v140, v47\n\t" \
    "v_smfmac_f32_16x16x64_bf16 v[76:79], v[44:47], a[136:143], v10\n\t" \
    "ds_read_b128 v[64:67], v0 offset:2048\n\t" \
    "ds_read_b128 v[68:71], v0 offset:3072\n\t" \
    "v_cmp_eq_u32 vcc, -1, v140\n\t" \
    "s_cbranch_vccnz vv_Gbad272_%=\n\t" \
    "vv_Gc274_%=:\n\t" \
    "s_waitcnt lgkmcnt(5)\n\t" \
    "v_smfmac_f32_16x16x64_bf16 v[72:75], v[48:51], a[48:55], v10\n\t" \
    "s_waitcnt lgkmcnt(4)\n\t" \
    "v_smfmac_f32_16x16x64_bf16 v[72:75], v[52:55], a[56:63], v10\n\t" \
    "s_waitcnt lgkmcnt(3)\n\t" \
    "v_smfmac_f32_16x16x64_bf16 v[72:75], v[56:59], a[0:7], v10\n\t" \
    "s_waitcnt lgkmcnt(2)\n\t" \
    "v_smfmac_f32_16x16x64_bf16 v[72:75], v[60:63], a[8:15], v10\n\t" \
    "s_waitcnt lgkmcnt(1)\n\t" \
    "v_smfmac_f32_16x16x64_bf16 v[72:75], v[64:67], a[16:23], v10\n\t" \
    "s_waitcnt lgkmcnt(0)\n\t" \
    "v_smfmac_f32_16x16x64_bf16 v[72:75], v[68:71], a[24:31], v10\n\t" \
    "v_smfmac_f32_16x16x64_bf16 v[76:79], v[48:51], a[120:127], v10\n\t" \
    "s_nop 1\n\t" \
    "s_nop 0\n\t" \
    "v_smfmac_f32_16x16x64_bf16 v[76:79], v[52:55], a[128:135], v10\n\t" \
    "s_nop 1\n\t" \
    "s_nop 0\n\t" \
    "v_smfmac_f32_16x16x64_bf16 v[76:79], v[56:59], a[72:79], v10\n\t" \
    "v_add_f32 v72, v72, v74\n\t" \
    "v_smfmac_f32_16x16x64_bf16 v[76:79], v[60:63], a[80:87], v10\n\t" \
    "v_add_f32 v73, v73, v75\n\t" \
    "v_smfmac_f32_16x16x64_bf16 v[76:79], v[64:67], a[88:95], v10\n\t" \
    "v_cvt_pk_bf16_f32 v80, v72, v73\n\t" \
    "v_smfmac_f32_16x16x64_bf16 v[76:79], v[68:71], a[96:103], v10\n\t" \
    "ds_write_b32 v12, v80 offset:0\n\t" \
    "s_nop 6\n\t" \
    "v_add_f32 v76, v76, v78\n\t" \
    "v_add_f32 v77, v77, v79\n\t" \
    "v_cvt_pk_bf16_f32 v81, v76, v77\n\t" \
    "ds_write_b32 v12, v81 offset:64\n\t" \
    "s_waitcnt lgkmcnt(0)\n\t" \
    "s_barrier\n\t" \
    "ds_read_b64_tr_b16 v[84:85], v13 offset:0\n\t" \
    "ds_read_b64_tr_b16 v[86:87], v13 offset:128\n\t" \
    "ds_read_b64_tr_b16 v[88:89], v13 offset:1024\n\t" \
    "ds_read_b64_tr_b16 v[90:91], v13 offset:1152\n\t" \
    "s_waitcnt lgkmcnt(2)\n\t" \
    "v_smfmac_f32_16x16x64_bf16 v[92:95], v[84:87], a[144:151], v10\n\t" \
    "s_nop 2\n\t" \
    "s_waitcnt lgkmcnt(0)\n\t" \
    "v_smfmac_f32_16x16x64_bf16 v[92:95], v[88:91], a[152:159], v10\n\t" \
    "s_nop 2\n\t" \
    "v_smfmac_f32_16x16x64_bf16 v[96:99], v[84:87], a[160:167], v10\n\t" \
    "s_nop 2\n\t" \
    "v_smfmac_f32_16x16x64_bf16 v[96:99], v[88:91], a[168:175], v10\n\t" \
    "s_nop 2\n\t" \
    "v_smfmac_f32_16x16x64_bf16 v[100:103], v[84:87], a[176:183], v10\n\t" \
    "s_nop 0\n\t" \
    "v_add_f32 v92, v92, v94\n\t" \
    "v_add_f32 v93, v93, v95\n\t" \
    "v_smfmac_f32_16x16x64_bf16 v[100:103], v[88:91], a[184:191], v10\n\t" \
    "s_nop 1\n\t" \
    "v_cvt_pk_bf16_f32 v128, v92, v93\n\t" \
    "v_smfmac_f32_16x16x64_bf16 v[104:107], v[84:87], a[192:199], v10\n\t" \
    "s_nop 0\n\t" \
    "v_add_f32 v96, v96, v98\n\t" \
    "v_add_f32 v97, v97, v99\n\t" \
    "v_smfmac_f32_16x16x64_bf16 v[104:107], v[88:91], a[200:207], v10\n\t" \
    "s_nop 1\n\t" \
    "v_cvt_pk_bf16_f32 v129, v96, v97\n\t" \
    "v_smfmac_f32_16x16x64_bf16 v[124:127], v[84:87], v[240:247], v10\n\t" \
    "s_nop 0\n\t" \
    "v_add_f32 v100, v100, v102\n\t" \
    "v_add_f32 v101, v101, v103\n\t" \
    "v_smfmac_f32_16x16x64_bf16 v[124:127], v[88:91], v[248:255], v10\n\t" \
    "s_nop 1\n\t" \
    "global_load_dword v140, v20, s[46:47] sc1\n\t" \
    "v_cvt_pk_bf16_f32 v130, v100, v101\n\t" \
    "v_smfmac_f32_16x16x64_bf16 v[108:111], v[84:87], a[208:215], v10\n\t" \
    "s_nop 0\n\t" \
    "v_add_f32 v104, v104, v106\n\t" \
    "v_add_f32 v105, v105, v107\n\t" \
    "v_smfmac_f32_16x16x64_bf16 v[108:111], v[88:91], a[216:223], v10\n\t" \
    "s_nop 0\n\t" \
    "v_cvt_pk_bf16_f32 v131, v104, v105\n\t" \
    "v_permlane32_swap_b32 v128, v130\n\t" \
    "v_smfmac_f32_16x16x64_bf16 v[112:115], v[84:87], a[224:231], v10\n\t" \
    "s_nop 0\n\t" \
    "v_permlane32_swap_b32 v129, v131\n\t" \
    "s_nop 1\n\t" \
    "v_permlane16_swap_b32 v128, v129\n\t" \
    "v_smfmac_f32_16x16x64_bf16 v[112:115], v[88:91], a[232:239], v10\n\t" \
    "v_permlane16_swap_b32 v130, v131\n\t" \
    "global_store_dwordx4 v14, v[128:131], s[38:39] sc1\n\t" \
    "v_add_f32 v124, v124, v126\n\t" \
    "v_smfmac_f32_16x16x64_bf16 v[116:119], v[84:87], a[240:247], v10\n\t" \
    "s_nop 0\n\t" \
    "v_add_f32 v125, v125, v127\n\t" \
    "v_cvt_pk_bf16_f32 v136, v124, v125\n\t" \
    "v_smfmac_f32_16x16x64_bf16 v[116:119], v[88:91], a[248:255], v10\n\t" \
    "global_store_dword v16, v136, s[38:39] sc1\n\t" \
    "v_add_f32 v108, v108, v110\n\t" \
    "v_add_f32 v109, v109, v111\n\t" \
    "v_smfmac_f32_16x16x64_bf16 v[120:123], v[84:87], v[224:231], v10\n\t" \
    "s_nop 0\n\t" \
    "v_cvt_pk_bf16_f32 v132, v108, v109\n\t" \
    "v_add_f32 v112, v112, v114\n\t" \
    "v_smfmac_f32_16x16x64_bf16 v[120:123], v[88:91], v[232:239], v10\n\t" \
    "v_add_f32 v113, v113, v115\n\t" \
    "v_cvt_pk_bf16_f32 v133, v112, v113\n\t" \
    "v_add_f32 v116, v116, v118\n\t" \
    "v_add_f32 v117, v117, v119\n\t" \
    "v_cvt_pk_bf16_f32 v134, v116, v117\n\t" \
    "s_nop 2\n\t" \
    "v_pk_add_f32 v[120:121], v[120:121], v[122:123]\n\t" \
    "v_cvt_pk_bf16_f32 v135, v120, v121\n\t" \
    "v_permlane32_swap_b32 v132, v134\n\t" \
    "s_nop 0\n\t" \
    "v_permlane32_swap_b32 v133, v135\n\t" \
    "s_nop 1\n\t" \
    "v_permlane16_swap_b32 v132, v133\n\t" \
    "v_permlane16_swap_b32 v134, v135\n\t" \
    "global_store_dwordx4 v15, v[132:135], s[38:39] sc1\n\t" \
    "s_nop 1\n\t" \
    "global_store_dwordx4 v17, v[128:131], s[40:41] sc1\n\t" \
    "global_store_dwordx4 v17, v[132:135], s[40:41] offset:1024 sc1\n\t" \
    "global_store_dword v18, v136, s[40:41] sc1\n\t" \
    "s_cmp_eq_u32 s65, 63\n\t" \
    "s_cbranch_scc1 vv_end65_%=\n\t" \
    "s_mov_b32 s71, 3\n\t" \
    "vv_E76_%=:\n\t" \
    "s_memtime s[54:55]\n\t" \
    "s_nop 0\n\t" \
    "s_memrealtime s[4:5]\n\t" \
    "s_nop 0\n\t" \
    "s_memrealtime s[6:7]\n\t" \
    "s_nop 0\n\t" \
    "s_memrealtime s[8:9]\n\t" \
    "s_nop 0\n\t" \
    "s_memrealtime s[10:11]\n\t" \
    "s_nop 0\n\t" \
    "s_memrealtime s[12:13]\n\t" \
    "s_nop 0\n\t" \
    "s_memrealtime s[14:15]\n\t" \
    "s_nop 0\n\t" \
    "s_memrealtime s[16:17]\n\t" \
    "s_nop 0\n\t" \
    "s_memrealtime s[18:19]\n\t" \
    "s_nop 0\n\t" \
    "s_memrealtime s[20:21]\n\t" \
    "s_nop 0\n\t" \
    "s_memrealtime s[22:23]\n\t" \
    "s_nop 0\n\t" \
    "s_memrealtime s[24:25]\n\t" \
    "s_nop 0\n\t" \
    "s_memrealtime s[26:27]\n\t" \
    "s_nop 0\n\t" \
    "s_memrealtime s[28:29]\n\t" \
    "s_nop 0\n\t" \
    "s_memtime s[56:57]\n\t" \
    "s_mov_b32 s70, 1\n\t" \
    "s_sub_u32 s71, s71, 1\n\t" \
    "s_cselect_b32 s69, 0, -1\n\t" \
    "s_waitcnt lgkmcnt(0)\n\t" \
    "s_sub_u32 s54, s56, s54\n\t" \
    "s_and_b32 s54, s54, s69\n\t" \
    "s_cmp_gt_u32 s54, 124\n\t" \
    "s_cbranch_scc1 vv_E76_%=\n\t" \
    "s_cmp_eq_u32 s4, s28\n\t" \
    "s_addc_u32 s70, s70, 0\n\t" \
    "s_cmp_eq_u32 s6, s28\n\t" \
    "s_addc_u32 s70, s70, 0\n\t" \
    "s_cmp_eq_u32 s8, s28\n\t" \
    "s_addc_u32 s70, s70, 0\n\t" \
    "s_cmp_eq_u32 s10, s28\n\t" \
    "s_addc_u32 s70, s70, 0\n\t" \
    "s_cmp_eq_u32 s12, s28\n\t" \
    "s_addc_u32 s70, s70, 0\n\t" \
    "s_cmp_eq_u32 s14, s28\n\t" \
    "s_addc_u32 s70, s70, 0\n\t" \
    "s_cmp_eq_u32 s16, s28\n\t" \
    "s_addc_u32 s70, s70, 0\n\t" \
    "s_cmp_eq_u32 s18, s28\n\t" \
    "s_addc_u32 s70, s70, 0\n\t" \
    "s_cmp_eq_u32 s20, s28\n\t" \
    "s_addc_u32 s70, s70, 0\n\t" \
    "s_cmp_eq_u32 s22, s28\n\t" \
    "s_addc_u32 s70, s70, 0\n\t" \
    "s_cmp_eq_u32 s24, s28\n\t" \
    "s_addc_u32 s70, s70, 0\n\t" \
    "s_cmp_eq_u32 s26, s28\n\t" \
    "s_addc_u32 s70, s70, 0\n\t" \
    "s_mov_b32 s75, s73\n\t" \
    "s_mov_b32 s76, s74\n\t" \
    "s_lshl_b32 s70, s70, 3\n\t" \
    "s_sub_u32 s73, s56, s70\n\t" \
    "s_lshl_b32 s74, s28, 4\n\t" \
    "s_sub_u32 s69, s73, s75\n\t" \
    "s_sub_u32 s70, s74, s76\n\t" \
    "s_sub_u32 s71, s70, 1600\n\t" \
    "s_cmp_lt_u32 s71, 400000\n\t" \
    "s_cbranch_scc0 vv_K78_%=\n\t" \
    "v_cvt_f32_u32 v141, s69\n\t" \
    "v_cvt_f32_u32 v142, s70\n\t" \
    "v_rcp_f32 v143, v141\n\t" \
    "v_rcp_f32 v144, v142\n\t" \
    "v_mul_f32 v143, v143, v142\n\t" \
    "v_mul_f32 v144, v144, v141\n\t" \
    "v_mul_f32 v143, 0x4f800000, v143\n\t" \
    "v_mul_f32 v144, 0x4f000000, v144\n\t" \
    "v_cvt_u32_f32 v143, v143\n\t" \
    "v_cvt_u32_f32 v144, v144\n\t" \
    "v_readfirstlane_b32 s69, v143\n\t" \
    "v_readfirstlane_b32 s70, v144\n\t" \
    "s_nop 1\n\t" \
    "s_sub_u32 s69, s69, s78\n\t" \
    "s_ashr_i32 s69, s69, 2\n\t" \
    "s_add_u32 s78, s78, s69\n\t" \
    "s_sub_u32 s70, s70, s77\n\t" \
    "s_ashr_i32 s70, s70, 2\n\t" \
    "s_add_u32 s77, s77, s70\n\t" \
    "vv_K78_%=:\n\t" \
    "s_waitcnt vmcnt(6)\n\t" \
    "v_cmp_eq_u32 vcc, -1, v140\n\t" \
    "s_cbranch_vccz vv_Hok80_%=\n\t" \
    "vv_H79_%=:\n\t" \
    "global_load_dword v140, v20, s[46:47] sc1\n\t" \
    "s_waitcnt vmcnt(0)\n\t" \
    "v_cmp_eq_u32 vcc, -1, v140\n\t" \
    "s_cbranch_vccz vv_Hc81_%=\n\t" \
    "s_cmp_eq_u32 s68, 0\n\t" \
    "s_cbranch_scc1 vv_F82_%=\n\t" \
    "s_sub_u32 s68, s68, 1\n\t" \
    "s_sleep 5\n\t" \
    "s_branch vv_H79_%=\n\t" \
    "vv_F82_%=:\n\t" \
    "v_mov_b32 v141, 6095104\n\t" \
    "v_mov_b32 v142, 6\n\t" \
    "global_store_dword v141, v142, s[36:37] sc1\n\t" \
    "s_waitcnt vmcnt(0)\n\t" \
    "s_branch vv_Hc81_%=\n\t" \
    "vv_Hc81_%=:\n\t" \
    "vv_Hok80_%=:\n\t" \
    "v_subrev_u32 v140, s80, v140\n\t" \
    "v_and_b32 v20, -5, v20\n\t" \
    "s_nop 0\n\t" \
    "v_max_i32_dpp v140, v140, v140 row_ror:1 row_mask:0xf bank_mask:0xf\n\t" \
    "s_nop 1\n\t" \
    "v_max_i32_dpp v140, v140, v140 row_ror:2 row_mask:0xf bank_mask:0xf\n\t" \
    "s_nop 1\n\t" \
    "v_max_i32_dpp v140, v140, v140 row_ror:4 row_mask:0xf bank_mask:0xf\n\t" \
    "s_nop 1\n\t" \
    "v_max_i32_dpp v140, v140, v140 row_ror:8 row_mask:0xf bank_mask:0xf\n\t" \
    "s_nop 0\n\t" \
    "v_readlane_b32 s69, v140, 0\n\t" \
    "v_readlane_b32 s71, v140, 16\n\t" \
    "v_readlane_b32 s70, v140, 32\n\t" \
    "v_readlane_b32 s81, v140, 48\n\t" \
    "s_max_i32 s69, s69, s71\n\t" \
    "s_nop 0\n\t" \
    "s_max_i32 s71, s70, s81\n\t" \
    "s_max_i32 s69, s69, s71\n\t" \
    "s_add_u32 s81, s69, s80\n\t" \
    "v_mov_b32 v72, 0\n\t" \
    "v_mov_b32 v73, 0\n\t" \
    "v_mov_b32 v74, 0\n\t" \
    "v_mov_b32 v75, 0\n\t" \
    "v_mov_b32 v76, 0\n\t" \
    "v_mov_b32 v77, 0\n\t" \
    "v_mov_b32 v78, 0\n\t" \
    "v_mov_b32 v79, 0\n\t" \
    "v_mov_b32 v92, 0\n\t" \
    "v_mov_b32 v93, 0\n\t" \
    "v_mov_b32 v94, 0\n\t" \
    "v_mov_b32 v95, 0\n\t" \
    "v_mov_b32 v96, 0\n\t" \
    "v_mov_b32 v97, 0\n\t" \
    "v_mov_b32 v98, 0\n\t" \
    "v_mov_b32 v99, 0\n\t" \
    "v_mov_b32 v100, 0\n\t" \
    "v_mov_b32 v101, 0\n\t" \
    "v_mov_b32 v102, 0\n\t" \
    "v_mov_b32 v103, 0\n\t" \
    "v_mov_b32 v104, 0\n\t" \
    "v_mov_b32 v105, 0\n\t" \
    "v_mov_b32 v106, 0\n\t" \
    "v_mov_b32 v107, 0\n\t" \
    "v_mov_b32 v108, 0\n\t" \
    "v_mov_b32 v109, 0\n\t" \
    "v_mov_b32 v110, 0\n\t" \
    "v_mov_b32 v111, 0\n\t" \
    "v_mov_b32 v112, 0\n\t" \
    "v_mov_b32 v113, 0\n\t" \
    "v_mov_b32 v114, 0\n\t" \
    "v_mov_b32 v115, 0\n\t" \
    "v_mov_b32 v116, 0\n\t" \
    "v_mov_b32 v117, 0\n\t" \
    "v_mov_b32 v118, 0\n\t" \
    "v_mov_b32 v119, 0\n\t" \
    "v_mov_b32 v120, 0\n\t" \
    "v_mov_b32 v121, 0\n\t" \
    "v_mov_b32 v122, 0\n\t" \
    "v_mov_b32 v123, 0\n\t" \
    "v_mov_b32 v124, 0\n\t" \
    "v_mov_b32 v125, 0\n\t" \
    "v_mov_b32 v126, 0\n\t" \
    "v_mov_b32 v127, 0\n\t" \
    "s_cmp_lg_u32 s65, 0\n\t" \
    "s_cbranch_scc1 vv_Oc85_%=\n\t" \
    "s_add_u32 s79, s81, 240\n\t" \
    "s_sub_u32 s70, s79, s74\n\t" \
    "s_max_i32 s70, s70, 0\n\t" \
    "s_min_i32 s70, s70, 16000\n\t" \
    "s_lshl_b32 s70, s70, 1\n\t" \
    "s_mul_hi_u32 s70, s70, s77\n\t" \
    "s_add_u32 s70, s70, s73\n\t" \
    "vv_W86_%=:\n\t" \
    "s_memtime s[56:57]\n\t" \
    "s_waitcnt lgkmcnt(0)\n\t" \
    "s_sub_u32 s69, s70, s56\n\t" \
    "s_cmp_gt_i32 s69, 24\n\t" \
    "s_cbranch_scc1 vv_W86_%=\n\t" \
    "global_load_dword v140, v35, s[46:47] sc1\n\t" \
    "s_waitcnt vmcnt(0)\n\t" \
    "v_cmp_eq_u32 vcc, -1, v140\n\t" \
    "s_cbranch_vccz vv_Ook84_%=\n\t" \
    "vv_O83_%=:\n\t" \
    "global_load_dword v140, v35, s[46:47] offset:1536 sc1\n\t" \
    "s_waitcnt vmcnt(0)\n\t" \
    "v_cmp_eq_u32 vcc, -1, v140\n\t" \
    "s_cbranch_vccz vv_Ook84_%=\n\t" \
    "s_cmp_eq_u32 s68, 0\n\t" \
    "s_cbranch_scc1 vv_F87_%=\n\t" \
    "s_sub_u32 s68, s68, 1\n\t" \
    "s_sleep 3\n\t" \
    "s_branch vv_O83_%=\n\t" \
    "vv_F87_%=:\n\t" \
    "v_mov_b32 v141, 6095104\n\t" \
    "v_mov_b32 v142, 17\n\t" \
    "global_store_dword v141, v142, s[36:37] sc1\n\t" \
    "s_waitcnt vmcnt(0)\n\t" \
    "s_branch vv_Ook84_%=\n\t" \
    "vv_Ook84_%=:\n\t" \
    "v_subrev_u32 v140, s80, v140\n\t" \
    "s_nop 1\n\t" \
    "v_max_i32_dpp v140, v140, v140 row_ror:1 row_mask:0xf bank_mask:0xf\n\t" \
    "s_nop 1\n\t" \
    "v_max_i32_dpp v140, v140, v140 row_ror:2 row_mask:0xf bank_mask:0xf\n\t" \
    "s_nop 1\n\t" \
    "v_max_i32_dpp v140, v140, v140 row_ror:4 row_mask:0xf bank_mask:0xf\n\t" \
    "s_nop 1\n\t" \
    "v_max_i32_dpp v140, v140, v140 row_ror:8 row_mask:0xf bank_mask:0xf\n\t" \
    "s_nop 0\n\t" \
    "v_readlane_b32 s69, v140, 0\n\t" \
    "v_readlane_b32 s71, v140, 16\n\t" \
    "v_readlane_b32 s70, v140, 32\n\t" \
    "v_readlane_b32 s81, v140, 48\n\t" \
    "s_max_i32 s69, s69, s71\n\t" \
    "s_nop 0\n\t" \
    "s_max_i32 s71, s70, s81\n\t" \
    "s_max_i32 s69, s69, s71\n\t" \
    "s_add_u32 s81, s69, s80\n\t" \
    "vv_Oc85_%=:\n\t" \
    "s_mul_hi_u32 s69, s85, s78\n\t" \
    "s_add_u32 s79, s81, s69\n\t" \
    "s_add_u32 s79, s79, s87\n\t" \
    "s_cmp_ge_u32 s65, 36\n\t" \
    "s_cselect_b32 s69, 28, 0\n\t" \
    "s_sub_u32 s79, s79, s69\n\t" \
    "s_cmp_lt_u32 s65, 8\n\t" \
    "s_cselect_b32 s69, 96, 0\n\t" \
    "s_add_u32 s79, s79, s69\n\t" \
    "s_add_u32 s80, s79, s88\n\t" \
    "s_add_u32 s65, s65, 1\n\t" \
    "s_branch vv_go66_%=\n\t" \
    "vv_Gbad71_%=:\n\t" \
    "s_waitcnt vmcnt(0) lgkmcnt(0)\n\t" \
    "s_nop 7\n\t" \
    "v_mov_b32 v72, 0\n\t" \
    "v_mov_b32 v73, 0\n\t" \
    "v_mov_b32 v74, 0\n\t" \
    "v_mov_b32 v75, 0\n\t" \
    "v_mov_b32 v76, 0\n\t" \
    "v_mov_b32 v77, 0\n\t" \
    "v_mov_b32 v78, 0\n\t" \
    "v_mov_b32 v79, 0\n\t" \
    "s_cmp_eq_u32 s68, 0\n\t" \
    "s_cbranch_scc1 vv_F88_%=\n\t" \
    "s_sub_u32 s68, s68, 1\n\t" \
    "s_branch vv_G70_%=\n\t" \
    "vv_F88_%=:\n\t" \
    "v_mov_b32 v140, 6095104\n\t" \
    "v_mov_b32 v141, 1\n\t" \
    "global_store_dword v140, v141, s[36:37] sc1\n\t" \
    "s_waitcnt vmcnt(0)\n\t" \
    "s_branch vv_Gc73_%=\n\t" \
    "vv_Gbad272_%=:\n\t" \
    "global_load_dwordx4 v[44:47], v2, s[46:47] sc1\n\t" \
    "s_waitcnt vmcnt(0)\n\t" \
    "v_max3_u32 v140, v44, v45, v46\n\t" \
    "v_max_u32 v140, v140, v47\n\t" \
    "v_cmp_eq_u32 vcc, -1, v140\n\t" \
    "s_cbranch_vccz vv_Gre275_%=\n\t" \
    "s_cmp_eq_u32 s68, 0\n\t" \
    "s_cbranch_scc1 vv_F89_%=\n\t" \
    "s_sub_u32 s68, s68, 1\n\t" \
    "s_sleep 1\n\t" \
    "s_branch vv_Gbad272_%=\n\t" \
    "vv_F89_%=:\n\t" \
    "v_mov_b32 v140, 6095104\n\t" \
    "v_mov_b32 v141, 2\n\t" \
    "global_store_dword v140, v141, s[36:37] sc1\n\t" \
    "s_waitcnt vmcnt(0)\n\t" \
    "s_branch vv_Gre275_%=\n\t" \
    "vv_Gre275_%=:\n\t" \
    "v_mov_b32 v72, 0\n\t" \
    "v_mov_b32 v73, 0\n\t" \
    "v_mov_b32 v74, 0\n\t" \
    "v_mov_b32 v75, 0\n\t" \
    "v_mov_b32 v76, 0\n\t" \
    "v_mov_b32 v77, 0\n\t" \
    "v_mov_b32 v78, 0\n\t" \
    "v_mov_b32 v79, 0\n\t" \
    "v_smfmac_f32_16x16x64_bf16 v[72:75], v[36:39], a[32:39], v10\n\t" \
    "s_nop 0\n\t" \
    "v_smfmac_f32_16x16x64_bf16 v[76:79], v[36:39], a[104:111], v10\n\t" \
    "v_smfmac_f32_16x16x64_bf16 v[72:75], v[40:43], a[40:47], v10\n\t" \
    "v_smfmac_f32_16x16x64_bf16 v[76:79], v[40:43], a[112:119], v10\n\t" \
    "v_smfmac_f32_16x16x64_bf16 v[72:75], v[44:47], a[64:71], v10\n\t" \
    "v_smfmac_f32_16x16x64_bf16 v[76:79], v[44:47], a[136:143], v10\n\t" \
    "s_branch vv_Gc274_%=\n\t" \
    "vv_end65_%=:\n\t" \
    "s_endpgm\n\t" \
    "vv_loop13_%=:\n\t" \
    "v_mov_b32 v72, 0\n\t" \
    "v_mov_b32 v73, 0\n\t" \
    "v_mov_b32 v74, 0\n\t" \
    "v_mov_b32 v75, 0\n\t" \
    "v_mov_b32 v76, 0\n\t" \
    "v_mov_b32 v77, 0\n\t" \
    "v_mov_b32 v78, 0\n\t" \
    "v_mov_b32 v79, 0\n\t" \
    "v_mov_b32 v92, 0\n\t" \
    "v_mov_b32 v93, 0\n\t" \
    "v_mov_b32 v94, 0\n\t" \
    "v_mov_b32 v95, 0\n\t" \
    "v_mov_b32 v96, 0\n\t" \
    "v_mov_b32 v97, 0\n\t" \
    "v_mov_b32 v98, 0\n\t" \
    "v_mov_b32 v99, 0\n\t" \
    "v_mov_b32 v100, 0\n\t" \
    "v_mov_b32 v101, 0\n\t" \
    "v_mov_b32 v102, 0\n\t" \
    "v_mov_b32 v103, 0\n\t" \
    "v_mov_b32 v104, 0\n\t" \
    "v_mov_b32 v105, 0\n\t" \
    "v_mov_b32 v106, 0\n\t" \
    "v_mov_b32 v107, 0\n\t" \
    "v_mov_b32 v108, 0\n\t" \
    "v_mov_b32 v109, 0\n\t" \
    "v_mov_b32 v110, 0\n\t" \
    "v_mov_b32 v111, 0\n\t" \
    "v_mov_b32 v112, 0\n\t" \
    "v_mov_b32 v113, 0\n\t" \
    "v_mov_b32 v114, 0\n\t" \
    "v_mov_b32 v115, 0\n\t" \
    "v_mov_b32 v116, 0\n\t" \
    "v_mov_b32 v117, 0\n\t" \
    "v_mov_b32 v118, 0\n\t" \
    "v_mov_b32 v119, 0\n\t" \
    "v_mov_b32 v120, 0\n\t" \
    "v_mov_b32 v121, 0\n\t" \
    "v_mov_b32 v122, 0\n\t" \
    "v_mov_b32 v123, 0\n\t" \
    "v_mov_b32 v124, 0\n\t" \
    "v_mov_b32 v125, 0\n\t" \
    "v_mov_b32 v126, 0\n\t" \
    "v_mov_b32 v127, 0\n\t" \
    "vv_go91_%=:\n\t" \
    "s_add_u32 s69, s65, 0\n\t" \
    "s_bitcmp1_b32 s69, 0\n\t" \
    "s_cselect_b32 s70, s63, s62\n\t" \
    "s_bfe_u32 s69, s69, 0x20001\n\t" \
    "s_lshl_b32 s69, s69, 19\n\t" \
    "s_add_u32 s69, s69, s70\n\t" \
    "s_add_u32 s46, s36, s69\n\t" \
    "s_addc_u32 s47, s37, 0\n\t" \
    "s_add_u32 s69, s65, 0\n\t" \
    "s_mul_i32 s70, s69, 171\n\t" \
    "s_lshr_b32 s70, s70, 9\n\t" \
    "s_mul_i32 s70, s70, 3\n\t" \
    "s_sub_u32 s69, s69, s70\n\t" \
    "s_mul_i32 s69, s69, 36\n\t" \
    "s_lshl_b32 s69, s69, 14\n\t" \
    "s_add_u32 s69, s69, s64\n\t" \
    "s_add_u32 s69, s69, 4194304\n\t" \
    "s_add_u32 s38, s36, s69\n\t" \
    "s_addc_u32 s39, s37, 0\n\t" \
    "s_mul_i32 s69, s65, 9216\n\t" \
    "s_add_u32 s69, s69, 123731968\n\t" \
    "s_add_u32 s40, s36, s69\n\t" \
    "s_addc_u32 s41, s37, 0\n\t" \
    "s_sub_u32 s70, s79, s74\n\t" \
    "s_max_i32 s70, s70, 0\n\t" \
    "s_min_i32 s70, s70, 16000\n\t" \
    "s_lshl_b32 s70, s70, 1\n\t" \
    "s_mul_hi_u32 s70, s70, s77\n\t" \
    "s_add_u32 s70, s70, s73\n\t" \
    "s_sub_u32 s70, s70, 100\n\t" \
    "s_getpc_b64 s[30:31]\n\t" \
    "vv_Wp93_%=:\n\t" \
    "s_add_u32 s30, s30, vv_We94_%=-vv_Wp93_%=\n\t" \
    "s_addc_u32 s31, s31, 0\n\t" \
    "vv_W92_%=:\n\t" \
    "s_memtime s[56:57]\n\t" \
    "s_waitcnt lgkmcnt(0)\n\t" \
    "s_sub_u32 s69, s70, s56\n\t" \
    "s_cmp_gt_i32 s69, 111\n\t" \
    "s_cbranch_scc1 vv_W92_%=\n\t" \
    "s_max_i32 s69, s69, 0\n\t" \
    "s_lshr_b32 s69, s69, 1\n\t" \
    "s_and_b32 s69, s69, 0x3c\n\t" \
    "s_sub_u32 s30, s30, s69\n\t" \
    "s_subb_u32 s31, s31, 0\n\t" \
    "s_setpc_b64 s[30:31]\n\t" \
    ".p2align 6\n\t" \
    "s_nop 0\n\t" \
    "s_nop 0\n\t" \
    "s_nop 0\n\t" \
    "s_nop 1\n\t" \
    "s_nop 1\n\t" \
    "s_nop 1\n\t" \
    "s_nop 1\n\t" \
    "s_nop 1\n\t" \
    "s_nop 1\n\t" \
    "s_nop 1\n\t" \
    "s_nop 1\n\t" \
    "s_nop 1\n\t" \
    "s_nop 1\n\t" \
    "s_nop 1\n\t" \
    "s_nop 1\n\t" \
    "s_nop 1\n\t" \
    "vv_We94_%=:\n\t" \
    "vv_G95_%=:\n\t" \
    "global_load_dwordx4 v[36:39], v1, s[46:47] sc1\n\t" \
    "s_nop 9\n\t" \
    "global_load_dwordx4 v[40:43], v1, s[46:47] offset:1024 sc1\n\t" \
    "s_nop 9\n\t" \
    "global_load_dwordx4 v[44:47], v2, s[46:47] sc1\n\t" \
    "s_waitcnt vmcnt(2)\n\t" \
    "v_smfmac_f32_16x16x64_bf16 v[72:75], v[36:39], a[48:55], v10\n\t" \
    "ds_write_b128 v0, v[36:39] offset:6144\n\t" \
    "v_smfmac_f32_16x16x64_bf16 v[76:79], v[36:39], a[120:127], v10\n\t" \
    "v_max3_u32 v140, v36, v37, v38\n\t" \
    "s_waitcnt vmcnt(1)\n\t" \
    "ds_write_b128 v0, v[40:43] offset:7168\n\t" \
    "v_smfmac_f32_16x16x64_bf16 v[72:75], v[40:43], a[56:63], v10\n\t" \
    "v_max3_u32 v140, v140, v39, v40\n\t" \
    "v_max3_u32 v140, v140, v41, v42\n\t" \
    "v_max_u32 v140, v140, v43\n\t" \
    "v_cmp_eq_u32 vcc, -1, v140\n\t" \
    "v_smfmac_f32_16x16x64_bf16 v[76:79], v[40:43], a[128:135], v10\n\t" \
    "s_cbranch_vccnz vv_Gbad96_%=\n\t" \
    "vv_Gc98_%=:\n\t" \
    "s_waitcnt lgkmcnt(0)\n\t" \
    "s_barrier\n\t" \
    "ds_read_b128 v[48:51], v0 offset:0\n\t" \
    "ds_read_b128 v[52:55], v0 offset:1024\n\t" \
    "s_waitcnt vmcnt(0)\n\t" \
    "v_smfmac_f32_16x16x64_bf16 v[72:75], v[44:47], a[64:71], v10\n\t" \
    "ds_read_b128 v[56:59], v0 offset:2048\n\t" \
    "ds_read_b128 v[60:63], v0 offset:3072\n\t" \
    "v_max3_u32 v140, v44, v45, v46\n\t" \
    "v_max_u32 v140, v140, v47\n\t" \
    "v_smfmac_f32_16x16x64_bf16 v[76:79], v[44:47], a[136:143], v10\n\t" \
    "ds_read_b128 v[64:67], v0 offset:4096\n\t" \
    "ds_read_b128 v[68:71], v0 offset:5120\n\t" \
    "v_cmp_eq_u32 vcc, -1, v140\n\t" \
    "s_cbranch_vccnz vv_Gbad297_%=\n\t" \
    "vv_Gc299_%=:\n\t" \
    "s_waitcnt lgkmcnt(5)\n\t" \
    "v_smfmac_f32_16x16x64_bf16 v[72:75], v[48:51], a[0:7], v10\n\t" \
    "s_waitcnt lgkmcnt(4)\n\t" \
    "v_smfmac_f32_16x16x64_bf16 v[72:75], v[52:55], a[8:15], v10\n\t" \
    "s_waitcnt lgkmcnt(3)\n\t" \
    "v_smfmac_f32_16x16x64_bf16 v[72:75], v[56:59], a[16:23], v10\n\t" \
    "s_waitcnt lgkmcnt(2)\n\t" \
    "v_smfmac_f32_16x16x64_bf16 v[72:75], v[60:63], a[24:31], v10\n\t" \
    "s_waitcnt lgkmcnt(1)\n\t" \
    "v_smfmac_f32_16x16x64_bf16 v[72:75], v[64:67], a[32:39], v10\n\t" \
    "s_waitcnt lgkmcnt(0)\n\t" \
    "v_smfmac_f32_16x16x64_bf16 v[72:75], v[68:71], a[40:47], v10\n\t" \
    "v_smfmac_f32_16x16x64_bf16 v[76:79], v[48:51], a[72:79], v10\n\t" \
    "s_nop 1\n\t" \
    "s_nop 0\n\t" \
    "v_smfmac_f32_16x16x64_bf16 v[76:79], v[52:55], a[80:87], v10\n\t" \
    "s_nop 1\n\t" \
    "s_nop 0\n\t" \
    "v_smfmac_f32_16x16x64_bf16 v[76:79], v[56:59], a[88:95], v10\n\t" \
    "v_add_f32 v72, v72, v74\n\t" \
    "v_smfmac_f32_16x16x64_bf16 v[76:79], v[60:63], a[96:103], v10\n\t" \
    "v_add_f32 v73, v73, v75\n\t" \
    "v_smfmac_f32_16x16x64_bf16 v[76:79], v[64:67], a[104:111], v10\n\t" \
    "v_cvt_pk_bf16_f32 v80, v72, v73\n\t" \
    "v_smfmac_f32_16x16x64_bf16 v[76:79], v[68:71], a[112:119], v10\n\t" \
    "ds_write_b32 v12, v80 offset:0\n\t" \
    "s_nop 6\n\t" \
    "v_add_f32 v76, v76, v78\n\t" \
    "v_add_f32 v77, v77, v79\n\t" \
    "v_cvt_pk_bf16_f32 v81, v76, v77\n\t" \
    "ds_write_b32 v12, v81 offset:64\n\t" \
    "s_waitcnt lgkmcnt(0)\n\t" \
    "s_barrier\n\t" \
    "ds_read_b64_tr_b16 v[84:85], v13 offset:0\n\t" \
    "ds_read_b64_tr_b16 v[86:87], v13 offset:128\n\t" \
    "ds_read_b64_tr_b16 v[88:89], v13 offset:1024\n\t" \
    "ds_read_b64_tr_b16 v[90:91], v13 offset:1152\n\t" \
    "s_waitcnt lgkmcnt(2)\n\t" \
    "v_smfmac_f32_16x16x64_bf16 v[92:95], v[84:87], a[144:151], v10\n\t" \
    "s_nop 2\n\t" \
    "s_waitcnt lgkmcnt(0)\n\t" \
    "v_smfmac_f32_16x16x64_bf16 v[92:95], v[88:91], a[152:159], v10\n\t" \
    "s_nop 2\n\t" \
    "v_smfmac_f32_16x16x64_bf16 v[96:99], v[84:87], a[160:167], v10\n\t" \
    "s_nop 2\n\t" \
    "v_smfmac_f32_16x16x64_bf16 v[96:99], v[88:91], a[168:175], v10\n\t" \
    "s_nop 2\n\t" \
    "v_smfmac_f32_16x16x64_bf16 v[100:103], v[84:87], a[176:183], v10\n\t" \
    "s_nop 0\n\t" \
    "v_add_f32 v92, v92, v94\n\t" \
    "v_add_f32 v93, v93, v95\n\t" \
    "v_smfmac_f32_16x16x64_bf16 v[100:103], v[88:91], a[184:191], v10\n\t" \
    "s_nop 1\n\t" \
    "v_cvt_pk_bf16_f32 v128, v92, v93\n\t" \
    "v_smfmac_f32_16x16x64_bf16 v[104:107], v[84:87], a[192:199], v10\n\t" \
    "s_nop 0\n\t" \
    "v_add_f32 v96, v96, v98\n\t" \
    "v_add_f32 v97, v97, v99\n\t" \
    "v_smfmac_f32_16x16x64_bf16 v[104:107], v[88:91], a[200:207], v10\n\t" \
    "s_nop 1\n\t" \
    "v_cvt_pk_bf16_f32 v129, v96, v97\n\t" \
    "v_smfmac_f32_16x16x64_bf16 v[124:127], v[84:87], v[240:247], v10\n\t" \
    "s_nop 0\n\t" \
    "v_add_f32 v100, v100, v102\n\t" \
    "v_add_f32 v101, v101, v103\n\t" \
    "v_smfmac_f32_16x16x64_bf16 v[124:127], v[88:91], v[248:255], v10\n\t" \
    "s_nop 1\n\t" \
    "global_load_dword v140, v20, s[46:47] sc1\n\t" \
    "v_cvt_pk_bf16_f32 v130, v100, v101\n\t" \
    "v_smfmac_f32_16x16x64_bf16 v[108:111], v[84:87], a[208:215], v10\n\t" \
    "s_nop 0\n\t" \
    "v_add_f32 v104, v104, v106\n\t" \
    "v_add_f32 v105, v105, v107\n\t" \
    "v_smfmac_f32_16x16x64_bf16 v[108:111], v[88:91], a[216:223], v10\n\t" \
    "s_nop 0\n\t" \
    "v_cvt_pk_bf16_f32 v131, v104, v105\n\t" \
    "v_permlane32_swap_b32 v128, v130\n\t" \
    "v_smfmac_f32_16x16x64_bf16 v[112:115], v[84:87], a[224:231], v10\n\t" \
    "s_nop 0\n\t" \
    "v_permlane32_swap_b32 v129, v131\n\t" \
    "s_nop 1\n\t" \
    "v_permlane16_swap_b32 v128, v129\n\t" \
    "v_smfmac_f32_16x16x64_bf16 v[112:115], v[88:91], a[232:239], v10\n\t" \
    "v_permlane16_swap_b32 v130, v131\n\t" \
    "global_store_dwordx4 v14, v[128:131], s[38:39] sc1\n\t" \
    "v_add_f32 v124, v124, v126\n\t" \
    "v_smfmac_f32_16x16x64_bf16 v[116:119], v[84:87], a[240:247], v10\n\t" \
    "s_nop 0\n\t" \
    "v_add_f32 v125, v125, v127\n\t" \
    "v_cvt_pk_bf16_f32 v136, v124, v125\n\t" \
    "v_smfmac_f32_16x16x64_bf16 v[116:119], v[88:91], a[248:255], v10\n\t" \
    "global_store_dword v16, v136, s[38:39] sc1\n\t" \
    "v_add_f32 v108, v108, v110\n\t" \
    "v_add_f32 v109, v109, v111\n\t" \
    "v_smfmac_f32_16x16x64_bf16 v[120:123], v[84:87], v[224:231], v10\n\t" \
    "s_nop 0\n\t" \
    "v_cvt_pk_bf16_f32 v132, v108, v109\n\t" \
    "v_add_f32 v112, v112, v114\n\t" \
    "v_smfmac_f32_16x16x64_bf16 v[120:123], v[88:91], v[232:239], v10\n\t" \
    "v_add_f32 v113, v113, v115\n\t" \
    "v_cvt_pk_bf16_f32 v133, v112, v113\n\t" \
    "v_add_f32 v116, v116, v118\n\t" \
    "v_add_f32 v117, v117, v119\n\t" \
    "v_cvt_pk_bf16_f32 v134, v116, v117\n\t" \
    "s_nop 2\n\t" \
    "v_pk_add_f32 v[120:121], v[120:121], v[122:123]\n\t" \
    "v_cvt_pk_bf16_f32 v135, v120, v121\n\t" \
    "v_permlane32_swap_b32 v132, v134\n\t" \
    "s_nop 0\n\t" \
    "v_permlane32_swap_b32 v133, v135\n\t" \
    "s_nop 1\n\t" \
    "v_permlane16_swap_b32 v132, v133\n\t" \
    "v_permlane16_swap_b32 v134, v135\n\t" \
    "global_store_dwordx4 v15, v[132:135], s[38:39] sc1\n\t" \
    "s_nop 1\n\t" \
    "global_store_dwordx4 v17, v[128:131], s[40:41] sc1\n\t" \
    "global_store_dwordx4 v17, v[132:135], s[40:41] offset:1024 sc1\n\t" \
    "global_store_dword v18, v136, s[40:41] sc1\n\t" \
    "s_cmp_eq_u32 s65, 63\n\t" \
    "s_cbranch_scc1 vv_end90_%=\n\t" \
    "s_mov_b32 s71, 3\n\t" \
    "vv_E101_%=:\n\t" \
    "s_memtime s[54:55]\n\t" \
    "s_nop 0\n\t" \
    "s_memrealtime s[4:5]\n\t" \
    "s_nop 0\n\t" \
    "s_memrealtime s[6:7]\n\t" \
    "s_nop 0\n\t" \
    "s_memrealtime s[8:9]\n\t" \
    "s_nop 0\n\t" \
    "s_memrealtime s[10:11]\n\t" \
    "s_nop 0\n\t" \
    "s_memrealtime s[12:13]\n\t" \
    "s_nop 0\n\t" \
    "s_memrealtime s[14:15]\n\t" \
    "s_nop 0\n\t" \
    "s_memrealtime s[16:17]\n\t" \
    "s_nop 0\n\t" \
    "s_memrealtime s[18:19]\n\t" \
    "s_nop 0\n\t" \
    "s_memrealtime s[20:21]\n\t" \
    "s_nop 0\n\t" \
    "s_memrealtime s[22:23]\n\t" \
    "s_nop 0\n\t" \
    "s_memrealtime s[24:25]\n\t" \
    "s_nop 0\n\t" \
    "s_memrealtime s[26:27]\n\t" \
    "s_nop 0\n\t" \
    "s_memrealtime s[28:29]\n\t" \
    "s_nop 0\n\t" \
    "s_memtime s[56:57]\n\t" \
    "s_mov_b32 s70, 1\n\t" \
    "s_sub_u32 s71, s71, 1\n\t" \
    "s_cselect_b32 s69, 0, -1\n\t" \
    "s_waitcnt lgkmcnt(0)\n\t" \
    "s_sub_u32 s54, s56, s54\n\t" \
    "s_and_b32 s54, s54, s69\n\t" \
    "s_cmp_gt_u32 s54, 124\n\t" \
    "s_cbranch_scc1 vv_E101_%=\n\t" \
    "s_cmp_eq_u32 s4, s28\n\t" \
    "s_addc_u32 s70, s70, 0\n\t" \
    "s_cmp_eq_u32 s6, s28\n\t" \
    "s_addc_u32 s70, s70, 0\n\t" \
    "s_cmp_eq_u32 s8, s28\n\t" \
    "s_addc_u32 s70, s70, 0\n\t" \
    "s_cmp_eq_u32 s10, s28\n\t" \
    "s_addc_u32 s70, s70, 0\n\t" \
    "s_cmp_eq_u32 s12, s28\n\t" \
    "s_addc_u32 s70, s70, 0\n\t" \
    "s_cmp_eq_u32 s14, s28\n\t" \
    "s_addc_u32 s70, s70, 0\n\t" \
    "s_cmp_eq_u32 s16, s28\n\t" \
    "s_addc_u32 s70, s70, 0\n\t" \
    "s_cmp_eq_u32 s18, s28\n\t" \
    "s_addc_u32 s70, s70, 0\n\t" \
    "s_cmp_eq_u32 s20, s28\n\t" \
    "s_addc_u32 s70, s70, 0\n\t" \
    "s_cmp_eq_u32 s22, s28\n\t" \
    "s_addc_u32 s70, s70, 0\n\t" \
    "s_cmp_eq_u32 s24, s28\n\t" \
    "s_addc_u32 s70, s70, 0\n\t" \
    "s_cmp_eq_u32 s26, s28\n\t" \
    "s_addc_u32 s70, s70, 0\n\t" \
    "s_mov_b32 s75, s73\n\t" \
    "s_mov_b32 s76, s74\n\t" \
    "s_lshl_b32 s70, s70, 3\n\t" \
    "s_sub_u32 s73, s56, s70\n\t" \
    "s_lshl_b32 s74, s28, 4\n\t" \
    "s_sub_u32 s69, s73, s75\n\t" \
    "s_sub_u32 s70, s74, s76\n\t" \
    "s_sub_u32 s71, s70, 1600\n\t" \
    "s_cmp_lt_u32 s71, 400000\n\t" \
    "s_cbranch_scc0 vv_K103_%=\n\t" \
    "v_cvt_f32_u32 v141, s69\n\t" \
    "v_cvt_f32_u32 v142, s70\n\t" \
    "v_rcp_f32 v143, v141\n\t" \
    "v_rcp_f32 v144, v142\n\t" \
    "v_mul_f32 v143, v143, v142\n\t" \
    "v_mul_f32 v144, v144, v141\n\t" \
    "v_mul_f32 v143, 0x4f800000, v143\n\t" \
    "v_mul_f32 v144, 0x4f000000, v144\n\t" \
    "v_cvt_u32_f32 v143, v143\n\t" \
    "v_cvt_u32_f32 v144, v144\n\t" \
    "v_readfirstlane_b32 s69, v143\n\t" \
    "v_readfirstlane_b32 s70, v144\n\t" \
    "s_nop 1\n\t" \
    "s_sub_u32 s69, s69, s78\n\t" \
    "s_ashr_i32 s69, s69, 2\n\t" \
    "s_add_u32 s78, s78, s69\n\t" \
    "s_sub_u32 s70, s70, s77\n\t" \
    "s_ashr_i32 s70, s70, 2\n\t" \
    "s_add_u32 s77, s77, s70\n\t" \
    "vv_K103_%=:\n\t" \
    "s_waitcnt vmcnt(6)\n\t" \
    "v_cmp_eq_u32 vcc, -1, v140\n\t" \
    "s_cbranch_vccz vv_Hok105_%=\n\t" \
    "vv_H104_%=:\n\t" \
    "global_load_dword v140, v20, s[46:47] sc1\n\t" \
    "s_waitcnt vmcnt(0)\n\t" \
    "v_cmp_eq_u32 vcc, -1, v140\n\t" \
    "s_cbranch_vccz vv_Hc106_%=\n\t" \
    "s_cmp_eq_u32 s68, 0\n\t" \
    "s_cbranch_scc1 vv_F107_%=\n\t" \
    "s_sub_u32 s68, s68, 1\n\t" \
    "s_sleep 5\n\t" \
    "s_branch vv_H104_%=\n\t" \
    "vv_F107_%=:\n\t" \
    "v_mov_b32 v141, 6095104\n\t" \
    "v_mov_b32 v142, 6\n\t" \
    "global_store_dword v141, v142, s[36:37] sc1\n\t" \
    "s_waitcnt vmcnt(0)\n\t" \
    "s_branch vv_Hc106_%=\n\t" \
    "vv_Hc106_%=:\n\t" \
    "vv_Hok105_%=:\n\t" \
    "v_subrev_u32 v140, s80, v140\n\t" \
    "v_and_b32 v20, -5, v20\n\t" \
    "s_nop 0\n\t" \
    "v_max_i32_dpp v140, v140, v140 row_ror:1 row_mask:0xf bank_mask:0xf\n\t" \
    "s_nop 1\n\t" \
    "v_max_i32_dpp v140, v140, v140 row_ror:2 row_mask:0xf bank_mask:0xf\n\t" \
    "s_nop 1\n\t" \
    "v_max_i32_dpp v140, v140, v140 row_ror:4 row_mask:0xf bank_mask:0xf\n\t" \
    "s_nop 1\n\t" \
    "v_max_i32_dpp v140, v140, v140 row_ror:8 row_mask:0xf bank_mask:0xf\n\t" \
    "s_nop 0\n\t" \
    "v_readlane_b32 s69, v140, 0\n\t" \
    "v_readlane_b32 s71, v140, 16\n\t" \
    "v_readlane_b32 s70, v140, 32\n\t" \
    "v_readlane_b32 s81, v140, 48\n\t" \
    "s_max_i32 s69, s69, s71\n\t" \
    "s_nop 0\n\t" \
    "s_max_i32 s71, s70, s81\n\t" \
    "s_max_i32 s69, s69, s71\n\t" \
    "s_add_u32 s81, s69, s80\n\t" \
    "v_mov_b32 v72, 0\n\t" \
    "v_mov_b32 v73, 0\n\t" \
    "v_mov_b32 v74, 0\n\t" \
    "v_mov_b32 v75, 0\n\t" \
    "v_mov_b32 v76, 0\n\t" \
    "v_mov_b32 v77, 0\n\t" \
    "v_mov_b32 v78, 0\n\t" \
    "v_mov_b32 v79, 0\n\t" \
    "v_mov_b32 v92, 0\n\t" \
    "v_mov_b32 v93, 0\n\t" \
    "v_mov_b32 v94, 0\n\t" \
    "v_mov_b32 v95, 0\n\t" \
    "v_mov_b32 v96, 0\n\t" \
    "v_mov_b32 v97, 0\n\t" \
    "v_mov_b32 v98, 0\n\t" \
    "v_mov_b32 v99, 0\n\t" \
    "v_mov_b32 v100, 0\n\t" \
    "v_mov_b32 v101, 0\n\t" \
    "v_mov_b32 v102, 0\n\t" \
    "v_mov_b32 v103, 0\n\t" \
    "v_mov_b32 v104, 0\n\t" \
    "v_mov_b32 v105, 0\n\t" \
    "v_mov_b32 v106, 0\n\t" \
    "v_mov_b32 v107, 0\n\t" \
    "v_mov_b32 v108, 0\n\t" \
    "v_mov_b32 v109, 0\n\t" \
    "v_mov_b32 v110, 0\n\t" \
    "v_mov_b32 v111, 0\n\t" \
    "v_mov_b32 v112, 0\n\t" \
    "v_mov_b32 v113, 0\n\t" \
    "v_mov_b32 v114, 0\n\t" \
    "v_mov_b32 v115, 0\n\t" \
    "v_mov_b32 v116, 0\n\t" \
    "v_mov_b32 v117, 0\n\t" \
    "v_mov_b32 v118, 0\n\t" \
    "v_mov_b32 v119, 0\n\t" \
    "v_mov_b32 v120, 0\n\t" \
    "v_mov_b32 v121, 0\n\t" \
    "v_mov_b32 v122, 0\n\t" \
    "v_mov_b32 v123, 0\n\t" \
    "v_mov_b32 v124, 0\n\t" \
    "v_mov_b32 v125, 0\n\t" \
    "v_mov_b32 v126, 0\n\t" \
    "v_mov_b32 v127, 0\n\t" \
    "s_cmp_lg_u32 s65, 0\n\t" \
    "s_cbranch_scc1 vv_Oc110_%=\n\t" \
    "s_add_u32 s79, s81, 240\n\t" \
    "s_sub_u32 s70, s79, s74\n\t" \
    "s_max_i32 s70, s70, 0\n\t" \
    "s_min_i32 s70, s70, 16000\n\t" \
    "s_lshl_b32 s70, s70, 1\n\t" \
    "s_mul_hi_u32 s70, s70, s77\n\t" \
    "s_add_u32 s70, s70, s73\n\t" \
    "vv_W111_%=:\n\t" \
    "s_memtime s[56:57]\n\t" \
    "s_waitcnt lgkmcnt(0)\n\t" \
    "s_sub_u32 s69, s70, s56\n\t" \
    "s_cmp_gt_i32 s69, 24\n\t" \
    "s_cbranch_scc1 vv_W111_%=\n\t" \
    "global_load_dword v140, v35, s[46:47] sc1\n\t" \
    "s_waitcnt vmcnt(0)\n\t" \
    "v_cmp_eq_u32 vcc, -1, v140\n\t" \
    "s_cbranch_vccz vv_Ook109_%=\n\t" \
    "vv_O108_%=:\n\t" \
    "global_load_dword v140, v35, s[46:47] offset:1536 sc1\n\t" \
    "s_waitcnt vmcnt(0)\n\t" \
    "v_cmp_eq_u32 vcc, -1, v140\n\t" \
    "s_cbranch_vccz vv_Ook109_%=\n\t" \
    "s_cmp_eq_u32 s68, 0\n\t" \
    "s_cbranch_scc1 vv_F112_%=\n\t" \
    "s_sub_u32 s68, s68, 1\n\t" \
    "s_sleep 3\n\t" \
    "s_branch vv_O108_%=\n\t" \
    "vv_F112_%=:\n\t" \
    "v_mov_b32 v141, 6095104\n\t" \
    "v_mov_b32 v142, 17\n\t" \
    "global_store_dword v141, v142, s[36:37] sc1\n\t" \
    "s_waitcnt vmcnt(0)\n\t" \
    "s_branch vv_Ook109_%=\n\t" \
    "vv_Ook109_%=:\n\t" \
    "v_subrev_u32 v140, s80, v140\n\t" \
    "s_nop 1\n\t" \
    "v_max_i32_dpp v140, v140, v140 row_ror:1 row_mask:0xf bank_mask:0xf\n\t" \
    "s_nop 1\n\t" \
    "v_max_i32_dpp v140, v140, v140 row_ror:2 row_mask:0xf bank_mask:0xf\n\t" \
    "s_nop 1\n\t" \
    "v_max_i32_dpp v140, v140, v140 row_ror:4 row_mask:0xf bank_mask:0xf\n\t" \
    "s_nop 1\n\t" \
    "v_max_i32_dpp v140, v140, v140 row_ror:8 row_mask:0xf bank_mask:0xf\n\t" \
    "s_nop 0\n\t" \
    "v_readlane_b32 s69, v140, 0\n\t" \
    "v_readlane_b32 s71, v140, 16\n\t" \
    "v_readlane_b32 s70, v140, 32\n\t" \
    "v_readlane_b32 s81, v140, 48\n\t" \
    "s_max_i32 s69, s69, s71\n\t" \
    "s_nop 0\n\t" \
    "s_max_i32 s71, s70, s81\n\t" \
    "s_max_i32 s69, s69, s71\n\t" \
    "s_add_u32 s81, s69, s80\n\t" \
    "vv_Oc110_%=:\n\t" \
    "s_mul_hi_u32 s69, s85, s78\n\t" \
    "s_add_u32 s79, s81, s69\n\t" \
    "s_add_u32 s79, s79, s87\n\t" \
    "s_cmp_ge_u32 s65, 36\n\t" \
    "s_cselect_b32 s69, 28, 0\n\t" \
    "s_sub_u32 s79, s79, s69\n\t" \
    "s_cmp_lt_u32 s65, 8\n\t" \
    "s_cselect_b32 s69, 96, 0\n\t" \
    "s_add_u32 s79, s79, s69\n\t" \
    "s_add_u32 s80, s79, s88\n\t" \
    "s_add_u32 s65, s65, 1\n\t" \
    "s_branch vv_go91_%=\n\t" \
    "vv_Gbad96_%=:\n\t" \
    "s_waitcnt vmcnt(0) lgkmcnt(0)\n\t" \
    "s_nop 7\n\t" \
    "v_mov_b32 v72, 0\n\t" \
    "v_mov_b32 v73, 0\n\t" \
    "v_mov_b32 v74, 0\n\t" \
    "v_mov_b32 v75, 0\n\t" \
    "v_mov_b32 v76, 0\n\t" \
    "v_mov_b32 v77, 0\n\t" \
    "v_mov_b32 v78, 0\n\t" \
    "v_mov_b32 v79, 0\n\t" \
    "s_cmp_eq_u32 s68, 0\n\t" \
    "s_cbranch_scc1 vv_F113_%=\n\t" \
    "s_sub_u32 s68, s68, 1\n\t" \
    "s_branch vv_G95_%=\n\t" \
    "vv_F113_%=:\n\t" \
    "v_mov_b32 v140, 6095104\n\t" \
    "v_mov_b32 v141, 1\n\t" \
    "global_store_dword v140, v141, s[36:37] sc1\n\t" \
    "s_waitcnt vmcnt(0)\n\t" \
    "s_branch vv_Gc98_%=\n\t" \
    "vv_Gbad297_%=:\n\t" \
    "global_load_dwordx4 v[44:47], v2, s[46:47] sc1\n\t" \
    "s_waitcnt vmcnt(0)\n\t" \
    "v_max3_u32 v140, v44, v45, v46\n\t" \
    "v_max_u32 v140, v140, v47\n\t" \
    "v_cmp_eq_u32 vcc, -1, v140\n\t" \
    "s_cbranch_vccz vv_Gre2100_%=\n\t" \
    "s_cmp_eq_u32 s68, 0\n\t" \
    "s_cbranch_scc1 vv_F114_%=\n\t" \
    "s_sub_u32 s68, s68, 1\n\t" \
    "s_sleep 1\n\t" \
    "s_branch vv_Gbad297_%=\n\t" \
    "vv_F114_%=:\n\t" \
    "v_mov_b32 v140, 6095104\n\t" \
    "v_mov_b32 v141, 2\n\t" \
    "global_store_dword v140, v141, s[36:37] sc1\n\t" \
    "s_waitcnt vmcnt(0)\n\t" \
    "s_branch vv_Gre2100_%=\n\t" \
    "vv_Gre2100_%=:\n\t" \
    "v_mov_b32 v72, 0\n\t" \
    "v_mov_b32 v73, 0\n\t" \
    "v_mov_b32 v74, 0\n\t" \
    "v_mov_b32 v75, 0\n\t" \
    "v_mov_b32 v76, 0\n\t" \
    "v_mov_b32 v77, 0\n\t" \
    "v_mov_b32 v78, 0\n\t" \
    "v_mov_b32 v79, 0\n\t" \
    "v_smfmac_f32_16x16x64_bf16 v[72:75], v[36:39], a[48:55], v10\n\t" \
    "s_nop 0\n\t" \
    "v_smfmac_f32_16x16x64_bf16 v[76:79], v[36:39], a[120:127], v10\n\t" \
    "v_smfmac_f32_16x16x64_bf16 v[72:75], v[40:43], a[56:63], v10\n\t" \
    "v_smfmac_f32_16x16x64_bf16 v[76:79], v[40:43], a[128:135], v10\n\t" \
    "v_smfmac_f32_16x16x64_bf16 v[72:75], v[44:47], a[64:71], v10\n\t" \
    "v_smfmac_f32_16x16x64_bf16 v[76:79], v[44:47], a[136:143], v10\n\t" \
    "s_branch vv_Gc299_%=\n\t" \
    "vv_end90_%=:\n\t" \
    "s_endpgm\n\t" \
    "gx_begin_%=:\n\t" \
    "v_mbcnt_lo_u32_b32 v223, -1, 0\n\t" \
    "v_mbcnt_hi_u32_b32 v223, -1, v223\n\t" \
    "s_mov_b32 s68, 262144\n\t" \
    "s_mov_b32 s72, 0\n\t" \
    "s_mov_b32 s83, -1\n\t" \
    "s_mov_b32 s65, 0\n\t" \
    "v_and_b32 v180, 3, v223\n\t" \
    "v_lshlrev_b32 v202, 3, v180\n\t" \
    "v_lshrrev_b32 v181, 5, v223\n\t" \
    "v_lshl_add_u32 v202, v181, 12, v202\n\t" \
    "v_bfe_u32 v182, v223, 2, 2\n\t" \
    "s_mov_b32 s70, 288\n\t" \
    "v_mad_u32_u24 v202, v182, s70, v202\n\t" \
    "v_bfe_u32 v183, v223, 4, 1\n\t" \
    "s_mov_b32 s70, 2176\n\t" \
    "v_mad_u32_u24 v202, v183, s70, v202\n\t" \
    "s_lshl_b32 s69, s66, 6\n\t" \
    "v_xor_b32 v202, s69, v202\n\t" \
    "v_xor_b32 v203, 32, v202\n\t" \
    "v_lshlrev_b32 v3, 4, v180\n\t" \
    "v_lshlrev_b32 v2, 5, v181\n\t" \
    "v_xor_b32 v3, v3, v2\n\t" \
    "s_bfe_u32 s71, s66, 0x10000\n\t" \
    "s_mul_i32 s69, s71, 576\n\t" \
    "s_bfe_u32 s71, s66, 0x10001\n\t" \
    "s_mul_i32 s71, s71, 18432\n\t" \
    "s_add_u32 s69, s69, s71\n\t" \
    "v_lshrrev_b32 v184, 2, v223\n\t" \
    "v_mov_b32 v1, s69\n\t" \
    "s_mov_b32 s70, 1152\n\t" \
    "v_mad_u32_u24 v1, v184, s70, v1\n\t" \
    "v_add_u32 v1, v1, v3\n\t" \
    "v_and_b32 v185, 15, v223\n\t" \
    "v_lshlrev_b32 v3, 6, v185\n\t" \
    "v_lshrrev_b32 v186, 4, v223\n\t" \
    "v_lshl_add_u32 v3, v186, 4, v3\n\t" \
    "v_bfe_u32 v187, v223, 3, 1\n\t" \
    "v_lshlrev_b32 v2, 5, v187\n\t" \
    "v_xor_b32 v3, v3, v2\n\t" \
    "v_mov_b32 v2, v3\n\t" \
    "s_mul_i32 s89, s66, 9216\n\t" \
    "s_mov_b32 s90, 0\n\t" \
    "s_waitcnt vmcnt(12) lgkmcnt(0)\n\t" \
    "s_barrier\n\t" \
    "s_add_u32 m0, s82, 65536\n\t" \
    "s_nop 0\n\t" \
    "global_load_lds_dwordx4 v200, s[42:43] nt\n\t" \
    "v_add_u32 v200, 131072, v200\n\t" \
    "s_add_u32 m0, s82, 69632\n\t" \
    "s_nop 0\n\t" \
    "global_load_lds_dwordx4 v200, s[42:43] nt\n\t" \
    "v_add_u32 v200, 131072, v200\n\t" \
    "s_add_u32 m0, s82, 73728\n\t" \
    "s_nop 0\n\t" \
    "global_load_lds_dwordx4 v200, s[42:43] nt\n\t" \
    "v_add_u32 v200, 131072, v200\n\t" \
    "s_add_u32 m0, s82, 77824\n\t" \
    "s_nop 0\n\t" \
    "global_load_lds_dwordx4 v200, s[42:43] nt\n\t" \
    "v_add_u32 v200, 131072, v200\n\t" \
    "v_add_u32 v202, 73728, v202\n\t" \
    "v_add_u32 v203, 73728, v203\n\t" \
    "ds_read_b64_tr_b16 a[144:145], v202 offset:0\n\t" \
    "ds_read_b64_tr_b16 a[146:147], v202 offset:1024\n\t" \
    "ds_read_b64_tr_b16 a[148:149], v202 offset:8192\n\t" \
    "ds_read_b64_tr_b16 a[150:151], v202 offset:9216\n\t" \
    "ds_read_b64_tr_b16 a[216:217], v203 offset:0\n\t" \
    "ds_read_b64_tr_b16 a[218:219], v203 offset:1024\n\t" \
    "ds_read_b64_tr_b16 a[220:221], v203 offset:8192\n\t" \
    "ds_read_b64_tr_b16 a[222:223], v203 offset:9216\n\t" \
    "v_lshlrev_b32 v0, 4, v223\n\t" \
    "s_mul_i32 s69, s66, 2048\n\t" \
    "s_add_u32 s69, s69, 147456\n\t" \
    "v_lshl_add_u32 v4, v223, 4, s69\n\t" \
    "s_mov_b32 s69, 147456\n\t" \
    "v_lshl_add_u32 v5, v223, 4, s69\n\t" \
    "v_lshlrev_b32 v6, 2, v223\n\t" \
    "v_mov_b32 v7, 0\n\t" \
    "s_mul_i32 s69, s66, 2048\n\t" \
    "v_lshl_add_u32 v8, v223, 4, s69\n\t" \
    "s_waitcnt vmcnt(12) lgkmcnt(0)\n\t" \
    "s_barrier\n\t" \
    "s_add_u32 m0, s82, 0\n\t" \
    "s_nop 0\n\t" \
    "global_load_lds_dwordx4 v200, s[42:43] nt\n\t" \
    "v_add_u32 v200, 131072, v200\n\t" \
    "s_add_u32 m0, s82, 4096\n\t" \
    "s_nop 0\n\t" \
    "global_load_lds_dwordx4 v200, s[42:43] nt\n\t" \
    "v_add_u32 v200, 131072, v200\n\t" \
    "s_add_u32 m0, s82, 8192\n\t" \
    "s_nop 0\n\t" \
    "global_load_lds_dwordx4 v200, s[42:43] nt\n\t" \
    "v_add_u32 v200, 131072, v200\n\t" \
    "s_add_u32 m0, s82, 12288\n\t" \
    "s_nop 0\n\t" \
    "global_load_lds_dwordx4 v200, s[42:43] nt\n\t" \
    "v_add_u32 v200, 131072, v200\n\t" \
    "ds_read_b64_tr_b16 a[152:153], v202 offset:16384\n\t" \
    "ds_read_b64_tr_b16 a[154:155], v202 offset:17408\n\t" \
    "ds_read_b64_tr_b16 a[156:157], v202 offset:24576\n\t" \
    "ds_read_b64_tr_b16 a[158:159], v202 offset:25600\n\t" \
    "ds_read_b64_tr_b16 a[224:225], v203 offset:16384\n\t" \
    "ds_read_b64_tr_b16 a[226:227], v203 offset:17408\n\t" \
    "ds_read_b64_tr_b16 a[228:229], v203 offset:24576\n\t" \
    "ds_read_b64_tr_b16 a[230:231], v203 offset:25600\n\t" \
    "s_waitcnt vmcnt(12) lgkmcnt(0)\n\t" \
    "s_barrier\n\t" \
    "s_add_u32 m0, s82, 16384\n\t" \
    "s_nop 0\n\t" \
    "global_load_lds_dwordx4 v200, s[42:43] nt\n\t" \
    "v_add_u32 v200, 131072, v200\n\t" \
    "s_add_u32 m0, s82, 20480\n\t" \
    "s_nop 0\n\t" \
    "global_load_lds_dwordx4 v200, s[42:43] nt\n\t" \
    "v_add_u32 v200, 131072, v200\n\t" \
    "s_add_u32 m0, s82, 24576\n\t" \
    "s_nop 0\n\t" \
    "global_load_lds_dwordx4 v200, s[42:43] nt\n\t" \
    "v_add_u32 v200, 131072, v200\n\t" \
    "s_add_u32 m0, s82, 28672\n\t" \
    "s_nop 0\n\t" \
    "global_load_lds_dwordx4 v200, s[42:43] nt\n\t" \
    "v_add_u32 v200, 131072, v200\n\t" \
    "s_add_u32 m0, s89, 0\n\t" \
    "s_nop 0\n\t" \
    "global_load_lds_dwordx4 v1, s[50:51] offset:0\n\t" \
    "s_add_u32 m0, s89, 960\n\t" \
    "s_nop 0\n\t" \
    "global_load_lds_dwordx4 v1, s[50:51] offset:64\n\t" \
    "s_add_u32 m0, s89, 1920\n\t" \
    "s_nop 0\n\t" \
    "global_load_lds_dwordx4 v1, s[50:51] offset:128\n\t" \
    "s_add_u32 m0, s89, 2880\n\t" \
    "s_nop 0\n\t" \
    "global_load_lds_dwordx4 v1, s[50:51] offset:192\n\t" \
    "s_add_u32 m0, s89, 3840\n\t" \
    "s_nop 0\n\t" \
    "global_load_lds_dwordx4 v1, s[50:51] offset:256\n\t" \
    "s_add_u32 m0, s89, 4800\n\t" \
    "s_nop 0\n\t" \
    "global_load_lds_dwordx4 v1, s[50:51] offset:320\n\t" \
    "s_add_u32 m0, s89, 5760\n\t" \
    "s_nop 0\n\t" \
    "global_load_lds_dwordx4 v1, s[50:51] offset:384\n\t" \
    "s_add_u32 m0, s89, 6720\n\t" \
    "s_nop 0\n\t" \
    "global_load_lds_dwordx4 v1, s[50:51] offset:448\n\t" \
    "s_add_u32 m0, s89, 7680\n\t" \
    "s_nop 0\n\t" \
    "global_load_lds_dwordx4 v1, s[50:51] offset:512\n\t" \
    "ds_read_b64_tr_b16 a[160:161], v202 offset:32768\n\t" \
    "ds_read_b64_tr_b16 a[162:163], v202 offset:33792\n\t" \
    "ds_read_b64_tr_b16 a[164:165], v202 offset:40960\n\t" \
    "ds_read_b64_tr_b16 a[166:167], v202 offset:41984\n\t" \
    "ds_read_b64_tr_b16 a[232:233], v203 offset:32768\n\t" \
    "ds_read_b64_tr_b16 a[234:235], v203 offset:33792\n\t" \
    "ds_read_b64_tr_b16 a[236:237], v203 offset:40960\n\t" \
    "ds_read_b64_tr_b16 a[238:239], v203 offset:41984\n\t" \
    "s_waitcnt vmcnt(21) lgkmcnt(0)\n\t" \
    "s_barrier\n\t" \
    "s_add_u32 m0, s82, 32768\n\t" \
    "s_nop 0\n\t" \
    "global_load_lds_dwordx4 v200, s[42:43] nt\n\t" \
    "v_add_u32 v200, 131072, v200\n\t" \
    "s_add_u32 m0, s82, 36864\n\t" \
    "s_nop 0\n\t" \
    "global_load_lds_dwordx4 v200, s[42:43] nt\n\t" \
    "v_add_u32 v200, 131072, v200\n\t" \
    "s_add_u32 m0, s82, 40960\n\t" \
    "s_nop 0\n\t" \
    "global_load_lds_dwordx4 v200, s[42:43] nt\n\t" \
    "v_add_u32 v200, 131072, v200\n\t" \
    "s_add_u32 m0, s82, 45056\n\t" \
    "s_nop 0\n\t" \
    "global_load_lds_dwordx4 v200, s[42:43] nt\n\t" \
    "v_add_u32 v200, 131072, v200\n\t" \
    "s_add_u32 s50, s50, 36864\n\t" \
    "s_addc_u32 s51, s51, 0\n\t" \
    "s_add_u32 m0, s89, 36864\n\t" \
    "s_nop 0\n\t" \
    "global_load_lds_dwordx4 v1, s[50:51] offset:0\n\t" \
    "s_add_u32 m0, s89, 37824\n\t" \
    "s_nop 0\n\t" \
    "global_load_lds_dwordx4 v1, s[50:51] offset:64\n\t" \
    "s_add_u32 m0, s89, 38784\n\t" \
    "s_nop 0\n\t" \
    "global_load_lds_dwordx4 v1, s[50:51] offset:128\n\t" \
    "s_add_u32 m0, s89, 39744\n\t" \
    "s_nop 0\n\t" \
    "global_load_lds_dwordx4 v1, s[50:51] offset:192\n\t" \
    "s_add_u32 m0, s89, 40704\n\t" \
    "s_nop 0\n\t" \
    "global_load_lds_dwordx4 v1, s[50:51] offset:256\n\t" \
    "s_add_u32 m0, s89, 41664\n\t" \
    "s_nop 0\n\t" \
    "global_load_lds_dwordx4 v1, s[50:51] offset:320\n\t" \
    "s_add_u32 m0, s89, 42624\n\t" \
    "s_nop 0\n\t" \
    "global_load_lds_dwordx4 v1, s[50:51] offset:384\n\t" \
    "s_add_u32 m0, s89, 43584\n\t" \
    "s_nop 0\n\t" \
    "global_load_lds_dwordx4 v1, s[50:51] offset:448\n\t" \
    "s_add_u32 m0, s89, 44544\n\t" \
    "s_nop 0\n\t" \
    "global_load_lds_dwordx4 v1, s[50:51] offset:512\n\t" \
    "ds_read_b64_tr_b16 a[168:169], v202 offset:49152\n\t" \
    "ds_read_b64_tr_b16 a[170:171], v202 offset:50176\n\t" \
    "ds_read_b64_tr_b16 a[172:173], v202 offset:57344\n\t" \
    "ds_read_b64_tr_b16 a[174:175], v202 offset:58368\n\t" \
    "ds_read_b64_tr_b16 a[240:241], v203 offset:49152\n\t" \
    "ds_read_b64_tr_b16 a[242:243], v203 offset:50176\n\t" \
    "ds_read_b64_tr_b16 a[244:245], v203 offset:57344\n\t" \
    "ds_read_b64_tr_b16 a[246:247], v203 offset:58368\n\t" \
    "s_waitcnt vmcnt(30) lgkmcnt(0)\n\t" \
    "s_barrier\n\t" \
    "s_add_u32 m0, s82, 49152\n\t" \
    "s_nop 0\n\t" \
    "global_load_lds_dwordx4 v200, s[42:43] nt\n\t" \
    "v_add_u32 v200, 131072, v200\n\t" \
    "s_add_u32 m0, s82, 53248\n\t" \
    "s_nop 0\n\t" \
    "global_load_lds_dwordx4 v200, s[42:43] nt\n\t" \
    "v_add_u32 v200, 131072, v200\n\t" \
    "s_add_u32 m0, s82, 57344\n\t" \
    "s_nop 0\n\t" \
    "global_load_lds_dwordx4 v200, s[42:43] nt\n\t" \
    "v_add_u32 v200, 131072, v200\n\t" \
    "s_add_u32 m0, s82, 61440\n\t" \
    "s_nop 0\n\t" \
    "global_load_lds_dwordx4 v200, s[42:43] nt\n\t" \
    "v_add_u32 v200, 131072, v200\n\t" \
    "v_add_u32 v202, 65536, v202\n\t" \
    "v_add_u32 v203, 65536, v203\n\t" \
    "ds_read_b64_tr_b16 a[176:177], v202 offset:0\n\t" \
    "ds_read_b64_tr_b16 a[178:179], v202 offset:1024\n\t" \
    "ds_read_b64_tr_b16 a[180:181], v202 offset:8192\n\t" \
    "ds_read_b64_tr_b16 a[182:183], v202 offset:9216\n\t" \
    "ds_read_b64_tr_b16 a[248:249], v203 offset:0\n\t" \
    "ds_read_b64_tr_b16 a[250:251], v203 offset:1024\n\t" \
    "ds_read_b64_tr_b16 a[252:253], v203 offset:8192\n\t" \
    "ds_read_b64_tr_b16 a[254:255], v203 offset:9216\n\t" \
    "s_waitcnt vmcnt(30) lgkmcnt(0)\n\t" \
    "s_barrier\n\t" \
    "v_subrev_u32 v200, 4718592, v200\n\t" \
    "s_add_u32 m0, s82, 65536\n\t" \
    "s_nop 0\n\t" \
    "global_load_lds_dwordx4 v200, s[44:45] nt\n\t" \
    "v_add_u32 v200, 131072, v200\n\t" \
    "s_add_u32 m0, s82, 69632\n\t" \
    "s_nop 0\n\t" \
    "global_load_lds_dwordx4 v200, s[44:45] nt\n\t" \
    "v_add_u32 v200, 131072, v200\n\t" \
    "s_add_u32 m0, s82, 73728\n\t" \
    "s_nop 0\n\t" \
    "global_load_lds_dwordx4 v200, s[44:45] nt\n\t" \
    "v_add_u32 v200, 131072, v200\n\t" \
    "s_add_u32 m0, s82, 77824\n\t" \
    "s_nop 0\n\t" \
    "global_load_lds_dwordx4 v200, s[44:45] nt\n\t" \
    "v_add_u32 v200, 131072, v200\n\t" \
    "v_add_u32 v202, -65536, v202\n\t" \
    "v_add_u32 v203, -65536, v203\n\t" \
    "ds_read_b64_tr_b16 a[184:185], v202 offset:0\n\t" \
    "ds_read_b64_tr_b16 a[186:187], v202 offset:1024\n\t" \
    "ds_read_b64_tr_b16 a[188:189], v202 offset:8192\n\t" \
    "ds_read_b64_tr_b16 a[190:191], v202 offset:9216\n\t" \
    "ds_read_b64_tr_b16 v[224:225], v203 offset:0\n\t" \
    "ds_read_b64_tr_b16 v[226:227], v203 offset:1024\n\t" \
    "ds_read_b64_tr_b16 v[228:229], v203 offset:8192\n\t" \
    "ds_read_b64_tr_b16 v[230:231], v203 offset:9216\n\t" \
    "s_waitcnt vmcnt(30) lgkmcnt(0)\n\t" \
    "s_barrier\n\t" \
    "s_add_u32 m0, s82, 0\n\t" \
    "s_nop 0\n\t" \
    "global_load_lds_dwordx4 v200, s[44:45] nt\n\t" \
    "v_add_u32 v200, 131072, v200\n\t" \
    "s_add_u32 m0, s82, 4096\n\t" \
    "s_nop 0\n\t" \
    "global_load_lds_dwordx4 v200, s[44:45] nt\n\t" \
    "v_add_u32 v200, 131072, v200\n\t" \
    "s_add_u32 m0, s82, 8192\n\t" \
    "s_nop 0\n\t" \
    "global_load_lds_dwordx4 v200, s[44:45] nt\n\t" \
    "v_add_u32 v200, 131072, v200\n\t" \
    "s_add_u32 m0, s82, 12288\n\t" \
    "s_nop 0\n\t" \
    "global_load_lds_dwordx4 v200, s[44:45] nt\n\t" \
    "v_add_u32 v200, 131072, v200\n\t" \
    "ds_read_b64_tr_b16 a[192:193], v202 offset:16384\n\t" \
    "ds_read_b64_tr_b16 a[194:195], v202 offset:17408\n\t" \
    "ds_read_b64_tr_b16 a[196:197], v202 offset:24576\n\t" \
    "ds_read_b64_tr_b16 a[198:199], v202 offset:25600\n\t" \
    "ds_read_b64_tr_b16 v[232:233], v203 offset:16384\n\t" \
    "ds_read_b64_tr_b16 v[234:235], v203 offset:17408\n\t" \
    "ds_read_b64_tr_b16 v[236:237], v203 offset:24576\n\t" \
    "ds_read_b64_tr_b16 v[238:239], v203 offset:25600\n\t" \
    "s_waitcnt vmcnt(21) lgkmcnt(0)\n\t" \
    "s_barrier\n\t" \
    "s_add_u32 m0, s82, 16384\n\t" \
    "s_nop 0\n\t" \
    "global_load_lds_dwordx4 v200, s[44:45] nt\n\t" \
    "v_add_u32 v200, 131072, v200\n\t" \
    "s_add_u32 m0, s82, 20480\n\t" \
    "s_nop 0\n\t" \
    "global_load_lds_dwordx4 v200, s[44:45] nt\n\t" \
    "v_add_u32 v200, 131072, v200\n\t" \
    "s_add_u32 m0, s82, 24576\n\t" \
    "s_nop 0\n\t" \
    "global_load_lds_dwordx4 v200, s[44:45] nt\n\t" \
    "v_add_u32 v200, 131072, v200\n\t" \
    "s_add_u32 m0, s82, 28672\n\t" \
    "s_nop 0\n\t" \
    "global_load_lds_dwordx4 v200, s[44:45] nt\n\t" \
    "v_add_u32 v200, 131072, v200\n\t" \
    "v_mbcnt_lo_u32_b32 v164, -1, 0\n\t" \
    "v_mbcnt_hi_u32_b32 v164, -1, v164\n\t" \
    "s_cmp_eq_u32 s66, 0\n\t" \
    "s_cselect_b32 s69, 59, 11\n\t" \
    "v_min_u32 v164, s69, v164\n\t" \
    "v_lshlrev_b32 v164, 6, v164\n\t" \
    "v_add_u32 v164, 6258688, v164\n\t" \
    "v_mov_b32 v165, 6266880\n\t" \
    "global_load_dword v165, v165, s[36:37] sc1\n\t" \
    "global_load_dwordx2 v[166:167], v164, s[36:37] sc1\n\t" \
    "ds_read_b64_tr_b16 a[200:201], v202 offset:32768\n\t" \
    "ds_read_b64_tr_b16 a[202:203], v202 offset:33792\n\t" \
    "ds_read_b64_tr_b16 a[204:205], v202 offset:40960\n\t" \
    "ds_read_b64_tr_b16 a[206:207], v202 offset:41984\n\t" \
    "ds_read_b64_tr_b16 v[240:241], v203 offset:32768\n\t" \
    "ds_read_b64_tr_b16 v[242:243], v203 offset:33792\n\t" \
    "ds_read_b64_tr_b16 v[244:245], v203 offset:40960\n\t" \
    "ds_read_b64_tr_b16 v[246:247], v203 offset:41984\n\t" \
    "s_waitcnt vmcnt(14) lgkmcnt(0)\n\t" \
    "s_barrier\n\t" \
    "s_add_u32 m0, s82, 32768\n\t" \
    "s_nop 0\n\t" \
    "global_load_lds_dwordx4 v200, s[44:45] nt\n\t" \
    "v_add_u32 v200, 131072, v200\n\t" \
    "s_add_u32 m0, s82, 36864\n\t" \
    "s_nop 0\n\t" \
    "global_load_lds_dwordx4 v200, s[44:45] nt\n\t" \
    "v_add_u32 v200, 131072, v200\n\t" \
    "s_add_u32 m0, s82, 40960\n\t" \
    "s_nop 0\n\t" \
    "global_load_lds_dwordx4 v200, s[44:45] nt\n\t" \
    "v_add_u32 v200, 131072, v200\n\t" \
    "s_add_u32 m0, s82, 45056\n\t" \
    "s_nop 0\n\t" \
    "global_load_lds_dwordx4 v200, s[44:45] nt\n\t" \
    "v_add_u32 v200, 131072, v200\n\t" \
    "ds_read_b64_tr_b16 a[208:209], v202 offset:49152\n\t" \
    "ds_read_b64_tr_b16 a[210:211], v202 offset:50176\n\t" \
    "ds_read_b64_tr_b16 a[212:213], v202 offset:57344\n\t" \
    "ds_read_b64_tr_b16 a[214:215], v202 offset:58368\n\t" \
    "ds_read_b64_tr_b16 v[248:249], v203 offset:49152\n\t" \
    "ds_read_b64_tr_b16 v[250:251], v203 offset:50176\n\t" \
    "ds_read_b64_tr_b16 v[252:253], v203 offset:57344\n\t" \
    "ds_read_b64_tr_b16 v[254:255], v203 offset:58368\n\t" \
    "s_waitcnt lgkmcnt(0)\n\t" \
    "s_waitcnt lgkmcnt(6)\n\t" \
    "v_add_u32 v2, 36864, v3\n\t" \
    "ds_read_b128 v[24:27], v2 offset:0\n\t" \
    "ds_read_b128 v[28:31], v2 offset:18432\n\t" \
    "ds_read_b128 v[32:35], v2 offset:1024\n\t" \
    "ds_read_b128 v[36:39], v2 offset:19456\n\t" \
    "s_waitcnt lgkmcnt(3)\n\t" \
    "v_mfma_f32_16x16x32_bf16 v[88:91], a[144:147], v[24:27], 0\n\t" \
    "ds_read_b128 v[40:43], v2 offset:2048\n\t" \
    "s_waitcnt lgkmcnt(3)\n\t" \
    "v_mfma_f32_16x16x32_bf16 v[92:95], a[144:147], v[28:31], 0\n\t" \
    "ds_read_b128 v[44:47], v2 offset:20480\n\t" \
    "v_mfma_f32_16x16x32_bf16 v[96:99], a[216:219], v[24:27], 0\n\t" \
    "v_mfma_f32_16x16x32_bf16 v[100:103], a[216:219], v[28:31], 0\n\t" \
    "s_waitcnt lgkmcnt(3)\n\t" \
    "v_mfma_f32_16x16x32_bf16 v[88:91], a[148:151], v[32:35], v[88:91]\n\t" \
    "ds_read_b128 v[48:51], v2 offset:3072\n\t" \
    "s_waitcnt lgkmcnt(3)\n\t" \
    "v_mfma_f32_16x16x32_bf16 v[92:95], a[148:151], v[36:39], v[92:95]\n\t" \
    "ds_read_b128 v[52:55], v2 offset:21504\n\t" \
    "v_mfma_f32_16x16x32_bf16 v[96:99], a[220:223], v[32:35], v[96:99]\n\t" \
    "v_mfma_f32_16x16x32_bf16 v[100:103], a[220:223], v[36:39], v[100:103]\n\t" \
    "s_waitcnt lgkmcnt(3)\n\t" \
    "v_mfma_f32_16x16x32_bf16 v[88:91], a[152:155], v[40:43], v[88:91]\n\t" \
    "ds_read_b128 v[56:59], v2 offset:4096\n\t" \
    "s_waitcnt lgkmcnt(3)\n\t" \
    "v_mfma_f32_16x16x32_bf16 v[92:95], a[152:155], v[44:47], v[92:95]\n\t" \
    "ds_read_b128 v[60:63], v2 offset:22528\n\t" \
    "v_mfma_f32_16x16x32_bf16 v[96:99], a[224:227], v[40:43], v[96:99]\n\t" \
    "v_mfma_f32_16x16x32_bf16 v[100:103], a[224:227], v[44:47], v[100:103]\n\t" \
    "s_waitcnt lgkmcnt(3)\n\t" \
    "v_mfma_f32_16x16x32_bf16 v[88:91], a[156:159], v[48:51], v[88:91]\n\t" \
    "ds_read_b128 v[64:67], v2 offset:5120\n\t" \
    "s_waitcnt lgkmcnt(3)\n\t" \
    "v_mfma_f32_16x16x32_bf16 v[92:95], a[156:159], v[52:55], v[92:95]\n\t" \
    "ds_read_b128 v[68:71], v2 offset:23552\n\t" \
    "v_mfma_f32_16x16x32_bf16 v[96:99], a[228:231], v[48:51], v[96:99]\n\t" \
    "v_mfma_f32_16x16x32_bf16 v[100:103], a[228:231], v[52:55], v[100:103]\n\t" \
    "s_waitcnt lgkmcnt(3)\n\t" \
    "v_mfma_f32_16x16x32_bf16 v[88:91], a[160:163], v[56:59], v[88:91]\n\t" \
    "ds_read_b128 v[24:27], v2 offset:6144\n\t" \
    "s_waitcnt lgkmcnt(3)\n\t" \
    "v_mfma_f32_16x16x32_bf16 v[92:95], a[160:163], v[60:63], v[92:95]\n\t" \
    "ds_read_b128 v[28:31], v2 offset:24576\n\t" \
    "v_mfma_f32_16x16x32_bf16 v[96:99], a[232:235], v[56:59], v[96:99]\n\t" \
    "v_mfma_f32_16x16x32_bf16 v[100:103], a[232:235], v[60:63], v[100:103]\n\t" \
    "s_waitcnt lgkmcnt(3)\n\t" \
    "v_mfma_f32_16x16x32_bf16 v[88:91], a[164:167], v[64:67], v[88:91]\n\t" \
    "ds_read_b128 v[32:35], v2 offset:7168\n\t" \
    "s_waitcnt lgkmcnt(3)\n\t" \
    "v_mfma_f32_16x16x32_bf16 v[92:95], a[164:167], v[68:71], v[92:95]\n\t" \
    "ds_read_b128 v[36:39], v2 offset:25600\n\t" \
    "v_mfma_f32_16x16x32_bf16 v[96:99], a[236:239], v[64:67], v[96:99]\n\t" \
    "v_mfma_f32_16x16x32_bf16 v[100:103], a[236:239], v[68:71], v[100:103]\n\t" \
    "s_waitcnt lgkmcnt(3)\n\t" \
    "v_mfma_f32_16x16x32_bf16 v[88:91], a[168:171], v[24:27], v[88:91]\n\t" \
    "ds_read_b128 v[40:43], v2 offset:8192\n\t" \
    "s_waitcnt lgkmcnt(3)\n\t" \
    "v_mfma_f32_16x16x32_bf16 v[92:95], a[168:171], v[28:31], v[92:95]\n\t" \
    "ds_read_b128 v[44:47], v2 offset:26624\n\t" \
    "v_mfma_f32_16x16x32_bf16 v[96:99], a[240:243], v[24:27], v[96:99]\n\t" \
    "v_mfma_f32_16x16x32_bf16 v[100:103], a[240:243], v[28:31], v[100:103]\n\t" \
    "s_waitcnt lgkmcnt(3)\n\t" \
    "v_mfma_f32_16x16x32_bf16 v[88:91], a[172:175], v[32:35], v[88:91]\n\t" \
    "ds_read_b128 v[48:51], v2 offset:9216\n\t" \
    "s_waitcnt lgkmcnt(3)\n\t" \
    "v_mfma_f32_16x16x32_bf16 v[92:95], a[172:175], v[36:39], v[92:95]\n\t" \
    "ds_read_b128 v[52:55], v2 offset:27648\n\t" \
    "v_mfma_f32_16x16x32_bf16 v[96:99], a[244:247], v[32:35], v[96:99]\n\t" \
    "v_mfma_f32_16x16x32_bf16 v[100:103], a[244:247], v[36:39], v[100:103]\n\t" \
    "s_waitcnt lgkmcnt(3)\n\t" \
    "v_mfma_f32_16x16x32_bf16 v[88:91], a[176:179], v[40:43], v[88:91]\n\t" \
    "ds_read_b128 v[56:59], v2 offset:10240\n\t" \
    "s_waitcnt lgkmcnt(3)\n\t" \
    "v_mfma_f32_16x16x32_bf16 v[92:95], a[176:179], v[44:47], v[92:95]\n\t" \
    "ds_read_b128 v[60:63], v2 offset:28672\n\t" \
    "v_mfma_f32_16x16x32_bf16 v[96:99], a[248:251], v[40:43], v[96:99]\n\t" \
    "v_mfma_f32_16x16x32_bf16 v[100:103], a[248:251], v[44:47], v[100:103]\n\t" \
    "s_waitcnt lgkmcnt(3)\n\t" \
    "v_mfma_f32_16x16x32_bf16 v[88:91], a[180:183], v[48:51], v[88:91]\n\t" \
    "ds_read_b128 v[64:67], v2 offset:11264\n\t" \
    "s_waitcnt lgkmcnt(3)\n\t" \
    "v_mfma_f32_16x16x32_bf16 v[92:95], a[180:183], v[52:55], v[92:95]\n\t" \
    "ds_read_b128 v[68:71], v2 offset:29696\n\t" \
    "v_mfma_f32_16x16x32_bf16 v[96:99], a[252:255], v[48:51], v[96:99]\n\t" \
    "v_mfma_f32_16x16x32_bf16 v[100:103], a[252:255], v[52:55], v[100:103]\n\t" \
    "s_waitcnt lgkmcnt(3)\n\t" \
    "v_mfma_f32_16x16x32_bf16 v[88:91], a[184:187], v[56:59], v[88:91]\n\t" \
    "ds_read_b128 v[24:27], v2 offset:12288\n\t" \
    "s_waitcnt lgkmcnt(3)\n\t" \
    "v_mfma_f32_16x16x32_bf16 v[92:95], a[184:187], v[60:63], v[92:95]\n\t" \
    "ds_read_b128 v[28:31], v2 offset:30720\n\t" \
    "v_mfma_f32_16x16x32_bf16 v[96:99], v[224:227], v[56:59], v[96:99]\n\t" \
    "v_mfma_f32_16x16x32_bf16 v[100:103], v[224:227], v[60:63], v[100:103]\n\t" \
    "s_waitcnt lgkmcnt(3)\n\t" \
    "v_mfma_f32_16x16x32_bf16 v[88:91], a[188:191], v[64:67], v[88:91]\n\t" \
    "ds_read_b128 v[32:35], v2 offset:13312\n\t" \
    "s_waitcnt lgkmcnt(3)\n\t" \
    "v_mfma_f32_16x16x32_bf16 v[92:95], a[188:191], v[68:71], v[92:95]\n\t" \
    "ds_read_b128 v[36:39], v2 offset:31744\n\t" \
    "v_mfma_f32_16x16x32_bf16 v[96:99], v[228:231], v[64:67], v[96:99]\n\t" \
    "v_mfma_f32_16x16x32_bf16 v[100:103], v[228:231], v[68:71], v[100:103]\n\t" \
    "s_waitcnt lgkmcnt(3)\n\t" \
    "v_mfma_f32_16x16x32_bf16 v[88:91], a[192:195], v[24:27], v[88:91]\n\t" \
    "ds_read_b128 v[40:43], v2 offset:14336\n\t" \
    "s_waitcnt lgkmcnt(3)\n\t" \
    "v_mfma_f32_16x16x32_bf16 v[92:95], a[192:195], v[28:31], v[92:95]\n\t" \
    "ds_read_b128 v[44:47], v2 offset:32768\n\t" \
    "v_mfma_f32_16x16x32_bf16 v[96:99], v[232:235], v[24:27], v[96:99]\n\t" \
    "v_mfma_f32_16x16x32_bf16 v[100:103], v[232:235], v[28:31], v[100:103]\n\t" \
    "s_waitcnt lgkmcnt(3)\n\t" \
    "v_mfma_f32_16x16x32_bf16 v[88:91], a[196:199], v[32:35], v[88:91]\n\t" \
    "ds_read_b128 v[48:51], v2 offset:15360\n\t" \
    "s_waitcnt lgkmcnt(3)\n\t" \
    "v_mfma_f32_16x16x32_bf16 v[92:95], a[196:199], v[36:39], v[92:95]\n\t" \
    "ds_read_b128 v[52:55], v2 offset:33792\n\t" \
    "v_mfma_f32_16x16x32_bf16 v[96:99], v[236:239], v[32:35], v[96:99]\n\t" \
    "v_mfma_f32_16x16x32_bf16 v[100:103], v[236:239], v[36:39], v[100:103]\n\t" \
    "s_waitcnt lgkmcnt(3)\n\t" \
    "v_mfma_f32_16x16x32_bf16 v[88:91], a[200:203], v[40:43], v[88:91]\n\t" \
    "ds_read_b128 v[56:59], v2 offset:16384\n\t" \
    "s_waitcnt lgkmcnt(3)\n\t" \
    "v_mfma_f32_16x16x32_bf16 v[92:95], a[200:203], v[44:47], v[92:95]\n\t" \
    "ds_read_b128 v[60:63], v2 offset:34816\n\t" \
    "v_mfma_f32_16x16x32_bf16 v[96:99], v[240:243], v[40:43], v[96:99]\n\t" \
    "v_mfma_f32_16x16x32_bf16 v[100:103], v[240:243], v[44:47], v[100:103]\n\t" \
    "s_waitcnt lgkmcnt(3)\n\t" \
    "v_mfma_f32_16x16x32_bf16 v[88:91], a[204:207], v[48:51], v[88:91]\n\t" \
    "ds_read_b128 v[64:67], v2 offset:17408\n\t" \
    "s_waitcnt lgkmcnt(3)\n\t" \
    "v_mfma_f32_16x16x32_bf16 v[92:95], a[204:207], v[52:55], v[92:95]\n\t" \
    "ds_read_b128 v[68:71], v2 offset:35840\n\t" \
    "v_mfma_f32_16x16x32_bf16 v[96:99], v[244:247], v[48:51], v[96:99]\n\t" \
    "v_mfma_f32_16x16x32_bf16 v[100:103], v[244:247], v[52:55], v[100:103]\n\t" \
    "s_waitcnt lgkmcnt(3)\n\t" \
    "v_mfma_f32_16x16x32_bf16 v[88:91], a[208:211], v[56:59], v[88:91]\n\t" \
    "s_waitcnt lgkmcnt(2)\n\t" \
    "v_mfma_f32_16x16x32_bf16 v[92:95], a[208:211], v[60:63], v[92:95]\n\t" \
    "v_mfma_f32_16x16x32_bf16 v[96:99], v[248:251], v[56:59], v[96:99]\n\t" \
    "v_mfma_f32_16x16x32_bf16 v[100:103], v[248:251], v[60:63], v[100:103]\n\t" \
    "s_waitcnt lgkmcnt(1)\n\t" \
    "v_mfma_f32_16x16x32_bf16 v[88:91], a[212:215], v[64:67], v[88:91]\n\t" \
    "s_waitcnt lgkmcnt(0)\n\t" \
    "v_mfma_f32_16x16x32_bf16 v[92:95], a[212:215], v[68:71], v[92:95]\n\t" \
    "v_mfma_f32_16x16x32_bf16 v[96:99], v[252:255], v[64:67], v[96:99]\n\t" \
    "v_mfma_f32_16x16x32_bf16 v[100:103], v[252:255], v[68:71], v[100:103]\n\t" \
    "s_mul_i32 s70, s67, 7\n\t" \
    "s_add_u32 s69, s65, 1\n\t" \
    "s_xor_b32 s69, s69, s70\n\t" \
    "s_and_b32 s69, s69, 63\n\t" \
    "s_lshl_b32 s69, s69, 13\n\t" \
    "s_mul_i32 s70, s67, 524288\n\t" \
    "s_add_u32 s69, s69, s70\n\t" \
    "s_add_u32 s69, s69, 6291456\n\t" \
    "s_add_u32 s38, s36, s69\n\t" \
    "s_addc_u32 s39, s37, 0\n\t" \
    "s_add_u32 s40, s38, 16777216\n\t" \
    "s_addc_u32 s41, s39, 0\n\t" \
    "v_cvt_pk_bf16_f32 v112, v88, v89\n\t" \
    "v_cvt_pk_bf16_f32 v113, v90, v91\n\t" \
    "v_cvt_pk_bf16_f32 v114, v96, v97\n\t" \
    "v_cvt_pk_bf16_f32 v115, v98, v99\n\t" \
    "global_store_dwordx4 v8, v[112:115], s[40:41] offset:0 sc1\n\t" \
    "v_cvt_pk_bf16_f32 v116, v92, v93\n\t" \
    "v_cvt_pk_bf16_f32 v117, v94, v95\n\t" \
    "v_cvt_pk_bf16_f32 v118, v100, v101\n\t" \
    "v_cvt_pk_bf16_f32 v119, v102, v103\n\t" \
    "global_store_dwordx4 v8, v[116:119], s[40:41] offset:1024 sc1\n\t" \
    "s_waitcnt lgkmcnt(6)\n\t" \
    "v_add_u32 v2, 0, v3\n\t" \
    "ds_read_b128 v[24:27], v2 offset:0\n\t" \
    "ds_read_b128 v[28:31], v2 offset:18432\n\t" \
    "ds_read_b128 v[32:35], v2 offset:1024\n\t" \
    "ds_read_b128 v[36:39], v2 offset:19456\n\t" \
    "s_waitcnt lgkmcnt(3)\n\t" \
    "v_mfma_f32_16x16x32_bf16 v[88:91], a[144:147], v[24:27], 0\n\t" \
    "ds_read_b128 v[40:43], v2 offset:2048\n\t" \
    "s_waitcnt lgkmcnt(3)\n\t" \
    "v_mfma_f32_16x16x32_bf16 v[92:95], a[144:147], v[28:31], 0\n\t" \
    "ds_read_b128 v[44:47], v2 offset:20480\n\t" \
    "v_mfma_f32_16x16x32_bf16 v[96:99], a[216:219], v[24:27], 0\n\t" \
    "v_mfma_f32_16x16x32_bf16 v[100:103], a[216:219], v[28:31], 0\n\t" \
    "s_waitcnt lgkmcnt(3)\n\t" \
    "v_mfma_f32_16x16x32_bf16 v[88:91], a[148:151], v[32:35], v[88:91]\n\t" \
    "ds_read_b128 v[48:51], v2 offset:3072\n\t" \
    "s_waitcnt lgkmcnt(3)\n\t" \
    "v_mfma_f32_16x16x32_bf16 v[92:95], a[148:151], v[36:39], v[92:95]\n\t" \
    "ds_read_b128 v[52:55], v2 offset:21504\n\t" \
    "v_mfma_f32_16x16x32_bf16 v[96:99], a[220:223], v[32:35], v[96:99]\n\t" \
    "v_mfma_f32_16x16x32_bf16 v[100:103], a[220:223], v[36:39], v[100:103]\n\t" \
    "s_waitcnt lgkmcnt(3)\n\t" \
    "v_mfma_f32_16x16x32_bf16 v[88:91], a[152:155], v[40:43], v[88:91]\n\t" \
    "ds_read_b128 v[56:59], v2 offset:4096\n\t" \
    "s_waitcnt lgkmcnt(3)\n\t" \
    "v_mfma_f32_16x16x32_bf16 v[92:95], a[152:155], v[44:47], v[92:95]\n\t" \
    "ds_read_b128 v[60:63], v2 offset:22528\n\t" \
    "v_mfma_f32_16x16x32_bf16 v[96:99], a[224:227], v[40:43], v[96:99]\n\t" \
    "v_mfma_f32_16x16x32_bf16 v[100:103], a[224:227], v[44:47], v[100:103]\n\t" \
    "s_waitcnt lgkmcnt(3)\n\t" \
    "v_mfma_f32_16x16x32_bf16 v[88:91], a[156:159], v[48:51], v[88:91]\n\t" \
    "ds_read_b128 v[64:67], v2 offset:5120\n\t" \
    "s_waitcnt lgkmcnt(3)\n\t" \
    "v_mfma_f32_16x16x32_bf16 v[92:95], a[156:159], v[52:55], v[92:95]\n\t" \
    "ds_read_b128 v[68:71], v2 offset:23552\n\t" \
    "v_mfma_f32_16x16x32_bf16 v[96:99], a[228:231], v[48:51], v[96:99]\n\t" \
    "v_mfma_f32_16x16x32_bf16 v[100:103], a[228:231], v[52:55], v[100:103]\n\t" \
    "s_waitcnt lgkmcnt(3)\n\t" \
    "v_mfma_f32_16x16x32_bf16 v[88:91], a[160:163], v[56:59], v[88:91]\n\t" \
    "ds_read_b128 v[24:27], v2 offset:6144\n\t" \
    "s_waitcnt lgkmcnt(3)\n\t" \
    "v_mfma_f32_16x16x32_bf16 v[92:95], a[160:163], v[60:63], v[92:95]\n\t" \
    "ds_read_b128 v[28:31], v2 offset:24576\n\t" \
    "v_mfma_f32_16x16x32_bf16 v[96:99], a[232:235], v[56:59], v[96:99]\n\t" \
    "v_mfma_f32_16x16x32_bf16 v[100:103], a[232:235], v[60:63], v[100:103]\n\t" \
    "s_waitcnt lgkmcnt(3)\n\t" \
    "v_mfma_f32_16x16x32_bf16 v[88:91], a[164:167], v[64:67], v[88:91]\n\t" \
    "ds_read_b128 v[32:35], v2 offset:7168\n\t" \
    "s_waitcnt lgkmcnt(3)\n\t" \
    "v_mfma_f32_16x16x32_bf16 v[92:95], a[164:167], v[68:71], v[92:95]\n\t" \
    "ds_read_b128 v[36:39], v2 offset:25600\n\t" \
    "v_mfma_f32_16x16x32_bf16 v[96:99], a[236:239], v[64:67], v[96:99]\n\t" \
    "v_mfma_f32_16x16x32_bf16 v[100:103], a[236:239], v[68:71], v[100:103]\n\t" \
    "s_waitcnt lgkmcnt(3)\n\t" \
    "v_mfma_f32_16x16x32_bf16 v[88:91], a[168:171], v[24:27], v[88:91]\n\t" \
    "ds_read_b128 v[40:43], v2 offset:8192\n\t" \
    "s_waitcnt lgkmcnt(3)\n\t" \
    "v_mfma_f32_16x16x32_bf16 v[92:95], a[168:171], v[28:31], v[92:95]\n\t" \
    "ds_read_b128 v[44:47], v2 offset:26624\n\t" \
    "v_mfma_f32_16x16x32_bf16 v[96:99], a[240:243], v[24:27], v[96:99]\n\t" \
    "v_mfma_f32_16x16x32_bf16 v[100:103], a[240:243], v[28:31], v[100:103]\n\t" \
    "s_waitcnt lgkmcnt(3)\n\t" \
    "v_mfma_f32_16x16x32_bf16 v[88:91], a[172:175], v[32:35], v[88:91]\n\t" \
    "ds_read_b128 v[48:51], v2 offset:9216\n\t" \
    "s_waitcnt lgkmcnt(3)\n\t" \
    "v_mfma_f32_16x16x32_bf16 v[92:95], a[172:175], v[36:39], v[92:95]\n\t" \
    "ds_read_b128 v[52:55], v2 offset:27648\n\t" \
    "v_mfma_f32_16x16x32_bf16 v[96:99], a[244:247], v[32:35], v[96:99]\n\t" \
    "v_mfma_f32_16x16x32_bf16 v[100:103], a[244:247], v[36:39], v[100:103]\n\t" \
    "s_waitcnt lgkmcnt(3)\n\t" \
    "v_mfma_f32_16x16x32_bf16 v[88:91], a[176:179], v[40:43], v[88:91]\n\t" \
    "ds_read_b128 v[56:59], v2 offset:10240\n\t" \
    "s_waitcnt lgkmcnt(3)\n\t" \
    "v_mfma_f32_16x16x32_bf16 v[92:95], a[176:179], v[44:47], v[92:95]\n\t" \
    "ds_read_b128 v[60:63], v2 offset:28672\n\t" \
    "v_mfma_f32_16x16x32_bf16 v[96:99], a[248:251], v[40:43], v[96:99]\n\t" \
    "v_mfma_f32_16x16x32_bf16 v[100:103], a[248:251], v[44:47], v[100:103]\n\t" \
    "s_waitcnt lgkmcnt(3)\n\t" \
    "v_mfma_f32_16x16x32_bf16 v[88:91], a[180:183], v[48:51], v[88:91]\n\t" \
    "ds_read_b128 v[64:67], v2 offset:11264\n\t" \
    "s_waitcnt lgkmcnt(3)\n\t" \
    "v_mfma_f32_16x16x32_bf16 v[92:95], a[180:183], v[52:55], v[92:95]\n\t" \
    "ds_read_b128 v[68:71], v2 offset:29696\n\t" \
    "v_mfma_f32_16x16x32_bf16 v[96:99], a[252:255], v[48:51], v[96:99]\n\t" \
    "v_mfma_f32_16x16x32_bf16 v[100:103], a[252:255], v[52:55], v[100:103]\n\t" \
    "s_waitcnt lgkmcnt(3)\n\t" \
    "v_mfma_f32_16x16x32_bf16 v[88:91], a[184:187], v[56:59], v[88:91]\n\t" \
    "ds_read_b128 v[24:27], v2 offset:12288\n\t" \
    "s_waitcnt lgkmcnt(3)\n\t" \
    "v_mfma_f32_16x16x32_bf16 v[92:95], a[184:187], v[60:63], v[92:95]\n\t" \
    "ds_read_b128 v[28:31], v2 offset:30720\n\t" \
    "v_mfma_f32_16x16x32_bf16 v[96:99], v[224:227], v[56:59], v[96:99]\n\t" \
    "v_mfma_f32_16x16x32_bf16 v[100:103], v[224:227], v[60:63], v[100:103]\n\t" \
    "s_waitcnt lgkmcnt(3)\n\t" \
    "v_mfma_f32_16x16x32_bf16 v[88:91], a[188:191], v[64:67], v[88:91]\n\t" \
    "ds_read_b128 v[32:35], v2 offset:13312\n\t" \
    "s_waitcnt lgkmcnt(3)\n\t" \
    "v_mfma_f32_16x16x32_bf16 v[92:95], a[188:191], v[68:71], v[92:95]\n\t" \
    "ds_read_b128 v[36:39], v2 offset:31744\n\t" \
    "v_mfma_f32_16x16x32_bf16 v[96:99], v[228:231], v[64:67], v[96:99]\n\t" \
    "v_mfma_f32_16x16x32_bf16 v[100:103], v[228:231], v[68:71], v[100:103]\n\t" \
    "s_waitcnt lgkmcnt(3)\n\t" \
    "v_mfma_f32_16x16x32_bf16 v[88:91], a[192:195], v[24:27], v[88:91]\n\t" \
    "ds_read_b128 v[40:43], v2 offset:14336\n\t" \
    "s_waitcnt lgkmcnt(3)\n\t" \
    "v_mfma_f32_16x16x32_bf16 v[92:95], a[192:195], v[28:31], v[92:95]\n\t" \
    "ds_read_b128 v[44:47], v2 offset:32768\n\t" \
    "v_mfma_f32_16x16x32_bf16 v[96:99], v[232:235], v[24:27], v[96:99]\n\t" \
    "v_mfma_f32_16x16x32_bf16 v[100:103], v[232:235], v[28:31], v[100:103]\n\t" \
    "s_waitcnt lgkmcnt(3)\n\t" \
    "v_mfma_f32_16x16x32_bf16 v[88:91], a[196:199], v[32:35], v[88:91]\n\t" \
    "ds_read_b128 v[48:51], v2 offset:15360\n\t" \
    "s_waitcnt lgkmcnt(3)\n\t" \
    "v_mfma_f32_16x16x32_bf16 v[92:95], a[196:199], v[36:39], v[92:95]\n\t" \
    "ds_read_b128 v[52:55], v2 offset:33792\n\t" \
    "v_mfma_f32_16x16x32_bf16 v[96:99], v[236:239], v[32:35], v[96:99]\n\t" \
    "v_mfma_f32_16x16x32_bf16 v[100:103], v[236:239], v[36:39], v[100:103]\n\t" \
    "s_waitcnt lgkmcnt(3)\n\t" \
    "v_mfma_f32_16x16x32_bf16 v[88:91], a[200:203], v[40:43], v[88:91]\n\t" \
    "ds_read_b128 v[56:59], v2 offset:16384\n\t" \
    "s_waitcnt lgkmcnt(3)\n\t" \
    "v_mfma_f32_16x16x32_bf16 v[92:95], a[200:203], v[44:47], v[92:95]\n\t" \
    "ds_read_b128 v[60:63], v2 offset:34816\n\t" \
    "v_mfma_f32_16x16x32_bf16 v[96:99], v[240:243], v[40:43], v[96:99]\n\t" \
    "v_mfma_f32_16x16x32_bf16 v[100:103], v[240:243], v[44:47], v[100:103]\n\t" \
    "s_waitcnt lgkmcnt(3)\n\t" \
    "v_mfma_f32_16x16x32_bf16 v[88:91], a[204:207], v[48:51], v[88:91]\n\t" \
    "ds_read_b128 v[64:67], v2 offset:17408\n\t" \
    "s_waitcnt lgkmcnt(3)\n\t" \
    "v_mfma_f32_16x16x32_bf16 v[92:95], a[204:207], v[52:55], v[92:95]\n\t" \
    "ds_read_b128 v[68:71], v2 offset:35840\n\t" \
    "v_mfma_f32_16x16x32_bf16 v[96:99], v[244:247], v[48:51], v[96:99]\n\t" \
    "v_mfma_f32_16x16x32_bf16 v[100:103], v[244:247], v[52:55], v[100:103]\n\t" \
    "s_waitcnt lgkmcnt(3)\n\t" \
    "v_mfma_f32_16x16x32_bf16 v[88:91], a[208:211], v[56:59], v[88:91]\n\t" \
    "s_waitcnt lgkmcnt(2)\n\t" \
    "v_mfma_f32_16x16x32_bf16 v[92:95], a[208:211], v[60:63], v[92:95]\n\t" \
    "v_mfma_f32_16x16x32_bf16 v[96:99], v[248:251], v[56:59], v[96:99]\n\t" \
    "v_mfma_f32_16x16x32_bf16 v[100:103], v[248:251], v[60:63], v[100:103]\n\t" \
    "s_waitcnt lgkmcnt(1)\n\t" \
    "v_mfma_f32_16x16x32_bf16 v[88:91], a[212:215], v[64:67], v[88:91]\n\t" \
    "s_waitcnt lgkmcnt(0)\n\t" \
    "v_mfma_f32_16x16x32_bf16 v[92:95], a[212:215], v[68:71], v[92:95]\n\t" \
    "v_mfma_f32_16x16x32_bf16 v[96:99], v[252:255], v[64:67], v[96:99]\n\t" \
    "v_mfma_f32_16x16x32_bf16 v[100:103], v[252:255], v[68:71], v[100:103]\n\t" \
    "s_mul_i32 s70, s67, 7\n\t" \
    "s_add_u32 s69, s65, 0\n\t" \
    "s_xor_b32 s69, s69, s70\n\t" \
    "s_and_b32 s69, s69, 63\n\t" \
    "s_lshl_b32 s69, s69, 13\n\t" \
    "s_mul_i32 s70, s67, 524288\n\t" \
    "s_add_u32 s69, s69, s70\n\t" \
    "s_add_u32 s69, s69, 6291456\n\t" \
    "s_add_u32 s38, s36, s69\n\t" \
    "s_addc_u32 s39, s37, 0\n\t" \
    "s_add_u32 s40, s38, 16777216\n\t" \
    "s_addc_u32 s41, s39, 0\n\t" \
    "v_cvt_pk_bf16_f32 v112, v88, v89\n\t" \
    "v_cvt_pk_bf16_f32 v113, v90, v91\n\t" \
    "v_cvt_pk_bf16_f32 v114, v96, v97\n\t" \
    "v_cvt_pk_bf16_f32 v115, v98, v99\n\t" \
    "global_store_dwordx4 v8, v[112:115], s[40:41] offset:0 sc1\n\t" \
    "v_cvt_pk_bf16_f32 v116, v92, v93\n\t" \
    "v_cvt_pk_bf16_f32 v117, v94, v95\n\t" \
    "v_cvt_pk_bf16_f32 v118, v100, v101\n\t" \
    "v_cvt_pk_bf16_f32 v119, v102, v103\n\t" \
    "global_store_dwordx4 v8, v[116:119], s[40:41] offset:1024 sc1\n\t" \
    "s_waitcnt vmcnt(18) lgkmcnt(0)\n\t" \
    "s_barrier\n\t" \
    "s_add_u32 m0, s82, 49152\n\t" \
    "s_nop 0\n\t" \
    "global_load_lds_dwordx4 v200, s[44:45] nt\n\t" \
    "v_add_u32 v200, 131072, v200\n\t" \
    "s_add_u32 m0, s82, 53248\n\t" \
    "s_nop 0\n\t" \
    "global_load_lds_dwordx4 v200, s[44:45] nt\n\t" \
    "v_add_u32 v200, 131072, v200\n\t" \
    "s_add_u32 m0, s82, 57344\n\t" \
    "s_nop 0\n\t" \
    "global_load_lds_dwordx4 v200, s[44:45] nt\n\t" \
    "v_add_u32 v200, 131072, v200\n\t" \
    "s_add_u32 m0, s82, 61440\n\t" \
    "s_nop 0\n\t" \
    "global_load_lds_dwordx4 v200, s[44:45] nt\n\t" \
    "v_add_u32 v200, 131072, v200\n\t" \
    "v_add_u32 v202, 65536, v202\n\t" \
    "v_add_u32 v203, 65536, v203\n\t" \
    "ds_read_b64_tr_b16 a[0:1], v202 offset:0\n\t" \
    "ds_read_b64_tr_b16 a[2:3], v202 offset:1024\n\t" \
    "ds_read_b64_tr_b16 a[4:5], v202 offset:8192\n\t" \
    "ds_read_b64_tr_b16 a[6:7], v202 offset:9216\n\t" \
    "ds_read_b64_tr_b16 a[72:73], v203 offset:0\n\t" \
    "ds_read_b64_tr_b16 a[74:75], v203 offset:1024\n\t" \
    "ds_read_b64_tr_b16 a[76:77], v203 offset:8192\n\t" \
    "ds_read_b64_tr_b16 a[78:79], v203 offset:9216\n\t" \
    "s_waitcnt vmcnt(18) lgkmcnt(0)\n\t" \
    "s_barrier\n\t" \
    "s_add_u32 m0, s82, 65536\n\t" \
    "s_nop 0\n\t" \
    "global_load_lds_dwordx4 v200, s[44:45] nt\n\t" \
    "v_add_u32 v200, 131072, v200\n\t" \
    "s_add_u32 m0, s82, 69632\n\t" \
    "s_nop 0\n\t" \
    "global_load_lds_dwordx4 v200, s[44:45] nt\n\t" \
    "v_add_u32 v200, 131072, v200\n\t" \
    "s_add_u32 m0, s82, 73728\n\t" \
    "s_nop 0\n\t" \
    "global_load_lds_dwordx4 v200, s[44:45] nt\n\t" \
    "v_add_u32 v200, 131072, v200\n\t" \
    "s_add_u32 m0, s82, 77824\n\t" \
    "s_nop 0\n\t" \
    "global_load_lds_dwordx4 v200, s[44:45] nt\n\t" \
    "v_add_u32 v200, 131072, v200\n\t" \
    "v_add_u32 v202, -65536, v202\n\t" \
    "v_add_u32 v203, -65536, v203\n\t" \
    "ds_read_b64_tr_b16 a[8:9], v202 offset:0\n\t" \
    "ds_read_b64_tr_b16 a[10:11], v202 offset:1024\n\t" \
    "ds_read_b64_tr_b16 a[12:13], v202 offset:8192\n\t" \
    "ds_read_b64_tr_b16 a[14:15], v202 offset:9216\n\t" \
    "ds_read_b64_tr_b16 a[80:81], v203 offset:0\n\t" \
    "ds_read_b64_tr_b16 a[82:83], v203 offset:1024\n\t" \
    "ds_read_b64_tr_b16 a[84:85], v203 offset:8192\n\t" \
    "ds_read_b64_tr_b16 a[86:87], v203 offset:9216\n\t" \
    "s_waitcnt vmcnt(18) lgkmcnt(0)\n\t" \
    "s_barrier\n\t" \
    "s_add_u32 m0, s82, 0\n\t" \
    "s_nop 0\n\t" \
    "global_load_lds_dwordx4 v200, s[44:45] nt\n\t" \
    "v_add_u32 v200, 131072, v200\n\t" \
    "s_add_u32 m0, s82, 4096\n\t" \
    "s_nop 0\n\t" \
    "global_load_lds_dwordx4 v200, s[44:45] nt\n\t" \
    "v_add_u32 v200, 131072, v200\n\t" \
    "s_add_u32 m0, s82, 8192\n\t" \
    "s_nop 0\n\t" \
    "global_load_lds_dwordx4 v200, s[44:45] nt\n\t" \
    "v_add_u32 v200, 131072, v200\n\t" \
    "s_add_u32 m0, s82, 12288\n\t" \
    "s_nop 0\n\t" \
    "global_load_lds_dwordx4 v200, s[44:45] nt\n\t" \
    "v_add_u32 v200, 131072, v200\n\t" \
    "ds_read_b64_tr_b16 a[16:17], v202 offset:16384\n\t" \
    "ds_read_b64_tr_b16 a[18:19], v202 offset:17408\n\t" \
    "ds_read_b64_tr_b16 a[20:21], v202 offset:24576\n\t" \
    "ds_read_b64_tr_b16 a[22:23], v202 offset:25600\n\t" \
    "ds_read_b64_tr_b16 a[88:89], v203 offset:16384\n\t" \
    "ds_read_b64_tr_b16 a[90:91], v203 offset:17408\n\t" \
    "ds_read_b64_tr_b16 a[92:93], v203 offset:24576\n\t" \
    "ds_read_b64_tr_b16 a[94:95], v203 offset:25600\n\t" \
    "s_waitcnt vmcnt(16) lgkmcnt(0)\n\t" \
    "s_barrier\n\t" \
    "s_add_u32 m0, s82, 16384\n\t" \
    "s_nop 0\n\t" \
    "global_load_lds_dwordx4 v200, s[44:45] nt\n\t" \
    "v_add_u32 v200, 131072, v200\n\t" \
    "s_add_u32 m0, s82, 20480\n\t" \
    "s_nop 0\n\t" \
    "global_load_lds_dwordx4 v200, s[44:45] nt\n\t" \
    "v_add_u32 v200, 131072, v200\n\t" \
    "s_add_u32 m0, s82, 24576\n\t" \
    "s_nop 0\n\t" \
    "global_load_lds_dwordx4 v200, s[44:45] nt\n\t" \
    "v_add_u32 v200, 131072, v200\n\t" \
    "s_add_u32 m0, s82, 28672\n\t" \
    "s_nop 0\n\t" \
    "global_load_lds_dwordx4 v200, s[44:45] nt\n\t" \
    "v_add_u32 v200, 131072, v200\n\t" \
    "ds_read_b64_tr_b16 a[24:25], v202 offset:32768\n\t" \
    "ds_read_b64_tr_b16 a[26:27], v202 offset:33792\n\t" \
    "ds_read_b64_tr_b16 a[28:29], v202 offset:40960\n\t" \
    "ds_read_b64_tr_b16 a[30:31], v202 offset:41984\n\t" \
    "ds_read_b64_tr_b16 a[96:97], v203 offset:32768\n\t" \
    "ds_read_b64_tr_b16 a[98:99], v203 offset:33792\n\t" \
    "ds_read_b64_tr_b16 a[100:101], v203 offset:40960\n\t" \
    "ds_read_b64_tr_b16 a[102:103], v203 offset:41984\n\t" \
    "s_waitcnt vmcnt(12) lgkmcnt(0)\n\t" \
    "s_barrier\n\t" \
    "s_add_u32 m0, s82, 32768\n\t" \
    "s_nop 0\n\t" \
    "global_load_lds_dwordx4 v200, s[44:45] nt\n\t" \
    "v_add_u32 v200, 131072, v200\n\t" \
    "s_add_u32 m0, s82, 36864\n\t" \
    "s_nop 0\n\t" \
    "global_load_lds_dwordx4 v200, s[44:45] nt\n\t" \
    "v_add_u32 v200, 131072, v200\n\t" \
    "s_add_u32 m0, s82, 40960\n\t" \
    "s_nop 0\n\t" \
    "global_load_lds_dwordx4 v200, s[44:45] nt\n\t" \
    "v_add_u32 v200, 131072, v200\n\t" \
    "s_add_u32 m0, s82, 45056\n\t" \
    "s_nop 0\n\t" \
    "global_load_lds_dwordx4 v200, s[44:45] nt\n\t" \
    "v_add_u32 v200, 131072, v200\n\t" \
    "v_sub_u32 v166, v166, v165\n\t" \
    "v_cmp_ne_u32 vcc, 1, v166\n\t" \
    "s_cbranch_vccz gx_Cok2_%=\n\t" \
    "gx_C1_%=:\n\t" \
    "global_load_dwordx2 v[166:167], v164, s[36:37] sc1\n\t" \
    "s_waitcnt vmcnt(0)\n\t" \
    "v_sub_u32 v166, v166, v165\n\t" \
    "v_cmp_ne_u32 vcc, 1, v166\n\t" \
    "s_cbranch_vccz gx_Cok2_%=\n\t" \
    "s_cmp_eq_u32 s68, 0\n\t" \
    "s_cbranch_scc1 gx_F3_%=\n\t" \
    "s_sub_u32 s68, s68, 1\n\t" \
    "s_sleep 4\n\t" \
    "s_branch gx_C1_%=\n\t" \
    "gx_F3_%=:\n\t" \
    "v_mov_b32 v168, 6095104\n\t" \
    "v_mov_b32 v169, 4\n\t" \
    "global_store_dword v168, v169, s[36:37] sc1\n\t" \
    "s_waitcnt vmcnt(0)\n\t" \
    "s_branch gx_Cok2_%=\n\t" \
    "gx_Cok2_%=:\n\t" \
    "s_cmp_lg_u32 s66, 0\n\t" \
    "s_cbranch_scc1 gx_Ca4_%=\n\t" \
    "v_mov_b32 v164, 6176768\n\t" \
    "s_mov_b64 exec, 1\n\t" \
    "global_atomic_add v164, v166, s[36:37] sc1\n\t" \
    "s_mov_b64 exec, -1\n\t" \
    "gx_Ca4_%=:\n\t" \
    "v_min_u32_dpp v167, v167, v167 row_shl:6 row_mask:0xf bank_mask:0xf\n\t" \
    "s_nop 0\n\t" \
    "v_readlane_b32 s69, v167, 4\n\t" \
    "v_readlane_b32 s70, v167, 5\n\t" \
    "s_nop 1\n\t" \
    "s_cmp_lt_u32 s70, s69\n\t" \
    "s_cselect_b32 s52, 8192, 0\n\t" \
    "s_xor_b32 s52, s52, 8192\n\t" \
    "s_cmp_lg_u32 s66, 0\n\t" \
    "s_cbranch_scc1 gx_SF5_%=\n\t" \
    "s_lshl_b32 s69, s67, 7\n\t" \
    "s_add_u32 s69, s69, 6160384\n\t" \
    "s_add_u32 s69, s69, s52\n\t" \
    "s_add_u32 s70, s65, 1\n\t" \
    "v_mov_b32 v156, s69\n\t" \
    "v_mov_b32 v157, s70\n\t" \
    "s_mov_b64 exec, 1\n\t" \
    "global_store_dword v156, v157, s[36:37] sc1\n\t" \
    "s_mov_b64 exec, -1\n\t" \
    "gx_SF5_%=:\n\t" \
    "ds_read_b64_tr_b16 a[32:33], v202 offset:49152\n\t" \
    "ds_read_b64_tr_b16 a[34:35], v202 offset:50176\n\t" \
    "ds_read_b64_tr_b16 a[36:37], v202 offset:57344\n\t" \
    "ds_read_b64_tr_b16 a[38:39], v202 offset:58368\n\t" \
    "ds_read_b64_tr_b16 a[104:105], v203 offset:49152\n\t" \
    "ds_read_b64_tr_b16 a[106:107], v203 offset:50176\n\t" \
    "ds_read_b64_tr_b16 a[108:109], v203 offset:57344\n\t" \
    "ds_read_b64_tr_b16 a[110:111], v203 offset:58368\n\t" \
    "s_waitcnt vmcnt(12) lgkmcnt(0)\n\t" \
    "s_barrier\n\t" \
    "v_add_u32 v202, 65536, v202\n\t" \
    "v_add_u32 v203, 65536, v203\n\t" \
    "ds_read_b64_tr_b16 a[40:41], v202 offset:0\n\t" \
    "ds_read_b64_tr_b16 a[42:43], v202 offset:1024\n\t" \
    "ds_read_b64_tr_b16 a[44:45], v202 offset:8192\n\t" \
    "ds_read_b64_tr_b16 a[46:47], v202 offset:9216\n\t" \
    "ds_read_b64_tr_b16 a[112:113], v203 offset:0\n\t" \
    "ds_read_b64_tr_b16 a[114:115], v203 offset:1024\n\t" \
    "ds_read_b64_tr_b16 a[116:117], v203 offset:8192\n\t" \
    "ds_read_b64_tr_b16 a[118:119], v203 offset:9216\n\t" \
    "s_waitcnt vmcnt(8) lgkmcnt(0)\n\t" \
    "s_barrier\n\t" \
    "v_add_u32 v202, -65536, v202\n\t" \
    "v_add_u32 v203, -65536, v203\n\t" \
    "ds_read_b64_tr_b16 a[48:49], v202 offset:0\n\t" \
    "ds_read_b64_tr_b16 a[50:51], v202 offset:1024\n\t" \
    "ds_read_b64_tr_b16 a[52:53], v202 offset:8192\n\t" \
    "ds_read_b64_tr_b16 a[54:55], v202 offset:9216\n\t" \
    "ds_read_b64_tr_b16 a[120:121], v203 offset:0\n\t" \
    "ds_read_b64_tr_b16 a[122:123], v203 offset:1024\n\t" \
    "ds_read_b64_tr_b16 a[124:125], v203 offset:8192\n\t" \
    "ds_read_b64_tr_b16 a[126:127], v203 offset:9216\n\t" \
    "s_waitcnt vmcnt(4) lgkmcnt(0)\n\t" \
    "s_barrier\n\t" \
    "ds_read_b64_tr_b16 a[56:57], v202 offset:16384\n\t" \
    "ds_read_b64_tr_b16 a[58:59], v202 offset:17408\n\t" \
    "ds_read_b64_tr_b16 a[60:61], v202 offset:24576\n\t" \
    "ds_read_b64_tr_b16 a[62:63], v202 offset:25600\n\t" \
    "ds_read_b64_tr_b16 a[128:129], v203 offset:16384\n\t" \
    "ds_read_b64_tr_b16 a[130:131], v203 offset:17408\n\t" \
    "ds_read_b64_tr_b16 a[132:133], v203 offset:24576\n\t" \
    "ds_read_b64_tr_b16 a[134:135], v203 offset:25600\n\t" \
    "s_waitcnt vmcnt(0) lgkmcnt(0)\n\t" \
    "s_barrier\n\t" \
    "ds_read_b64_tr_b16 a[64:65], v202 offset:32768\n\t" \
    "ds_read_b64_tr_b16 a[66:67], v202 offset:33792\n\t" \
    "ds_read_b64_tr_b16 a[68:69], v202 offset:40960\n\t" \
    "ds_read_b64_tr_b16 a[70:71], v202 offset:41984\n\t" \
    "ds_read_b64_tr_b16 a[136:137], v203 offset:32768\n\t" \
    "ds_read_b64_tr_b16 a[138:139], v203 offset:33792\n\t" \
    "ds_read_b64_tr_b16 a[140:141], v203 offset:40960\n\t" \
    "ds_read_b64_tr_b16 a[142:143], v203 offset:41984\n\t" \
    "s_waitcnt lgkmcnt(0)\n\t" \
    "s_barrier\n\t" \
    "ds_read_b128 v[24:27], v2 offset:0\n\t" \
    "ds_read_b128 v[28:31], v2 offset:18432\n\t" \
    "ds_read_b128 v[32:35], v2 offset:1024\n\t" \
    "ds_read_b128 v[36:39], v2 offset:19456\n\t" \
    "ds_read_b128 v[40:43], v2 offset:2048\n\t" \
    "ds_read_b128 v[44:47], v2 offset:20480\n\t" \
    "s_cmp_lt_u32 s65, 62\n\t" \
    "s_cselect_b32 s69, 36864, 0\n\t" \
    "s_add_u32 s50, s50, s69\n\t" \
    "s_addc_u32 s51, s51, 0\n\t" \
    "s_add_u32 s90, s90, 36864\n\t" \
    "s_cmp_eq_u32 s90, 147456\n\t" \
    "s_cselect_b32 s90, 0, s90\n\t" \
    "s_add_u32 s91, s90, 36864\n\t" \
    "s_cmp_eq_u32 s91, 147456\n\t" \
    "s_cselect_b32 s91, 0, s91\n\t" \
    "s_add_u32 s91, s91, s89\n\t" \
    "v_add_u32 v17, s90, v3\n\t" \
    "s_branch gx_mid8_%=\n\t" \
    ".p2align 6\n\t" \
    "s_nop 0\n\t" \
    "s_nop 0\n\t" \
    "gx_loop6_%=:\n\t" \
    "s_cmp_lt_u32 s65, 62\n\t" \
    "s_cselect_b32 s69, 36864, 0\n\t" \
    "s_add_u32 s50, s50, s69\n\t" \
    "s_addc_u32 s51, s51, 0\n\t" \
    "s_add_u32 s90, s90, 36864\n\t" \
    "s_cmp_eq_u32 s90, 147456\n\t" \
    "s_cselect_b32 s90, 0, s90\n\t" \
    "s_add_u32 s91, s90, 36864\n\t" \
    "s_cmp_eq_u32 s91, 147456\n\t" \
    "s_cselect_b32 s91, 0, s91\n\t" \
    "s_add_u32 s91, s91, s89\n\t" \
    "v_add_u32 v17, s90, v3\n\t" \
    "s_waitcnt lgkmcnt(5)\n\t" \
    "v_mfma_f32_16x16x32_bf16 v[88:91], a[144:147], v[24:27], 0\n\t" \
    "ds_read_b128 v[48:51], v2 offset:3072\n\t" \
    "s_waitcnt lgkmcnt(5)\n\t" \
    "v_mfma_f32_16x16x32_bf16 v[92:95], a[144:147], v[28:31], 0\n\t" \
    "ds_read_b128 v[52:55], v2 offset:21504\n\t" \
    "v_mfma_f32_16x16x32_bf16 v[96:99], a[216:219], v[24:27], 0\n\t" \
    "v_mfma_f32_16x16x32_bf16 v[100:103], a[216:219], v[28:31], 0\n\t" \
    "s_waitcnt lgkmcnt(5)\n\t" \
    "v_mfma_f32_16x16x32_bf16 v[88:91], a[148:151], v[32:35], v[88:91]\n\t" \
    "ds_read_b128 v[56:59], v2 offset:4096\n\t" \
    "s_waitcnt lgkmcnt(5)\n\t" \
    "v_mfma_f32_16x16x32_bf16 v[92:95], a[148:151], v[36:39], v[92:95]\n\t" \
    "ds_read_b128 v[60:63], v2 offset:22528\n\t" \
    "v_mfma_f32_16x16x32_bf16 v[96:99], a[220:223], v[32:35], v[96:99]\n\t" \
    "v_mfma_f32_16x16x32_bf16 v[100:103], a[220:223], v[36:39], v[100:103]\n\t" \
    "s_waitcnt lgkmcnt(5)\n\t" \
    "v_mfma_f32_16x16x32_bf16 v[88:91], a[152:155], v[40:43], v[88:91]\n\t" \
    "ds_read_b128 v[64:67], v2 offset:5120\n\t" \
    "s_waitcnt lgkmcnt(5)\n\t" \
    "v_mfma_f32_16x16x32_bf16 v[92:95], a[152:155], v[44:47], v[92:95]\n\t" \
    "ds_read_b128 v[68:71], v2 offset:23552\n\t" \
    "v_mfma_f32_16x16x32_bf16 v[96:99], a[224:227], v[40:43], v[96:99]\n\t" \
    "v_mfma_f32_16x16x32_bf16 v[100:103], a[224:227], v[44:47], v[100:103]\n\t" \
    "s_waitcnt vmcnt(0)\n\t" \
    "s_barrier\n\t" \
    "s_cmp_lg_u32 s66, 0\n\t" \
    "s_cbranch_scc1 gx_SF9_%=\n\t" \
    "s_lshl_b32 s69, s67, 7\n\t" \
    "s_add_u32 s69, s69, 6098944\n\t" \
    "s_add_u32 s69, s69, s52\n\t" \
    "s_add_u32 s70, s65, -1\n\t" \
    "v_mov_b32 v156, s69\n\t" \
    "v_mov_b32 v157, s70\n\t" \
    "s_add_u32 s69, s69, 61440\n\t" \
    "s_max_i32 s70, s70, 1\n\t" \
    "v_writelane_b32 v156, s69, 1\n\t" \
    "v_writelane_b32 v157, s70, 1\n\t" \
    "s_mov_b64 exec, 3\n\t" \
    "global_store_dword v156, v157, s[36:37] sc1\n\t" \
    "s_mov_b64 exec, -1\n\t" \
    "gx_SF9_%=:\n\t" \
    "s_waitcnt lgkmcnt(5)\n\t" \
    "v_mfma_f32_16x16x32_bf16 v[88:91], a[156:159], v[48:51], v[88:91]\n\t" \
    "ds_read_b128 v[24:27], v2 offset:6144\n\t" \
    "s_waitcnt lgkmcnt(5)\n\t" \
    "v_mfma_f32_16x16x32_bf16 v[92:95], a[156:159], v[52:55], v[92:95]\n\t" \
    "ds_read_b128 v[28:31], v2 offset:24576\n\t" \
    "v_mfma_f32_16x16x32_bf16 v[96:99], a[228:231], v[48:51], v[96:99]\n\t" \
    "v_mfma_f32_16x16x32_bf16 v[100:103], a[228:231], v[52:55], v[100:103]\n\t" \
    "s_waitcnt lgkmcnt(5)\n\t" \
    "v_mfma_f32_16x16x32_bf16 v[88:91], a[160:163], v[56:59], v[88:91]\n\t" \
    "ds_read_b128 v[32:35], v2 offset:7168\n\t" \
    "s_waitcnt lgkmcnt(5)\n\t" \
    "v_mfma_f32_16x16x32_bf16 v[92:95], a[160:163], v[60:63], v[92:95]\n\t" \
    "ds_read_b128 v[36:39], v2 offset:25600\n\t" \
    "v_mfma_f32_16x16x32_bf16 v[96:99], a[232:235], v[56:59], v[96:99]\n\t" \
    "v_cvt_pk_bf16_f32 v104, v72, v73\n\t" \
    "v_mfma_f32_16x16x32_bf16 v[100:103], a[232:235], v[60:63], v[100:103]\n\t" \
    "v_cvt_pk_bf16_f32 v105, v74, v75\n\t" \
    "s_waitcnt lgkmcnt(5)\n\t" \
    "v_mfma_f32_16x16x32_bf16 v[88:91], a[164:167], v[64:67], v[88:91]\n\t" \
    "ds_read_b128 v[40:43], v2 offset:8192\n\t" \
    "s_waitcnt lgkmcnt(5)\n\t" \
    "v_mfma_f32_16x16x32_bf16 v[92:95], a[164:167], v[68:71], v[92:95]\n\t" \
    "ds_read_b128 v[44:47], v2 offset:26624\n\t" \
    "v_mfma_f32_16x16x32_bf16 v[96:99], a[236:239], v[64:67], v[96:99]\n\t" \
    "v_cvt_pk_bf16_f32 v106, v80, v81\n\t" \
    "v_mfma_f32_16x16x32_bf16 v[100:103], a[236:239], v[68:71], v[100:103]\n\t" \
    "v_cvt_pk_bf16_f32 v107, v82, v83\n\t" \
    "global_store_dwordx4 v8, v[104:107], s[38:39] offset:0 sc1\n\t" \
    "s_waitcnt lgkmcnt(5)\n\t" \
    "v_mfma_f32_16x16x32_bf16 v[88:91], a[168:171], v[24:27], v[88:91]\n\t" \
    "ds_read_b128 v[48:51], v2 offset:9216\n\t" \
    "s_waitcnt lgkmcnt(5)\n\t" \
    "v_mfma_f32_16x16x32_bf16 v[92:95], a[168:171], v[28:31], v[92:95]\n\t" \
    "ds_read_b128 v[52:55], v2 offset:27648\n\t" \
    "v_mfma_f32_16x16x32_bf16 v[96:99], a[240:243], v[24:27], v[96:99]\n\t" \
    "v_cvt_pk_bf16_f32 v108, v76, v77\n\t" \
    "v_mfma_f32_16x16x32_bf16 v[100:103], a[240:243], v[28:31], v[100:103]\n\t" \
    "v_cvt_pk_bf16_f32 v109, v78, v79\n\t" \
    "s_waitcnt lgkmcnt(5)\n\t" \
    "v_mfma_f32_16x16x32_bf16 v[88:91], a[172:175], v[32:35], v[88:91]\n\t" \
    "ds_read_b128 v[56:59], v2 offset:10240\n\t" \
    "s_waitcnt lgkmcnt(5)\n\t" \
    "v_mfma_f32_16x16x32_bf16 v[92:95], a[172:175], v[36:39], v[92:95]\n\t" \
    "ds_read_b128 v[60:63], v2 offset:28672\n\t" \
    "v_mfma_f32_16x16x32_bf16 v[96:99], a[244:247], v[32:35], v[96:99]\n\t" \
    "v_cvt_pk_bf16_f32 v110, v84, v85\n\t" \
    "v_mfma_f32_16x16x32_bf16 v[100:103], a[244:247], v[36:39], v[100:103]\n\t" \
    "v_cvt_pk_bf16_f32 v111, v86, v87\n\t" \
    "global_store_dwordx4 v8, v[108:111], s[38:39] offset:1024 sc1\n\t" \
    "s_waitcnt lgkmcnt(5)\n\t" \
    "v_mfma_f32_16x16x32_bf16 v[88:91], a[176:179], v[40:43], v[88:91]\n\t" \
    "ds_read_b128 v[64:67], v2 offset:11264\n\t" \
    "s_waitcnt lgkmcnt(5)\n\t" \
    "v_mfma_f32_16x16x32_bf16 v[92:95], a[176:179], v[44:47], v[92:95]\n\t" \
    "ds_read_b128 v[68:71], v2 offset:29696\n\t" \
    "v_mfma_f32_16x16x32_bf16 v[96:99], a[248:251], v[40:43], v[96:99]\n\t" \
    "v_mfma_f32_16x16x32_bf16 v[100:103], a[248:251], v[44:47], v[100:103]\n\t" \
    "s_waitcnt lgkmcnt(5)\n\t" \
    "v_mfma_f32_16x16x32_bf16 v[88:91], a[180:183], v[48:51], v[88:91]\n\t" \
    "ds_read_b128 v[24:27], v2 offset:12288\n\t" \
    "s_waitcnt lgkmcnt(5)\n\t" \
    "v_mfma_f32_16x16x32_bf16 v[92:95], a[180:183], v[52:55], v[92:95]\n\t" \
    "ds_read_b128 v[28:31], v2 offset:30720\n\t" \
    "v_mfma_f32_16x16x32_bf16 v[96:99], a[252:255], v[48:51], v[96:99]\n\t" \
    "s_add_u32 m0, s91, 0\n\t" \
    "v_mfma_f32_16x16x32_bf16 v[100:103], a[252:255], v[52:55], v[100:103]\n\t" \
    "global_load_lds_dwordx4 v1, s[50:51] offset:0\n\t" \
    "s_waitcnt lgkmcnt(5)\n\t" \
    "v_mfma_f32_16x16x32_bf16 v[88:91], a[184:187], v[56:59], v[88:91]\n\t" \
    "ds_read_b128 v[32:35], v2 offset:13312\n\t" \
    "s_waitcnt lgkmcnt(5)\n\t" \
    "v_mfma_f32_16x16x32_bf16 v[92:95], a[184:187], v[60:63], v[92:95]\n\t" \
    "ds_read_b128 v[36:39], v2 offset:31744\n\t" \
    "v_mfma_f32_16x16x32_bf16 v[96:99], v[224:227], v[56:59], v[96:99]\n\t" \
    "s_add_u32 m0, s91, 960\n\t" \
    "v_mfma_f32_16x16x32_bf16 v[100:103], v[224:227], v[60:63], v[100:103]\n\t" \
    "global_load_lds_dwordx4 v1, s[50:51] offset:64\n\t" \
    "s_waitcnt lgkmcnt(5)\n\t" \
    "v_mfma_f32_16x16x32_bf16 v[88:91], a[188:191], v[64:67], v[88:91]\n\t" \
    "ds_read_b128 v[40:43], v2 offset:14336\n\t" \
    "s_waitcnt lgkmcnt(5)\n\t" \
    "v_mfma_f32_16x16x32_bf16 v[92:95], a[188:191], v[68:71], v[92:95]\n\t" \
    "ds_read_b128 v[44:47], v2 offset:32768\n\t" \
    "v_mfma_f32_16x16x32_bf16 v[96:99], v[228:231], v[64:67], v[96:99]\n\t" \
    "s_add_u32 m0, s91, 1920\n\t" \
    "v_mfma_f32_16x16x32_bf16 v[100:103], v[228:231], v[68:71], v[100:103]\n\t" \
    "global_load_lds_dwordx4 v1, s[50:51] offset:128\n\t" \
    "s_waitcnt lgkmcnt(5)\n\t" \
    "v_mfma_f32_16x16x32_bf16 v[88:91], a[192:195], v[24:27], v[88:91]\n\t" \
    "ds_read_b128 v[48:51], v2 offset:15360\n\t" \
    "s_waitcnt lgkmcnt(5)\n\t" \
    "v_mfma_f32_16x16x32_bf16 v[92:95], a[192:195], v[28:31], v[92:95]\n\t" \
    "ds_read_b128 v[52:55], v2 offset:33792\n\t" \
    "v_mfma_f32_16x16x32_bf16 v[96:99], v[232:235], v[24:27], v[96:99]\n\t" \
    "s_add_u32 m0, s91, 2880\n\t" \
    "v_mfma_f32_16x16x32_bf16 v[100:103], v[232:235], v[28:31], v[100:103]\n\t" \
    "global_load_lds_dwordx4 v1, s[50:51] offset:192\n\t" \
    "s_waitcnt lgkmcnt(5)\n\t" \
    "v_mfma_f32_16x16x32_bf16 v[88:91], a[196:199], v[32:35], v[88:91]\n\t" \
    "ds_read_b128 v[56:59], v2 offset:16384\n\t" \
    "s_waitcnt lgkmcnt(5)\n\t" \
    "v_mfma_f32_16x16x32_bf16 v[92:95], a[196:199], v[36:39], v[92:95]\n\t" \
    "ds_read_b128 v[60:63], v2 offset:34816\n\t" \
    "v_mfma_f32_16x16x32_bf16 v[96:99], v[236:239], v[32:35], v[96:99]\n\t" \
    "s_add_u32 m0, s91, 3840\n\t" \
    "v_mfma_f32_16x16x32_bf16 v[100:103], v[236:239], v[36:39], v[100:103]\n\t" \
    "global_load_lds_dwordx4 v1, s[50:51] offset:256\n\t" \
    "s_waitcnt lgkmcnt(5)\n\t" \
    "v_mfma_f32_16x16x32_bf16 v[88:91], a[200:203], v[40:43], v[88:91]\n\t" \
    "ds_read_b128 v[64:67], v2 offset:17408\n\t" \
    "s_waitcnt lgkmcnt(5)\n\t" \
    "v_mfma_f32_16x16x32_bf16 v[92:95], a[200:203], v[44:47], v[92:95]\n\t" \
    "ds_read_b128 v[68:71], v2 offset:35840\n\t" \
    "v_mfma_f32_16x16x32_bf16 v[96:99], v[240:243], v[40:43], v[96:99]\n\t" \
    "s_add_u32 m0, s91, 4800\n\t" \
    "v_mfma_f32_16x16x32_bf16 v[100:103], v[240:243], v[44:47], v[100:103]\n\t" \
    "global_load_lds_dwordx4 v1, s[50:51] offset:320\n\t" \
    "s_waitcnt lgkmcnt(5)\n\t" \
    "v_mfma_f32_16x16x32_bf16 v[88:91], a[204:207], v[48:51], v[88:91]\n\t" \
    "ds_read_b128 v[24:27], v2 offset:0\n\t" \
    "s_waitcnt lgkmcnt(5)\n\t" \
    "v_mfma_f32_16x16x32_bf16 v[92:95], a[204:207], v[52:55], v[92:95]\n\t" \
    "ds_read_b128 v[28:31], v2 offset:18432\n\t" \
    "v_mfma_f32_16x16x32_bf16 v[96:99], v[244:247], v[48:51], v[96:99]\n\t" \
    "s_add_u32 m0, s91, 5760\n\t" \
    "v_mfma_f32_16x16x32_bf16 v[100:103], v[244:247], v[52:55], v[100:103]\n\t" \
    "global_load_lds_dwordx4 v1, s[50:51] offset:384\n\t" \
    "s_waitcnt lgkmcnt(5)\n\t" \
    "v_mfma_f32_16x16x32_bf16 v[88:91], a[208:211], v[56:59], v[88:91]\n\t" \
    "ds_read_b128 v[32:35], v2 offset:1024\n\t" \
    "s_waitcnt lgkmcnt(5)\n\t" \
    "v_mfma_f32_16x16x32_bf16 v[92:95], a[208:211], v[60:63], v[92:95]\n\t" \
    "ds_read_b128 v[36:39], v2 offset:19456\n\t" \
    "v_mfma_f32_16x16x32_bf16 v[96:99], v[248:251], v[56:59], v[96:99]\n\t" \
    "s_add_u32 m0, s91, 6720\n\t" \
    "v_mfma_f32_16x16x32_bf16 v[100:103], v[248:251], v[60:63], v[100:103]\n\t" \
    "global_load_lds_dwordx4 v1, s[50:51] offset:448\n\t" \
    "s_waitcnt lgkmcnt(5)\n\t" \
    "v_mfma_f32_16x16x32_bf16 v[88:91], a[212:215], v[64:67], v[88:91]\n\t" \
    "ds_read_b128 v[40:43], v2 offset:2048\n\t" \
    "s_waitcnt lgkmcnt(5)\n\t" \
    "v_mfma_f32_16x16x32_bf16 v[92:95], a[212:215], v[68:71], v[92:95]\n\t" \
    "ds_read_b128 v[44:47], v2 offset:20480\n\t" \
    "v_mfma_f32_16x16x32_bf16 v[96:99], v[252:255], v[64:67], v[96:99]\n\t" \
    "s_add_u32 m0, s91, 7680\n\t" \
    "v_mfma_f32_16x16x32_bf16 v[100:103], v[252:255], v[68:71], v[100:103]\n\t" \
    "global_load_lds_dwordx4 v1, s[50:51] offset:512\n\t" \
    "gx_mid8_%=:\n\t" \
    "s_waitcnt lgkmcnt(5)\n\t" \
    "v_mfma_f32_16x16x32_bf16 v[72:75], a[0:3], v[24:27], 0\n\t" \
    "ds_read_b128 v[48:51], v2 offset:3072\n\t" \
    "s_waitcnt lgkmcnt(5)\n\t" \
    "v_mfma_f32_16x16x32_bf16 v[76:79], a[0:3], v[28:31], 0\n\t" \
    "ds_read_b128 v[52:55], v2 offset:21504\n\t" \
    "v_mfma_f32_16x16x32_bf16 v[80:83], a[72:75], v[24:27], 0\n\t" \
    "s_mul_i32 s70, s67, 7\n\t" \
    "s_add_u32 s69, s65, 0\n\t" \
    "s_xor_b32 s69, s69, s70\n\t" \
    "s_and_b32 s69, s69, 63\n\t" \
    "s_lshl_b32 s69, s69, 13\n\t" \
    "s_mul_i32 s70, s67, 524288\n\t" \
    "s_add_u32 s69, s69, s70\n\t" \
    "s_add_u32 s69, s69, 6291456\n\t" \
    "s_add_u32 s38, s36, s69\n\t" \
    "s_addc_u32 s39, s37, 0\n\t" \
    "v_mfma_f32_16x16x32_bf16 v[84:87], a[72:75], v[28:31], 0\n\t" \
    "s_add_u32 s40, s38, 16777216\n\t" \
    "s_addc_u32 s41, s39, 0\n\t" \
    "s_waitcnt lgkmcnt(5)\n\t" \
    "v_mfma_f32_16x16x32_bf16 v[72:75], a[4:7], v[32:35], v[72:75]\n\t" \
    "ds_read_b128 v[56:59], v2 offset:4096\n\t" \
    "s_waitcnt lgkmcnt(5)\n\t" \
    "v_mfma_f32_16x16x32_bf16 v[76:79], a[4:7], v[36:39], v[76:79]\n\t" \
    "ds_read_b128 v[60:63], v2 offset:22528\n\t" \
    "v_mfma_f32_16x16x32_bf16 v[80:83], a[76:79], v[32:35], v[80:83]\n\t" \
    "v_mfma_f32_16x16x32_bf16 v[84:87], a[76:79], v[36:39], v[84:87]\n\t" \
    "s_waitcnt lgkmcnt(5)\n\t" \
    "v_mfma_f32_16x16x32_bf16 v[72:75], a[8:11], v[40:43], v[72:75]\n\t" \
    "ds_read_b128 v[64:67], v2 offset:5120\n\t" \
    "s_waitcnt lgkmcnt(5)\n\t" \
    "v_mfma_f32_16x16x32_bf16 v[76:79], a[8:11], v[44:47], v[76:79]\n\t" \
    "ds_read_b128 v[68:71], v2 offset:23552\n\t" \
    "v_mfma_f32_16x16x32_bf16 v[80:83], a[80:83], v[40:43], v[80:83]\n\t" \
    "v_cvt_pk_bf16_f32 v112, v88, v89\n\t" \
    "v_mfma_f32_16x16x32_bf16 v[84:87], a[80:83], v[44:47], v[84:87]\n\t" \
    "v_cvt_pk_bf16_f32 v113, v90, v91\n\t" \
    "s_waitcnt lgkmcnt(5)\n\t" \
    "v_mfma_f32_16x16x32_bf16 v[72:75], a[12:15], v[48:51], v[72:75]\n\t" \
    "ds_read_b128 v[24:27], v2 offset:6144\n\t" \
    "s_waitcnt lgkmcnt(5)\n\t" \
    "v_mfma_f32_16x16x32_bf16 v[76:79], a[12:15], v[52:55], v[76:79]\n\t" \
    "ds_read_b128 v[28:31], v2 offset:24576\n\t" \
    "v_mfma_f32_16x16x32_bf16 v[80:83], a[84:87], v[48:51], v[80:83]\n\t" \
    "v_cvt_pk_bf16_f32 v114, v96, v97\n\t" \
    "v_mfma_f32_16x16x32_bf16 v[84:87], a[84:87], v[52:55], v[84:87]\n\t" \
    "v_cvt_pk_bf16_f32 v115, v98, v99\n\t" \
    "global_store_dwordx4 v8, v[112:115], s[40:41] offset:0 sc1\n\t" \
    "s_waitcnt lgkmcnt(5)\n\t" \
    "v_mfma_f32_16x16x32_bf16 v[72:75], a[16:19], v[56:59], v[72:75]\n\t" \
    "ds_read_b128 v[32:35], v2 offset:7168\n\t" \
    "s_waitcnt lgkmcnt(5)\n\t" \
    "v_mfma_f32_16x16x32_bf16 v[76:79], a[16:19], v[60:63], v[76:79]\n\t" \
    "ds_read_b128 v[36:39], v2 offset:25600\n\t" \
    "v_mfma_f32_16x16x32_bf16 v[80:83], a[88:91], v[56:59], v[80:83]\n\t" \
    "v_cvt_pk_bf16_f32 v116, v92, v93\n\t" \
    "v_mfma_f32_16x16x32_bf16 v[84:87], a[88:91], v[60:63], v[84:87]\n\t" \
    "v_cvt_pk_bf16_f32 v117, v94, v95\n\t" \
    "s_waitcnt lgkmcnt(5)\n\t" \
    "v_mfma_f32_16x16x32_bf16 v[72:75], a[20:23], v[64:67], v[72:75]\n\t" \
    "ds_read_b128 v[40:43], v2 offset:8192\n\t" \
    "s_waitcnt lgkmcnt(5)\n\t" \
    "v_mfma_f32_16x16x32_bf16 v[76:79], a[20:23], v[68:71], v[76:79]\n\t" \
    "ds_read_b128 v[44:47], v2 offset:26624\n\t" \
    "v_mfma_f32_16x16x32_bf16 v[80:83], a[92:95], v[64:67], v[80:83]\n\t" \
    "v_cvt_pk_bf16_f32 v118, v100, v101\n\t" \
    "v_mfma_f32_16x16x32_bf16 v[84:87], a[92:95], v[68:71], v[84:87]\n\t" \
    "v_cvt_pk_bf16_f32 v119, v102, v103\n\t" \
    "global_store_dwordx4 v8, v[116:119], s[40:41] offset:1024 sc1\n\t" \
    "s_waitcnt lgkmcnt(5)\n\t" \
    "v_mfma_f32_16x16x32_bf16 v[72:75], a[24:27], v[24:27], v[72:75]\n\t" \
    "ds_read_b128 v[48:51], v2 offset:9216\n\t" \
    "s_waitcnt lgkmcnt(5)\n\t" \
    "v_mfma_f32_16x16x32_bf16 v[76:79], a[24:27], v[28:31], v[76:79]\n\t" \
    "ds_read_b128 v[52:55], v2 offset:27648\n\t" \
    "v_mfma_f32_16x16x32_bf16 v[80:83], a[96:99], v[24:27], v[80:83]\n\t" \
    "v_mfma_f32_16x16x32_bf16 v[84:87], a[96:99], v[28:31], v[84:87]\n\t" \
    "s_waitcnt lgkmcnt(5)\n\t" \
    "v_mfma_f32_16x16x32_bf16 v[72:75], a[28:31], v[32:35], v[72:75]\n\t" \
    "ds_read_b128 v[56:59], v2 offset:10240\n\t" \
    "s_waitcnt lgkmcnt(5)\n\t" \
    "v_mfma_f32_16x16x32_bf16 v[76:79], a[28:31], v[36:39], v[76:79]\n\t" \
    "ds_read_b128 v[60:63], v2 offset:28672\n\t" \
    "v_mfma_f32_16x16x32_bf16 v[80:83], a[100:103], v[32:35], v[80:83]\n\t" \
    "v_mfma_f32_16x16x32_bf16 v[84:87], a[100:103], v[36:39], v[84:87]\n\t" \
    "s_waitcnt lgkmcnt(5)\n\t" \
    "v_mfma_f32_16x16x32_bf16 v[72:75], a[32:35], v[40:43], v[72:75]\n\t" \
    "ds_read_b128 v[64:67], v2 offset:11264\n\t" \
    "s_waitcnt lgkmcnt(5)\n\t" \
    "v_mfma_f32_16x16x32_bf16 v[76:79], a[32:35], v[44:47], v[76:79]\n\t" \
    "ds_read_b128 v[68:71], v2 offset:29696\n\t" \
    "v_mfma_f32_16x16x32_bf16 v[80:83], a[104:107], v[40:43], v[80:83]\n\t" \
    "v_mfma_f32_16x16x32_bf16 v[84:87], a[104:107], v[44:47], v[84:87]\n\t" \
    "s_waitcnt lgkmcnt(5)\n\t" \
    "v_mfma_f32_16x16x32_bf16 v[72:75], a[36:39], v[48:51], v[72:75]\n\t" \
    "ds_read_b128 v[24:27], v2 offset:12288\n\t" \
    "s_waitcnt lgkmcnt(5)\n\t" \
    "v_mfma_f32_16x16x32_bf16 v[76:79], a[36:39], v[52:55], v[76:79]\n\t" \
    "ds_read_b128 v[28:31], v2 offset:30720\n\t" \
    "v_mfma_f32_16x16x32_bf16 v[80:83], a[108:111], v[48:51], v[80:83]\n\t" \
    "v_mfma_f32_16x16x32_bf16 v[84:87], a[108:111], v[52:55], v[84:87]\n\t" \
    "s_waitcnt lgkmcnt(5)\n\t" \
    "v_mfma_f32_16x16x32_bf16 v[72:75], a[40:43], v[56:59], v[72:75]\n\t" \
    "ds_read_b128 v[32:35], v2 offset:13312\n\t" \
    "s_waitcnt lgkmcnt(5)\n\t" \
    "v_mfma_f32_16x16x32_bf16 v[76:79], a[40:43], v[60:63], v[76:79]\n\t" \
    "ds_read_b128 v[36:39], v2 offset:31744\n\t" \
    "v_mfma_f32_16x16x32_bf16 v[80:83], a[112:115], v[56:59], v[80:83]\n\t" \
    "v_mfma_f32_16x16x32_bf16 v[84:87], a[112:115], v[60:63], v[84:87]\n\t" \
    "s_waitcnt lgkmcnt(5)\n\t" \
    "v_mfma_f32_16x16x32_bf16 v[72:75], a[44:47], v[64:67], v[72:75]\n\t" \
    "ds_read_b128 v[40:43], v2 offset:14336\n\t" \
    "s_waitcnt lgkmcnt(5)\n\t" \
    "v_mfma_f32_16x16x32_bf16 v[76:79], a[44:47], v[68:71], v[76:79]\n\t" \
    "ds_read_b128 v[44:47], v2 offset:32768\n\t" \
    "v_mfma_f32_16x16x32_bf16 v[80:83], a[116:119], v[64:67], v[80:83]\n\t" \
    "v_mfma_f32_16x16x32_bf16 v[84:87], a[116:119], v[68:71], v[84:87]\n\t" \
    "s_waitcnt lgkmcnt(5)\n\t" \
    "v_mfma_f32_16x16x32_bf16 v[72:75], a[48:51], v[24:27], v[72:75]\n\t" \
    "ds_read_b128 v[48:51], v2 offset:15360\n\t" \
    "s_waitcnt lgkmcnt(5)\n\t" \
    "v_mfma_f32_16x16x32_bf16 v[76:79], a[48:51], v[28:31], v[76:79]\n\t" \
    "ds_read_b128 v[52:55], v2 offset:33792\n\t" \
    "v_mfma_f32_16x16x32_bf16 v[80:83], a[120:123], v[24:27], v[80:83]\n\t" \
    "v_mfma_f32_16x16x32_bf16 v[84:87], a[120:123], v[28:31], v[84:87]\n\t" \
    "s_waitcnt lgkmcnt(5)\n\t" \
    "v_mfma_f32_16x16x32_bf16 v[72:75], a[52:55], v[32:35], v[72:75]\n\t" \
    "ds_read_b128 v[56:59], v2 offset:16384\n\t" \
    "s_waitcnt lgkmcnt(5)\n\t" \
    "v_mfma_f32_16x16x32_bf16 v[76:79], a[52:55], v[36:39], v[76:79]\n\t" \
    "ds_read_b128 v[60:63], v2 offset:34816\n\t" \
    "v_mfma_f32_16x16x32_bf16 v[80:83], a[124:127], v[32:35], v[80:83]\n\t" \
    "v_mfma_f32_16x16x32_bf16 v[84:87], a[124:127], v[36:39], v[84:87]\n\t" \
    "s_cmp_lg_u32 s65, 0\n\t" \
    "s_cbranch_scc1 gx_g110_%=\n\t" \
    "s_add_u32 m0, s91, 0\n\t" \
    "s_nop 0\n\t" \
    "global_load_lds_dwordx4 v1, s[50:51] offset:0\n\t" \
    "s_add_u32 m0, s91, 960\n\t" \
    "s_nop 0\n\t" \
    "global_load_lds_dwordx4 v1, s[50:51] offset:64\n\t" \
    "s_add_u32 m0, s91, 1920\n\t" \
    "s_nop 0\n\t" \
    "global_load_lds_dwordx4 v1, s[50:51] offset:128\n\t" \
    "s_add_u32 m0, s91, 2880\n\t" \
    "s_nop 0\n\t" \
    "global_load_lds_dwordx4 v1, s[50:51] offset:192\n\t" \
    "s_add_u32 m0, s91, 3840\n\t" \
    "s_nop 0\n\t" \
    "global_load_lds_dwordx4 v1, s[50:51] offset:256\n\t" \
    "s_add_u32 m0, s91, 4800\n\t" \
    "s_nop 0\n\t" \
    "global_load_lds_dwordx4 v1, s[50:51] offset:320\n\t" \
    "s_add_u32 m0, s91, 5760\n\t" \
    "s_nop 0\n\t" \
    "global_load_lds_dwordx4 v1, s[50:51] offset:384\n\t" \
    "s_add_u32 m0, s91, 6720\n\t" \
    "s_nop 0\n\t" \
    "global_load_lds_dwordx4 v1, s[50:51] offset:448\n\t" \
    "s_add_u32 m0, s91, 7680\n\t" \
    "s_nop 0\n\t" \
    "global_load_lds_dwordx4 v1, s[50:51] offset:512\n\t" \
    "gx_g110_%=:\n\t" \
    "s_waitcnt lgkmcnt(5)\n\t" \
    "v_mfma_f32_16x16x32_bf16 v[72:75], a[56:59], v[40:43], v[72:75]\n\t" \
    "ds_read_b128 v[64:67], v2 offset:17408\n\t" \
    "s_waitcnt lgkmcnt(5)\n\t" \
    "v_mfma_f32_16x16x32_bf16 v[76:79], a[56:59], v[44:47], v[76:79]\n\t" \
    "ds_read_b128 v[68:71], v2 offset:35840\n\t" \
    "v_mfma_f32_16x16x32_bf16 v[80:83], a[128:131], v[40:43], v[80:83]\n\t" \
    "v_mfma_f32_16x16x32_bf16 v[84:87], a[128:131], v[44:47], v[84:87]\n\t" \
    "s_waitcnt lgkmcnt(5)\n\t" \
    "v_mfma_f32_16x16x32_bf16 v[72:75], a[60:63], v[48:51], v[72:75]\n\t" \
    "ds_read_b128 v[24:27], v17 offset:0\n\t" \
    "s_waitcnt lgkmcnt(5)\n\t" \
    "v_mfma_f32_16x16x32_bf16 v[76:79], a[60:63], v[52:55], v[76:79]\n\t" \
    "ds_read_b128 v[28:31], v17 offset:18432\n\t" \
    "v_mfma_f32_16x16x32_bf16 v[80:83], a[132:135], v[48:51], v[80:83]\n\t" \
    "v_mfma_f32_16x16x32_bf16 v[84:87], a[132:135], v[52:55], v[84:87]\n\t" \
    "s_waitcnt lgkmcnt(5)\n\t" \
    "v_mfma_f32_16x16x32_bf16 v[72:75], a[64:67], v[56:59], v[72:75]\n\t" \
    "ds_read_b128 v[32:35], v17 offset:1024\n\t" \
    "s_waitcnt lgkmcnt(5)\n\t" \
    "v_mfma_f32_16x16x32_bf16 v[76:79], a[64:67], v[60:63], v[76:79]\n\t" \
    "ds_read_b128 v[36:39], v17 offset:19456\n\t" \
    "v_mfma_f32_16x16x32_bf16 v[80:83], a[136:139], v[56:59], v[80:83]\n\t" \
    "v_mfma_f32_16x16x32_bf16 v[84:87], a[136:139], v[60:63], v[84:87]\n\t" \
    "s_waitcnt lgkmcnt(5)\n\t" \
    "v_mfma_f32_16x16x32_bf16 v[72:75], a[68:71], v[64:67], v[72:75]\n\t" \
    "ds_read_b128 v[40:43], v17 offset:2048\n\t" \
    "s_waitcnt lgkmcnt(5)\n\t" \
    "v_mfma_f32_16x16x32_bf16 v[76:79], a[68:71], v[68:71], v[76:79]\n\t" \
    "ds_read_b128 v[44:47], v17 offset:20480\n\t" \
    "v_mfma_f32_16x16x32_bf16 v[80:83], a[140:143], v[64:67], v[80:83]\n\t" \
    "v_mfma_f32_16x16x32_bf16 v[84:87], a[140:143], v[68:71], v[84:87]\n\t" \
    "v_mov_b32 v2, v17\n\t" \
    "s_add_u32 s65, s65, 1\n\t" \
    "s_cmp_lt_u32 s65, 64\n\t" \
    "s_cbranch_scc1 gx_loop6_%=\n\t" \
    "s_nop 7\n\t" \
    "v_cvt_pk_bf16_f32 v104, v72, v73\n\t" \
    "v_cvt_pk_bf16_f32 v105, v74, v75\n\t" \
    "v_cvt_pk_bf16_f32 v106, v80, v81\n\t" \
    "v_cvt_pk_bf16_f32 v107, v82, v83\n\t" \
    "global_store_dwordx4 v8, v[104:107], s[38:39] offset:0 sc1\n\t" \
    "v_cvt_pk_bf16_f32 v108, v76, v77\n\t" \
    "v_cvt_pk_bf16_f32 v109, v78, v79\n\t" \
    "v_cvt_pk_bf16_f32 v110, v84, v85\n\t" \
    "v_cvt_pk_bf16_f32 v111, v86, v87\n\t" \
    "global_store_dwordx4 v8, v[108:111], s[38:39] offset:1024 sc1\n\t" \
    "s_waitcnt vmcnt(0) lgkmcnt(0)\n\t" \
    "s_barrier\n\t" \
    "s_cmp_lg_u32 s66, 0\n\t" \
    "s_cbranch_scc1 gx_SF11_%=\n\t" \
    "s_lshl_b32 s69, s67, 7\n\t" \
    "s_add_u32 s69, s69, 6098944\n\t" \
    "s_add_u32 s69, s69, s52\n\t" \
    "s_add_u32 s70, s65, 0\n\t" \
    "v_mov_b32 v156, s69\n\t" \
    "v_mov_b32 v157, s70\n\t" \
    "s_add_u32 s69, s69, 61440\n\t" \
    "s_max_i32 s70, s70, 1\n\t" \
    "v_writelane_b32 v156, s69, 1\n\t" \
    "v_writelane_b32 v157, s70, 1\n\t" \
    "s_mov_b64 exec, 3\n\t" \
    "global_store_dword v156, v157, s[36:37] sc1\n\t" \
    "s_mov_b64 exec, -1\n\t" \
    "gx_SF11_%=:\n\t" \
    "s_waitcnt vmcnt(0)\n\t" \
    "s_branch ea_done_%=\n\t" \
    "gv_begin_%=:\n\t" \
    "v_mbcnt_lo_u32_b32 v223, -1, 0\n\t" \
    "v_mbcnt_hi_u32_b32 v223, -1, v223\n\t" \
    "s_mov_b32 s68, 262144\n\t" \
    "s_mov_b32 s72, 0\n\t" \
    "s_mov_b32 s83, -1\n\t" \
    "s_mov_b32 s65, 0\n\t" \
    "v_and_b32 v180, 3, v223\n\t" \
    "v_lshlrev_b32 v202, 3, v180\n\t" \
    "v_lshrrev_b32 v181, 5, v223\n\t" \
    "v_lshl_add_u32 v202, v181, 12, v202\n\t" \
    "v_bfe_u32 v182, v223, 2, 2\n\t" \
    "s_mov_b32 s70, 288\n\t" \
    "v_mad_u32_u24 v202, v182, s70, v202\n\t" \
    "v_bfe_u32 v183, v223, 4, 1\n\t" \
    "s_mov_b32 s70, 2176\n\t" \
    "v_mad_u32_u24 v202, v183, s70, v202\n\t" \
    "s_lshl_b32 s69, s66, 6\n\t" \
    "v_xor_b32 v202, s69, v202\n\t" \
    "v_xor_b32 v203, 32, v202\n\t" \
    "s_mul_i32 s69, s66, 9216\n\t" \
    "v_and_b32 v184, 1, v223\n\t" \
    "v_lshl_add_u32 v201, v184, 4, s69\n\t" \
    "v_lshrrev_b32 v185, 3, v223\n\t" \
    "s_mov_b32 s70, 1152\n\t" \
    "v_mad_u32_u24 v201, v185, s70, v201\n\t" \
    "v_bfe_u32 v186, v223, 1, 2\n\t" \
    "v_lshlrev_b32 v204, 5, v186\n\t" \
    "v_lshrrev_b32 v187, 4, v223\n\t" \
    "v_lshlrev_b32 v205, 5, v187\n\t" \
    "v_xor_b32 v204, v204, v205\n\t" \
    "v_add_u32 v201, v201, v204\n\t" \
    "s_mul_i32 s96, s66, 9216\n\t" \
    "v_lshlrev_b32 v207, 3, v180\n\t" \
    "v_bfe_u32 v188, v223, 2, 1\n\t" \
    "v_lshl_add_u32 v207, v188, 7, v207\n\t" \
    "v_bfe_u32 v189, v223, 3, 2\n\t" \
    "s_mov_b32 s70, 288\n\t" \
    "v_mad_u32_u24 v207, v189, s70, v207\n\t" \
    "s_mov_b32 s70, 9216\n\t" \
    "v_mad_u32_u24 v207, v181, s70, v207\n\t" \
    "s_mul_i32 s69, s66, 9\n\t" \
    "s_add_u32 s69, s69, 0\n\t" \
    "s_and_b32 s70, s69, 3\n\t" \
    "s_lshl_b32 s70, s70, 5\n\t" \
    "s_lshr_b32 s69, s69, 2\n\t" \
    "s_lshl_b32 s69, s69, 10\n\t" \
    "v_xor_b32 v204, s70, v207\n\t" \
    "v_add_u32 v204, s69, v204\n\t" \
    "s_mul_i32 s69, s66, 9\n\t" \
    "s_add_u32 s69, s69, 1\n\t" \
    "s_and_b32 s70, s69, 3\n\t" \
    "s_lshl_b32 s70, s70, 5\n\t" \
    "s_lshr_b32 s69, s69, 2\n\t" \
    "s_lshl_b32 s69, s69, 10\n\t" \
    "v_xor_b32 v205, s70, v207\n\t" \
    "v_add_u32 v205, s69, v205\n\t" \
    "s_mul_i32 s69, s66, 9\n\t" \
    "s_add_u32 s69, s69, 2\n\t" \
    "s_and_b32 s70, s69, 3\n\t" \
    "s_lshl_b32 s70, s70, 5\n\t" \
    "s_lshr_b32 s69, s69, 2\n\t" \
    "s_lshl_b32 s69, s69, 10\n\t" \
    "v_xor_b32 v206, s70, v207\n\t" \
    "v_add_u32 v206, s69, v206\n\t" \
    "s_mul_i32 s69, s66, 9\n\t" \
    "s_add_u32 s69, s69, 3\n\t" \
    "s_and_b32 s70, s69, 3\n\t" \
    "s_lshl_b32 s70, s70, 5\n\t" \
    "s_lshr_b32 s69, s69, 2\n\t" \
    "s_lshl_b32 s69, s69, 10\n\t" \
    "v_xor_b32 v207, s70, v207\n\t" \
    "v_add_u32 v207, s69, v207\n\t" \
    "s_waitcnt vmcnt(12) lgkmcnt(0)\n\t" \
    "s_barrier\n\t" \
    "s_add_u32 m0, s82, 65536\n\t" \
    "s_nop 0\n\t" \
    "global_load_lds_dwordx4 v200, s[42:43] nt\n\t" \
    "v_add_u32 v200, 131072, v200\n\t" \
    "s_add_u32 m0, s82, 69632\n\t" \
    "s_nop 0\n\t" \
    "global_load_lds_dwordx4 v200, s[42:43] nt\n\t" \
    "v_add_u32 v200, 131072, v200\n\t" \
    "s_add_u32 m0, s82, 73728\n\t" \
    "s_nop 0\n\t" \
    "global_load_lds_dwordx4 v200, s[42:43] nt\n\t" \
    "v_add_u32 v200, 131072, v200\n\t" \
    "s_add_u32 m0, s82, 77824\n\t" \
    "s_nop 0\n\t" \
    "global_load_lds_dwordx4 v200, s[42:43] nt\n\t" \
    "v_add_u32 v200, 131072, v200\n\t" \
    "ds_read_b64_tr_b16 a[0:1], v202 offset:36864\n\t" \
    "ds_read_b64_tr_b16 a[2:3], v202 offset:37888\n\t" \
    "ds_read_b64_tr_b16 a[4:5], v202 offset:45056\n\t" \
    "ds_read_b64_tr_b16 a[6:7], v202 offset:46080\n\t" \
    "ds_read_b64_tr_b16 a[72:73], v203 offset:36864\n\t" \
    "ds_read_b64_tr_b16 a[74:75], v203 offset:37888\n\t" \
    "ds_read_b64_tr_b16 a[76:77], v203 offset:45056\n\t" \
    "ds_read_b64_tr_b16 a[78:79], v203 offset:46080\n\t" \
    "v_lshlrev_b32 v3, 4, v180\n\t" \
    "v_lshlrev_b32 v2, 5, v181\n\t" \
    "v_xor_b32 v3, v3, v2\n\t" \
    "s_bfe_u32 s71, s66, 0x10000\n\t" \
    "s_mul_i32 s69, s71, 576\n\t" \
    "s_bfe_u32 s71, s66, 0x10001\n\t" \
    "s_mul_i32 s71, s71, 18432\n\t" \
    "s_add_u32 s69, s69, s71\n\t" \
    "v_lshrrev_b32 v190, 2, v223\n\t" \
    "v_mov_b32 v1, s69\n\t" \
    "s_mov_b32 s70, 1152\n\t" \
    "v_mad_u32_u24 v1, v190, s70, v1\n\t" \
    "v_add_u32 v1, v1, v3\n\t" \
    "v_and_b32 v191, 15, v223\n\t" \
    "v_lshlrev_b32 v3, 6, v191\n\t" \
    "v_lshl_add_u32 v3, v187, 4, v3\n\t" \
    "v_bfe_u32 v192, v223, 3, 1\n\t" \
    "v_lshlrev_b32 v2, 5, v192\n\t" \
    "v_xor_b32 v3, v3, v2\n\t" \
    "v_mov_b32 v2, v3\n\t" \
    "s_lshr_b32 s65, s67, 5\n\t" \
    "s_mul_i32 s69, s65, 36864\n\t" \
    "s_add_u32 s50, s50, s69\n\t" \
    "s_addc_u32 s51, s51, 0\n\t" \
    "s_add_u32 s38, s36, 48234496\n\t" \
    "s_addc_u32 s39, s37, 0\n\t" \
    "s_mul_i32 s89, s66, 9216\n\t" \
    "s_mov_b32 s90, 0\n\t" \
    "s_waitcnt vmcnt(12) lgkmcnt(0)\n\t" \
    "s_barrier\n\t" \
    "s_add_u32 m0, s82, 81920\n\t" \
    "s_nop 0\n\t" \
    "global_load_lds_dwordx4 v200, s[42:43] nt\n\t" \
    "v_add_u32 v200, 131072, v200\n\t" \
    "s_add_u32 m0, s82, 86016\n\t" \
    "s_nop 0\n\t" \
    "global_load_lds_dwordx4 v200, s[42:43] nt\n\t" \
    "v_add_u32 v200, 131072, v200\n\t" \
    "s_add_u32 m0, s82, 90112\n\t" \
    "s_nop 0\n\t" \
    "global_load_lds_dwordx4 v200, s[42:43] nt\n\t" \
    "v_add_u32 v200, 131072, v200\n\t" \
    "s_add_u32 m0, s82, 94208\n\t" \
    "s_nop 0\n\t" \
    "global_load_lds_dwordx4 v200, s[42:43] nt\n\t" \
    "v_add_u32 v200, 131072, v200\n\t" \
    "ds_read_b64_tr_b16 a[8:9], v202 offset:53248\n\t" \
    "ds_read_b64_tr_b16 a[10:11], v202 offset:54272\n\t" \
    "ds_read_b64_tr_b16 a[12:13], v202 offset:61440\n\t" \
    "ds_read_b64_tr_b16 a[14:15], v202 offset:62464\n\t" \
    "ds_read_b64_tr_b16 a[80:81], v203 offset:53248\n\t" \
    "ds_read_b64_tr_b16 a[82:83], v203 offset:54272\n\t" \
    "ds_read_b64_tr_b16 a[84:85], v203 offset:61440\n\t" \
    "ds_read_b64_tr_b16 a[86:87], v203 offset:62464\n\t" \
    "v_lshlrev_b32 v0, 4, v223\n\t" \
    "s_mul_i32 s69, s66, 2048\n\t" \
    "s_add_u32 s69, s69, 147456\n\t" \
    "v_lshl_add_u32 v4, v223, 4, s69\n\t" \
    "s_mov_b32 s69, 147456\n\t" \
    "v_lshl_add_u32 v5, v223, 4, s69\n\t" \
    "v_lshlrev_b32 v6, 2, v223\n\t" \
    "v_mov_b32 v7, 0\n\t" \
    "s_waitcnt vmcnt(12) lgkmcnt(0)\n\t" \
    "s_barrier\n\t" \
    "s_add_u32 m0, s82, 98304\n\t" \
    "s_nop 0\n\t" \
    "global_load_lds_dwordx4 v200, s[42:43] nt\n\t" \
    "v_add_u32 v200, 131072, v200\n\t" \
    "s_add_u32 m0, s82, 102400\n\t" \
    "s_nop 0\n\t" \
    "global_load_lds_dwordx4 v200, s[42:43] nt\n\t" \
    "v_add_u32 v200, 131072, v200\n\t" \
    "s_add_u32 m0, s82, 106496\n\t" \
    "s_nop 0\n\t" \
    "global_load_lds_dwordx4 v200, s[42:43] nt\n\t" \
    "v_add_u32 v200, 131072, v200\n\t" \
    "s_add_u32 m0, s82, 110592\n\t" \
    "s_nop 0\n\t" \
    "global_load_lds_dwordx4 v200, s[42:43] nt\n\t" \
    "v_add_u32 v200, 131072, v200\n\t" \
    "v_add_u32 v202, 69632, v202\n\t" \
    "v_add_u32 v203, 69632, v203\n\t" \
    "ds_read_b64_tr_b16 a[16:17], v202 offset:0\n\t" \
    "ds_read_b64_tr_b16 a[18:19], v202 offset:1024\n\t" \
    "ds_read_b64_tr_b16 a[20:21], v202 offset:8192\n\t" \
    "ds_read_b64_tr_b16 a[22:23], v202 offset:9216\n\t" \
    "ds_read_b64_tr_b16 a[88:89], v203 offset:0\n\t" \
    "ds_read_b64_tr_b16 a[90:91], v203 offset:1024\n\t" \
    "ds_read_b64_tr_b16 a[92:93], v203 offset:8192\n\t" \
    "ds_read_b64_tr_b16 a[94:95], v203 offset:9216\n\t" \
    "s_bfe_u32 s71, s67, 0x50000\n\t" \
    "s_mul_i32 s69, s71, 32\n\t" \
    "s_add_u32 s69, s69, 262148\n\t" \
    "v_lshl_add_u32 v19, v180, 16, s69\n\t" \
    "v_lshl_add_u32 v19, v181, 13, v19\n\t" \
    "v_bfe_u32 v193, v223, 2, 3\n\t" \
    "v_lshlrev_b32 v199, 10, v193\n\t" \
    "v_min_u32 v199, 4096, v199\n\t" \
    "v_add_u32 v19, v199, v19\n\t" \
    "s_mul_i32 s69, s67, 32\n\t" \
    "s_add_u32 s69, s69, 6193152\n\t" \
    "v_lshl_add_u32 v20, v180, 11, s69\n\t" \
    "s_lshl_b32 s69, s67, 7\n\t" \
    "s_add_u32 s69, s69, 6111232\n\t" \
    "v_writelane_b32 v20, s69, 4\n\t" \
    "s_bfe_u32 s71, s67, 0x50000\n\t" \
    "s_mul_i32 s69, s71, 1024\n\t" \
    "v_lshl_add_u32 v17, v223, 4, s69\n\t" \
    "s_mul_i32 s69, s66, 9\n\t" \
    "s_add_u32 s69, s69, 0\n\t" \
    "s_mul_i32 s70, s69, 7\n\t" \
    "s_and_b32 s70, s70, 63\n\t" \
    "s_lshl_b32 s70, s70, 15\n\t" \
    "s_mul_i32 s69, s69, 2097152\n\t" \
    "s_add_u32 s69, s69, s70\n\t" \
    "v_add_u32 v8, s69, v17\n\t" \
    "s_mul_i32 s69, s66, 9\n\t" \
    "s_add_u32 s69, s69, 1\n\t" \
    "s_mul_i32 s70, s69, 7\n\t" \
    "s_and_b32 s70, s70, 63\n\t" \
    "s_lshl_b32 s70, s70, 15\n\t" \
    "s_mul_i32 s69, s69, 2097152\n\t" \
    "s_add_u32 s69, s69, s70\n\t" \
    "v_add_u32 v9, s69, v17\n\t" \
    "s_mul_i32 s69, s66, 9\n\t" \
    "s_add_u32 s69, s69, 2\n\t" \
    "s_mul_i32 s70, s69, 7\n\t" \
    "s_and_b32 s70, s70, 63\n\t" \
    "s_lshl_b32 s70, s70, 15\n\t" \
    "s_mul_i32 s69, s69, 2097152\n\t" \
    "s_add_u32 s69, s69, s70\n\t" \
    "v_add_u32 v10, s69, v17\n\t" \
    "s_mul_i32 s69, s66, 9\n\t" \
    "s_add_u32 s69, s69, 3\n\t" \
    "s_mul_i32 s70, s69, 7\n\t" \
    "s_and_b32 s70, s70, 63\n\t" \
    "s_lshl_b32 s70, s70, 15\n\t" \
    "s_mul_i32 s69, s69, 2097152\n\t" \
    "s_add_u32 s69, s69, s70\n\t" \
    "v_add_u32 v11, s69, v17\n\t" \
    "s_mul_i32 s69, s66, 9\n\t" \
    "s_add_u32 s69, s69, 4\n\t" \
    "s_mul_i32 s70, s69, 7\n\t" \
    "s_and_b32 s70, s70, 63\n\t" \
    "s_lshl_b32 s70, s70, 15\n\t" \
    "s_mul_i32 s69, s69, 2097152\n\t" \
    "s_add_u32 s69, s69, s70\n\t" \
    "v_add_u32 v12, s69, v17\n\t" \
    "s_mul_i32 s69, s66, 9\n\t" \
    "s_add_u32 s69, s69, 5\n\t" \
    "s_mul_i32 s70, s69, 7\n\t" \
    "s_and_b32 s70, s70, 63\n\t" \
    "s_lshl_b32 s70, s70, 15\n\t" \
    "s_mul_i32 s69, s69, 2097152\n\t" \
    "s_add_u32 s69, s69, s70\n\t" \
    "v_add_u32 v13, s69, v17\n\t" \
    "s_mul_i32 s69, s66, 9\n\t" \
    "s_add_u32 s69, s69, 6\n\t" \
    "s_mul_i32 s70, s69, 7\n\t" \
    "s_and_b32 s70, s70, 63\n\t" \
    "s_lshl_b32 s70, s70, 15\n\t" \
    "s_mul_i32 s69, s69, 2097152\n\t" \
    "s_add_u32 s69, s69, s70\n\t" \
    "v_add_u32 v14, s69, v17\n\t" \
    "s_mul_i32 s69, s66, 9\n\t" \
    "s_add_u32 s69, s69, 7\n\t" \
    "s_mul_i32 s70, s69, 7\n\t" \
    "s_and_b32 s70, s70, 63\n\t" \
    "s_lshl_b32 s70, s70, 15\n\t" \
    "s_mul_i32 s69, s69, 2097152\n\t" \
    "s_add_u32 s69, s69, s70\n\t" \
    "v_add_u32 v15, s69, v17\n\t" \
    "s_mul_i32 s69, s66, 9\n\t" \
    "s_add_u32 s69, s69, 8\n\t" \
    "s_mul_i32 s70, s69, 7\n\t" \
    "s_and_b32 s70, s70, 63\n\t" \
    "s_lshl_b32 s70, s70, 15\n\t" \
    "s_mul_i32 s69, s69, 2097152\n\t" \
    "s_add_u32 s69, s69, s70\n\t" \
    "v_add_u32 v16, s69, v17\n\t" \
    "s_waitcnt vmcnt(12) lgkmcnt(0)\n\t" \
    "s_barrier\n\t" \
    "s_add_u32 m0, s82, 0\n\t" \
    "s_nop 0\n\t" \
    "global_load_lds_dwordx4 v200, s[42:43] nt\n\t" \
    "v_add_u32 v200, 131072, v200\n\t" \
    "s_add_u32 m0, s82, 4096\n\t" \
    "s_nop 0\n\t" \
    "global_load_lds_dwordx4 v200, s[42:43] nt\n\t" \
    "v_add_u32 v200, 131072, v200\n\t" \
    "s_add_u32 m0, s82, 8192\n\t" \
    "s_nop 0\n\t" \
    "global_load_lds_dwordx4 v200, s[42:43] nt\n\t" \
    "v_add_u32 v200, 131072, v200\n\t" \
    "s_add_u32 m0, s82, 12288\n\t" \
    "s_nop 0\n\t" \
    "global_load_lds_dwordx4 v200, s[42:43] nt\n\t" \
    "v_add_u32 v200, 131072, v200\n\t" \
    "ds_read_b64_tr_b16 a[24:25], v202 offset:16384\n\t" \
    "ds_read_b64_tr_b16 a[26:27], v202 offset:17408\n\t" \
    "ds_read_b64_tr_b16 a[28:29], v202 offset:24576\n\t" \
    "ds_read_b64_tr_b16 a[30:31], v202 offset:25600\n\t" \
    "ds_read_b64_tr_b16 a[96:97], v203 offset:16384\n\t" \
    "ds_read_b64_tr_b16 a[98:99], v203 offset:17408\n\t" \
    "ds_read_b64_tr_b16 a[100:101], v203 offset:24576\n\t" \
    "ds_read_b64_tr_b16 a[102:103], v203 offset:25600\n\t" \
    "s_waitcnt vmcnt(12) lgkmcnt(0)\n\t" \
    "s_barrier\n\t" \
    "s_add_u32 m0, s82, 16384\n\t" \
    "s_nop 0\n\t" \
    "global_load_lds_dwordx4 v200, s[42:43] nt\n\t" \
    "v_add_u32 v200, 131072, v200\n\t" \
    "s_add_u32 m0, s82, 20480\n\t" \
    "s_nop 0\n\t" \
    "global_load_lds_dwordx4 v200, s[42:43] nt\n\t" \
    "v_add_u32 v200, 131072, v200\n\t" \
    "s_add_u32 m0, s82, 24576\n\t" \
    "s_nop 0\n\t" \
    "global_load_lds_dwordx4 v200, s[42:43] nt\n\t" \
    "v_add_u32 v200, 131072, v200\n\t" \
    "s_add_u32 m0, s82, 28672\n\t" \
    "s_nop 0\n\t" \
    "global_load_lds_dwordx4 v200, s[42:43] nt\n\t" \
    "v_add_u32 v200, 131072, v200\n\t" \
    "s_add_u32 m0, s89, 0\n\t" \
    "s_nop 0\n\t" \
    "global_load_lds_dwordx4 v1, s[50:51] offset:0\n\t" \
    "s_add_u32 m0, s89, 960\n\t" \
    "s_nop 0\n\t" \
    "global_load_lds_dwordx4 v1, s[50:51] offset:64\n\t" \
    "s_add_u32 m0, s89, 1920\n\t" \
    "s_nop 0\n\t" \
    "global_load_lds_dwordx4 v1, s[50:51] offset:128\n\t" \
    "s_add_u32 m0, s89, 2880\n\t" \
    "s_nop 0\n\t" \
    "global_load_lds_dwordx4 v1, s[50:51] offset:192\n\t" \
    "s_add_u32 m0, s89, 3840\n\t" \
    "s_nop 0\n\t" \
    "global_load_lds_dwordx4 v1, s[50:51] offset:256\n\t" \
    "s_add_u32 m0, s89, 4800\n\t" \
    "s_nop 0\n\t" \
    "global_load_lds_dwordx4 v1, s[50:51] offset:320\n\t" \
    "s_add_u32 m0, s89, 5760\n\t" \
    "s_nop 0\n\t" \
    "global_load_lds_dwordx4 v1, s[50:51] offset:384\n\t" \
    "s_add_u32 m0, s89, 6720\n\t" \
    "s_nop 0\n\t" \
    "global_load_lds_dwordx4 v1, s[50:51] offset:448\n\t" \
    "s_add_u32 m0, s89, 7680\n\t" \
    "s_nop 0\n\t" \
    "global_load_lds_dwordx4 v1, s[50:51] offset:512\n\t" \
    "ds_read_b64_tr_b16 a[32:33], v202 offset:32768\n\t" \
    "ds_read_b64_tr_b16 a[34:35], v202 offset:33792\n\t" \
    "ds_read_b64_tr_b16 a[36:37], v202 offset:40960\n\t" \
    "ds_read_b64_tr_b16 a[38:39], v202 offset:41984\n\t" \
    "ds_read_b64_tr_b16 a[104:105], v203 offset:32768\n\t" \
    "ds_read_b64_tr_b16 a[106:107], v203 offset:33792\n\t" \
    "ds_read_b64_tr_b16 a[108:109], v203 offset:40960\n\t" \
    "ds_read_b64_tr_b16 a[110:111], v203 offset:41984\n\t" \
    "s_waitcnt vmcnt(21) lgkmcnt(0)\n\t" \
    "s_barrier\n\t" \
    "s_add_u32 m0, s96, 73728\n\t" \
    "s_nop 0\n\t" \
    "global_load_lds_dwordx4 v201, s[44:45] offset:0 nt\n\t" \
    "s_add_u32 m0, s96, 74624\n\t" \
    "s_nop 0\n\t" \
    "global_load_lds_dwordx4 v201, s[44:45] offset:128 nt\n\t" \
    "s_add_u32 m0, s96, 75520\n\t" \
    "s_nop 0\n\t" \
    "global_load_lds_dwordx4 v201, s[44:45] offset:256 nt\n\t" \
    "s_add_u32 m0, s96, 76416\n\t" \
    "s_nop 0\n\t" \
    "global_load_lds_dwordx4 v201, s[44:45] offset:384 nt\n\t" \
    "s_add_u32 m0, s96, 77312\n\t" \
    "s_nop 0\n\t" \
    "global_load_lds_dwordx4 v201, s[44:45] offset:512 nt\n\t" \
    "s_add_u32 m0, s96, 78208\n\t" \
    "s_nop 0\n\t" \
    "global_load_lds_dwordx4 v201, s[44:45] offset:640 nt\n\t" \
    "s_add_u32 m0, s96, 79104\n\t" \
    "s_nop 0\n\t" \
    "global_load_lds_dwordx4 v201, s[44:45] offset:768 nt\n\t" \
    "s_add_u32 m0, s96, 80000\n\t" \
    "s_nop 0\n\t" \
    "global_load_lds_dwordx4 v201, s[44:45] offset:896 nt\n\t" \
    "s_add_u32 m0, s96, 80896\n\t" \
    "s_nop 0\n\t" \
    "global_load_lds_dwordx4 v201, s[44:45] offset:1024 nt\n\t" \
    "v_add_u32 v201, 36864, v201\n\t" \
    "ds_read_b64_tr_b16 a[40:41], v202 offset:49152\n\t" \
    "ds_read_b64_tr_b16 a[42:43], v202 offset:50176\n\t" \
    "ds_read_b64_tr_b16 a[44:45], v202 offset:57344\n\t" \
    "ds_read_b64_tr_b16 a[46:47], v202 offset:58368\n\t" \
    "ds_read_b64_tr_b16 a[112:113], v203 offset:49152\n\t" \
    "ds_read_b64_tr_b16 a[114:115], v203 offset:50176\n\t" \
    "ds_read_b64_tr_b16 a[116:117], v203 offset:57344\n\t" \
    "ds_read_b64_tr_b16 a[118:119], v203 offset:58368\n\t" \
    "s_waitcnt vmcnt(26) lgkmcnt(0)\n\t" \
    "s_barrier\n\t" \
    "v_add_u32 v202, 65536, v202\n\t" \
    "v_add_u32 v203, 65536, v203\n\t" \
    "ds_read_b64_tr_b16 a[48:49], v202 offset:0\n\t" \
    "ds_read_b64_tr_b16 a[50:51], v202 offset:1024\n\t" \
    "ds_read_b64_tr_b16 a[52:53], v202 offset:8192\n\t" \
    "ds_read_b64_tr_b16 a[54:55], v202 offset:9216\n\t" \
    "ds_read_b64_tr_b16 a[120:121], v203 offset:0\n\t" \
    "ds_read_b64_tr_b16 a[122:123], v203 offset:1024\n\t" \
    "ds_read_b64_tr_b16 a[124:125], v203 offset:8192\n\t" \
    "ds_read_b64_tr_b16 a[126:127], v203 offset:9216\n\t" \
    "s_waitcnt vmcnt(22) lgkmcnt(0)\n\t" \
    "s_barrier\n\t" \
    "s_add_u32 m0, s96, 110592\n\t" \
    "s_nop 0\n\t" \
    "global_load_lds_dwordx4 v201, s[44:45] offset:0 nt\n\t" \
    "s_add_u32 m0, s96, 111488\n\t" \
    "s_nop 0\n\t" \
    "global_load_lds_dwordx4 v201, s[44:45] offset:128 nt\n\t" \
    "s_add_u32 m0, s96, 112384\n\t" \
    "s_nop 0\n\t" \
    "global_load_lds_dwordx4 v201, s[44:45] offset:256 nt\n\t" \
    "s_add_u32 m0, s96, 113280\n\t" \
    "s_nop 0\n\t" \
    "global_load_lds_dwordx4 v201, s[44:45] offset:384 nt\n\t" \
    "s_add_u32 m0, s96, 114176\n\t" \
    "s_nop 0\n\t" \
    "global_load_lds_dwordx4 v201, s[44:45] offset:512 nt\n\t" \
    "s_add_u32 m0, s96, 115072\n\t" \
    "s_nop 0\n\t" \
    "global_load_lds_dwordx4 v201, s[44:45] offset:640 nt\n\t" \
    "s_add_u32 m0, s96, 115968\n\t" \
    "s_nop 0\n\t" \
    "global_load_lds_dwordx4 v201, s[44:45] offset:768 nt\n\t" \
    "s_add_u32 m0, s96, 116864\n\t" \
    "s_nop 0\n\t" \
    "global_load_lds_dwordx4 v201, s[44:45] offset:896 nt\n\t" \
    "s_add_u32 m0, s96, 117760\n\t" \
    "s_nop 0\n\t" \
    "global_load_lds_dwordx4 v201, s[44:45] offset:1024 nt\n\t" \
    "v_add_u32 v201, 36864, v201\n\t" \
    "v_mbcnt_lo_u32_b32 v164, -1, 0\n\t" \
    "v_mbcnt_hi_u32_b32 v164, -1, v164\n\t" \
    "s_cmp_eq_u32 s66, 0\n\t" \
    "s_cselect_b32 s69, 59, 11\n\t" \
    "v_min_u32 v164, s69, v164\n\t" \
    "v_lshlrev_b32 v164, 6, v164\n\t" \
    "v_add_u32 v164, 6258688, v164\n\t" \
    "v_mov_b32 v165, 6266880\n\t" \
    "global_load_dword v165, v165, s[36:37] sc1\n\t" \
    "global_load_dwordx2 v[166:167], v164, s[36:37] sc1\n\t" \
    "v_add_u32 v202, -98304, v202\n\t" \
    "v_add_u32 v203, -98304, v203\n\t" \
    "ds_read_b64_tr_b16 a[56:57], v202 offset:0\n\t" \
    "ds_read_b64_tr_b16 a[58:59], v202 offset:1024\n\t" \
    "ds_read_b64_tr_b16 a[60:61], v202 offset:8192\n\t" \
    "ds_read_b64_tr_b16 a[62:63], v202 offset:9216\n\t" \
    "ds_read_b64_tr_b16 a[128:129], v203 offset:0\n\t" \
    "ds_read_b64_tr_b16 a[130:131], v203 offset:1024\n\t" \
    "ds_read_b64_tr_b16 a[132:133], v203 offset:8192\n\t" \
    "ds_read_b64_tr_b16 a[134:135], v203 offset:9216\n\t" \
    "s_waitcnt vmcnt(29) lgkmcnt(0)\n\t" \
    "s_barrier\n\t" \
    "ds_read_b64_tr_b16 a[64:65], v202 offset:16384\n\t" \
    "ds_read_b64_tr_b16 a[66:67], v202 offset:17408\n\t" \
    "ds_read_b64_tr_b16 a[68:69], v202 offset:24576\n\t" \
    "ds_read_b64_tr_b16 a[70:71], v202 offset:25600\n\t" \
    "ds_read_b64_tr_b16 a[136:137], v203 offset:16384\n\t" \
    "ds_read_b64_tr_b16 a[138:139], v203 offset:17408\n\t" \
    "ds_read_b64_tr_b16 a[140:141], v203 offset:24576\n\t" \
    "ds_read_b64_tr_b16 a[142:143], v203 offset:25600\n\t" \
    "s_waitcnt vmcnt(11) lgkmcnt(0)\n\t" \
    "s_barrier\n\t" \
    "s_add_u32 m0, s96, 36864\n\t" \
    "s_nop 0\n\t" \
    "global_load_lds_dwordx4 v201, s[44:45] offset:0 nt\n\t" \
    "s_add_u32 m0, s96, 37760\n\t" \
    "s_nop 0\n\t" \
    "global_load_lds_dwordx4 v201, s[44:45] offset:128 nt\n\t" \
    "s_add_u32 m0, s96, 38656\n\t" \
    "s_nop 0\n\t" \
    "global_load_lds_dwordx4 v201, s[44:45] offset:256 nt\n\t" \
    "s_add_u32 m0, s96, 39552\n\t" \
    "s_nop 0\n\t" \
    "global_load_lds_dwordx4 v201, s[44:45] offset:384 nt\n\t" \
    "s_add_u32 m0, s96, 40448\n\t" \
    "s_nop 0\n\t" \
    "global_load_lds_dwordx4 v201, s[44:45] offset:512 nt\n\t" \
    "s_add_u32 m0, s96, 41344\n\t" \
    "s_nop 0\n\t" \
    "global_load_lds_dwordx4 v201, s[44:45] offset:640 nt\n\t" \
    "s_add_u32 m0, s96, 42240\n\t" \
    "s_nop 0\n\t" \
    "global_load_lds_dwordx4 v201, s[44:45] offset:768 nt\n\t" \
    "s_add_u32 m0, s96, 43136\n\t" \
    "s_nop 0\n\t" \
    "global_load_lds_dwordx4 v201, s[44:45] offset:896 nt\n\t" \
    "s_add_u32 m0, s96, 44032\n\t" \
    "s_nop 0\n\t" \
    "global_load_lds_dwordx4 v201, s[44:45] offset:1024 nt\n\t" \
    "v_add_u32 v201, 36864, v201\n\t" \
    "v_add_u32 v204, 73728, v204\n\t" \
    "v_add_u32 v205, 73728, v205\n\t" \
    "v_add_u32 v206, 73728, v206\n\t" \
    "v_add_u32 v207, 73728, v207\n\t" \
    "ds_read_b64_tr_b16 a[144:145], v204 offset:0\n\t" \
    "ds_read_b64_tr_b16 a[146:147], v204 offset:18432\n\t" \
    "ds_read_b64_tr_b16 a[160:161], v205 offset:0\n\t" \
    "ds_read_b64_tr_b16 a[162:163], v205 offset:18432\n\t" \
    "ds_read_b64_tr_b16 a[176:177], v206 offset:0\n\t" \
    "ds_read_b64_tr_b16 a[178:179], v206 offset:18432\n\t" \
    "s_waitcnt lgkmcnt(6)\n\t" \
    "ds_read_b64_tr_b16 a[192:193], v207 offset:0\n\t" \
    "ds_read_b64_tr_b16 a[194:195], v207 offset:18432\n\t" \
    "ds_read_b64_tr_b16 a[208:209], v204 offset:1024\n\t" \
    "ds_read_b64_tr_b16 a[210:211], v204 offset:19456\n\t" \
    "ds_read_b64_tr_b16 a[224:225], v205 offset:1024\n\t" \
    "ds_read_b64_tr_b16 a[226:227], v205 offset:19456\n\t" \
    "s_waitcnt lgkmcnt(6)\n\t" \
    "ds_read_b64_tr_b16 a[240:241], v206 offset:1024\n\t" \
    "ds_read_b64_tr_b16 a[242:243], v206 offset:19456\n\t" \
    "ds_read_b64_tr_b16 v[224:225], v207 offset:1024\n\t" \
    "ds_read_b64_tr_b16 v[226:227], v207 offset:19456\n\t" \
    "ds_read_b64_tr_b16 v[240:241], v204 offset:2048\n\t" \
    "ds_read_b64_tr_b16 v[242:243], v204 offset:20480\n\t" \
    "s_waitcnt lgkmcnt(6)\n\t" \
    "s_waitcnt lgkmcnt(6)\n\t" \
    "ds_read_b128 v[24:27], v2 offset:0\n\t" \
    "ds_read_b128 v[28:31], v2 offset:18432\n\t" \
    "ds_read_b128 v[32:35], v2 offset:1024\n\t" \
    "ds_read_b128 v[36:39], v2 offset:19456\n\t" \
    "s_waitcnt lgkmcnt(3)\n\t" \
    "v_mfma_f32_16x16x32_bf16 v[72:75], a[0:3], v[24:27], 0\n\t" \
    "ds_read_b128 v[40:43], v2 offset:2048\n\t" \
    "s_waitcnt lgkmcnt(3)\n\t" \
    "v_mfma_f32_16x16x32_bf16 v[76:79], a[0:3], v[28:31], 0\n\t" \
    "ds_read_b128 v[44:47], v2 offset:20480\n\t" \
    "v_mfma_f32_16x16x32_bf16 v[80:83], a[72:75], v[24:27], 0\n\t" \
    "v_mfma_f32_16x16x32_bf16 v[84:87], a[72:75], v[28:31], 0\n\t" \
    "s_waitcnt lgkmcnt(3)\n\t" \
    "v_mfma_f32_16x16x32_bf16 v[72:75], a[4:7], v[32:35], v[72:75]\n\t" \
    "ds_read_b128 v[48:51], v2 offset:3072\n\t" \
    "s_waitcnt lgkmcnt(3)\n\t" \
    "v_mfma_f32_16x16x32_bf16 v[76:79], a[4:7], v[36:39], v[76:79]\n\t" \
    "ds_read_b128 v[52:55], v2 offset:21504\n\t" \
    "v_mfma_f32_16x16x32_bf16 v[80:83], a[76:79], v[32:35], v[80:83]\n\t" \
    "v_mfma_f32_16x16x32_bf16 v[84:87], a[76:79], v[36:39], v[84:87]\n\t" \
    "s_waitcnt lgkmcnt(3)\n\t" \
    "v_mfma_f32_16x16x32_bf16 v[72:75], a[8:11], v[40:43], v[72:75]\n\t" \
    "ds_read_b128 v[56:59], v2 offset:4096\n\t" \
    "s_waitcnt lgkmcnt(3)\n\t" \
    "v_mfma_f32_16x16x32_bf16 v[76:79], a[8:11], v[44:47], v[76:79]\n\t" \
    "ds_read_b128 v[60:63], v2 offset:22528\n\t" \
    "v_mfma_f32_16x16x32_bf16 v[80:83], a[80:83], v[40:43], v[80:83]\n\t" \
    "v_mfma_f32_16x16x32_bf16 v[84:87], a[80:83], v[44:47], v[84:87]\n\t" \
    "s_waitcnt lgkmcnt(3)\n\t" \
    "v_mfma_f32_16x16x32_bf16 v[72:75], a[12:15], v[48:51], v[72:75]\n\t" \
    "ds_read_b128 v[64:67], v2 offset:5120\n\t" \
    "s_waitcnt lgkmcnt(3)\n\t" \
    "v_mfma_f32_16x16x32_bf16 v[76:79], a[12:15], v[52:55], v[76:79]\n\t" \
    "ds_read_b128 v[68:71], v2 offset:23552\n\t" \
    "v_mfma_f32_16x16x32_bf16 v[80:83], a[84:87], v[48:51], v[80:83]\n\t" \
    "v_mfma_f32_16x16x32_bf16 v[84:87], a[84:87], v[52:55], v[84:87]\n\t" \
    "s_waitcnt lgkmcnt(3)\n\t" \
    "v_mfma_f32_16x16x32_bf16 v[72:75], a[16:19], v[56:59], v[72:75]\n\t" \
    "s_waitcnt lgkmcnt(2)\n\t" \
    "v_mfma_f32_16x16x32_bf16 v[76:79], a[16:19], v[60:63], v[76:79]\n\t" \
    "v_mfma_f32_16x16x32_bf16 v[80:83], a[88:91], v[56:59], v[80:83]\n\t" \
    "v_mfma_f32_16x16x32_bf16 v[84:87], a[88:91], v[60:63], v[84:87]\n\t" \
    "s_waitcnt lgkmcnt(1)\n\t" \
    "v_mfma_f32_16x16x32_bf16 v[72:75], a[20:23], v[64:67], v[72:75]\n\t" \
    "s_waitcnt lgkmcnt(0)\n\t" \
    "v_mfma_f32_16x16x32_bf16 v[76:79], a[20:23], v[68:71], v[76:79]\n\t" \
    "v_mfma_f32_16x16x32_bf16 v[80:83], a[92:95], v[64:67], v[80:83]\n\t" \
    "v_mfma_f32_16x16x32_bf16 v[84:87], a[92:95], v[68:71], v[84:87]\n\t" \
    "s_waitcnt vmcnt(11) lgkmcnt(0)\n\t" \
    "s_barrier\n\t" \
    "s_add_u32 m0, s96, 73728\n\t" \
    "s_nop 0\n\t" \
    "global_load_lds_dwordx4 v201, s[44:45] offset:0 nt\n\t" \
    "s_add_u32 m0, s96, 74624\n\t" \
    "s_nop 0\n\t" \
    "global_load_lds_dwordx4 v201, s[44:45] offset:128 nt\n\t" \
    "s_add_u32 m0, s96, 75520\n\t" \
    "s_nop 0\n\t" \
    "global_load_lds_dwordx4 v201, s[44:45] offset:256 nt\n\t" \
    "s_add_u32 m0, s96, 76416\n\t" \
    "s_nop 0\n\t" \
    "global_load_lds_dwordx4 v201, s[44:45] offset:384 nt\n\t" \
    "s_add_u32 m0, s96, 77312\n\t" \
    "s_nop 0\n\t" \
    "global_load_lds_dwordx4 v201, s[44:45] offset:512 nt\n\t" \
    "s_add_u32 m0, s96, 78208\n\t" \
    "s_nop 0\n\t" \
    "global_load_lds_dwordx4 v201, s[44:45] offset:640 nt\n\t" \
    "s_add_u32 m0, s96, 79104\n\t" \
    "s_nop 0\n\t" \
    "global_load_lds_dwordx4 v201, s[44:45] offset:768 nt\n\t" \
    "s_add_u32 m0, s96, 80000\n\t" \
    "s_nop 0\n\t" \
    "global_load_lds_dwordx4 v201, s[44:45] offset:896 nt\n\t" \
    "s_add_u32 m0, s96, 80896\n\t" \
    "s_nop 0\n\t" \
    "global_load_lds_dwordx4 v201, s[44:45] offset:1024 nt\n\t" \
    "v_add_u32 v201, 36864, v201\n\t" \
    "ds_read_b64_tr_b16 a[148:149], v204 offset:36864\n\t" \
    "ds_read_b64_tr_b16 a[150:151], v204 offset:55296\n\t" \
    "ds_read_b64_tr_b16 a[164:165], v205 offset:36864\n\t" \
    "ds_read_b64_tr_b16 a[166:167], v205 offset:55296\n\t" \
    "ds_read_b64_tr_b16 a[180:181], v206 offset:36864\n\t" \
    "ds_read_b64_tr_b16 a[182:183], v206 offset:55296\n\t" \
    "s_waitcnt lgkmcnt(6)\n\t" \
    "ds_read_b64_tr_b16 a[196:197], v207 offset:36864\n\t" \
    "ds_read_b64_tr_b16 a[198:199], v207 offset:55296\n\t" \
    "ds_read_b64_tr_b16 a[212:213], v204 offset:37888\n\t" \
    "ds_read_b64_tr_b16 a[214:215], v204 offset:56320\n\t" \
    "ds_read_b64_tr_b16 a[228:229], v205 offset:37888\n\t" \
    "ds_read_b64_tr_b16 a[230:231], v205 offset:56320\n\t" \
    "s_waitcnt lgkmcnt(6)\n\t" \
    "ds_read_b64_tr_b16 a[244:245], v206 offset:37888\n\t" \
    "ds_read_b64_tr_b16 a[246:247], v206 offset:56320\n\t" \
    "ds_read_b64_tr_b16 v[228:229], v207 offset:37888\n\t" \
    "ds_read_b64_tr_b16 v[230:231], v207 offset:56320\n\t" \
    "ds_read_b64_tr_b16 v[244:245], v204 offset:38912\n\t" \
    "ds_read_b64_tr_b16 v[246:247], v204 offset:57344\n\t" \
    "s_waitcnt lgkmcnt(6)\n\t" \
    "s_waitcnt lgkmcnt(6)\n\t" \
    "ds_read_b128 v[24:27], v2 offset:6144\n\t" \
    "ds_read_b128 v[28:31], v2 offset:24576\n\t" \
    "ds_read_b128 v[32:35], v2 offset:7168\n\t" \
    "ds_read_b128 v[36:39], v2 offset:25600\n\t" \
    "s_waitcnt lgkmcnt(3)\n\t" \
    "v_mfma_f32_16x16x32_bf16 v[72:75], a[24:27], v[24:27], v[72:75]\n\t" \
    "ds_read_b128 v[40:43], v2 offset:8192\n\t" \
    "s_waitcnt lgkmcnt(3)\n\t" \
    "v_mfma_f32_16x16x32_bf16 v[76:79], a[24:27], v[28:31], v[76:79]\n\t" \
    "ds_read_b128 v[44:47], v2 offset:26624\n\t" \
    "v_mfma_f32_16x16x32_bf16 v[80:83], a[96:99], v[24:27], v[80:83]\n\t" \
    "v_mfma_f32_16x16x32_bf16 v[84:87], a[96:99], v[28:31], v[84:87]\n\t" \
    "s_waitcnt lgkmcnt(3)\n\t" \
    "v_mfma_f32_16x16x32_bf16 v[72:75], a[28:31], v[32:35], v[72:75]\n\t" \
    "ds_read_b128 v[48:51], v2 offset:9216\n\t" \
    "s_waitcnt lgkmcnt(3)\n\t" \
    "v_mfma_f32_16x16x32_bf16 v[76:79], a[28:31], v[36:39], v[76:79]\n\t" \
    "ds_read_b128 v[52:55], v2 offset:27648\n\t" \
    "v_mfma_f32_16x16x32_bf16 v[80:83], a[100:103], v[32:35], v[80:83]\n\t" \
    "v_mfma_f32_16x16x32_bf16 v[84:87], a[100:103], v[36:39], v[84:87]\n\t" \
    "s_waitcnt lgkmcnt(3)\n\t" \
    "v_mfma_f32_16x16x32_bf16 v[72:75], a[32:35], v[40:43], v[72:75]\n\t" \
    "ds_read_b128 v[56:59], v2 offset:10240\n\t" \
    "s_waitcnt lgkmcnt(3)\n\t" \
    "v_mfma_f32_16x16x32_bf16 v[76:79], a[32:35], v[44:47], v[76:79]\n\t" \
    "ds_read_b128 v[60:63], v2 offset:28672\n\t" \
    "v_mfma_f32_16x16x32_bf16 v[80:83], a[104:107], v[40:43], v[80:83]\n\t" \
    "v_mfma_f32_16x16x32_bf16 v[84:87], a[104:107], v[44:47], v[84:87]\n\t" \
    "s_waitcnt lgkmcnt(3)\n\t" \
    "v_mfma_f32_16x16x32_bf16 v[72:75], a[36:39], v[48:51], v[72:75]\n\t" \
    "ds_read_b128 v[64:67], v2 offset:11264\n\t" \
    "s_waitcnt lgkmcnt(3)\n\t" \
    "v_mfma_f32_16x16x32_bf16 v[76:79], a[36:39], v[52:55], v[76:79]\n\t" \
    "ds_read_b128 v[68:71], v2 offset:29696\n\t" \
    "v_mfma_f32_16x16x32_bf16 v[80:83], a[108:111], v[48:51], v[80:83]\n\t" \
    "v_mfma_f32_16x16x32_bf16 v[84:87], a[108:111], v[52:55], v[84:87]\n\t" \
    "s_waitcnt lgkmcnt(3)\n\t" \
    "v_mfma_f32_16x16x32_bf16 v[72:75], a[40:43], v[56:59], v[72:75]\n\t" \
    "s_waitcnt lgkmcnt(2)\n\t" \
    "v_mfma_f32_16x16x32_bf16 v[76:79], a[40:43], v[60:63], v[76:79]\n\t" \
    "v_mfma_f32_16x16x32_bf16 v[80:83], a[112:115], v[56:59], v[80:83]\n\t" \
    "v_mfma_f32_16x16x32_bf16 v[84:87], a[112:115], v[60:63], v[84:87]\n\t" \
    "s_waitcnt lgkmcnt(1)\n\t" \
    "v_mfma_f32_16x16x32_bf16 v[72:75], a[44:47], v[64:67], v[72:75]\n\t" \
    "s_waitcnt lgkmcnt(0)\n\t" \
    "v_mfma_f32_16x16x32_bf16 v[76:79], a[44:47], v[68:71], v[76:79]\n\t" \
    "v_mfma_f32_16x16x32_bf16 v[80:83], a[116:119], v[64:67], v[80:83]\n\t" \
    "v_mfma_f32_16x16x32_bf16 v[84:87], a[116:119], v[68:71], v[84:87]\n\t" \
    "s_waitcnt vmcnt(9) lgkmcnt(0)\n\t" \
    "s_barrier\n\t" \
    "s_add_u32 s50, s50, 73728\n\t" \
    "s_addc_u32 s51, s51, 0\n\t" \
    "s_add_u32 m0, s89, 110592\n\t" \
    "s_nop 0\n\t" \
    "global_load_lds_dwordx4 v1, s[50:51] offset:0\n\t" \
    "s_add_u32 m0, s89, 111552\n\t" \
    "s_nop 0\n\t" \
    "global_load_lds_dwordx4 v1, s[50:51] offset:64\n\t" \
    "s_add_u32 m0, s89, 112512\n\t" \
    "s_nop 0\n\t" \
    "global_load_lds_dwordx4 v1, s[50:51] offset:128\n\t" \
    "s_add_u32 m0, s89, 113472\n\t" \
    "s_nop 0\n\t" \
    "global_load_lds_dwordx4 v1, s[50:51] offset:192\n\t" \
    "s_add_u32 m0, s89, 114432\n\t" \
    "s_nop 0\n\t" \
    "global_load_lds_dwordx4 v1, s[50:51] offset:256\n\t" \
    "s_add_u32 m0, s89, 115392\n\t" \
    "s_nop 0\n\t" \
    "global_load_lds_dwordx4 v1, s[50:51] offset:320\n\t" \
    "s_add_u32 m0, s89, 116352\n\t" \
    "s_nop 0\n\t" \
    "global_load_lds_dwordx4 v1, s[50:51] offset:384\n\t" \
    "s_add_u32 m0, s89, 117312\n\t" \
    "s_nop 0\n\t" \
    "global_load_lds_dwordx4 v1, s[50:51] offset:448\n\t" \
    "s_add_u32 m0, s89, 118272\n\t" \
    "s_nop 0\n\t" \
    "global_load_lds_dwordx4 v1, s[50:51] offset:512\n\t" \
    "v_add_u32 v204, -36864, v204\n\t" \
    "v_add_u32 v205, -36864, v205\n\t" \
    "v_add_u32 v206, -36864, v206\n\t" \
    "v_add_u32 v207, -36864, v207\n\t" \
    "ds_read_b64_tr_b16 a[152:153], v204 offset:0\n\t" \
    "ds_read_b64_tr_b16 a[154:155], v204 offset:18432\n\t" \
    "ds_read_b64_tr_b16 a[168:169], v205 offset:0\n\t" \
    "ds_read_b64_tr_b16 a[170:171], v205 offset:18432\n\t" \
    "ds_read_b64_tr_b16 a[184:185], v206 offset:0\n\t" \
    "ds_read_b64_tr_b16 a[186:187], v206 offset:18432\n\t" \
    "s_waitcnt lgkmcnt(6)\n\t" \
    "ds_read_b64_tr_b16 a[200:201], v207 offset:0\n\t" \
    "ds_read_b64_tr_b16 a[202:203], v207 offset:18432\n\t" \
    "ds_read_b64_tr_b16 a[216:217], v204 offset:1024\n\t" \
    "ds_read_b64_tr_b16 a[218:219], v204 offset:19456\n\t" \
    "ds_read_b64_tr_b16 a[232:233], v205 offset:1024\n\t" \
    "ds_read_b64_tr_b16 a[234:235], v205 offset:19456\n\t" \
    "s_waitcnt lgkmcnt(6)\n\t" \
    "ds_read_b64_tr_b16 a[248:249], v206 offset:1024\n\t" \
    "ds_read_b64_tr_b16 a[250:251], v206 offset:19456\n\t" \
    "ds_read_b64_tr_b16 v[232:233], v207 offset:1024\n\t" \
    "ds_read_b64_tr_b16 v[234:235], v207 offset:19456\n\t" \
    "ds_read_b64_tr_b16 v[248:249], v204 offset:2048\n\t" \
    "ds_read_b64_tr_b16 v[250:251], v204 offset:20480\n\t" \
    "s_waitcnt lgkmcnt(6)\n\t" \
    "s_waitcnt lgkmcnt(6)\n\t" \
    "ds_read_b128 v[24:27], v2 offset:12288\n\t" \
    "ds_read_b128 v[28:31], v2 offset:30720\n\t" \
    "ds_read_b128 v[32:35], v2 offset:13312\n\t" \
    "ds_read_b128 v[36:39], v2 offset:31744\n\t" \
    "s_waitcnt lgkmcnt(3)\n\t" \
    "v_mfma_f32_16x16x32_bf16 v[72:75], a[48:51], v[24:27], v[72:75]\n\t" \
    "ds_read_b128 v[40:43], v2 offset:14336\n\t" \
    "s_waitcnt lgkmcnt(3)\n\t" \
    "v_mfma_f32_16x16x32_bf16 v[76:79], a[48:51], v[28:31], v[76:79]\n\t" \
    "ds_read_b128 v[44:47], v2 offset:32768\n\t" \
    "v_mfma_f32_16x16x32_bf16 v[80:83], a[120:123], v[24:27], v[80:83]\n\t" \
    "v_mfma_f32_16x16x32_bf16 v[84:87], a[120:123], v[28:31], v[84:87]\n\t" \
    "s_waitcnt lgkmcnt(3)\n\t" \
    "v_mfma_f32_16x16x32_bf16 v[72:75], a[52:55], v[32:35], v[72:75]\n\t" \
    "ds_read_b128 v[48:51], v2 offset:15360\n\t" \
    "s_waitcnt lgkmcnt(3)\n\t" \
    "v_mfma_f32_16x16x32_bf16 v[76:79], a[52:55], v[36:39], v[76:79]\n\t" \
    "ds_read_b128 v[52:55], v2 offset:33792\n\t" \
    "v_mfma_f32_16x16x32_bf16 v[80:83], a[124:127], v[32:35], v[80:83]\n\t" \
    "v_mfma_f32_16x16x32_bf16 v[84:87], a[124:127], v[36:39], v[84:87]\n\t" \
    "s_waitcnt lgkmcnt(3)\n\t" \
    "v_mfma_f32_16x16x32_bf16 v[72:75], a[56:59], v[40:43], v[72:75]\n\t" \
    "ds_read_b128 v[56:59], v2 offset:16384\n\t" \
    "s_waitcnt lgkmcnt(3)\n\t" \
    "v_mfma_f32_16x16x32_bf16 v[76:79], a[56:59], v[44:47], v[76:79]\n\t" \
    "ds_read_b128 v[60:63], v2 offset:34816\n\t" \
    "v_mfma_f32_16x16x32_bf16 v[80:83], a[128:131], v[40:43], v[80:83]\n\t" \
    "v_mfma_f32_16x16x32_bf16 v[84:87], a[128:131], v[44:47], v[84:87]\n\t" \
    "s_waitcnt lgkmcnt(3)\n\t" \
    "v_mfma_f32_16x16x32_bf16 v[72:75], a[60:63], v[48:51], v[72:75]\n\t" \
    "ds_read_b128 v[64:67], v2 offset:17408\n\t" \
    "s_waitcnt lgkmcnt(3)\n\t" \
    "v_mfma_f32_16x16x32_bf16 v[76:79], a[60:63], v[52:55], v[76:79]\n\t" \
    "ds_read_b128 v[68:71], v2 offset:35840\n\t" \
    "v_mfma_f32_16x16x32_bf16 v[80:83], a[132:135], v[48:51], v[80:83]\n\t" \
    "v_mfma_f32_16x16x32_bf16 v[84:87], a[132:135], v[52:55], v[84:87]\n\t" \
    "s_waitcnt lgkmcnt(3)\n\t" \
    "v_mfma_f32_16x16x32_bf16 v[72:75], a[64:67], v[56:59], v[72:75]\n\t" \
    "s_waitcnt lgkmcnt(2)\n\t" \
    "v_mfma_f32_16x16x32_bf16 v[76:79], a[64:67], v[60:63], v[76:79]\n\t" \
    "v_mfma_f32_16x16x32_bf16 v[80:83], a[136:139], v[56:59], v[80:83]\n\t" \
    "v_mfma_f32_16x16x32_bf16 v[84:87], a[136:139], v[60:63], v[84:87]\n\t" \
    "s_waitcnt lgkmcnt(1)\n\t" \
    "v_mfma_f32_16x16x32_bf16 v[72:75], a[68:71], v[64:67], v[72:75]\n\t" \
    "s_waitcnt lgkmcnt(0)\n\t" \
    "v_mfma_f32_16x16x32_bf16 v[76:79], a[68:71], v[68:71], v[76:79]\n\t" \
    "v_mfma_f32_16x16x32_bf16 v[80:83], a[140:143], v[64:67], v[80:83]\n\t" \
    "v_mfma_f32_16x16x32_bf16 v[84:87], a[140:143], v[68:71], v[84:87]\n\t" \
    "s_waitcnt vmcnt(9) lgkmcnt(0)\n\t" \
    "s_barrier\n\t" \
    "ds_read_b64_tr_b16 a[156:157], v204 offset:36864\n\t" \
    "ds_read_b64_tr_b16 a[158:159], v204 offset:55296\n\t" \
    "ds_read_b64_tr_b16 a[172:173], v205 offset:36864\n\t" \
    "ds_read_b64_tr_b16 a[174:175], v205 offset:55296\n\t" \
    "ds_read_b64_tr_b16 a[188:189], v206 offset:36864\n\t" \
    "ds_read_b64_tr_b16 a[190:191], v206 offset:55296\n\t" \
    "s_waitcnt lgkmcnt(6)\n\t" \
    "ds_read_b64_tr_b16 a[204:205], v207 offset:36864\n\t" \
    "ds_read_b64_tr_b16 a[206:207], v207 offset:55296\n\t" \
    "ds_read_b64_tr_b16 a[220:221], v204 offset:37888\n\t" \
    "ds_read_b64_tr_b16 a[222:223], v204 offset:56320\n\t" \
    "ds_read_b64_tr_b16 a[236:237], v205 offset:37888\n\t" \
    "ds_read_b64_tr_b16 a[238:239], v205 offset:56320\n\t" \
    "s_waitcnt lgkmcnt(6)\n\t" \
    "ds_read_b64_tr_b16 a[252:253], v206 offset:37888\n\t" \
    "ds_read_b64_tr_b16 a[254:255], v206 offset:56320\n\t" \
    "ds_read_b64_tr_b16 v[236:237], v207 offset:37888\n\t" \
    "ds_read_b64_tr_b16 v[238:239], v207 offset:56320\n\t" \
    "ds_read_b64_tr_b16 v[252:253], v204 offset:38912\n\t" \
    "ds_read_b64_tr_b16 v[254:255], v204 offset:57344\n\t" \
    "s_waitcnt lgkmcnt(6)\n\t" \
    "s_waitcnt lgkmcnt(0)\n\t" \
    "s_barrier\n\t" \
    "v_sub_u32 v166, v166, v165\n\t" \
    "v_cmp_ne_u32 vcc, 1, v166\n\t" \
    "s_cbranch_vccz gv_Cok2_%=\n\t" \
    "gv_C1_%=:\n\t" \
    "global_load_dwordx2 v[166:167], v164, s[36:37] sc1\n\t" \
    "s_waitcnt vmcnt(0)\n\t" \
    "v_sub_u32 v166, v166, v165\n\t" \
    "v_cmp_ne_u32 vcc, 1, v166\n\t" \
    "s_cbranch_vccz gv_Cok2_%=\n\t" \
    "s_cmp_eq_u32 s68, 0\n\t" \
    "s_cbranch_scc1 gv_F3_%=\n\t" \
    "s_sub_u32 s68, s68, 1\n\t" \
    "s_sleep 4\n\t" \
    "s_branch gv_C1_%=\n\t" \
    "gv_F3_%=:\n\t" \
    "v_mov_b32 v168, 6095104\n\t" \
    "v_mov_b32 v169, 4\n\t" \
    "global_store_dword v168, v169, s[36:37] sc1\n\t" \
    "s_waitcnt vmcnt(0)\n\t" \
    "s_branch gv_Cok2_%=\n\t" \
    "gv_Cok2_%=:\n\t" \
    "s_cmp_lg_u32 s66, 0\n\t" \
    "s_cbranch_scc1 gv_Ca4_%=\n\t" \
    "v_mov_b32 v164, 6176768\n\t" \
    "s_mov_b64 exec, 1\n\t" \
    "global_atomic_add v164, v166, s[36:37] sc1\n\t" \
    "s_mov_b64 exec, -1\n\t" \
    "gv_Ca4_%=:\n\t" \
    "v_min_u32_dpp v167, v167, v167 row_shl:6 row_mask:0xf bank_mask:0xf\n\t" \
    "s_nop 0\n\t" \
    "v_readlane_b32 s69, v167, 4\n\t" \
    "v_readlane_b32 s70, v167, 5\n\t" \
    "s_nop 1\n\t" \
    "s_cmp_lt_u32 s70, s69\n\t" \
    "s_cselect_b32 s52, 8192, 0\n\t" \
    "s_xor_b32 s52, s52, 8192\n\t" \
    "v_xor_b32 v20, s52, v20\n\t" \
    "s_lshr_b32 s69, s67, 5\n\t" \
    "s_or_b32 s69, s69, s66\n\t" \
    "s_cbranch_scc1 gv_npr5_%=\n\t" \
    "s_memrealtime s[54:55]\n\t" \
    "s_waitcnt lgkmcnt(0)\n\t" \
    "s_lshl_b32 s70, s54, 4\n\t" \
    "s_add_u32 s70, s70, 4160\n\t" \
    "v_mov_b32 v157, s70\n\t" \
    "global_store_dword v19, v157, s[36:37] sc1\n\t" \
    "gv_npr5_%=:\n\t" \
    "s_cmp_lt_u32 s65, 60\n\t" \
    "s_cselect_b32 s69, 73728, 0\n\t" \
    "s_add_u32 s50, s50, s69\n\t" \
    "s_addc_u32 s51, s51, 0\n\t" \
    "s_sub_u32 s90, s90, 36864\n\t" \
    "s_cselect_b32 s90, 110592, s90\n\t" \
    "s_sub_u32 s91, s90, 36864\n\t" \
    "s_cselect_b32 s91, 110592, s91\n\t" \
    "s_add_u32 s91, s91, s89\n\t" \
    "v_add_u32 v17, s90, v3\n\t" \
    "s_lshl_b32 s40, s65, 15\n\t" \
    "s_branch gv_mid8_%=\n\t" \
    ".p2align 6\n\t" \
    "s_nop 0\n\t" \
    "s_nop 0\n\t" \
    "gv_loop6_%=:\n\t" \
    "s_cmp_lt_u32 s65, 60\n\t" \
    "s_cselect_b32 s69, 73728, 0\n\t" \
    "s_add_u32 s50, s50, s69\n\t" \
    "s_addc_u32 s51, s51, 0\n\t" \
    "s_sub_u32 s90, s90, 36864\n\t" \
    "s_cselect_b32 s90, 110592, s90\n\t" \
    "s_sub_u32 s91, s90, 36864\n\t" \
    "s_cselect_b32 s91, 110592, s91\n\t" \
    "s_add_u32 s91, s91, s89\n\t" \
    "v_add_u32 v17, s90, v3\n\t" \
    "s_lshl_b32 s40, s65, 15\n\t" \
    "s_waitcnt lgkmcnt(5)\n\t" \
    "v_mfma_f32_16x16x32_bf16 v[72:75], a[0:3], v[24:27], 0\n\t" \
    "ds_read_b128 v[48:51], v2 offset:3072\n\t" \
    "s_waitcnt lgkmcnt(5)\n\t" \
    "v_mfma_f32_16x16x32_bf16 v[76:79], a[0:3], v[28:31], 0\n\t" \
    "ds_read_b128 v[52:55], v2 offset:21504\n\t" \
    "v_mfma_f32_16x16x32_bf16 v[80:83], a[72:75], v[24:27], 0\n\t" \
    "s_add_u32 m0, s91, 0\n\t" \
    "v_mfma_f32_16x16x32_bf16 v[84:87], a[72:75], v[28:31], 0\n\t" \
    "global_load_lds_dwordx4 v1, s[50:51] offset:0\n\t" \
    "s_waitcnt lgkmcnt(5)\n\t" \
    "v_mfma_f32_16x16x32_bf16 v[72:75], a[4:7], v[32:35], v[72:75]\n\t" \
    "ds_read_b128 v[56:59], v2 offset:4096\n\t" \
    "s_waitcnt lgkmcnt(5)\n\t" \
    "v_mfma_f32_16x16x32_bf16 v[76:79], a[4:7], v[36:39], v[76:79]\n\t" \
    "ds_read_b128 v[60:63], v2 offset:22528\n\t" \
    "v_mfma_f32_16x16x32_bf16 v[80:83], a[76:79], v[32:35], v[80:83]\n\t" \
    "s_add_u32 m0, s91, 960\n\t" \
    "v_mfma_f32_16x16x32_bf16 v[84:87], a[76:79], v[36:39], v[84:87]\n\t" \
    "global_load_lds_dwordx4 v1, s[50:51] offset:64\n\t" \
    "s_waitcnt lgkmcnt(5)\n\t" \
    "v_mfma_f32_16x16x32_bf16 v[72:75], a[8:11], v[40:43], v[72:75]\n\t" \
    "ds_read_b128 v[64:67], v2 offset:5120\n\t" \
    "s_waitcnt lgkmcnt(5)\n\t" \
    "v_mfma_f32_16x16x32_bf16 v[76:79], a[8:11], v[44:47], v[76:79]\n\t" \
    "ds_read_b128 v[68:71], v2 offset:23552\n\t" \
    "v_mfma_f32_16x16x32_bf16 v[80:83], a[80:83], v[40:43], v[80:83]\n\t" \
    "s_add_u32 m0, s91, 1920\n\t" \
    "v_mfma_f32_16x16x32_bf16 v[84:87], a[80:83], v[44:47], v[84:87]\n\t" \
    "global_load_lds_dwordx4 v1, s[50:51] offset:128\n\t" \
    "s_waitcnt lgkmcnt(5)\n\t" \
    "v_mfma_f32_16x16x32_bf16 v[72:75], a[12:15], v[48:51], v[72:75]\n\t" \
    "ds_read_b128 v[24:27], v2 offset:6144\n\t" \
    "s_waitcnt lgkmcnt(5)\n\t" \
    "v_mfma_f32_16x16x32_bf16 v[76:79], a[12:15], v[52:55], v[76:79]\n\t" \
    "ds_read_b128 v[28:31], v2 offset:24576\n\t" \
    "v_mfma_f32_16x16x32_bf16 v[80:83], a[84:87], v[48:51], v[80:83]\n\t" \
    "s_add_u32 m0, s91, 2880\n\t" \
    "v_mfma_f32_16x16x32_bf16 v[84:87], a[84:87], v[52:55], v[84:87]\n\t" \
    "global_load_lds_dwordx4 v1, s[50:51] offset:192\n\t" \
    "s_waitcnt lgkmcnt(5)\n\t" \
    "v_mfma_f32_16x16x32_bf16 v[72:75], a[16:19], v[56:59], v[72:75]\n\t" \
    "ds_read_b128 v[32:35], v2 offset:7168\n\t" \
    "s_waitcnt lgkmcnt(5)\n\t" \
    "v_mfma_f32_16x16x32_bf16 v[76:79], a[16:19], v[60:63], v[76:79]\n\t" \
    "ds_read_b128 v[36:39], v2 offset:25600\n\t" \
    "v_mfma_f32_16x16x32_bf16 v[80:83], a[88:91], v[56:59], v[80:83]\n\t" \
    "s_add_u32 m0, s91, 3840\n\t" \
    "v_mfma_f32_16x16x32_bf16 v[84:87], a[88:91], v[60:63], v[84:87]\n\t" \
    "global_load_lds_dwordx4 v1, s[50:51] offset:256\n\t" \
    "s_waitcnt lgkmcnt(5)\n\t" \
    "v_mfma_f32_16x16x32_bf16 v[72:75], a[20:23], v[64:67], v[72:75]\n\t" \
    "ds_read_b128 v[40:43], v2 offset:8192\n\t" \
    "s_waitcnt lgkmcnt(5)\n\t" \
    "v_mfma_f32_16x16x32_bf16 v[76:79], a[20:23], v[68:71], v[76:79]\n\t" \
    "ds_read_b128 v[44:47], v2 offset:26624\n\t" \
    "v_mfma_f32_16x16x32_bf16 v[80:83], a[92:95], v[64:67], v[80:83]\n\t" \
    "s_add_u32 m0, s91, 4800\n\t" \
    "v_mfma_f32_16x16x32_bf16 v[84:87], a[92:95], v[68:71], v[84:87]\n\t" \
    "global_load_lds_dwordx4 v1, s[50:51] offset:320\n\t" \
    "s_waitcnt lgkmcnt(5)\n\t" \
    "v_mfma_f32_16x16x32_bf16 v[72:75], a[24:27], v[24:27], v[72:75]\n\t" \
    "ds_read_b128 v[48:51], v2 offset:9216\n\t" \
    "s_waitcnt lgkmcnt(5)\n\t" \
    "v_mfma_f32_16x16x32_bf16 v[76:79], a[24:27], v[28:31], v[76:79]\n\t" \
    "ds_read_b128 v[52:55], v2 offset:27648\n\t" \
    "v_mfma_f32_16x16x32_bf16 v[80:83], a[96:99], v[24:27], v[80:83]\n\t" \
    "s_add_u32 m0, s91, 5760\n\t" \
    "v_mfma_f32_16x16x32_bf16 v[84:87], a[96:99], v[28:31], v[84:87]\n\t" \
    "global_load_lds_dwordx4 v1, s[50:51] offset:384\n\t" \
    "s_waitcnt lgkmcnt(5)\n\t" \
    "v_mfma_f32_16x16x32_bf16 v[72:75], a[28:31], v[32:35], v[72:75]\n\t" \
    "ds_read_b128 v[56:59], v2 offset:10240\n\t" \
    "s_waitcnt lgkmcnt(5)\n\t" \
    "v_mfma_f32_16x16x32_bf16 v[76:79], a[28:31], v[36:39], v[76:79]\n\t" \
    "ds_read_b128 v[60:63], v2 offset:28672\n\t" \
    "v_mfma_f32_16x16x32_bf16 v[80:83], a[100:103], v[32:35], v[80:83]\n\t" \
    "s_add_u32 m0, s91, 6720\n\t" \
    "v_mfma_f32_16x16x32_bf16 v[84:87], a[100:103], v[36:39], v[84:87]\n\t" \
    "global_load_lds_dwordx4 v1, s[50:51] offset:448\n\t" \
    "s_waitcnt lgkmcnt(5)\n\t" \
    "v_mfma_f32_16x16x32_bf16 v[72:75], a[32:35], v[40:43], v[72:75]\n\t" \
    "ds_read_b128 v[64:67], v2 offset:11264\n\t" \
    "s_waitcnt lgkmcnt(5)\n\t" \
    "v_mfma_f32_16x16x32_bf16 v[76:79], a[32:35], v[44:47], v[76:79]\n\t" \
    "ds_read_b128 v[68:71], v2 offset:29696\n\t" \
    "v_mfma_f32_16x16x32_bf16 v[80:83], a[104:107], v[40:43], v[80:83]\n\t" \
    "s_add_u32 m0, s91, 7680\n\t" \
    "v_mfma_f32_16x16x32_bf16 v[84:87], a[104:107], v[44:47], v[84:87]\n\t" \
    "global_load_lds_dwordx4 v1, s[50:51] offset:512\n\t" \
    "s_waitcnt lgkmcnt(5)\n\t" \
    "v_mfma_f32_16x16x32_bf16 v[72:75], a[36:39], v[48:51], v[72:75]\n\t" \
    "ds_read_b128 v[24:27], v2 offset:12288\n\t" \
    "s_waitcnt lgkmcnt(5)\n\t" \
    "v_mfma_f32_16x16x32_bf16 v[76:79], a[36:39], v[52:55], v[76:79]\n\t" \
    "ds_read_b128 v[28:31], v2 offset:30720\n\t" \
    "v_mfma_f32_16x16x32_bf16 v[80:83], a[108:111], v[48:51], v[80:83]\n\t" \
    "v_mfma_f32_16x16x32_bf16 v[84:87], a[108:111], v[52:55], v[84:87]\n\t" \
    "s_waitcnt lgkmcnt(5)\n\t" \
    "v_mfma_f32_16x16x32_bf16 v[72:75], a[40:43], v[56:59], v[72:75]\n\t" \
    "ds_read_b128 v[32:35], v2 offset:13312\n\t" \
    "s_waitcnt lgkmcnt(5)\n\t" \
    "v_mfma_f32_16x16x32_bf16 v[76:79], a[40:43], v[60:63], v[76:79]\n\t" \
    "ds_read_b128 v[36:39], v2 offset:31744\n\t" \
    "v_mfma_f32_16x16x32_bf16 v[80:83], a[112:115], v[56:59], v[80:83]\n\t" \
    "v_mfma_f32_16x16x32_bf16 v[84:87], a[112:115], v[60:63], v[84:87]\n\t" \
    "s_waitcnt lgkmcnt(5)\n\t" \
    "v_mfma_f32_16x16x32_bf16 v[72:75], a[44:47], v[64:67], v[72:75]\n\t" \
    "ds_read_b128 v[40:43], v2 offset:14336\n\t" \
    "s_waitcnt lgkmcnt(5)\n\t" \
    "v_mfma_f32_16x16x32_bf16 v[76:79], a[44:47], v[68:71], v[76:79]\n\t" \
    "ds_read_b128 v[44:47], v2 offset:32768\n\t" \
    "v_mfma_f32_16x16x32_bf16 v[80:83], a[116:119], v[64:67], v[80:83]\n\t" \
    "v_mfma_f32_16x16x32_bf16 v[84:87], a[116:119], v[68:71], v[84:87]\n\t" \
    "s_waitcnt lgkmcnt(5)\n\t" \
    "v_mfma_f32_16x16x32_bf16 v[72:75], a[48:51], v[24:27], v[72:75]\n\t" \
    "ds_read_b128 v[48:51], v2 offset:15360\n\t" \
    "s_waitcnt lgkmcnt(5)\n\t" \
    "v_mfma_f32_16x16x32_bf16 v[76:79], a[48:51], v[28:31], v[76:79]\n\t" \
    "ds_read_b128 v[52:55], v2 offset:33792\n\t" \
    "v_mfma_f32_16x16x32_bf16 v[80:83], a[120:123], v[24:27], v[80:83]\n\t" \
    "v_mfma_f32_16x16x32_bf16 v[84:87], a[120:123], v[28:31], v[84:87]\n\t" \
    "s_waitcnt lgkmcnt(5)\n\t" \
    "v_mfma_f32_16x16x32_bf16 v[72:75], a[52:55], v[32:35], v[72:75]\n\t" \
    "ds_read_b128 v[56:59], v2 offset:16384\n\t" \
    "s_waitcnt lgkmcnt(5)\n\t" \
    "v_mfma_f32_16x16x32_bf16 v[76:79], a[52:55], v[36:39], v[76:79]\n\t" \
    "ds_read_b128 v[60:63], v2 offset:34816\n\t" \
    "v_mfma_f32_16x16x32_bf16 v[80:83], a[124:127], v[32:35], v[80:83]\n\t" \
    "v_mfma_f32_16x16x32_bf16 v[84:87], a[124:127], v[36:39], v[84:87]\n\t" \
    "s_waitcnt lgkmcnt(5)\n\t" \
    "v_mfma_f32_16x16x32_bf16 v[72:75], a[56:59], v[40:43], v[72:75]\n\t" \
    "ds_read_b128 v[64:67], v2 offset:17408\n\t" \
    "s_waitcnt lgkmcnt(5)\n\t" \
    "v_mfma_f32_16x16x32_bf16 v[76:79], a[56:59], v[44:47], v[76:79]\n\t" \
    "ds_read_b128 v[68:71], v2 offset:35840\n\t" \
    "v_mfma_f32_16x16x32_bf16 v[80:83], a[128:131], v[40:43], v[80:83]\n\t" \
    "v_mfma_f32_16x16x32_bf16 v[84:87], a[128:131], v[44:47], v[84:87]\n\t" \
    "s_waitcnt lgkmcnt(5)\n\t" \
    "v_mfma_f32_16x16x32_bf16 v[72:75], a[60:63], v[48:51], v[72:75]\n\t" \
    "s_waitcnt lgkmcnt(4)\n\t" \
    "v_mfma_f32_16x16x32_bf16 v[76:79], a[60:63], v[52:55], v[76:79]\n\t" \
    "v_mfma_f32_16x16x32_bf16 v[80:83], a[132:135], v[48:51], v[80:83]\n\t" \
    "v_mfma_f32_16x16x32_bf16 v[84:87], a[132:135], v[52:55], v[84:87]\n\t" \
    "s_waitcnt lgkmcnt(3)\n\t" \
    "v_mfma_f32_16x16x32_bf16 v[72:75], a[64:67], v[56:59], v[72:75]\n\t" \
    "s_waitcnt lgkmcnt(2)\n\t" \
    "v_mfma_f32_16x16x32_bf16 v[76:79], a[64:67], v[60:63], v[76:79]\n\t" \
    "v_mfma_f32_16x16x32_bf16 v[80:83], a[136:139], v[56:59], v[80:83]\n\t" \
    "v_mfma_f32_16x16x32_bf16 v[84:87], a[136:139], v[60:63], v[84:87]\n\t" \
    "s_waitcnt lgkmcnt(1)\n\t" \
    "v_mfma_f32_16x16x32_bf16 v[72:75], a[68:71], v[64:67], v[72:75]\n\t" \
    "s_waitcnt lgkmcnt(0)\n\t" \
    "v_mfma_f32_16x16x32_bf16 v[76:79], a[68:71], v[68:71], v[76:79]\n\t" \
    "v_mfma_f32_16x16x32_bf16 v[80:83], a[140:143], v[64:67], v[80:83]\n\t" \
    "v_mfma_f32_16x16x32_bf16 v[84:87], a[140:143], v[68:71], v[84:87]\n\t" \
    "gv_mid8_%=:\n\t" \
    "v_mov_b32 v2, v17\n\t" \
    "s_nop 2\n\t" \
    "v_cvt_pk_bf16_f32 v104, v72, v73\n\t" \
    "v_cvt_pk_bf16_f32 v105, v74, v75\n\t" \
    "s_nop 0\n\t" \
    "v_cvt_pk_bf16_f32 v106, v80, v81\n\t" \
    "v_cvt_pk_bf16_f32 v107, v82, v83\n\t" \
    "ds_write_b128 v4, v[104:107] offset:0\n\t" \
    "v_cvt_pk_bf16_f32 v108, v76, v77\n\t" \
    "v_cvt_pk_bf16_f32 v109, v78, v79\n\t" \
    "v_cvt_pk_bf16_f32 v110, v84, v85\n\t" \
    "v_cvt_pk_bf16_f32 v111, v86, v87\n\t" \
    "ds_write_b128 v4, v[108:111] offset:1024\n\t" \
    "s_waitcnt vmcnt(18) lgkmcnt(0)\n\t" \
    "s_barrier\n\t" \
    "ds_read_b128 v[164:167], v5 offset:0\n\t" \
    "ds_read_b128 v[168:171], v5 offset:1024\n\t" \
    "ds_read_b128 v[172:175], v5 offset:2048\n\t" \
    "ds_read_b128 v[176:179], v5 offset:3072\n\t" \
    "ds_read_b128 v[180:183], v5 offset:4096\n\t" \
    "ds_read_b128 v[184:187], v5 offset:5120\n\t" \
    "ds_read_b128 v[188:191], v5 offset:6144\n\t" \
    "ds_read_b128 v[192:195], v5 offset:7168\n\t" \
    "s_cmp_lt_u32 s65, 10\n\t" \
    "s_cbranch_scc1 gv_fl9_%=\n\t" \
    "s_cmp_lg_u32 s66, 0\n\t" \
    "s_cbranch_scc1 gv_SF10_%=\n\t" \
    "s_add_u32 s70, s65, -4\n\t" \
    "v_mov_b32 v157, s70\n\t" \
    "s_mov_b64 exec, 31\n\t" \
    "global_store_dword v20, v157, s[36:37] sc1\n\t" \
    "s_mov_b64 exec, -1\n\t" \
    "gv_SF10_%=:\n\t" \
    "gv_fl9_%=:\n\t" \
    "v_xor_b32 v4, 0x2000, v4\n\t" \
    "v_xor_b32 v5, 0x2000, v5\n\t" \
    "s_waitcnt lgkmcnt(7)\n\t" \
    "v_mfma_f32_16x16x32_bf16 v[72:75], v[164:167], a[144:147], 0\n\t" \
    "s_waitcnt lgkmcnt(6)\n\t" \
    "v_mfma_f32_16x16x32_bf16 v[76:79], v[168:171], a[144:147], 0\n\t" \
    "s_waitcnt lgkmcnt(5)\n\t" \
    "v_mfma_f32_16x16x32_bf16 v[72:75], v[172:175], a[148:151], v[72:75]\n\t" \
    "s_waitcnt lgkmcnt(4)\n\t" \
    "v_mfma_f32_16x16x32_bf16 v[76:79], v[176:179], a[148:151], v[76:79]\n\t" \
    "s_waitcnt lgkmcnt(3)\n\t" \
    "v_mfma_f32_16x16x32_bf16 v[72:75], v[180:183], a[152:155], v[72:75]\n\t" \
    "s_waitcnt lgkmcnt(2)\n\t" \
    "v_mfma_f32_16x16x32_bf16 v[76:79], v[184:187], a[152:155], v[76:79]\n\t" \
    "s_waitcnt lgkmcnt(1)\n\t" \
    "v_mfma_f32_16x16x32_bf16 v[72:75], v[188:191], a[156:159], v[72:75]\n\t" \
    "s_waitcnt lgkmcnt(0)\n\t" \
    "v_mfma_f32_16x16x32_bf16 v[76:79], v[192:195], a[156:159], v[76:79]\n\t" \
    "v_mfma_f32_16x16x32_bf16 v[80:83], v[164:167], a[160:163], 0\n\t" \
    "v_mfma_f32_16x16x32_bf16 v[84:87], v[168:171], a[160:163], 0\n\t" \
    "v_mfma_f32_16x16x32_bf16 v[80:83], v[172:175], a[164:167], v[80:83]\n\t" \
    "v_mfma_f32_16x16x32_bf16 v[84:87], v[176:179], a[164:167], v[84:87]\n\t" \
    "v_mfma_f32_16x16x32_bf16 v[80:83], v[180:183], a[168:171], v[80:83]\n\t" \
    "v_mfma_f32_16x16x32_bf16 v[84:87], v[184:187], a[168:171], v[84:87]\n\t" \
    "v_cvt_pk_bf16_f32 v104, v72, v73\n\t" \
    "v_mfma_f32_16x16x32_bf16 v[80:83], v[188:191], a[172:175], v[80:83]\n\t" \
    "v_cvt_pk_bf16_f32 v105, v74, v75\n\t" \
    "v_mfma_f32_16x16x32_bf16 v[84:87], v[192:195], a[172:175], v[84:87]\n\t" \
    "v_cvt_pk_bf16_f32 v106, v76, v77\n\t" \
    "v_mfma_f32_16x16x32_bf16 v[88:91], v[164:167], a[176:179], 0\n\t" \
    "v_cvt_pk_bf16_f32 v107, v78, v79\n\t" \
    "v_mfma_f32_16x16x32_bf16 v[92:95], v[168:171], a[176:179], 0\n\t" \
    "v_xor_b32 v18, s40, v8\n\t" \
    "v_mfma_f32_16x16x32_bf16 v[88:91], v[172:175], a[180:183], v[88:91]\n\t" \
    "global_store_dwordx4 v18, v[104:107], s[38:39] sc1\n\t" \
    "v_mfma_f32_16x16x32_bf16 v[92:95], v[176:179], a[180:183], v[92:95]\n\t" \
    "v_mfma_f32_16x16x32_bf16 v[88:91], v[180:183], a[184:187], v[88:91]\n\t" \
    "v_mfma_f32_16x16x32_bf16 v[92:95], v[184:187], a[184:187], v[92:95]\n\t" \
    "v_cvt_pk_bf16_f32 v108, v80, v81\n\t" \
    "v_mfma_f32_16x16x32_bf16 v[88:91], v[188:191], a[188:191], v[88:91]\n\t" \
    "v_cvt_pk_bf16_f32 v109, v82, v83\n\t" \
    "v_mfma_f32_16x16x32_bf16 v[92:95], v[192:195], a[188:191], v[92:95]\n\t" \
    "v_cvt_pk_bf16_f32 v110, v84, v85\n\t" \
    "v_mfma_f32_16x16x32_bf16 v[72:75], v[164:167], a[192:195], 0\n\t" \
    "v_cvt_pk_bf16_f32 v111, v86, v87\n\t" \
    "v_mfma_f32_16x16x32_bf16 v[76:79], v[168:171], a[192:195], 0\n\t" \
    "v_xor_b32 v18, s40, v9\n\t" \
    "v_mfma_f32_16x16x32_bf16 v[72:75], v[172:175], a[196:199], v[72:75]\n\t" \
    "global_store_dwordx4 v18, v[108:111], s[38:39] sc1\n\t" \
    "v_mfma_f32_16x16x32_bf16 v[76:79], v[176:179], a[196:199], v[76:79]\n\t" \
    "v_mfma_f32_16x16x32_bf16 v[72:75], v[180:183], a[200:203], v[72:75]\n\t" \
    "v_mfma_f32_16x16x32_bf16 v[76:79], v[184:187], a[200:203], v[76:79]\n\t" \
    "v_cvt_pk_bf16_f32 v104, v88, v89\n\t" \
    "v_mfma_f32_16x16x32_bf16 v[72:75], v[188:191], a[204:207], v[72:75]\n\t" \
    "v_cvt_pk_bf16_f32 v105, v90, v91\n\t" \
    "v_mfma_f32_16x16x32_bf16 v[76:79], v[192:195], a[204:207], v[76:79]\n\t" \
    "v_cvt_pk_bf16_f32 v106, v92, v93\n\t" \
    "v_mfma_f32_16x16x32_bf16 v[80:83], v[164:167], a[208:211], 0\n\t" \
    "v_cvt_pk_bf16_f32 v107, v94, v95\n\t" \
    "v_mfma_f32_16x16x32_bf16 v[84:87], v[168:171], a[208:211], 0\n\t" \
    "v_xor_b32 v18, s40, v10\n\t" \
    "v_mfma_f32_16x16x32_bf16 v[80:83], v[172:175], a[212:215], v[80:83]\n\t" \
    "global_store_dwordx4 v18, v[104:107], s[38:39] sc1\n\t" \
    "v_mfma_f32_16x16x32_bf16 v[84:87], v[176:179], a[212:215], v[84:87]\n\t" \
    "v_mfma_f32_16x16x32_bf16 v[80:83], v[180:183], a[216:219], v[80:83]\n\t" \
    "v_mfma_f32_16x16x32_bf16 v[84:87], v[184:187], a[216:219], v[84:87]\n\t" \
    "v_cvt_pk_bf16_f32 v108, v72, v73\n\t" \
    "v_mfma_f32_16x16x32_bf16 v[80:83], v[188:191], a[220:223], v[80:83]\n\t" \
    "v_cvt_pk_bf16_f32 v109, v74, v75\n\t" \
    "v_mfma_f32_16x16x32_bf16 v[84:87], v[192:195], a[220:223], v[84:87]\n\t" \
    "v_cvt_pk_bf16_f32 v110, v76, v77\n\t" \
    "v_mfma_f32_16x16x32_bf16 v[88:91], v[164:167], a[224:227], 0\n\t" \
    "v_cvt_pk_bf16_f32 v111, v78, v79\n\t" \
    "v_mfma_f32_16x16x32_bf16 v[92:95], v[168:171], a[224:227], 0\n\t" \
    "v_xor_b32 v18, s40, v11\n\t" \
    "v_mfma_f32_16x16x32_bf16 v[88:91], v[172:175], a[228:231], v[88:91]\n\t" \
    "global_store_dwordx4 v18, v[108:111], s[38:39] sc1\n\t" \
    "v_mfma_f32_16x16x32_bf16 v[92:95], v[176:179], a[228:231], v[92:95]\n\t" \
    "v_mfma_f32_16x16x32_bf16 v[88:91], v[180:183], a[232:235], v[88:91]\n\t" \
    "v_mfma_f32_16x16x32_bf16 v[92:95], v[184:187], a[232:235], v[92:95]\n\t" \
    "v_cvt_pk_bf16_f32 v104, v80, v81\n\t" \
    "v_mfma_f32_16x16x32_bf16 v[88:91], v[188:191], a[236:239], v[88:91]\n\t" \
    "v_cvt_pk_bf16_f32 v105, v82, v83\n\t" \
    "v_mfma_f32_16x16x32_bf16 v[92:95], v[192:195], a[236:239], v[92:95]\n\t" \
    "v_cvt_pk_bf16_f32 v106, v84, v85\n\t" \
    "v_mfma_f32_16x16x32_bf16 v[72:75], v[164:167], a[240:243], 0\n\t" \
    "v_cvt_pk_bf16_f32 v107, v86, v87\n\t" \
    "v_mfma_f32_16x16x32_bf16 v[76:79], v[168:171], a[240:243], 0\n\t" \
    "v_xor_b32 v18, s40, v12\n\t" \
    "v_mfma_f32_16x16x32_bf16 v[72:75], v[172:175], a[244:247], v[72:75]\n\t" \
    "global_store_dwordx4 v18, v[104:107], s[38:39] sc1\n\t" \
    "v_mfma_f32_16x16x32_bf16 v[76:79], v[176:179], a[244:247], v[76:79]\n\t" \
    "v_mfma_f32_16x16x32_bf16 v[72:75], v[180:183], a[248:251], v[72:75]\n\t" \
    "v_mfma_f32_16x16x32_bf16 v[76:79], v[184:187], a[248:251], v[76:79]\n\t" \
    "v_cvt_pk_bf16_f32 v108, v88, v89\n\t" \
    "v_mfma_f32_16x16x32_bf16 v[72:75], v[188:191], a[252:255], v[72:75]\n\t" \
    "v_cvt_pk_bf16_f32 v109, v90, v91\n\t" \
    "v_mfma_f32_16x16x32_bf16 v[76:79], v[192:195], a[252:255], v[76:79]\n\t" \
    "v_cvt_pk_bf16_f32 v110, v92, v93\n\t" \
    "v_mfma_f32_16x16x32_bf16 v[80:83], v[164:167], v[224:227], 0\n\t" \
    "v_cvt_pk_bf16_f32 v111, v94, v95\n\t" \
    "v_mfma_f32_16x16x32_bf16 v[84:87], v[168:171], v[224:227], 0\n\t" \
    "v_xor_b32 v18, s40, v13\n\t" \
    "v_mfma_f32_16x16x32_bf16 v[80:83], v[172:175], v[228:231], v[80:83]\n\t" \
    "global_store_dwordx4 v18, v[108:111], s[38:39] sc1\n\t" \
    "v_mfma_f32_16x16x32_bf16 v[84:87], v[176:179], v[228:231], v[84:87]\n\t" \
    "v_mfma_f32_16x16x32_bf16 v[80:83], v[180:183], v[232:235], v[80:83]\n\t" \
    "v_mfma_f32_16x16x32_bf16 v[84:87], v[184:187], v[232:235], v[84:87]\n\t" \
    "v_cvt_pk_bf16_f32 v104, v72, v73\n\t" \
    "v_mfma_f32_16x16x32_bf16 v[80:83], v[188:191], v[236:239], v[80:83]\n\t" \
    "v_cvt_pk_bf16_f32 v105, v74, v75\n\t" \
    "v_mfma_f32_16x16x32_bf16 v[84:87], v[192:195], v[236:239], v[84:87]\n\t" \
    "v_cvt_pk_bf16_f32 v106, v76, v77\n\t" \
    "s_cmp_ge_u32 s65, 4\n\t" \
    "s_cbranch_scc1 gv_f1st11_%=\n\t" \
    "s_waitcnt vmcnt(6)\n\t" \
    "s_barrier\n\t" \
    "gv_f1st11_%=:\n\t" \
    "v_mfma_f32_16x16x32_bf16 v[88:91], v[164:167], v[240:243], 0\n\t" \
    "v_cvt_pk_bf16_f32 v107, v78, v79\n\t" \
    "ds_read_b128 v[24:27], v17 offset:0\n\t" \
    "v_mfma_f32_16x16x32_bf16 v[92:95], v[168:171], v[240:243], 0\n\t" \
    "v_xor_b32 v18, s40, v14\n\t" \
    "ds_read_b128 v[28:31], v17 offset:18432\n\t" \
    "v_mfma_f32_16x16x32_bf16 v[88:91], v[172:175], v[244:247], v[88:91]\n\t" \
    "global_store_dwordx4 v18, v[104:107], s[38:39] sc1\n\t" \
    "ds_read_b128 v[32:35], v17 offset:1024\n\t" \
    "v_mfma_f32_16x16x32_bf16 v[92:95], v[176:179], v[244:247], v[92:95]\n\t" \
    "ds_read_b128 v[36:39], v17 offset:19456\n\t" \
    "v_mfma_f32_16x16x32_bf16 v[88:91], v[180:183], v[248:251], v[88:91]\n\t" \
    "ds_read_b128 v[40:43], v17 offset:2048\n\t" \
    "v_mfma_f32_16x16x32_bf16 v[92:95], v[184:187], v[248:251], v[92:95]\n\t" \
    "v_cvt_pk_bf16_f32 v108, v80, v81\n\t" \
    "ds_read_b128 v[44:47], v17 offset:20480\n\t" \
    "v_mfma_f32_16x16x32_bf16 v[88:91], v[188:191], v[252:255], v[88:91]\n\t" \
    "v_cvt_pk_bf16_f32 v109, v82, v83\n\t" \
    "v_mfma_f32_16x16x32_bf16 v[92:95], v[192:195], v[252:255], v[92:95]\n\t" \
    "v_cvt_pk_bf16_f32 v110, v84, v85\n\t" \
    "v_cvt_pk_bf16_f32 v111, v86, v87\n\t" \
    "v_xor_b32 v18, s40, v15\n\t" \
    "global_store_dwordx4 v18, v[108:111], s[38:39] sc1\n\t" \
    "s_nop 1\n\t" \
    "v_cvt_pk_bf16_f32 v104, v88, v89\n\t" \
    "v_cvt_pk_bf16_f32 v105, v90, v91\n\t" \
    "v_cvt_pk_bf16_f32 v106, v92, v93\n\t" \
    "v_cvt_pk_bf16_f32 v107, v94, v95\n\t" \
    "v_xor_b32 v18, s40, v16\n\t" \
    "global_store_dwordx4 v18, v[104:107], s[38:39] sc1\n\t" \
    "s_sub_u32 s69, s65, 6\n\t" \
    "s_cmp_lt_u32 s69, 56\n\t" \
    "s_cbranch_scc1 gv_nofast7_%=\n\t" \
    "s_waitcnt vmcnt(0)\n\t" \
    "s_barrier\n\t" \
    "s_cmp_lg_u32 s66, 0\n\t" \
    "s_cbranch_scc1 gv_SF12_%=\n\t" \
    "s_add_u32 s70, s65, 0\n\t" \
    "v_mov_b32 v157, s70\n\t" \
    "s_mov_b64 exec, 31\n\t" \
    "global_store_dword v20, v157, s[36:37] sc1\n\t" \
    "s_mov_b64 exec, -1\n\t" \
    "gv_SF12_%=:\n\t" \
    "s_cmp_ge_u32 s65, 2\n\t" \
    "s_cbranch_scc1 gv_nofast7_%=\n\t" \
    "s_add_u32 m0, s91, 0\n\t" \
    "s_nop 0\n\t" \
    "global_load_lds_dwordx4 v1, s[50:51] offset:0\n\t" \
    "s_add_u32 m0, s91, 960\n\t" \
    "s_nop 0\n\t" \
    "global_load_lds_dwordx4 v1, s[50:51] offset:64\n\t" \
    "s_add_u32 m0, s91, 1920\n\t" \
    "s_nop 0\n\t" \
    "global_load_lds_dwordx4 v1, s[50:51] offset:128\n\t" \
    "s_add_u32 m0, s91, 2880\n\t" \
    "s_nop 0\n\t" \
    "global_load_lds_dwordx4 v1, s[50:51] offset:192\n\t" \
    "s_add_u32 m0, s91, 3840\n\t" \
    "s_nop 0\n\t" \
    "global_load_lds_dwordx4 v1, s[50:51] offset:256\n\t" \
    "s_add_u32 m0, s91, 4800\n\t" \
    "s_nop 0\n\t" \
    "global_load_lds_dwordx4 v1, s[50:51] offset:320\n\t" \
    "s_add_u32 m0, s91, 5760\n\t" \
    "s_nop 0\n\t" \
    "global_load_lds_dwordx4 v1, s[50:51] offset:384\n\t" \
    "s_add_u32 m0, s91, 6720\n\t" \
    "s_nop 0\n\t" \
    "global_load_lds_dwordx4 v1, s[50:51] offset:448\n\t" \
    "s_add_u32 m0, s91, 7680\n\t" \
    "s_nop 0\n\t" \
    "global_load_lds_dwordx4 v1, s[50:51] offset:512\n\t" \
    "gv_nofast7_%=:\n\t" \
    "s_add_u32 s65, s65, 2\n\t" \
    "s_cmp_lt_u32 s65, 64\n\t" \
    "s_cbranch_scc1 gv_loop6_%=\n\t" \
    "s_waitcnt vmcnt(0) lgkmcnt(0)\n\t" \
    "s_barrier\n\t" \
    "ea_done_%=:\n\t" \
    "s_waitcnt lgkmcnt(0)\n\t" \
    "ea_last_%=:\n\t" \
    ""
#define ASM_R \
    "s_mov_b64 s[36:37], %0\n\t" \
    "s_mov_b64 s[42:43], %1\n\t" \
    "s_mov_b64 s[44:45], %2\n\t" \
    "s_mov_b32 s66, %3\n\t" \
    "s_mov_b32 s67, %4\n\t" \
    "s_mov_b64 s[50:51], %5\n\t" \
    "s_mov_b32 s82, %7\n\t" \
    "s_load_dwordx8 s[84:91], %6, 0\n\t" \
    "v_mbcnt_lo_u32_b32 v223, -1, 0\n\t" \
    "v_mbcnt_hi_u32_b32 v223, -1, v223\n\t" \
    "s_getpc_b64 s[46:47]\n\t" \
    "rr_pc_%=:\n\t" \
    "s_lshl_b32 s69, s66, 13\n\t" \
    "v_lshl_add_u32 v222, v223, 7, s69\n\t" \
    "v_min_u32 v222, rr_last_%=-rr_pc_%=-4, v222\n\t" \
    "global_load_dword v222, v222, s[46:47]\n\t" \
    "v_lshlrev_b32 v223, 2, v223\n\t" \
    "v_add_u32 v223, s82, v223\n\t" \
    "ds_read_b32 v0, v223 offset:0\n\t" \
    "ds_read_b32 v1, v223 offset:256\n\t" \
    "ds_read_b32 v2, v223 offset:512\n\t" \
    "ds_read_b32 v3, v223 offset:768\n\t" \
    "ds_read_b32 v4, v223 offset:1024\n\t" \
    "ds_read_b32 v5, v223 offset:1280\n\t" \
    "ds_read_b32 v6, v223 offset:1536\n\t" \
    "ds_read_b32 v7, v223 offset:1792\n\t" \
    "ds_read_b32 v8, v223 offset:2048\n\t" \
    "ds_read_b32 v9, v223 offset:2304\n\t" \
    "ds_read_b32 v10, v223 offset:2560\n\t" \
    "ds_read_b32 v11, v223 offset:2816\n\t" \
    "s_waitcnt lgkmcnt(0)\n\t" \
    "ds_read_b32 v12, v223 offset:3072\n\t" \
    "ds_read_b32 v13, v223 offset:3328\n\t" \
    "ds_read_b32 v14, v223 offset:3584\n\t" \
    "ds_read_b32 v15, v223 offset:3840\n\t" \
    "ds_read_b32 v16, v223 offset:4096\n\t" \
    "ds_read_b32 v17, v223 offset:4352\n\t" \
    "ds_read_b32 v18, v223 offset:4608\n\t" \
    "ds_read_b32 v19, v223 offset:4864\n\t" \
    "ds_read_b32 v20, v223 offset:5120\n\t" \
    "ds_read_b32 v21, v223 offset:5376\n\t" \
    "ds_read_b32 v22, v223 offset:5632\n\t" \
    "ds_read_b32 v23, v223 offset:5888\n\t" \
    "s_waitcnt lgkmcnt(0)\n\t" \
    "ds_read_b32 v24, v223 offset:6144\n\t" \
    "ds_read_b32 v25, v223 offset:6400\n\t" \
    "ds_read_b32 v26, v223 offset:6656\n\t" \
    "ds_read_b32 v27, v223 offset:6912\n\t" \
    "ds_read_b32 v28, v223 offset:7168\n\t" \
    "ds_read_b32 v29, v223 offset:7424\n\t" \
    "ds_read_b32 v30, v223 offset:7680\n\t" \
    "ds_read_b32 v31, v223 offset:7936\n\t" \
    "ds_read_b32 v32, v223 offset:8192\n\t" \
    "ds_read_b32 v33, v223 offset:8448\n\t" \
    "ds_read_b32 v34, v223 offset:8704\n\t" \
    "ds_read_b32 v35, v223 offset:8960\n\t" \
    "s_waitcnt lgkmcnt(0)\n\t" \
    "ds_read_b32 v36, v223 offset:9216\n\t" \
    "ds_read_b32 v37, v223 offset:9472\n\t" \
    "ds_read_b32 v38, v223 offset:9728\n\t" \
    "ds_read_b32 v39, v223 offset:9984\n\t" \
    "ds_read_b32 v40, v223 offset:10240\n\t" \
    "ds_read_b32 v41, v223 offset:10496\n\t" \
    "ds_read_b32 v42, v223 offset:10752\n\t" \
    "ds_read_b32 v43, v223 offset:11008\n\t" \
    "ds_read_b32 v44, v223 offset:11264\n\t" \
    "ds_read_b32 v45, v223 offset:11520\n\t" \
    "ds_read_b32 v46, v223 offset:11776\n\t" \
    "ds_read_b32 v164, v223 offset:12032\n\t" \
    "s_waitcnt lgkmcnt(0)\n\t" \
    "ds_read_b32 v165, v223 offset:12288\n\t" \
    "ds_read_b32 v166, v223 offset:12544\n\t" \
    "ds_read_b32 v167, v223 offset:12800\n\t" \
    "ds_read_b32 v196, v223 offset:13312\n\t" \
    "ds_read_b32 v197, v223 offset:13568\n\t" \
    "ds_read_b32 v198, v223 offset:13824\n\t" \
    "s_waitcnt lgkmcnt(0)\n\t" \
    "s_mov_b32 s68, 262144\n\t" \
    "s_mov_b32 s72, 0\n\t" \
    "s_mov_b32 s83, -1\n\t" \
    "s_mov_b32 s65, 0\n\t" \
    "v_add_u32 v181, 65536, v2\n\t" \
    "v_add_u32 v182, 131072, v2\n\t" \
    "v_add_u32 v183, 196608, v2\n\t" \
    "s_mov_b32 s77, 3019898880\n\t" \
    "s_mov_b32 s78, 3054198966\n\t" \
    "s_mov_b32 s71, 3\n\t" \
    "rr_E1_%=:\n\t" \
    "s_memtime s[54:55]\n\t" \
    "s_nop 0\n\t" \
    "s_memrealtime s[4:5]\n\t" \
    "s_nop 0\n\t" \
    "s_memrealtime s[6:7]\n\t" \
    "s_nop 0\n\t" \
    "s_memrealtime s[8:9]\n\t" \
    "s_nop 0\n\t" \
    "s_memrealtime s[10:11]\n\t" \
    "s_nop 0\n\t" \
    "s_memrealtime s[12:13]\n\t" \
    "s_nop 0\n\t" \
    "s_memrealtime s[14:15]\n\t" \
    "s_nop 0\n\t" \
    "s_memrealtime s[16:17]\n\t" \
    "s_nop 0\n\t" \
    "s_memrealtime s[18:19]\n\t" \
    "s_nop 0\n\t" \
    "s_memrealtime s[20:21]\n\t" \
    "s_nop 0\n\t" \
    "s_memrealtime s[22:23]\n\t" \
    "s_nop 0\n\t" \
    "s_memrealtime s[24:25]\n\t" \
    "s_nop 0\n\t" \
    "s_memrealtime s[26:27]\n\t" \
    "s_nop 0\n\t" \
    "s_memrealtime s[28:29]\n\t" \
    "s_nop 0\n\t" \
    "s_memtime s[56:57]\n\t" \
    "s_mov_b32 s70, 1\n\t" \
    "s_sub_u32 s71, s71, 1\n\t" \
    "s_cselect_b32 s69, 0, -1\n\t" \
    "s_waitcnt lgkmcnt(0)\n\t" \
    "s_sub_u32 s54, s56, s54\n\t" \
    "s_and_b32 s54, s54, s69\n\t" \
    "s_cmp_gt_u32 s54, 124\n\t" \
    "s_cbranch_scc1 rr_E1_%=\n\t" \
    "s_cmp_eq_u32 s4, s28\n\t" \
    "s_addc_u32 s70, s70, 0\n\t" \
    "s_cmp_eq_u32 s6, s28\n\t" \
    "s_addc_u32 s70, s70, 0\n\t" \
    "s_cmp_eq_u32 s8, s28\n\t" \
    "s_addc_u32 s70, s70, 0\n\t" \
    "s_cmp_eq_u32 s10, s28\n\t" \
    "s_addc_u32 s70, s70, 0\n\t" \
    "s_cmp_eq_u32 s12, s28\n\t" \
    "s_addc_u32 s70, s70, 0\n\t" \
    "s_cmp_eq_u32 s14, s28\n\t" \
    "s_addc_u32 s70, s70, 0\n\t" \
    "s_cmp_eq_u32 s16, s28\n\t" \
    "s_addc_u32 s70, s70, 0\n\t" \
    "s_cmp_eq_u32 s18, s28\n\t" \
    "s_addc_u32 s70, s70, 0\n\t" \
    "s_cmp_eq_u32 s20, s28\n\t" \
    "s_addc_u32 s70, s70, 0\n\t" \
    "s_cmp_eq_u32 s22, s28\n\t" \
    "s_addc_u32 s70, s70, 0\n\t" \
    "s_cmp_eq_u32 s24, s28\n\t" \
    "s_addc_u32 s70, s70, 0\n\t" \
    "s_cmp_eq_u32 s26, s28\n\t" \
    "s_addc_u32 s70, s70, 0\n\t" \
    "s_mov_b32 s75, s73\n\t" \
    "s_mov_b32 s76, s74\n\t" \
    "s_lshl_b32 s70, s70, 3\n\t" \
    "s_sub_u32 s73, s56, s70\n\t" \
    "s_lshl_b32 s74, s28, 4\n\t" \
    "v_mov_b32 v172, 0x1f0000\n\t" \
    "v_mov_b32 v173, 0x3f0000\n\t" \
    "global_load_dword v172, v172, s[36:37]\n\t" \
    "global_load_dword v173, v173, s[36:37]\n\t" \
    "s_mov_b32 s65, 63\n\t" \
    "s_mul_i32 s70, s67, 7\n\t" \
    "s_add_u32 s69, s65, 0\n\t" \
    "s_xor_b32 s69, s69, s70\n\t" \
    "s_and_b32 s69, s69, 63\n\t" \
    "s_lshl_b32 s69, s69, 15\n\t" \
    "s_mul_i32 s70, s67, 2097152\n\t" \
    "s_add_u32 s69, s69, s70\n\t" \
    "s_add_u32 s69, s69, 48234496\n\t" \
    "s_add_u32 s38, s36, s69\n\t" \
    "s_addc_u32 s39, s37, 0\n\t" \
    "v_mov_b32 v174, 0\n\t" \
    "global_load_dword v174, v174, s[38:39]\n\t" \
    "v_mbcnt_lo_u32_b32 v184, -1, 0\n\t" \
    "v_mbcnt_hi_u32_b32 v184, -1, v184\n\t" \
    "v_min_u32 v184, 59, v184\n\t" \
    "v_lshlrev_b32 v184, 6, v184\n\t" \
    "v_add_u32 v184, 6258688, v184\n\t" \
    "v_mov_b32 v185, 6266880\n\t" \
    "global_load_dword v185, v185, s[36:37] sc1\n\t" \
    "global_load_dwordx2 v[186:187], v184, s[36:37] sc1\n\t" \
    "s_waitcnt vmcnt(0)\n\t" \
    "v_sub_u32 v186, v186, v185\n\t" \
    "v_cmp_ne_u32 vcc, 1, v186\n\t" \
    "s_cbranch_vccz rr_Cok4_%=\n\t" \
    "rr_C3_%=:\n\t" \
    "global_load_dwordx2 v[186:187], v184, s[36:37] sc1\n\t" \
    "s_waitcnt vmcnt(0)\n\t" \
    "v_sub_u32 v186, v186, v185\n\t" \
    "v_cmp_ne_u32 vcc, 1, v186\n\t" \
    "s_cbranch_vccz rr_Cok4_%=\n\t" \
    "s_cmp_eq_u32 s68, 0\n\t" \
    "s_cbranch_scc1 rr_F5_%=\n\t" \
    "s_sub_u32 s68, s68, 1\n\t" \
    "s_sleep 4\n\t" \
    "s_branch rr_C3_%=\n\t" \
    "rr_F5_%=:\n\t" \
    "v_mov_b32 v188, 6095104\n\t" \
    "v_mov_b32 v189, 4\n\t" \
    "global_store_dword v188, v189, s[36:37] sc1\n\t" \
    "s_waitcnt vmcnt(0)\n\t" \
    "s_branch rr_Cok4_%=\n\t" \
    "rr_Cok4_%=:\n\t" \
    "v_min_u32_dpp v187, v187, v187 row_shl:6 row_mask:0xf bank_mask:0xf\n\t" \
    "s_nop 0\n\t" \
    "v_readlane_b32 s69, v187, 0\n\t" \
    "v_readlane_b32 s70, v187, 1\n\t" \
    "s_nop 1\n\t" \
    "s_cmp_lt_u32 s70, s69\n\t" \
    "s_cselect_b32 s62, 8192, 0\n\t" \
    "s_cselect_b32 s69, s70, s69\n\t" \
    "s_mov_b32 s71, s69\n\t" \
    "v_readlane_b32 s69, v187, 2\n\t" \
    "v_readlane_b32 s70, v187, 3\n\t" \
    "s_nop 1\n\t" \
    "s_cmp_lt_u32 s70, s69\n\t" \
    "s_cselect_b32 s63, 8192, 0\n\t" \
    "s_cselect_b32 s69, s70, s69\n\t" \
    "s_max_u32 s71, s69, s71\n\t" \
    "v_readlane_b32 s69, v187, 4\n\t" \
    "v_readlane_b32 s70, v187, 5\n\t" \
    "s_nop 1\n\t" \
    "s_cmp_lt_u32 s70, s69\n\t" \
    "s_cselect_b32 s64, 8192, 0\n\t" \
    "s_cselect_b32 s69, s70, s69\n\t" \
    "s_max_u32 s71, s69, s71\n\t" \
    "s_add_u32 s63, s63, 0x200000\n\t" \
    "s_xor_b32 s52, s64, 8192\n\t" \
    "s_cmp_gt_u32 s71, 625\n\t" \
    "s_cselect_b32 s69, 1, 0\n\t" \
    "s_mul_i32 s70, s69, 160\n\t" \
    "s_add_u32 s86, s86, s70\n\t" \
    "s_mul_i32 s70, s69, 536\n\t" \
    "s_add_u32 s87, s87, s70\n\t" \
    "s_mul_i32 s70, s69, 256\n\t" \
    "s_add_u32 s88, s88, s70\n\t" \
    "v_xor_b32 v6, s52, v6\n\t" \
    "v_lshlrev_b32 v178, 3, v0\n\t" \
    "v_and_b32 v178, 0xf80, v178\n\t" \
    "s_add_u32 s69, s52, 6148096\n\t" \
    "v_add_u32 v178, s69, v178\n\t" \
    "v_mov_b32 v172, -1\n\t" \
    "v_mov_b32 v173, -1\n\t" \
    "v_mov_b32 v174, -1\n\t" \
    "v_mov_b32 v175, -1\n\t" \
    "v_mov_b32 v170, 0\n\t" \
    "s_mov_b32 s65, s66\n\t" \
    "s_sub_u32 s80, s74, 32000\n\t" \
    "v_mov_b32 v177, -1\n\t" \
    "s_cmp_lg_u32 s51, 0\n\t" \
    "s_cselect_b32 s42, 0, 0\n\t" \
    "s_cselect_b32 s43, 32, 0\n\t" \
    "s_cmp_le_u32 s51, s65\n\t" \
    "s_cbranch_scc1 rr_n316_%=\n\t" \
    "s_add_u32 s69, s65, 2\n\t" \
    "s_mul_i32 s70, s69, 171\n\t" \
    "s_lshr_b32 s70, s70, 9\n\t" \
    "s_mul_i32 s70, s70, 3\n\t" \
    "s_sub_u32 s69, s69, s70\n\t" \
    "s_mul_i32 s69, s69, 36\n\t" \
    "s_lshl_b32 s69, s69, 14\n\t" \
    "s_add_u32 s69, s69, s64\n\t" \
    "s_add_u32 s69, s69, 4194304\n\t" \
    "s_add_u32 s40, s36, s69\n\t" \
    "s_addc_u32 s41, s37, 0\n\t" \
    "global_store_dwordx4 v196, v[172:175], s[40:41] sc1\n\t" \
    "global_store_dwordx4 v197, v[172:175], s[40:41] sc1\n\t" \
    "global_store_dwordx4 v198, v[172:175], s[40:41] sc1\n\t" \
    "s_add_u32 s69, s65, 2\n\t" \
    "s_mul_i32 s70, s69, 171\n\t" \
    "s_lshr_b32 s70, s70, 9\n\t" \
    "s_mul_i32 s70, s70, 3\n\t" \
    "s_sub_u32 s69, s69, s70\n\t" \
    "s_mul_i32 s69, s69, 36\n\t" \
    "s_add_u32 s69, s69, s67\n\t" \
    "s_lshl_b32 s69, s69, 14\n\t" \
    "s_add_u32 s69, s69, s64\n\t" \
    "s_add_u32 s69, s69, 4194304\n\t" \
    "s_add_u32 s40, s36, s69\n\t" \
    "s_addc_u32 s41, s37, 0\n\t" \
    "global_store_dwordx4 v0, v[172:175], s[40:41] offset:0 sc1\n\t" \
    "global_store_dwordx4 v0, v[172:175], s[40:41] offset:1024 sc1\n\t" \
    "global_store_dwordx4 v0, v[172:175], s[40:41] offset:2048 sc1\n\t" \
    "global_store_dwordx4 v0, v[172:175], s[40:41] offset:3072 sc1\n\t" \
    "global_store_dwordx4 v1, v[172:175], s[40:41] offset:0 sc1\n\t" \
    "global_store_dwordx4 v1, v[172:175], s[40:41] offset:1024 sc1\n\t" \
    "global_store_dwordx4 v1, v[172:175], s[40:41] offset:2048 sc1\n\t" \
    "global_store_dwordx4 v1, v[172:175], s[40:41] offset:3072 sc1\n\t" \
    "rr_n316_%=:\n\t" \
    "s_cmp_eq_u32 s51, 0\n\t" \
    "s_cbranch_scc1 rr_arm17_%=\n\t" \
    "s_cmp_gt_u32 s65, 61\n\t" \
    "s_cbranch_scc1 rr_arm17_%=\n\t" \
    "s_add_u32 s69, s65, 2\n\t" \
    "s_bitcmp1_b32 s69, 0\n\t" \
    "s_cselect_b32 s70, s63, s62\n\t" \
    "s_bfe_u32 s69, s69, 0x20001\n\t" \
    "s_lshl_b32 s69, s69, 19\n\t" \
    "s_add_u32 s69, s69, s70\n\t" \
    "s_add_u32 s46, s36, s69\n\t" \
    "s_addc_u32 s47, s37, 0\n\t" \
    "global_store_dwordx4 v164, v[172:175], s[46:47] sc1\n\t" \
    "global_store_dwordx4 v165, v[172:175], s[46:47] sc1\n\t" \
    "global_store_dwordx4 v166, v[172:175], s[46:47] sc1\n\t" \
    "global_store_dwordx4 v167, v[172:175], s[46:47] sc1\n\t" \
    "rr_arm17_%=:\n\t" \
    "s_waitcnt vmcnt(0)\n\t" \
    "s_add_u32 s52, s36, s62\n\t" \
    "s_addc_u32 s53, s37, 0\n\t" \
    "v_lshrrev_b32 v187, 9, v0\n\t" \
    "v_lshl_add_u32 v187, v187, 2, v7\n\t" \
    "v_add_u32 v187, 3072, v187\n\t" \
    "v_mov_b32 v189, -1\n\t" \
    "s_sleep 40\n\t" \
    "s_mov_b32 s71, 3\n\t" \
    "rr_E18_%=:\n\t" \
    "s_memtime s[54:55]\n\t" \
    "s_nop 0\n\t" \
    "s_memrealtime s[4:5]\n\t" \
    "s_nop 0\n\t" \
    "s_memrealtime s[6:7]\n\t" \
    "s_nop 0\n\t" \
    "s_memrealtime s[8:9]\n\t" \
    "s_nop 0\n\t" \
    "s_memrealtime s[10:11]\n\t" \
    "s_nop 0\n\t" \
    "s_memrealtime s[12:13]\n\t" \
    "s_nop 0\n\t" \
    "s_memrealtime s[14:15]\n\t" \
    "s_nop 0\n\t" \
    "s_memrealtime s[16:17]\n\t" \
    "s_nop 0\n\t" \
    "s_memrealtime s[18:19]\n\t" \
    "s_nop 0\n\t" \
    "s_memrealtime s[20:21]\n\t" \
    "s_nop 0\n\t" \
    "s_memrealtime s[22:23]\n\t" \
    "s_nop 0\n\t" \
    "s_memrealtime s[24:25]\n\t" \
    "s_nop 0\n\t" \
    "s_memrealtime s[26:27]\n\t" \
    "s_nop 0\n\t" \
    "s_memrealtime s[28:29]\n\t" \
    "s_nop 0\n\t" \
    "s_memtime s[56:57]\n\t" \
    "s_mov_b32 s70, 1\n\t" \
    "s_sub_u32 s71, s71, 1\n\t" \
    "s_cselect_b32 s69, 0, -1\n\t" \
    "s_waitcnt lgkmcnt(0)\n\t" \
    "s_sub_u32 s54, s56, s54\n\t" \
    "s_and_b32 s54, s54, s69\n\t" \
    "s_cmp_gt_u32 s54, 124\n\t" \
    "s_cbranch_scc1 rr_E18_%=\n\t" \
    "s_cmp_eq_u32 s4, s28\n\t" \
    "s_addc_u32 s70, s70, 0\n\t" \
    "s_cmp_eq_u32 s6, s28\n\t" \
    "s_addc_u32 s70, s70, 0\n\t" \
    "s_cmp_eq_u32 s8, s28\n\t" \
    "s_addc_u32 s70, s70, 0\n\t" \
    "s_cmp_eq_u32 s10, s28\n\t" \
    "s_addc_u32 s70, s70, 0\n\t" \
    "s_cmp_eq_u32 s12, s28\n\t" \
    "s_addc_u32 s70, s70, 0\n\t" \
    "s_cmp_eq_u32 s14, s28\n\t" \
    "s_addc_u32 s70, s70, 0\n\t" \
    "s_cmp_eq_u32 s16, s28\n\t" \
    "s_addc_u32 s70, s70, 0\n\t" \
    "s_cmp_eq_u32 s18, s28\n\t" \
    "s_addc_u32 s70, s70, 0\n\t" \
    "s_cmp_eq_u32 s20, s28\n\t" \
    "s_addc_u32 s70, s70, 0\n\t" \
    "s_cmp_eq_u32 s22, s28\n\t" \
    "s_addc_u32 s70, s70, 0\n\t" \
    "s_cmp_eq_u32 s24, s28\n\t" \
    "s_addc_u32 s70, s70, 0\n\t" \
    "s_cmp_eq_u32 s26, s28\n\t" \
    "s_addc_u32 s70, s70, 0\n\t" \
    "s_mov_b32 s75, s73\n\t" \
    "s_mov_b32 s76, s74\n\t" \
    "s_lshl_b32 s70, s70, 3\n\t" \
    "s_sub_u32 s73, s56, s70\n\t" \
    "s_lshl_b32 s74, s28, 4\n\t" \
    "s_sub_u32 s69, s73, s75\n\t" \
    "s_sub_u32 s70, s74, s76\n\t" \
    "s_sub_u32 s71, s70, 1600\n\t" \
    "s_cmp_lt_u32 s71, 400000\n\t" \
    "s_cbranch_scc0 rr_K20_%=\n\t" \
    "v_cvt_f32_u32 v190, s69\n\t" \
    "v_cvt_f32_u32 v191, s70\n\t" \
    "v_rcp_f32 v192, v190\n\t" \
    "v_rcp_f32 v193, v191\n\t" \
    "v_mul_f32 v192, v192, v191\n\t" \
    "v_mul_f32 v193, v193, v190\n\t" \
    "v_mul_f32 v192, 0x4f800000, v192\n\t" \
    "v_mul_f32 v193, 0x4f000000, v193\n\t" \
    "v_cvt_u32_f32 v192, v192\n\t" \
    "v_cvt_u32_f32 v193, v193\n\t" \
    "v_readfirstlane_b32 s69, v192\n\t" \
    "v_readfirstlane_b32 s70, v193\n\t" \
    "s_nop 1\n\t" \
    "s_mov_b32 s78, s69\n\t" \
    "s_mov_b32 s77, s70\n\t" \
    "rr_K20_%=:\n\t" \
    "s_add_u32 s69, s65, 0\n\t" \
    "s_bitcmp1_b32 s69, 0\n\t" \
    "s_cselect_b32 s70, s63, s62\n\t" \
    "s_bfe_u32 s69, s69, 0x20001\n\t" \
    "s_lshl_b32 s69, s69, 19\n\t" \
    "s_add_u32 s69, s69, s70\n\t" \
    "s_add_u32 s46, s36, s69\n\t" \
    "s_addc_u32 s47, s37, 0\n\t" \
    "s_add_u32 s69, s65, 1\n\t" \
    "s_bitcmp1_b32 s69, 0\n\t" \
    "s_cselect_b32 s70, s63, s62\n\t" \
    "s_bfe_u32 s69, s69, 0x20001\n\t" \
    "s_lshl_b32 s69, s69, 19\n\t" \
    "s_add_u32 s69, s69, s70\n\t" \
    "s_add_u32 s48, s36, s69\n\t" \
    "s_addc_u32 s49, s37, 0\n\t" \
    "s_mul_i32 s70, s67, 7\n\t" \
    "s_add_u32 s69, s65, 0\n\t" \
    "s_xor_b32 s69, s69, s70\n\t" \
    "s_and_b32 s69, s69, 63\n\t" \
    "s_lshl_b32 s69, s69, 15\n\t" \
    "s_mul_i32 s70, s67, 2097152\n\t" \
    "s_add_u32 s69, s69, s70\n\t" \
    "s_add_u32 s69, s69, 48234496\n\t" \
    "s_add_u32 s38, s36, s69\n\t" \
    "s_addc_u32 s39, s37, 0\n\t" \
    "s_add_u32 s69, s65, 0\n\t" \
    "s_mul_i32 s70, s69, 171\n\t" \
    "s_lshr_b32 s70, s70, 9\n\t" \
    "s_mul_i32 s70, s70, 3\n\t" \
    "s_sub_u32 s69, s69, s70\n\t" \
    "s_mul_i32 s69, s69, 36\n\t" \
    "s_add_u32 s69, s69, s67\n\t" \
    "s_lshl_b32 s69, s69, 14\n\t" \
    "s_add_u32 s69, s69, s64\n\t" \
    "s_add_u32 s69, s69, 4194304\n\t" \
    "s_add_u32 s40, s36, s69\n\t" \
    "s_addc_u32 s41, s37, 0\n\t" \
    "s_mov_b32 s91, 0\n\t" \
    "s_mov_b32 s96, 0x55555555\n\t" \
    "s_mov_b32 s58, 0\n\t" \
    "s_mov_b32 s59, -1\n\t" \
    "v_mov_b32 v160, 0\n\t" \
    "v_mov_b32 v161, 0\n\t" \
    "v_mov_b32 v162, 0\n\t" \
    "v_mov_b32 v163, 0\n\t" \
    "v_mov_b32 v185, 0\n\t" \
    "s_mov_b32 s91, 3\n\t" \
    "s_mov_b64 exec, 0\n\t" \
    "s_branch rr_h025_%=\n\t" \
    "rr_dry27_%=:\n\t" \
    "s_mov_b64 exec, -1\n\t" \
    "s_mov_b32 s91, 0\n\t" \
    "s_waitcnt vmcnt(0) lgkmcnt(0)\n\t" \
    "s_nop 7\n\t" \
    "v_mov_b32 v160, 0\n\t" \
    "v_mov_b32 v161, 0\n\t" \
    "v_mov_b32 v162, 0\n\t" \
    "v_mov_b32 v163, 0\n\t" \
    "rr_pg21_%=:\n\t" \
    "v_cmp_eq_u32 vcc, -1, v189\n\t" \
    "s_cmp_eq_u32 s96, 0\n\t" \
    "s_cselect_b32 s69, vcc_lo, 0\n\t" \
    "s_or_b32 s69, s69, vcc_hi\n\t" \
    "s_cbranch_scc0 rr_ph28_%=\n\t" \
    "s_sleep 10\n\t" \
    "global_load_dword v189, v187, s[52:53] sc1\n\t" \
    "s_mov_b32 s71, 3\n\t" \
    "rr_E29_%=:\n\t" \
    "s_memtime s[54:55]\n\t" \
    "s_nop 0\n\t" \
    "s_memrealtime s[4:5]\n\t" \
    "s_nop 0\n\t" \
    "s_memrealtime s[6:7]\n\t" \
    "s_nop 0\n\t" \
    "s_memrealtime s[8:9]\n\t" \
    "s_nop 0\n\t" \
    "s_memrealtime s[10:11]\n\t" \
    "s_nop 0\n\t" \
    "s_memrealtime s[12:13]\n\t" \
    "s_nop 0\n\t" \
    "s_memrealtime s[14:15]\n\t" \
    "s_nop 0\n\t" \
    "s_memrealtime s[16:17]\n\t" \
    "s_nop 0\n\t" \
    "s_memrealtime s[18:19]\n\t" \
    "s_nop 0\n\t" \
    "s_memrealtime s[20:21]\n\t" \
    "s_nop 0\n\t" \
    "s_memrealtime s[22:23]\n\t" \
    "s_nop 0\n\t" \
    "s_memrealtime s[24:25]\n\t" \
    "s_nop 0\n\t" \
    "s_memrealtime s[26:27]\n\t" \
    "s_nop 0\n\t" \
    "s_memrealtime s[28:29]\n\t" \
    "s_nop 0\n\t" \
    "s_memtime s[56:57]\n\t" \
    "s_mov_b32 s70, 1\n\t" \
    "s_sub_u32 s71, s71, 1\n\t" \
    "s_cselect_b32 s69, 0, -1\n\t" \
    "s_waitcnt lgkmcnt(0)\n\t" \
    "s_sub_u32 s54, s56, s54\n\t" \
    "s_and_b32 s54, s54, s69\n\t" \
    "s_cmp_gt_u32 s54, 124\n\t" \
    "s_cbranch_scc1 rr_E29_%=\n\t" \
    "s_cmp_eq_u32 s4, s28\n\t" \
    "s_addc_u32 s70, s70, 0\n\t" \
    "s_cmp_eq_u32 s6, s28\n\t" \
    "s_addc_u32 s70, s70, 0\n\t" \
    "s_cmp_eq_u32 s8, s28\n\t" \
    "s_addc_u32 s70, s70, 0\n\t" \
    "s_cmp_eq_u32 s10, s28\n\t" \
    "s_addc_u32 s70, s70, 0\n\t" \
    "s_cmp_eq_u32 s12, s28\n\t" \
    "s_addc_u32 s70, s70, 0\n\t" \
    "s_cmp_eq_u32 s14, s28\n\t" \
    "s_addc_u32 s70, s70, 0\n\t" \
    "s_cmp_eq_u32 s16, s28\n\t" \
    "s_addc_u32 s70, s70, 0\n\t" \
    "s_cmp_eq_u32 s18, s28\n\t" \
    "s_addc_u32 s70, s70, 0\n\t" \
    "s_cmp_eq_u32 s20, s28\n\t" \
    "s_addc_u32 s70, s70, 0\n\t" \
    "s_cmp_eq_u32 s22, s28\n\t" \
    "s_addc_u32 s70, s70, 0\n\t" \
    "s_cmp_eq_u32 s24, s28\n\t" \
    "s_addc_u32 s70, s70, 0\n\t" \
    "s_cmp_eq_u32 s26, s28\n\t" \
    "s_addc_u32 s70, s70, 0\n\t" \
    "s_mov_b32 s75, s73\n\t" \
    "s_mov_b32 s76, s74\n\t" \
    "s_lshl_b32 s70, s70, 3\n\t" \
    "s_sub_u32 s73, s56, s70\n\t" \
    "s_lshl_b32 s74, s28, 4\n\t" \
    "s_waitcnt vmcnt(0)\n\t" \
    "rr_ph28_%=:\n\t" \
    "v_cmp_ne_u32 s[34:35], -1, v189\n\t" \
    "s_memtime s[56:57]\n\t" \
    "s_waitcnt lgkmcnt(0)\n\t" \
    "s_sub_u32 s69, s56, s73\n\t" \
    "s_mul_hi_u32 s69, s69, s78\n\t" \
    "s_add_u32 s69, s69, s74\n\t" \
    "s_add_u32 s69, s69, 1280\n\t" \
    "v_sub_u32 v184, s69, v189\n\t" \
    "v_cmp_le_i32 vcc, 0, v184\n\t" \
    "s_nop 1\n\t" \
    "s_and_b64 s[54:55], vcc, s[34:35]\n\t" \
    "s_and_b64 s[54:55], s[54:55], s[58:59]\n\t" \
    "s_cbranch_scc0 rr_pn22_%=\n\t" \
    "s_mov_b64 exec, s[54:55]\n\t" \
    "global_load_dword v177, v6, s[36:37] sc1\n\t" \
    "s_mov_b64 exec, -1\n\t" \
    "s_waitcnt vmcnt(0)\n\t" \
    "v_cmp_le_i32 vcc, s65, v177\n\t" \
    "s_nop 1\n\t" \
    "s_andn2_b32 s59, s59, vcc_hi\n\t" \
    "s_lshr_b32 s69, vcc_hi, 1\n\t" \
    "s_and_b32 s69, s69, vcc_hi\n\t" \
    "s_and_b32 s54, s69, s96\n\t" \
    "s_andn2_b32 s96, s96, s54\n\t" \
    "s_mov_b32 s55, s54\n\t" \
    "s_lshr_b64 s[54:55], s[54:55], s50\n\t" \
    "s_bitcmp1_b32 s54, 0\n\t" \
    "s_cbranch_scc0 rr_pk31_%=\n\t" \
    "global_load_dwordx4 a[0:3], v26, s[38:39] offset:0 sc1\n\t" \
    "global_load_dwordx4 a[4:7], v26, s[38:39] offset:1024 sc1\n\t" \
    "rr_pk31_%=:\n\t" \
    "s_bitcmp1_b32 s54, 2\n\t" \
    "s_cbranch_scc0 rr_pk32_%=\n\t" \
    "global_load_dwordx4 a[8:11], v27, s[38:39] offset:0 sc1\n\t" \
    "global_load_dwordx4 a[12:15], v27, s[38:39] offset:1024 sc1\n\t" \
    "rr_pk32_%=:\n\t" \
    "s_bitcmp1_b32 s54, 4\n\t" \
    "s_cbranch_scc0 rr_pk33_%=\n\t" \
    "global_load_dwordx4 a[16:19], v28, s[38:39] offset:0 sc1\n\t" \
    "global_load_dwordx4 a[20:23], v28, s[38:39] offset:1024 sc1\n\t" \
    "rr_pk33_%=:\n\t" \
    "s_bitcmp1_b32 s54, 6\n\t" \
    "s_cbranch_scc0 rr_pk34_%=\n\t" \
    "global_load_dwordx4 a[24:27], v29, s[38:39] offset:0 sc1\n\t" \
    "global_load_dwordx4 a[28:31], v29, s[38:39] offset:1024 sc1\n\t" \
    "rr_pk34_%=:\n\t" \
    "s_bitcmp1_b32 s54, 8\n\t" \
    "s_cbranch_scc0 rr_pk35_%=\n\t" \
    "global_load_dwordx4 a[32:35], v30, s[38:39] offset:0 sc1\n\t" \
    "global_load_dwordx4 a[36:39], v30, s[38:39] offset:1024 sc1\n\t" \
    "rr_pk35_%=:\n\t" \
    "s_bitcmp1_b32 s54, 10\n\t" \
    "s_cbranch_scc0 rr_pk36_%=\n\t" \
    "global_load_dwordx4 a[40:43], v31, s[38:39] offset:0 sc1\n\t" \
    "global_load_dwordx4 a[44:47], v31, s[38:39] offset:1024 sc1\n\t" \
    "rr_pk36_%=:\n\t" \
    "s_bitcmp1_b32 s54, 12\n\t" \
    "s_cbranch_scc0 rr_pk37_%=\n\t" \
    "global_load_dwordx4 a[48:51], v32, s[38:39] offset:0 sc1\n\t" \
    "global_load_dwordx4 a[52:55], v32, s[38:39] offset:1024 sc1\n\t" \
    "rr_pk37_%=:\n\t" \
    "s_bitcmp1_b32 s54, 14\n\t" \
    "s_cbranch_scc0 rr_pk38_%=\n\t" \
    "global_load_dwordx4 a[56:59], v33, s[38:39] offset:0 sc1\n\t" \
    "global_load_dwordx4 a[60:63], v33, s[38:39] offset:1024 sc1\n\t" \
    "rr_pk38_%=:\n\t" \
    "s_bitcmp1_b32 s54, 16\n\t" \
    "s_cbranch_scc0 rr_pk39_%=\n\t" \
    "global_load_dwordx4 a[64:67], v34, s[38:39] offset:0 sc1\n\t" \
    "global_load_dwordx4 a[68:71], v34, s[38:39] offset:1024 sc1\n\t" \
    "rr_pk39_%=:\n\t" \
    "s_bitcmp1_b32 s54, 18\n\t" \
    "s_cbranch_scc0 rr_pk40_%=\n\t" \
    "global_load_dwordx4 a[72:75], v35, s[38:39] offset:0 sc1\n\t" \
    "global_load_dwordx4 a[76:79], v35, s[38:39] offset:1024 sc1\n\t" \
    "rr_pk40_%=:\n\t" \
    "s_bitcmp1_b32 s54, 20\n\t" \
    "s_cbranch_scc0 rr_pk41_%=\n\t" \
    "global_load_dwordx4 a[80:83], v36, s[38:39] offset:0 sc1\n\t" \
    "global_load_dwordx4 a[84:87], v36, s[38:39] offset:1024 sc1\n\t" \
    "rr_pk41_%=:\n\t" \
    "s_bitcmp1_b32 s54, 22\n\t" \
    "s_cbranch_scc0 rr_pk42_%=\n\t" \
    "global_load_dwordx4 a[88:91], v37, s[38:39] offset:0 sc1\n\t" \
    "global_load_dwordx4 a[92:95], v37, s[38:39] offset:1024 sc1\n\t" \
    "rr_pk42_%=:\n\t" \
    "s_bitcmp1_b32 s54, 24\n\t" \
    "s_cbranch_scc0 rr_pk43_%=\n\t" \
    "global_load_dwordx4 a[96:99], v38, s[38:39] offset:0 sc1\n\t" \
    "global_load_dwordx4 a[100:103], v38, s[38:39] offset:1024 sc1\n\t" \
    "rr_pk43_%=:\n\t" \
    "s_bitcmp1_b32 s54, 26\n\t" \
    "s_cbranch_scc0 rr_pk44_%=\n\t" \
    "global_load_dwordx4 a[104:107], v39, s[38:39] offset:0 sc1\n\t" \
    "global_load_dwordx4 a[108:111], v39, s[38:39] offset:1024 sc1\n\t" \
    "rr_pk44_%=:\n\t" \
    "s_bitcmp1_b32 s54, 28\n\t" \
    "s_cbranch_scc0 rr_pk45_%=\n\t" \
    "global_load_dwordx4 a[112:115], v40, s[38:39] offset:0 sc1\n\t" \
    "global_load_dwordx4 a[116:119], v40, s[38:39] offset:1024 sc1\n\t" \
    "rr_pk45_%=:\n\t" \
    "s_bitcmp1_b32 s54, 30\n\t" \
    "s_cbranch_scc0 rr_pk46_%=\n\t" \
    "global_load_dwordx4 a[120:123], v41, s[38:39] offset:0 sc1\n\t" \
    "global_load_dwordx4 a[124:127], v41, s[38:39] offset:1024 sc1\n\t" \
    "rr_pk46_%=:\n\t" \
    "rr_pn22_%=:\n\t" \
    "s_orn2_b32 s69, s96, s34\n\t" \
    "s_cbranch_scc0 rr_pe23_%=\n\t" \
    "s_cmp_eq_u32 s68, 0\n\t" \
    "s_cbranch_scc1 rr_F47_%=\n\t" \
    "s_sub_u32 s68, s68, 1\n\t" \
    "s_branch rr_pg21_%=\n\t" \
    "rr_F47_%=:\n\t" \
    "v_mov_b32 v184, 6095104\n\t" \
    "v_mov_b32 v185, 10\n\t" \
    "global_store_dword v184, v185, s[36:37] sc1\n\t" \
    "s_waitcnt vmcnt(0)\n\t" \
    "s_branch rr_pe23_%=\n\t" \
    "rr_pe23_%=:\n\t" \
    "s_mov_b32 s83, s65\n\t" \
    "v_mov_b32 v184, v189\n\t" \
    "v_subrev_u32 v184, s80, v184\n\t" \
    "s_nop 1\n\t" \
    "v_max_i32_dpp v184, v184, v184 row_ror:1 row_mask:0xf bank_mask:0xf\n\t" \
    "s_nop 1\n\t" \
    "v_max_i32_dpp v184, v184, v184 row_ror:2 row_mask:0xf bank_mask:0xf\n\t" \
    "s_nop 1\n\t" \
    "v_max_i32_dpp v184, v184, v184 row_ror:4 row_mask:0xf bank_mask:0xf\n\t" \
    "s_nop 1\n\t" \
    "v_max_i32_dpp v184, v184, v184 row_ror:8 row_mask:0xf bank_mask:0xf\n\t" \
    "s_nop 0\n\t" \
    "v_readlane_b32 s69, v184, 0\n\t" \
    "v_readlane_b32 s71, v184, 16\n\t" \
    "v_readlane_b32 s70, v184, 32\n\t" \
    "v_readlane_b32 s81, v184, 48\n\t" \
    "s_max_i32 s69, s69, s71\n\t" \
    "s_nop 0\n\t" \
    "s_max_i32 s71, s70, s81\n\t" \
    "s_max_i32 s69, s69, s71\n\t" \
    "s_add_u32 s81, s69, s80\n\t" \
    "s_cmp_lg_u32 s65, 0\n\t" \
    "s_cbranch_scc1 rr_u226_%=\n\t" \
    "s_waitcnt vmcnt(0)\n\t" \
    "global_load_dword v177, v6, s[36:37] sc1\n\t" \
    "v_mov_b32 v184, v189\n\t" \
    "v_subrev_u32 v184, s80, v184\n\t" \
    "s_nop 1\n\t" \
    "v_max_i32_dpp v184, v184, v184 row_ror:1 row_mask:0xf bank_mask:0xf\n\t" \
    "s_nop 1\n\t" \
    "v_max_i32_dpp v184, v184, v184 row_ror:2 row_mask:0xf bank_mask:0xf\n\t" \
    "s_nop 1\n\t" \
    "v_max_i32_dpp v184, v184, v184 row_ror:4 row_mask:0xf bank_mask:0xf\n\t" \
    "s_nop 1\n\t" \
    "v_max_i32_dpp v184, v184, v184 row_ror:8 row_mask:0xf bank_mask:0xf\n\t" \
    "s_nop 0\n\t" \
    "v_readlane_b32 s69, v184, 0\n\t" \
    "v_readlane_b32 s71, v184, 16\n\t" \
    "s_nop 1\n\t" \
    "s_max_i32 s69, s69, s71\n\t" \
    "s_add_u32 s81, s69, s80\n\t" \
    "s_memtime s[56:57]\n\t" \
    "s_waitcnt lgkmcnt(0)\n\t" \
    "s_sub_u32 s69, s56, s73\n\t" \
    "s_mul_hi_u32 s69, s69, s78\n\t" \
    "s_add_u32 s69, s69, s74\n\t" \
    "s_add_u32 s69, s69, 480\n\t" \
    "s_sub_u32 s69, s69, s81\n\t" \
    "s_max_i32 s69, s69, 0\n\t" \
    "s_add_u32 s69, s69, s81\n\t" \
    "v_mov_b32 v184, s69\n\t" \
    "s_add_u32 s69, s69, 160\n\t" \
    "v_and_b32_e64 v185, -2, s69\n\t" \
    "global_store_dword v3, v185, s[46:47] sc1\n\t" \
    "s_branch rr_h025_%=\n\t" \
    "rr_pf13_%=:\n\t" \
    "s_cmp_gt_u32 s65, 62\n\t" \
    "s_cbranch_scc1 rr_end8_%=\n\t" \
    "v_min_i32_dpp v177, v177, v177 row_ror:1 row_mask:0xf bank_mask:0xf\n\t" \
    "s_nop 1\n\t" \
    "v_min_i32_dpp v177, v177, v177 row_ror:2 row_mask:0xf bank_mask:0xf\n\t" \
    "s_nop 1\n\t" \
    "v_min_i32_dpp v177, v177, v177 row_ror:4 row_mask:0xf bank_mask:0xf\n\t" \
    "s_nop 1\n\t" \
    "v_min_i32_dpp v177, v177, v177 row_ror:8 row_mask:0xf bank_mask:0xf\n\t" \
    "s_nop 0\n\t" \
    "v_readlane_b32 s69, v177, 0\n\t" \
    "v_readlane_b32 s70, v177, 16\n\t" \
    "s_nop 1\n\t" \
    "s_min_i32 s69, s69, s70\n\t" \
    "s_max_i32 s83, s83, s69\n\t" \
    "rr_WF48_%=:\n\t" \
    "s_cmp_ge_i32 s83, s65\n\t" \
    "s_cbranch_scc1 rr_WFok49_%=\n\t" \
    "global_load_dword v184, v6, s[36:37] sc1\n\t" \
    "s_waitcnt vmcnt(0)\n\t" \
    "v_min_i32_dpp v184, v184, v184 row_ror:1 row_mask:0xf bank_mask:0xf\n\t" \
    "s_nop 1\n\t" \
    "v_min_i32_dpp v184, v184, v184 row_ror:2 row_mask:0xf bank_mask:0xf\n\t" \
    "s_nop 1\n\t" \
    "v_min_i32_dpp v184, v184, v184 row_ror:4 row_mask:0xf bank_mask:0xf\n\t" \
    "s_nop 1\n\t" \
    "v_min_i32_dpp v184, v184, v184 row_ror:8 row_mask:0xf bank_mask:0xf\n\t" \
    "s_nop 0\n\t" \
    "v_readlane_b32 s69, v184, 0\n\t" \
    "v_readlane_b32 s70, v184, 16\n\t" \
    "s_nop 1\n\t" \
    "s_min_i32 s83, s69, s70\n\t" \
    "s_cmp_ge_i32 s83, s65\n\t" \
    "s_cbranch_scc1 rr_WFok49_%=\n\t" \
    "s_cmp_eq_u32 s68, 0\n\t" \
    "s_cbranch_scc1 rr_F50_%=\n\t" \
    "s_sub_u32 s68, s68, 1\n\t" \
    "s_sleep 2\n\t" \
    "s_branch rr_WF48_%=\n\t" \
    "rr_F50_%=:\n\t" \
    "v_mov_b32 v184, 6095104\n\t" \
    "v_mov_b32 v185, 10\n\t" \
    "global_store_dword v184, v185, s[36:37] sc1\n\t" \
    "s_waitcnt vmcnt(0)\n\t" \
    "s_branch rr_WFok49_%=\n\t" \
    "rr_WFok49_%=:\n\t" \
    "s_mul_i32 s70, s67, 7\n\t" \
    "s_add_u32 s69, s65, 0\n\t" \
    "s_xor_b32 s69, s69, s70\n\t" \
    "s_and_b32 s69, s69, 63\n\t" \
    "s_lshl_b32 s69, s69, 15\n\t" \
    "s_mul_i32 s70, s67, 2097152\n\t" \
    "s_add_u32 s69, s69, s70\n\t" \
    "s_add_u32 s69, s69, 48234496\n\t" \
    "s_add_u32 s38, s36, s69\n\t" \
    "s_addc_u32 s39, s37, 0\n\t" \
    "s_mul_hi_u32 s69, s85, s78\n\t" \
    "s_add_u32 s79, s81, s69\n\t" \
    "s_add_u32 s79, s79, s87\n\t" \
    "s_cmp_ge_u32 s65, 38\n\t" \
    "s_cselect_b32 s69, 28, 0\n\t" \
    "s_sub_u32 s79, s79, s69\n\t" \
    "s_cmp_lt_u32 s65, 10\n\t" \
    "s_cselect_b32 s69, 96, 0\n\t" \
    "s_add_u32 s79, s79, s69\n\t" \
    "s_add_u32 s80, s79, s88\n\t" \
    "s_add_u32 s79, s79, -96\n\t" \
    "s_add_u32 s79, s79, s42\n\t" \
    "s_sub_u32 s70, s79, s74\n\t" \
    "s_max_i32 s70, s70, 0\n\t" \
    "s_min_i32 s70, s70, 16000\n\t" \
    "s_lshl_b32 s70, s70, 1\n\t" \
    "s_mul_hi_u32 s70, s70, s77\n\t" \
    "s_add_u32 s70, s70, s73\n\t" \
    "rr_W51_%=:\n\t" \
    "s_memtime s[56:57]\n\t" \
    "s_waitcnt lgkmcnt(0)\n\t" \
    "s_sub_u32 s69, s70, s56\n\t" \
    "s_cmp_gt_i32 s69, 24\n\t" \
    "s_cbranch_scc1 rr_W51_%=\n\t" \
    "s_add_u32 s69, s65, 4\n\t" \
    "s_min_i32 s69, s69, 62\n\t" \
    "s_cmp_ge_i32 s83, s69\n\t" \
    "s_cbranch_scc1 rr_nfl52_%=\n\t" \
    "global_load_dword v177, v6, s[36:37] sc1\n\t" \
    "rr_nfl52_%=:\n\t" \
    "global_load_dwordx4 a[0:3], v26, s[38:39] offset:0 sc1\n\t" \
    "global_load_dwordx4 a[4:7], v26, s[38:39] offset:1024 sc1\n\t" \
    "global_load_dwordx4 a[8:11], v27, s[38:39] offset:0 sc1\n\t" \
    "global_load_dwordx4 a[12:15], v27, s[38:39] offset:1024 sc1\n\t" \
    "global_load_dwordx4 a[16:19], v28, s[38:39] offset:0 sc1\n\t" \
    "global_load_dwordx4 a[20:23], v28, s[38:39] offset:1024 sc1\n\t" \
    "global_load_dwordx4 a[24:27], v29, s[38:39] offset:0 sc1\n\t" \
    "global_load_dwordx4 a[28:31], v29, s[38:39] offset:1024 sc1\n\t" \
    "global_load_dwordx4 a[32:35], v30, s[38:39] offset:0 sc1\n\t" \
    "global_load_dwordx4 a[36:39], v30, s[38:39] offset:1024 sc1\n\t" \
    "global_load_dwordx4 a[40:43], v31, s[38:39] offset:0 sc1\n\t" \
    "global_load_dwordx4 a[44:47], v31, s[38:39] offset:1024 sc1\n\t" \
    "global_load_dwordx4 a[48:51], v32, s[38:39] offset:0 sc1\n\t" \
    "global_load_dwordx4 a[52:55], v32, s[38:39] offset:1024 sc1\n\t" \
    "global_load_dwordx4 a[56:59], v33, s[38:39] offset:0 sc1\n\t" \
    "global_load_dwordx4 a[60:63], v33, s[38:39] offset:1024 sc1\n\t" \
    "global_load_dwordx4 a[64:67], v34, s[38:39] offset:0 sc1\n\t" \
    "global_load_dwordx4 a[68:71], v34, s[38:39] offset:1024 sc1\n\t" \
    "global_load_dwordx4 a[72:75], v35, s[38:39] offset:0 sc1\n\t" \
    "global_load_dwordx4 a[76:79], v35, s[38:39] offset:1024 sc1\n\t" \
    "global_load_dwordx4 a[80:83], v36, s[38:39] offset:0 sc1\n\t" \
    "global_load_dwordx4 a[84:87], v36, s[38:39] offset:1024 sc1\n\t" \
    "global_load_dwordx4 a[88:91], v37, s[38:39] offset:0 sc1\n\t" \
    "global_load_dwordx4 a[92:95], v37, s[38:39] offset:1024 sc1\n\t" \
    "global_load_dwordx4 a[96:99], v38, s[38:39] offset:0 sc1\n\t" \
    "global_load_dwordx4 a[100:103], v38, s[38:39] offset:1024 sc1\n\t" \
    "global_load_dwordx4 a[104:107], v39, s[38:39] offset:0 sc1\n\t" \
    "global_load_dwordx4 a[108:111], v39, s[38:39] offset:1024 sc1\n\t" \
    "global_load_dwordx4 a[112:115], v40, s[38:39] offset:0 sc1\n\t" \
    "global_load_dwordx4 a[116:119], v40, s[38:39] offset:1024 sc1\n\t" \
    "global_load_dwordx4 a[120:123], v41, s[38:39] offset:0 sc1\n\t" \
    "global_load_dwordx4 a[124:127], v41, s[38:39] offset:1024 sc1\n\t" \
    "s_mul_hi_u32 s69, s85, s78\n\t" \
    "s_add_u32 s79, s81, s69\n\t" \
    "s_add_u32 s79, s79, s87\n\t" \
    "s_cmp_ge_u32 s65, 38\n\t" \
    "s_cselect_b32 s69, 28, 0\n\t" \
    "s_sub_u32 s79, s79, s69\n\t" \
    "s_cmp_lt_u32 s65, 10\n\t" \
    "s_cselect_b32 s69, 96, 0\n\t" \
    "s_add_u32 s79, s79, s69\n\t" \
    "s_add_u32 s80, s79, s88\n\t" \
    "s_add_u32 s79, s79, 320\n\t" \
    "s_add_u32 s79, s79, s43\n\t" \
    "s_sub_u32 s70, s79, s74\n\t" \
    "s_max_i32 s70, s70, 0\n\t" \
    "s_min_i32 s70, s70, 16000\n\t" \
    "s_lshl_b32 s70, s70, 1\n\t" \
    "s_mul_hi_u32 s70, s70, s77\n\t" \
    "s_add_u32 s70, s70, s73\n\t" \
    "rr_W53_%=:\n\t" \
    "s_memtime s[56:57]\n\t" \
    "s_waitcnt lgkmcnt(0)\n\t" \
    "s_sub_u32 s69, s70, s56\n\t" \
    "s_cmp_gt_i32 s69, 24\n\t" \
    "s_cbranch_scc1 rr_W53_%=\n\t" \
    "s_cmp_eq_u32 s51, 0\n\t" \
    "s_cbranch_scc1 rr_arm54_%=\n\t" \
    "global_store_dwordx4 v0, v[172:175], s[40:41] offset:0 sc1\n\t" \
    "global_store_dwordx4 v0, v[172:175], s[40:41] offset:1024 sc1\n\t" \
    "global_store_dwordx4 v0, v[172:175], s[40:41] offset:2048 sc1\n\t" \
    "global_store_dwordx4 v0, v[172:175], s[40:41] offset:3072 sc1\n\t" \
    "global_store_dwordx4 v1, v[172:175], s[40:41] offset:0 sc1\n\t" \
    "global_store_dwordx4 v1, v[172:175], s[40:41] offset:1024 sc1\n\t" \
    "global_store_dwordx4 v1, v[172:175], s[40:41] offset:2048 sc1\n\t" \
    "global_store_dwordx4 v1, v[172:175], s[40:41] offset:3072 sc1\n\t" \
    "s_cmp_gt_u32 s65, 61\n\t" \
    "s_cbranch_scc1 rr_arm54_%=\n\t" \
    "s_add_u32 s69, s65, 2\n\t" \
    "s_bitcmp1_b32 s69, 0\n\t" \
    "s_cselect_b32 s70, s63, s62\n\t" \
    "s_bfe_u32 s69, s69, 0x20001\n\t" \
    "s_lshl_b32 s69, s69, 19\n\t" \
    "s_add_u32 s69, s69, s70\n\t" \
    "s_add_u32 s46, s36, s69\n\t" \
    "s_addc_u32 s47, s37, 0\n\t" \
    "global_store_dwordx4 v164, v[172:175], s[46:47] sc1\n\t" \
    "global_store_dwordx4 v165, v[172:175], s[46:47] sc1\n\t" \
    "global_store_dwordx4 v166, v[172:175], s[46:47] sc1\n\t" \
    "global_store_dwordx4 v167, v[172:175], s[46:47] sc1\n\t" \
    "rr_arm54_%=:\n\t" \
    "s_waitcnt vmcnt(0)\n\t" \
    "v_min_i32_dpp v177, v177, v177 row_ror:1 row_mask:0xf bank_mask:0xf\n\t" \
    "s_nop 1\n\t" \
    "v_min_i32_dpp v177, v177, v177 row_ror:2 row_mask:0xf bank_mask:0xf\n\t" \
    "s_nop 1\n\t" \
    "v_min_i32_dpp v177, v177, v177 row_ror:4 row_mask:0xf bank_mask:0xf\n\t" \
    "s_nop 1\n\t" \
    "v_min_i32_dpp v177, v177, v177 row_ror:8 row_mask:0xf bank_mask:0xf\n\t" \
    "s_nop 0\n\t" \
    "v_readlane_b32 s69, v177, 0\n\t" \
    "v_readlane_b32 s70, v177, 16\n\t" \
    "s_nop 1\n\t" \
    "s_min_i32 s69, s69, s70\n\t" \
    "s_max_i32 s83, s83, s69\n\t" \
    "s_cmp_lg_u32 s65, 40\n\t" \
    "s_cbranch_scc1 rr_f255_%=\n\t" \
    "s_cmp_lg_u32 s67, 35\n\t" \
    "s_cbranch_scc1 rr_f255_%=\n\t" \
    "rr_f2l56_%=:\n\t" \
    "global_load_dword v186, v178, s[36:37] sc1\n\t" \
    "s_waitcnt vmcnt(0)\n\t" \
    "v_cmp_ne_u32 vcc, 1, v186\n\t" \
    "s_cbranch_vccz rr_f255_%=\n\t" \
    "s_cmp_eq_u32 s68, 0\n\t" \
    "s_cbranch_scc1 rr_F57_%=\n\t" \
    "s_sub_u32 s68, s68, 1\n\t" \
    "s_sleep 2\n\t" \
    "s_branch rr_f2l56_%=\n\t" \
    "rr_F57_%=:\n\t" \
    "v_mov_b32 v187, 6095104\n\t" \
    "v_mov_b32 v188, 11\n\t" \
    "global_store_dword v187, v188, s[36:37] sc1\n\t" \
    "s_waitcnt vmcnt(0)\n\t" \
    "s_branch rr_f255_%=\n\t" \
    "rr_f255_%=:\n\t" \
    "s_cmp_eq_u32 s51, 0\n\t" \
    "s_cbranch_scc1 rr_sl24_%=\n\t" \
    "s_add_u32 s69, s65, -1\n\t" \
    "s_bitcmp1_b32 s69, 0\n\t" \
    "s_cselect_b32 s70, s63, s62\n\t" \
    "s_bfe_u32 s69, s69, 0x20001\n\t" \
    "s_lshl_b32 s69, s69, 19\n\t" \
    "s_add_u32 s69, s69, s70\n\t" \
    "s_add_u32 s46, s36, s69\n\t" \
    "s_addc_u32 s47, s37, 0\n\t" \
    "s_add_u32 s79, s80, 480\n\t" \
    "s_sub_u32 s70, s79, s74\n\t" \
    "s_max_i32 s70, s70, 0\n\t" \
    "s_min_i32 s70, s70, 16000\n\t" \
    "s_lshl_b32 s70, s70, 1\n\t" \
    "s_mul_hi_u32 s70, s70, s77\n\t" \
    "s_add_u32 s70, s70, s73\n\t" \
    "rr_W58_%=:\n\t" \
    "s_memtime s[56:57]\n\t" \
    "s_waitcnt lgkmcnt(0)\n\t" \
    "s_sub_u32 s69, s70, s56\n\t" \
    "s_cmp_gt_i32 s69, 24\n\t" \
    "s_cbranch_scc1 rr_W58_%=\n\t" \
    "v_add_u32 v187, 4096, v7\n\t" \
    "global_load_dword v184, v187, s[46:47] sc1\n\t" \
    "s_waitcnt vmcnt(0)\n\t" \
    "v_cmp_eq_u32 vcc, -1, v184\n\t" \
    "s_cbranch_vccz rr_Hok60_%=\n\t" \
    "rr_H59_%=:\n\t" \
    "global_load_dword v184, v187, s[46:47] sc1\n\t" \
    "s_waitcnt vmcnt(0)\n\t" \
    "v_cmp_eq_u32 vcc, -1, v184\n\t" \
    "s_cbranch_vccz rr_Hc61_%=\n\t" \
    "s_cmp_eq_u32 s68, 0\n\t" \
    "s_cbranch_scc1 rr_F62_%=\n\t" \
    "s_sub_u32 s68, s68, 1\n\t" \
    "s_sleep 4\n\t" \
    "s_branch rr_H59_%=\n\t" \
    "rr_F62_%=:\n\t" \
    "v_mov_b32 v185, 6095104\n\t" \
    "v_mov_b32 v186, 19\n\t" \
    "global_store_dword v185, v186, s[36:37] sc1\n\t" \
    "s_waitcnt vmcnt(0)\n\t" \
    "s_branch rr_Hc61_%=\n\t" \
    "rr_Hc61_%=:\n\t" \
    "rr_Hok60_%=:\n\t" \
    "v_subrev_u32 v184, s80, v184\n\t" \
    "s_nop 1\n\t" \
    "v_max_i32_dpp v184, v184, v184 row_ror:1 row_mask:0xf bank_mask:0xf\n\t" \
    "s_nop 1\n\t" \
    "v_max_i32_dpp v184, v184, v184 row_ror:2 row_mask:0xf bank_mask:0xf\n\t" \
    "s_nop 1\n\t" \
    "v_max_i32_dpp v184, v184, v184 row_ror:4 row_mask:0xf bank_mask:0xf\n\t" \
    "s_nop 1\n\t" \
    "v_max_i32_dpp v184, v184, v184 row_ror:8 row_mask:0xf bank_mask:0xf\n\t" \
    "s_nop 0\n\t" \
    "v_readlane_b32 s69, v184, 0\n\t" \
    "v_readlane_b32 s71, v184, 16\n\t" \
    "s_nop 1\n\t" \
    "s_max_i32 s69, s69, s71\n\t" \
    "s_add_u32 s81, s69, s80\n\t" \
    "s_mul_hi_u32 s69, s85, s78\n\t" \
    "s_add_u32 s79, s81, s69\n\t" \
    "s_add_u32 s79, s79, s87\n\t" \
    "s_cmp_ge_u32 s65, 37\n\t" \
    "s_cselect_b32 s69, 28, 0\n\t" \
    "s_sub_u32 s79, s79, s69\n\t" \
    "s_cmp_lt_u32 s65, 9\n\t" \
    "s_cselect_b32 s69, 96, 0\n\t" \
    "s_add_u32 s79, s79, s69\n\t" \
    "s_add_u32 s80, s79, s88\n\t" \
    "s_add_u32 s79, s79, 96\n\t" \
    "s_add_u32 s69, s65, 1\n\t" \
    "s_mul_i32 s70, s69, 171\n\t" \
    "s_lshr_b32 s70, s70, 9\n\t" \
    "s_mul_i32 s70, s70, 3\n\t" \
    "s_sub_u32 s69, s69, s70\n\t" \
    "s_mul_i32 s69, s69, 36\n\t" \
    "s_lshl_b32 s69, s69, 14\n\t" \
    "s_add_u32 s69, s69, s64\n\t" \
    "s_add_u32 s69, s69, 4194304\n\t" \
    "s_add_u32 s40, s36, s69\n\t" \
    "s_addc_u32 s41, s37, 0\n\t" \
    "s_sub_u32 s70, s79, s74\n\t" \
    "s_max_i32 s70, s70, 0\n\t" \
    "s_min_i32 s70, s70, 16000\n\t" \
    "s_lshl_b32 s70, s70, 1\n\t" \
    "s_mul_hi_u32 s70, s70, s77\n\t" \
    "s_add_u32 s70, s70, s73\n\t" \
    "rr_W63_%=:\n\t" \
    "s_memtime s[56:57]\n\t" \
    "s_waitcnt lgkmcnt(0)\n\t" \
    "s_sub_u32 s69, s70, s56\n\t" \
    "s_cmp_gt_i32 s69, 24\n\t" \
    "s_cbranch_scc1 rr_W63_%=\n\t" \
    "global_store_dwordx4 v196, v[172:175], s[40:41] sc1\n\t" \
    "global_store_dwordx4 v197, v[172:175], s[40:41] sc1\n\t" \
    "global_store_dwordx4 v198, v[172:175], s[40:41] sc1\n\t" \
    "s_branch rr_u226_%=\n\t" \
    "rr_sl24_%=:\n\t" \
    "ds_read_b64 v[184:185], v9\n\t" \
    "s_sub_u32 s70, s65, 1\n\t" \
    "s_waitcnt lgkmcnt(0)\n\t" \
    "v_readfirstlane_b32 s69, v185\n\t" \
    "v_readfirstlane_b32 s81, v184\n\t" \
    "s_nop 0\n\t" \
    "s_cmp_eq_u32 s69, s70\n\t" \
    "s_cbranch_scc1 rr_u226_%=\n\t" \
    "s_cmp_eq_u32 s68, 0\n\t" \
    "s_cbranch_scc1 rr_F64_%=\n\t" \
    "s_sub_u32 s68, s68, 1\n\t" \
    "s_sleep 2\n\t" \
    "s_branch rr_sl24_%=\n\t" \
    "rr_F64_%=:\n\t" \
    "v_mov_b32 v184, 6095104\n\t" \
    "v_mov_b32 v185, 14\n\t" \
    "global_store_dword v184, v185, s[36:37] sc1\n\t" \
    "s_waitcnt vmcnt(0)\n\t" \
    "s_branch rr_u226_%=\n\t" \
    "rr_u226_%=:\n\t" \
    "s_add_u32 s69, s65, -1\n\t" \
    "s_bitcmp1_b32 s69, 0\n\t" \
    "s_cselect_b32 s70, s63, s62\n\t" \
    "s_bfe_u32 s69, s69, 0x20001\n\t" \
    "s_lshl_b32 s69, s69, 19\n\t" \
    "s_add_u32 s69, s69, s70\n\t" \
    "s_add_u32 s46, s36, s69\n\t" \
    "s_addc_u32 s47, s37, 0\n\t" \
    "s_cmp_lg_u32 s65, 1\n\t" \
    "s_cbranch_scc1 rr_Oc67_%=\n\t" \
    "s_add_u32 s79, s81, 480\n\t" \
    "s_sub_u32 s70, s79, s74\n\t" \
    "s_max_i32 s70, s70, 0\n\t" \
    "s_min_i32 s70, s70, 16000\n\t" \
    "s_lshl_b32 s70, s70, 1\n\t" \
    "s_mul_hi_u32 s70, s70, s77\n\t" \
    "s_add_u32 s70, s70, s73\n\t" \
    "rr_W68_%=:\n\t" \
    "s_memtime s[56:57]\n\t" \
    "s_waitcnt lgkmcnt(0)\n\t" \
    "s_sub_u32 s69, s70, s56\n\t" \
    "s_cmp_gt_i32 s69, 24\n\t" \
    "s_cbranch_scc1 rr_W68_%=\n\t" \
    "global_load_dword v184, v46, s[46:47] sc1\n\t" \
    "s_waitcnt vmcnt(0)\n\t" \
    "v_cmp_eq_u32 vcc, -1, v184\n\t" \
    "s_cbranch_vccz rr_Ook66_%=\n\t" \
    "rr_O65_%=:\n\t" \
    "global_load_dword v184, v46, s[46:47] offset:1536 sc1\n\t" \
    "s_waitcnt vmcnt(0)\n\t" \
    "v_cmp_eq_u32 vcc, -1, v184\n\t" \
    "s_cbranch_vccz rr_Ook66_%=\n\t" \
    "s_cmp_eq_u32 s68, 0\n\t" \
    "s_cbranch_scc1 rr_F69_%=\n\t" \
    "s_sub_u32 s68, s68, 1\n\t" \
    "s_sleep 3\n\t" \
    "s_branch rr_O65_%=\n\t" \
    "rr_F69_%=:\n\t" \
    "v_mov_b32 v185, 6095104\n\t" \
    "v_mov_b32 v186, 18\n\t" \
    "global_store_dword v185, v186, s[36:37] sc1\n\t" \
    "s_waitcnt vmcnt(0)\n\t" \
    "s_branch rr_Ook66_%=\n\t" \
    "rr_Ook66_%=:\n\t" \
    "v_subrev_u32 v184, s80, v184\n\t" \
    "s_nop 1\n\t" \
    "v_max_i32_dpp v184, v184, v184 row_ror:1 row_mask:0xf bank_mask:0xf\n\t" \
    "s_nop 1\n\t" \
    "v_max_i32_dpp v184, v184, v184 row_ror:2 row_mask:0xf bank_mask:0xf\n\t" \
    "s_nop 1\n\t" \
    "v_max_i32_dpp v184, v184, v184 row_ror:4 row_mask:0xf bank_mask:0xf\n\t" \
    "s_nop 1\n\t" \
    "v_max_i32_dpp v184, v184, v184 row_ror:8 row_mask:0xf bank_mask:0xf\n\t" \
    "s_nop 0\n\t" \
    "v_readlane_b32 s69, v184, 0\n\t" \
    "v_readlane_b32 s71, v184, 16\n\t" \
    "v_readlane_b32 s70, v184, 32\n\t" \
    "v_readlane_b32 s81, v184, 48\n\t" \
    "s_max_i32 s69, s69, s71\n\t" \
    "s_nop 0\n\t" \
    "s_max_i32 s71, s70, s81\n\t" \
    "s_max_i32 s69, s69, s71\n\t" \
    "s_add_u32 s81, s69, s80\n\t" \
    "rr_Oc67_%=:\n\t" \
    "s_mul_hi_u32 s69, s85, s78\n\t" \
    "s_add_u32 s79, s81, s69\n\t" \
    "s_add_u32 s79, s79, s87\n\t" \
    "s_cmp_ge_u32 s65, 37\n\t" \
    "s_cselect_b32 s69, 28, 0\n\t" \
    "s_sub_u32 s79, s79, s69\n\t" \
    "s_cmp_lt_u32 s65, 9\n\t" \
    "s_cselect_b32 s69, 96, 0\n\t" \
    "s_add_u32 s79, s79, s69\n\t" \
    "s_add_u32 s80, s79, s88\n\t" \
    "s_sub_u32 s70, s79, s74\n\t" \
    "s_max_i32 s70, s70, 0\n\t" \
    "s_min_i32 s70, s70, 16000\n\t" \
    "s_lshl_b32 s70, s70, 1\n\t" \
    "s_mul_hi_u32 s70, s70, s77\n\t" \
    "s_add_u32 s70, s70, s73\n\t" \
    "rr_W70_%=:\n\t" \
    "s_memtime s[56:57]\n\t" \
    "s_waitcnt lgkmcnt(0)\n\t" \
    "s_sub_u32 s69, s70, s56\n\t" \
    "s_cmp_gt_i32 s69, 24\n\t" \
    "s_cbranch_scc1 rr_W70_%=\n\t" \
    "rr_act7_%=:\n\t" \
    "s_add_u32 s69, s65, 0\n\t" \
    "s_bitcmp1_b32 s69, 0\n\t" \
    "s_cselect_b32 s70, s63, s62\n\t" \
    "s_bfe_u32 s69, s69, 0x20001\n\t" \
    "s_lshl_b32 s69, s69, 19\n\t" \
    "s_add_u32 s69, s69, s70\n\t" \
    "s_add_u32 s46, s36, s69\n\t" \
    "s_addc_u32 s47, s37, 0\n\t" \
    "s_add_u32 s69, s65, 1\n\t" \
    "s_bitcmp1_b32 s69, 0\n\t" \
    "s_cselect_b32 s70, s63, s62\n\t" \
    "s_bfe_u32 s69, s69, 0x20001\n\t" \
    "s_lshl_b32 s69, s69, 19\n\t" \
    "s_add_u32 s69, s69, s70\n\t" \
    "s_add_u32 s48, s36, s69\n\t" \
    "s_addc_u32 s49, s37, 0\n\t" \
    "s_add_u32 s69, s65, 0\n\t" \
    "s_mul_i32 s70, s69, 171\n\t" \
    "s_lshr_b32 s70, s70, 9\n\t" \
    "s_mul_i32 s70, s70, 3\n\t" \
    "s_sub_u32 s69, s69, s70\n\t" \
    "s_mul_i32 s69, s69, 36\n\t" \
    "s_add_u32 s69, s69, s67\n\t" \
    "s_lshl_b32 s69, s69, 14\n\t" \
    "s_add_u32 s69, s69, s64\n\t" \
    "s_add_u32 s69, s69, 4194304\n\t" \
    "s_add_u32 s40, s36, s69\n\t" \
    "s_addc_u32 s41, s37, 0\n\t" \
    "s_add_u32 s69, s65, 2\n\t" \
    "s_cmp_ge_i32 s83, s69\n\t" \
    "s_cbranch_scc1 rr_nf71_%=\n\t" \
    "global_load_dword v177, v6, s[36:37] sc1\n\t" \
    "rr_nf71_%=:\n\t" \
    "s_mov_b32 s71, 3\n\t" \
    "rr_E72_%=:\n\t" \
    "s_memtime s[54:55]\n\t" \
    "s_nop 0\n\t" \
    "s_memrealtime s[4:5]\n\t" \
    "s_nop 0\n\t" \
    "s_memrealtime s[6:7]\n\t" \
    "s_nop 0\n\t" \
    "s_memrealtime s[8:9]\n\t" \
    "s_nop 0\n\t" \
    "s_memrealtime s[10:11]\n\t" \
    "s_nop 0\n\t" \
    "s_memrealtime s[12:13]\n\t" \
    "s_nop 0\n\t" \
    "s_memrealtime s[14:15]\n\t" \
    "s_nop 0\n\t" \
    "s_memrealtime s[16:17]\n\t" \
    "s_nop 0\n\t" \
    "s_memrealtime s[18:19]\n\t" \
    "s_nop 0\n\t" \
    "s_memrealtime s[20:21]\n\t" \
    "s_nop 0\n\t" \
    "s_memrealtime s[22:23]\n\t" \
    "s_nop 0\n\t" \
    "s_memrealtime s[24:25]\n\t" \
    "s_nop 0\n\t" \
    "s_memrealtime s[26:27]\n\t" \
    "s_nop 0\n\t" \
    "s_memrealtime s[28:29]\n\t" \
    "s_nop 0\n\t" \
    "s_memtime s[56:57]\n\t" \
    "s_mov_b32 s70, 1\n\t" \
    "s_sub_u32 s71, s71, 1\n\t" \
    "s_cselect_b32 s69, 0, -1\n\t" \
    "s_waitcnt lgkmcnt(0)\n\t" \
    "s_sub_u32 s54, s56, s54\n\t" \
    "s_and_b32 s54, s54, s69\n\t" \
    "s_cmp_gt_u32 s54, 124\n\t" \
    "s_cbranch_scc1 rr_E72_%=\n\t" \
    "s_cmp_eq_u32 s4, s28\n\t" \
    "s_addc_u32 s70, s70, 0\n\t" \
    "s_cmp_eq_u32 s6, s28\n\t" \
    "s_addc_u32 s70, s70, 0\n\t" \
    "s_cmp_eq_u32 s8, s28\n\t" \
    "s_addc_u32 s70, s70, 0\n\t" \
    "s_cmp_eq_u32 s10, s28\n\t" \
    "s_addc_u32 s70, s70, 0\n\t" \
    "s_cmp_eq_u32 s12, s28\n\t" \
    "s_addc_u32 s70, s70, 0\n\t" \
    "s_cmp_eq_u32 s14, s28\n\t" \
    "s_addc_u32 s70, s70, 0\n\t" \
    "s_cmp_eq_u32 s16, s28\n\t" \
    "s_addc_u32 s70, s70, 0\n\t" \
    "s_cmp_eq_u32 s18, s28\n\t" \
    "s_addc_u32 s70, s70, 0\n\t" \
    "s_cmp_eq_u32 s20, s28\n\t" \
    "s_addc_u32 s70, s70, 0\n\t" \
    "s_cmp_eq_u32 s22, s28\n\t" \
    "s_addc_u32 s70, s70, 0\n\t" \
    "s_cmp_eq_u32 s24, s28\n\t" \
    "s_addc_u32 s70, s70, 0\n\t" \
    "s_cmp_eq_u32 s26, s28\n\t" \
    "s_addc_u32 s70, s70, 0\n\t" \
    "s_mov_b32 s75, s73\n\t" \
    "s_mov_b32 s76, s74\n\t" \
    "s_lshl_b32 s70, s70, 3\n\t" \
    "s_sub_u32 s73, s56, s70\n\t" \
    "s_lshl_b32 s74, s28, 4\n\t" \
    "s_sub_u32 s69, s73, s75\n\t" \
    "s_sub_u32 s70, s74, s76\n\t" \
    "s_sub_u32 s71, s70, 1600\n\t" \
    "s_cmp_lt_u32 s71, 400000\n\t" \
    "s_cbranch_scc0 rr_K74_%=\n\t" \
    "v_cvt_f32_u32 v184, s69\n\t" \
    "v_cvt_f32_u32 v185, s70\n\t" \
    "v_rcp_f32 v186, v184\n\t" \
    "v_rcp_f32 v187, v185\n\t" \
    "v_mul_f32 v186, v186, v185\n\t" \
    "v_mul_f32 v187, v187, v184\n\t" \
    "v_mul_f32 v186, 0x4f800000, v186\n\t" \
    "v_mul_f32 v187, 0x4f000000, v187\n\t" \
    "v_cvt_u32_f32 v186, v186\n\t" \
    "v_cvt_u32_f32 v187, v187\n\t" \
    "v_readfirstlane_b32 s69, v186\n\t" \
    "v_readfirstlane_b32 s70, v187\n\t" \
    "s_nop 1\n\t" \
    "s_sub_u32 s69, s69, s78\n\t" \
    "s_ashr_i32 s69, s69, 2\n\t" \
    "s_add_u32 s78, s78, s69\n\t" \
    "s_sub_u32 s70, s70, s77\n\t" \
    "s_ashr_i32 s70, s70, 2\n\t" \
    "s_add_u32 s77, s77, s70\n\t" \
    "rr_K74_%=:\n\t" \
    "s_mov_b32 s91, 0\n\t" \
    "v_mov_b32 v160, 0\n\t" \
    "v_mov_b32 v161, 0\n\t" \
    "v_mov_b32 v162, 0\n\t" \
    "v_mov_b32 v163, 0\n\t" \
    "s_sub_u32 s70, s80, s74\n\t" \
    "s_max_i32 s70, s70, 0\n\t" \
    "s_min_i32 s70, s70, 16000\n\t" \
    "s_lshl_b32 s70, s70, 1\n\t" \
    "s_mul_hi_u32 s70, s70, s77\n\t" \
    "s_add_u32 s70, s70, s73\n\t" \
    "s_sub_u32 s70, s70, 100\n\t" \
    "s_getpc_b64 s[30:31]\n\t" \
    "rr_Wp81_%=:\n\t" \
    "s_add_u32 s30, s30, rr_We82_%=-rr_Wp81_%=\n\t" \
    "s_addc_u32 s31, s31, 0\n\t" \
    "rr_W80_%=:\n\t" \
    "s_memtime s[56:57]\n\t" \
    "s_waitcnt lgkmcnt(0)\n\t" \
    "s_sub_u32 s69, s70, s56\n\t" \
    "s_cmp_gt_i32 s69, 111\n\t" \
    "s_cbranch_scc1 rr_W80_%=\n\t" \
    "s_max_i32 s69, s69, 0\n\t" \
    "s_lshr_b32 s69, s69, 1\n\t" \
    "s_and_b32 s69, s69, 0x3c\n\t" \
    "s_sub_u32 s30, s30, s69\n\t" \
    "s_subb_u32 s31, s31, 0\n\t" \
    "s_setpc_b64 s[30:31]\n\t" \
    ".p2align 6\n\t" \
    "s_nop 0\n\t" \
    "s_nop 0\n\t" \
    "s_nop 0\n\t" \
    "s_nop 1\n\t" \
    "s_nop 1\n\t" \
    "s_nop 1\n\t" \
    "s_nop 1\n\t" \
    "s_nop 1\n\t" \
    "s_nop 1\n\t" \
    "s_nop 1\n\t" \
    "s_nop 1\n\t" \
    "s_nop 1\n\t" \
    "s_nop 1\n\t" \
    "s_nop 1\n\t" \
    "s_nop 1\n\t" \
    "s_nop 1\n\t" \
    "rr_We82_%=:\n\t" \
    "global_load_dword v184, v7, s[46:47] sc1\n\t" \
    "s_sub_u32 s97, s74, s86\n\t" \
    "s_sub_u32 s71, s73, 190\n\t" \
    "s_getpc_b64 s[30:31]\n\t" \
    "rr_wp83_%=:\n\t" \
    "s_add_u32 s30, s30, rr_again9_%=-rr_wp83_%=\n\t" \
    "s_addc_u32 s31, s31, (rr_again9_%=-rr_wp83_%=)>>32\n\t" \
    "s_waitcnt vmcnt(0) lgkmcnt(0)\n\t" \
    "rr_hok75_%=:\n\t" \
    "s_memtime s[56:57]\n\t" \
    "v_cmp_eq_u32 vcc, -1, v184\n\t" \
    "v_subrev_u32 v185, s97, v184\n\t" \
    "s_nop 1\n\t" \
    "v_max_i32_dpp v185, v185, v185 row_ror:1 row_mask:0xf bank_mask:0xf\n\t" \
    "s_cbranch_vccnz rr_hlate76_%=\n\t" \
    "s_nop 0\n\t" \
    "v_max_i32_dpp v185, v185, v185 row_ror:2 row_mask:0xf bank_mask:0xf\n\t" \
    "s_nop 1\n\t" \
    "v_max_i32_dpp v185, v185, v185 row_ror:4 row_mask:0xf bank_mask:0xf\n\t" \
    "s_nop 1\n\t" \
    "v_max_i32_dpp v185, v185, v185 row_ror:8 row_mask:0xf bank_mask:0xf\n\t" \
    "s_nop 0\n\t" \
    "v_readlane_b32 s69, v185, 0\n\t" \
    "v_readlane_b32 s70, v185, 16\n\t" \
    "s_waitcnt lgkmcnt(0)\n\t" \
    "s_sub_u32 s71, s71, s56\n\t" \
    "s_max_i32 s81, s69, s70\n\t" \
    "s_max_i32 s69, s81, 0\n\t" \
    "s_lshl_b32 s69, s69, 1\n\t" \
    "s_mul_hi_u32 s69, s69, s77\n\t" \
    "s_add_u32 s69, s69, s71\n\t" \
    "s_cmp_gt_i32 s69, 111\n\t" \
    "s_cbranch_scc1 rr_wl77_%=\n\t" \
    "rr_wf79_%=:\n\t" \
    "s_max_i32 s69, s69, 0\n\t" \
    "s_lshr_b32 s69, s69, 1\n\t" \
    "s_and_b32 s69, s69, 0x3c\n\t" \
    "s_sub_u32 s30, s30, s69\n\t" \
    "s_subb_u32 s31, s31, 0\n\t" \
    "s_setpc_b64 s[30:31]\n\t" \
    "rr_wl77_%=:\n\t" \
    "s_min_i32 s69, s69, 24000\n\t" \
    "s_add_u32 s70, s69, s56\n\t" \
    "s_add_u32 s70, s70, 62\n\t" \
    "rr_wp78_%=:\n\t" \
    "s_memtime s[56:57]\n\t" \
    "s_waitcnt lgkmcnt(0)\n\t" \
    "s_sub_u32 s69, s70, s56\n\t" \
    "s_cmp_gt_i32 s69, 111\n\t" \
    "s_cbranch_scc1 rr_wp78_%=\n\t" \
    "s_branch rr_wf79_%=\n\t" \
    ".p2align 6\n\t" \
    "s_nop 0\n\t" \
    "s_nop 0\n\t" \
    "s_nop 0\n\t" \
    "s_nop 1\n\t" \
    "s_nop 1\n\t" \
    "s_nop 1\n\t" \
    "s_nop 1\n\t" \
    "s_nop 1\n\t" \
    "s_nop 1\n\t" \
    "s_nop 1\n\t" \
    "s_nop 1\n\t" \
    "s_nop 1\n\t" \
    "s_nop 1\n\t" \
    "s_nop 1\n\t" \
    "s_nop 1\n\t" \
    "s_nop 1\n\t" \
    "rr_again9_%=:\n\t" \
    "global_load_dwordx4 v[48:51], v10, s[46:47] sc1\n\t" \
    "global_load_dwordx4 v[52:55], v11, s[46:47] sc1\n\t" \
    "global_load_dwordx4 v[56:59], v12, s[46:47] sc1\n\t" \
    "global_load_dwordx4 v[60:63], v13, s[46:47] sc1\n\t" \
    "global_load_dwordx4 v[64:67], v14, s[46:47] sc1\n\t" \
    "global_load_dwordx4 v[68:71], v15, s[46:47] sc1\n\t" \
    "global_load_dwordx4 v[72:75], v16, s[46:47] sc1\n\t" \
    "global_load_dwordx4 v[76:79], v17, s[46:47] sc1\n\t" \
    "global_load_dwordx4 v[80:83], v18, s[46:47] sc1\n\t" \
    "global_load_dwordx4 v[84:87], v19, s[46:47] sc1\n\t" \
    "global_load_dwordx4 v[88:91], v20, s[46:47] sc1\n\t" \
    "global_load_dwordx4 v[92:95], v21, s[46:47] sc1\n\t" \
    "global_load_dwordx4 v[96:99], v22, s[46:47] sc1\n\t" \
    "global_load_dwordx4 v[100:103], v23, s[46:47] sc1\n\t" \
    "global_load_dwordx4 v[104:107], v24, s[46:47] sc1\n\t" \
    "global_load_dwordx4 v[108:111], v25, s[46:47] sc1\n\t" \
    "global_load_dwordx4 v[112:115], v42, s[46:47] sc1\n\t" \
    "global_load_dwordx4 v[116:119], v43, s[46:47] sc1\n\t" \
    "global_load_dwordx4 v[120:123], v44, s[46:47] sc1\n\t" \
    "global_load_dwordx4 v[124:127], v45, s[46:47] sc1\n\t" \
    "global_load_dwordx4 v[128:131], v0, s[40:41] offset:0 sc1\n\t" \
    "global_load_dwordx4 v[132:135], v0, s[40:41] offset:1024 sc1\n\t" \
    "global_load_dwordx4 v[136:139], v0, s[40:41] offset:2048 sc1\n\t" \
    "global_load_dwordx4 v[140:143], v0, s[40:41] offset:3072 sc1\n\t" \
    "global_load_dwordx4 v[144:147], v1, s[40:41] offset:0 sc1\n\t" \
    "global_load_dwordx4 v[148:151], v1, s[40:41] offset:1024 sc1\n\t" \
    "global_load_dwordx4 v[152:155], v1, s[40:41] offset:2048 sc1\n\t" \
    "global_load_dwordx4 v[156:159], v1, s[40:41] offset:3072 sc1\n\t" \
    "s_cmp_lg_u32 s91, 0\n\t" \
    "s_cbranch_scc1 rr_nob12_%=\n\t" \
    "s_add_u32 s81, s81, s97\n\t" \
    "v_mov_b32 v184, s81\n\t" \
    "v_mov_b32 v185, s65\n\t" \
    "ds_write_b64 v9, v[184:185]\n\t" \
    "rr_nob12_%=:\n\t" \
    "s_waitcnt vmcnt(27)\n\t" \
    "v_smfmac_f32_16x16x64_bf16 v[160:163], v[48:51], a[0:7], v4\n\t" \
    "s_waitcnt vmcnt(26)\n\t" \
    "v_smfmac_f32_16x16x64_bf16 v[160:163], v[52:55], a[8:15], v4\n\t" \
    "s_waitcnt vmcnt(25)\n\t" \
    "v_smfmac_f32_16x16x64_bf16 v[160:163], v[56:59], a[16:23], v4\n\t" \
    "s_waitcnt vmcnt(24)\n\t" \
    "v_smfmac_f32_16x16x64_bf16 v[160:163], v[60:63], a[24:31], v4\n\t" \
    "s_waitcnt vmcnt(23)\n\t" \
    "v_smfmac_f32_16x16x64_bf16 v[160:163], v[64:67], a[32:39], v4\n\t" \
    "s_waitcnt vmcnt(22)\n\t" \
    "v_smfmac_f32_16x16x64_bf16 v[160:163], v[68:71], a[40:47], v4\n\t" \
    "s_waitcnt vmcnt(21)\n\t" \
    "v_smfmac_f32_16x16x64_bf16 v[160:163], v[72:75], a[48:55], v4\n\t" \
    "s_waitcnt vmcnt(20)\n\t" \
    "v_smfmac_f32_16x16x64_bf16 v[160:163], v[76:79], a[56:63], v4\n\t" \
    "s_waitcnt vmcnt(19)\n\t" \
    "v_smfmac_f32_16x16x64_bf16 v[160:163], v[80:83], a[64:71], v4\n\t" \
    "s_waitcnt vmcnt(18)\n\t" \
    "v_smfmac_f32_16x16x64_bf16 v[160:163], v[84:87], a[72:79], v4\n\t" \
    "s_waitcnt vmcnt(17)\n\t" \
    "v_smfmac_f32_16x16x64_bf16 v[160:163], v[88:91], a[80:87], v4\n\t" \
    "s_waitcnt vmcnt(16)\n\t" \
    "v_smfmac_f32_16x16x64_bf16 v[160:163], v[92:95], a[88:95], v4\n\t" \
    "s_waitcnt vmcnt(15)\n\t" \
    "v_smfmac_f32_16x16x64_bf16 v[160:163], v[96:99], a[96:103], v4\n\t" \
    "s_waitcnt vmcnt(14)\n\t" \
    "v_smfmac_f32_16x16x64_bf16 v[160:163], v[100:103], a[104:111], v4\n\t" \
    "s_waitcnt vmcnt(13)\n\t" \
    "v_smfmac_f32_16x16x64_bf16 v[160:163], v[104:107], a[112:119], v4\n\t" \
    "s_waitcnt vmcnt(12)\n\t" \
    "v_smfmac_f32_16x16x64_bf16 v[160:163], v[108:111], a[120:127], v4\n\t" \
    "s_waitcnt vmcnt(6)\n\t" \
    "v_smfmac_f32_16x16x64_bf16 v[160:163], v[112:115], v[128:135], v4\n\t" \
    "s_waitcnt vmcnt(4)\n\t" \
    "v_smfmac_f32_16x16x64_bf16 v[160:163], v[116:119], v[136:143], v4\n\t" \
    "s_waitcnt vmcnt(2)\n\t" \
    "v_smfmac_f32_16x16x64_bf16 v[160:163], v[120:123], v[144:151], v4\n\t" \
    "s_waitcnt vmcnt(0)\n\t" \
    "v_smfmac_f32_16x16x64_bf16 v[160:163], v[124:127], v[152:159], v4\n\t" \
    "s_nop 7\n\t" \
    "v_pk_add_f32 v[160:161], v[160:161], v[162:163]\n\t" \
    "v_cvt_pk_bf16_f32 v184, v160, v161\n\t" \
    "v_cmp_u_f32 vcc, v160, v161\n\t" \
    "s_cbranch_vccnz rr_bad10_%=\n\t" \
    "rr_cont11_%=:\n\t" \
    "v_mov_b32_dpp v185, v184 quad_perm:[1,0,3,2] row_mask:0xf bank_mask:0xf\n\t" \
    "v_perm_b32 v184, v185, v184, v5\n\t" \
    "global_store_dword v2, v184, s[48:49] sc1\n\t" \
    "global_store_dword v181, v184, s[48:49] sc1\n\t" \
    "global_store_dword v182, v184, s[48:49] sc1\n\t" \
    "global_store_dword v183, v184, s[48:49] sc1\n\t" \
    "s_cmp_eq_u32 s91, 3\n\t" \
    "s_cbranch_scc1 rr_dry27_%=\n\t" \
    "s_cmp_eq_u32 s65, 62\n\t" \
    "s_cbranch_scc1 rr_end8_%=\n\t" \
    "s_add_u32 s65, s65, 2\n\t" \
    "s_branch rr_pf13_%=\n\t" \
    "rr_bad10_%=:\n\t" \
    "s_waitcnt lgkmcnt(0)\n\t" \
    "v_max_u32 v185, v48, v49\n\t" \
    "v_max3_u32 v185, v185, v50, v51\n\t" \
    "v_max3_u32 v185, v185, v52, v53\n\t" \
    "v_max3_u32 v185, v185, v54, v55\n\t" \
    "v_max3_u32 v185, v185, v56, v57\n\t" \
    "v_max3_u32 v185, v185, v58, v59\n\t" \
    "v_max3_u32 v185, v185, v60, v61\n\t" \
    "v_max3_u32 v185, v185, v62, v63\n\t" \
    "v_max3_u32 v185, v185, v64, v65\n\t" \
    "v_max3_u32 v185, v185, v66, v67\n\t" \
    "v_max3_u32 v185, v185, v68, v69\n\t" \
    "v_max3_u32 v185, v185, v70, v71\n\t" \
    "v_max3_u32 v185, v185, v72, v73\n\t" \
    "v_max3_u32 v185, v185, v74, v75\n\t" \
    "v_max3_u32 v185, v185, v76, v77\n\t" \
    "v_max3_u32 v185, v185, v78, v79\n\t" \
    "v_max3_u32 v185, v185, v80, v81\n\t" \
    "v_max3_u32 v185, v185, v82, v83\n\t" \
    "v_max3_u32 v185, v185, v84, v85\n\t" \
    "v_max3_u32 v185, v185, v86, v87\n\t" \
    "v_max3_u32 v185, v185, v88, v89\n\t" \
    "v_max3_u32 v185, v185, v90, v91\n\t" \
    "v_max3_u32 v185, v185, v92, v93\n\t" \
    "v_max3_u32 v185, v185, v94, v95\n\t" \
    "v_max3_u32 v185, v185, v96, v97\n\t" \
    "v_max3_u32 v185, v185, v98, v99\n\t" \
    "v_max3_u32 v185, v185, v100, v101\n\t" \
    "v_max3_u32 v185, v185, v102, v103\n\t" \
    "v_max3_u32 v185, v185, v104, v105\n\t" \
    "v_max3_u32 v185, v185, v106, v107\n\t" \
    "v_max3_u32 v185, v185, v108, v109\n\t" \
    "v_max3_u32 v185, v185, v110, v111\n\t" \
    "v_max3_u32 v185, v185, v112, v113\n\t" \
    "v_max3_u32 v185, v185, v114, v115\n\t" \
    "v_max3_u32 v185, v185, v116, v117\n\t" \
    "v_max3_u32 v185, v185, v118, v119\n\t" \
    "v_max3_u32 v185, v185, v120, v121\n\t" \
    "v_max3_u32 v185, v185, v122, v123\n\t" \
    "v_max3_u32 v185, v185, v124, v125\n\t" \
    "v_max3_u32 v185, v185, v126, v127\n\t" \
    "v_max_u32 v186, v128, v129\n\t" \
    "v_max3_u32 v186, v186, v130, v131\n\t" \
    "v_max3_u32 v186, v186, v132, v133\n\t" \
    "v_max3_u32 v186, v186, v134, v135\n\t" \
    "v_max3_u32 v186, v186, v136, v137\n\t" \
    "v_max3_u32 v186, v186, v138, v139\n\t" \
    "v_max3_u32 v186, v186, v140, v141\n\t" \
    "v_max3_u32 v186, v186, v142, v143\n\t" \
    "v_max3_u32 v186, v186, v144, v145\n\t" \
    "v_max3_u32 v186, v186, v146, v147\n\t" \
    "v_max3_u32 v186, v186, v148, v149\n\t" \
    "v_max3_u32 v186, v186, v150, v151\n\t" \
    "v_max3_u32 v186, v186, v152, v153\n\t" \
    "v_max3_u32 v186, v186, v154, v155\n\t" \
    "v_max3_u32 v186, v186, v156, v157\n\t" \
    "v_max3_u32 v186, v186, v158, v159\n\t" \
    "v_max_u32 v185, v185, v186\n\t" \
    "v_cmp_eq_u32 vcc, -1, v185\n\t" \
    "s_cbranch_vccz rr_cont11_%=\n\t" \
    "s_mov_b32 s91, 1\n\t" \
    "v_mov_b32 v160, 0\n\t" \
    "v_mov_b32 v161, 0\n\t" \
    "v_mov_b32 v162, 0\n\t" \
    "v_mov_b32 v163, 0\n\t" \
    "s_cmp_eq_u32 s68, 0\n\t" \
    "s_cbranch_scc1 rr_F84_%=\n\t" \
    "s_sub_u32 s68, s68, 1\n\t" \
    "s_branch rr_again9_%=\n\t" \
    "rr_F84_%=:\n\t" \
    "v_mov_b32 v186, 6095104\n\t" \
    "v_mov_b32 v187, 3\n\t" \
    "global_store_dword v186, v187, s[36:37] sc1\n\t" \
    "s_waitcnt vmcnt(0)\n\t" \
    "s_branch rr_cont11_%=\n\t" \
    "rr_hlate76_%=:\n\t" \
    "s_waitcnt lgkmcnt(0)\n\t" \
    "v_add_u32 v189, 1024, v7\n\t" \
    "global_load_dword v186, v189, s[46:47] sc1\n\t" \
    "s_sleep 4\n\t" \
    "v_add_u32 v189, 2048, v7\n\t" \
    "global_load_dword v187, v189, s[46:47] sc1\n\t" \
    "s_waitcnt vmcnt(1)\n\t" \
    "v_cmp_eq_u32 vcc, -1, v186\n\t" \
    "s_cbranch_vccz rr_hg85_%=\n\t" \
    "v_add_u32 v189, 3072, v7\n\t" \
    "global_load_dword v186, v189, s[46:47] sc1\n\t" \
    "s_waitcnt vmcnt(1)\n\t" \
    "v_cmp_eq_u32 vcc, -1, v187\n\t" \
    "s_cbranch_vccz rr_hg86_%=\n\t" \
    "s_waitcnt vmcnt(0)\n\t" \
    "v_cmp_eq_u32 vcc, -1, v186\n\t" \
    "s_cbranch_vccz rr_hg85_%=\n\t" \
    "rr_hp87_%=:\n\t" \
    "global_load_dword v184, v189, s[46:47] sc1\n\t" \
    "s_waitcnt vmcnt(0)\n\t" \
    "v_cmp_eq_u32 vcc, -1, v184\n\t" \
    "s_cbranch_vccz rr_hpd88_%=\n\t" \
    "s_cmp_eq_u32 s68, 0\n\t" \
    "s_cbranch_scc1 rr_F89_%=\n\t" \
    "s_sub_u32 s68, s68, 1\n\t" \
    "s_sleep 1\n\t" \
    "s_branch rr_hp87_%=\n\t" \
    "rr_F89_%=:\n\t" \
    "v_mov_b32 v186, 6095104\n\t" \
    "v_mov_b32 v187, 7\n\t" \
    "global_store_dword v186, v187, s[36:37] sc1\n\t" \
    "s_waitcnt vmcnt(0)\n\t" \
    "s_branch rr_hpd88_%=\n\t" \
    "rr_hpd88_%=:\n\t" \
    "rr_h025_%=:\n\t" \
    "v_and_b32 v184, -2, v184\n\t" \
    "s_mov_b32 s71, 3\n\t" \
    "rr_E90_%=:\n\t" \
    "s_memtime s[54:55]\n\t" \
    "s_nop 0\n\t" \
    "s_memrealtime s[4:5]\n\t" \
    "s_nop 0\n\t" \
    "s_memrealtime s[6:7]\n\t" \
    "s_nop 0\n\t" \
    "s_memrealtime s[8:9]\n\t" \
    "s_nop 0\n\t" \
    "s_memrealtime s[10:11]\n\t" \
    "s_nop 0\n\t" \
    "s_memrealtime s[12:13]\n\t" \
    "s_nop 0\n\t" \
    "s_memrealtime s[14:15]\n\t" \
    "s_nop 0\n\t" \
    "s_memrealtime s[16:17]\n\t" \
    "s_nop 0\n\t" \
    "s_memrealtime s[18:19]\n\t" \
    "s_nop 0\n\t" \
    "s_memrealtime s[20:21]\n\t" \
    "s_nop 0\n\t" \
    "s_memrealtime s[22:23]\n\t" \
    "s_nop 0\n\t" \
    "s_memrealtime s[24:25]\n\t" \
    "s_nop 0\n\t" \
    "s_memrealtime s[26:27]\n\t" \
    "s_nop 0\n\t" \
    "s_memrealtime s[28:29]\n\t" \
    "s_nop 0\n\t" \
    "s_memtime s[56:57]\n\t" \
    "s_mov_b32 s70, 1\n\t" \
    "s_sub_u32 s71, s71, 1\n\t" \
    "s_cselect_b32 s69, 0, -1\n\t" \
    "s_waitcnt lgkmcnt(0)\n\t" \
    "s_sub_u32 s54, s56, s54\n\t" \
    "s_and_b32 s54, s54, s69\n\t" \
    "s_cmp_gt_u32 s54, 124\n\t" \
    "s_cbranch_scc1 rr_E90_%=\n\t" \
    "s_cmp_eq_u32 s4, s28\n\t" \
    "s_addc_u32 s70, s70, 0\n\t" \
    "s_cmp_eq_u32 s6, s28\n\t" \
    "s_addc_u32 s70, s70, 0\n\t" \
    "s_cmp_eq_u32 s8, s28\n\t" \
    "s_addc_u32 s70, s70, 0\n\t" \
    "s_cmp_eq_u32 s10, s28\n\t" \
    "s_addc_u32 s70, s70, 0\n\t" \
    "s_cmp_eq_u32 s12, s28\n\t" \
    "s_addc_u32 s70, s70, 0\n\t" \
    "s_cmp_eq_u32 s14, s28\n\t" \
    "s_addc_u32 s70, s70, 0\n\t" \
    "s_cmp_eq_u32 s16, s28\n\t" \
    "s_addc_u32 s70, s70, 0\n\t" \
    "s_cmp_eq_u32 s18, s28\n\t" \
    "s_addc_u32 s70, s70, 0\n\t" \
    "s_cmp_eq_u32 s20, s28\n\t" \
    "s_addc_u32 s70, s70, 0\n\t" \
    "s_cmp_eq_u32 s22, s28\n\t" \
    "s_addc_u32 s70, s70, 0\n\t" \
    "s_cmp_eq_u32 s24, s28\n\t" \
    "s_addc_u32 s70, s70, 0\n\t" \
    "s_cmp_eq_u32 s26, s28\n\t" \
    "s_addc_u32 s70, s70, 0\n\t" \
    "s_mov_b32 s75, s73\n\t" \
    "s_mov_b32 s76, s74\n\t" \
    "s_lshl_b32 s70, s70, 3\n\t" \
    "s_sub_u32 s73, s56, s70\n\t" \
    "s_lshl_b32 s74, s28, 4\n\t" \
    "s_sub_u32 s97, s74, s86\n\t" \
    "s_sub_u32 s71, s73, 190\n\t" \
    "s_getpc_b64 s[30:31]\n\t" \
    "rr_wp92_%=:\n\t" \
    "s_add_u32 s30, s30, rr_again9_%=-rr_wp92_%=\n\t" \
    "s_addc_u32 s31, s31, (rr_again9_%=-rr_wp92_%=)>>32\n\t" \
    "s_branch rr_hok75_%=\n\t" \
    "rr_hg85_%=:\n\t" \
    "v_mov_b32 v184, v186\n\t" \
    "s_branch rr_hok75_%=\n\t" \
    "rr_hg86_%=:\n\t" \
    "v_mov_b32 v184, v187\n\t" \
    "s_branch rr_hok75_%=\n\t" \
    "rr_end8_%=:\n\t" \
    "s_waitcnt vmcnt(0) lgkmcnt(0)\n\t" \
    "s_endpgm\n\t" \
    "rr_last_%=:\n\t" \
    ""
#define ASM_O \
    "s_mov_b64 s[36:37], %0\n\t" \
    "s_mov_b64 s[42:43], %1\n\t" \
    "s_mov_b64 s[44:45], %2\n\t" \
    "s_mov_b32 s66, %3\n\t" \
    "s_mov_b32 s67, %4\n\t" \
    "s_mov_b64 s[50:51], %5\n\t" \
    "s_mov_b32 s82, %7\n\t" \
    "v_mbcnt_lo_u32_b32 v223, -1, 0\n\t" \
    "v_mbcnt_hi_u32_b32 v223, -1, v223\n\t" \
    "s_getpc_b64 s[46:47]\n\t" \
    "oo_pc_%=:\n\t" \
    "s_lshl_b32 s69, s66, 13\n\t" \
    "v_lshl_add_u32 v222, v223, 7, s69\n\t" \
    "v_min_u32 v222, oo_last_%=-oo_pc_%=-4, v222\n\t" \
    "global_load_dword v222, v222, s[46:47]\n\t" \
    "v_lshlrev_b32 v223, 2, v223\n\t" \
    "v_add_u32 v223, s82, v223\n\t" \
    "ds_read_b32 v0, v223 offset:0\n\t" \
    "ds_read_b32 v1, v223 offset:256\n\t" \
    "ds_read_b32 v2, v223 offset:512\n\t" \
    "ds_read_b32 v3, v223 offset:768\n\t" \
    "ds_read_b32 v4, v223 offset:1024\n\t" \
    "ds_read_b32 v5, v223 offset:1280\n\t" \
    "ds_read_b32 v6, v223 offset:1536\n\t" \
    "ds_read_b32 v7, v223 offset:1792\n\t" \
    "ds_read_b32 v8, v223 offset:2048\n\t" \
    "ds_read_b32 v9, v223 offset:2304\n\t" \
    "ds_read_b32 v10, v223 offset:2560\n\t" \
    "ds_read_b32 v11, v223 offset:2816\n\t" \
    "s_waitcnt lgkmcnt(0)\n\t" \
    "ds_read_b32 v12, v223 offset:3072\n\t" \
    "ds_read_b32 v13, v223 offset:3328\n\t" \
    "ds_read_b32 v14, v223 offset:3584\n\t" \
    "ds_read_b32 v15, v223 offset:3840\n\t" \
    "ds_read_b32 v16, v223 offset:4096\n\t" \
    "ds_read_b32 v17, v223 offset:4352\n\t" \
    "s_waitcnt lgkmcnt(0)\n\t" \
    "s_mov_b32 s68, 262144\n\t" \
    "s_mov_b32 s72, 0\n\t" \
    "s_mov_b32 s83, -1\n\t" \
    "s_mov_b32 s65, 0\n\t" \
    "s_barrier\n\t" \
    "s_mov_b32 s65, s67\n\t" \
    "s_mov_b32 s96, s67\n\t" \
    "s_add_u32 s38, s36, 48234496\n\t" \
    "s_addc_u32 s39, s37, 0\n\t" \
    "oo_poll1_%=:\n\t" \
    "s_cmp_gt_u32 s96, 34\n\t" \
    "s_cselect_b64 vcc, -1, 0\n\t" \
    "v_cndmask_b32 v25, v12, v13, vcc\n\t" \
    "global_load_dword v24, v25, s[36:37] sc1\n\t" \
    "s_waitcnt vmcnt(0)\n\t" \
    "v_cmp_gt_i32 vcc, s96, v24\n\t" \
    "s_cbranch_vccz oo_go2_%=\n\t" \
    "s_cmp_eq_u32 s68, 0\n\t" \
    "s_cbranch_scc1 oo_F7_%=\n\t" \
    "s_sub_u32 s68, s68, 1\n\t" \
    "s_sleep 10\n\t" \
    "s_branch oo_poll1_%=\n\t" \
    "oo_F7_%=:\n\t" \
    "v_mov_b32 v25, 6095104\n\t" \
    "v_mov_b32 v26, 40\n\t" \
    "global_store_dword v25, v26, s[36:37] sc1\n\t" \
    "s_waitcnt vmcnt(0)\n\t" \
    "s_branch oo_go2_%=\n\t" \
    "oo_go2_%=:\n\t" \
    "s_mul_i32 s69, s96, 65536\n\t" \
    "s_add_u32 s69, s69, 146800640\n\t" \
    "s_add_u32 s40, s36, s69\n\t" \
    "s_addc_u32 s41, s37, 0\n\t" \
    "s_lshl_b32 s69, s96, 15\n\t" \
    "v_xor_b32 v18, s69, v0\n\t" \
    "v_xor_b32 v19, s69, v1\n\t" \
    "v_xor_b32 v20, s69, v2\n\t" \
    "v_xor_b32 v21, s69, v3\n\t" \
    "v_xor_b32 v22, s69, v4\n\t" \
    "v_xor_b32 v23, s69, v5\n\t" \
    "global_load_dwordx4 v[32:35], v6, s[40:41] offset:-4096 sc1\n\t" \
    "global_load_dwordx4 v[36:39], v6, s[40:41] offset:-3072 sc1\n\t" \
    "global_load_dwordx4 a[0:3], v18, s[38:39] offset:-4096 sc1\n\t" \
    "global_load_dwordx4 a[4:7], v19, s[38:39] offset:-4096 sc1\n\t" \
    "global_load_dwordx4 a[8:11], v20, s[38:39] offset:-4096 sc1\n\t" \
    "global_load_dwordx4 a[12:15], v21, s[38:39] offset:-4096 sc1\n\t" \
    "global_load_dwordx4 a[16:19], v22, s[38:39] offset:-4096 sc1\n\t" \
    "global_load_dwordx4 a[20:23], v23, s[38:39] offset:-4096 sc1\n\t" \
    "global_load_dwordx4 v[40:43], v6, s[40:41] offset:-2048 sc1\n\t" \
    "global_load_dwordx4 v[44:47], v6, s[40:41] offset:-1024 sc1\n\t" \
    "global_load_dwordx4 a[24:27], v18, s[38:39] offset:-3072 sc1\n\t" \
    "global_load_dwordx4 a[28:31], v19, s[38:39] offset:-3072 sc1\n\t" \
    "global_load_dwordx4 a[32:35], v20, s[38:39] offset:-3072 sc1\n\t" \
    "global_load_dwordx4 a[36:39], v21, s[38:39] offset:-3072 sc1\n\t" \
    "global_load_dwordx4 a[40:43], v22, s[38:39] offset:-3072 sc1\n\t" \
    "global_load_dwordx4 a[44:47], v23, s[38:39] offset:-3072 sc1\n\t" \
    "global_load_dwordx4 v[48:51], v6, s[40:41] offset:0 sc1\n\t" \
    "global_load_dwordx4 v[52:55], v6, s[40:41] offset:1024 sc1\n\t" \
    "global_load_dwordx4 a[48:51], v18, s[38:39] offset:-2048 sc1\n\t" \
    "global_load_dwordx4 a[52:55], v19, s[38:39] offset:-2048 sc1\n\t" \
    "global_load_dwordx4 a[56:59], v20, s[38:39] offset:-2048 sc1\n\t" \
    "global_load_dwordx4 a[60:63], v21, s[38:39] offset:-2048 sc1\n\t" \
    "global_load_dwordx4 a[64:67], v22, s[38:39] offset:-2048 sc1\n\t" \
    "global_load_dwordx4 a[68:71], v23, s[38:39] offset:-2048 sc1\n\t" \
    "global_load_dwordx4 v[56:59], v6, s[40:41] offset:2048 sc1\n\t" \
    "global_load_dwordx4 v[60:63], v6, s[40:41] offset:3072 sc1\n\t" \
    "global_load_dwordx4 a[72:75], v18, s[38:39] offset:-1024 sc1\n\t" \
    "global_load_dwordx4 a[76:79], v19, s[38:39] offset:-1024 sc1\n\t" \
    "global_load_dwordx4 a[80:83], v20, s[38:39] offset:-1024 sc1\n\t" \
    "global_load_dwordx4 a[84:87], v21, s[38:39] offset:-1024 sc1\n\t" \
    "global_load_dwordx4 a[88:91], v22, s[38:39] offset:-1024 sc1\n\t" \
    "global_load_dwordx4 a[92:95], v23, s[38:39] offset:-1024 sc1\n\t" \
    "oo_loop3_%=:\n\t" \
    "s_mul_i32 s69, s96, 16384\n\t" \
    "s_add_u32 s69, s69, 150994944\n\t" \
    "s_add_u32 s46, s36, s69\n\t" \
    "s_addc_u32 s47, s37, 0\n\t" \
    "s_mul_i32 s69, s96, 9216\n\t" \
    "s_add_u32 s69, s69, 0\n\t" \
    "s_add_u32 s48, s36, s69\n\t" \
    "s_addc_u32 s49, s37, 0\n\t" \
    "s_waitcnt vmcnt(29)\n\t" \
    "v_mfma_f32_16x16x32_bf16 v[72:75], a[0:3], v[32:35], 0\n\t" \
    "global_load_dwordx4 v[64:67], v7, s[40:41] offset:-4096 sc1\n\t" \
    "v_mfma_f32_16x16x32_bf16 v[76:79], a[0:3], v[36:39], 0\n\t" \
    "global_load_dwordx4 v[68:71], v7, s[40:41] offset:-3072 sc1\n\t" \
    "s_waitcnt vmcnt(30)\n\t" \
    "v_mfma_f32_16x16x32_bf16 v[80:83], a[4:7], v[32:35], 0\n\t" \
    "global_load_dwordx4 a[96:99], v18, s[38:39] offset:0 sc1\n\t" \
    "v_mfma_f32_16x16x32_bf16 v[84:87], a[4:7], v[36:39], 0\n\t" \
    "global_load_dwordx4 a[100:103], v19, s[38:39] offset:0 sc1\n\t" \
    "s_waitcnt vmcnt(31)\n\t" \
    "v_mfma_f32_16x16x32_bf16 v[88:91], a[8:11], v[32:35], 0\n\t" \
    "global_load_dwordx4 a[104:107], v20, s[38:39] offset:0 sc1\n\t" \
    "v_mfma_f32_16x16x32_bf16 v[92:95], a[8:11], v[36:39], 0\n\t" \
    "global_load_dwordx4 a[108:111], v21, s[38:39] offset:0 sc1\n\t" \
    "s_waitcnt vmcnt(32)\n\t" \
    "v_mfma_f32_16x16x32_bf16 v[96:99], a[12:15], v[32:35], 0\n\t" \
    "global_load_dwordx4 a[112:115], v22, s[38:39] offset:0 sc1\n\t" \
    "v_mfma_f32_16x16x32_bf16 v[100:103], a[12:15], v[36:39], 0\n\t" \
    "global_load_dwordx4 a[116:119], v23, s[38:39] offset:0 sc1\n\t" \
    "s_waitcnt vmcnt(33)\n\t" \
    "v_mfma_f32_16x16x32_bf16 v[104:107], a[16:19], v[32:35], 0\n\t" \
    "v_mfma_f32_16x16x32_bf16 v[108:111], a[16:19], v[36:39], 0\n\t" \
    "s_waitcnt vmcnt(32)\n\t" \
    "v_mfma_f32_16x16x32_bf16 v[112:115], a[20:23], v[32:35], 0\n\t" \
    "v_mfma_f32_16x16x32_bf16 v[116:119], a[20:23], v[36:39], 0\n\t" \
    "s_add_u32 s96, s96, 4\n\t" \
    "s_cmp_gt_u32 s96, 34\n\t" \
    "s_cselect_b64 vcc, -1, 0\n\t" \
    "v_cndmask_b32 v25, v12, v13, vcc\n\t" \
    "global_load_dword v24, v25, s[36:37] sc1\n\t" \
    "s_waitcnt vmcnt(30)\n\t" \
    "v_mfma_f32_16x16x32_bf16 v[72:75], a[24:27], v[40:43], v[72:75]\n\t" \
    "global_load_dwordx4 v[32:35], v7, s[40:41] offset:-2048 sc1\n\t" \
    "v_mfma_f32_16x16x32_bf16 v[76:79], a[24:27], v[44:47], v[76:79]\n\t" \
    "global_load_dwordx4 v[36:39], v7, s[40:41] offset:-1024 sc1\n\t" \
    "s_waitcnt vmcnt(31)\n\t" \
    "v_mfma_f32_16x16x32_bf16 v[80:83], a[28:31], v[40:43], v[80:83]\n\t" \
    "global_load_dwordx4 a[0:3], v18, s[38:39] offset:1024 sc1\n\t" \
    "v_mfma_f32_16x16x32_bf16 v[84:87], a[28:31], v[44:47], v[84:87]\n\t" \
    "global_load_dwordx4 a[4:7], v19, s[38:39] offset:1024 sc1\n\t" \
    "s_waitcnt vmcnt(32)\n\t" \
    "v_mfma_f32_16x16x32_bf16 v[88:91], a[32:35], v[40:43], v[88:91]\n\t" \
    "global_load_dwordx4 a[8:11], v20, s[38:39] offset:1024 sc1\n\t" \
    "v_mfma_f32_16x16x32_bf16 v[92:95], a[32:35], v[44:47], v[92:95]\n\t" \
    "global_load_dwordx4 a[12:15], v21, s[38:39] offset:1024 sc1\n\t" \
    "s_waitcnt vmcnt(33)\n\t" \
    "v_mfma_f32_16x16x32_bf16 v[96:99], a[36:39], v[40:43], v[96:99]\n\t" \
    "global_load_dwordx4 a[16:19], v22, s[38:39] offset:1024 sc1\n\t" \
    "v_mfma_f32_16x16x32_bf16 v[100:103], a[36:39], v[44:47], v[100:103]\n\t" \
    "global_load_dwordx4 a[20:23], v23, s[38:39] offset:1024 sc1\n\t" \
    "s_waitcnt vmcnt(34)\n\t" \
    "v_mfma_f32_16x16x32_bf16 v[104:107], a[40:43], v[40:43], v[104:107]\n\t" \
    "v_mfma_f32_16x16x32_bf16 v[108:111], a[40:43], v[44:47], v[108:111]\n\t" \
    "s_waitcnt vmcnt(33)\n\t" \
    "v_mfma_f32_16x16x32_bf16 v[112:115], a[44:47], v[40:43], v[112:115]\n\t" \
    "v_mfma_f32_16x16x32_bf16 v[116:119], a[44:47], v[44:47], v[116:119]\n\t" \
    "s_waitcnt vmcnt(30)\n\t" \
    "v_mfma_f32_16x16x32_bf16 v[72:75], a[48:51], v[48:51], v[72:75]\n\t" \
    "global_load_dwordx4 v[40:43], v7, s[40:41] offset:0 sc1\n\t" \
    "v_mfma_f32_16x16x32_bf16 v[76:79], a[48:51], v[52:55], v[76:79]\n\t" \
    "global_load_dwordx4 v[44:47], v7, s[40:41] offset:1024 sc1\n\t" \
    "s_waitcnt vmcnt(31)\n\t" \
    "v_mfma_f32_16x16x32_bf16 v[80:83], a[52:55], v[48:51], v[80:83]\n\t" \
    "global_load_dwordx4 a[24:27], v18, s[38:39] offset:2048 sc1\n\t" \
    "v_mfma_f32_16x16x32_bf16 v[84:87], a[52:55], v[52:55], v[84:87]\n\t" \
    "global_load_dwordx4 a[28:31], v19, s[38:39] offset:2048 sc1\n\t" \
    "s_waitcnt vmcnt(32)\n\t" \
    "v_mfma_f32_16x16x32_bf16 v[88:91], a[56:59], v[48:51], v[88:91]\n\t" \
    "global_load_dwordx4 a[32:35], v20, s[38:39] offset:2048 sc1\n\t" \
    "v_mfma_f32_16x16x32_bf16 v[92:95], a[56:59], v[52:55], v[92:95]\n\t" \
    "global_load_dwordx4 a[36:39], v21, s[38:39] offset:2048 sc1\n\t" \
    "s_waitcnt vmcnt(33)\n\t" \
    "v_mfma_f32_16x16x32_bf16 v[96:99], a[60:63], v[48:51], v[96:99]\n\t" \
    "global_load_dwordx4 a[40:43], v22, s[38:39] offset:2048 sc1\n\t" \
    "v_mfma_f32_16x16x32_bf16 v[100:103], a[60:63], v[52:55], v[100:103]\n\t" \
    "global_load_dwordx4 a[44:47], v23, s[38:39] offset:2048 sc1\n\t" \
    "s_waitcnt vmcnt(34)\n\t" \
    "v_mfma_f32_16x16x32_bf16 v[104:107], a[64:67], v[48:51], v[104:107]\n\t" \
    "v_mfma_f32_16x16x32_bf16 v[108:111], a[64:67], v[52:55], v[108:111]\n\t" \
    "s_waitcnt vmcnt(33)\n\t" \
    "v_mfma_f32_16x16x32_bf16 v[112:115], a[68:71], v[48:51], v[112:115]\n\t" \
    "v_mfma_f32_16x16x32_bf16 v[116:119], a[68:71], v[52:55], v[116:119]\n\t" \
    "s_waitcnt vmcnt(30)\n\t" \
    "v_mfma_f32_16x16x32_bf16 v[72:75], a[72:75], v[56:59], v[72:75]\n\t" \
    "global_load_dwordx4 v[48:51], v7, s[40:41] offset:2048 sc1\n\t" \
    "v_mfma_f32_16x16x32_bf16 v[76:79], a[72:75], v[60:63], v[76:79]\n\t" \
    "global_load_dwordx4 v[52:55], v7, s[40:41] offset:3072 sc1\n\t" \
    "s_waitcnt vmcnt(31)\n\t" \
    "v_mfma_f32_16x16x32_bf16 v[80:83], a[76:79], v[56:59], v[80:83]\n\t" \
    "global_load_dwordx4 a[48:51], v18, s[38:39] offset:3072 sc1\n\t" \
    "v_mfma_f32_16x16x32_bf16 v[84:87], a[76:79], v[60:63], v[84:87]\n\t" \
    "global_load_dwordx4 a[52:55], v19, s[38:39] offset:3072 sc1\n\t" \
    "s_waitcnt vmcnt(32)\n\t" \
    "v_mfma_f32_16x16x32_bf16 v[88:91], a[80:83], v[56:59], v[88:91]\n\t" \
    "global_load_dwordx4 a[56:59], v20, s[38:39] offset:3072 sc1\n\t" \
    "v_mfma_f32_16x16x32_bf16 v[92:95], a[80:83], v[60:63], v[92:95]\n\t" \
    "global_load_dwordx4 a[60:63], v21, s[38:39] offset:3072 sc1\n\t" \
    "s_waitcnt vmcnt(33)\n\t" \
    "v_mfma_f32_16x16x32_bf16 v[96:99], a[84:87], v[56:59], v[96:99]\n\t" \
    "global_load_dwordx4 a[64:67], v22, s[38:39] offset:3072 sc1\n\t" \
    "v_mfma_f32_16x16x32_bf16 v[100:103], a[84:87], v[60:63], v[100:103]\n\t" \
    "global_load_dwordx4 a[68:71], v23, s[38:39] offset:3072 sc1\n\t" \
    "s_waitcnt vmcnt(34)\n\t" \
    "v_mfma_f32_16x16x32_bf16 v[104:107], a[88:91], v[56:59], v[104:107]\n\t" \
    "v_mfma_f32_16x16x32_bf16 v[108:111], a[88:91], v[60:63], v[108:111]\n\t" \
    "s_waitcnt vmcnt(33)\n\t" \
    "v_mfma_f32_16x16x32_bf16 v[112:115], a[92:95], v[56:59], v[112:115]\n\t" \
    "v_mfma_f32_16x16x32_bf16 v[116:119], a[92:95], v[60:63], v[116:119]\n\t" \
    "s_waitcnt vmcnt(30)\n\t" \
    "v_mfma_f32_16x16x32_bf16 v[72:75], a[96:99], v[64:67], v[72:75]\n\t" \
    "global_load_dwordx4 v[56:59], v8, s[46:47] offset:0 sc1\n\t" \
    "v_mfma_f32_16x16x32_bf16 v[76:79], a[96:99], v[68:71], v[76:79]\n\t" \
    "global_load_dwordx4 v[60:63], v9, s[46:47] offset:0 sc1\n\t" \
    "s_waitcnt vmcnt(31)\n\t" \
    "v_mfma_f32_16x16x32_bf16 v[80:83], a[100:103], v[64:67], v[80:83]\n\t" \
    "global_load_dwordx4 a[72:75], v10, s[48:49] offset:0 sc1\n\t" \
    "v_mfma_f32_16x16x32_bf16 v[84:87], a[100:103], v[68:71], v[84:87]\n\t" \
    "global_load_dwordx4 a[76:79], v10, s[48:49] offset:256 sc1\n\t" \
    "s_waitcnt vmcnt(32)\n\t" \
    "v_mfma_f32_16x16x32_bf16 v[88:91], a[104:107], v[64:67], v[88:91]\n\t" \
    "global_load_dwordx4 a[80:83], v10, s[48:49] offset:512 sc1\n\t" \
    "v_mfma_f32_16x16x32_bf16 v[92:95], a[104:107], v[68:71], v[92:95]\n\t" \
    "global_load_dwordx4 a[84:87], v10, s[48:49] offset:768 sc1\n\t" \
    "s_waitcnt vmcnt(33)\n\t" \
    "v_mfma_f32_16x16x32_bf16 v[96:99], a[108:111], v[64:67], v[96:99]\n\t" \
    "global_load_dwordx4 a[88:91], v10, s[48:49] offset:1024 sc1\n\t" \
    "v_mfma_f32_16x16x32_bf16 v[100:103], a[108:111], v[68:71], v[100:103]\n\t" \
    "global_load_dwordx4 a[92:95], v10, s[48:49] offset:1280 sc1\n\t" \
    "s_waitcnt vmcnt(34)\n\t" \
    "v_mfma_f32_16x16x32_bf16 v[104:107], a[112:115], v[64:67], v[104:107]\n\t" \
    "v_mfma_f32_16x16x32_bf16 v[108:111], a[112:115], v[68:71], v[108:111]\n\t" \
    "s_waitcnt vmcnt(33)\n\t" \
    "v_mfma_f32_16x16x32_bf16 v[112:115], a[116:119], v[64:67], v[112:115]\n\t" \
    "v_mfma_f32_16x16x32_bf16 v[116:119], a[116:119], v[68:71], v[116:119]\n\t" \
    "s_waitcnt vmcnt(29)\n\t" \
    "v_mfma_f32_16x16x32_bf16 v[72:75], a[0:3], v[32:35], v[72:75]\n\t" \
    "global_load_dwordx4 v[64:67], v8, s[46:47] offset:1024 sc1\n\t" \
    "v_mfma_f32_16x16x32_bf16 v[76:79], a[0:3], v[36:39], v[76:79]\n\t" \
    "global_load_dwordx4 v[68:71], v9, s[46:47] offset:1024 sc1\n\t" \
    "s_waitcnt vmcnt(30)\n\t" \
    "v_mfma_f32_16x16x32_bf16 v[80:83], a[4:7], v[32:35], v[80:83]\n\t" \
    "global_load_dwordx4 a[96:99], v11, s[48:49] offset:0 sc1\n\t" \
    "v_mfma_f32_16x16x32_bf16 v[84:87], a[4:7], v[36:39], v[84:87]\n\t" \
    "global_load_dwordx4 a[100:103], v11, s[48:49] offset:256 sc1\n\t" \
    "s_waitcnt vmcnt(31)\n\t" \
    "v_mfma_f32_16x16x32_bf16 v[88:91], a[8:11], v[32:35], v[88:91]\n\t" \
    "global_load_dwordx4 a[104:107], v11, s[48:49] offset:512 sc1\n\t" \
    "v_mfma_f32_16x16x32_bf16 v[92:95], a[8:11], v[36:39], v[92:95]\n\t" \
    "global_load_dwordx4 a[108:111], v11, s[48:49] offset:768 sc1\n\t" \
    "s_waitcnt vmcnt(32)\n\t" \
    "v_mfma_f32_16x16x32_bf16 v[96:99], a[12:15], v[32:35], v[96:99]\n\t" \
    "global_load_dwordx4 a[112:115], v11, s[48:49] offset:1024 sc1\n\t" \
    "v_mfma_f32_16x16x32_bf16 v[100:103], a[12:15], v[36:39], v[100:103]\n\t" \
    "global_load_dwordx4 a[116:119], v11, s[48:49] offset:1280 sc1\n\t" \
    "s_waitcnt vmcnt(33)\n\t" \
    "v_mfma_f32_16x16x32_bf16 v[104:107], a[16:19], v[32:35], v[104:107]\n\t" \
    "v_mfma_f32_16x16x32_bf16 v[108:111], a[16:19], v[36:39], v[108:111]\n\t" \
    "s_waitcnt vmcnt(32)\n\t" \
    "v_mfma_f32_16x16x32_bf16 v[112:115], a[20:23], v[32:35], v[112:115]\n\t" \
    "v_mfma_f32_16x16x32_bf16 v[116:119], a[20:23], v[36:39], v[116:119]\n\t" \
    "v_cmp_gt_i32 vcc, s96, v24\n\t" \
    "s_cmp_ge_u32 s96, 58\n\t" \
    "s_cbranch_scc1 oo_stop4_%=\n\t" \
    "s_cbranch_vccnz oo_stop4_%=\n\t" \
    "s_mul_i32 s69, s96, 65536\n\t" \
    "s_add_u32 s69, s69, 146800640\n\t" \
    "s_add_u32 s40, s36, s69\n\t" \
    "s_addc_u32 s41, s37, 0\n\t" \
    "s_lshl_b32 s69, s96, 15\n\t" \
    "v_xor_b32 v18, s69, v0\n\t" \
    "v_xor_b32 v19, s69, v1\n\t" \
    "v_xor_b32 v20, s69, v2\n\t" \
    "v_xor_b32 v21, s69, v3\n\t" \
    "v_xor_b32 v22, s69, v4\n\t" \
    "v_xor_b32 v23, s69, v5\n\t" \
    "s_waitcnt vmcnt(29)\n\t" \
    "v_mfma_f32_16x16x32_bf16 v[72:75], a[24:27], v[40:43], v[72:75]\n\t" \
    "global_load_dwordx4 v[32:35], v6, s[40:41] offset:-4096 sc1\n\t" \
    "v_mfma_f32_16x16x32_bf16 v[76:79], a[24:27], v[44:47], v[76:79]\n\t" \
    "global_load_dwordx4 v[36:39], v6, s[40:41] offset:-3072 sc1\n\t" \
    "s_waitcnt vmcnt(30)\n\t" \
    "v_mfma_f32_16x16x32_bf16 v[80:83], a[28:31], v[40:43], v[80:83]\n\t" \
    "global_load_dwordx4 a[0:3], v18, s[38:39] offset:-4096 sc1\n\t" \
    "v_mfma_f32_16x16x32_bf16 v[84:87], a[28:31], v[44:47], v[84:87]\n\t" \
    "global_load_dwordx4 a[4:7], v19, s[38:39] offset:-4096 sc1\n\t" \
    "s_waitcnt vmcnt(31)\n\t" \
    "v_mfma_f32_16x16x32_bf16 v[88:91], a[32:35], v[40:43], v[88:91]\n\t" \
    "global_load_dwordx4 a[8:11], v20, s[38:39] offset:-4096 sc1\n\t" \
    "v_mfma_f32_16x16x32_bf16 v[92:95], a[32:35], v[44:47], v[92:95]\n\t" \
    "global_load_dwordx4 a[12:15], v21, s[38:39] offset:-4096 sc1\n\t" \
    "s_waitcnt vmcnt(32)\n\t" \
    "v_mfma_f32_16x16x32_bf16 v[96:99], a[36:39], v[40:43], v[96:99]\n\t" \
    "global_load_dwordx4 a[16:19], v22, s[38:39] offset:-4096 sc1\n\t" \
    "v_mfma_f32_16x16x32_bf16 v[100:103], a[36:39], v[44:47], v[100:103]\n\t" \
    "global_load_dwordx4 a[20:23], v23, s[38:39] offset:-4096 sc1\n\t" \
    "s_waitcnt vmcnt(33)\n\t" \
    "v_mfma_f32_16x16x32_bf16 v[104:107], a[40:43], v[40:43], v[104:107]\n\t" \
    "v_mfma_f32_16x16x32_bf16 v[108:111], a[40:43], v[44:47], v[108:111]\n\t" \
    "s_waitcnt vmcnt(32)\n\t" \
    "v_mfma_f32_16x16x32_bf16 v[112:115], a[44:47], v[40:43], v[112:115]\n\t" \
    "v_mfma_f32_16x16x32_bf16 v[116:119], a[44:47], v[44:47], v[116:119]\n\t" \
    "s_waitcnt vmcnt(29)\n\t" \
    "v_mfma_f32_16x16x32_bf16 v[72:75], a[48:51], v[48:51], v[72:75]\n\t" \
    "global_load_dwordx4 v[40:43], v6, s[40:41] offset:-2048 sc1\n\t" \
    "v_mfma_f32_16x16x32_bf16 v[76:79], a[48:51], v[52:55], v[76:79]\n\t" \
    "global_load_dwordx4 v[44:47], v6, s[40:41] offset:-1024 sc1\n\t" \
    "s_waitcnt vmcnt(30)\n\t" \
    "v_mfma_f32_16x16x32_bf16 v[80:83], a[52:55], v[48:51], v[80:83]\n\t" \
    "global_load_dwordx4 a[24:27], v18, s[38:39] offset:-3072 sc1\n\t" \
    "v_mfma_f32_16x16x32_bf16 v[84:87], a[52:55], v[52:55], v[84:87]\n\t" \
    "global_load_dwordx4 a[28:31], v19, s[38:39] offset:-3072 sc1\n\t" \
    "s_waitcnt vmcnt(31)\n\t" \
    "v_mfma_f32_16x16x32_bf16 v[88:91], a[56:59], v[48:51], v[88:91]\n\t" \
    "global_load_dwordx4 a[32:35], v20, s[38:39] offset:-3072 sc1\n\t" \
    "v_mfma_f32_16x16x32_bf16 v[92:95], a[56:59], v[52:55], v[92:95]\n\t" \
    "global_load_dwordx4 a[36:39], v21, s[38:39] offset:-3072 sc1\n\t" \
    "s_waitcnt vmcnt(32)\n\t" \
    "v_mfma_f32_16x16x32_bf16 v[96:99], a[60:63], v[48:51], v[96:99]\n\t" \
    "global_load_dwordx4 a[40:43], v22, s[38:39] offset:-3072 sc1\n\t" \
    "v_mfma_f32_16x16x32_bf16 v[100:103], a[60:63], v[52:55], v[100:103]\n\t" \
    "global_load_dwordx4 a[44:47], v23, s[38:39] offset:-3072 sc1\n\t" \
    "s_waitcnt vmcnt(33)\n\t" \
    "v_mfma_f32_16x16x32_bf16 v[104:107], a[64:67], v[48:51], v[104:107]\n\t" \
    "v_mfma_f32_16x16x32_bf16 v[108:111], a[64:67], v[52:55], v[108:111]\n\t" \
    "s_waitcnt vmcnt(32)\n\t" \
    "v_mfma_f32_16x16x32_bf16 v[112:115], a[68:71], v[48:51], v[112:115]\n\t" \
    "v_mfma_f32_16x16x32_bf16 v[116:119], a[68:71], v[52:55], v[116:119]\n\t" \
    "s_waitcnt vmcnt(29)\n\t" \
    "v_mfma_f32_16x16x32_bf16 v[72:75], a[72:75], v[56:59], v[72:75]\n\t" \
    "global_load_dwordx4 v[48:51], v6, s[40:41] offset:0 sc1\n\t" \
    "v_mfma_f32_16x16x32_bf16 v[76:79], a[72:75], v[60:63], v[76:79]\n\t" \
    "global_load_dwordx4 v[52:55], v6, s[40:41] offset:1024 sc1\n\t" \
    "s_waitcnt vmcnt(30)\n\t" \
    "v_mfma_f32_16x16x32_bf16 v[80:83], a[76:79], v[56:59], v[80:83]\n\t" \
    "global_load_dwordx4 a[48:51], v18, s[38:39] offset:-2048 sc1\n\t" \
    "v_mfma_f32_16x16x32_bf16 v[84:87], a[76:79], v[60:63], v[84:87]\n\t" \
    "global_load_dwordx4 a[52:55], v19, s[38:39] offset:-2048 sc1\n\t" \
    "s_waitcnt vmcnt(31)\n\t" \
    "v_mfma_f32_16x16x32_bf16 v[88:91], a[80:83], v[56:59], v[88:91]\n\t" \
    "global_load_dwordx4 a[56:59], v20, s[38:39] offset:-2048 sc1\n\t" \
    "v_mfma_f32_16x16x32_bf16 v[92:95], a[80:83], v[60:63], v[92:95]\n\t" \
    "global_load_dwordx4 a[60:63], v21, s[38:39] offset:-2048 sc1\n\t" \
    "s_waitcnt vmcnt(32)\n\t" \
    "v_mfma_f32_16x16x32_bf16 v[96:99], a[84:87], v[56:59], v[96:99]\n\t" \
    "global_load_dwordx4 a[64:67], v22, s[38:39] offset:-2048 sc1\n\t" \
    "v_mfma_f32_16x16x32_bf16 v[100:103], a[84:87], v[60:63], v[100:103]\n\t" \
    "global_load_dwordx4 a[68:71], v23, s[38:39] offset:-2048 sc1\n\t" \
    "s_waitcnt vmcnt(33)\n\t" \
    "v_mfma_f32_16x16x32_bf16 v[104:107], a[88:91], v[56:59], v[104:107]\n\t" \
    "v_mfma_f32_16x16x32_bf16 v[108:111], a[88:91], v[60:63], v[108:111]\n\t" \
    "s_waitcnt vmcnt(32)\n\t" \
    "v_mfma_f32_16x16x32_bf16 v[112:115], a[92:95], v[56:59], v[112:115]\n\t" \
    "v_mfma_f32_16x16x32_bf16 v[116:119], a[92:95], v[60:63], v[116:119]\n\t" \
    "s_waitcnt vmcnt(29)\n\t" \
    "v_mfma_f32_16x16x32_bf16 v[72:75], a[96:99], v[64:67], v[72:75]\n\t" \
    "global_load_dwordx4 v[56:59], v6, s[40:41] offset:2048 sc1\n\t" \
    "v_mfma_f32_16x16x32_bf16 v[76:79], a[96:99], v[68:71], v[76:79]\n\t" \
    "global_load_dwordx4 v[60:63], v6, s[40:41] offset:3072 sc1\n\t" \
    "s_waitcnt vmcnt(30)\n\t" \
    "v_mfma_f32_16x16x32_bf16 v[80:83], a[100:103], v[64:67], v[80:83]\n\t" \
    "global_load_dwordx4 a[72:75], v18, s[38:39] offset:-1024 sc1\n\t" \
    "v_mfma_f32_16x16x32_bf16 v[84:87], a[100:103], v[68:71], v[84:87]\n\t" \
    "global_load_dwordx4 a[76:79], v19, s[38:39] offset:-1024 sc1\n\t" \
    "s_waitcnt vmcnt(31)\n\t" \
    "v_mfma_f32_16x16x32_bf16 v[88:91], a[104:107], v[64:67], v[88:91]\n\t" \
    "global_load_dwordx4 a[80:83], v20, s[38:39] offset:-1024 sc1\n\t" \
    "v_mfma_f32_16x16x32_bf16 v[92:95], a[104:107], v[68:71], v[92:95]\n\t" \
    "global_load_dwordx4 a[84:87], v21, s[38:39] offset:-1024 sc1\n\t" \
    "s_waitcnt vmcnt(32)\n\t" \
    "v_mfma_f32_16x16x32_bf16 v[96:99], a[108:111], v[64:67], v[96:99]\n\t" \
    "global_load_dwordx4 a[88:91], v22, s[38:39] offset:-1024 sc1\n\t" \
    "v_mfma_f32_16x16x32_bf16 v[100:103], a[108:111], v[68:71], v[100:103]\n\t" \
    "global_load_dwordx4 a[92:95], v23, s[38:39] offset:-1024 sc1\n\t" \
    "s_waitcnt vmcnt(33)\n\t" \
    "v_mfma_f32_16x16x32_bf16 v[104:107], a[112:115], v[64:67], v[104:107]\n\t" \
    "v_mfma_f32_16x16x32_bf16 v[108:111], a[112:115], v[68:71], v[108:111]\n\t" \
    "s_waitcnt vmcnt(32)\n\t" \
    "v_mfma_f32_16x16x32_bf16 v[112:115], a[116:119], v[64:67], v[112:115]\n\t" \
    "v_mfma_f32_16x16x32_bf16 v[116:119], a[116:119], v[68:71], v[116:119]\n\t" \
    "s_mov_b32 s97, 1\n\t" \
    "s_branch oo_epi5_%=\n\t" \
    "oo_stop4_%=:\n\t" \
    "s_waitcnt vmcnt(29)\n\t" \
    "v_mfma_f32_16x16x32_bf16 v[72:75], a[24:27], v[40:43], v[72:75]\n\t" \
    "v_mfma_f32_16x16x32_bf16 v[76:79], a[24:27], v[44:47], v[76:79]\n\t" \
    "s_waitcnt vmcnt(28)\n\t" \
    "v_mfma_f32_16x16x32_bf16 v[80:83], a[28:31], v[40:43], v[80:83]\n\t" \
    "v_mfma_f32_16x16x32_bf16 v[84:87], a[28:31], v[44:47], v[84:87]\n\t" \
    "s_waitcnt vmcnt(27)\n\t" \
    "v_mfma_f32_16x16x32_bf16 v[88:91], a[32:35], v[40:43], v[88:91]\n\t" \
    "v_mfma_f32_16x16x32_bf16 v[92:95], a[32:35], v[44:47], v[92:95]\n\t" \
    "s_waitcnt vmcnt(26)\n\t" \
    "v_mfma_f32_16x16x32_bf16 v[96:99], a[36:39], v[40:43], v[96:99]\n\t" \
    "v_mfma_f32_16x16x32_bf16 v[100:103], a[36:39], v[44:47], v[100:103]\n\t" \
    "s_waitcnt vmcnt(25)\n\t" \
    "v_mfma_f32_16x16x32_bf16 v[104:107], a[40:43], v[40:43], v[104:107]\n\t" \
    "v_mfma_f32_16x16x32_bf16 v[108:111], a[40:43], v[44:47], v[108:111]\n\t" \
    "s_waitcnt vmcnt(24)\n\t" \
    "v_mfma_f32_16x16x32_bf16 v[112:115], a[44:47], v[40:43], v[112:115]\n\t" \
    "v_mfma_f32_16x16x32_bf16 v[116:119], a[44:47], v[44:47], v[116:119]\n\t" \
    "s_waitcnt vmcnt(21)\n\t" \
    "v_mfma_f32_16x16x32_bf16 v[72:75], a[48:51], v[48:51], v[72:75]\n\t" \
    "v_mfma_f32_16x16x32_bf16 v[76:79], a[48:51], v[52:55], v[76:79]\n\t" \
    "s_waitcnt vmcnt(20)\n\t" \
    "v_mfma_f32_16x16x32_bf16 v[80:83], a[52:55], v[48:51], v[80:83]\n\t" \
    "v_mfma_f32_16x16x32_bf16 v[84:87], a[52:55], v[52:55], v[84:87]\n\t" \
    "s_waitcnt vmcnt(19)\n\t" \
    "v_mfma_f32_16x16x32_bf16 v[88:91], a[56:59], v[48:51], v[88:91]\n\t" \
    "v_mfma_f32_16x16x32_bf16 v[92:95], a[56:59], v[52:55], v[92:95]\n\t" \
    "s_waitcnt vmcnt(18)\n\t" \
    "v_mfma_f32_16x16x32_bf16 v[96:99], a[60:63], v[48:51], v[96:99]\n\t" \
    "v_mfma_f32_16x16x32_bf16 v[100:103], a[60:63], v[52:55], v[100:103]\n\t" \
    "s_waitcnt vmcnt(17)\n\t" \
    "v_mfma_f32_16x16x32_bf16 v[104:107], a[64:67], v[48:51], v[104:107]\n\t" \
    "v_mfma_f32_16x16x32_bf16 v[108:111], a[64:67], v[52:55], v[108:111]\n\t" \
    "s_waitcnt vmcnt(16)\n\t" \
    "v_mfma_f32_16x16x32_bf16 v[112:115], a[68:71], v[48:51], v[112:115]\n\t" \
    "v_mfma_f32_16x16x32_bf16 v[116:119], a[68:71], v[52:55], v[116:119]\n\t" \
    "s_waitcnt vmcnt(13)\n\t" \
    "v_mfma_f32_16x16x32_bf16 v[72:75], a[72:75], v[56:59], v[72:75]\n\t" \
    "v_mfma_f32_16x16x32_bf16 v[76:79], a[72:75], v[60:63], v[76:79]\n\t" \
    "s_waitcnt vmcnt(12)\n\t" \
    "v_mfma_f32_16x16x32_bf16 v[80:83], a[76:79], v[56:59], v[80:83]\n\t" \
    "v_mfma_f32_16x16x32_bf16 v[84:87], a[76:79], v[60:63], v[84:87]\n\t" \
    "s_waitcnt vmcnt(11)\n\t" \
    "v_mfma_f32_16x16x32_bf16 v[88:91], a[80:83], v[56:59], v[88:91]\n\t" \
    "v_mfma_f32_16x16x32_bf16 v[92:95], a[80:83], v[60:63], v[92:95]\n\t" \
    "s_waitcnt vmcnt(10)\n\t" \
    "v_mfma_f32_16x16x32_bf16 v[96:99], a[84:87], v[56:59], v[96:99]\n\t" \
    "v_mfma_f32_16x16x32_bf16 v[100:103], a[84:87], v[60:63], v[100:103]\n\t" \
    "s_waitcnt vmcnt(9)\n\t" \
    "v_mfma_f32_16x16x32_bf16 v[104:107], a[88:91], v[56:59], v[104:107]\n\t" \
    "v_mfma_f32_16x16x32_bf16 v[108:111], a[88:91], v[60:63], v[108:111]\n\t" \
    "s_waitcnt vmcnt(8)\n\t" \
    "v_mfma_f32_16x16x32_bf16 v[112:115], a[92:95], v[56:59], v[112:115]\n\t" \
    "v_mfma_f32_16x16x32_bf16 v[116:119], a[92:95], v[60:63], v[116:119]\n\t" \
    "s_waitcnt vmcnt(5)\n\t" \
    "v_mfma_f32_16x16x32_bf16 v[72:75], a[96:99], v[64:67], v[72:75]\n\t" \
    "v_mfma_f32_16x16x32_bf16 v[76:79], a[96:99], v[68:71], v[76:79]\n\t" \
    "s_waitcnt vmcnt(4)\n\t" \
    "v_mfma_f32_16x16x32_bf16 v[80:83], a[100:103], v[64:67], v[80:83]\n\t" \
    "v_mfma_f32_16x16x32_bf16 v[84:87], a[100:103], v[68:71], v[84:87]\n\t" \
    "s_waitcnt vmcnt(3)\n\t" \
    "v_mfma_f32_16x16x32_bf16 v[88:91], a[104:107], v[64:67], v[88:91]\n\t" \
    "v_mfma_f32_16x16x32_bf16 v[92:95], a[104:107], v[68:71], v[92:95]\n\t" \
    "s_waitcnt vmcnt(2)\n\t" \
    "v_mfma_f32_16x16x32_bf16 v[96:99], a[108:111], v[64:67], v[96:99]\n\t" \
    "v_mfma_f32_16x16x32_bf16 v[100:103], a[108:111], v[68:71], v[100:103]\n\t" \
    "s_waitcnt vmcnt(1)\n\t" \
    "v_mfma_f32_16x16x32_bf16 v[104:107], a[112:115], v[64:67], v[104:107]\n\t" \
    "v_mfma_f32_16x16x32_bf16 v[108:111], a[112:115], v[68:71], v[108:111]\n\t" \
    "s_waitcnt vmcnt(0)\n\t" \
    "v_mfma_f32_16x16x32_bf16 v[112:115], a[116:119], v[64:67], v[112:115]\n\t" \
    "v_mfma_f32_16x16x32_bf16 v[116:119], a[116:119], v[68:71], v[116:119]\n\t" \
    "s_mov_b32 s97, 0\n\t" \
    "oo_epi5_%=:\n\t" \
    "s_mul_i32 s69, s65, 73728\n\t" \
    "s_add_u32 s44, s42, s69\n\t" \
    "s_addc_u32 s45, s43, 0\n\t" \
    "s_nop 7\n\t" \
    "ds_write_b128 v14, v[72:75] offset:0\n\t" \
    "ds_write_b128 v14, v[76:79] offset:1024\n\t" \
    "ds_write_b128 v14, v[80:83] offset:2048\n\t" \
    "ds_write_b128 v14, v[84:87] offset:3072\n\t" \
    "ds_write_b128 v14, v[88:91] offset:4096\n\t" \
    "ds_write_b128 v14, v[92:95] offset:5120\n\t" \
    "ds_write_b128 v14, v[96:99] offset:6144\n\t" \
    "ds_write_b128 v14, v[100:103] offset:7168\n\t" \
    "ds_write_b128 v14, v[104:107] offset:8192\n\t" \
    "ds_write_b128 v14, v[108:111] offset:9216\n\t" \
    "ds_write_b128 v14, v[112:115] offset:10240\n\t" \
    "ds_write_b128 v14, v[116:119] offset:11264\n\t" \
    "s_waitcnt lgkmcnt(0)\n\t" \
    "s_barrier\n\t" \
    "ds_read_b128 v[120:123], v15 offset:0\n\t" \
    "ds_read_b128 v[124:127], v15 offset:12288\n\t" \
    "ds_read_b128 v[128:131], v15 offset:24576\n\t" \
    "ds_read_b128 v[132:135], v15 offset:36864\n\t" \
    "ds_read_b128 v[136:139], v15 offset:4096\n\t" \
    "ds_read_b128 v[140:143], v15 offset:16384\n\t" \
    "ds_read_b128 v[144:147], v15 offset:28672\n\t" \
    "ds_read_b128 v[148:151], v15 offset:40960\n\t" \
    "ds_read_b128 v[152:155], v15 offset:8192\n\t" \
    "ds_read_b128 v[156:159], v15 offset:20480\n\t" \
    "ds_read_b128 v[160:163], v15 offset:32768\n\t" \
    "ds_read_b128 v[164:167], v15 offset:45056\n\t" \
    "v_xor_b32 v14, 0x10000, v14\n\t" \
    "v_xor_b32 v15, 0x10000, v15\n\t" \
    "s_waitcnt lgkmcnt(10)\n\t" \
    "v_pk_add_f32 v[120:121], v[120:121], v[124:125]\n\t" \
    "v_pk_add_f32 v[122:123], v[122:123], v[126:127]\n\t" \
    "s_waitcnt lgkmcnt(8)\n\t" \
    "v_pk_add_f32 v[128:129], v[128:129], v[132:133]\n\t" \
    "v_pk_add_f32 v[130:131], v[130:131], v[134:135]\n\t" \
    "v_pk_add_f32 v[120:121], v[120:121], v[128:129]\n\t" \
    "v_pk_add_f32 v[122:123], v[122:123], v[130:131]\n\t" \
    "v_cvt_pk_bf16_f32 v120, v120, v121\n\t" \
    "v_cvt_pk_bf16_f32 v121, v122, v123\n\t" \
    "global_store_dwordx2 v16, v[120:121], s[44:45] offset:0\n\t" \
    "s_waitcnt lgkmcnt(6)\n\t" \
    "v_pk_add_f32 v[136:137], v[136:137], v[140:141]\n\t" \
    "v_pk_add_f32 v[138:139], v[138:139], v[142:143]\n\t" \
    "s_waitcnt lgkmcnt(4)\n\t" \
    "v_pk_add_f32 v[144:145], v[144:145], v[148:149]\n\t" \
    "v_pk_add_f32 v[146:147], v[146:147], v[150:151]\n\t" \
    "v_pk_add_f32 v[136:137], v[136:137], v[144:145]\n\t" \
    "v_pk_add_f32 v[138:139], v[138:139], v[146:147]\n\t" \
    "v_cvt_pk_bf16_f32 v136, v136, v137\n\t" \
    "v_cvt_pk_bf16_f32 v137, v138, v139\n\t" \
    "global_store_dwordx2 v16, v[136:137], s[44:45] offset:64\n\t" \
    "s_waitcnt lgkmcnt(2)\n\t" \
    "v_pk_add_f32 v[152:153], v[152:153], v[156:157]\n\t" \
    "v_pk_add_f32 v[154:155], v[154:155], v[158:159]\n\t" \
    "s_waitcnt lgkmcnt(0)\n\t" \
    "v_pk_add_f32 v[160:161], v[160:161], v[164:165]\n\t" \
    "v_pk_add_f32 v[162:163], v[162:163], v[166:167]\n\t" \
    "v_pk_add_f32 v[152:153], v[152:153], v[160:161]\n\t" \
    "v_pk_add_f32 v[154:155], v[154:155], v[162:163]\n\t" \
    "v_cvt_pk_bf16_f32 v152, v152, v153\n\t" \
    "v_cvt_pk_bf16_f32 v153, v154, v155\n\t" \
    "global_store_dwordx2 v16, v[152:153], s[44:45] offset:128\n\t" \
    "s_mov_b32 s65, s96\n\t" \
    "s_cmp_lg_u32 s97, 0\n\t" \
    "s_cbranch_scc1 oo_loop3_%=\n\t" \
    "s_cmp_lt_u32 s65, 58\n\t" \
    "s_cbranch_scc1 oo_poll1_%=\n\t" \
    "s_waitcnt vmcnt(0) lgkmcnt(0)\n\t" \
    "s_barrier\n\t" \
    "oo_last_%=:\n\t" \
    ""
#define ASM_CV \
    "v_mov_b32 v0, %0\n\t" \
    "v_mov_b32 v2, %2\n\t" \
    "v_subrev_u32 v1, 768, %1\n\t" \
    "v_mov_b32 v4, 0\n\t" \
    "v_mov_b32 v5, 0\n\t" \
    "v_mov_b32 v6, 0\n\t" \
    "v_mov_b32 v7, 0\n\t" \
    "v_mov_b32 v8, 0\n\t" \
    "v_mov_b32 v9, 0\n\t" \
    "v_mov_b32 v10, 0\n\t" \
    "v_mov_b32 v11, 0\n\t" \
    "v_mov_b32 v12, 0\n\t" \
    "v_mov_b32 v13, 0\n\t" \
    "v_mov_b32 v14, 0\n\t" \
    "v_mov_b32 v15, 0\n\t" \
    "v_mov_b32 v16, 0\n\t" \
    "v_mov_b32 v17, 0\n\t" \
    "v_mov_b32 v18, 0\n\t" \
    "v_mov_b32 v19, 0\n\t" \
    "v_mov_b32 v20, 0\n\t" \
    "v_mov_b32 v21, 0\n\t" \
    "v_mov_b32 v22, 0\n\t" \
    "v_mov_b32 v23, 0\n\t" \
    "v_mov_b32 v24, 0\n\t" \
    "v_mov_b32 v25, 0\n\t" \
    "v_mov_b32 v26, 0\n\t" \
    "v_mov_b32 v27, 0\n\t" \
    "v_mov_b32 v28, 0\n\t" \
    "v_mov_b32 v29, 0\n\t" \
    "v_mov_b32 v30, 0\n\t" \
    "v_mov_b32 v31, 0\n\t" \
    "v_mov_b32 v32, 0\n\t" \
    "v_mov_b32 v33, 0\n\t" \
    "v_mov_b32 v34, 0\n\t" \
    "v_mov_b32 v35, 0\n\t" \
    "s_waitcnt lgkmcnt(0)\n\t" \
    "ds_read_b64 v[36:37], v0 offset:0\n\t" \
    "ds_read_b64 v[38:39], v0 offset:8\n\t" \
    "ds_read_b128 v[44:47], v1 offset:768\n\t" \
    "ds_read_b64 v[40:41], v0 offset:32\n\t" \
    "ds_read_b64 v[42:43], v0 offset:40\n\t" \
    "ds_read_b128 v[48:51], v1 offset:2304\n\t" \
    "ds_read_b128 v[52:55], v1 offset:3840\n\t" \
    "ds_read_b128 v[56:59], v1 offset:5376\n\t" \
    "s_mov_b32 s69, 4\n\t" \
    "cv_c1_%=:\n\t" \
    "s_waitcnt lgkmcnt(5)\n\t" \
    "v_mfma_f32_16x16x32_f16 v[4:7], v[36:39], v[44:47], v[4:7]\n\t" \
    "ds_read_b64 v[60:61], v0 offset:64\n\t" \
    "s_waitcnt lgkmcnt(4)\n\t" \
    "v_mfma_f32_16x16x32_f16 v[20:23], v[40:43], v[44:47], v[20:23]\n\t" \
    "ds_read_b64 v[62:63], v0 offset:72\n\t" \
    "s_waitcnt lgkmcnt(4)\n\t" \
    "v_mfma_f32_16x16x32_f16 v[8:11], v[36:39], v[48:51], v[8:11]\n\t" \
    "ds_read_b128 v[68:71], v1 offset:672\n\t" \
    "v_mfma_f32_16x16x32_f16 v[24:27], v[40:43], v[48:51], v[24:27]\n\t" \
    "ds_read_b64 v[64:65], v0 offset:96\n\t" \
    "s_waitcnt lgkmcnt(5)\n\t" \
    "v_mfma_f32_16x16x32_f16 v[12:15], v[36:39], v[52:55], v[12:15]\n\t" \
    "ds_read_b64 v[66:67], v0 offset:104\n\t" \
    "v_mfma_f32_16x16x32_f16 v[28:31], v[40:43], v[52:55], v[28:31]\n\t" \
    "ds_read_b128 v[72:75], v1 offset:2208\n\t" \
    "s_waitcnt lgkmcnt(6)\n\t" \
    "v_mfma_f32_16x16x32_f16 v[16:19], v[36:39], v[56:59], v[16:19]\n\t" \
    "ds_read_b128 v[76:79], v1 offset:3744\n\t" \
    "v_mfma_f32_16x16x32_f16 v[32:35], v[40:43], v[56:59], v[32:35]\n\t" \
    "ds_read_b128 v[80:83], v1 offset:5280\n\t" \
    "s_waitcnt lgkmcnt(5)\n\t" \
    "v_mfma_f32_16x16x32_f16 v[4:7], v[60:63], v[68:71], v[4:7]\n\t" \
    "ds_read_b64 v[84:85], v0 offset:128\n\t" \
    "s_waitcnt lgkmcnt(4)\n\t" \
    "v_mfma_f32_16x16x32_f16 v[20:23], v[64:67], v[68:71], v[20:23]\n\t" \
    "ds_read_b64 v[86:87], v0 offset:136\n\t" \
    "s_waitcnt lgkmcnt(4)\n\t" \
    "v_mfma_f32_16x16x32_f16 v[8:11], v[60:63], v[72:75], v[8:11]\n\t" \
    "ds_read_b128 v[92:95], v1 offset:576\n\t" \
    "v_mfma_f32_16x16x32_f16 v[24:27], v[64:67], v[72:75], v[24:27]\n\t" \
    "ds_read_b64 v[88:89], v0 offset:160\n\t" \
    "s_waitcnt lgkmcnt(5)\n\t" \
    "v_mfma_f32_16x16x32_f16 v[12:15], v[60:63], v[76:79], v[12:15]\n\t" \
    "ds_read_b64 v[90:91], v0 offset:168\n\t" \
    "v_mfma_f32_16x16x32_f16 v[28:31], v[64:67], v[76:79], v[28:31]\n\t" \
    "ds_read_b128 v[96:99], v1 offset:2112\n\t" \
    "s_waitcnt lgkmcnt(6)\n\t" \
    "v_mfma_f32_16x16x32_f16 v[16:19], v[60:63], v[80:83], v[16:19]\n\t" \
    "ds_read_b128 v[100:103], v1 offset:3648\n\t" \
    "v_mfma_f32_16x16x32_f16 v[32:35], v[64:67], v[80:83], v[32:35]\n\t" \
    "ds_read_b128 v[104:107], v1 offset:5184\n\t" \
    "s_waitcnt lgkmcnt(5)\n\t" \
    "v_mfma_f32_16x16x32_f16 v[4:7], v[84:87], v[92:95], v[4:7]\n\t" \
    "ds_read_b64 v[108:109], v0 offset:192\n\t" \
    "s_waitcnt lgkmcnt(4)\n\t" \
    "v_mfma_f32_16x16x32_f16 v[20:23], v[88:91], v[92:95], v[20:23]\n\t" \
    "ds_read_b64 v[110:111], v0 offset:200\n\t" \
    "s_waitcnt lgkmcnt(4)\n\t" \
    "v_mfma_f32_16x16x32_f16 v[8:11], v[84:87], v[96:99], v[8:11]\n\t" \
    "ds_read_b128 v[116:119], v1 offset:480\n\t" \
    "v_mfma_f32_16x16x32_f16 v[24:27], v[88:91], v[96:99], v[24:27]\n\t" \
    "ds_read_b64 v[112:113], v0 offset:224\n\t" \
    "s_waitcnt lgkmcnt(5)\n\t" \
    "v_mfma_f32_16x16x32_f16 v[12:15], v[84:87], v[100:103], v[12:15]\n\t" \
    "ds_read_b64 v[114:115], v0 offset:232\n\t" \
    "v_mfma_f32_16x16x32_f16 v[28:31], v[88:91], v[100:103], v[28:31]\n\t" \
    "ds_read_b128 v[120:123], v1 offset:2016\n\t" \
    "s_waitcnt lgkmcnt(6)\n\t" \
    "v_mfma_f32_16x16x32_f16 v[16:19], v[84:87], v[104:107], v[16:19]\n\t" \
    "ds_read_b128 v[124:127], v1 offset:3552\n\t" \
    "v_mfma_f32_16x16x32_f16 v[32:35], v[88:91], v[104:107], v[32:35]\n\t" \
    "ds_read_b128 v[128:131], v1 offset:5088\n\t" \
    "s_waitcnt lgkmcnt(5)\n\t" \
    "v_mfma_f32_16x16x32_f16 v[4:7], v[108:111], v[116:119], v[4:7]\n\t" \
    "ds_read_b64 v[36:37], v0 offset:256\n\t" \
    "s_waitcnt lgkmcnt(4)\n\t" \
    "v_mfma_f32_16x16x32_f16 v[20:23], v[112:115], v[116:119], v[20:23]\n\t" \
    "ds_read_b64 v[38:39], v0 offset:264\n\t" \
    "s_waitcnt lgkmcnt(4)\n\t" \
    "v_mfma_f32_16x16x32_f16 v[8:11], v[108:111], v[120:123], v[8:11]\n\t" \
    "ds_read_b128 v[44:47], v1 offset:384\n\t" \
    "v_mfma_f32_16x16x32_f16 v[24:27], v[112:115], v[120:123], v[24:27]\n\t" \
    "ds_read_b64 v[40:41], v0 offset:288\n\t" \
    "s_waitcnt lgkmcnt(5)\n\t" \
    "v_mfma_f32_16x16x32_f16 v[12:15], v[108:111], v[124:127], v[12:15]\n\t" \
    "ds_read_b64 v[42:43], v0 offset:296\n\t" \
    "v_mfma_f32_16x16x32_f16 v[28:31], v[112:115], v[124:127], v[28:31]\n\t" \
    "ds_read_b128 v[48:51], v1 offset:1920\n\t" \
    "s_waitcnt lgkmcnt(6)\n\t" \
    "v_mfma_f32_16x16x32_f16 v[16:19], v[108:111], v[128:131], v[16:19]\n\t" \
    "ds_read_b128 v[52:55], v1 offset:3456\n\t" \
    "v_mfma_f32_16x16x32_f16 v[32:35], v[112:115], v[128:131], v[32:35]\n\t" \
    "ds_read_b128 v[56:59], v1 offset:4992\n\t" \
    "v_add_u32 v0, 256, v0\n\t" \
    "v_subrev_u32 v1, 384, v1\n\t" \
    "s_sub_u32 s69, s69, 1\n\t" \
    "s_cmp_lg_u32 s69, 0\n\t" \
    "s_cbranch_scc1 cv_c1_%=\n\t" \
    "s_waitcnt lgkmcnt(0)\n\t" \
    "ds_read_b64 v[36:37], v0 offset:0\n\t" \
    "ds_read_b64 v[38:39], v0 offset:8\n\t" \
    "ds_read_b128 v[48:51], v1 offset:2304\n\t" \
    "ds_read_b64 v[40:41], v0 offset:32\n\t" \
    "ds_read_b64 v[42:43], v0 offset:40\n\t" \
    "ds_read_b128 v[52:55], v1 offset:3840\n\t" \
    "ds_read_b128 v[56:59], v1 offset:5376\n\t" \
    "ds_read_b64 v[60:61], v0 offset:64\n\t" \
    "ds_read_b64 v[62:63], v0 offset:72\n\t" \
    "ds_read_b128 v[72:75], v1 offset:2208\n\t" \
    "ds_read_b64 v[64:65], v0 offset:96\n\t" \
    "ds_read_b64 v[66:67], v0 offset:104\n\t" \
    "ds_read_b128 v[76:79], v1 offset:3744\n\t" \
    "ds_read_b128 v[80:83], v1 offset:5280\n\t" \
    "s_mov_b32 s69, 4\n\t" \
    "cv_c2_%=:\n\t" \
    "s_waitcnt lgkmcnt(11)\n\t" \
    "v_mfma_f32_16x16x32_f16 v[8:11], v[36:39], v[48:51], v[8:11]\n\t" \
    "ds_read_b64 v[84:85], v0 offset:128\n\t" \
    "ds_read_b64 v[86:87], v0 offset:136\n\t" \
    "s_waitcnt lgkmcnt(11)\n\t" \
    "v_mfma_f32_16x16x32_f16 v[24:27], v[40:43], v[48:51], v[24:27]\n\t" \
    "ds_read_b128 v[96:99], v1 offset:2112\n\t" \
    "ds_read_b64 v[88:89], v0 offset:160\n\t" \
    "s_waitcnt lgkmcnt(12)\n\t" \
    "v_mfma_f32_16x16x32_f16 v[12:15], v[36:39], v[52:55], v[12:15]\n\t" \
    "ds_read_b64 v[90:91], v0 offset:168\n\t" \
    "ds_read_b128 v[100:103], v1 offset:3648\n\t" \
    "v_mfma_f32_16x16x32_f16 v[28:31], v[40:43], v[52:55], v[28:31]\n\t" \
    "ds_read_b128 v[104:107], v1 offset:5184\n\t" \
    "s_waitcnt lgkmcnt(14)\n\t" \
    "v_mfma_f32_16x16x32_f16 v[16:19], v[36:39], v[56:59], v[16:19]\n\t" \
    "v_mfma_f32_16x16x32_f16 v[32:35], v[40:43], v[56:59], v[32:35]\n\t" \
    "s_waitcnt lgkmcnt(11)\n\t" \
    "v_mfma_f32_16x16x32_f16 v[8:11], v[60:63], v[72:75], v[8:11]\n\t" \
    "ds_read_b64 v[108:109], v0 offset:192\n\t" \
    "ds_read_b64 v[110:111], v0 offset:200\n\t" \
    "s_waitcnt lgkmcnt(11)\n\t" \
    "v_mfma_f32_16x16x32_f16 v[24:27], v[64:67], v[72:75], v[24:27]\n\t" \
    "ds_read_b128 v[120:123], v1 offset:2016\n\t" \
    "ds_read_b64 v[112:113], v0 offset:224\n\t" \
    "s_waitcnt lgkmcnt(12)\n\t" \
    "v_mfma_f32_16x16x32_f16 v[12:15], v[60:63], v[76:79], v[12:15]\n\t" \
    "ds_read_b64 v[114:115], v0 offset:232\n\t" \
    "ds_read_b128 v[124:127], v1 offset:3552\n\t" \
    "v_mfma_f32_16x16x32_f16 v[28:31], v[64:67], v[76:79], v[28:31]\n\t" \
    "ds_read_b128 v[128:131], v1 offset:5088\n\t" \
    "s_waitcnt lgkmcnt(14)\n\t" \
    "v_mfma_f32_16x16x32_f16 v[16:19], v[60:63], v[80:83], v[16:19]\n\t" \
    "v_mfma_f32_16x16x32_f16 v[32:35], v[64:67], v[80:83], v[32:35]\n\t" \
    "s_waitcnt lgkmcnt(11)\n\t" \
    "v_mfma_f32_16x16x32_f16 v[8:11], v[84:87], v[96:99], v[8:11]\n\t" \
    "ds_read_b64 v[36:37], v0 offset:256\n\t" \
    "ds_read_b64 v[38:39], v0 offset:264\n\t" \
    "s_waitcnt lgkmcnt(11)\n\t" \
    "v_mfma_f32_16x16x32_f16 v[24:27], v[88:91], v[96:99], v[24:27]\n\t" \
    "ds_read_b128 v[48:51], v1 offset:1920\n\t" \
    "ds_read_b64 v[40:41], v0 offset:288\n\t" \
    "s_waitcnt lgkmcnt(12)\n\t" \
    "v_mfma_f32_16x16x32_f16 v[12:15], v[84:87], v[100:103], v[12:15]\n\t" \
    "ds_read_b64 v[42:43], v0 offset:296\n\t" \
    "ds_read_b128 v[52:55], v1 offset:3456\n\t" \
    "v_mfma_f32_16x16x32_f16 v[28:31], v[88:91], v[100:103], v[28:31]\n\t" \
    "ds_read_b128 v[56:59], v1 offset:4992\n\t" \
    "s_waitcnt lgkmcnt(14)\n\t" \
    "v_mfma_f32_16x16x32_f16 v[16:19], v[84:87], v[104:107], v[16:19]\n\t" \
    "v_mfma_f32_16x16x32_f16 v[32:35], v[88:91], v[104:107], v[32:35]\n\t" \
    "s_waitcnt lgkmcnt(11)\n\t" \
    "v_mfma_f32_16x16x32_f16 v[8:11], v[108:111], v[120:123], v[8:11]\n\t" \
    "ds_read_b64 v[60:61], v0 offset:320\n\t" \
    "ds_read_b64 v[62:63], v0 offset:328\n\t" \
    "s_waitcnt lgkmcnt(11)\n\t" \
    "v_mfma_f32_16x16x32_f16 v[24:27], v[112:115], v[120:123], v[24:27]\n\t" \
    "ds_read_b128 v[72:75], v1 offset:1824\n\t" \
    "ds_read_b64 v[64:65], v0 offset:352\n\t" \
    "s_waitcnt lgkmcnt(12)\n\t" \
    "v_mfma_f32_16x16x32_f16 v[12:15], v[108:111], v[124:127], v[12:15]\n\t" \
    "ds_read_b64 v[66:67], v0 offset:360\n\t" \
    "ds_read_b128 v[76:79], v1 offset:3360\n\t" \
    "v_mfma_f32_16x16x32_f16 v[28:31], v[112:115], v[124:127], v[28:31]\n\t" \
    "ds_read_b128 v[80:83], v1 offset:4896\n\t" \
    "s_waitcnt lgkmcnt(14)\n\t" \
    "v_mfma_f32_16x16x32_f16 v[16:19], v[108:111], v[128:131], v[16:19]\n\t" \
    "v_mfma_f32_16x16x32_f16 v[32:35], v[112:115], v[128:131], v[32:35]\n\t" \
    "v_add_u32 v0, 256, v0\n\t" \
    "v_subrev_u32 v1, 384, v1\n\t" \
    "s_sub_u32 s69, s69, 1\n\t" \
    "s_cmp_lg_u32 s69, 0\n\t" \
    "s_cbranch_scc1 cv_c2_%=\n\t" \
    "s_waitcnt lgkmcnt(0)\n\t" \
    "ds_read_b64 v[36:37], v0 offset:0\n\t" \
    "ds_read_b64 v[38:39], v0 offset:8\n\t" \
    "ds_read_b128 v[52:55], v1 offset:3840\n\t" \
    "ds_read_b64 v[40:41], v0 offset:32\n\t" \
    "ds_read_b64 v[42:43], v0 offset:40\n\t" \
    "ds_read_b128 v[56:59], v1 offset:5376\n\t" \
    "ds_read_b64 v[60:61], v0 offset:64\n\t" \
    "ds_read_b64 v[62:63], v0 offset:72\n\t" \
    "ds_read_b128 v[76:79], v1 offset:3744\n\t" \
    "ds_read_b64 v[64:65], v0 offset:96\n\t" \
    "ds_read_b64 v[66:67], v0 offset:104\n\t" \
    "ds_read_b128 v[80:83], v1 offset:5280\n\t" \
    "s_mov_b32 s69, 4\n\t" \
    "cv_c3_%=:\n\t" \
    "s_waitcnt lgkmcnt(9)\n\t" \
    "v_mfma_f32_16x16x32_f16 v[12:15], v[36:39], v[52:55], v[12:15]\n\t" \
    "ds_read_b64 v[84:85], v0 offset:128\n\t" \
    "ds_read_b64 v[86:87], v0 offset:136\n\t" \
    "s_waitcnt lgkmcnt(9)\n\t" \
    "v_mfma_f32_16x16x32_f16 v[28:31], v[40:43], v[52:55], v[28:31]\n\t" \
    "ds_read_b128 v[100:103], v1 offset:3648\n\t" \
    "ds_read_b64 v[88:89], v0 offset:160\n\t" \
    "s_waitcnt lgkmcnt(10)\n\t" \
    "v_mfma_f32_16x16x32_f16 v[16:19], v[36:39], v[56:59], v[16:19]\n\t" \
    "ds_read_b64 v[90:91], v0 offset:168\n\t" \
    "ds_read_b128 v[104:107], v1 offset:5184\n\t" \
    "v_mfma_f32_16x16x32_f16 v[32:35], v[40:43], v[56:59], v[32:35]\n\t" \
    "s_waitcnt lgkmcnt(9)\n\t" \
    "v_mfma_f32_16x16x32_f16 v[12:15], v[60:63], v[76:79], v[12:15]\n\t" \
    "ds_read_b64 v[108:109], v0 offset:192\n\t" \
    "ds_read_b64 v[110:111], v0 offset:200\n\t" \
    "s_waitcnt lgkmcnt(9)\n\t" \
    "v_mfma_f32_16x16x32_f16 v[28:31], v[64:67], v[76:79], v[28:31]\n\t" \
    "ds_read_b128 v[124:127], v1 offset:3552\n\t" \
    "ds_read_b64 v[112:113], v0 offset:224\n\t" \
    "s_waitcnt lgkmcnt(10)\n\t" \
    "v_mfma_f32_16x16x32_f16 v[16:19], v[60:63], v[80:83], v[16:19]\n\t" \
    "ds_read_b64 v[114:115], v0 offset:232\n\t" \
    "ds_read_b128 v[128:131], v1 offset:5088\n\t" \
    "v_mfma_f32_16x16x32_f16 v[32:35], v[64:67], v[80:83], v[32:35]\n\t" \
    "s_waitcnt lgkmcnt(9)\n\t" \
    "v_mfma_f32_16x16x32_f16 v[12:15], v[84:87], v[100:103], v[12:15]\n\t" \
    "ds_read_b64 v[36:37], v0 offset:256\n\t" \
    "ds_read_b64 v[38:39], v0 offset:264\n\t" \
    "s_waitcnt lgkmcnt(9)\n\t" \
    "v_mfma_f32_16x16x32_f16 v[28:31], v[88:91], v[100:103], v[28:31]\n\t" \
    "ds_read_b128 v[52:55], v1 offset:3456\n\t" \
    "ds_read_b64 v[40:41], v0 offset:288\n\t" \
    "s_waitcnt lgkmcnt(10)\n\t" \
    "v_mfma_f32_16x16x32_f16 v[16:19], v[84:87], v[104:107], v[16:19]\n\t" \
    "ds_read_b64 v[42:43], v0 offset:296\n\t" \
    "ds_read_b128 v[56:59], v1 offset:4992\n\t" \
    "v_mfma_f32_16x16x32_f16 v[32:35], v[88:91], v[104:107], v[32:35]\n\t" \
    "s_waitcnt lgkmcnt(9)\n\t" \
    "v_mfma_f32_16x16x32_f16 v[12:15], v[108:111], v[124:127], v[12:15]\n\t" \
    "ds_read_b64 v[60:61], v0 offset:320\n\t" \
    "ds_read_b64 v[62:63], v0 offset:328\n\t" \
    "s_waitcnt lgkmcnt(9)\n\t" \
    "v_mfma_f32_16x16x32_f16 v[28:31], v[112:115], v[124:127], v[28:31]\n\t" \
    "ds_read_b128 v[76:79], v1 offset:3360\n\t" \
    "ds_read_b64 v[64:65], v0 offset:352\n\t" \
    "s_waitcnt lgkmcnt(10)\n\t" \
    "v_mfma_f32_16x16x32_f16 v[16:19], v[108:111], v[128:131], v[16:19]\n\t" \
    "ds_read_b64 v[66:67], v0 offset:360\n\t" \
    "ds_read_b128 v[80:83], v1 offset:4896\n\t" \
    "v_mfma_f32_16x16x32_f16 v[32:35], v[112:115], v[128:131], v[32:35]\n\t" \
    "v_add_u32 v0, 256, v0\n\t" \
    "v_subrev_u32 v1, 384, v1\n\t" \
    "s_sub_u32 s69, s69, 1\n\t" \
    "s_cmp_lg_u32 s69, 0\n\t" \
    "s_cbranch_scc1 cv_c3_%=\n\t" \
    "s_waitcnt lgkmcnt(0)\n\t" \
    "ds_read_b64 v[36:37], v0 offset:0\n\t" \
    "ds_read_b64 v[38:39], v0 offset:8\n\t" \
    "ds_read_b128 v[56:59], v1 offset:5376\n\t" \
    "ds_read_b64 v[40:41], v0 offset:32\n\t" \
    "ds_read_b64 v[42:43], v0 offset:40\n\t" \
    "ds_read_b64 v[60:61], v0 offset:64\n\t" \
    "ds_read_b64 v[62:63], v0 offset:72\n\t" \
    "ds_read_b128 v[80:83], v1 offset:5280\n\t" \
    "ds_read_b64 v[64:65], v0 offset:96\n\t" \
    "ds_read_b64 v[66:67], v0 offset:104\n\t" \
    "ds_read_b64 v[84:85], v0 offset:128\n\t" \
    "ds_read_b64 v[86:87], v0 offset:136\n\t" \
    "ds_read_b128 v[104:107], v1 offset:5184\n\t" \
    "ds_read_b64 v[88:89], v0 offset:160\n\t" \
    "ds_read_b64 v[90:91], v0 offset:168\n\t" \
    "s_mov_b32 s69, 4\n\t" \
    "cv_c4_%=:\n\t" \
    "s_waitcnt lgkmcnt(12)\n\t" \
    "v_mfma_f32_16x16x32_f16 v[16:19], v[36:39], v[56:59], v[16:19]\n\t" \
    "ds_read_b64 v[108:109], v0 offset:192\n\t" \
    "ds_read_b64 v[110:111], v0 offset:200\n\t" \
    "ds_read_b128 v[128:131], v1 offset:5088\n\t" \
    "s_waitcnt lgkmcnt(13)\n\t" \
    "v_mfma_f32_16x16x32_f16 v[32:35], v[40:43], v[56:59], v[32:35]\n\t" \
    "ds_read_b64 v[112:113], v0 offset:224\n\t" \
    "ds_read_b64 v[114:115], v0 offset:232\n\t" \
    "s_waitcnt lgkmcnt(12)\n\t" \
    "v_mfma_f32_16x16x32_f16 v[16:19], v[60:63], v[80:83], v[16:19]\n\t" \
    "ds_read_b64 v[36:37], v0 offset:256\n\t" \
    "ds_read_b64 v[38:39], v0 offset:264\n\t" \
    "ds_read_b128 v[56:59], v1 offset:4992\n\t" \
    "s_waitcnt lgkmcnt(13)\n\t" \
    "v_mfma_f32_16x16x32_f16 v[32:35], v[64:67], v[80:83], v[32:35]\n\t" \
    "ds_read_b64 v[40:41], v0 offset:288\n\t" \
    "ds_read_b64 v[42:43], v0 offset:296\n\t" \
    "s_waitcnt lgkmcnt(12)\n\t" \
    "v_mfma_f32_16x16x32_f16 v[16:19], v[84:87], v[104:107], v[16:19]\n\t" \
    "ds_read_b64 v[60:61], v0 offset:320\n\t" \
    "ds_read_b64 v[62:63], v0 offset:328\n\t" \
    "ds_read_b128 v[80:83], v1 offset:4896\n\t" \
    "s_waitcnt lgkmcnt(13)\n\t" \
    "v_mfma_f32_16x16x32_f16 v[32:35], v[88:91], v[104:107], v[32:35]\n\t" \
    "ds_read_b64 v[64:65], v0 offset:352\n\t" \
    "ds_read_b64 v[66:67], v0 offset:360\n\t" \
    "s_waitcnt lgkmcnt(12)\n\t" \
    "v_mfma_f32_16x16x32_f16 v[16:19], v[108:111], v[128:131], v[16:19]\n\t" \
    "ds_read_b64 v[84:85], v0 offset:384\n\t" \
    "ds_read_b64 v[86:87], v0 offset:392\n\t" \
    "ds_read_b128 v[104:107], v1 offset:4800\n\t" \
    "s_waitcnt lgkmcnt(13)\n\t" \
    "v_mfma_f32_16x16x32_f16 v[32:35], v[112:115], v[128:131], v[32:35]\n\t" \
    "ds_read_b64 v[88:89], v0 offset:416\n\t" \
    "ds_read_b64 v[90:91], v0 offset:424\n\t" \
    "v_add_u32 v0, 256, v0\n\t" \
    "v_subrev_u32 v1, 384, v1\n\t" \
    "s_sub_u32 s69, s69, 1\n\t" \
    "s_cmp_lg_u32 s69, 0\n\t" \
    "s_cbranch_scc1 cv_c4_%=\n\t" \
    "s_waitcnt lgkmcnt(0)\n\t" \
    "s_nop 7\n\t" \
    "ds_write_b128 v2, v[4:7] offset:0\n\t" \
    "ds_write_b128 v2, v[8:11] offset:1024\n\t" \
    "ds_write_b128 v2, v[12:15] offset:2048\n\t" \
    "ds_write_b128 v2, v[16:19] offset:3072\n\t" \
    "ds_write_b128 v2, v[20:23] offset:4096\n\t" \
    "ds_write_b128 v2, v[24:27] offset:5120\n\t" \
    "ds_write_b128 v2, v[28:31] offset:6144\n\t" \
    "ds_write_b128 v2, v[32:35] offset:7168\n\t" \
    "s_waitcnt lgkmcnt(0)\n\t" \
    "cv_last_%=:\n\t" \
    ""
#define ASM_CLOB "v0", "v1", "v2", "v3", "v4", "v5", "v6", "v7", "v8", "v9", "v10", "v11", "v12", "v13", "v14", "v15", "v16", "v17", "v18", "v19", "v20", "v21", "v22", "v23", "v24", "v25", "v26", "v27", "v28", "v29", "v30", "v31", "v32", "v33", "v34", "v35", "v36", "v37", "v38", "v39", "v40", "v41", "v42", "v43", "v44", "v45", "v46", "v47", "v48", "v49", "v50", "v51", "v52", "v53", "v54", "v55", "v56", "v57", "v58", "v59", "v60", "v61", "v62", "v63", "v64", "v65", "v66", "v67", "v68", "v69", "v70", "v71", "v72", "v73", "v74", "v75", "v76", "v77", "v78", "v79", "v80", "v81", "v82", "v83", "v84", "v85", "v86", "v87", "v88", "v89", "v90", "v91", "v92", "v93", "v94", "v95", "v96", "v97", "v98", "v99", "v100", "v101", "v102", "v103", "v104", "v105", "v106", "v107", "v108", "v109", "v110", "v111", "v112", "v113", "v114", "v115", "v116", "v117", "v118", "v119", "v120", "v121", "v122", "v123", "v124", "v125", "v126", "v127", "v128", "v129", "v130", "v131", "v132", "v133", "v134", "v135", "v136", "v137", "v138", "v139", "v140", "v141", "v142", "v143", "v144", "v145", "v146", "v147", "v148", "v149", "v150", "v151", "v152", "v153", "v154", "v155", "v156", "v157", "v158", "v159", "v160", "v161", "v162", "v163", "v164", "v165", "v166", "v167", "v168", "v169", "v170", "v171", "v172", "v173", "v174", "v175", "v176", "v177", "v178", "v179", "v180", "v181", "v182", "v183", "v184", "v185", "v186", "v187", "v188", "v189", "v190", "v191", "v192", "v193", "v194", "v195", "v196", "v197", "v198", "v199", "v200", "v201", "v202", "v203", "v204", "v205", "v206", "v207", "v208", "v209", "v210", "v211", "v212", "v213", "v214", "v215", "v222", "v223", "v224", "v225", "v226", "v227", "v228", "v229", "v230", "v231", "v232", "v233", "v234", "v235", "v236", "v237", "v238", "v239", "v240", "v241", "v242", "v243", "v244", "v245", "v246", "v247", "v248", "v249", "v250", "v251", "v252", "v253", "v254", "v255", "a0", "a1", "a2", "a3", "a4", "a5", "a6", "a7", "a8", "a9", "a10", "a11", "a12", "a13", "a14", "a15", "a16", "a17", "a18", "a19", "a20", "a21", "a22", "a23", "a24", "a25", "a26", "a27", "a28", "a29", "a30", "a31", "a32", "a33", "a34", "a35", "a36", "a37", "a38", "a39", "a40", "a41", "a42", "a43", "a44", "a45", "a46", "a47", "a48", "a49", "a50", "a51", "a52", "a53", "a54", "a55", "a56", "a57", "a58", "a59", "a60", "a61", "a62", "a63", "a64", "a65", "a66", "a67", "a68", "a69", "a70", "a71", "a72", "a73", "a74", "a75", "a76", "a77", "a78", "a79", "a80", "a81", "a82", "a83", "a84", "a85", "a86", "a87", "a88", "a89", "a90", "a91", "a92", "a93", "a94", "a95", "a96", "a97", "a98", "a99", "a100", "a101", "a102", "a103", "a104", "a105", "a106", "a107", "a108", "a109", "a110", "a111", "a112", "a113", "a114", "a115", "a116", "a117", "a118", "a119", "a120", "a121", "a122", "a123", "a124", "a125", "a126", "a127", "a128", "a129", "a130", "a131", "a132", "a133", "a134", "a135", "a136", "a137", "a138", "a139", "a140", "a141", "a142", "a143", "a144", "a145", "a146", "a147", "a148", "a149", "a150", "a151", "a152", "a153", "a154", "a155", "a156", "a157", "a158", "a159", "a160", "a161", "a162", "a163", "a164", "a165", "a166", "a167", "a168", "a169", "a170", "a171", "a172", "a173", "a174", "a175", "a176", "a177", "a178", "a179", "a180", "a181", "a182", "a183", "a184", "a185", "a186", "a187", "a188", "a189", "a190", "a191", "a192", "a193", "a194", "a195", "a196", "a197", "a198", "a199", "a200", "a201", "a202", "a203", "a204", "a205", "a206", "a207", "a208", "a209", "a210", "a211", "a212", "a213", "a214", "a215", "a216", "a217", "a218", "a219", "a220", "a221", "a222", "a223", "a224", "a225", "a226", "a227", "a228", "a229", "a230", "a231", "a232", "a233", "a234", "a235", "a236", "a237", "a238", "a239", "a240", "a241", "a242", "a243", "a244", "a245", "a246", "a247", "a248", "a249", "a250", "a251", "a252", "a253", "a254", "a255", "s33", "s34", "s35", "s36", "s37", "s38", "s39", "s40", "s41", "s42", "s43", "s44", "s45", "s46", "s47", "s48", "s49", "s50", "s51", "s52", "s53", "s54", "s55", "s56", "s57", "s58", "s59", "s60", "s61", "s62", "s63", "s64", "s65", "s66", "s67", "s68", "s69", "s70", "s71", "s72", "s73", "s74", "s75", "s76", "s77", "s78", "s79", "s80", "s81", "s82", "s83", "s84", "s85", "s86", "s87", "s88", "s89", "s90", "s91", "s92", "s93", "s94", "s95", "s96", "s97", "s98", "s99", "vcc", "scc", "memory"
#define CV_CLOB "v0", "v1", "v2", "v3", "v4", "v5", "v6", "v7", "v8", "v9", "v10", "v11", "v12", "v13", "v14", "v15", "v16", "v17", "v18", "v19", "v20", "v21", "v22", "v23", "v24", "v25", "v26", "v27", "v28", "v29", "v30", "v31", "v32", "v33", "v34", "v35", "v36", "v37", "v38", "v39", "v40", "v41", "v42", "v43", "v44", "v45", "v46", "v47", "v48", "v49", "v50", "v51", "v52", "v53", "v54", "v55", "v56", "v57", "v58", "v59", "v60", "v61", "v62", "v63", "v64", "v65", "v66", "v67", "v68", "v69", "v70", "v71", "v72", "v73", "v74", "v75", "v76", "v77", "v78", "v79", "v80", "v81", "v82", "v83", "v84", "v85", "v86", "v87", "v88", "v89", "v90", "v91", "v92", "v93", "v94", "v95", "v96", "v97", "v98", "v99", "v100", "v101", "v102", "v103", "v104", "v105", "v106", "v107", "v108", "v109", "v110", "v111", "v112", "v113", "v114", "v115", "v116", "v117", "v118", "v119", "v120", "v121", "v122", "v123", "v124", "v125", "v126", "v127", "v128", "v129", "v130", "v131", "s69", "scc", "memory"
static constexpr unsigned G_STEP = 0x80000;
static constexpr unsigned G_CPY = 0x10000;
static constexpr unsigned G_HOFF = 0x40000;
static constexpr unsigned G_RM = 0x4;
static constexpr unsigned G_RP = 0x2;
static constexpr unsigned G_HP = 0x20;
static constexpr unsigned G_HKB = 0x8;
static constexpr unsigned G_RHO = 0x1400;
static constexpr unsigned G_RHS = 0x600;
static constexpr unsigned G_RARM = 0x1;
static constexpr unsigned G_NARM = 0x4;
static constexpr unsigned G_WS_UM = 0x400000;
static constexpr unsigned G_WS_CTL = 0x5d0000;
static constexpr unsigned G_WS_DONE = 0x5f8000;
static constexpr unsigned G_WS_EP = 0x5fa000;
static constexpr unsigned G_WS_STAMPS = 0xc000000;
static constexpr unsigned G_C_TASK = 0x200;
static constexpr unsigned G_C_GX = 0x1000;
static constexpr unsigned G_C_GV = 0x4000;
static constexpr unsigned G_C_KM = 0x8000;
static constexpr unsigned G_C_UM = 0x9000;
static constexpr unsigned G_C_PC = 0xc000;
static constexpr unsigned G_C_UT = 0xd000;
static constexpr unsigned G_C_GK = 0x10000;
static constexpr unsigned G_C_YC = 0x11000;
static constexpr unsigned G_C_ERR = 0x100;
static constexpr unsigned G_C_ARR = 0x14000;
static constexpr unsigned G_C_KG = 0x1c000;
static constexpr unsigned G_C_END = 0x22000;
static constexpr unsigned G_CTF = 0x2000;
static constexpr unsigned G_GVC = 0x2;
static constexpr unsigned G_GVH = 0x14000;
static constexpr unsigned G_HS8 = 0x80000;
static constexpr unsigned G_HS4 = 0x40000;
static constexpr unsigned G_UXS = 0x200000;
static constexpr unsigned G_UMH = 0xa0000;
static constexpr unsigned G_D_QX = 0x600000;
static constexpr unsigned G_D_KX = 0x1600000;
static constexpr unsigned G_D_KM = 0x2600000;
static constexpr unsigned G_D_UX = 0x2e00000;
static constexpr unsigned G_D_UMX = 0x7600000;
static constexpr unsigned G_D_PX = 0x8c00000;
static constexpr unsigned G_D_PM = 0x9000000;
static constexpr unsigned G_D_KT = 0x9100000;
static constexpr unsigned G_D_UT = 0x9600000;
static constexpr unsigned G_D_YB = 0x9900000;
static constexpr unsigned G_D_END = 0x9c00000;
static constexpr unsigned G_SKM = 0x7;
static constexpr unsigned G_GVN = 0x2;
static constexpr unsigned G_STG = 0x9000;
static constexpr unsigned G_NA_ = 0x22;
static constexpr unsigned G_KROW = 0x30;
#define FLG(x_) (kDefault[5])
#define FLG2(x_) (kDefault[7])
static constexpr int kDefault[8] = {PRM_DEFAULT};
#define MFMA32(a_, b_, c_) __builtin_amdgcn_mfma_f32_16x16x32_bf16(__builtin_bit_cast(bf16x8, a_), __builtin_bit_cast(bf16x8, b_), c_, 0, 0, 0)
#define MFMA16(a_, b_, c_) __builtin_amdgcn_mfma_f32_16x16x16bf16_1k(__builtin_bit_cast(s16x4, a_), __builtin_bit_cast(s16x4, b_), c_, 0, 0, 0)
#define WLD8(d_, voff_, base_, off_) asm volatile("s_nop 4\n\tglobal_load_dwordx2 %0, %1, %2 offset:%3 sc1" : "=v"(d_) : "v"(voff_), "s"(base_), "i"(off_) : "memory")
#define WLD16(d_, voff_, base_, off_) asm volatile("s_nop 4\n\tglobal_load_dwordx4 %0, %1, %2 offset:%3 sc1" : "=v"(d_) : "v"(voff_), "s"(base_), "i"(off_) : "memory")
#define WST16(voff_, d_, base_, off_) asm volatile("s_nop 4\n\tglobal_store_dwordx4 %0, %1, %2 offset:%3 sc1\n\ts_nop 1" :: "v"(voff_), "v"(d_), "s"(base_), "i"(off_) : "memory")
#define WLD16X4(d0, d1, d2, d3, voff_, base_) asm volatile("s_nop 4\n\tglobal_load_dwordx4 %0, %4, %5 sc1\n\tglobal_load_dwordx4 %1, %4, %5 offset:1024 sc1\n\t" \
    "global_load_dwordx4 %2, %4, %5 offset:2048 sc1\n\tglobal_load_dwordx4 %3, %4, %5 offset:3072 sc1" \
    : "=&v"(d0), "=&v"(d1), "=&v"(d2), "=&v"(d3) : "v"(voff_), "s"(base_) : "memory")

__device__ __forceinline__ u32x4 pack8(const f32x4& a, const f32x4& b) {
    return u32x4{pk2bf(a[0], a[1]), pk2bf(a[2], a[3]), pk2bf(b[0], b[1]), pk2bf(b[2], b[3])};
}
template <class T> __device__ __forceinline__ T* uni(T* q) {
    const u64 v = (u64)q;
    unsigned lo = __builtin_amdgcn_readfirstlane((unsigned)v), hi = __builtin_amdgcn_readfirstlane((unsigned)(v >> 32));
    asm volatile("s_nop 4" : "+s"(lo), "+s"(hi));
    return (T*)(((u64)hi << 32) | lo);
}
#define CTLO (G_WS_CTL + (unsigned)p.prm[0])
template <bool FAST = false>
__device__ __forceinline__ void wait_word(const MainP& p, const char* f, int want, unsigned code) {
    unsigned tries = 0;
    while (__any((int)__hip_atomic_load((const gu32*)f, RLX_AGENT) < want)) {
        if (++tries > kWaitCap) { __hip_atomic_store((gu32*)(p.ws + G_WS_CTL + G_C_ERR), code, RLX_AGENT); break; }
        if ((tries & 1023u) == 0u && __hip_atomic_load((const gu32*)(p.ws + G_WS_CTL + G_C_ERR), RLX_AGENT) != 0xffffffffu) break;
        if (FAST) __builtin_amdgcn_s_sleep(1); else __builtin_amdgcn_s_sleep(10);
    }
}

__device__ __forceinline__ int skew(int s, int id) { return (s ^ (G_SKM * id)) & 63; }
__device__ __forceinline__ unsigned umx_base(int h) { return G_D_UMX + (h / 3) * 0x200000 + (h % 3) * G_UMH; }
typedef GAS unsigned long long gu64;
__device__ __forceinline__ void init_done(char* ws, int slot, unsigned val) {
    const unsigned e = __hip_atomic_load((const gu32*)(ws + G_WS_EP), RLX_AGENT);
    __hip_atomic_store((gu64*)(ws + G_WS_DONE + slot * 64), ((u64)val << 32) | (e + 1u), RLX_AGENT);
}
__device__ __forceinline__ void init_seen(char* ws, int lane, unsigned e) {
    for (unsigned tries = 0; tries < kWaitCap; ++tries) {
        if (!__any(__hip_atomic_load((const gu32*)(ws + G_WS_DONE + min(lane, 59) * 64), RLX_AGENT) != e + 1u)) break;
        __builtin_amdgcn_s_sleep(8);
    }
}
__device__ __forceinline__ void init_count(char* ws, int lane) { if (lane == 0) __hip_atomic_fetch_add((gu32*)(ws + G_WS_CTL + G_C_ARR), 1u, RLX_AGENT); }
__device__ __forceinline__ void fill_block(char* ws, int b, int tid, const u32x4& v0, const u32x4& v1) {
    const unsigned o = b * 8192 + tid * 16, o2 = o + 4096;
    asm volatile("global_store_dwordx4 %0, %1, %2 sc1\n\ts_nop 1" :: "v"(o), "v"(v0), "s"(ws) : "memory");
    asm volatile("global_store_dwordx4 %0, %1, %2 sc1\n\ts_nop 1" :: "v"(o2), "v"(v1), "s"(ws) : "memory");
}
__device__ __forceinline__ void init_fill(char* ws, const u16* mem0, int f, int tid) {
    const u32x4 sent = {0xffffffffu, 0xffffffffu, 0xffffffffu, 0xffffffffu};
    u32x4 v0 = sent, v1 = sent;
    if (f < 16) {
#pragma unroll
        for (int k = 0; k < 2; ++k) {
            const int q = tid + 256 * k + ((f & 2) ? 256 : 0), m = q & 15;
            if (!(f & 2) || (q >= 512 && q < 576)) {
                const unsigned* src = (const unsigned*)(mem0 + (2 * (m >> 2) + (m & 1)) * DM + 16 * (q >> 4) + (m & 2));
                const u32x4 o = {min(src[0], 0xfffffffeu), min(src[2], 0xfffffffeu), min(src[4], 0xfffffffeu), min(src[6], 0xfffffffeu)};
                if (k) v1 = o; else v0 = o;
            }
        }
    }
    constexpr int HB2 = 2 * ((G_HKB + 7) / 8), NH = 32 + 8 * G_RP, NP = NH + 8 * HB2, NU = 144;
#pragma unroll 1
    for (int q = f; q < NP + NU + (int)(G_C_END >> 13); q += 48) {
        const int u = q - 32, y = q - NH;
        fill_block(ws, q < 32 ? (q >> 4) * 256 + ((q >> 2) & 3) * 8 + 4 + (q & 3) : q < NH ? ((u >> 2) & 1) * 256 + (u >> 3) * 8 + (u & 3) : q < NP ? (y & 1) * 256 + 32 + ((y >> 1) & 3) * 8 + (y >> 3) : q < NP + NU ? q - NP + 512 : q - NP - NU + (int)(G_WS_CTL >> 13), tid, v0, v1);
        v0 = sent; v1 = sent;
    }
    const unsigned e = __hip_atomic_load((const gu32*)(ws + G_WS_EP), RLX_AGENT);
    asm volatile("s_waitcnt vmcnt(0)" :: "v"(e) : "memory");
    __syncthreads();
    if (tid == 0) __hip_atomic_store((gu64*)(ws + G_WS_DONE + (12 + f) * 64), (u64)(e + 1u), RLX_AGENT);
}
__device__ __forceinline__ void init_cal(char* ws, char* smem, int id, int tid, int n) {
    const int sl = id % 6, w = tid >> 6;
    char* a = ws + (size_t)(sl >> 1) * 0x200000 + 0x1B0000 + (id / 6) * 16384 + (sl & 1) * 8192 + w * 2048;
    volatile unsigned* lds = (volatile unsigned*)smem;
    const u32x4 z = {0u, 0u, 0u, 0u};
    if (tid == 0) lds[4] = 0u;
    asm volatile("global_store_dwordx4 %0, %1, off sc1\n\tglobal_store_dwordx4 %0, %1, off offset:1024 sc1\n\ts_waitcnt vmcnt(0)" :: "v"(a + (tid & 63) * 16), "v"(z) : "memory");
    __syncthreads();
    if ((tid & 63) == 0) {
        unsigned best = 0xfffffffeu;
#pragma unroll 1
        for (int it = 0; it < 2 * n; ++it) {
            const unsigned c0 = (unsigned)__builtin_amdgcn_s_memtime();
            const unsigned v = __hip_atomic_fetch_or((gu32*)(a + it * 128), 0u, RLX_AGENT);
            asm volatile("s_waitcnt vmcnt(0)" :: "v"(v) : "memory");
            best = min(best, (unsigned)__builtin_amdgcn_s_memtime() - c0);
            if (best < 500u) lds[4] = 1u;
            if (it + 1 >= n && lds[4]) break;
        }
        lds[w] = best;
    }
    __syncthreads();
    if (tid == 0) init_done(ws, id, min(min(lds[0], lds[1]), min(lds[2], lds[3])));
    __syncthreads();
}

#define TST(slot_, i_)
#define PIN8(a) asm volatile("" : "+v"(a[0]), "+v"(a[1]), "+v"(a[2]), "+v"(a[3]), "+v"(a[4]), "+v"(a[5]), "+v"(a[6]), "+v"(a[7]))
struct AOps { u32x4 q[4], k[8]; int fl; };
struct APtr { const char* q; const char* k; const char* m; unsigned voff, vkm, voffm, vfl; };
__device__ __forceinline__ void attn_batch(const MainP& p, AOps& o, const APtr& a) {
    const char* q2 = a.q + 4096; const char* k2 = a.k + 4096;
    WLD16(o.q[0], a.voff, a.q, 0); WLD16(o.q[1], a.voff, a.q, 2048); WLD16(o.q[2], a.voff, q2, 0); WLD16(o.q[3], a.voff, q2, 2048);
    WLD16X4(o.k[0], o.k[1], o.k[2], o.k[3], a.voff, a.k); WLD16X4(o.k[4], o.k[5], o.k[6], o.k[7], a.voff, k2);
    asm volatile("global_load_dword %0, %1, %2 sc1" : "=v"(o.fl) : "v"(a.vfl), "s"(p.ws) : "memory");
}
__device__ __forceinline__ void attn_pin(AOps& o) {
    asm volatile("" : "+v"(o.fl), "+v"(o.q[0]), "+v"(o.q[1]), "+v"(o.q[2]), "+v"(o.q[3])); PIN8(o.k);
}
static constexpr int NFINE = 6;
static constexpr int NG1 = 64 - NFINE;
__device__ __forceinline__ void score_round4(float* r, const f32x4& x) {
    asm volatile("v_cvt_pk_bf16_f32 %0, 0, %4\n\tv_cvt_pk_bf16_f32 %1, 0, %5\n\tv_cvt_pk_bf16_f32 %2, 0, %6\n\tv_cvt_pk_bf16_f32 %3, 0, %7\n\t"
                 "v_mul_f32 %0, 0x3db504f3, %0\n\tv_mul_f32 %1, 0x3db504f3, %1\n\tv_mul_f32 %2, 0x3db504f3, %2\n\tv_mul_f32 %3, 0x3db504f3, %3\n\t"
                 "v_cvt_pk_bf16_f32 %0, 0, %0\n\tv_cvt_pk_bf16_f32 %1, 0, %1\n\tv_cvt_pk_bf16_f32 %2, 0, %2\n\tv_cvt_pk_bf16_f32 %3, 0, %3"
                 : "=&v"(r[0]), "=&v"(r[1]), "=&v"(r[2]), "=&v"(r[3]) : "v"(x[0]), "v"(x[1]), "v"(x[2]), "v"(x[3]));
}
__device__ __forceinline__ float vmax3(float a, float b, float c) { float r; asm("v_max3_f32 %0, %1, %2, %3" : "=v"(r) : "v"(a), "v"(b), "v"(c)); return r; }
template <int N>
__device__ __forceinline__ void attn_soft(float (&q)[12], u32x2& Pm, u32x4& Px) {
    float m = vmax3(q[0], q[1], q[2]);
#pragma unroll
    for (int i = 3; i < N; i += 2) m = vmax3(m, q[i], q[i + 1 < N ? i + 1 : i]);
    { auto a = __builtin_amdgcn_permlane16_swap(__float_as_uint(m), __float_as_uint(m), false, false); m = vmax3(m, __uint_as_float(a[0]), __uint_as_float(a[1])); }
    { auto a = __builtin_amdgcn_permlane32_swap(__float_as_uint(m), __float_as_uint(m), false, false); m = vmax3(m, __uint_as_float(a[0]), __uint_as_float(a[1])); }
    const float ml = -m * 1.4426950408889634f;
#pragma unroll
    for (int i = 0; i < N; ++i) q[i] = __builtin_amdgcn_exp2f(fmaf(q[i], 1.4426950408889634f, ml));
    float sum = 0.5f * ((q[0] + q[1]) + (q[2] + q[3])) + ((q[4] + q[5]) + (q[6] + q[7]));
    if (N > 8) sum += (q[8] + q[9]) + (q[10] + q[11]);
    sum = rowsum4(sum);
    const float inv = __builtin_amdgcn_rcpf(sum);
    Pm = u32x2{pk2bf(q[0] * inv, q[1] * inv), pk2bf(q[2] * inv, q[3] * inv)};
    Px = u32x4{pk2bf(q[4] * inv, q[5] * inv), pk2bf(q[6] * inv, q[7] * inv), N > 8 ? pk2bf(q[8] * inv, q[9] * inv) : 0u, N > 8 ? pk2bf(q[10] * inv, q[11] * inv) : 0u};
}
static constexpr int NA = G_NA_;
__device__ __forceinline__ int attn_next(int s) { return s < NA ? s + 1 : s + 2; }
__device__ __forceinline__ unsigned attn_flag(const MainP& p, int s, int hq) { return CTLO + G_C_PC + ((s > NA && (s & 1)) ? 1024 : 0) + hq * 128; }
struct BOps { u32x4 q[8], k[8]; };
#define WLD16X4A(d0, d1, d2, d3, voff_, base_) asm volatile("s_nop 4\n\tglobal_load_dwordx4 %0, %4, %5 sc1\n\tglobal_load_dwordx4 %1, %4, %5 offset:1024 sc1\n\t" \
    "global_load_dwordx4 %2, %4, %5 offset:2048 sc1\n\tglobal_load_dwordx4 %3, %4, %5 offset:3072 sc1" \
    : "=&a"(d0), "=&a"(d1), "=&a"(d2), "=&a"(d3) : "v"(voff_), "s"(base_) : "memory")
#define PIN8A(a) asm volatile("" : "+a"(a[0]), "+a"(a[1]), "+a"(a[2]), "+a"(a[3]), "+a"(a[4]), "+a"(a[5]), "+a"(a[6]), "+a"(a[7]))
#define MFMA_AV0(acc, a_, b_) asm volatile("v_mfma_f32_16x16x32_bf16 %0, %1, %2, 0" : "=&v"(acc) : "a"(a_), "v"(b_))
#define MFMA_AV(acc, a_, b_) asm volatile("v_mfma_f32_16x16x32_bf16 %0, %1, %2, %0" : "+v"(acc) : "a"(a_), "v"(b_))
__device__ __forceinline__ void attn_batch8(const MainP& p, BOps& o, const APtr& a) {
    const char* q2 = a.q + 4096; const char* k2 = a.k + 4096;
    WLD16X4(o.q[0], o.q[1], o.q[2], o.q[3], a.voff, a.q); WLD16X4(o.q[4], o.q[5], o.q[6], o.q[7], a.voff, q2);
    WLD16X4A(o.k[0], o.k[1], o.k[2], o.k[3], a.voff, a.k); WLD16X4A(o.k[4], o.k[5], o.k[6], o.k[7], a.voff, k2);
}
__device__ __forceinline__ void attn_step(const MainP& p, BOps& o, BOps& n1, BOps& n2, APtr& a, int& kg, int& kk, int s, int h, int w, int lane) {
    const int g = lane >> 4, c = lane & 15;
    const float scale = 0.08838834764831845f;
    TST(s * 64 + h * 2, 0);
    const int s2 = attn_next(attn_next(s));
    if (kg <= s2 || kk < s) {
        VMC0();
        wait_word<true>(p, p.ws + a.vfl, (lane & 1) ? s : s2 + 1, 13u);
    }
    TST(s * 64 + h * 2, 1);
    a.q = p.ws + G_D_QX + (size_t)h * G_HS8 + skew(s2, h) * 8192; a.k = a.q + (G_D_KX - G_D_QX); a.m = p.ws + G_D_KM + (size_t)h * G_HS4 + skew(s, h) * 4096;
    __builtin_amdgcn_sched_barrier(0);
    u32x4 km[4];
    int fl;
    WLD16X4A(km[0], km[1], km[2], km[3], a.vkm, a.m);
    asm volatile("global_load_dword %0, %1, %2 sc1" : "=v"(fl) : "v"(a.vfl), "s"(p.ws) : "memory");
    attn_batch8(p, n2, a);
    f32x4 y[5];
#pragma unroll
    for (int wc = 0; wc < 4; ++wc)
#pragma unroll
        for (int i = 0; i < 3; ++i) { if (wc == 0) MFMA_AV0(y[i], o.k[i >> 1], o.q[(i + 1) >> 1]); else MFMA_AV(y[i], o.k[wc * 2 + (i >> 1)], o.q[wc * 2 + ((i + 1) >> 1)]); }
    asm volatile("s_nop 7" : "+v"(y[0]), "+v"(y[1]), "+v"(y[2]));
    float q0[12], q1[12];
    score_round4(q0 + 4, y[0]); score_round4(q1 + 4, y[1]); score_round4(q1 + 8, y[2]);
#pragma unroll
    for (int i = 0; i < 4; ++i) {
        const bool vis = 4 * g + i <= c;
        q0[4 + i] = vis ? q0[4 + i] : -INFINITY; q1[8 + i] = vis ? q1[8 + i] : -INFINITY;
    }
    __builtin_amdgcn_sched_barrier(0);
    asm volatile("s_waitcnt vmcnt(16)\n\ts_barrier" : "+a"(km[0]), "+a"(km[1]), "+a"(km[2]), "+a"(km[3]), "+v"(fl) :: "memory");
    PIN8(n1.q); PIN8A(n1.k);
    { const unsigned fo = attn_flag(p, s, h >> 2); const int fv = s - 1;
      asm volatile("s_cmp_lg_u32 %3, 0\n\ts_cbranch_scc1 1f\n\tglobal_store_dword %0, %1, %2 sc1\n1:" :: "v"(fo), "v"(fv), "s"(p.ws), "s"(w) : "scc", "memory"); }
#pragma unroll
    for (int wc = 0; wc < 4; ++wc)
#pragma unroll
        for (int i = 0; i < 2; ++i) { if (wc == 0) MFMA_AV0(y[3 + i], km[0], o.q[i]); else MFMA_AV(y[3 + i], km[wc], o.q[wc * 2 + i]); }
    asm volatile("s_nop 7" : "+v"(y[3]), "+v"(y[4]));
    kg = __builtin_amdgcn_readlane(fl, 0); kk = __builtin_amdgcn_readlane(fl, 1);
    score_round4(q0, y[3]); score_round4(q1, y[4]);
    u32x2 Pm0, Pm1; u32x4 Px0, Px1;
    attn_soft<8>(q0, Pm0, Px0); attn_soft<12>(q1, Pm1, Px1);
    char* pxb = p.ws + G_D_PX + (size_t)(s * 32 + h) * 2048;
    char* pmb = p.ws + G_D_PM + (size_t)s * 16384 + h * 256; char* pm2 = pmb + 8192;
    WST16(a.voff, Px0, pxb, 0); WST16(a.voff, Px1, pxb, 1024);
    asm volatile("global_store_dwordx2 %0, %1, %3 sc1\n\tglobal_store_dwordx2 %0, %2, %4 sc1" :: "v"(a.voffm), "v"(Pm0), "v"(Pm1), "s"(pmb), "s"(pm2) : "memory");
    { const unsigned fo = attn_flag(p, s, h >> 2);
      asm volatile("s_cmp_lt_u32 %4, 46\n\ts_cbranch_scc1 1f\n\ts_waitcnt vmcnt(0)\n\ts_barrier\n\ts_cmp_lg_u32 %3, 0\n\ts_cbranch_scc1 1f\n\tglobal_store_dword %0, %1, %2 sc1\n1:"
                   :: "v"(fo), "v"(s), "s"(p.ws), "s"(w), "s"(s) : "scc", "memory"); }
    TST(s * 64 + h * 2, 2);
}
__device__ __forceinline__ void attn_role(const MainP& p, int s, int h, int w, int lane) {
    APtr a;
    a.q = p.ws + G_D_QX + (size_t)h * G_HS8 + skew(s, h) * 8192; a.k = a.q + (G_D_KX - G_D_QX);
    a.voff = lane * 16; a.vkm = (lane & 55) * 16; a.voffm = (lane & 15) * 16 + ((lane >> 4) & 1) * 8;
    a.vfl = CTLO + ((lane & 1) ? G_C_KM : G_C_GX) + h * 128;
    BOps o0, o1, o2;
    wait_word(p, p.ws + CTLO + G_C_GX + h * 128, attn_next(s) + 1, 10u);
    attn_batch8(p, o0, a);
    a.q = p.ws + G_D_QX + (size_t)h * G_HS8 + skew(attn_next(s), h) * 8192; a.k = a.q + (G_D_KX - G_D_QX);
    attn_batch8(p, o1, a);
    VMC0();
    PIN8(o0.q); PIN8A(o0.k); PIN8(o1.q); PIN8A(o1.k);
    int kg = -1, kk = -1;
    static_assert(NA % 2 == 0 && NG1 + 3 < NSEG, "ranges");
#pragma unroll 1
    for (;;) {
        attn_step(p, o0, o1, o2, a, kg, kk, s, h, w, lane);
        if ((s = attn_next(s)) >= NG1) break;
        attn_step(p, o1, o2, o0, a, kg, kk, s, h, w, lane);
        if ((s = attn_next(s)) >= NG1) break;
        attn_step(p, o2, o0, o1, a, kg, kk, s, h, w, lane);
        if ((s = attn_next(s)) >= NG1) break;
    }
    VMC0();
}
__device__ __forceinline__ unsigned umax4(const u32x4& v) { return max(max(v[0], v[1]), max(v[2], v[3])); }
__device__ __forceinline__ bool give_up(const MainP& p, unsigned& tries, unsigned code) {
    if (++tries <= kWaitCap / 4) return false;
    __hip_atomic_store((gu32*)(p.ws + G_WS_CTL + G_C_ERR), code, RLX_AGENT);
    return true;
}
__device__ __forceinline__ void task_attn_tail(const MainP& p, int s, int h, int w, int lane) {
    const int g = lane >> 4, c = lane & 15, qt = w & 1;
    const float scale = 0.08838834764831845f;
    APtr a;
    a.q = p.ws + G_D_QX + (size_t)h * G_HS8 + skew(s, h) * 8192 + qt * 1024; a.k = p.ws + G_D_KX + (size_t)h * G_HS8 + skew(s, h) * 8192; a.m = p.ws + G_D_KM + (size_t)h * G_HS4 + skew(s, h) * 4096;
    a.voff = lane * 16; a.vkm = (lane & 55) * 16; a.voffm = c * 16 + (g & 1) * 8; a.vfl = G_WS_CTL;
    AOps o;
    wait_word(p, p.ws + CTLO + G_C_GX + h * 128, s + 1, 14u);
    attn_batch(p, o, a);
    VMC0();
    attn_pin(o);
    float q[12];
    u32x4 km[4];
#pragma unroll
    for (int kt = 1; kt >= -1; --kt) {
        if (kt < 0) {
            TST(s * 64 + h * 2 + qt, 0);
            wait_word(p, p.ws + CTLO + G_C_KM + h * 128, s - 2, 15u);
            if (FLG(p.prm[5]) & 2) wait_word<true>(p, p.ws + CTLO + G_C_KM + h * 128, s - 1, 15u);
            for (unsigned tries = 0;;) {
                WLD16X4(km[0], km[1], km[2], km[3], a.vkm, a.m);
                asm volatile("s_waitcnt vmcnt(0)" : "+v"(km[0]), "+v"(km[1]), "+v"(km[2]), "+v"(km[3]) :: "memory");
                if (!__any(max(max(umax4(km[0]), umax4(km[1])), max(umax4(km[2]), umax4(km[3]))) == 0xffffffffu) || give_up(p, tries, 16u)) break;
            }
            TST(s * 64 + h * 2 + qt, 1);
        }
        f32x4 y = {0.f, 0.f, 0.f, 0.f};
#pragma unroll
        for (int wc = 0; wc < 4; ++wc) y = MFMA32(kt < 0 ? km[wc] : o.k[wc * 2 + (kt < 0 ? 0 : kt)], o.q[wc], y);
#pragma unroll
        for (int i = 0; i < 4; ++i) q[4 + 4 * kt + i] = (16 * kt + 4 * g + i <= 16 * qt + c) ? bfr(bfr(y[i]) * scale) : -INFINITY;
    }
    u32x2 Pm; u32x4 Px;
    attn_soft<12>(q, Pm, Px);
    char* pxb = p.ws + G_D_PX + (size_t)(s * 32 + h) * 2048 + qt * 1024;
    char* pmb = p.ws + G_D_PM + (size_t)s * 16384 + h * 256 + qt * 8192;
    WST16(a.voff, Px, pxb, 0);
    asm volatile("global_store_dwordx2 %0, %1, %2 sc1" :: "v"(a.voffm), "v"(Pm), "s"(pmb) : "memory");
    TST(s * 64 + h * 2 + qt, 2);
}

#define RLD(d_, voff_, base_, off_) asm volatile("global_load_dwordx4 %0, %1, %2 offset:%3 sc1" : "=v"(d_) : "v"(voff_), "s"(base_), "i"(off_) : "memory")
#define RLDA(d_, voff_, base_, off_) asm volatile("global_load_dwordx4 %0, %1, %2 offset:%3 sc1" : "=a"(d_) : "v"(voff_), "s"(base_), "i"(off_) : "memory")
#define MFMA_ACC_A(acc, a_, b_) asm volatile("v_mfma_f32_16x16x32_bf16 %0, %1, %2, %0" : "+v"(acc) : "a"(a_), "v"(b_))
#define VMW(n_) asm volatile("s_waitcnt vmcnt(%0)" :: "i"(n_) : "memory")
template <int NT, int DEPTH, bool TAIL>
__device__ __forceinline__ void task_out(const MainP& p, char* smem, int s, int ct0, int w, int lane) {
    const int g = lane >> 4, c = lane & 15;
    const unsigned voff = lane * 16;
    f32x4 acc[NT][2];
#pragma unroll
    for (int t = 0; t < NT; ++t) { acc[t][0] = f32x4{0.f, 0.f, 0.f, 0.f}; acc[t][1] = acc[t][0]; asm volatile("" : "+v"(acc[t][0]), "+v"(acc[t][1])); }
    const char* ub[NT];
#pragma unroll
    for (int t = 0; t < NT; ++t) ub[t] = uni(p.ws + G_D_UX + (size_t)(ct0 + t) * G_UXS + skew(s, ct0 + t) * 32768 + w * 8192);
    const char* pb = uni(p.ws + G_D_PX + (size_t)(s * 32 + w * 8) * 2048);
    const char* um = uni(p.ws + s * 9216 + ct0 * 256);
    const unsigned vu0 = umx_base(w * 8 + g) + c * 16, vu1 = umx_base(w * 8 + 4 + g) + c * 16;
    const char* pm = uni(p.ws + G_D_PM + (size_t)s * 16384 + w * 2048);
    constexpr int NR = DEPTH + 1 > 10 ? 10 : DEPTH + 1;
    u32x4 a[NR][NT], b[10][2];
    unsigned vb = voff, va = voff;
    TST(4096 + (s * 36 + ct0) * 4 + w, 0);
    const int f0 = (int)__hip_atomic_load((const gu32*)(p.ws + CTLO + (lane < 8 ? G_C_UM + (w * 8 + lane) * 128 : lane < 16 ? G_C_GV + ((s & (G_GVN - 1)) * 32 + w * 8 + lane - 8) * 128
                                                                                                                                : attn_flag(p, s, w * 2 + (lane & 1)) - CTLO)), RLX_AGENT);
    const bool early = !TAIL && !__any(f0 < s);
    if (!early) wait_word(p, p.ws + CTLO + (lane < 8 ? G_C_GV + ((s & (G_GVN - 1)) * 32 + w * 8 + lane) * 128 : G_C_UM + w * 8 * 128), lane < 8 ? s : s - 3, 21u);
#define ALOAD(q) do { if ((q) < 8) { _Pragma("unroll") for (int t = 0; t < NT; ++t) RLDA(a[(q) % NR][t], va, ub[t], 0);  \
                                     asm volatile("v_add_u32 %0, 0x400, %0" : "+v"(va)); }                                            \
                      else { _Pragma("unroll") for (int t = 0; t < NT; ++t) RLDA(a[(q) % NR][t], ((q) & 1) ? vu1 : vu0, um, t * 256); } } while (0)
    constexpr int D0 = DEPTH < 8 ? DEPTH : 8;
    asm volatile("s_nop 4");
#pragma unroll
    for (int q = 0; q < D0; ++q) ALOAD(q);
    asm volatile("s_cmp_lg_u32 %0, 0\n\ts_cbranch_scc1 1f\n\ts_waitcnt vmcnt(0)\n1:\n\ts_waitcnt vmcnt(30)" :: "s"(__builtin_amdgcn_readfirstlane((int)early)) : "scc", "memory");
    unsigned tries = 0;
    if (TAIL) {
        const char* sp = pm + (lane & 7) * 256 + ((lane >> 3) & 1) * 8192;
        const int dl = (FLG(p.prm[5]) >> 26) & 31;
        if (dl) {
            wait_word<true>(p, p.ws + CTLO + G_C_KM + ((ct0 >> 1) + w) * 128, s - 1, 25u);
            for (int i = 0; i < dl + (s < 63 ? 5 : 0); ++i) __builtin_amdgcn_s_sleep(2);
        } else
        while (__any(__hip_atomic_load((const gu32*)sp, RLX_AGENT) == 0xffffffffu) && !give_up(p, tries, 23u)) __builtin_amdgcn_s_sleep(1);
    } else if (!early) wait_word(p, p.ws + CTLO + (lane < 8 ? G_C_UM + (w * 8 + lane) * 128 : attn_flag(p, s, w * 2 + (lane & 1)) - CTLO), s, 22u);
    if (!early) TST(4096 + (s * 36 + ct0) * 4 + w, 1);
    for (;;) {
    asm volatile("s_nop 4");
#pragma unroll
    for (int q = 0; q < 8; ++q) { RLD(b[q][0], vb, pb, 0); RLD(b[q][1], vb, pb, 1024); asm volatile("v_add_u32 %0, 0x800, %0" : "+v"(vb)); }
#pragma unroll
    for (int q = 8; q < 10; ++q) { RLD(b[q][0], voff, pm, (q & 1) * 1024); RLD(b[q][1], voff, pm + 8192, (q & 1) * 1024); }
#pragma unroll
    for (int q = D0; q < NR; ++q) ALOAD(q);
#pragma unroll
    for (int q = 0; q < 10; ++q) {
        const int issuedA = (NR + q < 10 ? NR + q : 10) - D0;
        if (q < D0) VMW(2 * (9 - q) + issuedA * NT); else VMW((issuedA - (q - D0 + 1)) * NT);
#pragma unroll
        for (int t = 0; t < NT; ++t) { MFMA_ACC_A(acc[t][0], a[q % NR][t], b[q][0]); MFMA_ACC_A(acc[t][1], a[q % NR][t], b[q][1]); }
        if (NR + q < 10) ALOAD(NR + q);
    }
    asm volatile("s_nop 7" ::: "memory");
#pragma unroll
    for (int t = 0; t < NT; ++t) asm volatile("" : "+v"(acc[t][0]), "+v"(acc[t][1]));
    if (!TAIL) break;
    f32x4 chk = acc[0][0];
#pragma unroll
    for (int t = 0; t < NT; ++t) chk += acc[t][1] + acc[t][0];
    const float ck = (chk[0] + chk[1]) + (chk[2] + chk[3]);
    if (!__any(ck != ck)) break;
    unsigned mx = 0;
#pragma unroll
    for (int q = 0; q < 10; ++q) { asm volatile("" : "+v"(b[q][0]), "+v"(b[q][1])); mx = max(mx, max(umax4(b[q][0]), umax4(b[q][1]))); }
#pragma unroll
    for (int t = 0; t < NT; ++t) { asm volatile("" : "+a"(a[8 % NR][t]), "+a"(a[9 % NR][t])); mx = max(mx, max(umax4(a[8 % NR][t]), umax4(a[9 % NR][t]))); }
    if (!__any(mx == 0xffffffffu) || give_up(p, tries, 24u)) break;
    vb = voff;
#pragma unroll
    for (int t = 0; t < NT; ++t) { acc[t][0] = f32x4{0.f, 0.f, 0.f, 0.f}; acc[t][1] = acc[t][0]; asm volatile("" : "+v"(acc[t][0]), "+v"(acc[t][1])); }
    }
    TST(4096 + (s * 36 + ct0) * 4 + w, 2);
    f32x4* red = (f32x4*)smem;
#pragma unroll
    for (int t = 0; t < NT; ++t) { red[((w * NT + t) * 2 + 0) * 64 + lane] = acc[t][0]; red[((w * NT + t) * 2 + 1) * 64 + lane] = acc[t][1]; }
    __syncthreads();
    u16* orow = p.out + (size_t)(32 * s + 16 * (w & 1) + c) * OUTW + 16 * ct0 + 4 * g;
#pragma unroll
    for (int t = 0; t < NT; ++t)
        if ((t & 1) == (w >> 1)) {
            const int m = w & 1;
            const f32x4 v = (red[((0 * NT + t) * 2 + m) * 64 + lane] + red[((1 * NT + t) * 2 + m) * 64 + lane]) +
                            (red[((2 * NT + t) * 2 + m) * 64 + lane] + red[((3 * NT + t) * 2 + m) * 64 + lane]);
            *(u32x2*)(orow + 16 * t) = u32x2{pk2bf(v[0], v[1]), pk2bf(v[2], v[3])};
        }
}

static constexpr int KROW = G_KROW;
static constexpr int KARR = 80 * KROW;
static constexpr int UCS = 2208;
struct BCh {
    u16 uc[4][UCS];
    f32x4 cst[NST];
    u16 Kh[KARR + 256];
    u16 ub[SL];
};
static_assert(4 * sizeof(BCh) <= LDS_TOTAL - 32, "LDS");
__device__ __forceinline__ unsigned wave_umax(unsigned m) {
#pragma unroll
    for (int o = 32; o; o >>= 1) m = max(m, (unsigned)__shfl_xor((int)m, o));
    return m;
}
__device__ __forceinline__ unsigned scale_exp(unsigned absbits) { return min(max(absbits >> 23, 15u), 254u); }
__device__ __forceinline__ unsigned pk2h(float a, float b) { unsigned r; asm("v_cvt_pk_f16_f32 %0, %1, %2" : "=v"(r) : "v"(a), "v"(b)); return r; }
__device__ __forceinline__ unsigned prm(unsigned hi, unsigned lo, unsigned sel) { return __builtin_amdgcn_perm(hi, lo, sel); }
#define BST(i)
#define BST2(i)
__device__ __forceinline__ void task_ut(const MainP& p, char* smem, int rb, int w, int lane) {
    const int tid = w * 64 + lane, g = lane >> 4, c = lane & 15;
    const u32x4* src = (const u32x4*)(p.U + (size_t)rb * 64 * DM);
    u32x4 v[18];
#pragma unroll
    for (int i = 0; i < 18; ++i) {
        v[i] = src[tid + 256 * i];
    }
    BST2(0);
#pragma unroll
    for (int i = 0; i < 18; ++i) { const int ch = tid + 256 * i, row = ch / 72; *(u32x4*)(smem + row * 1184 + (ch - 72 * row) * 16) = v[i]; }
    __syncthreads();
    const unsigned ad = (unsigned)(uintptr_t)smem + (16 * g + (c >> 2)) * 1184 + (w * 144 + 4 * (c & 3)) * 2;
#pragma unroll
    for (int i = 0; i < 9; ++i) {
        u32x2 x0, x1, x2, x3;
        asm volatile("ds_read_b64_tr_b16 %0, %4 offset:%5\n\tds_read_b64_tr_b16 %1, %4 offset:%6\n\tds_read_b64_tr_b16 %2, %4 offset:%7\n\tds_read_b64_tr_b16 %3, %4 offset:%8\n\ts_waitcnt lgkmcnt(0)"
                     : "=&v"(x0), "=&v"(x1), "=&v"(x2), "=&v"(x3) : "v"(ad), "i"(i * 32), "i"(4 * 1184 + i * 32), "i"(8 * 1184 + i * 32), "i"(12 * 1184 + i * 32) : "memory");
        v[2 * i] = u32x4{x0[0], x0[1], x1[0], x1[1]}; v[2 * i + 1] = u32x4{x2[0], x2[1], x3[0], x3[1]};
    }
    __syncthreads();
    BST2(1);
    char* tw = smem + w * (144 * 144);
#pragma unroll
    for (int i = 0; i < 9; ++i) { *(u32x4*)(tw + (16 * i + c) * 144 + g * 32) = v[2 * i]; *(u32x4*)(tw + (16 * i + c) * 144 + g * 32 + 16) = v[2 * i + 1]; }
    const char* dst = uni(p.ws + G_D_UT + (size_t)rb * 73728 + w * 18432);
#pragma unroll
    for (int i = 0; i < 18; ++i) {
        const u32x4 o = *(const u32x4*)(tw + (8 * i + (lane >> 3)) * 144 + (lane & 7) * 16);
        const unsigned vi = lane * 16 + i * 1024;
        WST16(vi, o, dst, 0);
    }
    BST2(2);
    const u32x4 sent = {0xffffffffu, 0xffffffffu, 0xffffffffu, 0xffffffffu};
#pragma unroll 1
    for (int sg = NG1; sg < 64; ++sg) {
        const unsigned ov = umx_base(rb) + sg * 9216 + tid * 16, ok = G_D_KM + rb * G_HS4 + skew(sg, rb) * 4096 + tid * 16;
        const unsigned op = tid < 128 ? G_D_PX + (sg * 32 + rb) * 2048 + tid * 16
                          : tid < 192 ? ov + 6144 : G_D_PM + sg * 16384 + ((tid >> 4) & 1) * 8192 + rb * 256 + (tid & 15) * 16;
        asm volatile("global_store_dwordx4 %0, %4, %5 sc1\n\tglobal_store_dwordx4 %1, %4, %5 sc1\n\tglobal_store_dwordx4 %2, %4, %5 sc1\n\t"
                     "global_store_dwordx4 %3, %4, %5 sc1\n\ts_nop 1" :: "v"(ov), "v"(ov + 4096), "v"(ok), "v"(op), "v"(sent), "s"(p.ws) : "memory");
    }
    BST2(3);
    VMC0();
    BST2(4);
    __syncthreads();
    if (tid == 0) __hip_atomic_store((gu32*)(p.ws + CTLO + G_C_UT + rb * 128), 1u, RLX_AGENT);
}
struct KPar { u16 ldt, pa[2][4]; };
__device__ __forceinline__ KPar kgen_par(const MainP& p, int d, int lane) {
    KPar k;
    k.ldt = p.logdt[d];
#pragma unroll
    for (int q = 0; q < 2; ++q) { const int i = d * NST + lane + 64 * q; k.pa[q][0] = p.logA[i]; k.pa[q][1] = p.Aim[i]; k.pa[q][2] = p.Cre[i]; k.pa[q][3] = p.Cim[i]; }
    return k;
}
struct Pull { const char* a; const char* b; const char* c; unsigned at; };
__device__ __forceinline__ void pull_inputs(Pull& q, int w, bool now) {
    if (q.a && (now || (int)((unsigned)__builtin_amdgcn_s_memrealtime() - q.at) >= 0)) {
#pragma unroll
        for (int i = 0; i < 3; ++i)
            if (i < 2 || w == 0) asm volatile("s_mov_b32 m0, %3\n\ts_nop 0\n\tglobal_load_lds_dword %0, %1\n\tglobal_load_lds_dword %0, %2" :: "v"(threadIdx.x * 128 + i * 32768), "s"(q.a), "s"(q.b), "s"(155648) : "memory");
        asm volatile("global_load_lds_dword %0, %1" :: "v"(min(threadIdx.x, 143u) * 128), "s"(q.c) : "memory");
        q.a = nullptr;
    }
}
__device__ __forceinline__ void task_kgen(const MainP& p, char* smem, int d, int w, int lane, const KPar& kp, bool first, unsigned ep, Pull& pl) {
    BCh& L = reinterpret_cast<BCh*>(smem)[w];
    const int g = lane >> 4, c = lane & 15, d0 = d;
    const int tid = (d & 3) * 64 + lane;
    unsigned* Kd = (unsigned*)L.uc;
    unsigned ek;
    BST(10);
    const auto& pa = kp.pa;
    const float dt = bfr(expf(bf2f(kp.ldt)));
#pragma unroll
    for (int q = 0; q < 2; ++q) {
        const float ar = -bfr(expf(bf2f(pa[q][0])));
        const float cr = bf2f(pa[q][2]) * dt, ci = bf2f(pa[q][3]) * dt;
        L.cst[lane + 64 * q] = f32x4{ar * dt * 1.4426950408889634f, bf2f(pa[q][1]) * dt * 0.15915494309189535f,
                                     __log2f(hypotf(cr, ci)), atan2f(ci, cr) * 0.15915494309189535f};
    }
    BST(11);
    {
        f32x4 acc[3];
#pragma unroll
        for (int v = 0; v < 3; ++v) acc[v] = f32x4{0.f, 0.f, 0.f, 0.f};
        auto base_of = [](int v, int r) { return v == 0 ? (r & 7) + 128 * (r >> 3) : v == 1 ? (r < 8 ? 256 + 2 * (r & 3) + 128 * (r >> 2) : 512 + 4 * (r & 1) + 128 * ((r - 8) >> 1))
                                                                                           : 1024 + 128 * min(r, 8); };
        const float b0 = (float)base_of(0, c), b1 = (float)base_of(1, c), b2 = (float)base_of(2, c), off = (float)(8 * c);
#define KG_POLAR(h_, l_, lg_, f_, a_, b_) do { const float m_ = __builtin_amdgcn_exp2f(fmaf(cc[0], b_, lg_)), p_ = __builtin_amdgcn_fractf(fmaf(a_, b_, f_)); \
            const float re_ = m_ * __builtin_amdgcn_cosf(p_), im_ = m_ * __builtin_amdgcn_sinf(p_);                                                            \
            h_ = pk2bf(re_, im_); l_ = pk2bf(re_ - lo2f(h_), im_ - hi2f(h_)); } while (0)
#pragma unroll 1
        for (int t = 0; t < NST / 16; ++t) {
            u32x4 fh, fl, ah[3], al[3];
            pull_inputs(pl, w, false);
#pragma unroll
            for (int j = 0; j < 4; ++j) {
                const f32x4 cc = L.cst[16 * t + 4 * g + j];
                KG_POLAR(fh[j], fl[j], 0.f, 0.f, -cc[1], off);
                KG_POLAR(ah[0][j], al[0][j], cc[2], cc[3], cc[1], b0); KG_POLAR(ah[1][j], al[1][j], cc[2], cc[3], cc[1], b1); KG_POLAR(ah[2][j], al[2][j], cc[2], cc[3], cc[1], b2);
            }
#pragma unroll
            for (int v = 0; v < 3; ++v) { acc[v] = MFMA32(al[v], fh, acc[v]); acc[v] = MFMA32(ah[v], fl, acc[v]); acc[v] = MFMA32(ah[v], fh, acc[v]); }
        }
        BST(12);
        unsigned mx = 0;
#pragma unroll
        for (int v = 0; v < 3; ++v)
#pragma unroll
            for (int i = 0; i < 4; ++i)
                if (base_of(v, 4 * g + i) + 8 * c <= 2048) mx = max(mx, __float_as_uint(acc[v][i]) & 0x7fffffffu);
        ek = scale_exp(wave_umax(mx));
        const float sk = __uint_as_float((268u - ek) << 23);
#pragma unroll
        for (int v = 0; v < 3; ++v)
#pragma unroll
            for (int i = 0; i < 4; ++i) {
                const int pos = base_of(v, 4 * g + i) + 8 * c;
                if (pos <= 2048) Kd[pos] = pk2h(acc[v][i] * sk, 0.f);
            }
    }
    char* kt = p.ws + G_D_KT + (size_t)d * 8192;
    char* kt2 = kt + 4096;
    const unsigned voff = lane * 16;
#pragma unroll
    for (int q = 0; q < 4; ++q) {
        const int e = lane + 64 * q;
        unsigned t[8], k[8];
        const unsigned k0 = Kd[8 * e], k8 = Kd[8 * e + 8];
        if (q == 0) {
            const u32x4 a = *(const u32x4*)&Kd[8 * e], b = *(const u32x4*)&Kd[8 * e + 4];
            const bool two = lane >= 32;
            t[0] = a[0]; t[1] = two ? a[0] : a[1]; t[2] = a[2]; t[3] = two ? b[0] : a[3]; t[4] = b[0]; t[5] = two ? b[0] : b[1]; t[6] = b[2]; t[7] = two ? k8 : b[3];
        } else if (q == 1) {
            const unsigned k4 = Kd[8 * e + 4];
            t[0] = t[1] = t[2] = k0; t[3] = t[4] = t[5] = k4; t[6] = t[7] = k8;
        } else {
            t[0] = t[1] = t[2] = t[3] = k0; t[4] = (lane & 1) ? k8 : k0; t[5] = t[6] = t[7] = k8;
        }
#pragma unroll
        for (int j = 0; j < 8; ++j) k[j] = t[7 - j];
        u32x4 kh;
#pragma unroll
        for (int j = 0; j < 4; ++j) kh[j] = prm(k[2 * j + 1], k[2 * j], 0x05040100u);
        WST16(voff, kh, kt, q * 1024);
    }
    if (lane < 8) { const u32x4 e4 = {ek, ek, ek, ek}; WST16(voff, e4, kt2, 0); }
    BST(13);
    VMC0();
    BST(14);
    if (first) init_seen(p.ws, lane, ep);
    BST(15);
    if (lane == 0) __hip_atomic_fetch_add((gu32*)(p.ws + G_WS_CTL + G_C_KG + (d >> 2) * 128), 1u, RLX_AGENT);
}
__device__ __forceinline__ void task_s4d(const MainP& p, char* smem, int d0, int w, int lane, int& nb, bool more) {
    BCh* LL = reinterpret_cast<BCh*>(smem);
    BCh& L = LL[w];
    const int tid = w * 64 + lane, g = lane >> 4, c = lane & 15, d = d0 + w;
    BST(0);
    wait_word(p, p.ws + (lane < 32 ? CTLO + G_C_UT + lane * 128 : G_WS_CTL + G_C_KG + (d0 >> 2) * 128), lane < 32 ? 1 : 3, 30u);
    const float dsk = bf2f(p.Dskip[d]);
    const char* kt = p.ws + G_D_KT + (size_t)d * 8192;
    const char* kt2 = kt + 4096;
    const char* ut = p.ws + G_D_UT + d * 128;
    const unsigned voff = lane * 16;
    u32x4 kh[4], uu[4];
    unsigned ek;
#pragma unroll
    for (int q = 0; q < 4; ++q) { const unsigned vu = (8 * q + (lane >> 3)) * 73728 + (lane & 7) * 16; WLD16(uu[q], vu, ut, 0); }
    WLD16X4(kh[0], kh[1], kh[2], kh[3], voff, kt);
    asm volatile("global_load_dword %0, %1, %2 sc1" : "=v"(ek) : "v"(0u), "s"(kt2) : "memory");
    const u32x4 z4 = {0u, 0u, 0u, 0u};
    *(u32x4*)&L.Kh[8 * lane] = z4;
    *(u32x4*)&L.Kh[512 + 8 * (lane & 31)] = z4;
    *(u32x4*)&L.uc[0][64 + SL] = z4;
    *(u32x4*)&L.uc[(lane >> 3) & 3][8 * (lane & 7)] = z4;
    asm volatile("s_waitcnt vmcnt(0)" : "+v"(kh[0]), "+v"(kh[1]), "+v"(kh[2]), "+v"(kh[3]), "+v"(ek),
                 "+v"(uu[0]), "+v"(uu[1]), "+v"(uu[2]), "+v"(uu[3]) :: "memory");
    BST(1);
    unsigned mu = 0;
#pragma unroll
    for (int q = 0; q < 4; ++q)
#pragma unroll
        for (int j = 0; j < 4; ++j) mu = max(mu, max(uu[q][j] & 0x7fff0000u, (uu[q][j] << 16) & 0x7fff0000u));
    const unsigned eu = scale_exp(wave_umax(mu));
    const float su = __uint_as_float((268u - eu) << 23), iu = __uint_as_float((eu - 14u) << 23), ik = __uint_as_float((ek - 13u) << 23);
#pragma unroll
    for (int q = 0; q < 4; ++q) {
        const int e = lane + 64 * q, at = (16 + (e >> 2)) * KROW + 24 - 8 * (e & 3);
        u32x4 h;
#pragma unroll
        for (int j = 0; j < 4; ++j) h[j] = pk2h(lo2f(uu[q][j]) * su, hi2f(uu[q][j]) * su);
        *(u32x4*)&L.uc[0][64 + 8 * e] = h;
        *(u32x4*)&L.ub[8 * e] = uu[q];
        *(u32x4*)&L.Kh[at] = kh[q];
    }
    BST(2);
    BST(3);
#pragma unroll
    for (int q = 0; q < 4; ++q) {
        const int e = lane + 64 * q;
        const int ue = 56 + 8 * e;
        const u32x4 a = *(const u32x4*)&L.uc[0][ue];
        const u32x2 b = *(const u32x2*)&L.uc[0][ue + 8];
        const unsigned s0 = __builtin_amdgcn_alignbit(a[1], a[0], 16), s1 = __builtin_amdgcn_alignbit(a[2], a[1], 16), s2 = __builtin_amdgcn_alignbit(a[3], a[2], 16),
                       s3 = __builtin_amdgcn_alignbit(b[0], a[3], 16), s4 = __builtin_amdgcn_alignbit(b[1], b[0], 16);
        *(u32x4*)&L.uc[1][ue] = u32x4{s0, s1, s2, s3};
        *(u32x4*)&L.uc[2][ue] = u32x4{a[1], a[2], a[3], b[0]};
        *(u32x4*)&L.uc[3][ue] = u32x4{s1, s2, s3, s4};
    }
    if (lane == 0) {
        const u32x4 a = *(const u32x4*)&L.uc[0][56 + SL];
        const unsigned s0 = __builtin_amdgcn_alignbit(a[1], a[0], 16), s1 = __builtin_amdgcn_alignbit(a[2], a[1], 16), s2 = __builtin_amdgcn_alignbit(a[3], a[2], 16), s3 = a[3] >> 16;
        *(u32x4*)&L.uc[1][56 + SL] = u32x4{s0, s1, s2, s3};
        *(u32x4*)&L.uc[2][56 + SL] = u32x4{a[1], a[2], a[3], 0u};
        *(u32x4*)&L.uc[3][56 + SL] = u32x4{s1, s2, s3, 0u};
    }
    BST(4);
    {
        const int o = 33 + c + 8 * g;
        const unsigned ua = (unsigned)(uintptr_t)&L.uc[o & 3][o & ~3], ka = (unsigned)(uintptr_t)&L.Kh[(16 + c) * KROW + 8 * g];
        const f32x4* res = (const f32x4*)L.Kh + lane;
        asm volatile(ASM_CV :: "v"(ua), "v"(ka), "v"((unsigned)(uintptr_t)res) : CV_CLOB);
        BST(5);
#pragma unroll
        for (int par = 0; par < 2; ++par)
#pragma unroll
            for (int t = 0; t < 4; ++t) {
                const int n = 16 * (par + 2 * (16 * t + c)) + 4 * g;
                const u32x2 uu = *(const u32x2*)&L.ub[n];
                const f32x4 y4 = res[(par * 4 + t) * 64] * fminf(ik, iu) * fmaxf(ik, iu);
                const float r0 = bfr(y4[0]) + bfr(lo2f(uu[0]) * dsk), r1 = bfr(y4[1]) + bfr(hi2f(uu[0]) * dsk);
                const float r2 = bfr(y4[2]) + bfr(lo2f(uu[1]) * dsk), r3 = bfr(y4[3]) + bfr(hi2f(uu[1]) * dsk);
                *(u32x2*)&L.uc[1][n] = u32x2{pk2bf(r0, r1), pk2bf(r2, r3)};
            }
    }
    __syncthreads();
    BST(6);
    u32x4 y[4];
#pragma unroll
    for (int ch = 0; ch < 4; ++ch) y[ch] = *(const u32x4*)&LL[ch].uc[1][8 * tid];
    const unsigned vt = tid * 16;
    char* yb = p.ws + G_D_YB + (size_t)(d0 >> 2) * 16384;
#pragma unroll
    for (int i = 0; i < 4; ++i) {
        const u32x4 o = {prm(y[1][i], y[0][i], 0x05040100u), prm(y[3][i], y[2][i], 0x05040100u), prm(y[1][i], y[0][i], 0x07060302u), prm(y[3][i], y[2][i], 0x07060302u)};
        char* yi = yb + i * 4096;
        WST16(vt, o, yi, 0);
    }
    BST(7);
    if (tid == 0 && more) nb = (int)(__hip_atomic_fetch_add((gu32*)(p.ws + CTLO + G_C_TASK + 128), 1u, RLX_AGENT) + 1u);
    VMC0();
    __syncthreads();
    if (tid == 0) __hip_atomic_fetch_add((gu32*)(p.ws + CTLO + G_C_YC + (d0 >> 6) * 128), 1u, RLX_AGENT);
    BST(8);
}
__device__ __forceinline__ void task_merge(const MainP& p, char* smem, int d0, int i, int w, int lane) {
    const unsigned vt = (w * 64 + lane) * 16;
    wait_word(p, p.ws + CTLO + G_C_YC + (d0 >> 6) * 128, 15, 31u);
    {
        const char* sb = p.ws + G_D_YB + (size_t)(d0 >> 6) * 262144 + i * 4096;
        u32x4 z[16];
#pragma unroll
        for (int j = 0; j < 16; ++j) { const char* sj = sb + j * 16384; WLD16(z[j], vt, sj, 0); }
        asm volatile("s_waitcnt vmcnt(0)" : "+v"(z[0]), "+v"(z[1]), "+v"(z[2]), "+v"(z[3]), "+v"(z[4]), "+v"(z[5]), "+v"(z[6]), "+v"(z[7]),
                     "+v"(z[8]), "+v"(z[9]), "+v"(z[10]), "+v"(z[11]), "+v"(z[12]), "+v"(z[13]), "+v"(z[14]), "+v"(z[15]) :: "memory");
        char* tw = smem + w * 18432;
#pragma unroll
        for (int j = 0; j < 8; ++j) {
            *(u32x4*)(tw + lane * 144 + j * 16) = u32x4{z[2 * j][0], z[2 * j][1], z[2 * j + 1][0], z[2 * j + 1][1]};
            *(u32x4*)(tw + (64 + lane) * 144 + j * 16) = u32x4{z[2 * j][2], z[2 * j][3], z[2 * j + 1][2], z[2 * j + 1][3]};
        }
#pragma unroll
        for (int k = 0; k < 16; ++k) {
            const int q = 8 * k + (lane >> 3);
            const int row = 8 * (64 * w + (q & 63)) + 2 * i + (q >> 6);
            *(u32x4*)(p.out + (size_t)row * OUTW + DM + (d0 & ~63) + 8 * (lane & 7)) = *(const u32x4*)(tw + q * 144 + (lane & 7) * 16);
        }
    }
}

static constexpr int NKG = 144, E_B = NKG + 36;
static constexpr int Q1 = 0, Q2 = Q1 + (NFINE - 2) * 25;
static constexpr int NTASK = E_B + Q2 + 68;

__device__ __forceinline__ void worker_body(const MainP& p, char* smem, const int w, const int lane, int quota) {
    const int tid = w * 64 + lane;
    int* slot = (int*)(smem + LDS_TOTAL - 16);
    int nb = -1;
    int nst = 0;
    gu32* qa = (gu32*)(p.ws + CTLO + G_C_TASK);
    for (;;) {
        if (tid == 0) {
            int t = E_B;
            if (quota > 0) {
                const int k = nb >= 0 ? nb : (int)(__hip_atomic_fetch_add(qa + 32, 1u, RLX_AGENT) + 1u);
                if (k < E_B) t = k;
                nb = -1;
            }
            if (t >= E_B) t = E_B + (int)(__hip_atomic_fetch_add(qa, 1u, RLX_AGENT) + 1u);
            *slot = t;
        }
        __syncthreads();
        const int t = __builtin_amdgcn_readfirstlane(*slot);
        if (t >= NTASK) break;
        quota = t < E_B ? quota - 1 : 0;
        if (t < E_B) {
            if (t < 128 || (t >= 144 && t < 160)) task_s4d(p, smem, 4 * (t < 128 ? t : t - 16), w, lane, nb, quota > 0);
            else task_merge(p, smem, 64 * ((t < 144 ? t - 128 : t - 144) >> 2), t & 3, w, lane);
        } else {
            const int u = t - E_B;
            const bool last2 = u >= Q2;
            const int sg = last2 ? 62 + (u - Q2) / 34 : NG1 + (u - Q1) / 25, r = last2 ? (u - Q2) % 34 : (u - Q1) % 25;
            if (r < 16) task_attn_tail(p, sg, 2 * r + (w >> 1), w, lane);
            else if (last2) task_out<2, 9, true>(p, smem, sg, 2 * (r - 16), w, lane);
            else task_out<4, 9, true>(p, smem, sg, 4 * (r - 16), w, lane);
            if (last2) break;
        }
        __syncthreads();
    }
}

__host__ __device__ constexpr unsigned phys(unsigned x) { return (x >> 13) * 16384 + (x & 8191); }
__host__ __device__ constexpr unsigned aMB(int f, int b) { return phys(f < 8 ? 16384 + f * 1024 + b : 28672 + b); }
__host__ __device__ constexpr unsigned aPX(int pr, int b) { return phys(pr * 1024 + b); }
__host__ __device__ constexpr unsigned aPM(int u, int b) { return phys(24576 + u * 1024 + b); }
#define IST(k)

static constexpr int L_RW = 160000;
__device__ __forceinline__ int lane_id() { int l; asm volatile("v_mbcnt_lo_u32_b32 %0, -1, 0\n\tv_mbcnt_hi_u32_b32 %0, -1, %0" : "=v"(l)); return l; }
typedef const __attribute__((address_space(4))) MainP* KArg;

__global__ __launch_bounds__(256, 1) void main_kernel(MainP p_) {
    __shared__ __attribute__((aligned(16))) char smem[LDS_TOTAL];
    KArg ka = (KArg)__builtin_amdgcn_kernarg_segment_ptr();
    static_assert(offsetof(MainP, W) == 0 && offsetof(MainP, ws) == 0x20 && offsetof(MainP, X) == 0x28 && offsetof(MainP, prm) == 0x40, "argument layout");
    asm volatile(ASM_EA :: "s"(ka), "s"(blockIdx.x), "v"(threadIdx.x) : ASM_CLOB);
    asm volatile("" : "+s"(ka));
    const unsigned t00 = (unsigned)__builtin_amdgcn_s_memrealtime();
    const int b = blockIdx.x, x = b & 7, ix = b >> 3;
    const int w = __builtin_amdgcn_readfirstlane(threadIdx.x >> 6);
    const bool isr = x < 4 && ix >= 16;
    if (x < 4 && ix >= 29) init_cal(ka->ws, smem, x * 3 + ix - 29, threadIdx.x, (FLG(ka->prm[5]) >> 16) & 7);
    if (x >= 4 ? ix >= 24 : (ix >= 25 && ix < 29)) init_fill(ka->ws, ka->mem0, x >= 4 ? (ix - 24) * 4 + (x & 3) : 32 + (ix - 25) * 4 + x, threadIdx.x);
    IST(0);
    const int NS = (FLG(ka->prm[5]) >> 8) & 3, NF = (FLG(ka->prm[5]) >> 21) & 7, RK = (FLG(ka->prm[5]) >> 10) & 3;
    const int NSP = G_RARM ? 7 : FLG2(ka->prm[7]) & 7;
    const int NA1 = 18 + 4 * NSP, NB1 = NA1 - 3 * max(NSP - 4, 0), N1 = RK ? NA1 + (RK - 1) * NB1 : 0, N2 = N1 + (28 - 4 * NSP) * NS, NKE = min(N2 + max(24 * NF - 8, 0), NKG);
    if (ix >= (x < 4 ? 16 : 24)) {
        MainP p;
        p.ws = ka->ws; p.logA = ka->logA; p.Aim = ka->Aim; p.Cre = ka->Cre; p.Cim = ka->Cim; p.logdt = ka->logdt;
        Pull pl = {nullptr, nullptr, nullptr, t00 + 400 + 50 * ((FLG(ka->prm[5]) >> 4) & 15)};
        const bool own = x < 4 && ix < 25 + NSP;
        int d0, nr, dd;
        if (own) {
            const int jt = ix < 25 ? ix - 16 : ix - 25, solo = jt < NSP;
            const int k = ix >= 25 ? 18 + NSP + 3 * jt + w - 1 : solo ? 3 * jt + w - 1 : NSP + 2 * jt + w - 2;
            d0 = k < NB1 ? x * NB1 + k : 4 * NB1 * RK + x * (NA1 - NB1) + k - NB1; nr = w < 2 - solo ? 0 : k < NB1 ? RK : min(RK, 1); dd = 4 * NB1;
            if (threadIdx.x < 8) ((unsigned*)(smem + L_RW))[threadIdx.x] = 0xffffffffu;
        } else if (x < 4) { d0 = 4 * (N1 + (ix - 25 - NSP) * 4 + x) + w; nr = NS; dd = 4 * (28 - 4 * NSP); }
        else {
            d0 = 4 * (N2 + (ix - 24) * 4 + (x & 3)) + w; nr = ix < 28 ? NF : ix < 30 ? max(NF - 1, 0) : 0; dd = 96;
            const size_t o = (size_t)((ix - 24) * 4 + (x & 3)) * 73728; pl.a = (const char*)ka->X + o; pl.b = (const char*)ka->U + o;
            pl.c = (const char*)((x & 2) ? ((x & 1) ? ka->Cim : ka->Cre) : ((x & 1) ? ka->Aim : ka->logA)) + (ix - 24) * 18432;
        }
        while (nr > 0 && d0 + dd * (nr - 1) >= 4 * NKG) --nr;
        KPar kn;
        if (nr > 0) kn = kgen_par(p, d0, threadIdx.x & 63);
        __builtin_amdgcn_sched_barrier(0);
        const unsigned ep = __hip_atomic_load((const gu32*)(p.ws + G_WS_EP), RLX_AGENT);
        asm volatile("s_waitcnt vmcnt(0)" :: "v"(ep) : "memory");
        __syncthreads();
        if (x >= 4 && ix == 24) {
            const char* xs = (const char*)ka->X + threadIdx.x * 128;
#pragma unroll
            for (int i = 0; i < 7; ++i) asm volatile("s_mov_b32 m0, %1\n\ts_nop 0\n\tglobal_load_lds_dword %0, off" :: "v"(xs + i * 32768), "s"(155648) : "memory");
        }
        {
            const int q = x < 4 ? ix - 25 : ix - 26;
            if (q >= 0 && q < 4 && (FLG(ka->prm[5]) & 1)) {
                const int hs = (x & 3) * 8 + 4 * (q & 1) + ((1 - (int)((u64)ka->W[q & 2] >> 8)) & 3);
                const char* src = (q & 2) ? (const char*)ka->W[3] + (size_t)hs * 147456 + threadIdx.x * 128 : (const char*)ka->W[x < 4] + hs * 256 + (threadIdx.x >> 1) * 8192 + (threadIdx.x & 1) * 128;
                const unsigned st = (q & 2) ? 32768u : 1048576u;
#pragma unroll
                for (int i = 0; i < 5; ++i)
                    if (i < 4 || w < 2) {
                        if (FLG(ka->prm[5]) & 4) asm volatile("s_mov_b32 m0, %1\n\ts_nop 0\n\tglobal_load_lds_dword %0, off nt" :: "v"(src + (size_t)i * st), "s"(155648) : "memory");
                        else asm volatile("s_mov_b32 m0, %1\n\ts_nop 0\n\tglobal_load_lds_dword %0, off" :: "v"(src + (size_t)i * st), "s"(155648) : "memory");
                    }
            }
        }
        if (nr == 0) init_seen(p.ws, threadIdx.x & 63, ep);
#pragma unroll 1
        for (int r = 0; r < nr; ++r) {
            const KPar kp = kn;
            kn = kgen_par(p, d0 + dd * min(r + 1, nr - 1), threadIdx.x & 63);
            __builtin_amdgcn_sched_barrier(0);
            task_kgen(p, smem, d0 + dd * r, w, threadIdx.x & 63, kp, r == 0, ep, pl);
        }
        if (w == 0) init_count(p.ws, threadIdx.x & 63);
        while (pl.a) { pull_inputs(pl, w, false); __builtin_amdgcn_s_sleep(8); }
        if (x < 4 && !own) return;
        asm volatile("" : "+s"(ka));
    }
    if (isr) {
        const int lane = threadIdx.x & 63, g = lane >> 4, c = lane & 15;
        const int jt = ix < 25 ? ix - 16 : ix - 25, solo = jt < NSP, par = solo ? ix >= 25 : w;
        const unsigned lb = w * G_STG;
        unsigned* ct = (unsigned*)(smem + lb) + lane;
        asm volatile("" :: "v"(ct) : "memory");
#define CTW(k, val) ct[(k) * 64] = (unsigned)(val)
        const u64 ws = (u64)ka->ws, prm = (u64)ka + 0x40;
        const u64 stb = 0;
        IST(1);
        CTW(0, lane * 16);
        const int j = x * 9 + jt;
        const int rot = (jt * 16) / 9;
        if (w >= 2 - solo) return;
        CTW(1, lane * 16 + 4096);
        CTW(2, aMB(j >> 2, (j & 3) * 256 + (4 * g + 2 * ((c >> 1) & 1) + (c & 1)) * 16 + (c >> 2) * 4));
        for (int k = 0; k < (int)G_NARM; ++k) {
            constexpr int N0 = G_HKB, N1 = N0 + (9 * G_RM + 3) / 4, N2 = N1 + 5 * G_RP;
            constexpr int NO = G_RARM ? 7 : 9;
            static_assert(N2 <= NO * G_NARM && G_NARM <= 5, "shares");
            const int q = min(jt + NO * k, N2 - 1), qm = min(x * (N1 - N0) + q - N0, (int)(9 * G_RM) - 1), qp = x * 5 * G_RP + q - N1;
            const int cp = q < N0 ? 4 + x : q < N1 ? qm / 9 : qp / 20, kb = q < N0 ? q : q < N1 ? (qm % 9 == 8 ? 28 : 16 + qm % 9) : (qp % 20 < 16 ? qp % 20 : 8 + qp % 20);
            CTW(47 + k, cp * G_CPY + phys(kb * 1024 + lane * 16));
        }
        for (int k = 0; k < 3; ++k) { const int q = min(jt + 7 * k, 15); CTW(52 + k, (x * 9 + 7 + (q >> 3)) * 16384 + (q & 7) * 1024 + lane * 16); }
        const unsigned pc = x * G_RP / 4 * G_CPY;
        CTW(3, (lane & 3) * G_CPY + G_HOFF + G_RHO + ((lane >> 2) & 1) * G_RHS + j * 32); CTW(46, x * G_CPY + G_HOFF + G_RHO + min(lane, 35) * 32);
        CTW(4, (c & 2) ? 0xEEEE : 0x4444); CTW(5, (c & 1) ? 0x03020706u : 0x05040100u);
        CTW(6, G_GVC ? G_WS_CTL + G_C_GV + G_GVH + x * 2048 + (par * 32 + (lane & 31)) * 32 : G_WS_CTL + G_C_GV + (par * 32 + (lane & 31)) * 128); CTW(7, x * G_CPY + G_HOFF + (lane & 31) * G_HP); CTW(8, lane * 4); CTW(9, L_RW);
        for (int k = 0; k < 16; ++k) { const int i = (k + rot) & 15; CTW(10 + k, pc + aPX(i, lane * 16)); CTW(26 + k, i * 2048 + lane * 16); }
        for (int k = 0; k < 4; ++k) CTW(42 + k, pc + aPM(k, lane * 16));
        IST(2);
        asm volatile(ASM_R :: "s"(ws), "s"(ws), "s"(ws), "s"(__builtin_amdgcn_readfirstlane(par)), "s"(__builtin_amdgcn_readfirstlane(j)), "s"((u64)__builtin_amdgcn_readfirstlane(2 * rot) + ((u64)__builtin_amdgcn_readfirstlane(solo) << 32)), "s"(prm), "s"(lb), "s"(stb) : ASM_CLOB);
    }
    asm volatile("" : "+s"(ka));
    MainP p;
    p.prm[5] = ka->prm[5];
    p.U = ka->U; p.ws = ka->ws; p.logA = ka->logA; p.Aim = ka->Aim; p.Cre = ka->Cre; p.Cim = ka->Cim; p.logdt = ka->logdt; p.Dskip = ka->Dskip; p.out = ka->out;
    {
        auto cal = [&](int k) { return __hip_atomic_load((const gu32*)(p.ws + G_WS_DONE + k * 64 + 4), RLX_AGENT); };
        p.prm[0] = __builtin_amdgcn_readfirstlane((min(cal(5), cal(11)) < min(cal(4), cal(10)) ? 8192 : 0) ^ G_CTF);
    }
    const bool s4 = ix < 22 - ((FLG(p.prm[5]) >> 12) & 15);
    if (s4 && ix >= 8 && ix < 24) for (int i = 0; i < (int)(((ix & 7) * 4 + (x & 3)) * ((FLG(p.prm[5]) >> 24) & 3)); ++i) __builtin_amdgcn_s_sleep(3);
    if (x >= 4 && ix >= 8 && ix < 16) {
#pragma unroll 1
        for (int q = NKE + (ix - 8) * 4 + (x & 3); q < NKG; q += 32) {
            Pull pl = {nullptr, nullptr, nullptr, 0u};
            task_kgen(p, smem, 4 * q + w, w, lane_id(), kgen_par(p, 4 * q + w, lane_id()), false, 0u, pl);
        }
    }
    if (ix >= 24 && ix < 30) {
#pragma unroll 1
        for (int rb = (ix - 24) * 4 + (x & 3); rb < 32; rb += 8) {
            __syncthreads();
            task_ut(p, smem, rb, w, lane_id());
            if (ix < 28) break;
        }
        __syncthreads();
        const int lane = lane_id(), g = lane >> 4, c = lane & 15, ct0 = 6 * (ix - 24), s0 = x & 3;
        const unsigned lb = w * G_STG;
        unsigned* ct = (unsigned*)(smem + lb) + lane;
        asm volatile("" :: "v"(ct) : "memory");
        for (int t = 0; t < 6; ++t) CTW(t, (ct0 + t) * G_UXS + skew(0, ct0 + t) * 32768 + w * 8192 + 4096 + lane * 16);
        CTW(6, w * 16384 + 4096 + lane * 16); CTW(7, w * 16384 + 12288 + lane * 16); CTW(8, w * 2048 + lane * 16); CTW(9, w * 2048 + 8192 + lane * 16);
        CTW(10, umx_base(w * 8 + g) + ct0 * 256 + c * 16); CTW(11, umx_base(w * 8 + 4 + g) + ct0 * 256 + c * 16);
        const unsigned fa = lane < 8 ? CTLO + G_C_UM + (w * 8 + lane) * 128 : CTLO + G_C_GV + ((s0 & 1) * 32 + w * 8 + (lane & 7)) * 128;
        CTW(12, lane < 16 ? fa : attn_flag(p, s0, w * 2 + (lane & 1))); CTW(13, lane < 16 ? fa : attn_flag(p, s0 + 60, w * 2 + (lane & 1)));
        CTW(14, w * 12288 + lane * 16); CTW(15, w * 1024 + lane * 16);
        CTW(16, (16 * (w & 1) + c) * (OUTW * 2) + (16 * ct0 + 16 * (w >> 1) + 4 * g) * 2); CTW(17, lane * 4);
        static_assert(NG1 == 58, "as in the generated block");
        const u64 ws = (u64)p.ws;
        const u64 stb = 0;
        asm volatile(ASM_O :: "s"(ws), "s"((u64)p.out), "s"(ws), "s"(w), "s"(s0), "s"(ws), "s"(ws), "s"(lb), "s"(stb) : ASM_CLOB);
        asm volatile("" : "+s"(ka));
        p.prm[5] = ka->prm[5];
        p.U = ka->U; p.ws = ka->ws; p.logA = ka->logA; p.Aim = ka->Aim; p.Cre = ka->Cre; p.Cim = ka->Cim; p.logdt = ka->logdt; p.Dskip = ka->Dskip; p.out = ka->out;
    }
    if ((ix & 22) == 22 && x >= 4) { attn_role(p, ix >= 30 ? 0 : NA + 1, (x & 3) * 8 + (ix & 1) * 4 + w, w, lane_id());
        if (b == 255) {
            wait_word(p, p.ws + G_WS_CTL + G_C_ARR, 255, 50u);
            if (w == 0 && lane_id() == 0) __hip_atomic_fetch_add((gu32*)(p.ws + G_WS_EP), 1u, RLX_AGENT);
        }
        __syncthreads();
    }
    worker_body(p, smem, w, lane_id(), s4 ? 1000 : 0);
}

extern "C" __global__ void code_end_marker() {}

static constexpr int g_prm[8] = {PRM_DEFAULT};

extern "C" void kernel_launch(void* const* d_in, const int* in_sizes, int n_in, void* d_out, int out_size,
                              void* d_ws, size_t ws_size, hipStream_t stream) {
    char* ws = (char*)(((uintptr_t)d_ws + 0x1FFFFF) & ~(uintptr_t)0x1FFFFF);
    MainP p;
    for (int i = 0; i < 8; ++i) p.prm[i] = g_prm[i];
    p.mem0 = (const u16*)d_in[1];
    for (int i = 0; i < 4; ++i) p.W[i] = (const u16*)d_in[2 + i];
    p.X = (const u16*)d_in[0]; p.U = (const u16*)d_in[7]; p.ws = ws;
    p.logA = (const u16*)d_in[8]; p.Aim = (const u16*)d_in[9]; p.Cre = (const u16*)d_in[10]; p.Cim = (const u16*)d_in[11];
    p.logdt = (const u16*)d_in[12]; p.Dskip = (const u16*)d_in[13]; p.out = (u16*)d_out;
    main_kernel<<<256, 256, 0, stream>>>(p);
}
```

```cpp
#include <hip/hip_runtime.h>
#include <hip/hip_bf16.h>
#include <stdint.h>
#include <stddef.h>

typedef __attribute__((ext_vector_type(8))) __bf16 bf16x8;
typedef __attribute__((ext_vector_type(4))) short s16x4;
typedef __attribute__((ext_vector_type(4))) float f32x4;
typedef __attribute__((ext_vector_type(4))) unsigned u32x4;
typedef __attribute__((ext_vector_type(2))) unsigned u32x2;
typedef __attribute__((ext_vector_type(2))) float f32x2;
typedef unsigned short u16;
typedef unsigned long long u64;

#define GAS __attribute__((address_space(1)))
typedef GAS unsigned gu32;
#define RLX_AGENT __ATOMIC_RELAXED, __HIP_MEMORY_SCOPE_AGENT

static constexpr int DM = 576, NH = 32, DH = 128, MM = 8, SEG = 32, NSEG = 64, SL = 2048;
static constexpr int HD = NH * DH;
static constexpr int OUTW = 2 * DM;
static constexpr int NST = 128;

__device__ __forceinline__ float bf2f(u16 h) { return __uint_as_float(((unsigned)h) << 16); }
typedef __attribute__((ext_vector_type(2))) __bf16 bf16x2_t;
__device__ __forceinline__ unsigned pk2bf(float a, float b) {
    bf16x2_t v; v[0] = (__bf16)a; v[1] = (__bf16)b;
    return __builtin_bit_cast(unsigned, v);
}
__device__ __forceinline__ float bfr(float f) { return __uint_as_float(pk2bf(f, f) << 16); }
__device__ __forceinline__ float lo2f(unsigned u) { return __uint_as_float(u << 16); }
__device__ __forceinline__ float hi2f(unsigned u) { return __uint_as_float(u & 0xffff0000u); }
__device__ __forceinline__ float rowmax4(float x) {
    auto a = __builtin_amdgcn_permlane16_swap(__float_as_uint(x), __float_as_uint(x), false, false);
    x = fmaxf(__uint_as_float(a[0]), __uint_as_float(a[1]));
    auto b = __builtin_amdgcn_permlane32_swap(__float_as_uint(x), __float_as_uint(x), false, false);
    return fmaxf(__uint_as_float(b[0]), __uint_as_float(b[1]));
}
__device__ __forceinline__ float rowsum4(float x) {
    auto a = __builtin_amdgcn_permlane16_swap(__float_as_uint(x), __float_as_uint(x), false, false);
    x = __uint_as_float(a[0]) + __uint_as_float(a[1]);
    auto b = __builtin_amdgcn_permlane32_swap(__float_as_uint(x), __float_as_uint(x), false, false);
    return __uint_as_float(b[0]) + __uint_as_float(b[1]);
}

struct MainP {
    const u16* W[4];
    char* ws; const u16* X; const u16* mem0; const u16* U;
    int prm[8];
    const u16* logA; const u16* Aim; const u16* Cre; const u16* Cim; const u16* logdt; const u16* Dskip;
    u16* out;
};
static constexpr int LDS_TOTAL = 163840;
static constexpr unsigned kWaitCap = 1u << 18;
#define VMC0() asm volatile("s_waitcnt vmcnt(0)" ::: "memory")
#define PRM_DEFAULT 1090, 600, 96, 668, 824, 1615005475, 15728816, 7
#define ASM_EA \
    "s_load_dwordx8 s[40:47], %0, 0\n\t" \
    "s_load_dwordx4 s[36:39], %0, 0x20\n\t" \
    "s_load_dwordx8 s[84:91], %0, 0x40\n\t" \
    "s_getpc_b64 s[54:55]\n\t" \
    "ea_pc_%=:\n\t" \
    "s_bfe_u32 s69, %1, 0x20003\n\t" \
    "s_lshl_b32 s69, s69, 15\n\t" \
    "v_lshl_add_u32 v1, %2, 7, s69\n\t" \
    "v_min_u32 v1, code_end_marker-ea_pc_%=, v1\n\t" \
    "s_mov_b32 m0, 155648\n\t" \
    "v_readfirstlane_b32 s66, %2\n\t" \
    "global_load_lds_dword v1, s[54:55]\n\t" \
    "s_bitcmp1_b32 %1, 2\n\t" \
    "s_cselect_b32 s69, 24, 16\n\t" \
    "s_lshr_b32 s70, %1, 3\n\t" \
    "s_cmp_ge_u32 s70, s69\n\t" \
    "s_cbranch_scc1 ea_done_%=\n\t" \
    "s_lshr_b32 s66, s66, 6\n\t" \
    "s_and_b32 s69, %1, 0xc4\n\t" \
    "s_cmp_eq_u32 s69, 4\n\t" \
    "s_cselect_b32 s69, 72, 36\n\t" \
    "s_add_u32 s82, s66, s69\n\t" \
    "s_lshl_b32 s82, s82, 10\n\t" \
    "s_mov_b32 m0, s82\n\t" \
    "s_and_b32 s67, %1, 3\n\t" \
    "s_lshl_b32 s67, s67, 3\n\t" \
    "s_and_b32 s69, s70, 7\n\t" \
    "s_add_u32 s67, s67, s69\n\t" \
    "s_lshl_b32 s69, s67, 8\n\t" \
    "v_lshrrev_b32 v1, 4, %2\n\t" \
    "v_lshrrev_b32 v2, 3, %2\n\t" \
    "v_and_b32 v2, 6, v2\n\t" \
    "v_and_or_b32 v2, v1, 8, v2\n\t" \
    "v_xor_b32 v2, v2, %2\n\t" \
    "v_and_b32 v2, 15, v2\n\t" \
    "v_lshl_add_u32 v2, v2, 4, s69\n\t" \
    "v_lshl_add_u32 v200, v1, 13, v2\n\t" \
    "s_mov_b32 s71, 16\n\t" \
    "s_bitcmp1_b32 %1, 2\n\t" \
    "s_waitcnt lgkmcnt(0)\n\t" \
    "s_cselect_b64 s[48:49], s[42:43], s[40:41]\n\t" \
    "s_cselect_b64 s[40:41], s[40:41], s[42:43]\n\t" \
    "s_cmp_ge_u32 s70, 8\n\t" \
    "s_cselect_b64 s[42:43], s[44:45], s[48:49]\n\t" \
    "s_cselect_b64 s[44:45], s[46:47], s[40:41]\n\t" \
    "ea_lp1_%=:\n\t" \
    "global_load_lds_dwordx4 v200, s[42:43]\n\t" \
    "v_add_u32 v200, 131072, v200\n\t" \
    "s_add_u32 m0, m0, 4096\n\t" \
    "s_sub_u32 s71, s71, 1\n\t" \
    "s_cmp_lg_u32 s71, 0\n\t" \
    "s_cbranch_scc1 ea_lp1_%=\n\t" \
    "s_mov_b64 s[50:51], s[38:39]\n\t" \
    "s_cmp_lt_u32 s70, 8\n\t" \
    "s_cbranch_scc1 ea_w_%=\n\t" \
    "s_mul_i32 s69, s67, 147456\n\t" \
    "s_add_u32 s44, s44, s69\n\t" \
    "s_addc_u32 s45, s45, 0\n\t" \
    "s_and_b32 s69, s70, 16\n\t" \
    "s_lshl_b32 s69, s69, 1\n\t" \
    "s_add_u32 s67, s67, s69\n\t" \
    "s_bitcmp1_b32 %1, 2\n\t" \
    "s_cbranch_scc1 gv_begin_%=\n\t" \
    "s_branch vv_begin_%=\n\t" \
    "ea_w_%=:\n\t" \
    "s_bitcmp1_b32 %1, 2\n\t" \
    "s_cbranch_scc1 gx_begin_%=\n\t" \
    "qk_begin_%=:\n\t" \
    "v_mbcnt_lo_u32_b32 v223, -1, 0\n\t" \
    "v_mbcnt_hi_u32_b32 v223, -1, v223\n\t" \
    "s_mov_b32 s68, 262144\n\t" \
    "s_mov_b32 s72, 0\n\t" \
    "s_mov_b32 s83, -1\n\t" \
    "s_mov_b32 s65, 0\n\t" \
    "v_and_b32 v180, 3, v223\n\t" \
    "v_lshlrev_b32 v202, 3, v180\n\t" \
    "v_lshrrev_b32 v181, 5, v223\n\t" \
    "v_lshl_add_u32 v202, v181, 12, v202\n\t" \
    "v_bfe_u32 v182, v223, 2, 2\n\t" \
    "s_mov_b32 s70, 288\n\t" \
    "v_mad_u32_u24 v202, v182, s70, v202\n\t" \
    "v_bfe_u32 v183, v223, 4, 1\n\t" \
    "s_mov_b32 s70, 2176\n\t" \
    "v_mad_u32_u24 v202, v183, s70, v202\n\t" \
    "s_lshl_b32 s69, s66, 6\n\t" \
    "v_xor_b32 v202, s69, v202\n\t" \
    "v_xor_b32 v203, 32, v202\n\t" \
    "v_lshlrev_b32 v0, 4, v223\n\t" \
    "s_mul_i32 s69, s66, 2048\n\t" \
    "s_bfe_u32 s71, s67, 0x20003\n\t" \
    "s_mul_i32 s71, s71, 65536\n\t" \
    "s_add_u32 s69, s69, s71\n\t" \
    "s_add_u32 s69, s69, 32768\n\t" \
    "v_lshl_add_u32 v1, v223, 4, s69\n\t" \
    "s_bfe_u32 s71, s67, 0x20003\n\t" \
    "s_mul_i32 s69, s71, 65536\n\t" \
    "s_add_u32 s69, s69, 53248\n\t" \
    "v_lshl_add_u32 v2, v223, 4, s69\n\t" \
    "s_bfe_u32 s71, s67, 0x20003\n\t" \
    "s_mul_i32 s69, s71, 65536\n\t" \
    "s_add_u32 s69, s69, 32768\n\t" \
    "v_lshl_add_u32 v34, v223, 4, s69\n\t" \
    "s_mov_b32 s69, 17476\n\t" \
    "v_bfe_u32 v184, v223, 1, 1\n\t" \
    "v_mov_b32 v10, s69\n\t" \
    "s_mov_b32 s70, 43690\n\t" \
    "v_mad_u32_u24 v10, v184, s70, v10\n\t" \
    "v_mov_b32 v11, 0\n\t" \
    "s_waitcnt vmcnt(12) lgkmcnt(0)\n\t" \
    "s_barrier\n\t" \
    "s_add_u32 m0, s82, 65536\n\t" \
    "s_nop 0\n\t" \
    "global_load_lds_dwordx4 v200, s[42:43]\n\t" \
    "v_add_u32 v200, 131072, v200\n\t" \
    "s_add_u32 m0, s82, 69632\n\t" \
    "s_nop 0\n\t" \
    "global_load_lds_dwordx4 v200, s[42:43]\n\t" \
    "v_add_u32 v200, 131072, v200\n\t" \
    "s_add_u32 m0, s82, 73728\n\t" \
    "s_nop 0\n\t" \
    "global_load_lds_dwordx4 v200, s[42:43]\n\t" \
    "v_add_u32 v200, 131072, v200\n\t" \
    "s_add_u32 m0, s82, 77824\n\t" \
    "s_nop 0\n\t" \
    "global_load_lds_dwordx4 v200, s[42:43]\n\t" \
    "v_add_u32 v200, 131072, v200\n\t" \
    "ds_read_b64_tr_b16 a[0:1], v202 offset:36864\n\t" \
    "ds_read_b64_tr_b16 a[2:3], v202 offset:37888\n\t" \
    "ds_read_b64_tr_b16 a[4:5], v202 offset:45056\n\t" \
    "ds_read_b64_tr_b16 a[6:7], v202 offset:46080\n\t" \
    "ds_read_b64_tr_b16 a[72:73], v203 offset:36864\n\t" \
    "ds_read_b64_tr_b16 a[74:75], v203 offset:37888\n\t" \
    "ds_read_b64_tr_b16 a[76:77], v203 offset:45056\n\t" \
    "ds_read_b64_tr_b16 a[78:79], v203 offset:46080\n\t" \
    "s_mul_i32 s69, s66, 512\n\t" \
    "s_add_u32 s69, s69, 16384\n\t" \
    "v_and_b32 v185, 13, v223\n\t" \
    "v_lshl_add_u32 v12, v185, 5, s69\n\t" \
    "v_lshl_add_u32 v12, v184, 2, v12\n\t" \
    "v_lshrrev_b32 v186, 4, v223\n\t" \
    "v_lshl_add_u32 v12, v186, 3, v12\n\t" \
    "s_mov_b32 s69, 16384\n\t" \
    "v_and_b32 v187, 15, v223\n\t" \
    "v_lshl_add_u32 v13, v187, 3, s69\n\t" \
    "v_lshl_add_u32 v13, v186, 8, v13\n\t" \
    "s_bfe_u32 s71, s67, 0x20003\n\t" \
    "s_mul_i32 s69, s71, 65536\n\t" \
    "s_add_u32 s69, s69, 266240\n\t" \
    "v_and_b32 v188, 31, v223\n\t" \
    "v_lshl_add_u32 v20, v188, 5, s69\n\t" \
    "v_lshl_add_u32 v20, v181, 2, v20\n\t" \
    "s_mul_i32 s69, s67, 32\n\t" \
    "s_add_u32 s69, s69, 262144\n\t" \
    "v_lshl_add_u32 v21, v180, 16, s69\n\t" \
    "v_bfe_u32 v189, v223, 2, 3\n\t" \
    "v_lshlrev_b32 v199, 10, v189\n\t" \
    "v_min_u32 v199, 4096, v199\n\t" \
    "v_add_u32 v21, v199, v21\n\t" \
    "s_bfe_u32 s71, s67, 0x20003\n\t" \
    "s_mul_i32 s69, s71, 65536\n\t" \
    "s_add_u32 s69, s69, 267264\n\t" \
    "v_min_u32 v35, 35, v223\n\t" \
    "v_lshl_add_u32 v35, v35, 5, s69\n\t" \
    "v_lshlrev_b32 v22, 2, v223\n\t" \
    "s_waitcnt vmcnt(12) lgkmcnt(0)\n\t" \
    "s_barrier\n\t" \
    "s_add_u32 m0, s82, 81920\n\t" \
    "s_nop 0\n\t" \
    "global_load_lds_dwordx4 v200, s[42:43]\n\t" \
    "v_add_u32 v200, 131072, v200\n\t" \
    "s_add_u32 m0, s82, 86016\n\t" \
    "s_nop 0\n\t" \
    "global_load_lds_dwordx4 v200, s[42:43]\n\t" \
    "v_add_u32 v200, 131072, v200\n\t" \
    "s_add_u32 m0, s82, 90112\n\t" \
    "s_nop 0\n\t" \
    "global_load_lds_dwordx4 v200, s[42:43]\n\t" \
    "v_add_u32 v200, 131072, v200\n\t" \
    "s_add_u32 m0, s82, 94208\n\t" \
    "s_nop 0\n\t" \
    "global_load_lds_dwordx4 v200, s[42:43]\n\t" \
    "v_add_u32 v200, 131072, v200\n\t" \
    "ds_read_b64_tr_b16 a[8:9], v202 offset:53248\n\t" \
    "ds_read_b64_tr_b16 a[10:11], v202 offset:54272\n\t" \
    "ds_read_b64_tr_b16 a[12:13], v202 offset:61440\n\t" \
    "ds_read_b64_tr_b16 a[14:15], v202 offset:62464\n\t" \
    "ds_read_b64_tr_b16 a[80:81], v203 offset:53248\n\t" \
    "ds_read_b64_tr_b16 a[82:83], v203 offset:54272\n\t" \
    "ds_read_b64_tr_b16 a[84:85], v203 offset:61440\n\t" \
    "ds_read_b64_tr_b16 a[86:87], v203 offset:62464\n\t" \
    "s_mul_i32 s69, s67, 128\n\t" \
    "s_add_u32 s69, s69, 6127616\n\t" \
    "v_mov_b32 v14, s69\n\t" \
    "v_and_b32 v190, 57, v223\n\t" \
    "v_lshlrev_b32 v23, 4, v190\n\t" \
    "v_lshl_add_u32 v23, v184, 6, v23\n\t" \
    "v_bfe_u32 v191, v223, 2, 1\n\t" \
    "v_lshl_add_u32 v23, v191, 5, v23\n\t" \
    "v_add_u32 v15, 4096, v23\n\t" \
    "s_mul_i32 s69, s66, 512\n\t" \
    "s_add_u32 s69, s69, 8192\n\t" \
    "v_lshl_add_u32 v16, v187, 4, s69\n\t" \
    "v_lshl_add_u32 v16, v186, 2, v16\n\t" \
    "s_mov_b32 s69, 8192\n\t" \
    "v_and_b32 v192, 1, v223\n\t" \
    "v_lshl_add_u32 v17, v192, 3, s69\n\t" \
    "v_lshrrev_b32 v193, 2, v223\n\t" \
    "v_lshl_add_u32 v17, v193, 4, v17\n\t" \
    "s_mul_i32 s69, s67, 128\n\t" \
    "s_add_u32 s69, s69, 6160384\n\t" \
    "v_mov_b32 v24, s69\n\t" \
    "s_waitcnt vmcnt(12) lgkmcnt(0)\n\t" \
    "s_barrier\n\t" \
    "s_add_u32 m0, s82, 98304\n\t" \
    "s_nop 0\n\t" \
    "global_load_lds_dwordx4 v200, s[42:43]\n\t" \
    "v_add_u32 v200, 131072, v200\n\t" \
    "s_add_u32 m0, s82, 102400\n\t" \
    "s_nop 0\n\t" \
    "global_load_lds_dwordx4 v200, s[42:43]\n\t" \
    "v_add_u32 v200, 131072, v200\n\t" \
    "s_add_u32 m0, s82, 106496\n\t" \
    "s_nop 0\n\t" \
    "global_load_lds_dwordx4 v200, s[42:43]\n\t" \
    "v_add_u32 v200, 131072, v200\n\t" \
    "s_add_u32 m0, s82, 110592\n\t" \
    "s_nop 0\n\t" \
    "global_load_lds_dwordx4 v200, s[42:43]\n\t" \
    "v_add_u32 v200, 131072, v200\n\t" \
    "v_add_u32 v202, 69632, v202\n\t" \
    "v_add_u32 v203, 69632, v203\n\t" \
    "ds_read_b64_tr_b16 a[16:17], v202 offset:0\n\t" \
    "ds_read_b64_tr_b16 a[18:19], v202 offset:1024\n\t" \
    "ds_read_b64_tr_b16 a[20:21], v202 offset:8192\n\t" \
    "ds_read_b64_tr_b16 a[22:23], v202 offset:9216\n\t" \
    "ds_read_b64_tr_b16 a[88:89], v203 offset:0\n\t" \
    "ds_read_b64_tr_b16 a[90:91], v203 offset:1024\n\t" \
    "ds_read_b64_tr_b16 a[92:93], v203 offset:8192\n\t" \
    "ds_read_b64_tr_b16 a[94:95], v203 offset:9216\n\t" \
    "s_bfe_u32 s71, s66, 0x10000\n\t" \
    "s_mul_i32 s69, s71, 128\n\t" \
    "s_bfe_u32 s71, s67, 0x40000\n\t" \
    "s_mul_i32 s71, s71, 512\n\t" \
    "s_add_u32 s69, s69, s71\n\t" \
    "s_bfe_u32 s71, s67, 0x10004\n\t" \
    "s_mul_i32 s71, s71, 16384\n\t" \
    "s_add_u32 s69, s69, s71\n\t" \
    "v_lshl_add_u32 v18, v180, 2, s69\n\t" \
    "v_lshl_add_u32 v18, v191, 5, v18\n\t" \
    "v_bfe_u32 v194, v223, 3, 1\n\t" \
    "v_lshl_add_u32 v18, v194, 8, v18\n\t" \
    "v_lshl_add_u32 v18, v183, 4, v18\n\t" \
    "v_lshl_add_u32 v18, v181, 6, v18\n\t" \
    "s_bfe_u32 s71, s66, 0x10000\n\t" \
    "s_mul_i32 s69, s71, 128\n\t" \
    "s_bfe_u32 s71, s67, 0x10000\n\t" \
    "s_mul_i32 s71, s71, 8\n\t" \
    "s_add_u32 s69, s69, s71\n\t" \
    "s_bfe_u32 s71, s67, 0x40001\n\t" \
    "s_mul_i32 s71, s71, 256\n\t" \
    "s_add_u32 s69, s69, s71\n\t" \
    "s_add_u32 s69, s69, 49152\n\t" \
    "v_lshl_add_u32 v19, v184, 5, s69\n\t" \
    "v_and_b32 v195, 5, v193\n\t" \
    "v_lshl_add_u32 v19, v195, 2, v19\n\t" \
    "v_lshl_add_u32 v19, v181, 6, v19\n\t" \
    "v_add_u32 v3, 65536, v18\n\t" \
    "v_add_u32 v4, 65536, v19\n\t" \
    "s_waitcnt vmcnt(12) lgkmcnt(0)\n\t" \
    "s_barrier\n\t" \
    "s_add_u32 m0, s82, 0\n\t" \
    "s_nop 0\n\t" \
    "global_load_lds_dwordx4 v200, s[42:43]\n\t" \
    "v_add_u32 v200, 131072, v200\n\t" \
    "s_add_u32 m0, s82, 4096\n\t" \
    "s_nop 0\n\t" \
    "global_load_lds_dwordx4 v200, s[42:43]\n\t" \
    "v_add_u32 v200, 131072, v200\n\t" \
    "s_add_u32 m0, s82, 8192\n\t" \
    "s_nop 0\n\t" \
    "global_load_lds_dwordx4 v200, s[42:43]\n\t" \
    "v_add_u32 v200, 131072, v200\n\t" \
    "s_add_u32 m0, s82, 12288\n\t" \
    "s_nop 0\n\t" \
    "global_load_lds_dwordx4 v200, s[42:43]\n\t" \
    "v_add_u32 v200, 131072, v200\n\t" \
    "ds_read_b64_tr_b16 a[24:25], v202 offset:16384\n\t" \
    "ds_read_b64_tr_b16 a[26:27], v202 offset:17408\n\t" \
    "ds_read_b64_tr_b16 a[28:29], v202 offset:24576\n\t" \
    "ds_read_b64_tr_b16 a[30:31], v202 offset:25600\n\t" \
    "ds_read_b64_tr_b16 a[96:97], v203 offset:16384\n\t" \
    "ds_read_b64_tr_b16 a[98:99], v203 offset:17408\n\t" \
    "ds_read_b64_tr_b16 a[100:101], v203 offset:24576\n\t" \
    "ds_read_b64_tr_b16 a[102:103], v203 offset:25600\n\t" \
    "s_mov_b32 s77, 3019898880\n\t" \
    "s_mov_b32 s78, 3054198966\n\t" \
    "s_mov_b32 s71, 3\n\t" \
    "qk_E1_%=:\n\t" \
    "s_memtime s[54:55]\n\t" \
    "s_nop 0\n\t" \
    "s_memrealtime s[4:5]\n\t" \
    "s_nop 0\n\t" \
    "s_memrealtime s[6:7]\n\t" \
    "s_nop 0\n\t" \
    "s_memrealtime s[8:9]\n\t" \
    "s_nop 0\n\t" \
    "s_memrealtime s[10:11]\n\t" \
    "s_nop 0\n\t" \
    "s_memrealtime s[12:13]\n\t" \
    "s_nop 0\n\t" \
    "s_memrealtime s[14:15]\n\t" \
    "s_nop 0\n\t" \
    "s_memrealtime s[16:17]\n\t" \
    "s_nop 0\n\t" \
    "s_memrealtime s[18:19]\n\t" \
    "s_nop 0\n\t" \
    "s_memrealtime s[20:21]\n\t" \
    "s_nop 0\n\t" \
    "s_memrealtime s[22:23]\n\t" \
    "s_nop 0\n\t" \
    "s_memrealtime s[24:25]\n\t" \
    "s_nop 0\n\t" \
    "s_memrealtime s[26:27]\n\t" \
    "s_nop 0\n\t" \
    "s_memrealtime s[28:29]\n\t" \
    "s_nop 0\n\t" \
    "s_memtime s[56:57]\n\t" \
    "s_mov_b32 s70, 1\n\t" \
    "s_sub_u32 s71, s71, 1\n\t" \
    "s_cselect_b32 s69, 0, -1\n\t" \
    "s_waitcnt lgkmcnt(0)\n\t" \
    "s_sub_u32 s54, s56, s54\n\t" \
    "s_and_b32 s54, s54, s69\n\t" \
    "s_cmp_gt_u32 s54, 124\n\t" \
    "s_cbranch_scc1 qk_E1_%=\n\t" \
    "s_cmp_eq_u32 s4, s28\n\t" \
    "s_addc_u32 s70, s70, 0\n\t" \
    "s_cmp_eq_u32 s6, s28\n\t" \
    "s_addc_u32 s70, s70, 0\n\t" \
    "s_cmp_eq_u32 s8, s28\n\t" \
    "s_addc_u32 s70, s70, 0\n\t" \
    "s_cmp_eq_u32 s10, s28\n\t" \
    "s_addc_u32 s70, s70, 0\n\t" \
    "s_cmp_eq_u32 s12, s28\n\t" \
    "s_addc_u32 s70, s70, 0\n\t" \
    "s_cmp_eq_u32 s14, s28\n\t" \
    "s_addc_u32 s70, s70, 0\n\t" \
    "s_cmp_eq_u32 s16, s28\n\t" \
    "s_addc_u32 s70, s70, 0\n\t" \
    "s_cmp_eq_u32 s18, s28\n\t" \
    "s_addc_u32 s70, s70, 0\n\t" \
    "s_cmp_eq_u32 s20, s28\n\t" \
    "s_addc_u32 s70, s70, 0\n\t" \
    "s_cmp_eq_u32 s22, s28\n\t" \
    "s_addc_u32 s70, s70, 0\n\t" \
    "s_cmp_eq_u32 s24, s28\n\t" \
    "s_addc_u32 s70, s70, 0\n\t" \
    "s_cmp_eq_u32 s26, s28\n\t" \
    "s_addc_u32 s70, s70, 0\n\t" \
    "s_mov_b32 s75, s73\n\t" \
    "s_mov_b32 s76, s74\n\t" \
    "s_lshl_b32 s70, s70, 3\n\t" \
    "s_sub_u32 s73, s56, s70\n\t" \
    "s_lshl_b32 s74, s28, 4\n\t" \
    "s_waitcnt vmcnt(12) lgkmcnt(0)\n\t" \
    "s_barrier\n\t" \
    "s_add_u32 m0, s82, 16384\n\t" \
    "s_nop 0\n\t" \
    "global_load_lds_dwordx4 v200, s[42:43]\n\t" \
    "v_add_u32 v200, 131072, v200\n\t" \
    "s_add_u32 m0, s82, 20480\n\t" \
    "s_nop 0\n\t" \
    "global_load_lds_dwordx4 v200, s[42:43]\n\t" \
    "v_add_u32 v200, 131072, v200\n\t" \
    "s_add_u32 m0, s82, 24576\n\t" \
    "s_nop 0\n\t" \
    "global_load_lds_dwordx4 v200, s[42:43]\n\t" \
    "v_add_u32 v200, 131072, v200\n\t" \
    "s_add_u32 m0, s82, 28672\n\t" \
    "s_nop 0\n\t" \
    "global_load_lds_dwordx4 v200, s[42:43]\n\t" \
    "v_add_u32 v200, 131072, v200\n\t" \
    "ds_read_b64_tr_b16 a[32:33], v202 offset:32768\n\t" \
    "ds_read_b64_tr_b16 a[34:35], v202 offset:33792\n\t" \
    "ds_read_b64_tr_b16 a[36:37], v202 offset:40960\n\t" \
    "ds_read_b64_tr_b16 a[38:39], v202 offset:41984\n\t" \
    "ds_read_b64_tr_b16 a[104:105], v203 offset:32768\n\t" \
    "ds_read_b64_tr_b16 a[106:107], v203 offset:33792\n\t" \
    "ds_read_b64_tr_b16 a[108:109], v203 offset:40960\n\t" \
    "ds_read_b64_tr_b16 a[110:111], v203 offset:41984\n\t" \
    "s_waitcnt vmcnt(12) lgkmcnt(0)\n\t" \
    "s_barrier\n\t" \
    "v_subrev_u32 v200, 4718592, v200\n\t" \
    "s_add_u32 m0, s82, 32768\n\t" \
    "s_nop 0\n\t" \
    "global_load_lds_dwordx4 v200, s[44:45]\n\t" \
    "v_add_u32 v200, 131072, v200\n\t" \
    "s_add_u32 m0, s82, 36864\n\t" \
    "s_nop 0\n\t" \
    "global_load_lds_dwordx4 v200, s[44:45]\n\t" \
    "v_add_u32 v200, 131072, v200\n\t" \
    "s_add_u32 m0, s82, 40960\n\t" \
    "s_nop 0\n\t" \
    "global_load_lds_dwordx4 v200, s[44:45]\n\t" \
    "v_add_u32 v200, 131072, v200\n\t" \
    "s_add_u32 m0, s82, 45056\n\t" \
    "s_nop 0\n\t" \
    "global_load_lds_dwordx4 v200, s[44:45]\n\t" \
    "v_add_u32 v200, 131072, v200\n\t" \
    "ds_read_b64_tr_b16 a[40:41], v202 offset:49152\n\t" \
    "ds_read_b64_tr_b16 a[42:43], v202 offset:50176\n\t" \
    "ds_read_b64_tr_b16 a[44:45], v202 offset:57344\n\t" \
    "ds_read_b64_tr_b16 a[46:47], v202 offset:58368\n\t" \
    "ds_read_b64_tr_b16 a[112:113], v203 offset:49152\n\t" \
    "ds_read_b64_tr_b16 a[114:115], v203 offset:50176\n\t" \
    "ds_read_b64_tr_b16 a[116:117], v203 offset:57344\n\t" \
    "ds_read_b64_tr_b16 a[118:119], v203 offset:58368\n\t" \
    "s_waitcnt vmcnt(12) lgkmcnt(0)\n\t" \
    "s_barrier\n\t" \
    "s_add_u32 m0, s82, 49152\n\t" \
    "s_nop 0\n\t" \
    "global_load_lds_dwordx4 v200, s[44:45]\n\t" \
    "v_add_u32 v200, 131072, v200\n\t" \
    "s_add_u32 m0, s82, 53248\n\t" \
    "s_nop 0\n\t" \
    "global_load_lds_dwordx4 v200, s[44:45]\n\t" \
    "v_add_u32 v200, 131072, v200\n\t" \
    "s_add_u32 m0, s82, 57344\n\t" \
    "s_nop 0\n\t" \
    "global_load_lds_dwordx4 v200, s[44:45]\n\t" \
    "v_add_u32 v200, 131072, v200\n\t" \
    "s_add_u32 m0, s82, 61440\n\t" \
    "s_nop 0\n\t" \
    "global_load_lds_dwordx4 v200, s[44:45]\n\t" \
    "v_add_u32 v200, 131072, v200\n\t" \
    "v_add_u32 v202, 65536, v202\n\t" \
    "v_add_u32 v203, 65536, v203\n\t" \
    "ds_read_b64_tr_b16 a[48:49], v202 offset:0\n\t" \
    "ds_read_b64_tr_b16 a[50:51], v202 offset:1024\n\t" \
    "ds_read_b64_tr_b16 a[52:53], v202 offset:8192\n\t" \
    "ds_read_b64_tr_b16 a[54:55], v202 offset:9216\n\t" \
    "ds_read_b64_tr_b16 a[120:121], v203 offset:0\n\t" \
    "ds_read_b64_tr_b16 a[122:123], v203 offset:1024\n\t" \
    "ds_read_b64_tr_b16 a[124:125], v203 offset:8192\n\t" \
    "ds_read_b64_tr_b16 a[126:127], v203 offset:9216\n\t" \
    "s_waitcnt vmcnt(12) lgkmcnt(0)\n\t" \
    "s_barrier\n\t" \
    "s_add_u32 m0, s82, 65536\n\t" \
    "s_nop 0\n\t" \
    "global_load_lds_dwordx4 v200, s[44:45]\n\t" \
    "v_add_u32 v200, 131072, v200\n\t" \
    "s_add_u32 m0, s82, 69632\n\t" \
    "s_nop 0\n\t" \
    "global_load_lds_dwordx4 v200, s[44:45]\n\t" \
    "v_add_u32 v200, 131072, v200\n\t" \
    "s_add_u32 m0, s82, 73728\n\t" \
    "s_nop 0\n\t" \
    "global_load_lds_dwordx4 v200, s[44:45]\n\t" \
    "v_add_u32 v200, 131072, v200\n\t" \
    "s_add_u32 m0, s82, 77824\n\t" \
    "s_nop 0\n\t" \
    "global_load_lds_dwordx4 v200, s[44:45]\n\t" \
    "v_add_u32 v200, 131072, v200\n\t" \
    "v_add_u32 v202, -98304, v202\n\t" \
    "v_add_u32 v203, -98304, v203\n\t" \
    "ds_read_b64_tr_b16 a[56:57], v202 offset:0\n\t" \
    "ds_read_b64_tr_b16 a[58:59], v202 offset:1024\n\t" \
    "ds_read_b64_tr_b16 a[60:61], v202 offset:8192\n\t" \
    "ds_read_b64_tr_b16 a[62:63], v202 offset:9216\n\t" \
    "ds_read_b64_tr_b16 a[128:129], v203 offset:0\n\t" \
    "ds_read_b64_tr_b16 a[130:131], v203 offset:1024\n\t" \
    "ds_read_b64_tr_b16 a[132:133], v203 offset:8192\n\t" \
    "ds_read_b64_tr_b16 a[134:135], v203 offset:9216\n\t" \
    "s_waitcnt vmcnt(12) lgkmcnt(0)\n\t" \
    "s_barrier\n\t" \
    "s_add_u32 m0, s82, 81920\n\t" \
    "s_nop 0\n\t" \
    "global_load_lds_dwordx4 v200, s[44:45]\n\t" \
    "v_add_u32 v200, 131072, v200\n\t" \
    "s_add_u32 m0, s82, 86016\n\t" \
    "s_nop 0\n\t" \
    "global_load_lds_dwordx4 v200, s[44:45]\n\t" \
    "v_add_u32 v200, 131072, v200\n\t" \
    "s_add_u32 m0, s82, 90112\n\t" \
    "s_nop 0\n\t" \
    "global_load_lds_dwordx4 v200, s[44:45]\n\t" \
    "v_add_u32 v200, 131072, v200\n\t" \
    "s_add_u32 m0, s82, 94208\n\t" \
    "s_nop 0\n\t" \
    "global_load_lds_dwordx4 v200, s[44:45]\n\t" \
    "v_add_u32 v200, 131072, v200\n\t" \
    "ds_read_b64_tr_b16 a[64:65], v202 offset:16384\n\t" \
    "ds_read_b64_tr_b16 a[66:67], v202 offset:17408\n\t" \
    "ds_read_b64_tr_b16 a[68:69], v202 offset:24576\n\t" \
    "ds_read_b64_tr_b16 a[70:71], v202 offset:25600\n\t" \
    "ds_read_b64_tr_b16 a[136:137], v203 offset:16384\n\t" \
    "ds_read_b64_tr_b16 a[138:139], v203 offset:17408\n\t" \
    "ds_read_b64_tr_b16 a[140:141], v203 offset:24576\n\t" \
    "ds_read_b64_tr_b16 a[142:143], v203 offset:25600\n\t" \
    "s_waitcnt vmcnt(12) lgkmcnt(0)\n\t" \
    "s_barrier\n\t" \
    "s_add_u32 m0, s82, 98304\n\t" \
    "s_nop 0\n\t" \
    "global_load_lds_dwordx4 v200, s[44:45]\n\t" \
    "v_add_u32 v200, 131072, v200\n\t" \
    "s_add_u32 m0, s82, 102400\n\t" \
    "s_nop 0\n\t" \
    "global_load_lds_dwordx4 v200, s[44:45]\n\t" \
    "v_add_u32 v200, 131072, v200\n\t" \
    "s_add_u32 m0, s82, 106496\n\t" \
    "s_nop 0\n\t" \
    "global_load_lds_dwordx4 v200, s[44:45]\n\t" \
    "v_add_u32 v200, 131072, v200\n\t" \
    "s_add_u32 m0, s82, 110592\n\t" \
    "s_nop 0\n\t" \
    "global_load_lds_dwordx4 v200, s[44:45]\n\t" \
    "v_add_u32 v200, 131072, v200\n\t" \
    "ds_read_b64_tr_b16 a[144:145], v202 offset:32768\n\t" \
    "ds_read_b64_tr_b16 a[146:147], v202 offset:33792\n\t" \
    "ds_read_b64_tr_b16 a[148:149], v202 offset:40960\n\t" \
    "ds_read_b64_tr_b16 a[150:151], v202 offset:41984\n\t" \
    "ds_read_b64_tr_b16 a[216:217], v203 offset:32768\n\t" \
    "ds_read_b64_tr_b16 a[218:219], v203 offset:33792\n\t" \
    "ds_read_b64_tr_b16 a[220:221], v203 offset:40960\n\t" \
    "ds_read_b64_tr_b16 a[222:223], v203 offset:41984\n\t" \
    "s_waitcnt vmcnt(12) lgkmcnt(0)\n\t" \
    "s_barrier\n\t" \
    "s_add_u32 m0, s82, 0\n\t" \
    "s_nop 0\n\t" \
    "global_load_lds_dwordx4 v200, s[44:45]\n\t" \
    "v_add_u32 v200, 131072, v200\n\t" \
    "s_add_u32 m0, s82, 4096\n\t" \
    "s_nop 0\n\t" \
    "global_load_lds_dwordx4 v200, s[44:45]\n\t" \
    "v_add_u32 v200, 131072, v200\n\t" \
    "s_add_u32 m0, s82, 8192\n\t" \
    "s_nop 0\n\t" \
    "global_load_lds_dwordx4 v200, s[44:45]\n\t" \
    "v_add_u32 v200, 131072, v200\n\t" \
    "s_add_u32 m0, s82, 12288\n\t" \
    "s_nop 0\n\t" \
    "global_load_lds_dwordx4 v200, s[44:45]\n\t" \
    "v_add_u32 v200, 131072, v200\n\t" \
    "ds_read_b64_tr_b16 a[152:153], v202 offset:49152\n\t" \
    "ds_read_b64_tr_b16 a[154:155], v202 offset:50176\n\t" \
    "ds_read_b64_tr_b16 a[156:157], v202 offset:57344\n\t" \
    "ds_read_b64_tr_b16 a[158:159], v202 offset:58368\n\t" \
    "ds_read_b64_tr_b16 a[224:225], v203 offset:49152\n\t" \
    "ds_read_b64_tr_b16 a[226:227], v203 offset:50176\n\t" \
    "ds_read_b64_tr_b16 a[228:229], v203 offset:57344\n\t" \
    "ds_read_b64_tr_b16 a[230:231], v203 offset:58368\n\t" \
    "s_waitcnt vmcnt(12) lgkmcnt(0)\n\t" \
    "s_barrier\n\t" \
    "s_add_u32 m0, s82, 16384\n\t" \
    "s_nop 0\n\t" \
    "global_load_lds_dwordx4 v200, s[44:45]\n\t" \
    "v_add_u32 v200, 131072, v200\n\t" \
    "s_add_u32 m0, s82, 20480\n\t" \
    "s_nop 0\n\t" \
    "global_load_lds_dwordx4 v200, s[44:45]\n\t" \
    "v_add_u32 v200, 131072, v200\n\t" \
    "s_add_u32 m0, s82, 24576\n\t" \
    "s_nop 0\n\t" \
    "global_load_lds_dwordx4 v200, s[44:45]\n\t" \
    "v_add_u32 v200, 131072, v200\n\t" \
    "s_add_u32 m0, s82, 28672\n\t" \
    "s_nop 0\n\t" \
    "global_load_lds_dwordx4 v200, s[44:45]\n\t" \
    "v_add_u32 v200, 131072, v200\n\t" \
    "v_add_u32 v202, 65536, v202\n\t" \
    "v_add_u32 v203, 65536, v203\n\t" \
    "ds_read_b64_tr_b16 a[160:161], v202 offset:0\n\t" \
    "ds_read_b64_tr_b16 a[162:163], v202 offset:1024\n\t" \
    "ds_read_b64_tr_b16 a[164:165], v202 offset:8192\n\t" \
    "ds_read_b64_tr_b16 a[166:167], v202 offset:9216\n\t" \
    "ds_read_b64_tr_b16 a[232:233], v203 offset:0\n\t" \
    "ds_read_b64_tr_b16 a[234:235], v203 offset:1024\n\t" \
    "ds_read_b64_tr_b16 a[236:237], v203 offset:8192\n\t" \
    "ds_read_b64_tr_b16 a[238:239], v203 offset:9216\n\t" \
    "s_waitcnt vmcnt(12) lgkmcnt(0)\n\t" \
    "s_barrier\n\t" \
    "s_add_u32 m0, s82, 32768\n\t" \
    "s_nop 0\n\t" \
    "global_load_lds_dwordx4 v200, s[44:45]\n\t" \
    "v_add_u32 v200, 131072, v200\n\t" \
    "s_add_u32 m0, s82, 36864\n\t" \
    "s_nop 0\n\t" \
    "global_load_lds_dwordx4 v200, s[44:45]\n\t" \
    "v_add_u32 v200, 131072, v200\n\t" \
    "s_add_u32 m0, s82, 40960\n\t" \
    "s_nop 0\n\t" \
    "global_load_lds_dwordx4 v200, s[44:45]\n\t" \
    "v_add_u32 v200, 131072, v200\n\t" \
    "s_add_u32 m0, s82, 45056\n\t" \
    "s_nop 0\n\t" \
    "global_load_lds_dwordx4 v200, s[44:45]\n\t" \
    "v_add_u32 v200, 131072, v200\n\t" \
    "ds_read_b64_tr_b16 a[168:169], v202 offset:16384\n\t" \
    "ds_read_b64_tr_b16 a[170:171], v202 offset:17408\n\t" \
    "ds_read_b64_tr_b16 a[172:173], v202 offset:24576\n\t" \
    "ds_read_b64_tr_b16 a[174:175], v202 offset:25600\n\t" \
    "ds_read_b64_tr_b16 a[240:241], v203 offset:16384\n\t" \
    "ds_read_b64_tr_b16 a[242:243], v203 offset:17408\n\t" \
    "ds_read_b64_tr_b16 a[244:245], v203 offset:24576\n\t" \
    "ds_read_b64_tr_b16 a[246:247], v203 offset:25600\n\t" \
    "s_waitcnt vmcnt(12) lgkmcnt(0)\n\t" \
    "s_barrier\n\t" \
    "s_add_u32 m0, s82, 49152\n\t" \
    "s_nop 0\n\t" \
    "global_load_lds_dwordx4 v200, s[44:45]\n\t" \
    "v_add_u32 v200, 131072, v200\n\t" \
    "s_add_u32 m0, s82, 53248\n\t" \
    "s_nop 0\n\t" \
    "global_load_lds_dwordx4 v200, s[44:45]\n\t" \
    "v_add_u32 v200, 131072, v200\n\t" \
    "s_add_u32 m0, s82, 57344\n\t" \
    "s_nop 0\n\t" \
    "global_load_lds_dwordx4 v200, s[44:45]\n\t" \
    "v_add_u32 v200, 131072, v200\n\t" \
    "s_add_u32 m0, s82, 61440\n\t" \
    "s_nop 0\n\t" \
    "global_load_lds_dwordx4 v200, s[44:45]\n\t" \
    "v_add_u32 v200, 131072, v200\n\t" \
    "ds_read_b64_tr_b16 a[176:177], v202 offset:32768\n\t" \
    "ds_read_b64_tr_b16 a[178:179], v202 offset:33792\n\t" \
    "ds_read_b64_tr_b16 a[180:181], v202 offset:40960\n\t" \
    "ds_read_b64_tr_b16 a[182:183], v202 offset:41984\n\t" \
    "ds_read_b64_tr_b16 a[248:249], v203 offset:32768\n\t" \
    "ds_read_b64_tr_b16 a[250:251], v203 offset:33792\n\t" \
    "ds_read_b64_tr_b16 a[252:253], v203 offset:40960\n\t" \
    "ds_read_b64_tr_b16 a[254:255], v203 offset:41984\n\t" \
    "s_waitcnt vmcnt(12) lgkmcnt(0)\n\t" \
    "s_barrier\n\t" \
    "v_mbcnt_lo_u32_b32 v160, -1, 0\n\t" \
    "v_mbcnt_hi_u32_b32 v160, -1, v160\n\t" \
    "v_min_u32 v160, 59, v160\n\t" \
    "v_lshlrev_b32 v160, 6, v160\n\t" \
    "v_add_u32 v160, 6258688, v160\n\t" \
    "v_mov_b32 v161, 6266880\n\t" \
    "global_load_dword v161, v161, s[36:37] sc1\n\t" \
    "global_load_dwordx2 v[162:163], v160, s[36:37] sc1\n\t" \
    "v_add_u32 v202, -65536, v202\n\t" \
    "v_add_u32 v203, -65536, v203\n\t" \
    "ds_read_b64_tr_b16 a[184:185], v202 offset:0\n\t" \
    "ds_read_b64_tr_b16 a[186:187], v202 offset:1024\n\t" \
    "ds_read_b64_tr_b16 a[188:189], v202 offset:8192\n\t" \
    "ds_read_b64_tr_b16 a[190:191], v202 offset:9216\n\t" \
    "ds_read_b64_tr_b16 v[224:225], v203 offset:0\n\t" \
    "ds_read_b64_tr_b16 v[226:227], v203 offset:1024\n\t" \
    "ds_read_b64_tr_b16 v[228:229], v203 offset:8192\n\t" \
    "ds_read_b64_tr_b16 v[230:231], v203 offset:9216\n\t" \
    "s_waitcnt vmcnt(10) lgkmcnt(0)\n\t" \
    "s_barrier\n\t" \
    "ds_read_b64_tr_b16 a[192:193], v202 offset:16384\n\t" \
    "ds_read_b64_tr_b16 a[194:195], v202 offset:17408\n\t" \
    "ds_read_b64_tr_b16 a[196:197], v202 offset:24576\n\t" \
    "ds_read_b64_tr_b16 a[198:199], v202 offset:25600\n\t" \
    "ds_read_b64_tr_b16 v[232:233], v203 offset:16384\n\t" \
    "ds_read_b64_tr_b16 v[234:235], v203 offset:17408\n\t" \
    "ds_read_b64_tr_b16 v[236:237], v203 offset:24576\n\t" \
    "ds_read_b64_tr_b16 v[238:239], v203 offset:25600\n\t" \
    "s_waitcnt vmcnt(6) lgkmcnt(0)\n\t" \
    "s_barrier\n\t" \
    "ds_read_b64_tr_b16 a[200:201], v202 offset:32768\n\t" \
    "ds_read_b64_tr_b16 a[202:203], v202 offset:33792\n\t" \
    "ds_read_b64_tr_b16 a[204:205], v202 offset:40960\n\t" \
    "ds_read_b64_tr_b16 a[206:207], v202 offset:41984\n\t" \
    "ds_read_b64_tr_b16 v[240:241], v203 offset:32768\n\t" \
    "ds_read_b64_tr_b16 v[242:243], v203 offset:33792\n\t" \
    "ds_read_b64_tr_b16 v[244:245], v203 offset:40960\n\t" \
    "ds_read_b64_tr_b16 v[246:247], v203 offset:41984\n\t" \
    "s_waitcnt vmcnt(2) lgkmcnt(0)\n\t" \
    "s_barrier\n\t" \
    "ds_read_b64_tr_b16 a[208:209], v202 offset:49152\n\t" \
    "ds_read_b64_tr_b16 a[210:211], v202 offset:50176\n\t" \
    "ds_read_b64_tr_b16 a[212:213], v202 offset:57344\n\t" \
    "ds_read_b64_tr_b16 a[214:215], v202 offset:58368\n\t" \
    "ds_read_b64_tr_b16 v[248:249], v203 offset:49152\n\t" \
    "ds_read_b64_tr_b16 v[250:251], v203 offset:50176\n\t" \
    "ds_read_b64_tr_b16 v[252:253], v203 offset:57344\n\t" \
    "ds_read_b64_tr_b16 v[254:255], v203 offset:58368\n\t" \
    "s_waitcnt vmcnt(0) lgkmcnt(0)\n\t" \
    "s_barrier\n\t" \
    "s_mul_i32 s70, s67, 7\n\t" \
    "s_add_u32 s69, s65, 0\n\t" \
    "s_xor_b32 s69, s69, s70\n\t" \
    "s_and_b32 s69, s69, 63\n\t" \
    "s_lshl_b32 s69, s69, 13\n\t" \
    "s_mul_i32 s70, s67, 524288\n\t" \
    "s_add_u32 s69, s69, s70\n\t" \
    "s_add_u32 s69, s69, 23068672\n\t" \
    "s_add_u32 s50, s36, s69\n\t" \
    "s_addc_u32 s51, s37, 0\n\t" \
    "v_sub_u32 v162, v162, v161\n\t" \
    "v_cmp_ne_u32 vcc, 1, v162\n\t" \
    "s_cbranch_vccz qk_Cok4_%=\n\t" \
    "qk_C3_%=:\n\t" \
    "global_load_dwordx2 v[162:163], v160, s[36:37] sc1\n\t" \
    "s_waitcnt vmcnt(0)\n\t" \
    "v_sub_u32 v162, v162, v161\n\t" \
    "v_cmp_ne_u32 vcc, 1, v162\n\t" \
    "s_cbranch_vccz qk_Cok4_%=\n\t" \
    "s_cmp_eq_u32 s68, 0\n\t" \
    "s_cbranch_scc1 qk_F5_%=\n\t" \
    "s_sub_u32 s68, s68, 1\n\t" \
    "s_sleep 4\n\t" \
    "s_branch qk_C3_%=\n\t" \
    "qk_F5_%=:\n\t" \
    "v_mov_b32 v164, 6095104\n\t" \
    "v_mov_b32 v165, 4\n\t" \
    "global_store_dword v164, v165, s[36:37] sc1\n\t" \
    "s_waitcnt vmcnt(0)\n\t" \
    "s_branch qk_Cok4_%=\n\t" \
    "qk_Cok4_%=:\n\t" \
    "s_cmp_lg_u32 s66, 0\n\t" \
    "s_cbranch_scc1 qk_Ca6_%=\n\t" \
    "v_mov_b32 v160, 6176768\n\t" \
    "s_mov_b64 exec, 1\n\t" \
    "global_atomic_add v160, v162, s[36:37] sc1\n\t" \
    "s_mov_b64 exec, -1\n\t" \
    "qk_Ca6_%=:\n\t" \
    "v_min_u32_dpp v163, v163, v163 row_shl:6 row_mask:0xf bank_mask:0xf\n\t" \
    "s_nop 0\n\t" \
    "v_readlane_b32 s69, v163, 0\n\t" \
    "v_readlane_b32 s70, v163, 1\n\t" \
    "s_nop 1\n\t" \
    "s_cmp_lt_u32 s70, s69\n\t" \
    "s_cselect_b32 s62, 8192, 0\n\t" \
    "s_cselect_b32 s69, s70, s69\n\t" \
    "s_mov_b32 s71, s69\n\t" \
    "v_readlane_b32 s69, v163, 2\n\t" \
    "v_readlane_b32 s70, v163, 3\n\t" \
    "s_nop 1\n\t" \
    "s_cmp_lt_u32 s70, s69\n\t" \
    "s_cselect_b32 s63, 8192, 0\n\t" \
    "s_cselect_b32 s69, s70, s69\n\t" \
    "s_max_u32 s71, s69, s71\n\t" \
    "v_readlane_b32 s69, v163, 4\n\t" \
    "v_readlane_b32 s70, v163, 5\n\t" \
    "s_nop 1\n\t" \
    "s_cmp_lt_u32 s70, s69\n\t" \
    "s_cselect_b32 s64, 8192, 0\n\t" \
    "s_cselect_b32 s69, s70, s69\n\t" \
    "s_max_u32 s71, s69, s71\n\t" \
    "s_add_u32 s63, s63, 0x200000\n\t" \
    "s_xor_b32 s52, s64, 8192\n\t" \
    "s_cmp_gt_u32 s71, 625\n\t" \
    "s_cselect_b32 s69, 1, 0\n\t" \
    "s_mul_i32 s70, s69, 160\n\t" \
    "s_add_u32 s86, s86, s70\n\t" \
    "s_mul_i32 s70, s69, 536\n\t" \
    "s_add_u32 s87, s87, s70\n\t" \
    "s_mul_i32 s70, s69, 256\n\t" \
    "s_add_u32 s88, s88, s70\n\t" \
    "v_add_u32 v14, s52, v14\n\t" \
    "v_add_u32 v24, s52, v24\n\t" \
    "v_mov_b32 v26, 0x3db504f3\n\t" \
    "v_mov_b32 v28, 0x3fb8aa3b\n\t" \
    "v_mov_b32 v27, 0x3db504f3\n\t" \
    "v_mov_b32 v29, 0x3fb8aa3b\n\t" \
    "v_mov_b32 v29, 0x3e0293ee\n\t" \
    "s_mov_b32 s48, 0xaaaaaaaa\n\t" \
    "s_mov_b32 s49, 0xaaaaaaaa\n\t" \
    "s_mov_b32 s58, 0x00ff00ff\n\t" \
    "s_mov_b32 s59, 0x00ff00ff\n\t" \
    "s_lshl_b32 s69, s66, 2\n\t" \
    "v_mov_b32 v30, s69\n\t" \
    "v_mov_b32 v31, 0\n\t" \
    "v_and_b32 v33, 12, v22\n\t" \
    "ds_write_b32 v30, v31 offset:20496\n\t" \
    "s_waitcnt lgkmcnt(0)\n\t" \
    "s_mov_b64 s[40:41], s[36:37]\n\t" \
    "s_mov_b64 s[44:45], 0\n\t" \
    "s_barrier\n\t" \
    "s_mov_b32 s71, 3\n\t" \
    "qk_E7_%=:\n\t" \
    "s_memtime s[54:55]\n\t" \
    "s_nop 0\n\t" \
    "s_memrealtime s[4:5]\n\t" \
    "s_nop 0\n\t" \
    "s_memrealtime s[6:7]\n\t" \
    "s_nop 0\n\t" \
    "s_memrealtime s[8:9]\n\t" \
    "s_nop 0\n\t" \
    "s_memrealtime s[10:11]\n\t" \
    "s_nop 0\n\t" \
    "s_memrealtime s[12:13]\n\t" \
    "s_nop 0\n\t" \
    "s_memrealtime s[14:15]\n\t" \
    "s_nop 0\n\t" \
    "s_memrealtime s[16:17]\n\t" \
    "s_nop 0\n\t" \
    "s_memrealtime s[18:19]\n\t" \
    "s_nop 0\n\t" \
    "s_memrealtime s[20:21]\n\t" \
    "s_nop 0\n\t" \
    "s_memrealtime s[22:23]\n\t" \
    "s_nop 0\n\t" \
    "s_memrealtime s[24:25]\n\t" \
    "s_nop 0\n\t" \
    "s_memrealtime s[26:27]\n\t" \
    "s_nop 0\n\t" \
    "s_memrealtime s[28:29]\n\t" \
    "s_nop 0\n\t" \
    "s_memtime s[56:57]\n\t" \
    "s_mov_b32 s70, 1\n\t" \
    "s_sub_u32 s71, s71, 1\n\t" \
    "s_cselect_b32 s69, 0, -1\n\t" \
    "s_waitcnt lgkmcnt(0)\n\t" \
    "s_sub_u32 s54, s56, s54\n\t" \
    "s_and_b32 s54, s54, s69\n\t" \
    "s_cmp_gt_u32 s54, 124\n\t" \
    "s_cbranch_scc1 qk_E7_%=\n\t" \
    "s_cmp_eq_u32 s4, s28\n\t" \
    "s_addc_u32 s70, s70, 0\n\t" \
    "s_cmp_eq_u32 s6, s28\n\t" \
    "s_addc_u32 s70, s70, 0\n\t" \
    "s_cmp_eq_u32 s8, s28\n\t" \
    "s_addc_u32 s70, s70, 0\n\t" \
    "s_cmp_eq_u32 s10, s28\n\t" \
    "s_addc_u32 s70, s70, 0\n\t" \
    "s_cmp_eq_u32 s12, s28\n\t" \
    "s_addc_u32 s70, s70, 0\n\t" \
    "s_cmp_eq_u32 s14, s28\n\t" \
    "s_addc_u32 s70, s70, 0\n\t" \
    "s_cmp_eq_u32 s16, s28\n\t" \
    "s_addc_u32 s70, s70, 0\n\t" \
    "s_cmp_eq_u32 s18, s28\n\t" \
    "s_addc_u32 s70, s70, 0\n\t" \
    "s_cmp_eq_u32 s20, s28\n\t" \
    "s_addc_u32 s70, s70, 0\n\t" \
    "s_cmp_eq_u32 s22, s28\n\t" \
    "s_addc_u32 s70, s70, 0\n\t" \
    "s_cmp_eq_u32 s24, s28\n\t" \
    "s_addc_u32 s70, s70, 0\n\t" \
    "s_cmp_eq_u32 s26, s28\n\t" \
    "s_addc_u32 s70, s70, 0\n\t" \
    "s_mov_b32 s75, s73\n\t" \
    "s_mov_b32 s76, s74\n\t" \
    "s_lshl_b32 s70, s70, 3\n\t" \
    "s_sub_u32 s73, s56, s70\n\t" \
    "s_lshl_b32 s74, s28, 4\n\t" \
    "s_sub_u32 s69, s73, s75\n\t" \
    "s_sub_u32 s70, s74, s76\n\t" \
    "s_sub_u32 s71, s70, 1600\n\t" \
    "s_cmp_lt_u32 s71, 400000\n\t" \
    "s_cbranch_scc0 qk_K9_%=\n\t" \
    "v_cvt_f32_u32 v160, s69\n\t" \
    "v_cvt_f32_u32 v161, s70\n\t" \
    "v_rcp_f32 v162, v160\n\t" \
    "v_rcp_f32 v163, v161\n\t" \
    "v_mul_f32 v162, v162, v161\n\t" \
    "v_mul_f32 v163, v163, v160\n\t" \
    "v_mul_f32 v162, 0x4f800000, v162\n\t" \
    "v_mul_f32 v163, 0x4f000000, v163\n\t" \
    "v_cvt_u32_f32 v162, v162\n\t" \
    "v_cvt_u32_f32 v163, v163\n\t" \
    "v_readfirstlane_b32 s69, v162\n\t" \
    "v_readfirstlane_b32 s70, v163\n\t" \
    "s_nop 1\n\t" \
    "s_mov_b32 s78, s69\n\t" \
    "s_mov_b32 s77, s70\n\t" \
    "qk_K9_%=:\n\t" \
    "s_mov_b32 s79, s74\n\t" \
    "s_add_u32 s80, s74, 2400\n\t" \
    "s_cmp_eq_u32 s66, 1\n\t" \
    "s_cbranch_scc1 qk_loop11_%=\n\t" \
    "s_cmp_eq_u32 s66, 2\n\t" \
    "s_cbranch_scc1 qk_loop12_%=\n\t" \
    "s_cmp_eq_u32 s66, 3\n\t" \
    "s_cbranch_scc1 qk_loop13_%=\n\t" \
    "qk_loop10_%=:\n\t" \
    "v_mov_b32 v72, 0\n\t" \
    "v_mov_b32 v73, 0\n\t" \
    "v_mov_b32 v74, 0\n\t" \
    "v_mov_b32 v75, 0\n\t" \
    "v_mov_b32 v76, 0\n\t" \
    "v_mov_b32 v77, 0\n\t" \
    "v_mov_b32 v78, 0\n\t" \
    "v_mov_b32 v79, 0\n\t" \
    "v_mov_b32 v80, 0\n\t" \
    "v_mov_b32 v81, 0\n\t" \
    "v_mov_b32 v82, 0\n\t" \
    "v_mov_b32 v83, 0\n\t" \
    "v_mov_b32 v84, 0\n\t" \
    "v_mov_b32 v85, 0\n\t" \
    "v_mov_b32 v86, 0\n\t" \
    "v_mov_b32 v87, 0\n\t" \
    "v_mov_b32 v88, 0\n\t" \
    "v_mov_b32 v89, 0\n\t" \
    "v_mov_b32 v90, 0\n\t" \
    "v_mov_b32 v91, 0\n\t" \
    "v_mov_b32 v92, 0\n\t" \
    "v_mov_b32 v93, 0\n\t" \
    "v_mov_b32 v94, 0\n\t" \
    "v_mov_b32 v95, 0\n\t" \
    "v_mov_b32 v96, 0\n\t" \
    "v_mov_b32 v97, 0\n\t" \
    "v_mov_b32 v98, 0\n\t" \
    "v_mov_b32 v99, 0\n\t" \
    "v_add_u32 v31, 1, v31\n\t" \
    "s_add_u32 s69, s65, 0\n\t" \
    "s_bitcmp1_b32 s69, 0\n\t" \
    "s_cselect_b32 s70, s63, s62\n\t" \
    "s_bfe_u32 s69, s69, 0x20001\n\t" \
    "s_lshl_b32 s69, s69, 19\n\t" \
    "s_add_u32 s69, s69, s70\n\t" \
    "s_add_u32 s46, s36, s69\n\t" \
    "s_addc_u32 s47, s37, 0\n\t" \
    "s_add_u32 s69, s65, 3\n\t" \
    "s_cmp_ge_i32 s83, s69\n\t" \
    "s_cselect_b64 vcc, -1, 0\n\t" \
    "v_mov_b32 v9, 6233984\n\t" \
    "v_cndmask_b32 v9, v24, v9, vcc\n\t" \
    "s_mul_i32 s70, s67, 7\n\t" \
    "s_add_u32 s69, s65, 1\n\t" \
    "s_xor_b32 s69, s69, s70\n\t" \
    "s_and_b32 s69, s69, 63\n\t" \
    "s_lshl_b32 s69, s69, 13\n\t" \
    "s_mul_i32 s70, s67, 524288\n\t" \
    "s_add_u32 s69, s69, s70\n\t" \
    "s_add_u32 s69, s69, 23068672\n\t" \
    "s_add_u32 s38, s36, s69\n\t" \
    "s_addc_u32 s39, s37, 0\n\t" \
    "qk_go19_%=:\n\t" \
    "s_sub_u32 s70, s79, s74\n\t" \
    "s_max_i32 s70, s70, 0\n\t" \
    "s_min_i32 s70, s70, 16000\n\t" \
    "s_lshl_b32 s70, s70, 1\n\t" \
    "s_mul_hi_u32 s70, s70, s77\n\t" \
    "s_add_u32 s70, s70, s73\n\t" \
    "s_sub_u32 s70, s70, 100\n\t" \
    "s_getpc_b64 s[30:31]\n\t" \
    "qk_Wp21_%=:\n\t" \
    "s_add_u32 s30, s30, qk_We22_%=-qk_Wp21_%=\n\t" \
    "s_addc_u32 s31, s31, 0\n\t" \
    "qk_W20_%=:\n\t" \
    "s_memtime s[56:57]\n\t" \
    "s_waitcnt lgkmcnt(0)\n\t" \
    "s_sub_u32 s69, s70, s56\n\t" \
    "s_cmp_gt_i32 s69, 111\n\t" \
    "s_cbranch_scc1 qk_W20_%=\n\t" \
    "s_max_i32 s69, s69, 0\n\t" \
    "s_lshr_b32 s69, s69, 1\n\t" \
    "s_and_b32 s69, s69, 0x3c\n\t" \
    "s_sub_u32 s30, s30, s69\n\t" \
    "s_subb_u32 s31, s31, 0\n\t" \
    "s_setpc_b64 s[30:31]\n\t" \
    ".p2align 6\n\t" \
    "s_nop 0\n\t" \
    "s_nop 0\n\t" \
    "s_nop 0\n\t" \
    "s_nop 1\n\t" \
    "s_nop 1\n\t" \
    "s_nop 1\n\t" \
    "s_nop 1\n\t" \
    "s_nop 1\n\t" \
    "s_nop 1\n\t" \
    "s_nop 1\n\t" \
    "s_nop 1\n\t" \
    "s_nop 1\n\t" \
    "s_nop 1\n\t" \
    "s_nop 1\n\t" \
    "s_nop 1\n\t" \
    "s_nop 1\n\t" \
    "qk_We22_%=:\n\t" \
    "qk_G23_%=:\n\t" \
    "global_load_dwordx4 v[36:39], v1, s[46:47] sc1\n\t" \
    "s_nop 9\n\t" \
    "global_load_dwordx4 v[40:43], v1, s[46:47] offset:1024 sc1\n\t" \
    "s_nop 9\n\t" \
    "global_load_dwordx4 v[44:47], v2, s[46:47] sc1\n\t" \
    "s_waitcnt vmcnt(2)\n\t" \
    "v_smfmac_f32_16x16x64_bf16 v[72:75], v[36:39], a[0:7], v10\n\t" \
    "ds_write_b128 v0, v[36:39] offset:0\n\t" \
    "v_smfmac_f32_16x16x64_bf16 v[76:79], v[36:39], a[72:79], v10\n\t" \
    "v_max3_u32 v160, v36, v37, v38\n\t" \
    "v_smfmac_f32_16x16x64_bf16 v[80:83], v[36:39], a[144:151], v10\n\t" \
    "v_smfmac_f32_16x16x64_bf16 v[84:87], v[36:39], a[216:223], v10\n\t" \
    "s_waitcnt vmcnt(1)\n\t" \
    "ds_write_b128 v0, v[40:43] offset:1024\n\t" \
    "v_smfmac_f32_16x16x64_bf16 v[72:75], v[40:43], a[8:15], v10\n\t" \
    "v_max3_u32 v160, v160, v39, v40\n\t" \
    "v_max3_u32 v160, v160, v41, v42\n\t" \
    "v_smfmac_f32_16x16x64_bf16 v[76:79], v[40:43], a[80:87], v10\n\t" \
    "v_max_u32 v160, v160, v43\n\t" \
    "v_cmp_eq_u32 vcc, -1, v160\n\t" \
    "v_smfmac_f32_16x16x64_bf16 v[80:83], v[40:43], a[152:159], v10\n\t" \
    "v_smfmac_f32_16x16x64_bf16 v[84:87], v[40:43], a[224:231], v10\n\t" \
    "s_cbranch_vccnz qk_Gbad24_%=\n\t" \
    "qk_Gc26_%=:\n\t" \
    "s_waitcnt lgkmcnt(0)\n\t" \
    "s_barrier\n\t" \
    "ds_read_b128 v[48:51], v0 offset:2048\n\t" \
    "ds_read_b128 v[52:55], v0 offset:3072\n\t" \
    "s_waitcnt vmcnt(0)\n\t" \
    "v_smfmac_f32_16x16x64_bf16 v[72:75], v[44:47], a[64:71], v10\n\t" \
    "ds_read_b128 v[56:59], v0 offset:4096\n\t" \
    "v_max3_u32 v160, v44, v45, v46\n\t" \
    "v_smfmac_f32_16x16x64_bf16 v[76:79], v[44:47], a[136:143], v10\n\t" \
    "ds_read_b128 v[60:63], v0 offset:5120\n\t" \
    "v_max_u32 v160, v160, v47\n\t" \
    "v_smfmac_f32_16x16x64_bf16 v[80:83], v[44:47], a[208:215], v10\n\t" \
    "ds_read_b128 v[64:67], v0 offset:6144\n\t" \
    "v_cmp_eq_u32 vcc, -1, v160\n\t" \
    "v_smfmac_f32_16x16x64_bf16 v[84:87], v[44:47], v[248:255], v10\n\t" \
    "ds_read_b128 v[68:71], v0 offset:7168\n\t" \
    "s_cbranch_vccnz qk_Gbad225_%=\n\t" \
    "qk_Gc227_%=:\n\t" \
    "s_waitcnt lgkmcnt(5)\n\t" \
    "v_smfmac_f32_16x16x64_bf16 v[72:75], v[48:51], a[16:23], v10\n\t" \
    "s_waitcnt lgkmcnt(4)\n\t" \
    "v_smfmac_f32_16x16x64_bf16 v[72:75], v[52:55], a[24:31], v10\n\t" \
    "s_waitcnt lgkmcnt(3)\n\t" \
    "v_smfmac_f32_16x16x64_bf16 v[72:75], v[56:59], a[32:39], v10\n\t" \
    "global_load_dword v25, v9, s[36:37] sc1\n\t" \
    "s_waitcnt lgkmcnt(2)\n\t" \
    "v_smfmac_f32_16x16x64_bf16 v[72:75], v[60:63], a[40:47], v10\n\t" \
    "s_waitcnt lgkmcnt(1)\n\t" \
    "v_smfmac_f32_16x16x64_bf16 v[72:75], v[64:67], a[48:55], v10\n\t" \
    "s_waitcnt lgkmcnt(0)\n\t" \
    "v_smfmac_f32_16x16x64_bf16 v[72:75], v[68:71], a[56:63], v10\n\t" \
    "v_smfmac_f32_16x16x64_bf16 v[76:79], v[48:51], a[88:95], v10\n\t" \
    "s_nop 1\n\t" \
    "s_nop 0\n\t" \
    "v_smfmac_f32_16x16x64_bf16 v[76:79], v[52:55], a[96:103], v10\n\t" \
    "s_nop 1\n\t" \
    "s_nop 0\n\t" \
    "v_smfmac_f32_16x16x64_bf16 v[76:79], v[56:59], a[104:111], v10\n\t" \
    "v_add_f32 v72, v72, v74\n\t" \
    "v_smfmac_f32_16x16x64_bf16 v[76:79], v[60:63], a[112:119], v10\n\t" \
    "v_add_f32 v73, v73, v75\n\t" \
    "v_smfmac_f32_16x16x64_bf16 v[76:79], v[64:67], a[120:127], v10\n\t" \
    "v_cvt_pk_bf16_f32 v156, v72, v73\n\t" \
    "v_smfmac_f32_16x16x64_bf16 v[76:79], v[68:71], a[128:135], v10\n\t" \
    "ds_write_b32 v12, v156 offset:0\n\t" \
    "v_smfmac_f32_16x16x64_bf16 v[80:83], v[48:51], a[160:167], v10\n\t" \
    "s_nop 1\n\t" \
    "s_nop 0\n\t" \
    "v_smfmac_f32_16x16x64_bf16 v[80:83], v[52:55], a[168:175], v10\n\t" \
    "s_nop 1\n\t" \
    "s_nop 0\n\t" \
    "v_smfmac_f32_16x16x64_bf16 v[80:83], v[56:59], a[176:183], v10\n\t" \
    "v_add_f32 v76, v76, v78\n\t" \
    "v_smfmac_f32_16x16x64_bf16 v[80:83], v[60:63], a[184:191], v10\n\t" \
    "v_add_f32 v77, v77, v79\n\t" \
    "v_smfmac_f32_16x16x64_bf16 v[80:83], v[64:67], a[192:199], v10\n\t" \
    "v_cvt_pk_bf16_f32 v157, v76, v77\n\t" \
    "v_smfmac_f32_16x16x64_bf16 v[80:83], v[68:71], a[200:207], v10\n\t" \
    "ds_write_b32 v12, v157 offset:64\n\t" \
    "v_smfmac_f32_16x16x64_bf16 v[84:87], v[48:51], a[232:239], v10\n\t" \
    "s_nop 1\n\t" \
    "s_nop 0\n\t" \
    "v_smfmac_f32_16x16x64_bf16 v[84:87], v[52:55], a[240:247], v10\n\t" \
    "s_nop 1\n\t" \
    "s_nop 0\n\t" \
    "v_smfmac_f32_16x16x64_bf16 v[84:87], v[56:59], a[248:255], v10\n\t" \
    "v_add_f32 v80, v80, v82\n\t" \
    "v_smfmac_f32_16x16x64_bf16 v[84:87], v[60:63], v[224:231], v10\n\t" \
    "v_add_f32 v81, v81, v83\n\t" \
    "v_smfmac_f32_16x16x64_bf16 v[84:87], v[64:67], v[232:239], v10\n\t" \
    "v_cvt_pk_bf16_f32 v158, v80, v81\n\t" \
    "v_smfmac_f32_16x16x64_bf16 v[84:87], v[68:71], v[240:247], v10\n\t" \
    "ds_write_b32 v16, v158 offset:0\n\t" \
    "s_nop 6\n\t" \
    "v_pk_add_f32 v[84:85], v[84:85], v[86:87]\n\t" \
    "v_cvt_pk_bf16_f32 v159, v84, v85\n\t" \
    "ds_write_b32 v16, v159 offset:256\n\t" \
    "s_cmp_eq_u32 s65, 0\n\t" \
    "s_cbranch_scc1 qk_k017_%=\n\t" \
    "qk_k0b18_%=:\n\t" \
    "s_waitcnt lgkmcnt(0)\n\t" \
    "s_barrier\n\t" \
    "ds_read_b64_tr_b16 v[132:133], v13 offset:0\n\t" \
    "ds_read_b64_tr_b16 v[134:135], v13 offset:128\n\t" \
    "ds_read_b64_tr_b16 v[136:137], v13 offset:1024\n\t" \
    "ds_read_b64_tr_b16 v[138:139], v13 offset:1152\n\t" \
    "ds_read_b64_tr_b16 v[140:141], v17 offset:0\n\t" \
    "ds_read_b64_tr_b16 v[142:143], v17 offset:256\n\t" \
    "ds_read_b64_tr_b16 v[144:145], v17 offset:512\n\t" \
    "ds_read_b64_tr_b16 v[146:147], v17 offset:768\n\t" \
    "ds_read_b64_tr_b16 v[148:149], v17 offset:1024\n\t" \
    "ds_read_b64_tr_b16 v[150:151], v17 offset:1280\n\t" \
    "ds_read_b64_tr_b16 v[152:153], v17 offset:1536\n\t" \
    "ds_read_b64_tr_b16 v[154:155], v17 offset:1792\n\t" \
    "s_waitcnt lgkmcnt(10)\n\t" \
    "v_smfmac_f32_16x16x64_bf16 v[88:91], v[132:135], v[100:107], v10\n\t" \
    "v_smfmac_f32_16x16x64_bf16 v[92:95], v[132:135], v[116:123], v10\n\t" \
    "s_waitcnt lgkmcnt(8)\n\t" \
    "v_smfmac_f32_16x16x64_bf16 v[88:91], v[136:139], v[108:115], v10\n\t" \
    "v_smfmac_f32_16x16x64_bf16 v[92:95], v[136:139], v[124:131], v10\n\t" \
    "s_waitcnt lgkmcnt(4)\n\t" \
    "v_smfmac_f32_16x16x64_bf16 v[96:99], v[132:135], v[140:147], v10\n\t" \
    "s_waitcnt lgkmcnt(0)\n\t" \
    "s_nop 1\n\t" \
    "v_smfmac_f32_16x16x64_bf16 v[96:99], v[136:139], v[148:155], v10\n\t" \
    "s_nop 0\n\t" \
    "v_add_f32 v168, v88, v90\n\t" \
    "v_add_f32 v169, v92, v94\n\t" \
    "v_max_f32 v178, v168, v169\n\t" \
    "v_mul_f32 v168, v168, v29\n\t" \
    "v_mul_f32 v169, v169, v29\n\t" \
    "v_max_f32_dpp v178, v178, v178 row_mirror row_mask:0xf bank_mask:0xf\n\t" \
    "s_nop 0\n\t" \
    "v_add_f32 v172, v96, v98\n\t" \
    "v_max_f32 v173, v178, v172\n\t" \
    "v_mul_f32 v172, v172, v29\n\t" \
    "s_nop 0\n\t" \
    "v_max_f32_dpp v173, v173, v173 quad_perm:[1,0,3,2] row_mask:0xf bank_mask:0xf\n\t" \
    "s_nop 1\n\t" \
    "v_max_f32_dpp v173, v173, v173 quad_perm:[2,3,0,1] row_mask:0xf bank_mask:0xf\n\t" \
    "s_nop 1\n\t" \
    "v_max_f32_dpp v173, v173, v173 row_half_mirror row_mask:0xf bank_mask:0xf\n\t" \
    "v_fma_f32 v172, -v173, v29, v172\n\t" \
    "v_fma_f32 v168, -v173, v29, v168\n\t" \
    "v_fma_f32 v169, -v173, v29, v169\n\t" \
    "v_exp_f32 v172, v172\n\t" \
    "v_exp_f32 v168, v168\n\t" \
    "v_exp_f32 v169, v169\n\t" \
    "v_fma_f32 v173, v172, 0.5, v168\n\t" \
    "v_add_f32 v173, v173, v169\n\t" \
    "v_mov_b32_dpp v174, v172 quad_perm:[0,0,2,2] row_mask:0xf bank_mask:0xf\n\t" \
    "v_mov_b32_dpp v175, v172 quad_perm:[1,1,3,3] row_mask:0xf bank_mask:0xf\n\t" \
    "v_add_f32_dpp v173, v173, v173 quad_perm:[1,0,3,2] row_mask:0xf bank_mask:0xf\n\t" \
    "v_mov_b32_dpp v176, v168 quad_perm:[1,0,3,2] row_mask:0xf bank_mask:0xf\n\t" \
    "v_mov_b32_dpp v177, v169 quad_perm:[1,0,3,2] row_mask:0xf bank_mask:0xf\n\t" \
    "v_add_f32_dpp v173, v173, v173 quad_perm:[2,3,0,1] row_mask:0xf bank_mask:0xf\n\t" \
    "v_cndmask_b32 v168, v168, v177, s[48:49]\n\t" \
    "v_cndmask_b32 v169, v176, v169, s[48:49]\n\t" \
    "v_add_f32_dpp v173, v173, v173 row_half_mirror row_mask:0xf bank_mask:0xf\n\t" \
    "s_nop 1\n\t" \
    "v_add_f32_dpp v173, v173, v173 row_mirror row_mask:0xf bank_mask:0xf\n\t" \
    "v_rcp_f32 v173, v173\n\t" \
    "s_nop 0\n\t" \
    "v_pk_mul_f32 v[168:169], v[168:169], v[172:173] op_sel:[0,1] op_sel_hi:[1,1]\n\t" \
    "v_pk_mul_f32 v[174:175], v[174:175], v[172:173] op_sel:[0,1] op_sel_hi:[1,1]\n\t" \
    "v_cvt_pk_bf16_f32 v176, v168, v169\n\t" \
    "global_store_dword v18, v176, s[46:47] sc1\n\t" \
    "global_store_dword v3, v176, s[46:47] sc1\n\t" \
    "v_cvt_pk_bf16_f32 v177, v174, v175\n\t" \
    "global_store_dword v19, v177, s[46:47] sc1\n\t" \
    "global_store_dword v4, v177, s[46:47] sc1\n\t" \
    "s_cmp_eq_u32 s65, 63\n\t" \
    "s_cbranch_scc1 qk_last14_%=\n\t" \
    "s_mov_b32 s71, 3\n\t" \
    "qk_E29_%=:\n\t" \
    "s_memtime s[54:55]\n\t" \
    "s_nop 0\n\t" \
    "s_memrealtime s[4:5]\n\t" \
    "s_nop 0\n\t" \
    "s_memrealtime s[6:7]\n\t" \
    "s_nop 0\n\t" \
    "s_memrealtime s[8:9]\n\t" \
    "s_nop 0\n\t" \
    "s_memrealtime s[10:11]\n\t" \
    "s_nop 0\n\t" \
    "s_memrealtime s[12:13]\n\t" \
    "s_nop 0\n\t" \
    "s_memrealtime s[14:15]\n\t" \
    "s_nop 0\n\t" \
    "s_memrealtime s[16:17]\n\t" \
    "s_nop 0\n\t" \
    "s_memrealtime s[18:19]\n\t" \
    "s_nop 0\n\t" \
    "s_memrealtime s[20:21]\n\t" \
    "s_nop 0\n\t" \
    "s_memrealtime s[22:23]\n\t" \
    "s_nop 0\n\t" \
    "s_memrealtime s[24:25]\n\t" \
    "s_nop 0\n\t" \
    "s_memrealtime s[26:27]\n\t" \
    "s_nop 0\n\t" \
    "s_memrealtime s[28:29]\n\t" \
    "s_nop 0\n\t" \
    "s_memtime s[56:57]\n\t" \
    "s_mov_b32 s70, 1\n\t" \
    "s_sub_u32 s71, s71, 1\n\t" \
    "s_cselect_b32 s69, 0, -1\n\t" \
    "s_waitcnt lgkmcnt(0)\n\t" \
    "s_sub_u32 s54, s56, s54\n\t" \
    "s_and_b32 s54, s54, s69\n\t" \
    "s_cmp_gt_u32 s54, 124\n\t" \
    "s_cbranch_scc1 qk_E29_%=\n\t" \
    "s_cmp_eq_u32 s4, s28\n\t" \
    "s_addc_u32 s70, s70, 0\n\t" \
    "s_cmp_eq_u32 s6, s28\n\t" \
    "s_addc_u32 s70, s70, 0\n\t" \
    "s_cmp_eq_u32 s8, s28\n\t" \
    "s_addc_u32 s70, s70, 0\n\t" \
    "s_cmp_eq_u32 s10, s28\n\t" \
    "s_addc_u32 s70, s70, 0\n\t" \
    "s_cmp_eq_u32 s12, s28\n\t" \
    "s_addc_u32 s70, s70, 0\n\t" \
    "s_cmp_eq_u32 s14, s28\n\t" \
    "s_addc_u32 s70, s70, 0\n\t" \
    "s_cmp_eq_u32 s16, s28\n\t" \
    "s_addc_u32 s70, s70, 0\n\t" \
    "s_cmp_eq_u32 s18, s28\n\t" \
    "s_addc_u32 s70, s70, 0\n\t" \
    "s_cmp_eq_u32 s20, s28\n\t" \
    "s_addc_u32 s70, s70, 0\n\t" \
    "s_cmp_eq_u32 s22, s28\n\t" \
    "s_addc_u32 s70, s70, 0\n\t" \
    "s_cmp_eq_u32 s24, s28\n\t" \
    "s_addc_u32 s70, s70, 0\n\t" \
    "s_cmp_eq_u32 s26, s28\n\t" \
    "s_addc_u32 s70, s70, 0\n\t" \
    "s_mov_b32 s75, s73\n\t" \
    "s_mov_b32 s76, s74\n\t" \
    "s_lshl_b32 s70, s70, 3\n\t" \
    "s_sub_u32 s73, s56, s70\n\t" \
    "s_lshl_b32 s74, s28, 4\n\t" \
    "s_sub_u32 s69, s73, s75\n\t" \
    "s_sub_u32 s70, s74, s76\n\t" \
    "s_sub_u32 s71, s70, 1600\n\t" \
    "s_cmp_lt_u32 s71, 400000\n\t" \
    "s_cbranch_scc0 qk_K31_%=\n\t" \
    "v_cvt_f32_u32 v160, s69\n\t" \
    "v_cvt_f32_u32 v161, s70\n\t" \
    "v_rcp_f32 v162, v160\n\t" \
    "v_rcp_f32 v163, v161\n\t" \
    "v_mul_f32 v162, v162, v161\n\t" \
    "v_mul_f32 v163, v163, v160\n\t" \
    "v_mul_f32 v162, 0x4f800000, v162\n\t" \
    "v_mul_f32 v163, 0x4f000000, v163\n\t" \
    "v_cvt_u32_f32 v162, v162\n\t" \
    "v_cvt_u32_f32 v163, v163\n\t" \
    "v_readfirstlane_b32 s69, v162\n\t" \
    "v_readfirstlane_b32 s70, v163\n\t" \
    "s_nop 1\n\t" \
    "s_sub_u32 s69, s69, s78\n\t" \
    "s_ashr_i32 s69, s69, 2\n\t" \
    "s_add_u32 s78, s78, s69\n\t" \
    "s_sub_u32 s70, s70, s77\n\t" \
    "s_ashr_i32 s70, s70, 2\n\t" \
    "s_add_u32 s77, s77, s70\n\t" \
    "qk_K31_%=:\n\t" \
    "s_waitcnt vmcnt(4)\n\t" \
    "v_readfirstlane_b32 s69, v25\n\t" \
    "s_add_u32 s71, s65, 1\n\t" \
    "s_max_i32 s83, s83, s69\n\t" \
    "qk_WF32_%=:\n\t" \
    "s_cmp_ge_i32 s83, s71\n\t" \
    "s_cbranch_scc1 qk_WFok33_%=\n\t" \
    "global_load_dword v160, v24, s[36:37] sc1\n\t" \
    "s_waitcnt vmcnt(0)\n\t" \
    "v_readfirstlane_b32 s83, v160\n\t" \
    "s_nop 1\n\t" \
    "s_cmp_ge_i32 s83, s71\n\t" \
    "s_cbranch_scc1 qk_WFok33_%=\n\t" \
    "s_cmp_eq_u32 s68, 0\n\t" \
    "s_cbranch_scc1 qk_F34_%=\n\t" \
    "s_sub_u32 s68, s68, 1\n\t" \
    "s_sleep 2\n\t" \
    "s_branch qk_WF32_%=\n\t" \
    "qk_F34_%=:\n\t" \
    "v_mov_b32 v160, 6095104\n\t" \
    "v_mov_b32 v161, 9\n\t" \
    "global_store_dword v160, v161, s[36:37] sc1\n\t" \
    "s_waitcnt vmcnt(0)\n\t" \
    "s_branch qk_WFok33_%=\n\t" \
    "qk_WFok33_%=:\n\t" \
    "v_mov_b32 v72, 0\n\t" \
    "v_mov_b32 v73, 0\n\t" \
    "v_mov_b32 v74, 0\n\t" \
    "v_mov_b32 v75, 0\n\t" \
    "v_mov_b32 v76, 0\n\t" \
    "v_mov_b32 v77, 0\n\t" \
    "v_mov_b32 v78, 0\n\t" \
    "v_mov_b32 v79, 0\n\t" \
    "v_mov_b32 v80, 0\n\t" \
    "v_mov_b32 v81, 0\n\t" \
    "v_mov_b32 v82, 0\n\t" \
    "v_mov_b32 v83, 0\n\t" \
    "v_mov_b32 v84, 0\n\t" \
    "v_mov_b32 v85, 0\n\t" \
    "v_mov_b32 v86, 0\n\t" \
    "v_mov_b32 v87, 0\n\t" \
    "v_mov_b32 v88, 0\n\t" \
    "v_mov_b32 v89, 0\n\t" \
    "v_mov_b32 v90, 0\n\t" \
    "v_mov_b32 v91, 0\n\t" \
    "v_mov_b32 v92, 0\n\t" \
    "v_mov_b32 v93, 0\n\t" \
    "v_mov_b32 v94, 0\n\t" \
    "v_mov_b32 v95, 0\n\t" \
    "v_mov_b32 v96, 0\n\t" \
    "v_mov_b32 v97, 0\n\t" \
    "v_mov_b32 v98, 0\n\t" \
    "v_mov_b32 v99, 0\n\t" \
    "s_add_u32 s79, s74, 416\n\t" \
    "s_sub_u32 s70, s79, s74\n\t" \
    "s_max_i32 s70, s70, 0\n\t" \
    "s_min_i32 s70, s70, 16000\n\t" \
    "s_lshl_b32 s70, s70, 1\n\t" \
    "s_mul_hi_u32 s70, s70, s77\n\t" \
    "s_add_u32 s70, s70, s73\n\t" \
    "qk_W35_%=:\n\t" \
    "s_memtime s[56:57]\n\t" \
    "s_waitcnt lgkmcnt(0)\n\t" \
    "s_sub_u32 s69, s70, s56\n\t" \
    "s_cmp_gt_i32 s69, 24\n\t" \
    "s_cbranch_scc1 qk_W35_%=\n\t" \
    "global_load_dwordx4 v[100:103], v23, s[38:39] offset:0 sc1\n\t" \
    "global_load_dwordx4 v[104:107], v23, s[38:39] offset:2048 sc1\n\t" \
    "global_load_dwordx4 v[108:111], v15, s[38:39] offset:0 sc1\n\t" \
    "global_load_dwordx4 v[112:115], v15, s[38:39] offset:2048 sc1\n\t" \
    "global_load_dwordx4 v[116:119], v23, s[38:39] offset:1024 sc1\n\t" \
    "global_load_dwordx4 v[120:123], v23, s[38:39] offset:3072 sc1\n\t" \
    "global_load_dwordx4 v[124:127], v15, s[38:39] offset:1024 sc1\n\t" \
    "global_load_dwordx4 v[128:131], v15, s[38:39] offset:3072 sc1\n\t" \
    "s_add_u32 s65, s65, 1\n\t" \
    "v_add_u32 v31, 1, v31\n\t" \
    "s_add_u32 s69, s65, 0\n\t" \
    "s_bitcmp1_b32 s69, 0\n\t" \
    "s_cselect_b32 s70, s63, s62\n\t" \
    "s_bfe_u32 s69, s69, 0x20001\n\t" \
    "s_lshl_b32 s69, s69, 19\n\t" \
    "s_add_u32 s69, s69, s70\n\t" \
    "s_add_u32 s46, s36, s69\n\t" \
    "s_addc_u32 s47, s37, 0\n\t" \
    "s_add_u32 s69, s65, 3\n\t" \
    "s_cmp_ge_i32 s83, s69\n\t" \
    "s_cselect_b64 vcc, -1, 0\n\t" \
    "v_mov_b32 v9, 6233984\n\t" \
    "v_cndmask_b32 v9, v24, v9, vcc\n\t" \
    "s_mul_i32 s70, s67, 7\n\t" \
    "s_add_u32 s69, s65, 1\n\t" \
    "s_xor_b32 s69, s69, s70\n\t" \
    "s_and_b32 s69, s69, 63\n\t" \
    "s_lshl_b32 s69, s69, 13\n\t" \
    "s_mul_i32 s70, s67, 524288\n\t" \
    "s_add_u32 s69, s69, s70\n\t" \
    "s_add_u32 s69, s69, 23068672\n\t" \
    "s_add_u32 s38, s36, s69\n\t" \
    "s_addc_u32 s39, s37, 0\n\t" \
    "s_waitcnt lgkmcnt(0)\n\t" \
    "s_barrier\n\t" \
    "ds_read_b32 v160, v11 offset:20480\n\t" \
    "s_waitcnt lgkmcnt(0)\n\t" \
    "v_readfirstlane_b32 s81, v160\n\t" \
    "s_nop 1\n\t" \
    "s_mul_hi_u32 s69, s85, s78\n\t" \
    "s_add_u32 s79, s81, s69\n\t" \
    "s_add_u32 s79, s79, s87\n\t" \
    "s_cmp_ge_u32 s65, 37\n\t" \
    "s_cselect_b32 s69, 28, 0\n\t" \
    "s_sub_u32 s79, s79, s69\n\t" \
    "s_cmp_lt_u32 s65, 9\n\t" \
    "s_cselect_b32 s69, 96, 0\n\t" \
    "s_add_u32 s79, s79, s69\n\t" \
    "s_cmp_ge_u32 s65, 58\n\t" \
    "s_cselect_b32 s69, 96, 0\n\t" \
    "s_add_u32 s79, s79, s69\n\t" \
    "s_add_u32 s80, s79, s88\n\t" \
    "s_branch qk_go19_%=\n\t" \
    "qk_Gbad24_%=:\n\t" \
    "s_waitcnt vmcnt(0) lgkmcnt(0)\n\t" \
    "s_nop 7\n\t" \
    "v_mov_b32 v72, 0\n\t" \
    "v_mov_b32 v73, 0\n\t" \
    "v_mov_b32 v74, 0\n\t" \
    "v_mov_b32 v75, 0\n\t" \
    "v_mov_b32 v76, 0\n\t" \
    "v_mov_b32 v77, 0\n\t" \
    "v_mov_b32 v78, 0\n\t" \
    "v_mov_b32 v79, 0\n\t" \
    "v_mov_b32 v80, 0\n\t" \
    "v_mov_b32 v81, 0\n\t" \
    "v_mov_b32 v82, 0\n\t" \
    "v_mov_b32 v83, 0\n\t" \
    "v_mov_b32 v84, 0\n\t" \
    "v_mov_b32 v85, 0\n\t" \
    "v_mov_b32 v86, 0\n\t" \
    "v_mov_b32 v87, 0\n\t" \
    "s_cmp_eq_u32 s68, 0\n\t" \
    "s_cbranch_scc1 qk_F36_%=\n\t" \
    "s_sub_u32 s68, s68, 1\n\t" \
    "s_branch qk_G23_%=\n\t" \
    "qk_F36_%=:\n\t" \
    "v_mov_b32 v160, 6095104\n\t" \
    "v_mov_b32 v161, 1\n\t" \
    "global_store_dword v160, v161, s[36:37] sc1\n\t" \
    "s_waitcnt vmcnt(0)\n\t" \
    "s_branch qk_Gc26_%=\n\t" \
    "qk_Gbad225_%=:\n\t" \
    "global_load_dwordx4 v[44:47], v2, s[46:47] sc1\n\t" \
    "s_waitcnt vmcnt(0)\n\t" \
    "v_max3_u32 v160, v44, v45, v46\n\t" \
    "v_max_u32 v160, v160, v47\n\t" \
    "v_cmp_eq_u32 vcc, -1, v160\n\t" \
    "s_cbranch_vccz qk_Gre228_%=\n\t" \
    "s_cmp_eq_u32 s68, 0\n\t" \
    "s_cbranch_scc1 qk_F37_%=\n\t" \
    "s_sub_u32 s68, s68, 1\n\t" \
    "s_sleep 1\n\t" \
    "s_branch qk_Gbad225_%=\n\t" \
    "qk_F37_%=:\n\t" \
    "v_mov_b32 v160, 6095104\n\t" \
    "v_mov_b32 v161, 2\n\t" \
    "global_store_dword v160, v161, s[36:37] sc1\n\t" \
    "s_waitcnt vmcnt(0)\n\t" \
    "s_branch qk_Gre228_%=\n\t" \
    "qk_Gre228_%=:\n\t" \
    "v_mov_b32 v72, 0\n\t" \
    "v_mov_b32 v73, 0\n\t" \
    "v_mov_b32 v74, 0\n\t" \
    "v_mov_b32 v75, 0\n\t" \
    "v_mov_b32 v76, 0\n\t" \
    "v_mov_b32 v77, 0\n\t" \
    "v_mov_b32 v78, 0\n\t" \
    "v_mov_b32 v79, 0\n\t" \
    "v_mov_b32 v80, 0\n\t" \
    "v_mov_b32 v81, 0\n\t" \
    "v_mov_b32 v82, 0\n\t" \
    "v_mov_b32 v83, 0\n\t" \
    "v_mov_b32 v84, 0\n\t" \
    "v_mov_b32 v85, 0\n\t" \
    "v_mov_b32 v86, 0\n\t" \
    "v_mov_b32 v87, 0\n\t" \
    "v_smfmac_f32_16x16x64_bf16 v[72:75], v[36:39], a[0:7], v10\n\t" \
    "v_smfmac_f32_16x16x64_bf16 v[76:79], v[36:39], a[72:79], v10\n\t" \
    "v_smfmac_f32_16x16x64_bf16 v[80:83], v[36:39], a[144:151], v10\n\t" \
    "v_smfmac_f32_16x16x64_bf16 v[84:87], v[36:39], a[216:223], v10\n\t" \
    "v_smfmac_f32_16x16x64_bf16 v[72:75], v[40:43], a[8:15], v10\n\t" \
    "v_smfmac_f32_16x16x64_bf16 v[76:79], v[40:43], a[80:87], v10\n\t" \
    "v_smfmac_f32_16x16x64_bf16 v[80:83], v[40:43], a[152:159], v10\n\t" \
    "v_smfmac_f32_16x16x64_bf16 v[84:87], v[40:43], a[224:231], v10\n\t" \
    "v_smfmac_f32_16x16x64_bf16 v[72:75], v[44:47], a[64:71], v10\n\t" \
    "v_smfmac_f32_16x16x64_bf16 v[76:79], v[44:47], a[136:143], v10\n\t" \
    "v_smfmac_f32_16x16x64_bf16 v[80:83], v[44:47], a[208:215], v10\n\t" \
    "v_smfmac_f32_16x16x64_bf16 v[84:87], v[44:47], v[248:255], v10\n\t" \
    "s_branch qk_Gc227_%=\n\t" \
    "qk_k017_%=:\n\t" \
    "qk_WF38_%=:\n\t" \
    "s_cmp_ge_i32 s83, s65\n\t" \
    "s_cbranch_scc1 qk_WFok39_%=\n\t" \
    "global_load_dword v160, v24, s[36:37] sc1\n\t" \
    "s_waitcnt vmcnt(0)\n\t" \
    "v_readfirstlane_b32 s83, v160\n\t" \
    "s_nop 1\n\t" \
    "s_cmp_ge_i32 s83, s65\n\t" \
    "s_cbranch_scc1 qk_WFok39_%=\n\t" \
    "s_cmp_eq_u32 s68, 0\n\t" \
    "s_cbranch_scc1 qk_F40_%=\n\t" \
    "s_sub_u32 s68, s68, 1\n\t" \
    "s_sleep 2\n\t" \
    "s_branch qk_WF38_%=\n\t" \
    "qk_F40_%=:\n\t" \
    "v_mov_b32 v160, 6095104\n\t" \
    "v_mov_b32 v161, 8\n\t" \
    "global_store_dword v160, v161, s[36:37] sc1\n\t" \
    "s_waitcnt vmcnt(0)\n\t" \
    "s_branch qk_WFok39_%=\n\t" \
    "qk_WFok39_%=:\n\t" \
    "s_memrealtime s[54:55]\n\t" \
    "s_waitcnt lgkmcnt(0)\n\t" \
    "s_lshl_b32 s69, s54, 4\n\t" \
    "s_add_u32 s69, s69, 1280\n\t" \
    "v_mov_b32 v160, s69\n\t" \
    "global_load_dwordx4 v[100:103], v23, s[50:51] offset:0 sc1\n\t" \
    "global_load_dwordx4 v[104:107], v23, s[50:51] offset:2048 sc1\n\t" \
    "global_load_dwordx4 v[108:111], v15, s[50:51] offset:0 sc1\n\t" \
    "global_load_dwordx4 v[112:115], v15, s[50:51] offset:2048 sc1\n\t" \
    "global_load_dwordx4 v[116:119], v23, s[50:51] offset:1024 sc1\n\t" \
    "global_load_dwordx4 v[120:123], v23, s[50:51] offset:3072 sc1\n\t" \
    "global_load_dwordx4 v[124:127], v15, s[50:51] offset:1024 sc1\n\t" \
    "global_load_dwordx4 v[128:131], v15, s[50:51] offset:3072 sc1\n\t" \
    "global_store_dword v21, v160, s[46:47] sc1\n\t" \
    "s_waitcnt vmcnt(1)\n\t" \
    "s_branch qk_k0b18_%=\n\t" \
    "qk_last14_%=:\n\t" \
    "s_endpgm\n\t" \
    "qk_loop11_%=:\n\t" \
    "v_mov_b32 v72, 0\n\t" \
    "v_mov_b32 v73, 0\n\t" \
    "v_mov_b32 v74, 0\n\t" \
    "v_mov_b32 v75, 0\n\t" \
    "v_mov_b32 v76, 0\n\t" \
    "v_mov_b32 v77, 0\n\t" \
    "v_mov_b32 v78, 0\n\t" \
    "v_mov_b32 v79, 0\n\t" \
    "v_mov_b32 v80, 0\n\t" \
    "v_mov_b32 v81, 0\n\t" \
    "v_mov_b32 v82, 0\n\t" \
    "v_mov_b32 v83, 0\n\t" \
    "v_mov_b32 v84, 0\n\t" \
    "v_mov_b32 v85, 0\n\t" \
    "v_mov_b32 v86, 0\n\t" \
    "v_mov_b32 v87, 0\n\t" \
    "v_mov_b32 v88, 0\n\t" \
    "v_mov_b32 v89, 0\n\t" \
    "v_mov_b32 v90, 0\n\t" \
    "v_mov_b32 v91, 0\n\t" \
    "v_mov_b32 v92, 0\n\t" \
    "v_mov_b32 v93, 0\n\t" \
    "v_mov_b32 v94, 0\n\t" \
    "v_mov_b32 v95, 0\n\t" \
    "v_mov_b32 v96, 0\n\t" \
    "v_mov_b32 v97, 0\n\t" \
    "v_mov_b32 v98, 0\n\t" \
    "v_mov_b32 v99, 0\n\t" \
    "v_add_u32 v31, 1, v31\n\t" \
    "s_add_u32 s69, s65, 0\n\t" \
    "s_bitcmp1_b32 s69, 0\n\t" \
    "s_cselect_b32 s70, s63, s62\n\t" \
    "s_bfe_u32 s69, s69, 0x20001\n\t" \
    "s_lshl_b32 s69, s69, 19\n\t" \
    "s_add_u32 s69, s69, s70\n\t" \
    "s_add_u32 s46, s36, s69\n\t" \
    "s_addc_u32 s47, s37, 0\n\t" \
    "s_add_u32 s69, s65, 3\n\t" \
    "s_cmp_ge_i32 s83, s69\n\t" \
    "s_cselect_b64 vcc, -1, 0\n\t" \
    "v_mov_b32 v9, 6233984\n\t" \
    "v_cndmask_b32 v9, v24, v9, vcc\n\t" \
    "s_mul_i32 s70, s67, 7\n\t" \
    "s_add_u32 s69, s65, 1\n\t" \
    "s_xor_b32 s69, s69, s70\n\t" \
    "s_and_b32 s69, s69, 63\n\t" \
    "s_lshl_b32 s69, s69, 13\n\t" \
    "s_mul_i32 s70, s67, 524288\n\t" \
    "s_add_u32 s69, s69, s70\n\t" \
    "s_add_u32 s69, s69, 23068672\n\t" \
    "s_add_u32 s38, s36, s69\n\t" \
    "s_addc_u32 s39, s37, 0\n\t" \
    "qk_go46_%=:\n\t" \
    "s_sub_u32 s70, s79, s74\n\t" \
    "s_max_i32 s70, s70, 0\n\t" \
    "s_min_i32 s70, s70, 16000\n\t" \
    "s_lshl_b32 s70, s70, 1\n\t" \
    "s_mul_hi_u32 s70, s70, s77\n\t" \
    "s_add_u32 s70, s70, s73\n\t" \
    "s_sub_u32 s70, s70, 100\n\t" \
    "s_getpc_b64 s[30:31]\n\t" \
    "qk_Wp48_%=:\n\t" \
    "s_add_u32 s30, s30, qk_We49_%=-qk_Wp48_%=\n\t" \
    "s_addc_u32 s31, s31, 0\n\t" \
    "qk_W47_%=:\n\t" \
    "s_memtime s[56:57]\n\t" \
    "s_waitcnt lgkmcnt(0)\n\t" \
    "s_sub_u32 s69, s70, s56\n\t" \
    "s_cmp_gt_i32 s69, 111\n\t" \
    "s_cbranch_scc1 qk_W47_%=\n\t" \
    "s_max_i32 s69, s69, 0\n\t" \
    "s_lshr_b32 s69, s69, 1\n\t" \
    "s_and_b32 s69, s69, 0x3c\n\t" \
    "s_sub_u32 s30, s30, s69\n\t" \
    "s_subb_u32 s31, s31, 0\n\t" \
    "s_setpc_b64 s[30:31]\n\t" \
    ".p2align 6\n\t" \
    "s_nop 0\n\t" \
    "s_nop 0\n\t" \
    "s_nop 0\n\t" \
    "s_nop 1\n\t" \
    "s_nop 1\n\t" \
    "s_nop 1\n\t" \
    "s_nop 1\n\t" \
    "s_nop 1\n\t" \
    "s_nop 1\n\t" \
    "s_nop 1\n\t" \
    "s_nop 1\n\t" \
    "s_nop 1\n\t" \
    "s_nop 1\n\t" \
    "s_nop 1\n\t" \
    "s_nop 1\n\t" \
    "s_nop 1\n\t" \
    "qk_We49_%=:\n\t" \
    "qk_G50_%=:\n\t" \
    "global_load_dwordx4 v[36:39], v1, s[46:47] sc1\n\t" \
    "s_nop 9\n\t" \
    "global_load_dwordx4 v[40:43], v1, s[46:47] offset:1024 sc1\n\t" \
    "s_nop 9\n\t" \
    "global_load_dwordx4 v[44:47], v2, s[46:47] sc1\n\t" \
    "s_waitcnt vmcnt(2)\n\t" \
    "v_smfmac_f32_16x16x64_bf16 v[72:75], v[36:39], a[16:23], v10\n\t" \
    "ds_write_b128 v0, v[36:39] offset:2048\n\t" \
    "v_smfmac_f32_16x16x64_bf16 v[76:79], v[36:39], a[88:95], v10\n\t" \
    "v_max3_u32 v160, v36, v37, v38\n\t" \
    "v_smfmac_f32_16x16x64_bf16 v[80:83], v[36:39], a[160:167], v10\n\t" \
    "v_smfmac_f32_16x16x64_bf16 v[84:87], v[36:39], a[232:239], v10\n\t" \
    "s_waitcnt vmcnt(1)\n\t" \
    "ds_write_b128 v0, v[40:43] offset:3072\n\t" \
    "v_smfmac_f32_16x16x64_bf16 v[72:75], v[40:43], a[24:31], v10\n\t" \
    "v_max3_u32 v160, v160, v39, v40\n\t" \
    "v_max3_u32 v160, v160, v41, v42\n\t" \
    "v_smfmac_f32_16x16x64_bf16 v[76:79], v[40:43], a[96:103], v10\n\t" \
    "v_max_u32 v160, v160, v43\n\t" \
    "v_cmp_eq_u32 vcc, -1, v160\n\t" \
    "v_smfmac_f32_16x16x64_bf16 v[80:83], v[40:43], a[168:175], v10\n\t" \
    "v_smfmac_f32_16x16x64_bf16 v[84:87], v[40:43], a[240:247], v10\n\t" \
    "s_cbranch_vccnz qk_Gbad51_%=\n\t" \
    "qk_Gc53_%=:\n\t" \
    "s_waitcnt lgkmcnt(0)\n\t" \
    "s_barrier\n\t" \
    "ds_read_b128 v[48:51], v0 offset:4096\n\t" \
    "ds_read_b128 v[52:55], v0 offset:5120\n\t" \
    "s_waitcnt vmcnt(0)\n\t" \
    "v_smfmac_f32_16x16x64_bf16 v[72:75], v[44:47], a[64:71], v10\n\t" \
    "ds_read_b128 v[56:59], v0 offset:6144\n\t" \
    "v_max3_u32 v160, v44, v45, v46\n\t" \
    "v_smfmac_f32_16x16x64_bf16 v[76:79], v[44:47], a[136:143], v10\n\t" \
    "ds_read_b128 v[60:63], v0 offset:7168\n\t" \
    "v_max_u32 v160, v160, v47\n\t" \
    "v_smfmac_f32_16x16x64_bf16 v[80:83], v[44:47], a[208:215], v10\n\t" \
    "ds_read_b128 v[64:67], v0 offset:0\n\t" \
    "v_cmp_eq_u32 vcc, -1, v160\n\t" \
    "v_smfmac_f32_16x16x64_bf16 v[84:87], v[44:47], v[248:255], v10\n\t" \
    "ds_read_b128 v[68:71], v0 offset:1024\n\t" \
    "s_cbranch_vccnz qk_Gbad252_%=\n\t" \
    "qk_Gc254_%=:\n\t" \
    "s_waitcnt lgkmcnt(5)\n\t" \
    "v_smfmac_f32_16x16x64_bf16 v[72:75], v[48:51], a[32:39], v10\n\t" \
    "s_waitcnt lgkmcnt(4)\n\t" \
    "v_smfmac_f32_16x16x64_bf16 v[72:75], v[52:55], a[40:47], v10\n\t" \
    "s_waitcnt lgkmcnt(3)\n\t" \
    "v_smfmac_f32_16x16x64_bf16 v[72:75], v[56:59], a[48:55], v10\n\t" \
    "global_load_dword v25, v9, s[36:37] sc1\n\t" \
    "s_waitcnt lgkmcnt(2)\n\t" \
    "v_smfmac_f32_16x16x64_bf16 v[72:75], v[60:63], a[56:63], v10\n\t" \
    "s_waitcnt lgkmcnt(1)\n\t" \
    "v_smfmac_f32_16x16x64_bf16 v[72:75], v[64:67], a[0:7], v10\n\t" \
    "s_waitcnt lgkmcnt(0)\n\t" \
    "v_smfmac_f32_16x16x64_bf16 v[72:75], v[68:71], a[8:15], v10\n\t" \
    "v_smfmac_f32_16x16x64_bf16 v[76:79], v[48:51], a[104:111], v10\n\t" \
    "s_nop 1\n\t" \
    "s_nop 0\n\t" \
    "v_smfmac_f32_16x16x64_bf16 v[76:79], v[52:55], a[112:119], v10\n\t" \
    "s_nop 1\n\t" \
    "s_nop 0\n\t" \
    "v_smfmac_f32_16x16x64_bf16 v[76:79], v[56:59], a[120:127], v10\n\t" \
    "v_add_f32 v72, v72, v74\n\t" \
    "v_smfmac_f32_16x16x64_bf16 v[76:79], v[60:63], a[128:135], v10\n\t" \
    "v_add_f32 v73, v73, v75\n\t" \
    "v_smfmac_f32_16x16x64_bf16 v[76:79], v[64:67], a[72:79], v10\n\t" \
    "v_cvt_pk_bf16_f32 v156, v72, v73\n\t" \
    "v_smfmac_f32_16x16x64_bf16 v[76:79], v[68:71], a[80:87], v10\n\t" \
    "ds_write_b32 v12, v156 offset:0\n\t" \
    "v_smfmac_f32_16x16x64_bf16 v[80:83], v[48:51], a[176:183], v10\n\t" \
    "s_nop 1\n\t" \
    "s_nop 0\n\t" \
    "v_smfmac_f32_16x16x64_bf16 v[80:83], v[52:55], a[184:191], v10\n\t" \
    "s_nop 1\n\t" \
    "s_nop 0\n\t" \
    "v_smfmac_f32_16x16x64_bf16 v[80:83], v[56:59], a[192:199], v10\n\t" \
    "v_add_f32 v76, v76, v78\n\t" \
    "v_smfmac_f32_16x16x64_bf16 v[80:83], v[60:63], a[200:207], v10\n\t" \
    "v_add_f32 v77, v77, v79\n\t" \
    "v_smfmac_f32_16x16x64_bf16 v[80:83], v[64:67], a[144:151], v10\n\t" \
    "v_cvt_pk_bf16_f32 v157, v76, v77\n\t" \
    "v_smfmac_f32_16x16x64_bf16 v[80:83], v[68:71], a[152:159], v10\n\t" \
    "ds_write_b32 v12, v157 offset:64\n\t" \
    "v_smfmac_f32_16x16x64_bf16 v[84:87], v[48:51], a[248:255], v10\n\t" \
    "s_nop 1\n\t" \
    "s_nop 0\n\t" \
    "v_smfmac_f32_16x16x64_bf16 v[84:87], v[52:55], v[224:231], v10\n\t" \
    "s_nop 1\n\t" \
    "s_nop 0\n\t" \
    "v_smfmac_f32_16x16x64_bf16 v[84:87], v[56:59], v[232:239], v10\n\t" \
    "v_add_f32 v80, v80, v82\n\t" \
    "v_smfmac_f32_16x16x64_bf16 v[84:87], v[60:63], v[240:247], v10\n\t" \
    "v_add_f32 v81, v81, v83\n\t" \
    "v_smfmac_f32_16x16x64_bf16 v[84:87], v[64:67], a[216:223], v10\n\t" \
    "v_cvt_pk_bf16_f32 v158, v80, v81\n\t" \
    "v_smfmac_f32_16x16x64_bf16 v[84:87], v[68:71], a[224:231], v10\n\t" \
    "ds_write_b32 v16, v158 offset:0\n\t" \
    "s_nop 6\n\t" \
    "v_pk_add_f32 v[84:85], v[84:85], v[86:87]\n\t" \
    "v_cvt_pk_bf16_f32 v159, v84, v85\n\t" \
    "ds_write_b32 v16, v159 offset:256\n\t" \
    "s_cmp_eq_u32 s65, 0\n\t" \
    "s_cbranch_scc1 qk_k044_%=\n\t" \
    "qk_k0b45_%=:\n\t" \
    "s_waitcnt lgkmcnt(0)\n\t" \
    "s_barrier\n\t" \
    "ds_read_b64_tr_b16 v[132:133], v13 offset:0\n\t" \
    "ds_read_b64_tr_b16 v[134:135], v13 offset:128\n\t" \
    "ds_read_b64_tr_b16 v[136:137], v13 offset:1024\n\t" \
    "ds_read_b64_tr_b16 v[138:139], v13 offset:1152\n\t" \
    "ds_read_b64_tr_b16 v[140:141], v17 offset:0\n\t" \
    "ds_read_b64_tr_b16 v[142:143], v17 offset:256\n\t" \
    "ds_read_b64_tr_b16 v[144:145], v17 offset:512\n\t" \
    "ds_read_b64_tr_b16 v[146:147], v17 offset:768\n\t" \
    "ds_read_b64_tr_b16 v[148:149], v17 offset:1024\n\t" \
    "ds_read_b64_tr_b16 v[150:151], v17 offset:1280\n\t" \
    "ds_read_b64_tr_b16 v[152:153], v17 offset:1536\n\t" \
    "ds_read_b64_tr_b16 v[154:155], v17 offset:1792\n\t" \
    "s_waitcnt lgkmcnt(10)\n\t" \
    "v_smfmac_f32_16x16x64_bf16 v[88:91], v[132:135], v[100:107], v10\n\t" \
    "v_smfmac_f32_16x16x64_bf16 v[92:95], v[132:135], v[116:123], v10\n\t" \
    "s_waitcnt lgkmcnt(8)\n\t" \
    "v_smfmac_f32_16x16x64_bf16 v[88:91], v[136:139], v[108:115], v10\n\t" \
    "v_smfmac_f32_16x16x64_bf16 v[92:95], v[136:139], v[124:131], v10\n\t" \
    "s_waitcnt lgkmcnt(4)\n\t" \
    "v_smfmac_f32_16x16x64_bf16 v[96:99], v[132:135], v[140:147], v10\n\t" \
    "s_waitcnt lgkmcnt(0)\n\t" \
    "s_nop 1\n\t" \
    "v_smfmac_f32_16x16x64_bf16 v[96:99], v[136:139], v[148:155], v10\n\t" \
    "s_nop 0\n\t" \
    "v_add_f32 v168, v89, v91\n\t" \
    "v_add_f32 v169, v93, v95\n\t" \
    "v_max_f32 v178, v168, v169\n\t" \
    "v_mul_f32 v168, v168, v29\n\t" \
    "v_mul_f32 v169, v169, v29\n\t" \
    "v_max_f32_dpp v178, v178, v178 row_mirror row_mask:0xf bank_mask:0xf\n\t" \
    "s_nop 0\n\t" \
    "v_add_f32 v172, v97, v99\n\t" \
    "v_max_f32 v173, v178, v172\n\t" \
    "v_mul_f32 v172, v172, v29\n\t" \
    "s_nop 0\n\t" \
    "v_max_f32_dpp v173, v173, v173 quad_perm:[1,0,3,2] row_mask:0xf bank_mask:0xf\n\t" \
    "s_nop 1\n\t" \
    "v_max_f32_dpp v173, v173, v173 quad_perm:[2,3,0,1] row_mask:0xf bank_mask:0xf\n\t" \
    "s_nop 1\n\t" \
    "v_max_f32_dpp v173, v173, v173 row_half_mirror row_mask:0xf bank_mask:0xf\n\t" \
    "v_fma_f32 v172, -v173, v29, v172\n\t" \
    "v_fma_f32 v168, -v173, v29, v168\n\t" \
    "v_fma_f32 v169, -v173, v29, v169\n\t" \
    "v_exp_f32 v172, v172\n\t" \
    "v_exp_f32 v168, v168\n\t" \
    "v_exp_f32 v169, v169\n\t" \
    "v_fma_f32 v173, v172, 0.5, v168\n\t" \
    "v_add_f32 v173, v173, v169\n\t" \
    "v_mov_b32_dpp v174, v172 quad_perm:[0,0,2,2] row_mask:0xf bank_mask:0xf\n\t" \
    "v_mov_b32_dpp v175, v172 quad_perm:[1,1,3,3] row_mask:0xf bank_mask:0xf\n\t" \
    "v_add_f32_dpp v173, v173, v173 quad_perm:[1,0,3,2] row_mask:0xf bank_mask:0xf\n\t" \
    "v_mov_b32_dpp v176, v168 quad_perm:[1,0,3,2] row_mask:0xf bank_mask:0xf\n\t" \
    "v_mov_b32_dpp v177, v169 quad_perm:[1,0,3,2] row_mask:0xf bank_mask:0xf\n\t" \
    "v_add_f32_dpp v173, v173, v173 quad_perm:[2,3,0,1] row_mask:0xf bank_mask:0xf\n\t" \
    "v_cndmask_b32 v168, v168, v177, s[48:49]\n\t" \
    "v_cndmask_b32 v169, v176, v169, s[48:49]\n\t" \
    "v_add_f32_dpp v173, v173, v173 row_half_mirror row_mask:0xf bank_mask:0xf\n\t" \
    "s_nop 1\n\t" \
    "v_add_f32_dpp v173, v173, v173 row_mirror row_mask:0xf bank_mask:0xf\n\t" \
    "v_rcp_f32 v173, v173\n\t" \
    "s_nop 0\n\t" \
    "v_pk_mul_f32 v[168:169], v[168:169], v[172:173] op_sel:[0,1] op_sel_hi:[1,1]\n\t" \
    "v_pk_mul_f32 v[174:175], v[174:175], v[172:173] op_sel:[0,1] op_sel_hi:[1,1]\n\t" \
    "v_cvt_pk_bf16_f32 v176, v168, v169\n\t" \
    "global_store_dword v18, v176, s[46:47] sc1\n\t" \
    "global_store_dword v3, v176, s[46:47] sc1\n\t" \
    "v_cvt_pk_bf16_f32 v177, v174, v175\n\t" \
    "global_store_dword v19, v177, s[46:47] sc1\n\t" \
    "global_store_dword v4, v177, s[46:47] sc1\n\t" \
    "s_cmp_eq_u32 s65, 63\n\t" \
    "s_cbranch_scc1 qk_last41_%=\n\t" \
    "s_mov_b32 s71, 3\n\t" \
    "qk_E56_%=:\n\t" \
    "s_memtime s[54:55]\n\t" \
    "s_nop 0\n\t" \
    "s_memrealtime s[4:5]\n\t" \
    "s_nop 0\n\t" \
    "s_memrealtime s[6:7]\n\t" \
    "s_nop 0\n\t" \
    "s_memrealtime s[8:9]\n\t" \
    "s_nop 0\n\t" \
    "s_memrealtime s[10:11]\n\t" \
    "s_nop 0\n\t" \
    "s_memrealtime s[12:13]\n\t" \
    "s_nop 0\n\t" \
    "s_memrealtime s[14:15]\n\t" \
    "s_nop 0\n\t" \
    "s_memrealtime s[16:17]\n\t" \
    "s_nop 0\n\t" \
    "s_memrealtime s[18:19]\n\t" \
    "s_nop 0\n\t" \
    "s_memrealtime s[20:21]\n\t" \
    "s_nop 0\n\t" \
    "s_memrealtime s[22:23]\n\t" \
    "s_nop 0\n\t" \
    "s_memrealtime s[24:25]\n\t" \
    "s_nop 0\n\t" \
    "s_memrealtime s[26:27]\n\t" \
    "s_nop 0\n\t" \
    "s_memrealtime s[28:29]\n\t" \
    "s_nop 0\n\t" \
    "s_memtime s[56:57]\n\t" \
    "s_mov_b32 s70, 1\n\t" \
    "s_sub_u32 s71, s71, 1\n\t" \
    "s_cselect_b32 s69, 0, -1\n\t" \
    "s_waitcnt lgkmcnt(0)\n\t" \
    "s_sub_u32 s54, s56, s54\n\t" \
    "s_and_b32 s54, s54, s69\n\t" \
    "s_cmp_gt_u32 s54, 124\n\t" \
    "s_cbranch_scc1 qk_E56_%=\n\t" \
    "s_cmp_eq_u32 s4, s28\n\t" \
    "s_addc_u32 s70, s70, 0\n\t" \
    "s_cmp_eq_u32 s6, s28\n\t" \
    "s_addc_u32 s70, s70, 0\n\t" \
    "s_cmp_eq_u32 s8, s28\n\t" \
    "s_addc_u32 s70, s70, 0\n\t" \
    "s_cmp_eq_u32 s10, s28\n\t" \
    "s_addc_u32 s70, s70, 0\n\t" \
    "s_cmp_eq_u32 s12, s28\n\t" \
    "s_addc_u32 s70, s70, 0\n\t" \
    "s_cmp_eq_u32 s14, s28\n\t" \
    "s_addc_u32 s70, s70, 0\n\t" \
    "s_cmp_eq_u32 s16, s28\n\t" \
    "s_addc_u32 s70, s70, 0\n\t" \
    "s_cmp_eq_u32 s18, s28\n\t" \
    "s_addc_u32 s70, s70, 0\n\t" \
    "s_cmp_eq_u32 s20, s28\n\t" \
    "s_addc_u32 s70, s70, 0\n\t" \
    "s_cmp_eq_u32 s22, s28\n\t" \
    "s_addc_u32 s70, s70, 0\n\t" \
    "s_cmp_eq_u32 s24, s28\n\t" \
    "s_addc_u32 s70, s70, 0\n\t" \
    "s_cmp_eq_u32 s26, s28\n\t" \
    "s_addc_u32 s70, s70, 0\n\t" \
    "s_mov_b32 s75, s73\n\t" \
    "s_mov_b32 s76, s74\n\t" \
    "s_lshl_b32 s70, s70, 3\n\t" \
    "s_sub_u32 s73, s56, s70\n\t" \
    "s_lshl_b32 s74, s28, 4\n\t" \
    "s_sub_u32 s69, s73, s75\n\t" \
    "s_sub_u32 s70, s74, s76\n\t" \
    "s_sub_u32 s71, s70, 1600\n\t" \
    "s_cmp_lt_u32 s71, 400000\n\t" \
    "s_cbranch_scc0 qk_K58_%=\n\t" \
    "v_cvt_f32_u32 v160, s69\n\t" \
    "v_cvt_f32_u32 v161, s70\n\t" \
    "v_rcp_f32 v162, v160\n\t" \
    "v_rcp_f32 v163, v161\n\t" \
    "v_mul_f32 v162, v162, v161\n\t" \
    "v_mul_f32 v163, v163, v160\n\t" \
    "v_mul_f32 v162, 0x4f800000, v162\n\t" \
    "v_mul_f32 v163, 0x4f000000, v163\n\t" \
    "v_cvt_u32_f32 v162, v162\n\t" \
    "v_cvt_u32_f32 v163, v163\n\t" \
    "v_readfirstlane_b32 s69, v162\n\t" \
    "v_readfirstlane_b32 s70, v163\n\t" \
    "s_nop 1\n\t" \
    "s_sub_u32 s69, s69, s78\n\t" \
    "s_ashr_i32 s69, s69, 2\n\t" \
    "s_add_u32 s78, s78, s69\n\t" \
    "s_sub_u32 s70, s70, s77\n\t" \
    "s_ashr_i32 s70, s70, 2\n\t" \
    "s_add_u32 s77, s77, s70\n\t" \
    "qk_K58_%=:\n\t" \
    "s_waitcnt vmcnt(4)\n\t" \
    "v_readfirstlane_b32 s69, v25\n\t" \
    "s_add_u32 s71, s65, 1\n\t" \
    "s_max_i32 s83, s83, s69\n\t" \
    "qk_WF59_%=:\n\t" \
    "s_cmp_ge_i32 s83, s71\n\t" \
    "s_cbranch_scc1 qk_WFok60_%=\n\t" \
    "global_load_dword v160, v24, s[36:37] sc1\n\t" \
    "s_waitcnt vmcnt(0)\n\t" \
    "v_readfirstlane_b32 s83, v160\n\t" \
    "s_nop 1\n\t" \
    "s_cmp_ge_i32 s83, s71\n\t" \
    "s_cbranch_scc1 qk_WFok60_%=\n\t" \
    "s_cmp_eq_u32 s68, 0\n\t" \
    "s_cbranch_scc1 qk_F61_%=\n\t" \
    "s_sub_u32 s68, s68, 1\n\t" \
    "s_sleep 2\n\t" \
    "s_branch qk_WF59_%=\n\t" \
    "qk_F61_%=:\n\t" \
    "v_mov_b32 v160, 6095104\n\t" \
    "v_mov_b32 v161, 9\n\t" \
    "global_store_dword v160, v161, s[36:37] sc1\n\t" \
    "s_waitcnt vmcnt(0)\n\t" \
    "s_branch qk_WFok60_%=\n\t" \
    "qk_WFok60_%=:\n\t" \
    "v_mov_b32 v72, 0\n\t" \
    "v_mov_b32 v73, 0\n\t" \
    "v_mov_b32 v74, 0\n\t" \
    "v_mov_b32 v75, 0\n\t" \
    "v_mov_b32 v76, 0\n\t" \
    "v_mov_b32 v77, 0\n\t" \
    "v_mov_b32 v78, 0\n\t" \
    "v_mov_b32 v79, 0\n\t" \
    "v_mov_b32 v80, 0\n\t" \
    "v_mov_b32 v81, 0\n\t" \
    "v_mov_b32 v82, 0\n\t" \
    "v_mov_b32 v83, 0\n\t" \
    "v_mov_b32 v84, 0\n\t" \
    "v_mov_b32 v85, 0\n\t" \
    "v_mov_b32 v86, 0\n\t" \
    "v_mov_b32 v87, 0\n\t" \
    "v_mov_b32 v88, 0\n\t" \
    "v_mov_b32 v89, 0\n\t" \
    "v_mov_b32 v90, 0\n\t" \
    "v_mov_b32 v91, 0\n\t" \
    "v_mov_b32 v92, 0\n\t" \
    "v_mov_b32 v93, 0\n\t" \
    "v_mov_b32 v94, 0\n\t" \
    "v_mov_b32 v95, 0\n\t" \
    "v_mov_b32 v96, 0\n\t" \
    "v_mov_b32 v97, 0\n\t" \
    "v_mov_b32 v98, 0\n\t" \
    "v_mov_b32 v99, 0\n\t" \
    "s_add_u32 s79, s74, 416\n\t" \
    "s_sub_u32 s70, s79, s74\n\t" \
    "s_max_i32 s70, s70, 0\n\t" \
    "s_min_i32 s70, s70, 16000\n\t" \
    "s_lshl_b32 s70, s70, 1\n\t" \
    "s_mul_hi_u32 s70, s70, s77\n\t" \
    "s_add_u32 s70, s70, s73\n\t" \
    "qk_W62_%=:\n\t" \
    "s_memtime s[56:57]\n\t" \
    "s_waitcnt lgkmcnt(0)\n\t" \
    "s_sub_u32 s69, s70, s56\n\t" \
    "s_cmp_gt_i32 s69, 24\n\t" \
    "s_cbranch_scc1 qk_W62_%=\n\t" \
    "global_load_dwordx4 v[100:103], v23, s[38:39] offset:0 sc1\n\t" \
    "global_load_dwordx4 v[104:107], v23, s[38:39] offset:2048 sc1\n\t" \
    "global_load_dwordx4 v[108:111], v15, s[38:39] offset:0 sc1\n\t" \
    "global_load_dwordx4 v[112:115], v15, s[38:39] offset:2048 sc1\n\t" \
    "global_load_dwordx4 v[116:119], v23, s[38:39] offset:1024 sc1\n\t" \
    "global_load_dwordx4 v[120:123], v23, s[38:39] offset:3072 sc1\n\t" \
    "global_load_dwordx4 v[124:127], v15, s[38:39] offset:1024 sc1\n\t" \
    "global_load_dwordx4 v[128:131], v15, s[38:39] offset:3072 sc1\n\t" \
    "s_add_u32 s65, s65, 1\n\t" \
    "v_add_u32 v31, 1, v31\n\t" \
    "s_add_u32 s69, s65, 0\n\t" \
    "s_bitcmp1_b32 s69, 0\n\t" \
    "s_cselect_b32 s70, s63, s62\n\t" \
    "s_bfe_u32 s69, s69, 0x20001\n\t" \
    "s_lshl_b32 s69, s69, 19\n\t" \
    "s_add_u32 s69, s69, s70\n\t" \
    "s_add_u32 s46, s36, s69\n\t" \
    "s_addc_u32 s47, s37, 0\n\t" \
    "s_add_u32 s69, s65, 3\n\t" \
    "s_cmp_ge_i32 s83, s69\n\t" \
    "s_cselect_b64 vcc, -1, 0\n\t" \
    "v_mov_b32 v9, 6233984\n\t" \
    "v_cndmask_b32 v9, v24, v9, vcc\n\t" \
    "s_mul_i32 s70, s67, 7\n\t" \
    "s_add_u32 s69, s65, 1\n\t" \
    "s_xor_b32 s69, s69, s70\n\t" \
    "s_and_b32 s69, s69, 63\n\t" \
    "s_lshl_b32 s69, s69, 13\n\t" \
    "s_mul_i32 s70, s67, 524288\n\t" \
    "s_add_u32 s69, s69, s70\n\t" \
    "s_add_u32 s69, s69, 23068672\n\t" \
    "s_add_u32 s38, s36, s69\n\t" \
    "s_addc_u32 s39, s37, 0\n\t" \
    "s_waitcnt lgkmcnt(0)\n\t" \
    "s_barrier\n\t" \
    "ds_read_b32 v160, v11 offset:20480\n\t" \
    "s_waitcnt lgkmcnt(0)\n\t" \
    "v_readfirstlane_b32 s81, v160\n\t" \
    "s_nop 1\n\t" \
    "s_mul_hi_u32 s69, s85, s78\n\t" \
    "s_add_u32 s79, s81, s69\n\t" \
    "s_add_u32 s79, s79, s87\n\t" \
    "s_cmp_ge_u32 s65, 37\n\t" \
    "s_cselect_b32 s69, 28, 0\n\t" \
    "s_sub_u32 s79, s79, s69\n\t" \
    "s_cmp_lt_u32 s65, 9\n\t" \
    "s_cselect_b32 s69, 96, 0\n\t" \
    "s_add_u32 s79, s79, s69\n\t" \
    "s_cmp_ge_u32 s65, 58\n\t" \
    "s_cselect_b32 s69, 96, 0\n\t" \
    "s_add_u32 s79, s79, s69\n\t" \
    "s_add_u32 s80, s79, s88\n\t" \
    "s_branch qk_go46_%=\n\t" \
    "qk_Gbad51_%=:\n\t" \
    "s_waitcnt vmcnt(0) lgkmcnt(0)\n\t" \
    "s_nop 7\n\t" \
    "v_mov_b32 v72, 0\n\t" \
    "v_mov_b32 v73, 0\n\t" \
    "v_mov_b32 v74, 0\n\t" \
    "v_mov_b32 v75, 0\n\t" \
    "v_mov_b32 v76, 0\n\t" \
    "v_mov_b32 v77, 0\n\t" \
    "v_mov_b32 v78, 0\n\t" \
    "v_mov_b32 v79, 0\n\t" \
    "v_mov_b32 v80, 0\n\t" \
    "v_mov_b32 v81, 0\n\t" \
    "v_mov_b32 v82, 0\n\t" \
    "v_mov_b32 v83, 0\n\t" \
    "v_mov_b32 v84, 0\n\t" \
    "v_mov_b32 v85, 0\n\t" \
    "v_mov_b32 v86, 0\n\t" \
    "v_mov_b32 v87, 0\n\t" \
    "s_cmp_eq_u32 s68, 0\n\t" \
    "s_cbranch_scc1 qk_F63_%=\n\t" \
    "s_sub_u32 s68, s68, 1\n\t" \
    "s_branch qk_G50_%=\n\t" \
    "qk_F63_%=:\n\t" \
    "v_mov_b32 v160, 6095104\n\t" \
    "v_mov_b32 v161, 1\n\t" \
    "global_store_dword v160, v161, s[36:37] sc1\n\t" \
    "s_waitcnt vmcnt(0)\n\t" \
    "s_branch qk_Gc53_%=\n\t" \
    "qk_Gbad252_%=:\n\t" \
    "global_load_dwordx4 v[44:47], v2, s[46:47] sc1\n\t" \
    "s_waitcnt vmcnt(0)\n\t" \
    "v_max3_u32 v160, v44, v45, v46\n\t" \
    "v_max_u32 v160, v160, v47\n\t" \
    "v_cmp_eq_u32 vcc, -1, v160\n\t" \
    "s_cbranch_vccz qk_Gre255_%=\n\t" \
    "s_cmp_eq_u32 s68, 0\n\t" \
    "s_cbranch_scc1 qk_F64_%=\n\t" \
    "s_sub_u32 s68, s68, 1\n\t" \
    "s_sleep 1\n\t" \
    "s_branch qk_Gbad252_%=\n\t" \
    "qk_F64_%=:\n\t" \
    "v_mov_b32 v160, 6095104\n\t" \
    "v_mov_b32 v161, 2\n\t" \
    "global_store_dword v160, v161, s[36:37] sc1\n\t" \
    "s_waitcnt vmcnt(0)\n\t" \
    "s_branch qk_Gre255_%=\n\t" \
    "qk_Gre255_%=:\n\t" \
    "v_mov_b32 v72, 0\n\t" \
    "v_mov_b32 v73, 0\n\t" \
    "v_mov_b32 v74, 0\n\t" \
    "v_mov_b32 v75, 0\n\t" \
    "v_mov_b32 v76, 0\n\t" \
    "v_mov_b32 v77, 0\n\t" \
    "v_mov_b32 v78, 0\n\t" \
    "v_mov_b32 v79, 0\n\t" \
    "v_mov_b32 v80, 0\n\t" \
    "v_mov_b32 v81, 0\n\t" \
    "v_mov_b32 v82, 0\n\t" \
    "v_mov_b32 v83, 0\n\t" \
    "v_mov_b32 v84, 0\n\t" \
    "v_mov_b32 v85, 0\n\t" \
    "v_mov_b32 v86, 0\n\t" \
    "v_mov_b32 v87, 0\n\t" \
    "v_smfmac_f32_16x16x64_bf16 v[72:75], v[36:39], a[16:23], v10\n\t" \
    "v_smfmac_f32_16x16x64_bf16 v[76:79], v[36:39], a[88:95], v10\n\t" \
    "v_smfmac_f32_16x16x64_bf16 v[80:83], v[36:39], a[160:167], v10\n\t" \
    "v_smfmac_f32_16x16x64_bf16 v[84:87], v[36:39], a[232:239], v10\n\t" \
    "v_smfmac_f32_16x16x64_bf16 v[72:75], v[40:43], a[24:31], v10\n\t" \
    "v_smfmac_f32_16x16x64_bf16 v[76:79], v[40:43], a[96:103], v10\n\t" \
    "v_smfmac_f32_16x16x64_bf16 v[80:83], v[40:43], a[168:175], v10\n\t" \
    "v_smfmac_f32_16x16x64_bf16 v[84:87], v[40:43], a[240:247], v10\n\t" \
    "v_smfmac_f32_16x16x64_bf16 v[72:75], v[44:47], a[64:71], v10\n\t" \
    "v_smfmac_f32_16x16x64_bf16 v[76:79], v[44:47], a[136:143], v10\n\t" \
    "v_smfmac_f32_16x16x64_bf16 v[80:83], v[44:47], a[208:215], v10\n\t" \
    "v_smfmac_f32_16x16x64_bf16 v[84:87], v[44:47], v[248:255], v10\n\t" \
    "s_branch qk_Gc254_%=\n\t" \
    "qk_k044_%=:\n\t" \
    "qk_WF65_%=:\n\t" \
    "s_cmp_ge_i32 s83, s65\n\t" \
    "s_cbranch_scc1 qk_WFok66_%=\n\t" \
    "global_load_dword v160, v24, s[36:37] sc1\n\t" \
    "s_waitcnt vmcnt(0)\n\t" \
    "v_readfirstlane_b32 s83, v160\n\t" \
    "s_nop 1\n\t" \
    "s_cmp_ge_i32 s83, s65\n\t" \
    "s_cbranch_scc1 qk_WFok66_%=\n\t" \
    "s_cmp_eq_u32 s68, 0\n\t" \
    "s_cbranch_scc1 qk_F67_%=\n\t" \
    "s_sub_u32 s68, s68, 1\n\t" \
    "s_sleep 2\n\t" \
    "s_branch qk_WF65_%=\n\t" \
    "qk_F67_%=:\n\t" \
    "v_mov_b32 v160, 6095104\n\t" \
    "v_mov_b32 v161, 8\n\t" \
    "global_store_dword v160, v161, s[36:37] sc1\n\t" \
    "s_waitcnt vmcnt(0)\n\t" \
    "s_branch qk_WFok66_%=\n\t" \
    "qk_WFok66_%=:\n\t" \
    "global_load_dwordx4 v[100:103], v23, s[50:51] offset:0 sc1\n\t" \
    "global_load_dwordx4 v[104:107], v23, s[50:51] offset:2048 sc1\n\t" \
    "global_load_dwordx4 v[108:111], v15, s[50:51] offset:0 sc1\n\t" \
    "global_load_dwordx4 v[112:115], v15, s[50:51] offset:2048 sc1\n\t" \
    "global_load_dwordx4 v[116:119], v23, s[50:51] offset:1024 sc1\n\t" \
    "global_load_dwordx4 v[120:123], v23, s[50:51] offset:3072 sc1\n\t" \
    "global_load_dwordx4 v[124:127], v15, s[50:51] offset:1024 sc1\n\t" \
    "global_load_dwordx4 v[128:131], v15, s[50:51] offset:3072 sc1\n\t" \
    "s_waitcnt vmcnt(0)\n\t" \
    "s_branch qk_k0b45_%=\n\t" \
    "qk_last41_%=:\n\t" \
    "s_endpgm\n\t" \
    "qk_loop12_%=:\n\t" \
    "v_mov_b32 v72, 0\n\t" \
    "v_mov_b32 v73, 0\n\t" \
    "v_mov_b32 v74, 0\n\t" \
    "v_mov_b32 v75, 0\n\t" \
    "v_mov_b32 v76, 0\n\t" \
    "v_mov_b32 v77, 0\n\t" \
    "v_mov_b32 v78, 0\n\t" \
    "v_mov_b32 v79, 0\n\t" \
    "v_mov_b32 v80, 0\n\t" \
    "v_mov_b32 v81, 0\n\t" \
    "v_mov_b32 v82, 0\n\t" \
    "v_mov_b32 v83, 0\n\t" \
    "v_mov_b32 v84, 0\n\t" \
    "v_mov_b32 v85, 0\n\t" \
    "v_mov_b32 v86, 0\n\t" \
    "v_mov_b32 v87, 0\n\t" \
    "v_add_u32 v31, 1, v31\n\t" \
    "s_add_u32 s69, s65, 0\n\t" \
    "s_bitcmp1_b32 s69, 0\n\t" \
    "s_cselect_b32 s70, s63, s62\n\t" \
    "s_bfe_u32 s69, s69, 0x20001\n\t" \
    "s_lshl_b32 s69, s69, 19\n\t" \
    "s_add_u32 s69, s69, s70\n\t" \
    "s_add_u32 s46, s36, s69\n\t" \
    "s_addc_u32 s47, s37, 0\n\t" \
    "s_mul_i32 s70, s67, 7\n\t" \
    "s_add_u32 s69, s65, 0\n\t" \
    "s_xor_b32 s69, s69, s70\n\t" \
    "s_and_b32 s69, s69, 63\n\t" \
    "s_lshl_b32 s69, s69, 12\n\t" \
    "s_mul_i32 s70, s67, 262144\n\t" \
    "s_add_u32 s69, s69, s70\n\t" \
    "s_add_u32 s69, s69, 39845888\n\t" \
    "s_add_u32 s40, s36, s69\n\t" \
    "s_addc_u32 s41, s37, 0\n\t" \
    "s_sub_u32 s69, s65, 1\n\t" \
    "v_mov_b32 v25, s69\n\t" \
    "qk_go73_%=:\n\t" \
    "s_sub_u32 s70, s79, s74\n\t" \
    "s_max_i32 s70, s70, 0\n\t" \
    "s_min_i32 s70, s70, 16000\n\t" \
    "s_lshl_b32 s70, s70, 1\n\t" \
    "s_mul_hi_u32 s70, s70, s77\n\t" \
    "s_add_u32 s70, s70, s73\n\t" \
    "s_sub_u32 s70, s70, 100\n\t" \
    "s_getpc_b64 s[30:31]\n\t" \
    "qk_Wp75_%=:\n\t" \
    "s_add_u32 s30, s30, qk_We76_%=-qk_Wp75_%=\n\t" \
    "s_addc_u32 s31, s31, 0\n\t" \
    "qk_W74_%=:\n\t" \
    "s_memtime s[56:57]\n\t" \
    "s_waitcnt lgkmcnt(0)\n\t" \
    "s_sub_u32 s69, s70, s56\n\t" \
    "s_cmp_gt_i32 s69, 111\n\t" \
    "s_cbranch_scc1 qk_W74_%=\n\t" \
    "s_max_i32 s69, s69, 0\n\t" \
    "s_lshr_b32 s69, s69, 1\n\t" \
    "s_and_b32 s69, s69, 0x3c\n\t" \
    "s_sub_u32 s30, s30, s69\n\t" \
    "s_subb_u32 s31, s31, 0\n\t" \
    "s_setpc_b64 s[30:31]\n\t" \
    ".p2align 6\n\t" \
    "s_nop 0\n\t" \
    "s_nop 0\n\t" \
    "s_nop 0\n\t" \
    "s_nop 1\n\t" \
    "s_nop 1\n\t" \
    "s_nop 1\n\t" \
    "s_nop 1\n\t" \
    "s_nop 1\n\t" \
    "s_nop 1\n\t" \
    "s_nop 1\n\t" \
    "s_nop 1\n\t" \
    "s_nop 1\n\t" \
    "s_nop 1\n\t" \
    "s_nop 1\n\t" \
    "s_nop 1\n\t" \
    "s_nop 1\n\t" \
    "qk_We76_%=:\n\t" \
    "qk_G77_%=:\n\t" \
    "global_load_dwordx4 v[36:39], v1, s[46:47] sc1\n\t" \
    "s_nop 9\n\t" \
    "global_load_dwordx4 v[40:43], v1, s[46:47] offset:1024 sc1\n\t" \
    "s_nop 9\n\t" \
    "global_load_dwordx4 v[44:47], v2, s[46:47] sc1\n\t" \
    "s_waitcnt vmcnt(2)\n\t" \
    "v_smfmac_f32_16x16x64_bf16 v[72:75], v[36:39], a[32:39], v10\n\t" \
    "ds_write_b128 v0, v[36:39] offset:4096\n\t" \
    "v_smfmac_f32_16x16x64_bf16 v[76:79], v[36:39], a[104:111], v10\n\t" \
    "v_max3_u32 v160, v36, v37, v38\n\t" \
    "v_smfmac_f32_16x16x64_bf16 v[80:83], v[36:39], a[176:183], v10\n\t" \
    "v_smfmac_f32_16x16x64_bf16 v[84:87], v[36:39], a[248:255], v10\n\t" \
    "s_waitcnt vmcnt(1)\n\t" \
    "ds_write_b128 v0, v[40:43] offset:5120\n\t" \
    "v_smfmac_f32_16x16x64_bf16 v[72:75], v[40:43], a[40:47], v10\n\t" \
    "v_max3_u32 v160, v160, v39, v40\n\t" \
    "v_max3_u32 v160, v160, v41, v42\n\t" \
    "v_smfmac_f32_16x16x64_bf16 v[76:79], v[40:43], a[112:119], v10\n\t" \
    "v_max_u32 v160, v160, v43\n\t" \
    "v_cmp_eq_u32 vcc, -1, v160\n\t" \
    "v_smfmac_f32_16x16x64_bf16 v[80:83], v[40:43], a[184:191], v10\n\t" \
    "v_smfmac_f32_16x16x64_bf16 v[84:87], v[40:43], v[224:231], v10\n\t" \
    "s_cbranch_vccnz qk_Gbad78_%=\n\t" \
    "qk_Gc80_%=:\n\t" \
    "s_waitcnt lgkmcnt(0)\n\t" \
    "s_barrier\n\t" \
    "ds_read_b128 v[48:51], v0 offset:6144\n\t" \
    "ds_read_b128 v[52:55], v0 offset:7168\n\t" \
    "s_waitcnt vmcnt(0)\n\t" \
    "v_smfmac_f32_16x16x64_bf16 v[72:75], v[44:47], a[64:71], v10\n\t" \
    "ds_read_b128 v[56:59], v0 offset:0\n\t" \
    "v_max3_u32 v160, v44, v45, v46\n\t" \
    "v_smfmac_f32_16x16x64_bf16 v[76:79], v[44:47], a[136:143], v10\n\t" \
    "ds_read_b128 v[60:63], v0 offset:1024\n\t" \
    "v_max_u32 v160, v160, v47\n\t" \
    "v_smfmac_f32_16x16x64_bf16 v[80:83], v[44:47], a[208:215], v10\n\t" \
    "ds_read_b128 v[64:67], v0 offset:2048\n\t" \
    "v_cmp_eq_u32 vcc, -1, v160\n\t" \
    "v_smfmac_f32_16x16x64_bf16 v[84:87], v[44:47], v[248:255], v10\n\t" \
    "ds_read_b128 v[68:71], v0 offset:3072\n\t" \
    "s_cbranch_vccnz qk_Gbad279_%=\n\t" \
    "qk_Gc281_%=:\n\t" \
    "s_waitcnt lgkmcnt(5)\n\t" \
    "v_smfmac_f32_16x16x64_bf16 v[72:75], v[48:51], a[48:55], v10\n\t" \
    "global_store_dword v14, v25, s[36:37] sc1\n\t" \
    "s_waitcnt lgkmcnt(4)\n\t" \
    "v_smfmac_f32_16x16x64_bf16 v[72:75], v[52:55], a[56:63], v10\n\t" \
    "s_waitcnt lgkmcnt(3)\n\t" \
    "v_smfmac_f32_16x16x64_bf16 v[72:75], v[56:59], a[0:7], v10\n\t" \
    "s_waitcnt lgkmcnt(2)\n\t" \
    "v_smfmac_f32_16x16x64_bf16 v[72:75], v[60:63], a[8:15], v10\n\t" \
    "s_waitcnt lgkmcnt(1)\n\t" \
    "v_smfmac_f32_16x16x64_bf16 v[72:75], v[64:67], a[16:23], v10\n\t" \
    "s_waitcnt lgkmcnt(0)\n\t" \
    "v_smfmac_f32_16x16x64_bf16 v[72:75], v[68:71], a[24:31], v10\n\t" \
    "v_smfmac_f32_16x16x64_bf16 v[76:79], v[48:51], a[120:127], v10\n\t" \
    "s_nop 1\n\t" \
    "s_nop 0\n\t" \
    "v_smfmac_f32_16x16x64_bf16 v[76:79], v[52:55], a[128:135], v10\n\t" \
    "s_nop 1\n\t" \
    "s_nop 0\n\t" \
    "v_smfmac_f32_16x16x64_bf16 v[76:79], v[56:59], a[72:79], v10\n\t" \
    "v_add_f32 v72, v72, v74\n\t" \
    "v_smfmac_f32_16x16x64_bf16 v[76:79], v[60:63], a[80:87], v10\n\t" \
    "v_add_f32 v73, v73, v75\n\t" \
    "v_smfmac_f32_16x16x64_bf16 v[76:79], v[64:67], a[88:95], v10\n\t" \
    "v_cvt_pk_bf16_f32 v156, v72, v73\n\t" \
    "v_smfmac_f32_16x16x64_bf16 v[76:79], v[68:71], a[96:103], v10\n\t" \
    "ds_write_b32 v12, v156 offset:0\n\t" \
    "v_smfmac_f32_16x16x64_bf16 v[80:83], v[48:51], a[192:199], v10\n\t" \
    "s_nop 1\n\t" \
    "s_nop 0\n\t" \
    "v_smfmac_f32_16x16x64_bf16 v[80:83], v[52:55], a[200:207], v10\n\t" \
    "s_nop 1\n\t" \
    "s_nop 0\n\t" \
    "v_smfmac_f32_16x16x64_bf16 v[80:83], v[56:59], a[144:151], v10\n\t" \
    "v_add_f32 v76, v76, v78\n\t" \
    "v_smfmac_f32_16x16x64_bf16 v[80:83], v[60:63], a[152:159], v10\n\t" \
    "v_add_f32 v77, v77, v79\n\t" \
    "v_smfmac_f32_16x16x64_bf16 v[80:83], v[64:67], a[160:167], v10\n\t" \
    "v_cvt_pk_bf16_f32 v157, v76, v77\n\t" \
    "v_smfmac_f32_16x16x64_bf16 v[80:83], v[68:71], a[168:175], v10\n\t" \
    "ds_write_b32 v12, v157 offset:64\n\t" \
    "v_smfmac_f32_16x16x64_bf16 v[84:87], v[48:51], v[232:239], v10\n\t" \
    "s_nop 1\n\t" \
    "s_nop 0\n\t" \
    "v_smfmac_f32_16x16x64_bf16 v[84:87], v[52:55], v[240:247], v10\n\t" \
    "s_nop 1\n\t" \
    "s_nop 0\n\t" \
    "v_smfmac_f32_16x16x64_bf16 v[84:87], v[56:59], a[216:223], v10\n\t" \
    "v_add_f32 v80, v80, v82\n\t" \
    "v_smfmac_f32_16x16x64_bf16 v[84:87], v[60:63], a[224:231], v10\n\t" \
    "v_add_f32 v81, v81, v83\n\t" \
    "v_smfmac_f32_16x16x64_bf16 v[84:87], v[64:67], a[232:239], v10\n\t" \
    "v_cvt_pk_bf16_f32 v158, v80, v81\n\t" \
    "v_smfmac_f32_16x16x64_bf16 v[84:87], v[68:71], a[240:247], v10\n\t" \
    "ds_write_b32 v16, v158 offset:0\n\t" \
    "s_nop 6\n\t" \
    "v_pk_add_f32 v[84:85], v[84:85], v[86:87]\n\t" \
    "v_cvt_pk_bf16_f32 v159, v84, v85\n\t" \
    "ds_write_b32 v16, v159 offset:256\n\t" \
    "s_waitcnt lgkmcnt(0)\n\t" \
    "s_barrier\n\t" \
    "s_sleep 1\n\t" \
    "ds_read_b64_tr_b16 v[140:141], v17 offset:0\n\t" \
    "ds_read_b64_tr_b16 v[142:143], v17 offset:256\n\t" \
    "ds_read_b64_tr_b16 v[144:145], v17 offset:512\n\t" \
    "ds_read_b64_tr_b16 v[146:147], v17 offset:768\n\t" \
    "ds_read_b64_tr_b16 v[148:149], v17 offset:1024\n\t" \
    "ds_read_b64_tr_b16 v[150:151], v17 offset:1280\n\t" \
    "ds_read_b64_tr_b16 v[152:153], v17 offset:1536\n\t" \
    "ds_read_b64_tr_b16 v[154:155], v17 offset:1792\n\t" \
    "s_waitcnt lgkmcnt(0)\n\t" \
    "s_cmp_eq_u32 s65, 63\n\t" \
    "s_cbranch_scc1 qk_last68_%=\n\t" \
    "s_mov_b32 s71, 3\n\t" \
    "qk_E83_%=:\n\t" \
    "s_memtime s[54:55]\n\t" \
    "s_nop 0\n\t" \
    "s_memrealtime s[4:5]\n\t" \
    "s_nop 0\n\t" \
    "s_memrealtime s[6:7]\n\t" \
    "s_nop 0\n\t" \
    "s_memrealtime s[8:9]\n\t" \
    "s_nop 0\n\t" \
    "s_memrealtime s[10:11]\n\t" \
    "s_nop 0\n\t" \
    "s_memrealtime s[12:13]\n\t" \
    "s_nop 0\n\t" \
    "s_memrealtime s[14:15]\n\t" \
    "s_nop 0\n\t" \
    "s_memrealtime s[16:17]\n\t" \
    "s_nop 0\n\t" \
    "s_memrealtime s[18:19]\n\t" \
    "s_nop 0\n\t" \
    "s_memrealtime s[20:21]\n\t" \
    "s_nop 0\n\t" \
    "s_memrealtime s[22:23]\n\t" \
    "s_nop 0\n\t" \
    "s_memrealtime s[24:25]\n\t" \
    "s_nop 0\n\t" \
    "s_memrealtime s[26:27]\n\t" \
    "s_nop 0\n\t" \
    "s_memrealtime s[28:29]\n\t" \
    "s_nop 0\n\t" \
    "s_memtime s[56:57]\n\t" \
    "s_mov_b32 s70, 1\n\t" \
    "s_sub_u32 s71, s71, 1\n\t" \
    "s_cselect_b32 s69, 0, -1\n\t" \
    "s_waitcnt lgkmcnt(0)\n\t" \
    "s_sub_u32 s54, s56, s54\n\t" \
    "s_and_b32 s54, s54, s69\n\t" \
    "s_cmp_gt_u32 s54, 124\n\t" \
    "s_cbranch_scc1 qk_E83_%=\n\t" \
    "s_cmp_eq_u32 s4, s28\n\t" \
    "s_addc_u32 s70, s70, 0\n\t" \
    "s_cmp_eq_u32 s6, s28\n\t" \
    "s_addc_u32 s70, s70, 0\n\t" \
    "s_cmp_eq_u32 s8, s28\n\t" \
    "s_addc_u32 s70, s70, 0\n\t" \
    "s_cmp_eq_u32 s10, s28\n\t" \
    "s_addc_u32 s70, s70, 0\n\t" \
    "s_cmp_eq_u32 s12, s28\n\t" \
    "s_addc_u32 s70, s70, 0\n\t" \
    "s_cmp_eq_u32 s14, s28\n\t" \
    "s_addc_u32 s70, s70, 0\n\t" \
    "s_cmp_eq_u32 s16, s28\n\t" \
    "s_addc_u32 s70, s70, 0\n\t" \
    "s_cmp_eq_u32 s18, s28\n\t" \
    "s_addc_u32 s70, s70, 0\n\t" \
    "s_cmp_eq_u32 s20, s28\n\t" \
    "s_addc_u32 s70, s70, 0\n\t" \
    "s_cmp_eq_u32 s22, s28\n\t" \
    "s_addc_u32 s70, s70, 0\n\t" \
    "s_cmp_eq_u32 s24, s28\n\t" \
    "s_addc_u32 s70, s70, 0\n\t" \
    "s_cmp_eq_u32 s26, s28\n\t" \
    "s_addc_u32 s70, s70, 0\n\t" \
    "s_mov_b32 s75, s73\n\t" \
    "s_mov_b32 s76, s74\n\t" \
    "s_lshl_b32 s70, s70, 3\n\t" \
    "s_sub_u32 s73, s56, s70\n\t" \
    "s_lshl_b32 s74, s28, 4\n\t" \
    "s_sub_u32 s69, s73, s75\n\t" \
    "s_sub_u32 s70, s74, s76\n\t" \
    "s_sub_u32 s71, s70, 1600\n\t" \
    "s_cmp_lt_u32 s71, 400000\n\t" \
    "s_cbranch_scc0 qk_K85_%=\n\t" \
    "v_cvt_f32_u32 v160, s69\n\t" \
    "v_cvt_f32_u32 v161, s70\n\t" \
    "v_rcp_f32 v162, v160\n\t" \
    "v_rcp_f32 v163, v161\n\t" \
    "v_mul_f32 v162, v162, v161\n\t" \
    "v_mul_f32 v163, v163, v160\n\t" \
    "v_mul_f32 v162, 0x4f800000, v162\n\t" \
    "v_mul_f32 v163, 0x4f000000, v163\n\t" \
    "v_cvt_u32_f32 v162, v162\n\t" \
    "v_cvt_u32_f32 v163, v163\n\t" \
    "v_readfirstlane_b32 s69, v162\n\t" \
    "v_readfirstlane_b32 s70, v163\n\t" \
    "s_nop 1\n\t" \
    "s_sub_u32 s69, s69, s78\n\t" \
    "s_ashr_i32 s69, s69, 2\n\t" \
    "s_add_u32 s78, s78, s69\n\t" \
    "s_sub_u32 s70, s70, s77\n\t" \
    "s_ashr_i32 s70, s70, 2\n\t" \
    "s_add_u32 s77, s77, s70\n\t" \
    "qk_K85_%=:\n\t" \
    "v_mov_b32 v72, 0\n\t" \
    "v_mov_b32 v73, 0\n\t" \
    "v_mov_b32 v74, 0\n\t" \
    "v_mov_b32 v75, 0\n\t" \
    "v_mov_b32 v76, 0\n\t" \
    "v_mov_b32 v77, 0\n\t" \
    "v_mov_b32 v78, 0\n\t" \
    "v_mov_b32 v79, 0\n\t" \
    "v_mov_b32 v80, 0\n\t" \
    "v_mov_b32 v81, 0\n\t" \
    "v_mov_b32 v82, 0\n\t" \
    "v_mov_b32 v83, 0\n\t" \
    "v_mov_b32 v84, 0\n\t" \
    "v_mov_b32 v85, 0\n\t" \
    "v_mov_b32 v86, 0\n\t" \
    "v_mov_b32 v87, 0\n\t" \
    "s_add_u32 s79, s74, 560\n\t" \
    "s_cmp_ge_u32 s65, 58\n\t" \
    "s_cselect_b32 s79, s74, s79\n\t" \
    "s_sub_u32 s70, s79, s74\n\t" \
    "s_max_i32 s70, s70, 0\n\t" \
    "s_min_i32 s70, s70, 16000\n\t" \
    "s_lshl_b32 s70, s70, 1\n\t" \
    "s_mul_hi_u32 s70, s70, s77\n\t" \
    "s_add_u32 s70, s70, s73\n\t" \
    "qk_W86_%=:\n\t" \
    "s_memtime s[56:57]\n\t" \
    "s_waitcnt lgkmcnt(0)\n\t" \
    "s_sub_u32 s69, s70, s56\n\t" \
    "s_cmp_gt_i32 s69, 24\n\t" \
    "s_cbranch_scc1 qk_W86_%=\n\t" \
    "s_mov_b64 exec, s[58:59]\n\t" \
    "global_store_dwordx4 v0, v[140:143], s[40:41] offset:0 sc1\n\t" \
    "global_store_dwordx4 v0, v[144:147], s[40:41] offset:1024 sc1\n\t" \
    "global_store_dwordx4 v0, v[148:151], s[40:41] offset:2048 sc1\n\t" \
    "global_store_dwordx4 v0, v[152:155], s[40:41] offset:3072 sc1\n\t" \
    "s_mov_b64 exec, -1\n\t" \
    "s_add_u32 s65, s65, 1\n\t" \
    "v_add_u32 v31, 1, v31\n\t" \
    "s_add_u32 s69, s65, 0\n\t" \
    "s_bitcmp1_b32 s69, 0\n\t" \
    "s_cselect_b32 s70, s63, s62\n\t" \
    "s_bfe_u32 s69, s69, 0x20001\n\t" \
    "s_lshl_b32 s69, s69, 19\n\t" \
    "s_add_u32 s69, s69, s70\n\t" \
    "s_add_u32 s46, s36, s69\n\t" \
    "s_addc_u32 s47, s37, 0\n\t" \
    "s_mul_i32 s70, s67, 7\n\t" \
    "s_add_u32 s69, s65, 0\n\t" \
    "s_xor_b32 s69, s69, s70\n\t" \
    "s_and_b32 s69, s69, 63\n\t" \
    "s_lshl_b32 s69, s69, 12\n\t" \
    "s_mul_i32 s70, s67, 262144\n\t" \
    "s_add_u32 s69, s69, s70\n\t" \
    "s_add_u32 s69, s69, 39845888\n\t" \
    "s_add_u32 s40, s36, s69\n\t" \
    "s_addc_u32 s41, s37, 0\n\t" \
    "s_sub_u32 s69, s65, 1\n\t" \
    "v_mov_b32 v25, s69\n\t" \
    "s_waitcnt lgkmcnt(0)\n\t" \
    "s_barrier\n\t" \
    "ds_read_b32 v160, v11 offset:20480\n\t" \
    "s_waitcnt lgkmcnt(0)\n\t" \
    "v_readfirstlane_b32 s81, v160\n\t" \
    "s_nop 1\n\t" \
    "s_mul_hi_u32 s69, s85, s78\n\t" \
    "s_add_u32 s79, s81, s69\n\t" \
    "s_add_u32 s79, s79, s87\n\t" \
    "s_cmp_ge_u32 s65, 37\n\t" \
    "s_cselect_b32 s69, 28, 0\n\t" \
    "s_sub_u32 s79, s79, s69\n\t" \
    "s_cmp_lt_u32 s65, 9\n\t" \
    "s_cselect_b32 s69, 96, 0\n\t" \
    "s_add_u32 s79, s79, s69\n\t" \
    "s_cmp_ge_u32 s65, 58\n\t" \
    "s_cselect_b32 s69, 96, 0\n\t" \
    "s_add_u32 s79, s79, s69\n\t" \
    "s_add_u32 s80, s79, s88\n\t" \
    "s_branch qk_go73_%=\n\t" \
    "qk_Gbad78_%=:\n\t" \
    "s_waitcnt vmcnt(0) lgkmcnt(0)\n\t" \
    "s_nop 7\n\t" \
    "v_mov_b32 v72, 0\n\t" \
    "v_mov_b32 v73, 0\n\t" \
    "v_mov_b32 v74, 0\n\t" \
    "v_mov_b32 v75, 0\n\t" \
    "v_mov_b32 v76, 0\n\t" \
    "v_mov_b32 v77, 0\n\t" \
    "v_mov_b32 v78, 0\n\t" \
    "v_mov_b32 v79, 0\n\t" \
    "v_mov_b32 v80, 0\n\t" \
    "v_mov_b32 v81, 0\n\t" \
    "v_mov_b32 v82, 0\n\t" \
    "v_mov_b32 v83, 0\n\t" \
    "v_mov_b32 v84, 0\n\t" \
    "v_mov_b32 v85, 0\n\t" \
    "v_mov_b32 v86, 0\n\t" \
    "v_mov_b32 v87, 0\n\t" \
    "s_cmp_eq_u32 s68, 0\n\t" \
    "s_cbranch_scc1 qk_F87_%=\n\t" \
    "s_sub_u32 s68, s68, 1\n\t" \
    "s_branch qk_G77_%=\n\t" \
    "qk_F87_%=:\n\t" \
    "v_mov_b32 v160, 6095104\n\t" \
    "v_mov_b32 v161, 1\n\t" \
    "global_store_dword v160, v161, s[36:37] sc1\n\t" \
    "s_waitcnt vmcnt(0)\n\t" \
    "s_branch qk_Gc80_%=\n\t" \
    "qk_Gbad279_%=:\n\t" \
    "global_load_dwordx4 v[44:47], v2, s[46:47] sc1\n\t" \
    "s_waitcnt vmcnt(0)\n\t" \
    "v_max3_u32 v160, v44, v45, v46\n\t" \
    "v_max_u32 v160, v160, v47\n\t" \
    "v_cmp_eq_u32 vcc, -1, v160\n\t" \
    "s_cbranch_vccz qk_Gre282_%=\n\t" \
    "s_cmp_eq_u32 s68, 0\n\t" \
    "s_cbranch_scc1 qk_F88_%=\n\t" \
    "s_sub_u32 s68, s68, 1\n\t" \
    "s_sleep 1\n\t" \
    "s_branch qk_Gbad279_%=\n\t" \
    "qk_F88_%=:\n\t" \
    "v_mov_b32 v160, 6095104\n\t" \
    "v_mov_b32 v161, 2\n\t" \
    "global_store_dword v160, v161, s[36:37] sc1\n\t" \
    "s_waitcnt vmcnt(0)\n\t" \
    "s_branch qk_Gre282_%=\n\t" \
    "qk_Gre282_%=:\n\t" \
    "v_mov_b32 v72, 0\n\t" \
    "v_mov_b32 v73, 0\n\t" \
    "v_mov_b32 v74, 0\n\t" \
    "v_mov_b32 v75, 0\n\t" \
    "v_mov_b32 v76, 0\n\t" \
    "v_mov_b32 v77, 0\n\t" \
    "v_mov_b32 v78, 0\n\t" \
    "v_mov_b32 v79, 0\n\t" \
    "v_mov_b32 v80, 0\n\t" \
    "v_mov_b32 v81, 0\n\t" \
    "v_mov_b32 v82, 0\n\t" \
    "v_mov_b32 v83, 0\n\t" \
    "v_mov_b32 v84, 0\n\t" \
    "v_mov_b32 v85, 0\n\t" \
    "v_mov_b32 v86, 0\n\t" \
    "v_mov_b32 v87, 0\n\t" \
    "v_smfmac_f32_16x16x64_bf16 v[72:75], v[36:39], a[32:39], v10\n\t" \
    "v_smfmac_f32_16x16x64_bf16 v[76:79], v[36:39], a[104:111], v10\n\t" \
    "v_smfmac_f32_16x16x64_bf16 v[80:83], v[36:39], a[176:183], v10\n\t" \
    "v_smfmac_f32_16x16x64_bf16 v[84:87], v[36:39], a[248:255], v10\n\t" \
    "v_smfmac_f32_16x16x64_bf16 v[72:75], v[40:43], a[40:47], v10\n\t" \
    "v_smfmac_f32_16x16x64_bf16 v[76:79], v[40:43], a[112:119], v10\n\t" \
    "v_smfmac_f32_16x16x64_bf16 v[80:83], v[40:43], a[184:191], v10\n\t" \
    "v_smfmac_f32_16x16x64_bf16 v[84:87], v[40:43], v[224:231], v10\n\t" \
    "v_smfmac_f32_16x16x64_bf16 v[72:75], v[44:47], a[64:71], v10\n\t" \
    "v_smfmac_f32_16x16x64_bf16 v[76:79], v[44:47], a[136:143], v10\n\t" \
    "v_smfmac_f32_16x16x64_bf16 v[80:83], v[44:47], a[208:215], v10\n\t" \
    "v_smfmac_f32_16x16x64_bf16 v[84:87], v[44:47], v[248:255], v10\n\t" \
    "s_branch qk_Gc281_%=\n\t" \
    "qk_last68_%=:\n\t" \
    "s_mov_b64 exec, s[58:59]\n\t" \
    "global_store_dwordx4 v0, v[140:143], s[40:41] offset:0 sc1\n\t" \
    "global_store_dwordx4 v0, v[144:147], s[40:41] offset:1024 sc1\n\t" \
    "global_store_dwordx4 v0, v[148:151], s[40:41] offset:2048 sc1\n\t" \
    "global_store_dwordx4 v0, v[152:155], s[40:41] offset:3072 sc1\n\t" \
    "s_mov_b64 exec, -1\n\t" \
    "s_endpgm\n\t" \
    "qk_loop13_%=:\n\t" \
    "v_mov_b32 v72, 0\n\t" \
    "v_mov_b32 v73, 0\n\t" \
    "v_mov_b32 v74, 0\n\t" \
    "v_mov_b32 v75, 0\n\t" \
    "v_mov_b32 v76, 0\n\t" \
    "v_mov_b32 v77, 0\n\t" \
    "v_mov_b32 v78, 0\n\t" \
    "v_mov_b32 v79, 0\n\t" \
    "v_mov_b32 v80, 0\n\t" \
    "v_mov_b32 v81, 0\n\t" \
    "v_mov_b32 v82, 0\n\t" \
    "v_mov_b32 v83, 0\n\t" \
    "v_mov_b32 v84, 0\n\t" \
    "v_mov_b32 v85, 0\n\t" \
    "v_mov_b32 v86, 0\n\t" \
    "v_mov_b32 v87, 0\n\t" \
    "v_add_u32 v31, 1, v31\n\t" \
    "s_add_u32 s69, s65, 0\n\t" \
    "s_bitcmp1_b32 s69, 0\n\t" \
    "s_cselect_b32 s70, s63, s62\n\t" \
    "s_bfe_u32 s69, s69, 0x20001\n\t" \
    "s_lshl_b32 s69, s69, 19\n\t" \
    "s_add_u32 s69, s69, s70\n\t" \
    "s_add_u32 s46, s36, s69\n\t" \
    "s_addc_u32 s47, s37, 0\n\t" \
    "qk_go94_%=:\n\t" \
    "s_sub_u32 s70, s79, s74\n\t" \
    "s_max_i32 s70, s70, 0\n\t" \
    "s_min_i32 s70, s70, 16000\n\t" \
    "s_lshl_b32 s70, s70, 1\n\t" \
    "s_mul_hi_u32 s70, s70, s77\n\t" \
    "s_add_u32 s70, s70, s73\n\t" \
    "s_sub_u32 s70, s70, 100\n\t" \
    "s_getpc_b64 s[30:31]\n\t" \
    "qk_Wp96_%=:\n\t" \
    "s_add_u32 s30, s30, qk_We97_%=-qk_Wp96_%=\n\t" \
    "s_addc_u32 s31, s31, 0\n\t" \
    "qk_W95_%=:\n\t" \
    "s_memtime s[56:57]\n\t" \
    "s_waitcnt lgkmcnt(0)\n\t" \
    "s_sub_u32 s69, s70, s56\n\t" \
    "s_cmp_gt_i32 s69, 111\n\t" \
    "s_cbranch_scc1 qk_W95_%=\n\t" \
    "s_max_i32 s69, s69, 0\n\t" \
    "s_lshr_b32 s69, s69, 1\n\t" \
    "s_and_b32 s69, s69, 0x3c\n\t" \
    "s_sub_u32 s30, s30, s69\n\t" \
    "s_subb_u32 s31, s31, 0\n\t" \
    "s_setpc_b64 s[30:31]\n\t" \
    ".p2align 6\n\t" \
    "s_nop 0\n\t" \
    "s_nop 0\n\t" \
    "s_nop 0\n\t" \
    "s_nop 1\n\t" \
    "s_nop 1\n\t" \
    "s_nop 1\n\t" \
    "s_nop 1\n\t" \
    "s_nop 1\n\t" \
    "s_nop 1\n\t" \
    "s_nop 1\n\t" \
    "s_nop 1\n\t" \
    "s_nop 1\n\t" \
    "s_nop 1\n\t" \
    "s_nop 1\n\t" \
    "s_nop 1\n\t" \
    "s_nop 1\n\t" \
    "qk_We97_%=:\n\t" \
    "qk_G98_%=:\n\t" \
    "global_load_dwordx4 v[36:39], v1, s[46:47] sc1\n\t" \
    "s_nop 9\n\t" \
    "global_load_dwordx4 v[40:43], v1, s[46:47] offset:1024 sc1\n\t" \
    "s_nop 9\n\t" \
    "global_load_dwordx4 v[44:47], v2, s[46:47] sc1\n\t" \
    "s_mul_hi_u32 s69, s84, s78\n\t" \
    "s_add_u32 s33, s74, s69\n\t" \
    "s_lshr_b32 s69, s90, 16\n\t" \
    "s_add_u32 s34, s33, s69\n\t" \
    "s_and_b32 s69, s90, 0xffff\n\t" \
    "s_sub_u32 s35, s80, s74\n\t" \
    "s_sub_u32 s35, s35, s69\n\t" \
    "s_add_u32 s99, s35, 1280\n\t" \
    "s_add_u32 s98, s34, 600\n\t" \
    "s_waitcnt vmcnt(2)\n\t" \
    "v_smfmac_f32_16x16x64_bf16 v[72:75], v[36:39], a[48:55], v10\n\t" \
    "ds_write_b128 v0, v[36:39] offset:6144\n\t" \
    "v_smfmac_f32_16x16x64_bf16 v[76:79], v[36:39], a[120:127], v10\n\t" \
    "v_max3_u32 v160, v36, v37, v38\n\t" \
    "v_smfmac_f32_16x16x64_bf16 v[80:83], v[36:39], a[192:199], v10\n\t" \
    "v_smfmac_f32_16x16x64_bf16 v[84:87], v[36:39], v[232:239], v10\n\t" \
    "s_waitcnt vmcnt(1)\n\t" \
    "ds_write_b128 v0, v[40:43] offset:7168\n\t" \
    "v_smfmac_f32_16x16x64_bf16 v[72:75], v[40:43], a[56:63], v10\n\t" \
    "v_max3_u32 v160, v160, v39, v40\n\t" \
    "v_max3_u32 v160, v160, v41, v42\n\t" \
    "v_smfmac_f32_16x16x64_bf16 v[76:79], v[40:43], a[128:135], v10\n\t" \
    "v_max_u32 v160, v160, v43\n\t" \
    "v_cmp_eq_u32 vcc, -1, v160\n\t" \
    "v_smfmac_f32_16x16x64_bf16 v[80:83], v[40:43], a[200:207], v10\n\t" \
    "v_smfmac_f32_16x16x64_bf16 v[84:87], v[40:43], v[240:247], v10\n\t" \
    "s_cbranch_vccnz qk_Gbad99_%=\n\t" \
    "qk_Gc101_%=:\n\t" \
    "s_waitcnt lgkmcnt(0)\n\t" \
    "s_barrier\n\t" \
    "s_memtime s[56:57]\n\t" \
    "ds_read_b128 v[48:51], v0 offset:0\n\t" \
    "ds_read_b128 v[52:55], v0 offset:1024\n\t" \
    "s_waitcnt vmcnt(0)\n\t" \
    "v_smfmac_f32_16x16x64_bf16 v[72:75], v[44:47], a[64:71], v10\n\t" \
    "ds_read_b128 v[56:59], v0 offset:2048\n\t" \
    "v_max3_u32 v160, v44, v45, v46\n\t" \
    "v_smfmac_f32_16x16x64_bf16 v[76:79], v[44:47], a[136:143], v10\n\t" \
    "ds_read_b128 v[60:63], v0 offset:3072\n\t" \
    "v_max_u32 v160, v160, v47\n\t" \
    "v_smfmac_f32_16x16x64_bf16 v[80:83], v[44:47], a[208:215], v10\n\t" \
    "ds_read_b128 v[64:67], v0 offset:4096\n\t" \
    "v_cmp_eq_u32 vcc, -1, v160\n\t" \
    "v_smfmac_f32_16x16x64_bf16 v[84:87], v[44:47], v[248:255], v10\n\t" \
    "ds_read_b128 v[68:71], v0 offset:5120\n\t" \
    "s_cbranch_vccnz qk_Gbad2100_%=\n\t" \
    "qk_Gc2102_%=:\n\t" \
    "s_sub_u32 s69, s56, s73\n\t" \
    "s_mul_hi_u32 s69, s69, s78\n\t" \
    "s_cmp_gt_i32 s69, s35\n\t" \
    "s_waitcnt lgkmcnt(5)\n\t" \
    "v_smfmac_f32_16x16x64_bf16 v[72:75], v[48:51], a[0:7], v10\n\t" \
    "s_cselect_b32 s70, s34, s33\n\t" \
    "s_cmp_gt_i32 s69, s99\n\t" \
    "s_cselect_b32 s70, s98, s70\n\t" \
    "s_waitcnt lgkmcnt(4)\n\t" \
    "v_smfmac_f32_16x16x64_bf16 v[72:75], v[52:55], a[8:15], v10\n\t" \
    "s_add_u32 s70, s69, s70\n\t" \
    "v_and_b32_e64 v162, -2, s70\n\t" \
    "s_waitcnt lgkmcnt(3)\n\t" \
    "v_smfmac_f32_16x16x64_bf16 v[72:75], v[56:59], a[16:23], v10\n\t" \
    "s_mov_b64 exec, s[44:45]\n\t" \
    "global_store_dword v21, v162, s[46:47] sc1\n\t" \
    "s_mov_b64 exec, -1\n\t" \
    "s_waitcnt lgkmcnt(2)\n\t" \
    "v_smfmac_f32_16x16x64_bf16 v[72:75], v[60:63], a[24:31], v10\n\t" \
    "s_waitcnt lgkmcnt(1)\n\t" \
    "v_smfmac_f32_16x16x64_bf16 v[72:75], v[64:67], a[32:39], v10\n\t" \
    "s_waitcnt lgkmcnt(0)\n\t" \
    "v_smfmac_f32_16x16x64_bf16 v[72:75], v[68:71], a[40:47], v10\n\t" \
    "v_smfmac_f32_16x16x64_bf16 v[76:79], v[48:51], a[72:79], v10\n\t" \
    "s_nop 1\n\t" \
    "s_nop 0\n\t" \
    "v_smfmac_f32_16x16x64_bf16 v[76:79], v[52:55], a[80:87], v10\n\t" \
    "s_nop 1\n\t" \
    "s_nop 0\n\t" \
    "v_smfmac_f32_16x16x64_bf16 v[76:79], v[56:59], a[88:95], v10\n\t" \
    "v_add_f32 v72, v72, v74\n\t" \
    "v_smfmac_f32_16x16x64_bf16 v[76:79], v[60:63], a[96:103], v10\n\t" \
    "v_add_f32 v73, v73, v75\n\t" \
    "v_smfmac_f32_16x16x64_bf16 v[76:79], v[64:67], a[104:111], v10\n\t" \
    "v_cvt_pk_bf16_f32 v156, v72, v73\n\t" \
    "v_smfmac_f32_16x16x64_bf16 v[76:79], v[68:71], a[112:119], v10\n\t" \
    "ds_write_b32 v12, v156 offset:0\n\t" \
    "v_smfmac_f32_16x16x64_bf16 v[80:83], v[48:51], a[144:151], v10\n\t" \
    "s_nop 1\n\t" \
    "s_nop 0\n\t" \
    "v_smfmac_f32_16x16x64_bf16 v[80:83], v[52:55], a[152:159], v10\n\t" \
    "s_nop 1\n\t" \
    "s_nop 0\n\t" \
    "v_smfmac_f32_16x16x64_bf16 v[80:83], v[56:59], a[160:167], v10\n\t" \
    "v_add_f32 v76, v76, v78\n\t" \
    "v_smfmac_f32_16x16x64_bf16 v[80:83], v[60:63], a[168:175], v10\n\t" \
    "v_add_f32 v77, v77, v79\n\t" \
    "v_smfmac_f32_16x16x64_bf16 v[80:83], v[64:67], a[176:183], v10\n\t" \
    "v_cvt_pk_bf16_f32 v157, v76, v77\n\t" \
    "v_smfmac_f32_16x16x64_bf16 v[80:83], v[68:71], a[184:191], v10\n\t" \
    "ds_write_b32 v12, v157 offset:64\n\t" \
    "v_smfmac_f32_16x16x64_bf16 v[84:87], v[48:51], a[216:223], v10\n\t" \
    "s_nop 1\n\t" \
    "s_nop 0\n\t" \
    "v_smfmac_f32_16x16x64_bf16 v[84:87], v[52:55], a[224:231], v10\n\t" \
    "s_nop 1\n\t" \
    "s_nop 0\n\t" \
    "v_smfmac_f32_16x16x64_bf16 v[84:87], v[56:59], a[232:239], v10\n\t" \
    "v_add_f32 v80, v80, v82\n\t" \
    "v_smfmac_f32_16x16x64_bf16 v[84:87], v[60:63], a[240:247], v10\n\t" \
    "v_add_f32 v81, v81, v83\n\t" \
    "v_smfmac_f32_16x16x64_bf16 v[84:87], v[64:67], a[248:255], v10\n\t" \
    "v_cvt_pk_bf16_f32 v158, v80, v81\n\t" \
    "v_smfmac_f32_16x16x64_bf16 v[84:87], v[68:71], v[224:231], v10\n\t" \
    "ds_write_b32 v16, v158 offset:0\n\t" \
    "s_nop 6\n\t" \
    "v_pk_add_f32 v[84:85], v[84:85], v[86:87]\n\t" \
    "v_cvt_pk_bf16_f32 v159, v84, v85\n\t" \
    "ds_write_b32 v16, v159 offset:256\n\t" \
    "s_waitcnt lgkmcnt(0)\n\t" \
    "s_barrier\n\t" \
    "s_cmp_eq_u32 s65, 63\n\t" \
    "s_cbranch_scc1 qk_last89_%=\n\t" \
    "s_mov_b64 s[44:45], 0xfffff\n\t" \
    "s_sub_u32 s70, s80, s74\n\t" \
    "s_max_i32 s70, s70, 0\n\t" \
    "s_min_i32 s70, s70, 16000\n\t" \
    "s_lshl_b32 s70, s70, 1\n\t" \
    "s_mul_hi_u32 s70, s70, s77\n\t" \
    "s_add_u32 s70, s70, s73\n\t" \
    "qk_W104_%=:\n\t" \
    "s_memtime s[56:57]\n\t" \
    "s_waitcnt lgkmcnt(0)\n\t" \
    "s_sub_u32 s69, s70, s56\n\t" \
    "s_cmp_gt_i32 s69, 24\n\t" \
    "s_cbranch_scc1 qk_W104_%=\n\t" \
    "global_load_dword v160, v20, s[46:47] sc1\n\t" \
    "s_mov_b32 s71, 3\n\t" \
    "qk_E108_%=:\n\t" \
    "s_memtime s[54:55]\n\t" \
    "s_nop 0\n\t" \
    "s_memrealtime s[4:5]\n\t" \
    "s_nop 0\n\t" \
    "s_memrealtime s[6:7]\n\t" \
    "s_nop 0\n\t" \
    "s_memrealtime s[8:9]\n\t" \
    "s_nop 0\n\t" \
    "s_memrealtime s[10:11]\n\t" \
    "s_nop 0\n\t" \
    "s_memrealtime s[12:13]\n\t" \
    "s_nop 0\n\t" \
    "s_memrealtime s[14:15]\n\t" \
    "s_nop 0\n\t" \
    "s_memrealtime s[16:17]\n\t" \
    "s_nop 0\n\t" \
    "s_memrealtime s[18:19]\n\t" \
    "s_nop 0\n\t" \
    "s_memrealtime s[20:21]\n\t" \
    "s_nop 0\n\t" \
    "s_memrealtime s[22:23]\n\t" \
    "s_nop 0\n\t" \
    "s_memrealtime s[24:25]\n\t" \
    "s_nop 0\n\t" \
    "s_memrealtime s[26:27]\n\t" \
    "s_nop 0\n\t" \
    "s_memrealtime s[28:29]\n\t" \
    "s_nop 0\n\t" \
    "s_memtime s[56:57]\n\t" \
    "s_mov_b32 s70, 1\n\t" \
    "s_sub_u32 s71, s71, 1\n\t" \
    "s_cselect_b32 s69, 0, -1\n\t" \
    "s_waitcnt lgkmcnt(0)\n\t" \
    "s_sub_u32 s54, s56, s54\n\t" \
    "s_and_b32 s54, s54, s69\n\t" \
    "s_cmp_gt_u32 s54, 124\n\t" \
    "s_cbranch_scc1 qk_E108_%=\n\t" \
    "s_cmp_eq_u32 s4, s28\n\t" \
    "s_addc_u32 s70, s70, 0\n\t" \
    "s_cmp_eq_u32 s6, s28\n\t" \
    "s_addc_u32 s70, s70, 0\n\t" \
    "s_cmp_eq_u32 s8, s28\n\t" \
    "s_addc_u32 s70, s70, 0\n\t" \
    "s_cmp_eq_u32 s10, s28\n\t" \
    "s_addc_u32 s70, s70, 0\n\t" \
    "s_cmp_eq_u32 s12, s28\n\t" \
    "s_addc_u32 s70, s70, 0\n\t" \
    "s_cmp_eq_u32 s14, s28\n\t" \
    "s_addc_u32 s70, s70, 0\n\t" \
    "s_cmp_eq_u32 s16, s28\n\t" \
    "s_addc_u32 s70, s70, 0\n\t" \
    "s_cmp_eq_u32 s18, s28\n\t" \
    "s_addc_u32 s70, s70, 0\n\t" \
    "s_cmp_eq_u32 s20, s28\n\t" \
    "s_addc_u32 s70, s70, 0\n\t" \
    "s_cmp_eq_u32 s22, s28\n\t" \
    "s_addc_u32 s70, s70, 0\n\t" \
    "s_cmp_eq_u32 s24, s28\n\t" \
    "s_addc_u32 s70, s70, 0\n\t" \
    "s_cmp_eq_u32 s26, s28\n\t" \
    "s_addc_u32 s70, s70, 0\n\t" \
    "s_mov_b32 s75, s73\n\t" \
    "s_mov_b32 s76, s74\n\t" \
    "s_lshl_b32 s70, s70, 3\n\t" \
    "s_sub_u32 s73, s56, s70\n\t" \
    "s_lshl_b32 s74, s28, 4\n\t" \
    "s_sub_u32 s69, s73, s75\n\t" \
    "s_sub_u32 s70, s74, s76\n\t" \
    "s_sub_u32 s71, s70, 1600\n\t" \
    "s_cmp_lt_u32 s71, 400000\n\t" \
    "s_cbranch_scc0 qk_K110_%=\n\t" \
    "v_cvt_f32_u32 v161, s69\n\t" \
    "v_cvt_f32_u32 v162, s70\n\t" \
    "v_rcp_f32 v163, v161\n\t" \
    "v_rcp_f32 v164, v162\n\t" \
    "v_mul_f32 v163, v163, v162\n\t" \
    "v_mul_f32 v164, v164, v161\n\t" \
    "v_mul_f32 v163, 0x4f800000, v163\n\t" \
    "v_mul_f32 v164, 0x4f000000, v164\n\t" \
    "v_cvt_u32_f32 v163, v163\n\t" \
    "v_cvt_u32_f32 v164, v164\n\t" \
    "v_readfirstlane_b32 s69, v163\n\t" \
    "v_readfirstlane_b32 s70, v164\n\t" \
    "s_nop 1\n\t" \
    "s_sub_u32 s69, s69, s78\n\t" \
    "s_ashr_i32 s69, s69, 2\n\t" \
    "s_add_u32 s78, s78, s69\n\t" \
    "s_sub_u32 s70, s70, s77\n\t" \
    "s_ashr_i32 s70, s70, 2\n\t" \
    "s_add_u32 s77, s77, s70\n\t" \
    "qk_K110_%=:\n\t" \
    "s_waitcnt vmcnt(0)\n\t" \
    "v_cmp_eq_u32 vcc, -1, v160\n\t" \
    "s_cbranch_vccz qk_Hok106_%=\n\t" \
    "qk_H105_%=:\n\t" \
    "global_load_dword v160, v20, s[46:47] sc1\n\t" \
    "s_waitcnt vmcnt(0)\n\t" \
    "v_cmp_eq_u32 vcc, -1, v160\n\t" \
    "s_cbranch_vccz qk_Hc107_%=\n\t" \
    "s_cmp_eq_u32 s68, 0\n\t" \
    "s_cbranch_scc1 qk_F111_%=\n\t" \
    "s_sub_u32 s68, s68, 1\n\t" \
    "s_sleep 1\n\t" \
    "s_branch qk_H105_%=\n\t" \
    "qk_F111_%=:\n\t" \
    "v_mov_b32 v161, 6095104\n\t" \
    "v_mov_b32 v162, 5\n\t" \
    "global_store_dword v161, v162, s[36:37] sc1\n\t" \
    "s_waitcnt vmcnt(0)\n\t" \
    "s_branch qk_Hc107_%=\n\t" \
    "qk_Hc107_%=:\n\t" \
    "qk_Hok106_%=:\n\t" \
    "v_subrev_u32 v160, s80, v160\n\t" \
    "v_and_b32 v20, -5, v20\n\t" \
    "s_nop 0\n\t" \
    "v_max_i32_dpp v160, v160, v160 row_ror:1 row_mask:0xf bank_mask:0xf\n\t" \
    "s_nop 1\n\t" \
    "v_max_i32_dpp v160, v160, v160 row_ror:2 row_mask:0xf bank_mask:0xf\n\t" \
    "s_nop 1\n\t" \
    "v_max_i32_dpp v160, v160, v160 row_ror:4 row_mask:0xf bank_mask:0xf\n\t" \
    "s_nop 1\n\t" \
    "v_max_i32_dpp v160, v160, v160 row_ror:8 row_mask:0xf bank_mask:0xf\n\t" \
    "s_nop 0\n\t" \
    "v_readlane_b32 s69, v160, 0\n\t" \
    "v_readlane_b32 s71, v160, 16\n\t" \
    "v_readlane_b32 s70, v160, 32\n\t" \
    "v_readlane_b32 s81, v160, 48\n\t" \
    "s_max_i32 s69, s69, s71\n\t" \
    "s_nop 0\n\t" \
    "s_max_i32 s71, s70, s81\n\t" \
    "s_max_i32 s69, s69, s71\n\t" \
    "s_add_u32 s81, s69, s80\n\t" \
    "v_mov_b32 v72, 0\n\t" \
    "v_mov_b32 v73, 0\n\t" \
    "v_mov_b32 v74, 0\n\t" \
    "v_mov_b32 v75, 0\n\t" \
    "v_mov_b32 v76, 0\n\t" \
    "v_mov_b32 v77, 0\n\t" \
    "v_mov_b32 v78, 0\n\t" \
    "v_mov_b32 v79, 0\n\t" \
    "v_mov_b32 v80, 0\n\t" \
    "v_mov_b32 v81, 0\n\t" \
    "v_mov_b32 v82, 0\n\t" \
    "v_mov_b32 v83, 0\n\t" \
    "v_mov_b32 v84, 0\n\t" \
    "v_mov_b32 v85, 0\n\t" \
    "v_mov_b32 v86, 0\n\t" \
    "v_mov_b32 v87, 0\n\t" \
    "s_cmp_lg_u32 s65, 0\n\t" \
    "s_cbranch_scc1 qk_Oc114_%=\n\t" \
    "s_add_u32 s79, s81, 240\n\t" \
    "s_sub_u32 s70, s79, s74\n\t" \
    "s_max_i32 s70, s70, 0\n\t" \
    "s_min_i32 s70, s70, 16000\n\t" \
    "s_lshl_b32 s70, s70, 1\n\t" \
    "s_mul_hi_u32 s70, s70, s77\n\t" \
    "s_add_u32 s70, s70, s73\n\t" \
    "qk_W115_%=:\n\t" \
    "s_memtime s[56:57]\n\t" \
    "s_waitcnt lgkmcnt(0)\n\t" \
    "s_sub_u32 s69, s70, s56\n\t" \
    "s_cmp_gt_i32 s69, 24\n\t" \
    "s_cbranch_scc1 qk_W115_%=\n\t" \
    "global_load_dword v160, v35, s[46:47] sc1\n\t" \
    "s_waitcnt vmcnt(0)\n\t" \
    "v_cmp_eq_u32 vcc, -1, v160\n\t" \
    "s_cbranch_vccz qk_Ook113_%=\n\t" \
    "qk_O112_%=:\n\t" \
    "global_load_dword v160, v35, s[46:47] offset:1536 sc1\n\t" \
    "s_waitcnt vmcnt(0)\n\t" \
    "v_cmp_eq_u32 vcc, -1, v160\n\t" \
    "s_cbranch_vccz qk_Ook113_%=\n\t" \
    "s_cmp_eq_u32 s68, 0\n\t" \
    "s_cbranch_scc1 qk_F116_%=\n\t" \
    "s_sub_u32 s68, s68, 1\n\t" \
    "s_sleep 3\n\t" \
    "s_branch qk_O112_%=\n\t" \
    "qk_F116_%=:\n\t" \
    "v_mov_b32 v161, 6095104\n\t" \
    "v_mov_b32 v162, 16\n\t" \
    "global_store_dword v161, v162, s[36:37] sc1\n\t" \
    "s_waitcnt vmcnt(0)\n\t" \
    "s_branch qk_Ook113_%=\n\t" \
    "qk_Ook113_%=:\n\t" \
    "v_subrev_u32 v160, s80, v160\n\t" \
    "s_nop 1\n\t" \
    "v_max_i32_dpp v160, v160, v160 row_ror:1 row_mask:0xf bank_mask:0xf\n\t" \
    "s_nop 1\n\t" \
    "v_max_i32_dpp v160, v160, v160 row_ror:2 row_mask:0xf bank_mask:0xf\n\t" \
    "s_nop 1\n\t" \
    "v_max_i32_dpp v160, v160, v160 row_ror:4 row_mask:0xf bank_mask:0xf\n\t" \
    "s_nop 1\n\t" \
    "v_max_i32_dpp v160, v160, v160 row_ror:8 row_mask:0xf bank_mask:0xf\n\t" \
    "s_nop 0\n\t" \
    "v_readlane_b32 s69, v160, 0\n\t" \
    "v_readlane_b32 s71, v160, 16\n\t" \
    "v_readlane_b32 s70, v160, 32\n\t" \
    "v_readlane_b32 s81, v160, 48\n\t" \
    "s_max_i32 s69, s69, s71\n\t" \
    "s_nop 0\n\t" \
    "s_max_i32 s71, s70, s81\n\t" \
    "s_max_i32 s69, s69, s71\n\t" \
    "s_add_u32 s81, s69, s80\n\t" \
    "qk_Oc114_%=:\n\t" \
    "v_mov_b32 v160, s81\n\t" \
    "ds_write_b32 v11, v160 offset:20480\n\t" \
    "s_add_u32 s65, s65, 1\n\t" \
    "v_add_u32 v31, 1, v31\n\t" \
    "s_add_u32 s69, s65, 0\n\t" \
    "s_bitcmp1_b32 s69, 0\n\t" \
    "s_cselect_b32 s70, s63, s62\n\t" \
    "s_bfe_u32 s69, s69, 0x20001\n\t" \
    "s_lshl_b32 s69, s69, 19\n\t" \
    "s_add_u32 s69, s69, s70\n\t" \
    "s_add_u32 s46, s36, s69\n\t" \
    "s_addc_u32 s47, s37, 0\n\t" \
    "s_waitcnt lgkmcnt(0)\n\t" \
    "s_barrier\n\t" \
    "ds_read_b32 v160, v11 offset:20480\n\t" \
    "s_waitcnt lgkmcnt(0)\n\t" \
    "v_readfirstlane_b32 s81, v160\n\t" \
    "s_nop 1\n\t" \
    "s_mul_hi_u32 s69, s85, s78\n\t" \
    "s_add_u32 s79, s81, s69\n\t" \
    "s_add_u32 s79, s79, s87\n\t" \
    "s_cmp_ge_u32 s65, 37\n\t" \
    "s_cselect_b32 s69, 28, 0\n\t" \
    "s_sub_u32 s79, s79, s69\n\t" \
    "s_cmp_lt_u32 s65, 9\n\t" \
    "s_cselect_b32 s69, 96, 0\n\t" \
    "s_add_u32 s79, s79, s69\n\t" \
    "s_cmp_ge_u32 s65, 58\n\t" \
    "s_cselect_b32 s69, 96, 0\n\t" \
    "s_add_u32 s79, s79, s69\n\t" \
    "s_add_u32 s80, s79, s88\n\t" \
    "s_branch qk_go94_%=\n\t" \
    "qk_Gbad99_%=:\n\t" \
    "s_waitcnt vmcnt(0) lgkmcnt(0)\n\t" \
    "s_nop 7\n\t" \
    "v_mov_b32 v72, 0\n\t" \
    "v_mov_b32 v73, 0\n\t" \
    "v_mov_b32 v74, 0\n\t" \
    "v_mov_b32 v75, 0\n\t" \
    "v_mov_b32 v76, 0\n\t" \
    "v_mov_b32 v77, 0\n\t" \
    "v_mov_b32 v78, 0\n\t" \
    "v_mov_b32 v79, 0\n\t" \
    "v_mov_b32 v80, 0\n\t" \
    "v_mov_b32 v81, 0\n\t" \
    "v_mov_b32 v82, 0\n\t" \
    "v_mov_b32 v83, 0\n\t" \
    "v_mov_b32 v84, 0\n\t" \
    "v_mov_b32 v85, 0\n\t" \
    "v_mov_b32 v86, 0\n\t" \
    "v_mov_b32 v87, 0\n\t" \
    "s_cmp_eq_u32 s68, 0\n\t" \
    "s_cbranch_scc1 qk_F117_%=\n\t" \
    "s_sub_u32 s68, s68, 1\n\t" \
    "s_branch qk_G98_%=\n\t" \
    "qk_F117_%=:\n\t" \
    "v_mov_b32 v160, 6095104\n\t" \
    "v_mov_b32 v161, 1\n\t" \
    "global_store_dword v160, v161, s[36:37] sc1\n\t" \
    "s_waitcnt vmcnt(0)\n\t" \
    "s_branch qk_Gc101_%=\n\t" \
    "qk_Gbad2100_%=:\n\t" \
    "global_load_dwordx4 v[44:47], v2, s[46:47] sc1\n\t" \
    "s_waitcnt vmcnt(0)\n\t" \
    "v_max3_u32 v160, v44, v45, v46\n\t" \
    "v_max_u32 v160, v160, v47\n\t" \
    "v_cmp_eq_u32 vcc, -1, v160\n\t" \
    "s_cbranch_vccz qk_Gre2103_%=\n\t" \
    "s_cmp_eq_u32 s68, 0\n\t" \
    "s_cbranch_scc1 qk_F118_%=\n\t" \
    "s_sub_u32 s68, s68, 1\n\t" \
    "s_sleep 1\n\t" \
    "s_branch qk_Gbad2100_%=\n\t" \
    "qk_F118_%=:\n\t" \
    "v_mov_b32 v160, 6095104\n\t" \
    "v_mov_b32 v161, 2\n\t" \
    "global_store_dword v160, v161, s[36:37] sc1\n\t" \
    "s_waitcnt vmcnt(0)\n\t" \
    "s_branch qk_Gre2103_%=\n\t" \
    "qk_Gre2103_%=:\n\t" \
    "v_mov_b32 v72, 0\n\t" \
    "v_mov_b32 v73, 0\n\t" \
    "v_mov_b32 v74, 0\n\t" \
    "v_mov_b32 v75, 0\n\t" \
    "v_mov_b32 v76, 0\n\t" \
    "v_mov_b32 v77, 0\n\t" \
    "v_mov_b32 v78, 0\n\t" \
    "v_mov_b32 v79, 0\n\t" \
    "v_mov_b32 v80, 0\n\t" \
    "v_mov_b32 v81, 0\n\t" \
    "v_mov_b32 v82, 0\n\t" \
    "v_mov_b32 v83, 0\n\t" \
    "v_mov_b32 v84, 0\n\t" \
    "v_mov_b32 v85, 0\n\t" \
    "v_mov_b32 v86, 0\n\t" \
    "v_mov_b32 v87, 0\n\t" \
    "v_smfmac_f32_16x16x64_bf16 v[72:75], v[36:39], a[48:55], v10\n\t" \
    "v_smfmac_f32_16x16x64_bf16 v[76:79], v[36:39], a[120:127], v10\n\t" \
    "v_smfmac_f32_16x16x64_bf16 v[80:83], v[36:39], a[192:199], v10\n\t" \
    "v_smfmac_f32_16x16x64_bf16 v[84:87], v[36:39], v[232:239], v10\n\t" \
    "v_smfmac_f32_16x16x64_bf16 v[72:75], v[40:43], a[56:63], v10\n\t" \
    "v_smfmac_f32_16x16x64_bf16 v[76:79], v[40:43], a[128:135], v10\n\t" \
    "v_smfmac_f32_16x16x64_bf16 v[80:83], v[40:43], a[200:207], v10\n\t" \
    "v_smfmac_f32_16x16x64_bf16 v[84:87], v[40:43], v[240:247], v10\n\t" \
    "v_smfmac_f32_16x16x64_bf16 v[72:75], v[44:47], a[64:71], v10\n\t" \
    "v_smfmac_f32_16x16x64_bf16 v[76:79], v[44:47], a[136:143], v10\n\t" \
    "v_smfmac_f32_16x16x64_bf16 v[80:83], v[44:47], a[208:215], v10\n\t" \
    "v_smfmac_f32_16x16x64_bf16 v[84:87], v[44:47], v[248:255], v10\n\t" \
    "s_waitcnt lgkmcnt(0)\n\t" \
    "s_memtime s[56:57]\n\t" \
    "s_waitcnt lgkmcnt(0)\n\t" \
    "s_branch qk_Gc2102_%=\n\t" \
    "qk_last89_%=:\n\t" \
    "s_endpgm\n\t" \
    "vv_begin_%=:\n\t" \
    "v_mbcnt_lo_u32_b32 v223, -1, 0\n\t" \
    "v_mbcnt_hi_u32_b32 v223, -1, v223\n\t" \
    "s_mov_b32 s68, 262144\n\t" \
    "s_mov_b32 s72, 0\n\t" \
    "s_mov_b32 s83, -1\n\t" \
    "s_mov_b32 s65, 0\n\t" \
    "v_and_b32 v180, 3, v223\n\t" \
    "v_lshlrev_b32 v202, 3, v180\n\t" \
    "v_lshrrev_b32 v181, 5, v223\n\t" \
    "v_lshl_add_u32 v202, v181, 12, v202\n\t" \
    "v_bfe_u32 v182, v223, 2, 2\n\t" \
    "s_mov_b32 s70, 288\n\t" \
    "v_mad_u32_u24 v202, v182, s70, v202\n\t" \
    "v_bfe_u32 v183, v223, 4, 1\n\t" \
    "s_mov_b32 s70, 2176\n\t" \
    "v_mad_u32_u24 v202, v183, s70, v202\n\t" \
    "s_lshl_b32 s69, s66, 6\n\t" \
    "v_xor_b32 v202, s69, v202\n\t" \
    "v_xor_b32 v203, 32, v202\n\t" \
    "s_mul_i32 s69, s66, 9216\n\t" \
    "v_and_b32 v184, 1, v223\n\t" \
    "v_lshl_add_u32 v201, v184, 4, s69\n\t" \
    "v_lshrrev_b32 v185, 3, v223\n\t" \
    "s_mov_b32 s70, 1152\n\t" \
    "v_mad_u32_u24 v201, v185, s70, v201\n\t" \
    "v_bfe_u32 v186, v223, 1, 2\n\t" \
    "v_lshlrev_b32 v204, 5, v186\n\t" \
    "v_lshrrev_b32 v187, 4, v223\n\t" \
    "v_lshlrev_b32 v205, 5, v187\n\t" \
    "v_xor_b32 v204, v204, v205\n\t" \
    "v_add_u32 v201, v201, v204\n\t" \
    "s_mul_i32 s96, s66, 9216\n\t" \
    "v_lshlrev_b32 v207, 3, v180\n\t" \
    "v_bfe_u32 v188, v223, 2, 1\n\t" \
    "v_lshl_add_u32 v207, v188, 7, v207\n\t" \
    "v_bfe_u32 v189, v223, 3, 2\n\t" \
    "s_mov_b32 s70, 288\n\t" \
    "v_mad_u32_u24 v207, v189, s70, v207\n\t" \
    "s_mov_b32 s70, 9216\n\t" \
    "v_mad_u32_u24 v207, v181, s70, v207\n\t" \
    "s_mul_i32 s69, s66, 9\n\t" \
    "s_add_u32 s69, s69, 0\n\t" \
    "s_and_b32 s70, s69, 3\n\t" \
    "s_lshl_b32 s70, s70, 5\n\t" \
    "s_lshr_b32 s69, s69, 2\n\t" \
    "s_lshl_b32 s69, s69, 10\n\t" \
    "v_xor_b32 v204, s70, v207\n\t" \
    "v_add_u32 v204, s69, v204\n\t" \
    "s_mul_i32 s69, s66, 9\n\t" \
    "s_add_u32 s69, s69, 1\n\t" \
    "s_and_b32 s70, s69, 3\n\t" \
    "s_lshl_b32 s70, s70, 5\n\t" \
    "s_lshr_b32 s69, s69, 2\n\t" \
    "s_lshl_b32 s69, s69, 10\n\t" \
    "v_xor_b32 v205, s70, v207\n\t" \
    "v_add_u32 v205, s69, v205\n\t" \
    "s_mul_i32 s69, s66, 9\n\t" \
    "s_add_u32 s69, s69, 2\n\t" \
    "s_and_b32 s70, s69, 3\n\t" \
    "s_lshl_b32 s70, s70, 5\n\t" \
    "s_lshr_b32 s69, s69, 2\n\t" \
    "s_lshl_b32 s69, s69, 10\n\t" \
    "v_xor_b32 v206, s70, v207\n\t" \
    "v_add_u32 v206, s69, v206\n\t" \
    "s_mul_i32 s69, s66, 9\n\t" \
    "s_add_u32 s69, s69, 3\n\t" \
    "s_and_b32 s70, s69, 3\n\t" \
    "s_lshl_b32 s70, s70, 5\n\t" \
    "s_lshr_b32 s69, s69, 2\n\t" \
    "s_lshl_b32 s69, s69, 10\n\t" \
    "v_xor_b32 v207, s70, v207\n\t" \
    "v_add_u32 v207, s69, v207\n\t" \
    "s_waitcnt vmcnt(12) lgkmcnt(0)\n\t" \
    "s_barrier\n\t" \
    "s_add_u32 m0, s82, 65536\n\t" \
    "s_nop 0\n\t" \
    "global_load_lds_dwordx4 v200, s[42:43]\n\t" \
    "v_add_u32 v200, 131072, v200\n\t" \
    "s_add_u32 m0, s82, 69632\n\t" \
    "s_nop 0\n\t" \
    "global_load_lds_dwordx4 v200, s[42:43]\n\t" \
    "v_add_u32 v200, 131072, v200\n\t" \
    "s_add_u32 m0, s82, 73728\n\t" \
    "s_nop 0\n\t" \
    "global_load_lds_dwordx4 v200, s[42:43]\n\t" \
    "v_add_u32 v200, 131072, v200\n\t" \
    "s_add_u32 m0, s82, 77824\n\t" \
    "s_nop 0\n\t" \
    "global_load_lds_dwordx4 v200, s[42:43]\n\t" \
    "v_add_u32 v200, 131072, v200\n\t" \
    "ds_read_b64_tr_b16 a[0:1], v202 offset:36864\n\t" \
    "ds_read_b64_tr_b16 a[2:3], v202 offset:37888\n\t" \
    "ds_read_b64_tr_b16 a[4:5], v202 offset:45056\n\t" \
    "ds_read_b64_tr_b16 a[6:7], v202 offset:46080\n\t" \
    "ds_read_b64_tr_b16 a[72:73], v203 offset:36864\n\t" \
    "ds_read_b64_tr_b16 a[74:75], v203 offset:37888\n\t" \
    "ds_read_b64_tr_b16 a[76:77], v203 offset:45056\n\t" \
    "ds_read_b64_tr_b16 a[78:79], v203 offset:46080\n\t" \
    "v_lshlrev_b32 v0, 4, v223\n\t" \
    "s_mul_i32 s69, s66, 2048\n\t" \
    "s_bfe_u32 s71, s67, 0x20003\n\t" \
    "s_mul_i32 s71, s71, 65536\n\t" \
    "s_add_u32 s69, s69, s71\n\t" \
    "s_add_u32 s69, s69, 32768\n\t" \
    "v_lshl_add_u32 v1, v223, 4, s69\n\t" \
    "s_bfe_u32 s71, s67, 0x20003\n\t" \
    "s_mul_i32 s69, s71, 65536\n\t" \
    "s_add_u32 s69, s69, 53248\n\t" \
    "v_lshl_add_u32 v2, v223, 4, s69\n\t" \
    "s_bfe_u32 s71, s67, 0x20003\n\t" \
    "s_mul_i32 s69, s71, 65536\n\t" \
    "s_add_u32 s69, s69, 32768\n\t" \
    "v_lshl_add_u32 v34, v223, 4, s69\n\t" \
    "s_mov_b32 s69, 17476\n\t" \
    "v_bfe_u32 v190, v223, 1, 1\n\t" \
    "v_mov_b32 v10, s69\n\t" \
    "s_mov_b32 s70, 43690\n\t" \
    "v_mad_u32_u24 v10, v190, s70, v10\n\t" \
    "v_mov_b32 v11, 0\n\t" \
    "s_waitcnt vmcnt(12) lgkmcnt(0)\n\t" \
    "s_barrier\n\t" \
    "s_add_u32 m0, s82, 81920\n\t" \
    "s_nop 0\n\t" \
    "global_load_lds_dwordx4 v200, s[42:43]\n\t" \
    "v_add_u32 v200, 131072, v200\n\t" \
    "s_add_u32 m0, s82, 86016\n\t" \
    "s_nop 0\n\t" \
    "global_load_lds_dwordx4 v200, s[42:43]\n\t" \
    "v_add_u32 v200, 131072, v200\n\t" \
    "s_add_u32 m0, s82, 90112\n\t" \
    "s_nop 0\n\t" \
    "global_load_lds_dwordx4 v200, s[42:43]\n\t" \
    "v_add_u32 v200, 131072, v200\n\t" \
    "s_add_u32 m0, s82, 94208\n\t" \
    "s_nop 0\n\t" \
    "global_load_lds_dwordx4 v200, s[42:43]\n\t" \
    "v_add_u32 v200, 131072, v200\n\t" \
    "ds_read_b64_tr_b16 a[8:9], v202 offset:53248\n\t" \
    "ds_read_b64_tr_b16 a[10:11], v202 offset:54272\n\t" \
    "ds_read_b64_tr_b16 a[12:13], v202 offset:61440\n\t" \
    "ds_read_b64_tr_b16 a[14:15], v202 offset:62464\n\t" \
    "ds_read_b64_tr_b16 a[80:81], v203 offset:53248\n\t" \
    "ds_read_b64_tr_b16 a[82:83], v203 offset:54272\n\t" \
    "ds_read_b64_tr_b16 a[84:85], v203 offset:61440\n\t" \
    "ds_read_b64_tr_b16 a[86:87], v203 offset:62464\n\t" \
    "s_mul_i32 s69, s66, 512\n\t" \
    "s_add_u32 s69, s69, 16384\n\t" \
    "v_and_b32 v191, 13, v223\n\t" \
    "v_lshl_add_u32 v12, v191, 5, s69\n\t" \
    "v_lshl_add_u32 v12, v190, 2, v12\n\t" \
    "v_lshl_add_u32 v12, v187, 3, v12\n\t" \
    "s_mov_b32 s69, 16384\n\t" \
    "v_and_b32 v192, 15, v223\n\t" \
    "v_lshl_add_u32 v13, v192, 3, s69\n\t" \
    "v_lshl_add_u32 v13, v187, 8, v13\n\t" \
    "s_bfe_u32 s71, s67, 0x20003\n\t" \
    "s_mul_i32 s69, s71, 65536\n\t" \
    "s_add_u32 s69, s69, 266240\n\t" \
    "v_and_b32 v193, 31, v223\n\t" \
    "v_lshl_add_u32 v20, v193, 5, s69\n\t" \
    "v_lshl_add_u32 v20, v181, 2, v20\n\t" \
    "s_mul_i32 s69, s67, 32\n\t" \
    "s_add_u32 s69, s69, 262144\n\t" \
    "v_lshl_add_u32 v21, v180, 16, s69\n\t" \
    "v_bfe_u32 v194, v223, 2, 3\n\t" \
    "v_lshlrev_b32 v199, 10, v194\n\t" \
    "v_min_u32 v199, 4096, v199\n\t" \
    "v_add_u32 v21, v199, v21\n\t" \
    "s_bfe_u32 s71, s67, 0x20003\n\t" \
    "s_mul_i32 s69, s71, 65536\n\t" \
    "s_add_u32 s69, s69, 267264\n\t" \
    "v_min_u32 v35, 35, v223\n\t" \
    "v_lshl_add_u32 v35, v35, 5, s69\n\t" \
    "v_lshlrev_b32 v22, 2, v223\n\t" \
    "s_waitcnt vmcnt(12) lgkmcnt(0)\n\t" \
    "s_barrier\n\t" \
    "s_add_u32 m0, s82, 98304\n\t" \
    "s_nop 0\n\t" \
    "global_load_lds_dwordx4 v200, s[42:43]\n\t" \
    "v_add_u32 v200, 131072, v200\n\t" \
    "s_add_u32 m0, s82, 102400\n\t" \
    "s_nop 0\n\t" \
    "global_load_lds_dwordx4 v200, s[42:43]\n\t" \
    "v_add_u32 v200, 131072, v200\n\t" \
    "s_add_u32 m0, s82, 106496\n\t" \
    "s_nop 0\n\t" \
    "global_load_lds_dwordx4 v200, s[42:43]\n\t" \
    "v_add_u32 v200, 131072, v200\n\t" \
    "s_add_u32 m0, s82, 110592\n\t" \
    "s_nop 0\n\t" \
    "global_load_lds_dwordx4 v200, s[42:43]\n\t" \
    "v_add_u32 v200, 131072, v200\n\t" \
    "v_add_u32 v202, 69632, v202\n\t" \
    "v_add_u32 v203, 69632, v203\n\t" \
    "ds_read_b64_tr_b16 a[16:17], v202 offset:0\n\t" \
    "ds_read_b64_tr_b16 a[18:19], v202 offset:1024\n\t" \
    "ds_read_b64_tr_b16 a[20:21], v202 offset:8192\n\t" \
    "ds_read_b64_tr_b16 a[22:23], v202 offset:9216\n\t" \
    "ds_read_b64_tr_b16 a[88:89], v203 offset:0\n\t" \
    "ds_read_b64_tr_b16 a[90:91], v203 offset:1024\n\t" \
    "ds_read_b64_tr_b16 a[92:93], v203 offset:8192\n\t" \
    "ds_read_b64_tr_b16 a[94:95], v203 offset:9216\n\t" \
    "s_mul_i32 s69, s66, 147456\n\t" \
    "s_mul_i32 s71, s67, 256\n\t" \
    "s_add_u32 s69, s69, s71\n\t" \
    "v_lshl_add_u32 v14, v192, 4, s69\n\t" \
    "v_lshl_add_u32 v14, v187, 14, v14\n\t" \
    "s_mul_i32 s69, s66, 147456\n\t" \
    "s_mul_i32 s71, s67, 256\n\t" \
    "s_add_u32 s69, s69, s71\n\t" \
    "s_add_u32 s69, s69, 65536\n\t" \
    "v_lshl_add_u32 v15, v192, 4, s69\n\t" \
    "v_lshl_add_u32 v15, v187, 14, v15\n\t" \
    "s_mul_i32 s69, s66, 147456\n\t" \
    "s_mul_i32 s71, s67, 256\n\t" \
    "s_add_u32 s69, s69, s71\n\t" \
    "s_add_u32 s69, s69, 131072\n\t" \
    "v_lshl_add_u32 v16, v192, 4, s69\n\t" \
    "v_lshl_add_u32 v16, v187, 2, v16\n\t" \
    "s_mul_i32 s97, s67, 171\n\t" \
    "s_lshr_b32 s97, s97, 9\n\t" \
    "s_mul_i32 s97, s97, 131072\n\t" \
    "s_mul_i32 s71, s66, 2304\n\t" \
    "s_add_u32 s69, s97, s71\n\t" \
    "s_mul_i32 s71, s67, 655360\n\t" \
    "s_add_u32 s69, s69, s71\n\t" \
    "v_lshl_add_u32 v17, v223, 4, s69\n\t" \
    "s_mul_i32 s71, s66, 2304\n\t" \
    "s_add_u32 s69, s97, s71\n\t" \
    "s_mul_i32 s71, s67, 655360\n\t" \
    "s_add_u32 s69, s69, s71\n\t" \
    "s_add_u32 s69, s69, 2048\n\t" \
    "v_lshl_add_u32 v18, v192, 4, s69\n\t" \
    "v_lshl_add_u32 v18, v187, 2, v18\n\t" \
    "s_waitcnt vmcnt(12) lgkmcnt(0)\n\t" \
    "s_barrier\n\t" \
    "s_add_u32 m0, s82, 0\n\t" \
    "s_nop 0\n\t" \
    "global_load_lds_dwordx4 v200, s[42:43]\n\t" \
    "v_add_u32 v200, 131072, v200\n\t" \
    "s_add_u32 m0, s82, 4096\n\t" \
    "s_nop 0\n\t" \
    "global_load_lds_dwordx4 v200, s[42:43]\n\t" \
    "v_add_u32 v200, 131072, v200\n\t" \
    "s_add_u32 m0, s82, 8192\n\t" \
    "s_nop 0\n\t" \
    "global_load_lds_dwordx4 v200, s[42:43]\n\t" \
    "v_add_u32 v200, 131072, v200\n\t" \
    "s_add_u32 m0, s82, 12288\n\t" \
    "s_nop 0\n\t" \
    "global_load_lds_dwordx4 v200, s[42:43]\n\t" \
    "v_add_u32 v200, 131072, v200\n\t" \
    "ds_read_b64_tr_b16 a[24:25], v202 offset:16384\n\t" \
    "ds_read_b64_tr_b16 a[26:27], v202 offset:17408\n\t" \
    "ds_read_b64_tr_b16 a[28:29], v202 offset:24576\n\t" \
    "ds_read_b64_tr_b16 a[30:31], v202 offset:25600\n\t" \
    "ds_read_b64_tr_b16 a[96:97], v203 offset:16384\n\t" \
    "ds_read_b64_tr_b16 a[98:99], v203 offset:17408\n\t" \
    "ds_read_b64_tr_b16 a[100:101], v203 offset:24576\n\t" \
    "ds_read_b64_tr_b16 a[102:103], v203 offset:25600\n\t" \
    "s_mov_b32 s77, 3019898880\n\t" \
    "s_mov_b32 s78, 3054198966\n\t" \
    "s_mov_b32 s71, 3\n\t" \
    "vv_E1_%=:\n\t" \
    "s_memtime s[54:55]\n\t" \
    "s_nop 0\n\t" \
    "s_memrealtime s[4:5]\n\t" \
    "s_nop 0\n\t" \
    "s_memrealtime s[6:7]\n\t" \
    "s_nop 0\n\t" \
    "s_memrealtime s[8:9]\n\t" \
    "s_nop 0\n\t" \
    "s_memrealtime s[10:11]\n\t" \
    "s_nop 0\n\t" \
    "s_memrealtime s[12:13]\n\t" \
    "s_nop 0\n\t" \
    "s_memrealtime s[14:15]\n\t" \
    "s_nop 0\n\t" \
    "s_memrealtime s[16:17]\n\t" \
    "s_nop 0\n\t" \
    "s_memrealtime s[18:19]\n\t" \
    "s_nop 0\n\t" \
    "s_memrealtime s[20:21]\n\t" \
    "s_nop 0\n\t" \
    "s_memrealtime s[22:23]\n\t" \
    "s_nop 0\n\t" \
    "s_memrealtime s[24:25]\n\t" \
    "s_nop 0\n\t" \
    "s_memrealtime s[26:27]\n\t" \
    "s_nop 0\n\t" \
    "s_memrealtime s[28:29]\n\t" \
    "s_nop 0\n\t" \
    "s_memtime s[56:57]\n\t" \
    "s_mov_b32 s70, 1\n\t" \
    "s_sub_u32 s71, s71, 1\n\t" \
    "s_cselect_b32 s69, 0, -1\n\t" \
    "s_waitcnt lgkmcnt(0)\n\t" \
    "s_sub_u32 s54, s56, s54\n\t" \
    "s_and_b32 s54, s54, s69\n\t" \
    "s_cmp_gt_u32 s54, 124\n\t" \
    "s_cbranch_scc1 vv_E1_%=\n\t" \
    "s_cmp_eq_u32 s4, s28\n\t" \
    "s_addc_u32 s70, s70, 0\n\t" \
    "s_cmp_eq_u32 s6, s28\n\t" \
    "s_addc_u32 s70, s70, 0\n\t" \
    "s_cmp_eq_u32 s8, s28\n\t" \
    "s_addc_u32 s70, s70, 0\n\t" \
    "s_cmp_eq_u32 s10, s28\n\t" \
    "s_addc_u32 s70, s70, 0\n\t" \
    "s_cmp_eq_u32 s12, s28\n\t" \
    "s_addc_u32 s70, s70, 0\n\t" \
    "s_cmp_eq_u32 s14, s28\n\t" \
    "s_addc_u32 s70, s70, 0\n\t" \
    "s_cmp_eq_u32 s16, s28\n\t" \
    "s_addc_u32 s70, s70, 0\n\t" \
    "s_cmp_eq_u32 s18, s28\n\t" \
    "s_addc_u32 s70, s70, 0\n\t" \
    "s_cmp_eq_u32 s20, s28\n\t" \
    "s_addc_u32 s70, s70, 0\n\t" \
    "s_cmp_eq_u32 s22, s28\n\t" \
    "s_addc_u32 s70, s70, 0\n\t" \
    "s_cmp_eq_u32 s24, s28\n\t" \
    "s_addc_u32 s70, s70, 0\n\t" \
    "s_cmp_eq_u32 s26, s28\n\t" \
    "s_addc_u32 s70, s70, 0\n\t" \
    "s_mov_b32 s75, s73\n\t" \
    "s_mov_b32 s76, s74\n\t" \
    "s_lshl_b32 s70, s70, 3\n\t" \
    "s_sub_u32 s73, s56, s70\n\t" \
    "s_lshl_b32 s74, s28, 4\n\t" \
    "s_waitcnt vmcnt(12) lgkmcnt(0)\n\t" \
    "s_barrier\n\t" \
    "s_add_u32 m0, s82, 16384\n\t" \
    "s_nop 0\n\t" \
    "global_load_lds_dwordx4 v200, s[42:43]\n\t" \
    "v_add_u32 v200, 131072, v200\n\t" \
    "s_add_u32 m0, s82, 20480\n\t" \
    "s_nop 0\n\t" \
    "global_load_lds_dwordx4 v200, s[42:43]\n\t" \
    "v_add_u32 v200, 131072, v200\n\t" \
    "s_add_u32 m0, s82, 24576\n\t" \
    "s_nop 0\n\t" \
    "global_load_lds_dwordx4 v200, s[42:43]\n\t" \
    "v_add_u32 v200, 131072, v200\n\t" \
    "s_add_u32 m0, s82, 28672\n\t" \
    "s_nop 0\n\t" \
    "global_load_lds_dwordx4 v200, s[42:43]\n\t" \
    "v_add_u32 v200, 131072, v200\n\t" \
    "ds_read_b64_tr_b16 a[32:33], v202 offset:32768\n\t" \
    "ds_read_b64_tr_b16 a[34:35], v202 offset:33792\n\t" \
    "ds_read_b64_tr_b16 a[36:37], v202 offset:40960\n\t" \
    "ds_read_b64_tr_b16 a[38:39], v202 offset:41984\n\t" \
    "ds_read_b64_tr_b16 a[104:105], v203 offset:32768\n\t" \
    "ds_read_b64_tr_b16 a[106:107], v203 offset:33792\n\t" \
    "ds_read_b64_tr_b16 a[108:109], v203 offset:40960\n\t" \
    "ds_read_b64_tr_b16 a[110:111], v203 offset:41984\n\t" \
    "s_waitcnt vmcnt(12) lgkmcnt(0)\n\t" \
    "s_barrier\n\t" \
    "s_add_u32 m0, s96, 0\n\t" \
    "s_nop 0\n\t" \
    "global_load_lds_dwordx4 v201, s[44:45] offset:0\n\t" \
    "s_add_u32 m0, s96, 896\n\t" \
    "s_nop 0\n\t" \
    "global_load_lds_dwordx4 v201, s[44:45] offset:128\n\t" \
    "s_add_u32 m0, s96, 1792\n\t" \
    "s_nop 0\n\t" \
    "global_load_lds_dwordx4 v201, s[44:45] offset:256\n\t" \
    "s_add_u32 m0, s96, 2688\n\t" \
    "s_nop 0\n\t" \
    "global_load_lds_dwordx4 v201, s[44:45] offset:384\n\t" \
    "s_add_u32 m0, s96, 3584\n\t" \
    "s_nop 0\n\t" \
    "global_load_lds_dwordx4 v201, s[44:45] offset:512\n\t" \
    "s_add_u32 m0, s96, 4480\n\t" \
    "s_nop 0\n\t" \
    "global_load_lds_dwordx4 v201, s[44:45] offset:640\n\t" \
    "s_add_u32 m0, s96, 5376\n\t" \
    "s_nop 0\n\t" \
    "global_load_lds_dwordx4 v201, s[44:45] offset:768\n\t" \
    "s_add_u32 m0, s96, 6272\n\t" \
    "s_nop 0\n\t" \
    "global_load_lds_dwordx4 v201, s[44:45] offset:896\n\t" \
    "s_add_u32 m0, s96, 7168\n\t" \
    "s_nop 0\n\t" \
    "global_load_lds_dwordx4 v201, s[44:45] offset:1024\n\t" \
    "v_add_u32 v201, 36864, v201\n\t" \
    "ds_read_b64_tr_b16 a[40:41], v202 offset:49152\n\t" \
    "ds_read_b64_tr_b16 a[42:43], v202 offset:50176\n\t" \
    "ds_read_b64_tr_b16 a[44:45], v202 offset:57344\n\t" \
    "ds_read_b64_tr_b16 a[46:47], v202 offset:58368\n\t" \
    "ds_read_b64_tr_b16 a[112:113], v203 offset:49152\n\t" \
    "ds_read_b64_tr_b16 a[114:115], v203 offset:50176\n\t" \
    "ds_read_b64_tr_b16 a[116:117], v203 offset:57344\n\t" \
    "ds_read_b64_tr_b16 a[118:119], v203 offset:58368\n\t" \
    "s_waitcnt vmcnt(17) lgkmcnt(0)\n\t" \
    "s_barrier\n\t" \
    "v_add_u32 v202, 65536, v202\n\t" \
    "v_add_u32 v203, 65536, v203\n\t" \
    "ds_read_b64_tr_b16 a[48:49], v202 offset:0\n\t" \
    "ds_read_b64_tr_b16 a[50:51], v202 offset:1024\n\t" \
    "ds_read_b64_tr_b16 a[52:53], v202 offset:8192\n\t" \
    "ds_read_b64_tr_b16 a[54:55], v202 offset:9216\n\t" \
    "ds_read_b64_tr_b16 a[120:121], v203 offset:0\n\t" \
    "ds_read_b64_tr_b16 a[122:123], v203 offset:1024\n\t" \
    "ds_read_b64_tr_b16 a[124:125], v203 offset:8192\n\t" \
    "ds_read_b64_tr_b16 a[126:127], v203 offset:9216\n\t" \
    "s_waitcnt vmcnt(13) lgkmcnt(0)\n\t" \
    "s_barrier\n\t" \
    "s_add_u32 m0, s96, 73728\n\t" \
    "s_nop 0\n\t" \
    "global_load_lds_dwordx4 v201, s[44:45] offset:0\n\t" \
    "s_add_u32 m0, s96, 74624\n\t" \
    "s_nop 0\n\t" \
    "global_load_lds_dwordx4 v201, s[44:45] offset:128\n\t" \
    "s_add_u32 m0, s96, 75520\n\t" \
    "s_nop 0\n\t" \
    "global_load_lds_dwordx4 v201, s[44:45] offset:256\n\t" \
    "s_add_u32 m0, s96, 76416\n\t" \
    "s_nop 0\n\t" \
    "global_load_lds_dwordx4 v201, s[44:45] offset:384\n\t" \
    "s_add_u32 m0, s96, 77312\n\t" \
    "s_nop 0\n\t" \
    "global_load_lds_dwordx4 v201, s[44:45] offset:512\n\t" \
    "s_add_u32 m0, s96, 78208\n\t" \
    "s_nop 0\n\t" \
    "global_load_lds_dwordx4 v201, s[44:45] offset:640\n\t" \
    "s_add_u32 m0, s96, 79104\n\t" \
    "s_nop 0\n\t" \
    "global_load_lds_dwordx4 v201, s[44:45] offset:768\n\t" \
    "s_add_u32 m0, s96, 80000\n\t" \
    "s_nop 0\n\t" \
    "global_load_lds_dwordx4 v201, s[44:45] offset:896\n\t" \
    "s_add_u32 m0, s96, 80896\n\t" \
    "s_nop 0\n\t" \
    "global_load_lds_dwordx4 v201, s[44:45] offset:1024\n\t" \
    "v_add_u32 v201, 36864, v201\n\t" \
    "v_add_u32 v202, -98304, v202\n\t" \
    "v_add_u32 v203, -98304, v203\n\t" \
    "ds_read_b64_tr_b16 a[56:57], v202 offset:0\n\t" \
    "ds_read_b64_tr_b16 a[58:59], v202 offset:1024\n\t" \
    "ds_read_b64_tr_b16 a[60:61], v202 offset:8192\n\t" \
    "ds_read_b64_tr_b16 a[62:63], v202 offset:9216\n\t" \
    "ds_read_b64_tr_b16 a[128:129], v203 offset:0\n\t" \
    "ds_read_b64_tr_b16 a[130:131], v203 offset:1024\n\t" \
    "ds_read_b64_tr_b16 a[132:133], v203 offset:8192\n\t" \
    "ds_read_b64_tr_b16 a[134:135], v203 offset:9216\n\t" \
    "s_waitcnt vmcnt(18) lgkmcnt(0)\n\t" \
    "s_barrier\n\t" \
    "s_add_u32 m0, s96, 110592\n\t" \
    "s_nop 0\n\t" \
    "global_load_lds_dwordx4 v201, s[44:45] offset:0\n\t" \
    "s_add_u32 m0, s96, 111488\n\t" \
    "s_nop 0\n\t" \
    "global_load_lds_dwordx4 v201, s[44:45] offset:128\n\t" \
    "s_add_u32 m0, s96, 112384\n\t" \
    "s_nop 0\n\t" \
    "global_load_lds_dwordx4 v201, s[44:45] offset:256\n\t" \
    "s_add_u32 m0, s96, 113280\n\t" \
    "s_nop 0\n\t" \
    "global_load_lds_dwordx4 v201, s[44:45] offset:384\n\t" \
    "s_add_u32 m0, s96, 114176\n\t" \
    "s_nop 0\n\t" \
    "global_load_lds_dwordx4 v201, s[44:45] offset:512\n\t" \
    "s_add_u32 m0, s96, 115072\n\t" \
    "s_nop 0\n\t" \
    "global_load_lds_dwordx4 v201, s[44:45] offset:640\n\t" \
    "s_add_u32 m0, s96, 115968\n\t" \
    "s_nop 0\n\t" \
    "global_load_lds_dwordx4 v201, s[44:45] offset:768\n\t" \
    "s_add_u32 m0, s96, 116864\n\t" \
    "s_nop 0\n\t" \
    "global_load_lds_dwordx4 v201, s[44:45] offset:896\n\t" \
    "s_add_u32 m0, s96, 117760\n\t" \
    "s_nop 0\n\t" \
    "global_load_lds_dwordx4 v201, s[44:45] offset:1024\n\t" \
    "v_add_u32 v201, 36864, v201\n\t" \
    "ds_read_b64_tr_b16 a[64:65], v202 offset:16384\n\t" \
    "ds_read_b64_tr_b16 a[66:67], v202 offset:17408\n\t" \
    "ds_read_b64_tr_b16 a[68:69], v202 offset:24576\n\t" \
    "ds_read_b64_tr_b16 a[70:71], v202 offset:25600\n\t" \
    "ds_read_b64_tr_b16 a[136:137], v203 offset:16384\n\t" \
    "ds_read_b64_tr_b16 a[138:139], v203 offset:17408\n\t" \
    "ds_read_b64_tr_b16 a[140:141], v203 offset:24576\n\t" \
    "ds_read_b64_tr_b16 a[142:143], v203 offset:25600\n\t" \
    "s_waitcnt vmcnt(18) lgkmcnt(0)\n\t" \
    "s_barrier\n\t" \
    "s_add_u32 m0, s96, 36864\n\t" \
    "s_nop 0\n\t" \
    "global_load_lds_dwordx4 v201, s[44:45] offset:0\n\t" \
    "s_add_u32 m0, s96, 37760\n\t" \
    "s_nop 0\n\t" \
    "global_load_lds_dwordx4 v201, s[44:45] offset:128\n\t" \
    "s_add_u32 m0, s96, 38656\n\t" \
    "s_nop 0\n\t" \
    "global_load_lds_dwordx4 v201, s[44:45] offset:256\n\t" \
    "s_add_u32 m0, s96, 39552\n\t" \
    "s_nop 0\n\t" \
    "global_load_lds_dwordx4 v201, s[44:45] offset:384\n\t" \
    "s_add_u32 m0, s96, 40448\n\t" \
    "s_nop 0\n\t" \
    "global_load_lds_dwordx4 v201, s[44:45] offset:512\n\t" \
    "s_add_u32 m0, s96, 41344\n\t" \
    "s_nop 0\n\t" \
    "global_load_lds_dwordx4 v201, s[44:45] offset:640\n\t" \
    "s_add_u32 m0, s96, 42240\n\t" \
    "s_nop 0\n\t" \
    "global_load_lds_dwordx4 v201, s[44:45] offset:768\n\t" \
    "s_add_u32 m0, s96, 43136\n\t" \
    "s_nop 0\n\t" \
    "global_load_lds_dwordx4 v201, s[44:45] offset:896\n\t" \
    "s_add_u32 m0, s96, 44032\n\t" \
    "s_nop 0\n\t" \
    "global_load_lds_dwordx4 v201, s[44:45] offset:1024\n\t" \
    "v_add_u32 v201, 36864, v201\n\t" \
    "ds_read_b64_tr_b16 a[144:145], v204 offset:0\n\t" \
    "ds_read_b64_tr_b16 a[146:147], v204 offset:18432\n\t" \
    "ds_read_b64_tr_b16 a[160:161], v205 offset:0\n\t" \
    "ds_read_b64_tr_b16 a[162:163], v205 offset:18432\n\t" \
    "ds_read_b64_tr_b16 a[176:177], v206 offset:0\n\t" \
    "ds_read_b64_tr_b16 a[178:179], v206 offset:18432\n\t" \
    "s_waitcnt lgkmcnt(6)\n\t" \
    "ds_read_b64_tr_b16 a[192:193], v207 offset:0\n\t" \
    "ds_read_b64_tr_b16 a[194:195], v207 offset:18432\n\t" \
    "ds_read_b64_tr_b16 a[208:209], v204 offset:1024\n\t" \
    "ds_read_b64_tr_b16 a[210:211], v204 offset:19456\n\t" \
    "ds_read_b64_tr_b16 a[224:225], v205 offset:1024\n\t" \
    "ds_read_b64_tr_b16 a[226:227], v205 offset:19456\n\t" \
    "s_waitcnt lgkmcnt(6)\n\t" \
    "ds_read_b64_tr_b16 a[240:241], v206 offset:1024\n\t" \
    "ds_read_b64_tr_b16 a[242:243], v206 offset:19456\n\t" \
    "ds_read_b64_tr_b16 v[224:225], v207 offset:1024\n\t" \
    "ds_read_b64_tr_b16 v[226:227], v207 offset:19456\n\t" \
    "ds_read_b64_tr_b16 v[240:241], v204 offset:2048\n\t" \
    "ds_read_b64_tr_b16 v[242:243], v204 offset:20480\n\t" \
    "s_waitcnt lgkmcnt(6)\n\t" \
    "s_waitcnt vmcnt(18) lgkmcnt(0)\n\t" \
    "s_barrier\n\t" \
    "v_mbcnt_lo_u32_b32 v140, -1, 0\n\t" \
    "v_mbcnt_hi_u32_b32 v140, -1, v140\n\t" \
    "v_min_u32 v140, 59, v140\n\t" \
    "v_lshlrev_b32 v140, 6, v140\n\t" \
    "v_add_u32 v140, 6258688, v140\n\t" \
    "v_mov_b32 v141, 6266880\n\t" \
    "global_load_dword v141, v141, s[36:37] sc1\n\t" \
    "global_load_dwordx2 v[142:143], v140, s[36:37] sc1\n\t" \
    "v_add_u32 v204, 73728, v204\n\t" \
    "v_add_u32 v205, 73728, v205\n\t" \
    "v_add_u32 v206, 73728, v206\n\t" \
    "v_add_u32 v207, 73728, v207\n\t" \
    "ds_read_b64_tr_b16 a[148:149], v204 offset:0\n\t" \
    "ds_read_b64_tr_b16 a[150:151], v204 offset:18432\n\t" \
    "ds_read_b64_tr_b16 a[164:165], v205 offset:0\n\t" \
    "ds_read_b64_tr_b16 a[166:167], v205 offset:18432\n\t" \
    "ds_read_b64_tr_b16 a[180:181], v206 offset:0\n\t" \
    "ds_read_b64_tr_b16 a[182:183], v206 offset:18432\n\t" \
    "s_waitcnt lgkmcnt(6)\n\t" \
    "ds_read_b64_tr_b16 a[196:197], v207 offset:0\n\t" \
    "ds_read_b64_tr_b16 a[198:199], v207 offset:18432\n\t" \
    "ds_read_b64_tr_b16 a[212:213], v204 offset:1024\n\t" \
    "ds_read_b64_tr_b16 a[214:215], v204 offset:19456\n\t" \
    "ds_read_b64_tr_b16 a[228:229], v205 offset:1024\n\t" \
    "ds_read_b64_tr_b16 a[230:231], v205 offset:19456\n\t" \
    "s_waitcnt lgkmcnt(6)\n\t" \
    "ds_read_b64_tr_b16 a[244:245], v206 offset:1024\n\t" \
    "ds_read_b64_tr_b16 a[246:247], v206 offset:19456\n\t" \
    "ds_read_b64_tr_b16 v[228:229], v207 offset:1024\n\t" \
    "ds_read_b64_tr_b16 v[230:231], v207 offset:19456\n\t" \
    "ds_read_b64_tr_b16 v[244:245], v204 offset:2048\n\t" \
    "ds_read_b64_tr_b16 v[246:247], v204 offset:20480\n\t" \
    "s_waitcnt lgkmcnt(6)\n\t" \
    "s_waitcnt vmcnt(11) lgkmcnt(0)\n\t" \
    "s_barrier\n\t" \
    "ds_read_b64_tr_b16 a[152:153], v204 offset:36864\n\t" \
    "ds_read_b64_tr_b16 a[154:155], v204 offset:55296\n\t" \
    "ds_read_b64_tr_b16 a[168:169], v205 offset:36864\n\t" \
    "ds_read_b64_tr_b16 a[170:171], v205 offset:55296\n\t" \
    "ds_read_b64_tr_b16 a[184:185], v206 offset:36864\n\t" \
    "ds_read_b64_tr_b16 a[186:187], v206 offset:55296\n\t" \
    "s_waitcnt lgkmcnt(6)\n\t" \
    "ds_read_b64_tr_b16 a[200:201], v207 offset:36864\n\t" \
    "ds_read_b64_tr_b16 a[202:203], v207 offset:55296\n\t" \
    "ds_read_b64_tr_b16 a[216:217], v204 offset:37888\n\t" \
    "ds_read_b64_tr_b16 a[218:219], v204 offset:56320\n\t" \
    "ds_read_b64_tr_b16 a[232:233], v205 offset:37888\n\t" \
    "ds_read_b64_tr_b16 a[234:235], v205 offset:56320\n\t" \
    "s_waitcnt lgkmcnt(6)\n\t" \
    "ds_read_b64_tr_b16 a[248:249], v206 offset:37888\n\t" \
    "ds_read_b64_tr_b16 a[250:251], v206 offset:56320\n\t" \
    "ds_read_b64_tr_b16 v[232:233], v207 offset:37888\n\t" \
    "ds_read_b64_tr_b16 v[234:235], v207 offset:56320\n\t" \
    "ds_read_b64_tr_b16 v[248:249], v204 offset:38912\n\t" \
    "ds_read_b64_tr_b16 v[250:251], v204 offset:57344\n\t" \
    "s_waitcnt lgkmcnt(6)\n\t" \
    "s_waitcnt vmcnt(2) lgkmcnt(0)\n\t" \
    "s_barrier\n\t" \
    "v_add_u32 v204, -36864, v204\n\t" \
    "v_add_u32 v205, -36864, v205\n\t" \
    "v_add_u32 v206, -36864, v206\n\t" \
    "v_add_u32 v207, -36864, v207\n\t" \
    "ds_read_b64_tr_b16 a[156:157], v204 offset:0\n\t" \
    "ds_read_b64_tr_b16 a[158:159], v204 offset:18432\n\t" \
    "ds_read_b64_tr_b16 a[172:173], v205 offset:0\n\t" \
    "ds_read_b64_tr_b16 a[174:175], v205 offset:18432\n\t" \
    "ds_read_b64_tr_b16 a[188:189], v206 offset:0\n\t" \
    "ds_read_b64_tr_b16 a[190:191], v206 offset:18432\n\t" \
    "s_waitcnt lgkmcnt(6)\n\t" \
    "ds_read_b64_tr_b16 a[204:205], v207 offset:0\n\t" \
    "ds_read_b64_tr_b16 a[206:207], v207 offset:18432\n\t" \
    "ds_read_b64_tr_b16 a[220:221], v204 offset:1024\n\t" \
    "ds_read_b64_tr_b16 a[222:223], v204 offset:19456\n\t" \
    "ds_read_b64_tr_b16 a[236:237], v205 offset:1024\n\t" \
    "ds_read_b64_tr_b16 a[238:239], v205 offset:19456\n\t" \
    "s_waitcnt lgkmcnt(6)\n\t" \
    "ds_read_b64_tr_b16 a[252:253], v206 offset:1024\n\t" \
    "ds_read_b64_tr_b16 a[254:255], v206 offset:19456\n\t" \
    "ds_read_b64_tr_b16 v[236:237], v207 offset:1024\n\t" \
    "ds_read_b64_tr_b16 v[238:239], v207 offset:19456\n\t" \
    "ds_read_b64_tr_b16 v[252:253], v204 offset:2048\n\t" \
    "ds_read_b64_tr_b16 v[254:255], v204 offset:20480\n\t" \
    "s_waitcnt lgkmcnt(6)\n\t" \
    "s_waitcnt vmcnt(0) lgkmcnt(0)\n\t" \
    "s_barrier\n\t" \
    "v_sub_u32 v142, v142, v141\n\t" \
    "v_cmp_ne_u32 vcc, 1, v142\n\t" \
    "s_cbranch_vccz vv_Cok4_%=\n\t" \
    "vv_C3_%=:\n\t" \
    "global_load_dwordx2 v[142:143], v140, s[36:37] sc1\n\t" \
    "s_waitcnt vmcnt(0)\n\t" \
    "v_sub_u32 v142, v142, v141\n\t" \
    "v_cmp_ne_u32 vcc, 1, v142\n\t" \
    "s_cbranch_vccz vv_Cok4_%=\n\t" \
    "s_cmp_eq_u32 s68, 0\n\t" \
    "s_cbranch_scc1 vv_F5_%=\n\t" \
    "s_sub_u32 s68, s68, 1\n\t" \
    "s_sleep 4\n\t" \
    "s_branch vv_C3_%=\n\t" \
    "vv_F5_%=:\n\t" \
    "v_mov_b32 v144, 6095104\n\t" \
    "v_mov_b32 v145, 4\n\t" \
    "global_store_dword v144, v145, s[36:37] sc1\n\t" \
    "s_waitcnt vmcnt(0)\n\t" \
    "s_branch vv_Cok4_%=\n\t" \
    "vv_Cok4_%=:\n\t" \
    "s_cmp_lg_u32 s66, 0\n\t" \
    "s_cbranch_scc1 vv_Ca6_%=\n\t" \
    "v_mov_b32 v140, 6176768\n\t" \
    "s_mov_b64 exec, 1\n\t" \
    "global_atomic_add v140, v142, s[36:37] sc1\n\t" \
    "s_mov_b64 exec, -1\n\t" \
    "vv_Ca6_%=:\n\t" \
    "v_min_u32_dpp v143, v143, v143 row_shl:6 row_mask:0xf bank_mask:0xf\n\t" \
    "s_nop 0\n\t" \
    "v_readlane_b32 s69, v143, 0\n\t" \
    "v_readlane_b32 s70, v143, 1\n\t" \
    "s_nop 1\n\t" \
    "s_cmp_lt_u32 s70, s69\n\t" \
    "s_cselect_b32 s62, 8192, 0\n\t" \
    "s_cselect_b32 s69, s70, s69\n\t" \
    "s_mov_b32 s71, s69\n\t" \
    "v_readlane_b32 s69, v143, 2\n\t" \
    "v_readlane_b32 s70, v143, 3\n\t" \
    "s_nop 1\n\t" \
    "s_cmp_lt_u32 s70, s69\n\t" \
    "s_cselect_b32 s63, 8192, 0\n\t" \
    "s_cselect_b32 s69, s70, s69\n\t" \
    "s_max_u32 s71, s69, s71\n\t" \
    "v_readlane_b32 s69, v143, 4\n\t" \
    "v_readlane_b32 s70, v143, 5\n\t" \
    "s_nop 1\n\t" \
    "s_cmp_lt_u32 s70, s69\n\t" \
    "s_cselect_b32 s64, 8192, 0\n\t" \
    "s_cselect_b32 s69, s70, s69\n\t" \
    "s_max_u32 s71, s69, s71\n\t" \
    "s_add_u32 s63, s63, 0x200000\n\t" \
    "s_xor_b32 s52, s64, 8192\n\t" \
    "s_cmp_gt_u32 s71, 625\n\t" \
    "s_cselect_b32 s69, 1, 0\n\t" \
    "s_mul_i32 s70, s69, 160\n\t" \
    "s_add_u32 s86, s86, s70\n\t" \
    "s_mul_i32 s70, s69, 536\n\t" \
    "s_add_u32 s87, s87, s70\n\t" \
    "s_mul_i32 s70, s69, 256\n\t" \
    "s_add_u32 s88, s88, s70\n\t" \
    "v_mov_b32 v148, -1\n\t" \
    "v_mov_b32 v149, -1\n\t" \
    "v_mov_b32 v150, -1\n\t" \
    "v_mov_b32 v151, -1\n\t" \
    "s_mov_b32 s71, 3\n\t" \
    "vv_E7_%=:\n\t" \
    "s_memtime s[54:55]\n\t" \
    "s_nop 0\n\t" \
    "s_memrealtime s[4:5]\n\t" \
    "s_nop 0\n\t" \
    "s_memrealtime s[6:7]\n\t" \
    "s_nop 0\n\t" \
    "s_memrealtime s[8:9]\n\t" \
    "s_nop 0\n\t" \
    "s_memrealtime s[10:11]\n\t" \
    "s_nop 0\n\t" \
    "s_memrealtime s[12:13]\n\t" \
    "s_nop 0\n\t" \
    "s_memrealtime s[14:15]\n\t" \
    "s_nop 0\n\t" \
    "s_memrealtime s[16:17]\n\t" \
    "s_nop 0\n\t" \
    "s_memrealtime s[18:19]\n\t" \
    "s_nop 0\n\t" \
    "s_memrealtime s[20:21]\n\t" \
    "s_nop 0\n\t" \
    "s_memrealtime s[22:23]\n\t" \
    "s_nop 0\n\t" \
    "s_memrealtime s[24:25]\n\t" \
    "s_nop 0\n\t" \
    "s_memrealtime s[26:27]\n\t" \
    "s_nop 0\n\t" \
    "s_memrealtime s[28:29]\n\t" \
    "s_nop 0\n\t" \
    "s_memtime s[56:57]\n\t" \
    "s_mov_b32 s70, 1\n\t" \
    "s_sub_u32 s71, s71, 1\n\t" \
    "s_cselect_b32 s69, 0, -1\n\t" \
    "s_waitcnt lgkmcnt(0)\n\t" \
    "s_sub_u32 s54, s56, s54\n\t" \
    "s_and_b32 s54, s54, s69\n\t" \
    "s_cmp_gt_u32 s54, 124\n\t" \
    "s_cbranch_scc1 vv_E7_%=\n\t" \
    "s_cmp_eq_u32 s4, s28\n\t" \
    "s_addc_u32 s70, s70, 0\n\t" \
    "s_cmp_eq_u32 s6, s28\n\t" \
    "s_addc_u32 s70, s70, 0\n\t" \
    "s_cmp_eq_u32 s8, s28\n\t" \
    "s_addc_u32 s70, s70, 0\n\t" \
    "s_cmp_eq_u32 s10, s28\n\t" \
    "s_addc_u32 s70, s70, 0\n\t" \
    "s_cmp_eq_u32 s12, s28\n\t" \
    "s_addc_u32 s70, s70, 0\n\t" \
    "s_cmp_eq_u32 s14, s28\n\t" \
    "s_addc_u32 s70, s70, 0\n\t" \
    "s_cmp_eq_u32 s16, s28\n\t" \
    "s_addc_u32 s70, s70, 0\n\t" \
    "s_cmp_eq_u32 s18, s28\n\t" \
    "s_addc_u32 s70, s70, 0\n\t" \
    "s_cmp_eq_u32 s20, s28\n\t" \
    "s_addc_u32 s70, s70, 0\n\t" \
    "s_cmp_eq_u32 s22, s28\n\t" \
    "s_addc_u32 s70, s70, 0\n\t" \
    "s_cmp_eq_u32 s24, s28\n\t" \
    "s_addc_u32 s70, s70, 0\n\t" \
    "s_cmp_eq_u32 s26, s28\n\t" \
    "s_addc_u32 s70, s70, 0\n\t" \
    "s_mov_b32 s75, s73\n\t" \
    "s_mov_b32 s76, s74\n\t" \
    "s_lshl_b32 s70, s70, 3\n\t" \
    "s_sub_u32 s73, s56, s70\n\t" \
    "s_lshl_b32 s74, s28, 4\n\t" \
    "s_sub_u32 s69, s73, s75\n\t" \
    "s_sub_u32 s70, s74, s76\n\t" \
    "s_sub_u32 s71, s70, 1600\n\t" \
    "s_cmp_lt_u32 s71, 400000\n\t" \
    "s_cbranch_scc0 vv_K9_%=\n\t" \
    "v_cvt_f32_u32 v140, s69\n\t" \
    "v_cvt_f32_u32 v141, s70\n\t" \
    "v_rcp_f32 v142, v140\n\t" \
    "v_rcp_f32 v143, v141\n\t" \
    "v_mul_f32 v142, v142, v141\n\t" \
    "v_mul_f32 v143, v143, v140\n\t" \
    "v_mul_f32 v142, 0x4f800000, v142\n\t" \
    "v_mul_f32 v143, 0x4f000000, v143\n\t" \
    "v_cvt_u32_f32 v142, v142\n\t" \
    "v_cvt_u32_f32 v143, v143\n\t" \
    "v_readfirstlane_b32 s69, v142\n\t" \
    "v_readfirstlane_b32 s70, v143\n\t" \
    "s_nop 1\n\t" \
    "s_mov_b32 s78, s69\n\t" \
    "s_mov_b32 s77, s70\n\t" \
    "vv_K9_%=:\n\t" \
    "s_mov_b32 s79, s74\n\t" \
    "s_sub_u32 s80, s74, 32000\n\t" \
    "s_cmp_eq_u32 s66, 1\n\t" \
    "s_cbranch_scc1 vv_loop11_%=\n\t" \
    "s_cmp_eq_u32 s66, 2\n\t" \
    "s_cbranch_scc1 vv_loop12_%=\n\t" \
    "s_cmp_eq_u32 s66, 3\n\t" \
    "s_cbranch_scc1 vv_loop13_%=\n\t" \
    "vv_loop10_%=:\n\t" \
    "v_mov_b32 v72, 0\n\t" \
    "v_mov_b32 v73, 0\n\t" \
    "v_mov_b32 v74, 0\n\t" \
    "v_mov_b32 v75, 0\n\t" \
    "v_mov_b32 v76, 0\n\t" \
    "v_mov_b32 v77, 0\n\t" \
    "v_mov_b32 v78, 0\n\t" \
    "v_mov_b32 v79, 0\n\t" \
    "v_mov_b32 v92, 0\n\t" \
    "v_mov_b32 v93, 0\n\t" \
    "v_mov_b32 v94, 0\n\t" \
    "v_mov_b32 v95, 0\n\t" \
    "v_mov_b32 v96, 0\n\t" \
    "v_mov_b32 v97, 0\n\t" \
    "v_mov_b32 v98, 0\n\t" \
    "v_mov_b32 v99, 0\n\t" \
    "v_mov_b32 v100, 0\n\t" \
    "v_mov_b32 v101, 0\n\t" \
    "v_mov_b32 v102, 0\n\t" \
    "v_mov_b32 v103, 0\n\t" \
    "v_mov_b32 v104, 0\n\t" \
    "v_mov_b32 v105, 0\n\t" \
    "v_mov_b32 v106, 0\n\t" \
    "v_mov_b32 v107, 0\n\t" \
    "v_mov_b32 v108, 0\n\t" \
    "v_mov_b32 v109, 0\n\t" \
    "v_mov_b32 v110, 0\n\t" \
    "v_mov_b32 v111, 0\n\t" \
    "v_mov_b32 v112, 0\n\t" \
    "v_mov_b32 v113, 0\n\t" \
    "v_mov_b32 v114, 0\n\t" \
    "v_mov_b32 v115, 0\n\t" \
    "v_mov_b32 v116, 0\n\t" \
    "v_mov_b32 v117, 0\n\t" \
    "v_mov_b32 v118, 0\n\t" \
    "v_mov_b32 v119, 0\n\t" \
    "v_mov_b32 v120, 0\n\t" \
    "v_mov_b32 v121, 0\n\t" \
    "v_mov_b32 v122, 0\n\t" \
    "v_mov_b32 v123, 0\n\t" \
    "v_mov_b32 v124, 0\n\t" \
    "v_mov_b32 v125, 0\n\t" \
    "v_mov_b32 v126, 0\n\t" \
    "v_mov_b32 v127, 0\n\t" \
    "vv_go15_%=:\n\t" \
    "s_add_u32 s69, s65, 0\n\t" \
    "s_bitcmp1_b32 s69, 0\n\t" \
    "s_cselect_b32 s70, s63, s62\n\t" \
    "s_bfe_u32 s69, s69, 0x20001\n\t" \
    "s_lshl_b32 s69, s69, 19\n\t" \
    "s_add_u32 s69, s69, s70\n\t" \
    "s_add_u32 s46, s36, s69\n\t" \
    "s_addc_u32 s47, s37, 0\n\t" \
    "s_add_u32 s69, s65, 0\n\t" \
    "s_mul_i32 s70, s69, 171\n\t" \
    "s_lshr_b32 s70, s70, 9\n\t" \
    "s_mul_i32 s70, s70, 3\n\t" \
    "s_sub_u32 s69, s69, s70\n\t" \
    "s_mul_i32 s69, s69, 36\n\t" \
    "s_lshl_b32 s69, s69, 14\n\t" \
    "s_add_u32 s69, s69, s64\n\t" \
    "s_add_u32 s69, s69, 4194304\n\t" \
    "s_add_u32 s38, s36, s69\n\t" \
    "s_addc_u32 s39, s37, 0\n\t" \
    "s_mul_i32 s69, s65, 9216\n\t" \
    "s_add_u32 s69, s69, 123731968\n\t" \
    "s_add_u32 s40, s36, s69\n\t" \
    "s_addc_u32 s41, s37, 0\n\t" \
    "s_sub_u32 s70, s79, s74\n\t" \
    "s_max_i32 s70, s70, 0\n\t" \
    "s_min_i32 s70, s70, 16000\n\t" \
    "s_lshl_b32 s70, s70, 1\n\t" \
    "s_mul_hi_u32 s70, s70, s77\n\t" \
    "s_add_u32 s70, s70, s73\n\t" \
    "s_sub_u32 s70, s70, 100\n\t" \
    "s_getpc_b64 s[30:31]\n\t" \
    "vv_Wp17_%=:\n\t" \
    "s_add_u32 s30, s30, vv_We18_%=-vv_Wp17_%=\n\t" \
    "s_addc_u32 s31, s31, 0\n\t" \
    "vv_W16_%=:\n\t" \
    "s_memtime s[56:57]\n\t" \
    "s_waitcnt lgkmcnt(0)\n\t" \
    "s_sub_u32 s69, s70, s56\n\t" \
    "s_cmp_gt_i32 s69, 111\n\t" \
    "s_cbranch_scc1 vv_W16_%=\n\t" \
    "s_max_i32 s69, s69, 0\n\t" \
    "s_lshr_b32 s69, s69, 1\n\t" \
    "s_and_b32 s69, s69, 0x3c\n\t" \
    "s_sub_u32 s30, s30, s69\n\t" \
    "s_subb_u32 s31, s31, 0\n\t" \
    "s_setpc_b64 s[30:31]\n\t" \
    ".p2align 6\n\t" \
    "s_nop 0\n\t" \
    "s_nop 0\n\t" \
    "s_nop 0\n\t" \
    "s_nop 1\n\t" \
    "s_nop 1\n\t" \
    "s_nop 1\n\t" \
    "s_nop 1\n\t" \
    "s_nop 1\n\t" \
    "s_nop 1\n\t" \
    "s_nop 1\n\t" \
    "s_nop 1\n\t" \
    "s_nop 1\n\t" \
    "s_nop 1\n\t" \
    "s_nop 1\n\t" \
    "s_nop 1\n\t" \
    "s_nop 1\n\t" \
    "vv_We18_%=:\n\t" \
    "vv_G19_%=:\n\t" \
    "global_load_dwordx4 v[36:39], v1, s[46:47] sc1\n\t" \
    "s_nop 9\n\t" \
    "global_load_dwordx4 v[40:43], v1, s[46:47] offset:1024 sc1\n\t" \
    "s_nop 9\n\t" \
    "global_load_dwordx4 v[44:47], v2, s[46:47] sc1\n\t" \
    "s_waitcnt vmcnt(2)\n\t" \
    "v_smfmac_f32_16x16x64_bf16 v[72:75], v[36:39], a[0:7], v10\n\t" \
    "ds_write_b128 v0, v[36:39] offset:0\n\t" \
    "v_smfmac_f32_16x16x64_bf16 v[76:79], v[36:39], a[72:79], v10\n\t" \
    "v_max3_u32 v140, v36, v37, v38\n\t" \
    "s_waitcnt vmcnt(1)\n\t" \
    "ds_write_b128 v0, v[40:43] offset:1024\n\t" \
    "v_smfmac_f32_16x16x64_bf16 v[72:75], v[40:43], a[8:15], v10\n\t" \
    "v_max3_u32 v140, v140, v39, v40\n\t" \
    "v_max3_u32 v140, v140, v41, v42\n\t" \
    "v_max_u32 v140, v140, v43\n\t" \
    "v_cmp_eq_u32 vcc, -1, v140\n\t" \
    "v_smfmac_f32_16x16x64_bf16 v[76:79], v[40:43], a[80:87], v10\n\t" \
    "s_cbranch_vccnz vv_Gbad20_%=\n\t" \
    "vv_Gc22_%=:\n\t" \
    "s_waitcnt lgkmcnt(0)\n\t" \
    "s_barrier\n\t" \
    "ds_read_b128 v[48:51], v0 offset:2048\n\t" \
    "ds_read_b128 v[52:55], v0 offset:3072\n\t" \
    "s_waitcnt vmcnt(0)\n\t" \
    "v_smfmac_f32_16x16x64_bf16 v[72:75], v[44:47], a[64:71], v10\n\t" \
    "ds_read_b128 v[56:59], v0 offset:4096\n\t" \
    "ds_read_b128 v[60:63], v0 offset:5120\n\t" \
    "v_max3_u32 v140, v44, v45, v46\n\t" \
    "v_max_u32 v140, v140, v47\n\t" \
    "v_smfmac_f32_16x16x64_bf16 v[76:79], v[44:47], a[136:143], v10\n\t" \
    "ds_read_b128 v[64:67], v0 offset:6144\n\t" \
    "ds_read_b128 v[68:71], v0 offset:7168\n\t" \
    "v_cmp_eq_u32 vcc, -1, v140\n\t" \
    "s_cbranch_vccnz vv_Gbad221_%=\n\t" \
    "vv_Gc223_%=:\n\t" \
    "s_waitcnt lgkmcnt(5)\n\t" \
    "v_smfmac_f32_16x16x64_bf16 v[72:75], v[48:51], a[16:23], v10\n\t" \
    "s_waitcnt lgkmcnt(4)\n\t" \
    "v_smfmac_f32_16x16x64_bf16 v[72:75], v[52:55], a[24:31], v10\n\t" \
    "s_waitcnt lgkmcnt(3)\n\t" \
    "v_smfmac_f32_16x16x64_bf16 v[72:75], v[56:59], a[32:39], v10\n\t" \
    "s_waitcnt lgkmcnt(2)\n\t" \
    "v_smfmac_f32_16x16x64_bf16 v[72:75], v[60:63], a[40:47], v10\n\t" \
    "s_waitcnt lgkmcnt(1)\n\t" \
    "v_smfmac_f32_16x16x64_bf16 v[72:75], v[64:67], a[48:55], v10\n\t" \
    "s_waitcnt lgkmcnt(0)\n\t" \
    "v_smfmac_f32_16x16x64_bf16 v[72:75], v[68:71], a[56:63], v10\n\t" \
    "v_smfmac_f32_16x16x64_bf16 v[76:79], v[48:51], a[88:95], v10\n\t" \
    "s_nop 1\n\t" \
    "s_nop 0\n\t" \
    "v_smfmac_f32_16x16x64_bf16 v[76:79], v[52:55], a[96:103], v10\n\t" \
    "s_nop 1\n\t" \
    "s_nop 0\n\t" \
    "v_smfmac_f32_16x16x64_bf16 v[76:79], v[56:59], a[104:111], v10\n\t" \
    "v_add_f32 v72, v72, v74\n\t" \
    "v_smfmac_f32_16x16x64_bf16 v[76:79], v[60:63], a[112:119], v10\n\t" \
    "v_add_f32 v73, v73, v75\n\t" \
    "v_smfmac_f32_16x16x64_bf16 v[76:79], v[64:67], a[120:127], v10\n\t" \
    "v_cvt_pk_bf16_f32 v80, v72, v73\n\t" \
    "v_smfmac_f32_16x16x64_bf16 v[76:79], v[68:71], a[128:135], v10\n\t" \
    "ds_write_b32 v12, v80 offset:0\n\t" \
    "s_nop 6\n\t" \
    "v_add_f32 v76, v76, v78\n\t" \
    "v_add_f32 v77, v77, v79\n\t" \
    "v_cvt_pk_bf16_f32 v81, v76, v77\n\t" \
    "ds_write_b32 v12, v81 offset:64\n\t" \
    "s_waitcnt lgkmcnt(0)\n\t" \
    "s_barrier\n\t" \
    "ds_read_b64_tr_b16 v[84:85], v13 offset:0\n\t" \
    "ds_read_b64_tr_b16 v[86:87], v13 offset:128\n\t" \
    "ds_read_b64_tr_b16 v[88:89], v13 offset:1024\n\t" \
    "ds_read_b64_tr_b16 v[90:91], v13 offset:1152\n\t" \
    "s_waitcnt lgkmcnt(2)\n\t" \
    "v_smfmac_f32_16x16x64_bf16 v[92:95], v[84:87], a[144:151], v10\n\t" \
    "s_nop 2\n\t" \
    "s_waitcnt lgkmcnt(0)\n\t" \
    "v_smfmac_f32_16x16x64_bf16 v[92:95], v[88:91], a[152:159], v10\n\t" \
    "s_nop 2\n\t" \
    "v_smfmac_f32_16x16x64_bf16 v[96:99], v[84:87], a[160:167], v10\n\t" \
    "s_nop 2\n\t" \
    "v_smfmac_f32_16x16x64_bf16 v[96:99], v[88:91], a[168:175], v10\n\t" \
    "s_nop 2\n\t" \
    "v_smfmac_f32_16x16x64_bf16 v[100:103], v[84:87], a[176:183], v10\n\t" \
    "s_nop 0\n\t" \
    "v_add_f32 v92, v92, v94\n\t" \
    "v_add_f32 v93, v93, v95\n\t" \
    "v_smfmac_f32_16x16x64_bf16 v[100:103], v[88:91], a[184:191], v10\n\t" \
    "s_nop 1\n\t" \
    "v_cvt_pk_bf16_f32 v128, v92, v93\n\t" \
    "v_smfmac_f32_16x16x64_bf16 v[104:107], v[84:87], a[192:199], v10\n\t" \
    "s_nop 0\n\t" \
    "v_add_f32 v96, v96, v98\n\t" \
    "v_add_f32 v97, v97, v99\n\t" \
    "v_smfmac_f32_16x16x64_bf16 v[104:107], v[88:91], a[200:207], v10\n\t" \
    "s_nop 1\n\t" \
    "v_cvt_pk_bf16_f32 v129, v96, v97\n\t" \
    "v_smfmac_f32_16x16x64_bf16 v[124:127], v[84:87], v[240:247], v10\n\t" \
    "s_nop 0\n\t" \
    "v_add_f32 v100, v100, v102\n\t" \
    "v_add_f32 v101, v101, v103\n\t" \
    "v_smfmac_f32_16x16x64_bf16 v[124:127], v[88:91], v[248:255], v10\n\t" \
    "s_nop 1\n\t" \
    "global_load_dword v140, v20, s[46:47] sc1\n\t" \
    "v_cvt_pk_bf16_f32 v130, v100, v101\n\t" \
    "v_smfmac_f32_16x16x64_bf16 v[108:111], v[84:87], a[208:215], v10\n\t" \
    "s_nop 0\n\t" \
    "v_add_f32 v104, v104, v106\n\t" \
    "v_add_f32 v105, v105, v107\n\t" \
    "v_smfmac_f32_16x16x64_bf16 v[108:111], v[88:91], a[216:223], v10\n\t" \
    "s_nop 0\n\t" \
    "v_cvt_pk_bf16_f32 v131, v104, v105\n\t" \
    "v_permlane32_swap_b32 v128, v130\n\t" \
    "v_smfmac_f32_16x16x64_bf16 v[112:115], v[84:87], a[224:231], v10\n\t" \
    "s_nop 0\n\t" \
    "v_permlane32_swap_b32 v129, v131\n\t" \
    "s_nop 1\n\t" \
    "v_permlane16_swap_b32 v128, v129\n\t" \
    "v_smfmac_f32_16x16x64_bf16 v[112:115], v[88:91], a[232:239], v10\n\t" \
    "v_permlane16_swap_b32 v130, v131\n\t" \
    "global_store_dwordx4 v14, v[128:131], s[38:39] sc1\n\t" \
    "v_add_f32 v124, v124, v126\n\t" \
    "v_smfmac_f32_16x16x64_bf16 v[116:119], v[84:87], a[240:247], v10\n\t" \
    "s_nop 0\n\t" \
    "v_add_f32 v125, v125, v127\n\t" \
    "v_cvt_pk_bf16_f32 v136, v124, v125\n\t" \
    "v_smfmac_f32_16x16x64_bf16 v[116:119], v[88:91], a[248:255], v10\n\t" \
    "global_store_dword v16, v136, s[38:39] sc1\n\t" \
    "v_add_f32 v108, v108, v110\n\t" \
    "v_add_f32 v109, v109, v111\n\t" \
    "v_smfmac_f32_16x16x64_bf16 v[120:123], v[84:87], v[224:231], v10\n\t" \
    "s_nop 0\n\t" \
    "v_cvt_pk_bf16_f32 v132, v108, v109\n\t" \
    "v_add_f32 v112, v112, v114\n\t" \
    "v_smfmac_f32_16x16x64_bf16 v[120:123], v[88:91], v[232:239], v10\n\t" \
    "v_add_f32 v113, v113, v115\n\t" \
    "v_cvt_pk_bf16_f32 v133, v112, v113\n\t" \
    "v_add_f32 v116, v116, v118\n\t" \
    "v_add_f32 v117, v117, v119\n\t" \
    "v_cvt_pk_bf16_f32 v134, v116, v117\n\t" \
    "s_nop 2\n\t" \
    "v_pk_add_f32 v[120:121], v[120:121], v[122:123]\n\t" \
    "v_cvt_pk_bf16_f32 v135, v120, v121\n\t" \
    "v_permlane32_swap_b32 v132, v134\n\t" \
    "s_nop 0\n\t" \
    "v_permlane32_swap_b32 v133, v135\n\t" \
    "s_nop 1\n\t" \
    "v_permlane16_swap_b32 v132, v133\n\t" \
    "v_permlane16_swap_b32 v134, v135\n\t" \
    "global_store_dwordx4 v15, v[132:135], s[38:39] sc1\n\t" \
    "s_nop 1\n\t" \
    "global_store_dwordx4 v17, v[128:131], s[40:41] sc1\n\t" \
    "global_store_dwordx4 v17, v[132:135], s[40:41] offset:1024 sc1\n\t" \
    "global_store_dword v18, v136, s[40:41] sc1\n\t" \
    "s_cmp_eq_u32 s65, 63\n\t" \
    "s_cbranch_scc1 vv_end14_%=\n\t" \
    "s_mov_b32 s71, 3\n\t" \
    "vv_E25_%=:\n\t" \
    "s_memtime s[54:55]\n\t" \
    "s_nop 0\n\t" \
    "s_memrealtime s[4:5]\n\t" \
    "s_nop 0\n\t" \
    "s_memrealtime s[6:7]\n\t" \
    "s_nop 0\n\t" \
    "s_memrealtime s[8:9]\n\t" \
    "s_nop 0\n\t" \
    "s_memrealtime s[10:11]\n\t" \
    "s_nop 0\n\t" \
    "s_memrealtime s[12:13]\n\t" \
    "s_nop 0\n\t" \
    "s_memrealtime s[14:15]\n\t" \
    "s_nop 0\n\t" \
    "s_memrealtime s[16:17]\n\t" \
    "s_nop 0\n\t" \
    "s_memrealtime s[18:19]\n\t" \
    "s_nop 0\n\t" \
    "s_memrealtime s[20:21]\n\t" \
    "s_nop 0\n\t" \
    "s_memrealtime s[22:23]\n\t" \
    "s_nop 0\n\t" \
    "s_memrealtime s[24:25]\n\t" \
    "s_nop 0\n\t" \
    "s_memrealtime s[26:27]\n\t" \
    "s_nop 0\n\t" \
    "s_memrealtime s[28:29]\n\t" \
    "s_nop 0\n\t" \
    "s_memtime s[56:57]\n\t" \
    "s_mov_b32 s70, 1\n\t" \
    "s_sub_u32 s71, s71, 1\n\t" \
    "s_cselect_b32 s69, 0, -1\n\t" \
    "s_waitcnt lgkmcnt(0)\n\t" \
    "s_sub_u32 s54, s56, s54\n\t" \
    "s_and_b32 s54, s54, s69\n\t" \
    "s_cmp_gt_u32 s54, 124\n\t" \
    "s_cbranch_scc1 vv_E25_%=\n\t" \
    "s_cmp_eq_u32 s4, s28\n\t" \
    "s_addc_u32 s70, s70, 0\n\t" \
    "s_cmp_eq_u32 s6, s28\n\t" \
    "s_addc_u32 s70, s70, 0\n\t" \
    "s_cmp_eq_u32 s8, s28\n\t" \
    "s_addc_u32 s70, s70, 0\n\t" \
    "s_cmp_eq_u32 s10, s28\n\t" \
    "s_addc_u32 s70, s70, 0\n\t" \
    "s_cmp_eq_u32 s12, s28\n\t" \
    "s_addc_u32 s70, s70, 0\n\t" \
    "s_cmp_eq_u32 s14, s28\n\t" \
    "s_addc_u32 s70, s70, 0\n\t" \
    "s_cmp_eq_u32 s16, s28\n\t" \
    "s_addc_u32 s70, s70, 0\n\t" \
    "s_cmp_eq_u32 s18, s28\n\t" \
    "s_addc_u32 s70, s70, 0\n\t" \
    "s_cmp_eq_u32 s20, s28\n\t" \
    "s_addc_u32 s70, s70, 0\n\t" \
    "s_cmp_eq_u32 s22, s28\n\t" \
    "s_addc_u32 s70, s70, 0\n\t" \
    "s_cmp_eq_u32 s24, s28\n\t" \
    "s_addc_u32 s70, s70, 0\n\t" \
    "s_cmp_eq_u32 s26, s28\n\t" \
    "s_addc_u32 s70, s70, 0\n\t" \
    "s_mov_b32 s75, s73\n\t" \
    "s_mov_b32 s76, s74\n\t" \
    "s_lshl_b32 s70, s70, 3\n\t" \
    "s_sub_u32 s73, s56, s70\n\t" \
    "s_lshl_b32 s74, s28, 4\n\t" \
    "s_sub_u32 s69, s73, s75\n\t" \
    "s_sub_u32 s70, s74, s76\n\t" \
    "s_sub_u32 s71, s70, 1600\n\t" \
    "s_cmp_lt_u32 s71, 400000\n\t" \
    "s_cbranch_scc0 vv_K27_%=\n\t" \
    "v_cvt_f32_u32 v141, s69\n\t" \
    "v_cvt_f32_u32 v142, s70\n\t" \
    "v_rcp_f32 v143, v141\n\t" \
    "v_rcp_f32 v144, v142\n\t" \
    "v_mul_f32 v143, v143, v142\n\t" \
    "v_mul_f32 v144, v144, v141\n\t" \
    "v_mul_f32 v143, 0x4f800000, v143\n\t" \
    "v_mul_f32 v144, 0x4f000000, v144\n\t" \
    "v_cvt_u32_f32 v143, v143\n\t" \
    "v_cvt_u32_f32 v144, v144\n\t" \
    "v_readfirstlane_b32 s69, v143\n\t" \
    "v_readfirstlane_b32 s70, v144\n\t" \
    "s_nop 1\n\t" \
    "s_sub_u32 s69, s69, s78\n\t" \
    "s_ashr_i32 s69, s69, 2\n\t" \
    "s_add_u32 s78, s78, s69\n\t" \
    "s_sub_u32 s70, s70, s77\n\t" \
    "s_ashr_i32 s70, s70, 2\n\t" \
    "s_add_u32 s77, s77, s70\n\t" \
    "vv_K27_%=:\n\t" \
    "s_waitcnt vmcnt(6)\n\t" \
    "v_cmp_eq_u32 vcc, -1, v140\n\t" \
    "s_cbranch_vccz vv_Hok29_%=\n\t" \
    "vv_H28_%=:\n\t" \
    "global_load_dword v140, v20, s[46:47] sc1\n\t" \
    "s_waitcnt vmcnt(0)\n\t" \
    "v_cmp_eq_u32 vcc, -1, v140\n\t" \
    "s_cbranch_vccz vv_Hc30_%=\n\t" \
    "s_cmp_eq_u32 s68, 0\n\t" \
    "s_cbranch_scc1 vv_F31_%=\n\t" \
    "s_sub_u32 s68, s68, 1\n\t" \
    "s_sleep 5\n\t" \
    "s_branch vv_H28_%=\n\t" \
    "vv_F31_%=:\n\t" \
    "v_mov_b32 v141, 6095104\n\t" \
    "v_mov_b32 v142, 6\n\t" \
    "global_store_dword v141, v142, s[36:37] sc1\n\t" \
    "s_waitcnt vmcnt(0)\n\t" \
    "s_branch vv_Hc30_%=\n\t" \
    "vv_Hc30_%=:\n\t" \
    "vv_Hok29_%=:\n\t" \
    "v_subrev_u32 v140, s80, v140\n\t" \
    "v_and_b32 v20, -5, v20\n\t" \
    "s_nop 0\n\t" \
    "v_max_i32_dpp v140, v140, v140 row_ror:1 row_mask:0xf bank_mask:0xf\n\t" \
    "s_nop 1\n\t" \
    "v_max_i32_dpp v140, v140, v140 row_ror:2 row_mask:0xf bank_mask:0xf\n\t" \
    "s_nop 1\n\t" \
    "v_max_i32_dpp v140, v140, v140 row_ror:4 row_mask:0xf bank_mask:0xf\n\t" \
    "s_nop 1\n\t" \
    "v_max_i32_dpp v140, v140, v140 row_ror:8 row_mask:0xf bank_mask:0xf\n\t" \
    "s_nop 0\n\t" \
    "v_readlane_b32 s69, v140, 0\n\t" \
    "v_readlane_b32 s71, v140, 16\n\t" \
    "v_readlane_b32 s70, v140, 32\n\t" \
    "v_readlane_b32 s81, v140, 48\n\t" \
    "s_max_i32 s69, s69, s71\n\t" \
    "s_nop 0\n\t" \
    "s_max_i32 s71, s70, s81\n\t" \
    "s_max_i32 s69, s69, s71\n\t" \
    "s_add_u32 s81, s69, s80\n\t" \
    "s_cmp_lg_u32 s66, 0\n\t" \
    "s_cbranch_scc1 vv_SF32_%=\n\t" \
    "s_lshl_b32 s69, s67, 7\n\t" \
    "s_add_u32 s69, s69, 6131712\n\t" \
    "s_add_u32 s69, s69, s52\n\t" \
    "s_add_u32 s70, s65, -1\n\t" \
    "v_mov_b32 v140, s69\n\t" \
    "v_mov_b32 v141, s70\n\t" \
    "s_mov_b64 exec, 1\n\t" \
    "global_store_dword v140, v141, s[36:37] sc1\n\t" \
    "s_mov_b64 exec, -1\n\t" \
    "vv_SF32_%=:\n\t" \
    "v_mov_b32 v72, 0\n\t" \
    "v_mov_b32 v73, 0\n\t" \
    "v_mov_b32 v74, 0\n\t" \
    "v_mov_b32 v75, 0\n\t" \
    "v_mov_b32 v76, 0\n\t" \
    "v_mov_b32 v77, 0\n\t" \
    "v_mov_b32 v78, 0\n\t" \
    "v_mov_b32 v79, 0\n\t" \
    "v_mov_b32 v92, 0\n\t" \
    "v_mov_b32 v93, 0\n\t" \
    "v_mov_b32 v94, 0\n\t" \
    "v_mov_b32 v95, 0\n\t" \
    "v_mov_b32 v96, 0\n\t" \
    "v_mov_b32 v97, 0\n\t" \
    "v_mov_b32 v98, 0\n\t" \
    "v_mov_b32 v99, 0\n\t" \
    "v_mov_b32 v100, 0\n\t" \
    "v_mov_b32 v101, 0\n\t" \
    "v_mov_b32 v102, 0\n\t" \
    "v_mov_b32 v103, 0\n\t" \
    "v_mov_b32 v104, 0\n\t" \
    "v_mov_b32 v105, 0\n\t" \
    "v_mov_b32 v106, 0\n\t" \
    "v_mov_b32 v107, 0\n\t" \
    "v_mov_b32 v108, 0\n\t" \
    "v_mov_b32 v109, 0\n\t" \
    "v_mov_b32 v110, 0\n\t" \
    "v_mov_b32 v111, 0\n\t" \
    "v_mov_b32 v112, 0\n\t" \
    "v_mov_b32 v113, 0\n\t" \
    "v_mov_b32 v114, 0\n\t" \
    "v_mov_b32 v115, 0\n\t" \
    "v_mov_b32 v116, 0\n\t" \
    "v_mov_b32 v117, 0\n\t" \
    "v_mov_b32 v118, 0\n\t" \
    "v_mov_b32 v119, 0\n\t" \
    "v_mov_b32 v120, 0\n\t" \
    "v_mov_b32 v121, 0\n\t" \
    "v_mov_b32 v122, 0\n\t" \
    "v_mov_b32 v123, 0\n\t" \
    "v_mov_b32 v124, 0\n\t" \
    "v_mov_b32 v125, 0\n\t" \
    "v_mov_b32 v126, 0\n\t" \
    "v_mov_b32 v127, 0\n\t" \
    "s_cmp_lg_u32 s65, 0\n\t" \
    "s_cbranch_scc1 vv_Oc35_%=\n\t" \
    "s_add_u32 s79, s81, 240\n\t" \
    "s_sub_u32 s70, s79, s74\n\t" \
    "s_max_i32 s70, s70, 0\n\t" \
    "s_min_i32 s70, s70, 16000\n\t" \
    "s_lshl_b32 s70, s70, 1\n\t" \
    "s_mul_hi_u32 s70, s70, s77\n\t" \
    "s_add_u32 s70, s70, s73\n\t" \
    "vv_W36_%=:\n\t" \
    "s_memtime s[56:57]\n\t" \
    "s_waitcnt lgkmcnt(0)\n\t" \
    "s_sub_u32 s69, s70, s56\n\t" \
    "s_cmp_gt_i32 s69, 24\n\t" \
    "s_cbranch_scc1 vv_W36_%=\n\t" \
    "global_load_dword v140, v35, s[46:47] sc1\n\t" \
    "s_waitcnt vmcnt(0)\n\t" \
    "v_cmp_eq_u32 vcc, -1, v140\n\t" \
    "s_cbranch_vccz vv_Ook34_%=\n\t" \
    "vv_O33_%=:\n\t" \
    "global_load_dword v140, v35, s[46:47] offset:1536 sc1\n\t" \
    "s_waitcnt vmcnt(0)\n\t" \
    "v_cmp_eq_u32 vcc, -1, v140\n\t" \
    "s_cbranch_vccz vv_Ook34_%=\n\t" \
    "s_cmp_eq_u32 s68, 0\n\t" \
    "s_cbranch_scc1 vv_F37_%=\n\t" \
    "s_sub_u32 s68, s68, 1\n\t" \
    "s_sleep 3\n\t" \
    "s_branch vv_O33_%=\n\t" \
    "vv_F37_%=:\n\t" \
    "v_mov_b32 v141, 6095104\n\t" \
    "v_mov_b32 v142, 17\n\t" \
    "global_store_dword v141, v142, s[36:37] sc1\n\t" \
    "s_waitcnt vmcnt(0)\n\t" \
    "s_branch vv_Ook34_%=\n\t" \
    "vv_Ook34_%=:\n\t" \
    "v_subrev_u32 v140, s80, v140\n\t" \
    "s_nop 1\n\t" \
    "v_max_i32_dpp v140, v140, v140 row_ror:1 row_mask:0xf bank_mask:0xf\n\t" \
    "s_nop 1\n\t" \
    "v_max_i32_dpp v140, v140, v140 row_ror:2 row_mask:0xf bank_mask:0xf\n\t" \
    "s_nop 1\n\t" \
    "v_max_i32_dpp v140, v140, v140 row_ror:4 row_mask:0xf bank_mask:0xf\n\t" \
    "s_nop 1\n\t" \
    "v_max_i32_dpp v140, v140, v140 row_ror:8 row_mask:0xf bank_mask:0xf\n\t" \
    "s_nop 0\n\t" \
    "v_readlane_b32 s69, v140, 0\n\t" \
    "v_readlane_b32 s71, v140, 16\n\t" \
    "v_readlane_b32 s70, v140, 32\n\t" \
    "v_readlane_b32 s81, v140, 48\n\t" \
    "s_max_i32 s69, s69, s71\n\t" \
    "s_nop 0\n\t" \
    "s_max_i32 s71, s70, s81\n\t" \
    "s_max_i32 s69, s69, s71\n\t" \
    "s_add_u32 s81, s69, s80\n\t" \
    "vv_Oc35_%=:\n\t" \
    "s_mul_hi_u32 s69, s85, s78\n\t" \
    "s_add_u32 s79, s81, s69\n\t" \
    "s_add_u32 s79, s79, s87\n\t" \
    "s_cmp_ge_u32 s65, 36\n\t" \
    "s_cselect_b32 s69, 28, 0\n\t" \
    "s_sub_u32 s79, s79, s69\n\t" \
    "s_cmp_lt_u32 s65, 8\n\t" \
    "s_cselect_b32 s69, 96, 0\n\t" \
    "s_add_u32 s79, s79, s69\n\t" \
    "s_cmp_ge_u32 s65, 57\n\t" \
    "s_cselect_b32 s69, 96, 0\n\t" \
    "s_add_u32 s79, s79, s69\n\t" \
    "s_add_u32 s80, s79, s88\n\t" \
    "s_add_u32 s65, s65, 1\n\t" \
    "s_branch vv_go15_%=\n\t" \
    "vv_Gbad20_%=:\n\t" \
    "s_waitcnt vmcnt(0) lgkmcnt(0)\n\t" \
    "s_nop 7\n\t" \
    "v_mov_b32 v72, 0\n\t" \
    "v_mov_b32 v73, 0\n\t" \
    "v_mov_b32 v74, 0\n\t" \
    "v_mov_b32 v75, 0\n\t" \
    "v_mov_b32 v76, 0\n\t" \
    "v_mov_b32 v77, 0\n\t" \
    "v_mov_b32 v78, 0\n\t" \
    "v_mov_b32 v79, 0\n\t" \
    "s_cmp_eq_u32 s68, 0\n\t" \
    "s_cbranch_scc1 vv_F38_%=\n\t" \
    "s_sub_u32 s68, s68, 1\n\t" \
    "s_branch vv_G19_%=\n\t" \
    "vv_F38_%=:\n\t" \
    "v_mov_b32 v140, 6095104\n\t" \
    "v_mov_b32 v141, 1\n\t" \
    "global_store_dword v140, v141, s[36:37] sc1\n\t" \
    "s_waitcnt vmcnt(0)\n\t" \
    "s_branch vv_Gc22_%=\n\t" \
    "vv_Gbad221_%=:\n\t" \
    "global_load_dwordx4 v[44:47], v2, s[46:47] sc1\n\t" \
    "s_waitcnt vmcnt(0)\n\t" \
    "v_max3_u32 v140, v44, v45, v46\n\t" \
    "v_max_u32 v140, v140, v47\n\t" \
    "v_cmp_eq_u32 vcc, -1, v140\n\t" \
    "s_cbranch_vccz vv_Gre224_%=\n\t" \
    "s_cmp_eq_u32 s68, 0\n\t" \
    "s_cbranch_scc1 vv_F39_%=\n\t" \
    "s_sub_u32 s68, s68, 1\n\t" \
    "s_sleep 1\n\t" \
    "s_branch vv_Gbad221_%=\n\t" \
    "vv_F39_%=:\n\t" \
    "v_mov_b32 v140, 6095104\n\t" \
    "v_mov_b32 v141, 2\n\t" \
    "global_store_dword v140, v141, s[36:37] sc1\n\t" \
    "s_waitcnt vmcnt(0)\n\t" \
    "s_branch vv_Gre224_%=\n\t" \
    "vv_Gre224_%=:\n\t" \
    "v_mov_b32 v72, 0\n\t" \
    "v_mov_b32 v73, 0\n\t" \
    "v_mov_b32 v74, 0\n\t" \
    "v_mov_b32 v75, 0\n\t" \
    "v_mov_b32 v76, 0\n\t" \
    "v_mov_b32 v77, 0\n\t" \
    "v_mov_b32 v78, 0\n\t" \
    "v_mov_b32 v79, 0\n\t" \
    "v_smfmac_f32_16x16x64_bf16 v[72:75], v[36:39], a[0:7], v10\n\t" \
    "s_nop 0\n\t" \
    "v_smfmac_f32_16x16x64_bf16 v[76:79], v[36:39], a[72:79], v10\n\t" \
    "v_smfmac_f32_16x16x64_bf16 v[72:75], v[40:43], a[8:15], v10\n\t" \
    "v_smfmac_f32_16x16x64_bf16 v[76:79], v[40:43], a[80:87], v10\n\t" \
    "v_smfmac_f32_16x16x64_bf16 v[72:75], v[44:47], a[64:71], v10\n\t" \
    "v_smfmac_f32_16x16x64_bf16 v[76:79], v[44:47], a[136:143], v10\n\t" \
    "s_branch vv_Gc223_%=\n\t" \
    "vv_end14_%=:\n\t" \
    "s_endpgm\n\t" \
    "vv_loop11_%=:\n\t" \
    "v_mov_b32 v72, 0\n\t" \
    "v_mov_b32 v73, 0\n\t" \
    "v_mov_b32 v74, 0\n\t" \
    "v_mov_b32 v75, 0\n\t" \
    "v_mov_b32 v76, 0\n\t" \
    "v_mov_b32 v77, 0\n\t" \
    "v_mov_b32 v78, 0\n\t" \
    "v_mov_b32 v79, 0\n\t" \
    "v_mov_b32 v92, 0\n\t" \
    "v_mov_b32 v93, 0\n\t" \
    "v_mov_b32 v94, 0\n\t" \
    "v_mov_b32 v95, 0\n\t" \
    "v_mov_b32 v96, 0\n\t" \
    "v_mov_b32 v97, 0\n\t" \
    "v_mov_b32 v98, 0\n\t" \
    "v_mov_b32 v99, 0\n\t" \
    "v_mov_b32 v100, 0\n\t" \
    "v_mov_b32 v101, 0\n\t" \
    "v_mov_b32 v102, 0\n\t" \
    "v_mov_b32 v103, 0\n\t" \
    "v_mov_b32 v104, 0\n\t" \
    "v_mov_b32 v105, 0\n\t" \
    "v_mov_b32 v106, 0\n\t" \
    "v_mov_b32 v107, 0\n\t" \
    "v_mov_b32 v108, 0\n\t" \
    "v_mov_b32 v109, 0\n\t" \
    "v_mov_b32 v110, 0\n\t" \
    "v_mov_b32 v111, 0\n\t" \
    "v_mov_b32 v112, 0\n\t" \
    "v_mov_b32 v113, 0\n\t" \
    "v_mov_b32 v114, 0\n\t" \
    "v_mov_b32 v115, 0\n\t" \
    "v_mov_b32 v116, 0\n\t" \
    "v_mov_b32 v117, 0\n\t" \
    "v_mov_b32 v118, 0\n\t" \
    "v_mov_b32 v119, 0\n\t" \
    "v_mov_b32 v120, 0\n\t" \
    "v_mov_b32 v121, 0\n\t" \
    "v_mov_b32 v122, 0\n\t" \
    "v_mov_b32 v123, 0\n\t" \
    "v_mov_b32 v124, 0\n\t" \
    "v_mov_b32 v125, 0\n\t" \
    "v_mov_b32 v126, 0\n\t" \
    "v_mov_b32 v127, 0\n\t" \
    "vv_go41_%=:\n\t" \
    "s_add_u32 s69, s65, 0\n\t" \
    "s_bitcmp1_b32 s69, 0\n\t" \
    "s_cselect_b32 s70, s63, s62\n\t" \
    "s_bfe_u32 s69, s69, 0x20001\n\t" \
    "s_lshl_b32 s69, s69, 19\n\t" \
    "s_add_u32 s69, s69, s70\n\t" \
    "s_add_u32 s46, s36, s69\n\t" \
    "s_addc_u32 s47, s37, 0\n\t" \
    "s_add_u32 s69, s65, 0\n\t" \
    "s_mul_i32 s70, s69, 171\n\t" \
    "s_lshr_b32 s70, s70, 9\n\t" \
    "s_mul_i32 s70, s70, 3\n\t" \
    "s_sub_u32 s69, s69, s70\n\t" \
    "s_mul_i32 s69, s69, 36\n\t" \
    "s_lshl_b32 s69, s69, 14\n\t" \
    "s_add_u32 s69, s69, s64\n\t" \
    "s_add_u32 s69, s69, 4194304\n\t" \
    "s_add_u32 s38, s36, s69\n\t" \
    "s_addc_u32 s39, s37, 0\n\t" \
    "s_mul_i32 s69, s65, 9216\n\t" \
    "s_add_u32 s69, s69, 123731968\n\t" \
    "s_add_u32 s40, s36, s69\n\t" \
    "s_addc_u32 s41, s37, 0\n\t" \
    "s_sub_u32 s70, s79, s74\n\t" \
    "s_max_i32 s70, s70, 0\n\t" \
    "s_min_i32 s70, s70, 16000\n\t" \
    "s_lshl_b32 s70, s70, 1\n\t" \
    "s_mul_hi_u32 s70, s70, s77\n\t" \
    "s_add_u32 s70, s70, s73\n\t" \
    "s_sub_u32 s70, s70, 100\n\t" \
    "s_getpc_b64 s[30:31]\n\t" \
    "vv_Wp43_%=:\n\t" \
    "s_add_u32 s30, s30, vv_We44_%=-vv_Wp43_%=\n\t" \
    "s_addc_u32 s31, s31, 0\n\t" \
    "vv_W42_%=:\n\t" \
    "s_memtime s[56:57]\n\t" \
    "s_waitcnt lgkmcnt(0)\n\t" \
    "s_sub_u32 s69, s70, s56\n\t" \
    "s_cmp_gt_i32 s69, 111\n\t" \
    "s_cbranch_scc1 vv_W42_%=\n\t" \
    "s_max_i32 s69, s69, 0\n\t" \
    "s_lshr_b32 s69, s69, 1\n\t" \
    "s_and_b32 s69, s69, 0x3c\n\t" \
    "s_sub_u32 s30, s30, s69\n\t" \
    "s_subb_u32 s31, s31, 0\n\t" \
    "s_setpc_b64 s[30:31]\n\t" \
    ".p2align 6\n\t" \
    "s_nop 0\n\t" \
    "s_nop 0\n\t" \
    "s_nop 0\n\t" \
    "s_nop 1\n\t" \
    "s_nop 1\n\t" \
    "s_nop 1\n\t" \
    "s_nop 1\n\t" \
    "s_nop 1\n\t" \
    "s_nop 1\n\t" \
    "s_nop 1\n\t" \
    "s_nop 1\n\t" \
    "s_nop 1\n\t" \
    "s_nop 1\n\t" \
    "s_nop 1\n\t" \
    "s_nop 1\n\t" \
    "s_nop 1\n\t" \
    "vv_We44_%=:\n\t" \
    "vv_G45_%=:\n\t" \
    "global_load_dwordx4 v[36:39], v1, s[46:47] sc1\n\t" \
    "s_nop 9\n\t" \
    "global_load_dwordx4 v[40:43], v1, s[46:47] offset:1024 sc1\n\t" \
    "s_nop 9\n\t" \
    "global_load_dwordx4 v[44:47], v2, s[46:47] sc1\n\t" \
    "s_waitcnt vmcnt(2)\n\t" \
    "v_smfmac_f32_16x16x64_bf16 v[72:75], v[36:39], a[16:23], v10\n\t" \
    "ds_write_b128 v0, v[36:39] offset:2048\n\t" \
    "v_smfmac_f32_16x16x64_bf16 v[76:79], v[36:39], a[88:95], v10\n\t" \
    "v_max3_u32 v140, v36, v37, v38\n\t" \
    "s_waitcnt vmcnt(1)\n\t" \
    "ds_write_b128 v0, v[40:43] offset:3072\n\t" \
    "v_smfmac_f32_16x16x64_bf16 v[72:75], v[40:43], a[24:31], v10\n\t" \
    "v_max3_u32 v140, v140, v39, v40\n\t" \
    "v_max3_u32 v140, v140, v41, v42\n\t" \
    "v_max_u32 v140, v140, v43\n\t" \
    "v_cmp_eq_u32 vcc, -1, v140\n\t" \
    "v_smfmac_f32_16x16x64_bf16 v[76:79], v[40:43], a[96:103], v10\n\t" \
    "s_cbranch_vccnz vv_Gbad46_%=\n\t" \
    "vv_Gc48_%=:\n\t" \
    "s_waitcnt lgkmcnt(0)\n\t" \
    "s_barrier\n\t" \
    "ds_read_b128 v[48:51], v0 offset:4096\n\t" \
    "ds_read_b128 v[52:55], v0 offset:5120\n\t" \
    "s_waitcnt vmcnt(0)\n\t" \
    "v_smfmac_f32_16x16x64_bf16 v[72:75], v[44:47], a[64:71], v10\n\t" \
    "ds_read_b128 v[56:59], v0 offset:6144\n\t" \
    "ds_read_b128 v[60:63], v0 offset:7168\n\t" \
    "v_max3_u32 v140, v44, v45, v46\n\t" \
    "v_max_u32 v140, v140, v47\n\t" \
    "v_smfmac_f32_16x16x64_bf16 v[76:79], v[44:47], a[136:143], v10\n\t" \
    "ds_read_b128 v[64:67], v0 offset:0\n\t" \
    "ds_read_b128 v[68:71], v0 offset:1024\n\t" \
    "v_cmp_eq_u32 vcc, -1, v140\n\t" \
    "s_cbranch_vccnz vv_Gbad247_%=\n\t" \
    "vv_Gc249_%=:\n\t" \
    "s_waitcnt lgkmcnt(5)\n\t" \
    "v_smfmac_f32_16x16x64_bf16 v[72:75], v[48:51], a[32:39], v10\n\t" \
    "s_waitcnt lgkmcnt(4)\n\t" \
    "v_smfmac_f32_16x16x64_bf16 v[72:75], v[52:55], a[40:47], v10\n\t" \
    "s_waitcnt lgkmcnt(3)\n\t" \
    "v_smfmac_f32_16x16x64_bf16 v[72:75], v[56:59], a[48:55], v10\n\t" \
    "s_waitcnt lgkmcnt(2)\n\t" \
    "v_smfmac_f32_16x16x64_bf16 v[72:75], v[60:63], a[56:63], v10\n\t" \
    "s_waitcnt lgkmcnt(1)\n\t" \
    "v_smfmac_f32_16x16x64_bf16 v[72:75], v[64:67], a[0:7], v10\n\t" \
    "s_waitcnt lgkmcnt(0)\n\t" \
    "v_smfmac_f32_16x16x64_bf16 v[72:75], v[68:71], a[8:15], v10\n\t" \
    "v_smfmac_f32_16x16x64_bf16 v[76:79], v[48:51], a[104:111], v10\n\t" \
    "s_nop 1\n\t" \
    "s_nop 0\n\t" \
    "v_smfmac_f32_16x16x64_bf16 v[76:79], v[52:55], a[112:119], v10\n\t" \
    "s_nop 1\n\t" \
    "s_nop 0\n\t" \
    "v_smfmac_f32_16x16x64_bf16 v[76:79], v[56:59], a[120:127], v10\n\t" \
    "v_add_f32 v72, v72, v74\n\t" \
    "v_smfmac_f32_16x16x64_bf16 v[76:79], v[60:63], a[128:135], v10\n\t" \
    "v_add_f32 v73, v73, v75\n\t" \
    "v_smfmac_f32_16x16x64_bf16 v[76:79], v[64:67], a[72:79], v10\n\t" \
    "v_cvt_pk_bf16_f32 v80, v72, v73\n\t" \
    "v_smfmac_f32_16x16x64_bf16 v[76:79], v[68:71], a[80:87], v10\n\t" \
    "ds_write_b32 v12, v80 offset:0\n\t" \
    "s_nop 6\n\t" \
    "v_add_f32 v76, v76, v78\n\t" \
    "v_add_f32 v77, v77, v79\n\t" \
    "v_cvt_pk_bf16_f32 v81, v76, v77\n\t" \
    "ds_write_b32 v12, v81 offset:64\n\t" \
    "s_waitcnt lgkmcnt(0)\n\t" \
    "s_barrier\n\t" \
    "ds_read_b64_tr_b16 v[84:85], v13 offset:0\n\t" \
    "ds_read_b64_tr_b16 v[86:87], v13 offset:128\n\t" \
    "ds_read_b64_tr_b16 v[88:89], v13 offset:1024\n\t" \
    "ds_read_b64_tr_b16 v[90:91], v13 offset:1152\n\t" \
    "s_waitcnt lgkmcnt(2)\n\t" \
    "v_smfmac_f32_16x16x64_bf16 v[92:95], v[84:87], a[144:151], v10\n\t" \
    "s_nop 2\n\t" \
    "s_waitcnt lgkmcnt(0)\n\t" \
    "v_smfmac_f32_16x16x64_bf16 v[92:95], v[88:91], a[152:159], v10\n\t" \
    "s_nop 2\n\t" \
    "v_smfmac_f32_16x16x64_bf16 v[96:99], v[84:87], a[160:167], v10\n\t" \
    "s_nop 2\n\t" \
    "v_smfmac_f32_16x16x64_bf16 v[96:99], v[88:91], a[168:175], v10\n\t" \
    "s_nop 2\n\t" \
    "v_smfmac_f32_16x16x64_bf16 v[100:103], v[84:87], a[176:183], v10\n\t" \
    "s_nop 0\n\t" \
    "v_add_f32 v92, v92, v94\n\t" \
    "v_add_f32 v93, v93, v95\n\t" \
    "v_smfmac_f32_16x16x64_bf16 v[100:103], v[88:91], a[184:191], v10\n\t" \
    "s_nop 1\n\t" \
    "v_cvt_pk_bf16_f32 v128, v92, v93\n\t" \
    "v_smfmac_f32_16x16x64_bf16 v[104:107], v[84:87], a[192:199], v10\n\t" \
    "s_nop 0\n\t" \
    "v_add_f32 v96, v96, v98\n\t" \
    "v_add_f32 v97, v97, v99\n\t" \
    "v_smfmac_f32_16x16x64_bf16 v[104:107], v[88:91], a[200:207], v10\n\t" \
    "s_nop 1\n\t" \
    "v_cvt_pk_bf16_f32 v129, v96, v97\n\t" \
    "v_smfmac_f32_16x16x64_bf16 v[124:127], v[84:87], v[240:247], v10\n\t" \
    "s_nop 0\n\t" \
    "v_add_f32 v100, v100, v102\n\t" \
    "v_add_f32 v101, v101, v103\n\t" \
    "v_smfmac_f32_16x16x64_bf16 v[124:127], v[88:91], v[248:255], v10\n\t" \
    "s_nop 1\n\t" \
    "global_load_dword v140, v20, s[46:47] sc1\n\t" \
    "v_cvt_pk_bf16_f32 v130, v100, v101\n\t" \
    "v_smfmac_f32_16x16x64_bf16 v[108:111], v[84:87], a[208:215], v10\n\t" \
    "s_nop 0\n\t" \
    "v_add_f32 v104, v104, v106\n\t" \
    "v_add_f32 v105, v105, v107\n\t" \
    "v_smfmac_f32_16x16x64_bf16 v[108:111], v[88:91], a[216:223], v10\n\t" \
    "s_nop 0\n\t" \
    "v_cvt_pk_bf16_f32 v131, v104, v105\n\t" \
    "v_permlane32_swap_b32 v128, v130\n\t" \
    "v_smfmac_f32_16x16x64_bf16 v[112:115], v[84:87], a[224:231], v10\n\t" \
    "s_nop 0\n\t" \
    "v_permlane32_swap_b32 v129, v131\n\t" \
    "s_nop 1\n\t" \
    "v_permlane16_swap_b32 v128, v129\n\t" \
    "v_smfmac_f32_16x16x64_bf16 v[112:115], v[88:91], a[232:239], v10\n\t" \
    "v_permlane16_swap_b32 v130, v131\n\t" \
    "global_store_dwordx4 v14, v[128:131], s[38:39] sc1\n\t" \
    "v_add_f32 v124, v124, v126\n\t" \
    "v_smfmac_f32_16x16x64_bf16 v[116:119], v[84:87], a[240:247], v10\n\t" \
    "s_nop 0\n\t" \
    "v_add_f32 v125, v125, v127\n\t" \
    "v_cvt_pk_bf16_f32 v136, v124, v125\n\t" \
    "v_smfmac_f32_16x16x64_bf16 v[116:119], v[88:91], a[248:255], v10\n\t" \
    "global_store_dword v16, v136, s[38:39] sc1\n\t" \
    "v_add_f32 v108, v108, v110\n\t" \
    "v_add_f32 v109, v109, v111\n\t" \
    "v_smfmac_f32_16x16x64_bf16 v[120:123], v[84:87], v[224:231], v10\n\t" \
    "s_nop 0\n\t" \
    "v_cvt_pk_bf16_f32 v132, v108, v109\n\t" \
    "v_add_f32 v112, v112, v114\n\t" \
    "v_smfmac_f32_16x16x64_bf16 v[120:123], v[88:91], v[232:239], v10\n\t" \
    "v_add_f32 v113, v113, v115\n\t" \
    "v_cvt_pk_bf16_f32 v133, v112, v113\n\t" \
    "v_add_f32 v116, v116, v118\n\t" \
    "v_add_f32 v117, v117, v119\n\t" \
    "v_cvt_pk_bf16_f32 v134, v116, v117\n\t" \
    "s_nop 2\n\t" \
    "v_pk_add_f32 v[120:121], v[120:121], v[122:123]\n\t" \
    "v_cvt_pk_bf16_f32 v135, v120, v121\n\t" \
    "v_permlane32_swap_b32 v132, v134\n\t" \
    "s_nop 0\n\t" \
    "v_permlane32_swap_b32 v133, v135\n\t" \
    "s_nop 1\n\t" \
    "v_permlane16_swap_b32 v132, v133\n\t" \
    "v_permlane16_swap_b32 v134, v135\n\t" \
    "global_store_dwordx4 v15, v[132:135], s[38:39] sc1\n\t" \
    "s_nop 1\n\t" \
    "global_store_dwordx4 v17, v[128:131], s[40:41] sc1\n\t" \
    "global_store_dwordx4 v17, v[132:135], s[40:41] offset:1024 sc1\n\t" \
    "global_store_dword v18, v136, s[40:41] sc1\n\t" \
    "s_cmp_eq_u32 s65, 63\n\t" \
    "s_cbranch_scc1 vv_end40_%=\n\t" \
    "s_mov_b32 s71, 3\n\t" \
    "vv_E51_%=:\n\t" \
    "s_memtime s[54:55]\n\t" \
    "s_nop 0\n\t" \
    "s_memrealtime s[4:5]\n\t" \
    "s_nop 0\n\t" \
    "s_memrealtime s[6:7]\n\t" \
    "s_nop 0\n\t" \
    "s_memrealtime s[8:9]\n\t" \
    "s_nop 0\n\t" \
    "s_memrealtime s[10:11]\n\t" \
    "s_nop 0\n\t" \
    "s_memrealtime s[12:13]\n\t" \
    "s_nop 0\n\t" \
    "s_memrealtime s[14:15]\n\t" \
    "s_nop 0\n\t" \
    "s_memrealtime s[16:17]\n\t" \
    "s_nop 0\n\t" \
    "s_memrealtime s[18:19]\n\t" \
    "s_nop 0\n\t" \
    "s_memrealtime s[20:21]\n\t" \
    "s_nop 0\n\t" \
    "s_memrealtime s[22:23]\n\t" \
    "s_nop 0\n\t" \
    "s_memrealtime s[24:25]\n\t" \
    "s_nop 0\n\t" \
    "s_memrealtime s[26:27]\n\t" \
    "s_nop 0\n\t" \
    "s_memrealtime s[28:29]\n\t" \
    "s_nop 0\n\t" \
    "s_memtime s[56:57]\n\t" \
    "s_mov_b32 s70, 1\n\t" \
    "s_sub_u32 s71, s71, 1\n\t" \
    "s_cselect_b32 s69, 0, -1\n\t" \
    "s_waitcnt lgkmcnt(0)\n\t" \
    "s_sub_u32 s54, s56, s54\n\t" \
    "s_and_b32 s54, s54, s69\n\t" \
    "s_cmp_gt_u32 s54, 124\n\t" \
    "s_cbranch_scc1 vv_E51_%=\n\t" \
    "s_cmp_eq_u32 s4, s28\n\t" \
    "s_addc_u32 s70, s70, 0\n\t" \
    "s_cmp_eq_u32 s6, s28\n\t" \
    "s_addc_u32 s70, s70, 0\n\t" \
    "s_cmp_eq_u32 s8, s28\n\t" \
    "s_addc_u32 s70, s70, 0\n\t" \
    "s_cmp_eq_u32 s10, s28\n\t" \
    "s_addc_u32 s70, s70, 0\n\t" \
    "s_cmp_eq_u32 s12, s28\n\t" \
    "s_addc_u32 s70, s70, 0\n\t" \
    "s_cmp_eq_u32 s14, s28\n\t" \
    "s_addc_u32 s70, s70, 0\n\t" \
    "s_cmp_eq_u32 s16, s28\n\t" \
    "s_addc_u32 s70, s70, 0\n\t" \
    "s_cmp_eq_u32 s18, s28\n\t" \
    "s_addc_u32 s70, s70, 0\n\t" \
    "s_cmp_eq_u32 s20, s28\n\t" \
    "s_addc_u32 s70, s70, 0\n\t" \
    "s_cmp_eq_u32 s22, s28\n\t" \
    "s_addc_u32 s70, s70, 0\n\t" \
    "s_cmp_eq_u32 s24, s28\n\t" \
    "s_addc_u32 s70, s70, 0\n\t" \
    "s_cmp_eq_u32 s26, s28\n\t" \
    "s_addc_u32 s70, s70, 0\n\t" \
    "s_mov_b32 s75, s73\n\t" \
    "s_mov_b32 s76, s74\n\t" \
    "s_lshl_b32 s70, s70, 3\n\t" \
    "s_sub_u32 s73, s56, s70\n\t" \
    "s_lshl_b32 s74, s28, 4\n\t" \
    "s_sub_u32 s69, s73, s75\n\t" \
    "s_sub_u32 s70, s74, s76\n\t" \
    "s_sub_u32 s71, s70, 1600\n\t" \
    "s_cmp_lt_u32 s71, 400000\n\t" \
    "s_cbranch_scc0 vv_K53_%=\n\t" \
    "v_cvt_f32_u32 v141, s69\n\t" \
    "v_cvt_f32_u32 v142, s70\n\t" \
    "v_rcp_f32 v143, v141\n\t" \
    "v_rcp_f32 v144, v142\n\t" \
    "v_mul_f32 v143, v143, v142\n\t" \
    "v_mul_f32 v144, v144, v141\n\t" \
    "v_mul_f32 v143, 0x4f800000, v143\n\t" \
    "v_mul_f32 v144, 0x4f000000, v144\n\t" \
    "v_cvt_u32_f32 v143, v143\n\t" \
    "v_cvt_u32_f32 v144, v144\n\t" \
    "v_readfirstlane_b32 s69, v143\n\t" \
    "v_readfirstlane_b32 s70, v144\n\t" \
    "s_nop 1\n\t" \
    "s_sub_u32 s69, s69, s78\n\t" \
    "s_ashr_i32 s69, s69, 2\n\t" \
    "s_add_u32 s78, s78, s69\n\t" \
    "s_sub_u32 s70, s70, s77\n\t" \
    "s_ashr_i32 s70, s70, 2\n\t" \
    "s_add_u32 s77, s77, s70\n\t" \
    "vv_K53_%=:\n\t" \
    "s_waitcnt vmcnt(6)\n\t" \
    "v_cmp_eq_u32 vcc, -1, v140\n\t" \
    "s_cbranch_vccz vv_Hok55_%=\n\t" \
    "vv_H54_%=:\n\t" \
    "global_load_dword v140, v20, s[46:47] sc1\n\t" \
    "s_waitcnt vmcnt(0)\n\t" \
    "v_cmp_eq_u32 vcc, -1, v140\n\t" \
    "s_cbranch_vccz vv_Hc56_%=\n\t" \
    "s_cmp_eq_u32 s68, 0\n\t" \
    "s_cbranch_scc1 vv_F57_%=\n\t" \
    "s_sub_u32 s68, s68, 1\n\t" \
    "s_sleep 5\n\t" \
    "s_branch vv_H54_%=\n\t" \
    "vv_F57_%=:\n\t" \
    "v_mov_b32 v141, 6095104\n\t" \
    "v_mov_b32 v142, 6\n\t" \
    "global_store_dword v141, v142, s[36:37] sc1\n\t" \
    "s_waitcnt vmcnt(0)\n\t" \
    "s_branch vv_Hc56_%=\n\t" \
    "vv_Hc56_%=:\n\t" \
    "vv_Hok55_%=:\n\t" \
    "v_subrev_u32 v140, s80, v140\n\t" \
    "v_and_b32 v20, -5, v20\n\t" \
    "s_nop 0\n\t" \
    "v_max_i32_dpp v140, v140, v140 row_ror:1 row_mask:0xf bank_mask:0xf\n\t" \
    "s_nop 1\n\t" \
    "v_max_i32_dpp v140, v140, v140 row_ror:2 row_mask:0xf bank_mask:0xf\n\t" \
    "s_nop 1\n\t" \
    "v_max_i32_dpp v140, v140, v140 row_ror:4 row_mask:0xf bank_mask:0xf\n\t" \
    "s_nop 1\n\t" \
    "v_max_i32_dpp v140, v140, v140 row_ror:8 row_mask:0xf bank_mask:0xf\n\t" \
    "s_nop 0\n\t" \
    "v_readlane_b32 s69, v140, 0\n\t" \
    "v_readlane_b32 s71, v140, 16\n\t" \
    "v_readlane_b32 s70, v140, 32\n\t" \
    "v_readlane_b32 s81, v140, 48\n\t" \
    "s_max_i32 s69, s69, s71\n\t" \
    "s_nop 0\n\t" \
    "s_max_i32 s71, s70, s81\n\t" \
    "s_max_i32 s69, s69, s71\n\t" \
    "s_add_u32 s81, s69, s80\n\t" \
    "v_mov_b32 v72, 0\n\t" \
    "v_mov_b32 v73, 0\n\t" \
    "v_mov_b32 v74, 0\n\t" \
    "v_mov_b32 v75, 0\n\t" \
    "v_mov_b32 v76, 0\n\t" \
    "v_mov_b32 v77, 0\n\t" \
    "v_mov_b32 v78, 0\n\t" \
    "v_mov_b32 v79, 0\n\t" \
    "v_mov_b32 v92, 0\n\t" \
    "v_mov_b32 v93, 0\n\t" \
    "v_mov_b32 v94, 0\n\t" \
    "v_mov_b32 v95, 0\n\t" \
    "v_mov_b32 v96, 0\n\t" \
    "v_mov_b32 v97, 0\n\t" \
    "v_mov_b32 v98, 0\n\t" \
    "v_mov_b32 v99, 0\n\t" \
    "v_mov_b32 v100, 0\n\t" \
    "v_mov_b32 v101, 0\n\t" \
    "v_mov_b32 v102, 0\n\t" \
    "v_mov_b32 v103, 0\n\t" \
    "v_mov_b32 v104, 0\n\t" \
    "v_mov_b32 v105, 0\n\t" \
    "v_mov_b32 v106, 0\n\t" \
    "v_mov_b32 v107, 0\n\t" \
    "v_mov_b32 v108, 0\n\t" \
    "v_mov_b32 v109, 0\n\t" \
    "v_mov_b32 v110, 0\n\t" \
    "v_mov_b32 v111, 0\n\t" \
    "v_mov_b32 v112, 0\n\t" \
    "v_mov_b32 v113, 0\n\t" \
    "v_mov_b32 v114, 0\n\t" \
    "v_mov_b32 v115, 0\n\t" \
    "v_mov_b32 v116, 0\n\t" \
    "v_mov_b32 v117, 0\n\t" \
    "v_mov_b32 v118, 0\n\t" \
    "v_mov_b32 v119, 0\n\t" \
    "v_mov_b32 v120, 0\n\t" \
    "v_mov_b32 v121, 0\n\t" \
    "v_mov_b32 v122, 0\n\t" \
    "v_mov_b32 v123, 0\n\t" \
    "v_mov_b32 v124, 0\n\t" \
    "v_mov_b32 v125, 0\n\t" \
    "v_mov_b32 v126, 0\n\t" \
    "v_mov_b32 v127, 0\n\t" \
    "s_cmp_lg_u32 s65, 0\n\t" \
    "s_cbranch_scc1 vv_Oc60_%=\n\t" \
    "s_add_u32 s79, s81, 240\n\t" \
    "s_sub_u32 s70, s79, s74\n\t" \
    "s_max_i32 s70, s70, 0\n\t" \
    "s_min_i32 s70, s70, 16000\n\t" \
    "s_lshl_b32 s70, s70, 1\n\t" \
    "s_mul_hi_u32 s70, s70, s77\n\t" \
    "s_add_u32 s70, s70, s73\n\t" \
    "vv_W61_%=:\n\t" \
    "s_memtime s[56:57]\n\t" \
    "s_waitcnt lgkmcnt(0)\n\t" \
    "s_sub_u32 s69, s70, s56\n\t" \
    "s_cmp_gt_i32 s69, 24\n\t" \
    "s_cbranch_scc1 vv_W61_%=\n\t" \
    "global_load_dword v140, v35, s[46:47] sc1\n\t" \
    "s_waitcnt vmcnt(0)\n\t" \
    "v_cmp_eq_u32 vcc, -1, v140\n\t" \
    "s_cbranch_vccz vv_Ook59_%=\n\t" \
    "vv_O58_%=:\n\t" \
    "global_load_dword v140, v35, s[46:47] offset:1536 sc1\n\t" \
    "s_waitcnt vmcnt(0)\n\t" \
    "v_cmp_eq_u32 vcc, -1, v140\n\t" \
    "s_cbranch_vccz vv_Ook59_%=\n\t" \
    "s_cmp_eq_u32 s68, 0\n\t" \
    "s_cbranch_scc1 vv_F62_%=\n\t" \
    "s_sub_u32 s68, s68, 1\n\t" \
    "s_sleep 3\n\t" \
    "s_branch vv_O58_%=\n\t" \
    "vv_F62_%=:\n\t" \
    "v_mov_b32 v141, 6095104\n\t" \
    "v_mov_b32 v142, 17\n\t" \
    "global_store_dword v141, v142, s[36:37] sc1\n\t" \
    "s_waitcnt vmcnt(0)\n\t" \
    "s_branch vv_Ook59_%=\n\t" \
    "vv_Ook59_%=:\n\t" \
    "v_subrev_u32 v140, s80, v140\n\t" \
    "s_nop 1\n\t" \
    "v_max_i32_dpp v140, v140, v140 row_ror:1 row_mask:0xf bank_mask:0xf\n\t" \
    "s_nop 1\n\t" \
    "v_max_i32_dpp v140, v140, v140 row_ror:2 row_mask:0xf bank_mask:0xf\n\t" \
    "s_nop 1\n\t" \
    "v_max_i32_dpp v140, v140, v140 row_ror:4 row_mask:0xf bank_mask:0xf\n\t" \
    "s_nop 1\n\t" \
    "v_max_i32_dpp v140, v140, v140 row_ror:8 row_mask:0xf bank_mask:0xf\n\t" \
    "s_nop 0\n\t" \
    "v_readlane_b32 s69, v140, 0\n\t" \
    "v_readlane_b32 s71, v140, 16\n\t" \
    "v_readlane_b32 s70, v140, 32\n\t" \
    "v_readlane_b32 s81, v140, 48\n\t" \
    "s_max_i32 s69, s69, s71\n\t" \
    "s_nop 0\n\t" \
    "s_max_i32 s71, s70, s81\n\t" \
    "s_max_i32 s69, s69, s71\n\t" \
    "s_add_u32 s81, s69, s80\n\t" \
    "vv_Oc60_%=:\n\t" \
    "s_mul_hi_u32 s69, s85, s78\n\t" \
    "s_add_u32 s79, s81, s69\n\t" \
    "s_add_u32 s79, s79, s87\n\t" \
    "s_cmp_ge_u32 s65, 36\n\t" \
    "s_cselect_b32 s69, 28, 0\n\t" \
    "s_sub_u32 s79, s79, s69\n\t" \
    "s_cmp_lt_u32 s65, 8\n\t" \
    "s_cselect_b32 s69, 96, 0\n\t" \
    "s_add_u32 s79, s79, s69\n\t" \
    "s_cmp_ge_u32 s65, 57\n\t" \
    "s_cselect_b32 s69, 96, 0\n\t" \
    "s_add_u32 s79, s79, s69\n\t" \
    "s_add_u32 s80, s79, s88\n\t" \
    "s_add_u32 s65, s65, 1\n\t" \
    "s_branch vv_go41_%=\n\t" \
    "vv_Gbad46_%=:\n\t" \
    "s_waitcnt vmcnt(0) lgkmcnt(0)\n\t" \
    "s_nop 7\n\t" \
    "v_mov_b32 v72, 0\n\t" \
    "v_mov_b32 v73, 0\n\t" \
    "v_mov_b32 v74, 0\n\t" \
    "v_mov_b32 v75, 0\n\t" \
    "v_mov_b32 v76, 0\n\t" \
    "v_mov_b32 v77, 0\n\t" \
    "v_mov_b32 v78, 0\n\t" \
    "v_mov_b32 v79, 0\n\t" \
    "s_cmp_eq_u32 s68, 0\n\t" \
    "s_cbranch_scc1 vv_F63_%=\n\t" \
    "s_sub_u32 s68, s68, 1\n\t" \
    "s_branch vv_G45_%=\n\t" \
    "vv_F63_%=:\n\t" \
    "v_mov_b32 v140, 6095104\n\t" \
    "v_mov_b32 v141, 1\n\t" \
    "global_store_dword v140, v141, s[36:37] sc1\n\t" \
    "s_waitcnt vmcnt(0)\n\t" \
    "s_branch vv_Gc48_%=\n\t" \
    "vv_Gbad247_%=:\n\t" \
    "global_load_dwordx4 v[44:47], v2, s[46:47] sc1\n\t" \
    "s_waitcnt vmcnt(0)\n\t" \
    "v_max3_u32 v140, v44, v45, v46\n\t" \
    "v_max_u32 v140, v140, v47\n\t" \
    "v_cmp_eq_u32 vcc, -1, v140\n\t" \
    "s_cbranch_vccz vv_Gre250_%=\n\t" \
    "s_cmp_eq_u32 s68, 0\n\t" \
    "s_cbranch_scc1 vv_F64_%=\n\t" \
    "s_sub_u32 s68, s68, 1\n\t" \
    "s_sleep 1\n\t" \
    "s_branch vv_Gbad247_%=\n\t" \
    "vv_F64_%=:\n\t" \
    "v_mov_b32 v140, 6095104\n\t" \
    "v_mov_b32 v141, 2\n\t" \
    "global_store_dword v140, v141, s[36:37] sc1\n\t" \
    "s_waitcnt vmcnt(0)\n\t" \
    "s_branch vv_Gre250_%=\n\t" \
    "vv_Gre250_%=:\n\t" \
    "v_mov_b32 v72, 0\n\t" \
    "v_mov_b32 v73, 0\n\t" \
    "v_mov_b32 v74, 0\n\t" \
    "v_mov_b32 v75, 0\n\t" \
    "v_mov_b32 v76, 0\n\t" \
    "v_mov_b32 v77, 0\n\t" \
    "v_mov_b32 v78, 0\n\t" \
    "v_mov_b32 v79, 0\n\t" \
    "v_smfmac_f32_16x16x64_bf16 v[72:75], v[36:39], a[16:23], v10\n\t" \
    "s_nop 0\n\t" \
    "v_smfmac_f32_16x16x64_bf16 v[76:79], v[36:39], a[88:95], v10\n\t" \
    "v_smfmac_f32_16x16x64_bf16 v[72:75], v[40:43], a[24:31], v10\n\t" \
    "v_smfmac_f32_16x16x64_bf16 v[76:79], v[40:43], a[96:103], v10\n\t" \
    "v_smfmac_f32_16x16x64_bf16 v[72:75], v[44:47], a[64:71], v10\n\t" \
    "v_smfmac_f32_16x16x64_bf16 v[76:79], v[44:47], a[136:143], v10\n\t" \
    "s_branch vv_Gc249_%=\n\t" \
    "vv_end40_%=:\n\t" \
    "s_endpgm\n\t" \
    "vv_loop12_%=:\n\t" \
    "v_mov_b32 v72, 0\n\t" \
    "v_mov_b32 v73, 0\n\t" \
    "v_mov_b32 v74, 0\n\t" \
    "v_mov_b32 v75, 0\n\t" \
    "v_mov_b32 v76, 0\n\t" \
    "v_mov_b32 v77, 0\n\t" \
    "v_mov_b32 v78, 0\n\t" \
    "v_mov_b32 v79, 0\n\t" \
    "v_mov_b32 v92, 0\n\t" \
    "v_mov_b32 v93, 0\n\t" \
    "v_mov_b32 v94, 0\n\t" \
    "v_mov_b32 v95, 0\n\t" \
    "v_mov_b32 v96, 0\n\t" \
    "v_mov_b32 v97, 0\n\t" \
    "v_mov_b32 v98, 0\n\t" \
    "v_mov_b32 v99, 0\n\t" \
    "v_mov_b32 v100, 0\n\t" \
    "v_mov_b32 v101, 0\n\t" \
    "v_mov_b32 v102, 0\n\t" \
    "v_mov_b32 v103, 0\n\t" \
    "v_mov_b32 v104, 0\n\t" \
    "v_mov_b32 v105, 0\n\t" \
    "v_mov_b32 v106, 0\n\t" \
    "v_mov_b32 v107, 0\n\t" \
    "v_mov_b32 v108, 0\n\t" \
    "v_mov_b32 v109, 0\n\t" \
    "v_mov_b32 v110, 0\n\t" \
    "v_mov_b32 v111, 0\n\t" \
    "v_mov_b32 v112, 0\n\t" \
    "v_mov_b32 v113, 0\n\t" \
    "v_mov_b32 v114, 0\n\t" \
    "v_mov_b32 v115, 0\n\t" \
    "v_mov_b32 v116, 0\n\t" \
    "v_mov_b32 v117, 0\n\t" \
    "v_mov_b32 v118, 0\n\t" \
    "v_mov_b32 v119, 0\n\t" \
    "v_mov_b32 v120, 0\n\t" \
    "v_mov_b32 v121, 0\n\t" \
    "v_mov_b32 v122, 0\n\t" \
    "v_mov_b32 v123, 0\n\t" \
    "v_mov_b32 v124, 0\n\t" \
    "v_mov_b32 v125, 0\n\t" \
    "v_mov_b32 v126, 0\n\t" \
    "v_mov_b32 v127, 0\n\t" \
    "vv_go66_%=:\n\t" \
    "s_add_u32 s69, s65, 0\n\t" \
    "s_bitcmp1_b32 s69, 0\n\t" \
    "s_cselect_b32 s70, s63, s62\n\t" \
    "s_bfe_u32 s69, s69, 0x20001\n\t" \
    "s_lshl_b32 s69, s69, 19\n\t" \
    "s_add_u32 s69, s69, s70\n\t" \
    "s_add_u32 s46, s36, s69\n\t" \
    "s_addc_u32 s47, s37, 0\n\t" \
    "s_add_u32 s69, s65, 0\n\t" \
    "s_mul_i32 s70, s69, 171\n\t" \
    "s_lshr_b32 s70, s70, 9\n\t" \
    "s_mul_i32 s70, s70, 3\n\t" \
    "s_sub_u32 s69, s69, s70\n\t" \
    "s_mul_i32 s69, s69, 36\n\t" \
    "s_lshl_b32 s69, s69, 14\n\t" \
    "s_add_u32 s69, s69, s64\n\t" \
    "s_add_u32 s69, s69, 4194304\n\t" \
    "s_add_u32 s38, s36, s69\n\t" \
    "s_addc_u32 s39, s37, 0\n\t" \
    "s_mul_i32 s69, s65, 9216\n\t" \
    "s_add_u32 s69, s69, 123731968\n\t" \
    "s_add_u32 s40, s36, s69\n\t" \
    "s_addc_u32 s41, s37, 0\n\t" \
    "s_sub_u32 s70, s79, s74\n\t" \
    "s_max_i32 s70, s70, 0\n\t" \
    "s_min_i32 s70, s70, 16000\n\t" \
    "s_lshl_b32 s70, s70, 1\n\t" \
    "s_mul_hi_u32 s70, s70, s77\n\t" \
    "s_add_u32 s70, s70, s73\n\t" \
    "s_sub_u32 s70, s70, 100\n\t" \
    "s_getpc_b64 s[30:31]\n\t" \
    "vv_Wp68_%=:\n\t" \
    "s_add_u32 s30, s30, vv_We69_%=-vv_Wp68_%=\n\t" \
    "s_addc_u32 s31, s31, 0\n\t" \
    "vv_W67_%=:\n\t" \
    "s_memtime s[56:57]\n\t" \
    "s_waitcnt lgkmcnt(0)\n\t" \
    "s_sub_u32 s69, s70, s56\n\t" \
    "s_cmp_gt_i32 s69, 111\n\t" \
    "s_cbranch_scc1 vv_W67_%=\n\t" \
    "s_max_i32 s69, s69, 0\n\t" \
    "s_lshr_b32 s69, s69, 1\n\t" \
    "s_and_b32 s69, s69, 0x3c\n\t" \
    "s_sub_u32 s30, s30, s69\n\t" \
    "s_subb_u32 s31, s31, 0\n\t" \
    "s_setpc_b64 s[30:31]\n\t" \
    ".p2align 6\n\t" \
    "s_nop 0\n\t" \
    "s_nop 0\n\t" \
    "s_nop 0\n\t" \
    "s_nop 1\n\t" \
    "s_nop 1\n\t" \
    "s_nop 1\n\t" \
    "s_nop 1\n\t" \
    "s_nop 1\n\t" \
    "s_nop 1\n\t" \
    "s_nop 1\n\t" \
    "s_nop 1\n\t" \
    "s_nop 1\n\t" \
    "s_nop 1\n\t" \
    "s_nop 1\n\t" \
    "s_nop 1\n\t" \
    "s_nop 1\n\t" \
    "vv_We69_%=:\n\t" \
    "vv_G70_%=:\n\t" \
    "global_load_dwordx4 v[36:39], v1, s[46:47] sc1\n\t" \
    "s_nop 9\n\t" \
    "global_load_dwordx4 v[40:43], v1, s[46:47] offset:1024 sc1\n\t" \
    "s_nop 9\n\t" \
    "global_load_dwordx4 v[44:47], v2, s[46:47] sc1\n\t" \
    "s_waitcnt vmcnt(2)\n\t" \
    "v_smfmac_f32_16x16x64_bf16 v[72:75], v[36:39], a[32:39], v10\n\t" \
    "ds_write_b128 v0, v[36:39] offset:4096\n\t" \
    "v_smfmac_f32_16x16x64_bf16 v[76:79], v[36:39], a[104:111], v10\n\t" \
    "v_max3_u32 v140, v36, v37, v38\n\t" \
    "s_waitcnt vmcnt(1)\n\t" \
    "ds_write_b128 v0, v[40:43] offset:5120\n\t" \
    "v_smfmac_f32_16x16x64_bf16 v[72:75], v[40:43], a[40:47], v10\n\t" \
    "v_max3_u32 v140, v140, v39, v40\n\t" \
    "v_max3_u32 v140, v140, v41, v42\n\t" \
    "v_max_u32 v140, v140, v43\n\t" \
    "v_cmp_eq_u32 vcc, -1, v140\n\t" \
    "v_smfmac_f32_16x16x64_bf16 v[76:79], v[40:43], a[112:119], v10\n\t" \
    "s_cbranch_vccnz vv_Gbad71_%=\n\t" \
    "vv_Gc73_%=:\n\t" \
    "s_waitcnt lgkmcnt(0)\n\t" \
    "s_barrier\n\t" \
    "ds_read_b128 v[48:51], v0 offset:6144\n\t" \
    "ds_read_b128 v[52:55], v0 offset:7168\n\t" \
    "s_waitcnt vmcnt(0)\n\t" \
    "v_smfmac_f32_16x16x64_bf16 v[72:75], v[44:47], a[64:71], v10\n\t" \
    "ds_read_b128 v[56:59], v0 offset:0\n\t" \
    "ds_read_b128 v[60:63], v0 offset:1024\n\t" \
    "v_max3_u32 v140, v44, v45, v46\n\t" \
    "v_max_u32 v140, v140, v47\n\t" \
    "v_smfmac_f32_16x16x64_bf16 v[76:79], v[44:47], a[136:143], v10\n\t" \
    "ds_read_b128 v[64:67], v0 offset:2048\n\t" \
    "ds_read_b128 v[68:71], v0 offset:3072\n\t" \
    "v_cmp_eq_u32 vcc, -1, v140\n\t" \
    "s_cbranch_vccnz vv_Gbad272_%=\n\t" \
    "vv_Gc274_%=:\n\t" \
    "s_waitcnt lgkmcnt(5)\n\t" \
    "v_smfmac_f32_16x16x64_bf16 v[72:75], v[48:51], a[48:55], v10\n\t" \
    "s_waitcnt lgkmcnt(4)\n\t" \
    "v_smfmac_f32_16x16x64_bf16 v[72:75], v[52:55], a[56:63], v10\n\t" \
    "s_waitcnt lgkmcnt(3)\n\t" \
    "v_smfmac_f32_16x16x64_bf16 v[72:75], v[56:59], a[0:7], v10\n\t" \
    "s_waitcnt lgkmcnt(2)\n\t" \
    "v_smfmac_f32_16x16x64_bf16 v[72:75], v[60:63], a[8:15], v10\n\t" \
    "s_waitcnt lgkmcnt(1)\n\t" \
    "v_smfmac_f32_16x16x64_bf16 v[72:75], v[64:67], a[16:23], v10\n\t" \
    "s_waitcnt lgkmcnt(0)\n\t" \
    "v_smfmac_f32_16x16x64_bf16 v[72:75], v[68:71], a[24:31], v10\n\t" \
    "v_smfmac_f32_16x16x64_bf16 v[76:79], v[48:51], a[120:127], v10\n\t" \
    "s_nop 1\n\t" \
    "s_nop 0\n\t" \
    "v_smfmac_f32_16x16x64_bf16 v[76:79], v[52:55], a[128:135], v10\n\t" \
    "s_nop 1\n\t" \
    "s_nop 0\n\t" \
    "v_smfmac_f32_16x16x64_bf16 v[76:79], v[56:59], a[72:79], v10\n\t" \
    "v_add_f32 v72, v72, v74\n\t" \
    "v_smfmac_f32_16x16x64_bf16 v[76:79], v[60:63], a[80:87], v10\n\t" \
    "v_add_f32 v73, v73, v75\n\t" \
    "v_smfmac_f32_16x16x64_bf16 v[76:79], v[64:67], a[88:95], v10\n\t" \
    "v_cvt_pk_bf16_f32 v80, v72, v73\n\t" \
    "v_smfmac_f32_16x16x64_bf16 v[76:79], v[68:71], a[96:103], v10\n\t" \
    "ds_write_b32 v12, v80 offset:0\n\t" \
    "s_nop 6\n\t" \
    "v_add_f32 v76, v76, v78\n\t" \
    "v_add_f32 v77, v77, v79\n\t" \
    "v_cvt_pk_bf16_f32 v81, v76, v77\n\t" \
    "ds_write_b32 v12, v81 offset:64\n\t" \
    "s_waitcnt lgkmcnt(0)\n\t" \
    "s_barrier\n\t" \
    "ds_read_b64_tr_b16 v[84:85], v13 offset:0\n\t" \
    "ds_read_b64_tr_b16 v[86:87], v13 offset:128\n\t" \
    "ds_read_b64_tr_b16 v[88:89], v13 offset:1024\n\t" \
    "ds_read_b64_tr_b16 v[90:91], v13 offset:1152\n\t" \
    "s_waitcnt lgkmcnt(2)\n\t" \
    "v_smfmac_f32_16x16x64_bf16 v[92:95], v[84:87], a[144:151], v10\n\t" \
    "s_nop 2\n\t" \
    "s_waitcnt lgkmcnt(0)\n\t" \
    "v_smfmac_f32_16x16x64_bf16 v[92:95], v[88:91], a[152:159], v10\n\t" \
    "s_nop 2\n\t" \
    "v_smfmac_f32_16x16x64_bf16 v[96:99], v[84:87], a[160:167], v10\n\t" \
    "s_nop 2\n\t" \
    "v_smfmac_f32_16x16x64_bf16 v[96:99], v[88:91], a[168:175], v10\n\t" \
    "s_nop 2\n\t" \
    "v_smfmac_f32_16x16x64_bf16 v[100:103], v[84:87], a[176:183], v10\n\t" \
    "s_nop 0\n\t" \
    "v_add_f32 v92, v92, v94\n\t" \
    "v_add_f32 v93, v93, v95\n\t" \
    "v_smfmac_f32_16x16x64_bf16 v[100:103], v[88:91], a[184:191], v10\n\t" \
    "s_nop 1\n\t" \
    "v_cvt_pk_bf16_f32 v128, v92, v93\n\t" \
    "v_smfmac_f32_16x16x64_bf16 v[104:107], v[84:87], a[192:199], v10\n\t" \
    "s_nop 0\n\t" \
    "v_add_f32 v96, v96, v98\n\t" \
    "v_add_f32 v97, v97, v99\n\t" \
    "v_smfmac_f32_16x16x64_bf16 v[104:107], v[88:91], a[200:207], v10\n\t" \
    "s_nop 1\n\t" \
    "v_cvt_pk_bf16_f32 v129, v96, v97\n\t" \
    "v_smfmac_f32_16x16x64_bf16 v[124:127], v[84:87], v[240:247], v10\n\t" \
    "s_nop 0\n\t" \
    "v_add_f32 v100, v100, v102\n\t" \
    "v_add_f32 v101, v101, v103\n\t" \
    "v_smfmac_f32_16x16x64_bf16 v[124:127], v[88:91], v[248:255], v10\n\t" \
    "s_nop 1\n\t" \
    "global_load_dword v140, v20, s[46:47] sc1\n\t" \
    "v_cvt_pk_bf16_f32 v130, v100, v101\n\t" \
    "v_smfmac_f32_16x16x64_bf16 v[108:111], v[84:87], a[208:215], v10\n\t" \
    "s_nop 0\n\t" \
    "v_add_f32 v104, v104, v106\n\t" \
    "v_add_f32 v105, v105, v107\n\t" \
    "v_smfmac_f32_16x16x64_bf16 v[108:111], v[88:91], a[216:223], v10\n\t" \
    "s_nop 0\n\t" \
    "v_cvt_pk_bf16_f32 v131, v104, v105\n\t" \
    "v_permlane32_swap_b32 v128, v130\n\t" \
    "v_smfmac_f32_16x16x64_bf16 v[112:115], v[84:87], a[224:231], v10\n\t" \
    "s_nop 0\n\t" \
    "v_permlane32_swap_b32 v129, v131\n\t" \
    "s_nop 1\n\t" \
    "v_permlane16_swap_b32 v128, v129\n\t" \
    "v_smfmac_f32_16x16x64_bf16 v[112:115], v[88:91], a[232:239], v10\n\t" \
    "v_permlane16_swap_b32 v130, v131\n\t" \
    "global_store_dwordx4 v14, v[128:131], s[38:39] sc1\n\t" \
    "v_add_f32 v124, v124, v126\n\t" \
    "v_smfmac_f32_16x16x64_bf16 v[116:119], v[84:87], a[240:247], v10\n\t" \
    "s_nop 0\n\t" \
    "v_add_f32 v125, v125, v127\n\t" \
    "v_cvt_pk_bf16_f32 v136, v124, v125\n\t" \
    "v_smfmac_f32_16x16x64_bf16 v[116:119], v[88:91], a[248:255], v10\n\t" \
    "global_store_dword v16, v136, s[38:39] sc1\n\t" \
    "v_add_f32 v108, v108, v110\n\t" \
    "v_add_f32 v109, v109, v111\n\t" \
    "v_smfmac_f32_16x16x64_bf16 v[120:123], v[84:87], v[224:231], v10\n\t" \
    "s_nop 0\n\t" \
    "v_cvt_pk_bf16_f32 v132, v108, v109\n\t" \
    "v_add_f32 v112, v112, v114\n\t" \
    "v_smfmac_f32_16x16x64_bf16 v[120:123], v[88:91], v[232:239], v10\n\t" \
    "v_add_f32 v113, v113, v115\n\t" \
    "v_cvt_pk_bf16_f32 v133, v112, v113\n\t" \
    "v_add_f32 v116, v116, v118\n\t" \
    "v_add_f32 v117, v117, v119\n\t" \
    "v_cvt_pk_bf16_f32 v134, v116, v117\n\t" \
    "s_nop 2\n\t" \
    "v_pk_add_f32 v[120:121], v[120:121], v[122:123]\n\t" \
    "v_cvt_pk_bf16_f32 v135, v120, v121\n\t" \
    "v_permlane32_swap_b32 v132, v134\n\t" \
    "s_nop 0\n\t" \
    "v_permlane32_swap_b32 v133, v135\n\t" \
    "s_nop 1\n\t" \
    "v_permlane16_swap_b32 v132, v133\n\t" \
    "v_permlane16_swap_b32 v134, v135\n\t" \
    "global_store_dwordx4 v15, v[132:135], s[38:39] sc1\n\t" \
    "s_nop 1\n\t" \
    "global_store_dwordx4 v17, v[128:131], s[40:41] sc1\n\t" \
    "global_store_dwordx4 v17, v[132:135], s[40:41] offset:1024 sc1\n\t" \
    "global_store_dword v18, v136, s[40:41] sc1\n\t" \
    "s_cmp_eq_u32 s65, 63\n\t" \
    "s_cbranch_scc1 vv_end65_%=\n\t" \
    "s_mov_b32 s71, 3\n\t" \
    "vv_E76_%=:\n\t" \
    "s_memtime s[54:55]\n\t" \
    "s_nop 0\n\t" \
    "s_memrealtime s[4:5]\n\t" \
    "s_nop 0\n\t" \
    "s_memrealtime s[6:7]\n\t" \
    "s_nop 0\n\t" \
    "s_memrealtime s[8:9]\n\t" \
    "s_nop 0\n\t" \
    "s_memrealtime s[10:11]\n\t" \
    "s_nop 0\n\t" \
    "s_memrealtime s[12:13]\n\t" \
    "s_nop 0\n\t" \
    "s_memrealtime s[14:15]\n\t" \
    "s_nop 0\n\t" \
    "s_memrealtime s[16:17]\n\t" \
    "s_nop 0\n\t" \
    "s_memrealtime s[18:19]\n\t" \
    "s_nop 0\n\t" \
    "s_memrealtime s[20:21]\n\t" \
    "s_nop 0\n\t" \
    "s_memrealtime s[22:23]\n\t" \
    "s_nop 0\n\t" \
    "s_memrealtime s[24:25]\n\t" \
    "s_nop 0\n\t" \
    "s_memrealtime s[26:27]\n\t" \
    "s_nop 0\n\t" \
    "s_memrealtime s[28:29]\n\t" \
    "s_nop 0\n\t" \
    "s_memtime s[56:57]\n\t" \
    "s_mov_b32 s70, 1\n\t" \
    "s_sub_u32 s71, s71, 1\n\t" \
    "s_cselect_b32 s69, 0, -1\n\t" \
    "s_waitcnt lgkmcnt(0)\n\t" \
    "s_sub_u32 s54, s56, s54\n\t" \
    "s_and_b32 s54, s54, s69\n\t" \
    "s_cmp_gt_u32 s54, 124\n\t" \
    "s_cbranch_scc1 vv_E76_%=\n\t" \
    "s_cmp_eq_u32 s4, s28\n\t" \
    "s_addc_u32 s70, s70, 0\n\t" \
    "s_cmp_eq_u32 s6, s28\n\t" \
    "s_addc_u32 s70, s70, 0\n\t" \
    "s_cmp_eq_u32 s8, s28\n\t" \
    "s_addc_u32 s70, s70, 0\n\t" \
    "s_cmp_eq_u32 s10, s28\n\t" \
    "s_addc_u32 s70, s70, 0\n\t" \
    "s_cmp_eq_u32 s12, s28\n\t" \
    "s_addc_u32 s70, s70, 0\n\t" \
    "s_cmp_eq_u32 s14, s28\n\t" \
    "s_addc_u32 s70, s70, 0\n\t" \
    "s_cmp_eq_u32 s16, s28\n\t" \
    "s_addc_u32 s70, s70, 0\n\t" \
    "s_cmp_eq_u32 s18, s28\n\t" \
    "s_addc_u32 s70, s70, 0\n\t" \
    "s_cmp_eq_u32 s20, s28\n\t" \
    "s_addc_u32 s70, s70, 0\n\t" \
    "s_cmp_eq_u32 s22, s28\n\t" \
    "s_addc_u32 s70, s70, 0\n\t" \
    "s_cmp_eq_u32 s24, s28\n\t" \
    "s_addc_u32 s70, s70, 0\n\t" \
    "s_cmp_eq_u32 s26, s28\n\t" \
    "s_addc_u32 s70, s70, 0\n\t" \
    "s_mov_b32 s75, s73\n\t" \
    "s_mov_b32 s76, s74\n\t" \
    "s_lshl_b32 s70, s70, 3\n\t" \
    "s_sub_u32 s73, s56, s70\n\t" \
    "s_lshl_b32 s74, s28, 4\n\t" \
    "s_sub_u32 s69, s73, s75\n\t" \
    "s_sub_u32 s70, s74, s76\n\t" \
    "s_sub_u32 s71, s70, 1600\n\t" \
    "s_cmp_lt_u32 s71, 400000\n\t" \
    "s_cbranch_scc0 vv_K78_%=\n\t" \
    "v_cvt_f32_u32 v141, s69\n\t" \
    "v_cvt_f32_u32 v142, s70\n\t" \
    "v_rcp_f32 v143, v141\n\t" \
    "v_rcp_f32 v144, v142\n\t" \
    "v_mul_f32 v143, v143, v142\n\t" \
    "v_mul_f32 v144, v144, v141\n\t" \
    "v_mul_f32 v143, 0x4f800000, v143\n\t" \
    "v_mul_f32 v144, 0x4f000000, v144\n\t" \
    "v_cvt_u32_f32 v143, v143\n\t" \
    "v_cvt_u32_f32 v144, v144\n\t" \
    "v_readfirstlane_b32 s69, v143\n\t" \
    "v_readfirstlane_b32 s70, v144\n\t" \
    "s_nop 1\n\t" \
    "s_sub_u32 s69, s69, s78\n\t" \
    "s_ashr_i32 s69, s69, 2\n\t" \
    "s_add_u32 s78, s78, s69\n\t" \
    "s_sub_u32 s70, s70, s77\n\t" \
    "s_ashr_i32 s70, s70, 2\n\t" \
    "s_add_u32 s77, s77, s70\n\t" \
    "vv_K78_%=:\n\t" \
    "s_waitcnt vmcnt(6)\n\t" \
    "v_cmp_eq_u32 vcc, -1, v140\n\t" \
    "s_cbranch_vccz vv_Hok80_%=\n\t" \
    "vv_H79_%=:\n\t" \
    "global_load_dword v140, v20, s[46:47] sc1\n\t" \
    "s_waitcnt vmcnt(0)\n\t" \
    "v_cmp_eq_u32 vcc, -1, v140\n\t" \
    "s_cbranch_vccz vv_Hc81_%=\n\t" \
    "s_cmp_eq_u32 s68, 0\n\t" \
    "s_cbranch_scc1 vv_F82_%=\n\t" \
    "s_sub_u32 s68, s68, 1\n\t" \
    "s_sleep 5\n\t" \
    "s_branch vv_H79_%=\n\t" \
    "vv_F82_%=:\n\t" \
    "v_mov_b32 v141, 6095104\n\t" \
    "v_mov_b32 v142, 6\n\t" \
    "global_store_dword v141, v142, s[36:37] sc1\n\t" \
    "s_waitcnt vmcnt(0)\n\t" \
    "s_branch vv_Hc81_%=\n\t" \
    "vv_Hc81_%=:\n\t" \
    "vv_Hok80_%=:\n\t" \
    "v_subrev_u32 v140, s80, v140\n\t" \
    "v_and_b32 v20, -5, v20\n\t" \
    "s_nop 0\n\t" \
    "v_max_i32_dpp v140, v140, v140 row_ror:1 row_mask:0xf bank_mask:0xf\n\t" \
    "s_nop 1\n\t" \
    "v_max_i32_dpp v140, v140, v140 row_ror:2 row_mask:0xf bank_mask:0xf\n\t" \
    "s_nop 1\n\t" \
    "v_max_i32_dpp v140, v140, v140 row_ror:4 row_mask:0xf bank_mask:0xf\n\t" \
    "s_nop 1\n\t" \
    "v_max_i32_dpp v140, v140, v140 row_ror:8 row_mask:0xf bank_mask:0xf\n\t" \
    "s_nop 0\n\t" \
    "v_readlane_b32 s69, v140, 0\n\t" \
    "v_readlane_b32 s71, v140, 16\n\t" \
    "v_readlane_b32 s70, v140, 32\n\t" \
    "v_readlane_b32 s81, v140, 48\n\t" \
    "s_max_i32 s69, s69, s71\n\t" \
    "s_nop 0\n\t" \
    "s_max_i32 s71, s70, s81\n\t" \
    "s_max_i32 s69, s69, s71\n\t" \
    "s_add_u32 s81, s69, s80\n\t" \
    "v_mov_b32 v72, 0\n\t" \
    "v_mov_b32 v73, 0\n\t" \
    "v_mov_b32 v74, 0\n\t" \
    "v_mov_b32 v75, 0\n\t" \
    "v_mov_b32 v76, 0\n\t" \
    "v_mov_b32 v77, 0\n\t" \
    "v_mov_b32 v78, 0\n\t" \
    "v_mov_b32 v79, 0\n\t" \
    "v_mov_b32 v92, 0\n\t" \
    "v_mov_b32 v93, 0\n\t" \
    "v_mov_b32 v94, 0\n\t" \
    "v_mov_b32 v95, 0\n\t" \
    "v_mov_b32 v96, 0\n\t" \
    "v_mov_b32 v97, 0\n\t" \
    "v_mov_b32 v98, 0\n\t" \
    "v_mov_b32 v99, 0\n\t" \
    "v_mov_b32 v100, 0\n\t" \
    "v_mov_b32 v101, 0\n\t" \
    "v_mov_b32 v102, 0\n\t" \
    "v_mov_b32 v103, 0\n\t" \
    "v_mov_b32 v104, 0\n\t" \
    "v_mov_b32 v105, 0\n\t" \
    "v_mov_b32 v106, 0\n\t" \
    "v_mov_b32 v107, 0\n\t" \
    "v_mov_b32 v108, 0\n\t" \
    "v_mov_b32 v109, 0\n\t" \
    "v_mov_b32 v110, 0\n\t" \
    "v_mov_b32 v111, 0\n\t" \
    "v_mov_b32 v112, 0\n\t" \
    "v_mov_b32 v113, 0\n\t" \
    "v_mov_b32 v114, 0\n\t" \
    "v_mov_b32 v115, 0\n\t" \
    "v_mov_b32 v116, 0\n\t" \
    "v_mov_b32 v117, 0\n\t" \
    "v_mov_b32 v118, 0\n\t" \
    "v_mov_b32 v119, 0\n\t" \
    "v_mov_b32 v120, 0\n\t" \
    "v_mov_b32 v121, 0\n\t" \
    "v_mov_b32 v122, 0\n\t" \
    "v_mov_b32 v123, 0\n\t" \
    "v_mov_b32 v124, 0\n\t" \
    "v_mov_b32 v125, 0\n\t" \
    "v_mov_b32 v126, 0\n\t" \
    "v_mov_b32 v127, 0\n\t" \
    "s_cmp_lg_u32 s65, 0\n\t" \
    "s_cbranch_scc1 vv_Oc85_%=\n\t" \
    "s_add_u32 s79, s81, 240\n\t" \
    "s_sub_u32 s70, s79, s74\n\t" \
    "s_max_i32 s70, s70, 0\n\t" \
    "s_min_i32 s70, s70, 16000\n\t" \
    "s_lshl_b32 s70, s70, 1\n\t" \
    "s_mul_hi_u32 s70, s70, s77\n\t" \
    "s_add_u32 s70, s70, s73\n\t" \
    "vv_W86_%=:\n\t" \
    "s_memtime s[56:57]\n\t" \
    "s_waitcnt lgkmcnt(0)\n\t" \
    "s_sub_u32 s69, s70, s56\n\t" \
    "s_cmp_gt_i32 s69, 24\n\t" \
    "s_cbranch_scc1 vv_W86_%=\n\t" \
    "global_load_dword v140, v35, s[46:47] sc1\n\t" \
    "s_waitcnt vmcnt(0)\n\t" \
    "v_cmp_eq_u32 vcc, -1, v140\n\t" \
    "s_cbranch_vccz vv_Ook84_%=\n\t" \
    "vv_O83_%=:\n\t" \
    "global_load_dword v140, v35, s[46:47] offset:1536 sc1\n\t" \
    "s_waitcnt vmcnt(0)\n\t" \
    "v_cmp_eq_u32 vcc, -1, v140\n\t" \
    "s_cbranch_vccz vv_Ook84_%=\n\t" \
    "s_cmp_eq_u32 s68, 0\n\t" \
    "s_cbranch_scc1 vv_F87_%=\n\t" \
    "s_sub_u32 s68, s68, 1\n\t" \
    "s_sleep 3\n\t" \
    "s_branch vv_O83_%=\n\t" \
    "vv_F87_%=:\n\t" \
    "v_mov_b32 v141, 6095104\n\t" \
    "v_mov_b32 v142, 17\n\t" \
    "global_store_dword v141, v142, s[36:37] sc1\n\t" \
    "s_waitcnt vmcnt(0)\n\t" \
    "s_branch vv_Ook84_%=\n\t" \
    "vv_Ook84_%=:\n\t" \
    "v_subrev_u32 v140, s80, v140\n\t" \
    "s_nop 1\n\t" \
    "v_max_i32_dpp v140, v140, v140 row_ror:1 row_mask:0xf bank_mask:0xf\n\t" \
    "s_nop 1\n\t" \
    "v_max_i32_dpp v140, v140, v140 row_ror:2 row_mask:0xf bank_mask:0xf\n\t" \
    "s_nop 1\n\t" \
    "v_max_i32_dpp v140, v140, v140 row_ror:4 row_mask:0xf bank_mask:0xf\n\t" \
    "s_nop 1\n\t" \
    "v_max_i32_dpp v140, v140, v140 row_ror:8 row_mask:0xf bank_mask:0xf\n\t" \
    "s_nop 0\n\t" \
    "v_readlane_b32 s69, v140, 0\n\t" \
    "v_readlane_b32 s71, v140, 16\n\t" \
    "v_readlane_b32 s70, v140, 32\n\t" \
    "v_readlane_b32 s81, v140, 48\n\t" \
    "s_max_i32 s69, s69, s71\n\t" \
    "s_nop 0\n\t" \
    "s_max_i32 s71, s70, s81\n\t" \
    "s_max_i32 s69, s69, s71\n\t" \
    "s_add_u32 s81, s69, s80\n\t" \
    "vv_Oc85_%=:\n\t" \
    "s_mul_hi_u32 s69, s85, s78\n\t" \
    "s_add_u32 s79, s81, s69\n\t" \
    "s_add_u32 s79, s79, s87\n\t" \
    "s_cmp_ge_u32 s65, 36\n\t" \
    "s_cselect_b32 s69, 28, 0\n\t" \
    "s_sub_u32 s79, s79, s69\n\t" \
    "s_cmp_lt_u32 s65, 8\n\t" \
    "s_cselect_b32 s69, 96, 0\n\t" \
    "s_add_u32 s79, s79, s69\n\t" \
    "s_cmp_ge_u32 s65, 57\n\t" \
    "s_cselect_b32 s69, 96, 0\n\t" \
    "s_add_u32 s79, s79, s69\n\t" \
    "s_add_u32 s80, s79, s88\n\t" \
    "s_add_u32 s65, s65, 1\n\t" \
    "s_branch vv_go66_%=\n\t" \
    "vv_Gbad71_%=:\n\t" \
    "s_waitcnt vmcnt(0) lgkmcnt(0)\n\t" \
    "s_nop 7\n\t" \
    "v_mov_b32 v72, 0\n\t" \
    "v_mov_b32 v73, 0\n\t" \
    "v_mov_b32 v74, 0\n\t" \
    "v_mov_b32 v75, 0\n\t" \
    "v_mov_b32 v76, 0\n\t" \
    "v_mov_b32 v77, 0\n\t" \
    "v_mov_b32 v78, 0\n\t" \
    "v_mov_b32 v79, 0\n\t" \
    "s_cmp_eq_u32 s68, 0\n\t" \
    "s_cbranch_scc1 vv_F88_%=\n\t" \
    "s_sub_u32 s68, s68, 1\n\t" \
    "s_branch vv_G70_%=\n\t" \
    "vv_F88_%=:\n\t" \
    "v_mov_b32 v140, 6095104\n\t" \
    "v_mov_b32 v141, 1\n\t" \
    "global_store_dword v140, v141, s[36:37] sc1\n\t" \
    "s_waitcnt vmcnt(0)\n\t" \
    "s_branch vv_Gc73_%=\n\t" \
    "vv_Gbad272_%=:\n\t" \
    "global_load_dwordx4 v[44:47], v2, s[46:47] sc1\n\t" \
    "s_waitcnt vmcnt(0)\n\t" \
    "v_max3_u32 v140, v44, v45, v46\n\t" \
    "v_max_u32 v140, v140, v47\n\t" \
    "v_cmp_eq_u32 vcc, -1, v140\n\t" \
    "s_cbranch_vccz vv_Gre275_%=\n\t" \
    "s_cmp_eq_u32 s68, 0\n\t" \
    "s_cbranch_scc1 vv_F89_%=\n\t" \
    "s_sub_u32 s68, s68, 1\n\t" \
    "s_sleep 1\n\t" \
    "s_branch vv_Gbad272_%=\n\t" \
    "vv_F89_%=:\n\t" \
    "v_mov_b32 v140, 6095104\n\t" \
    "v_mov_b32 v141, 2\n\t" \
    "global_store_dword v140, v141, s[36:37] sc1\n\t" \
    "s_waitcnt vmcnt(0)\n\t" \
    "s_branch vv_Gre275_%=\n\t" \
    "vv_Gre275_%=:\n\t" \
    "v_mov_b32 v72, 0\n\t" \
    "v_mov_b32 v73, 0\n\t" \
    "v_mov_b32 v74, 0\n\t" \
    "v_mov_b32 v75, 0\n\t" \
    "v_mov_b32 v76, 0\n\t" \
    "v_mov_b32 v77, 0\n\t" \
    "v_mov_b32 v78, 0\n\t" \
    "v_mov_b32 v79, 0\n\t" \
    "v_smfmac_f32_16x16x64_bf16 v[72:75], v[36:39], a[32:39], v10\n\t" \
    "s_nop 0\n\t" \
    "v_smfmac_f32_16x16x64_bf16 v[76:79], v[36:39], a[104:111], v10\n\t" \
    "v_smfmac_f32_16x16x64_bf16 v[72:75], v[40:43], a[40:47], v10\n\t" \
    "v_smfmac_f32_16x16x64_bf16 v[76:79], v[40:43], a[112:119], v10\n\t" \
    "v_smfmac_f32_16x16x64_bf16 v[72:75], v[44:47], a[64:71], v10\n\t" \
    "v_smfmac_f32_16x16x64_bf16 v[76:79], v[44:47], a[136:143], v10\n\t" \
    "s_branch vv_Gc274_%=\n\t" \
    "vv_end65_%=:\n\t" \
    "s_endpgm\n\t" \
    "vv_loop13_%=:\n\t" \
    "v_mov_b32 v72, 0\n\t" \
    "v_mov_b32 v73, 0\n\t" \
    "v_mov_b32 v74, 0\n\t" \
    "v_mov_b32 v75, 0\n\t" \
    "v_mov_b32 v76, 0\n\t" \
    "v_mov_b32 v77, 0\n\t" \
    "v_mov_b32 v78, 0\n\t" \
    "v_mov_b32 v79, 0\n\t" \
    "v_mov_b32 v92, 0\n\t" \
    "v_mov_b32 v93, 0\n\t" \
    "v_mov_b32 v94, 0\n\t" \
    "v_mov_b32 v95, 0\n\t" \
    "v_mov_b32 v96, 0\n\t" \
    "v_mov_b32 v97, 0\n\t" \
    "v_mov_b32 v98, 0\n\t" \
    "v_mov_b32 v99, 0\n\t" \
    "v_mov_b32 v100, 0\n\t" \
    "v_mov_b32 v101, 0\n\t" \
    "v_mov_b32 v102, 0\n\t" \
    "v_mov_b32 v103, 0\n\t" \
    "v_mov_b32 v104, 0\n\t" \
    "v_mov_b32 v105, 0\n\t" \
    "v_mov_b32 v106, 0\n\t" \
    "v_mov_b32 v107, 0\n\t" \
    "v_mov_b32 v108, 0\n\t" \
    "v_mov_b32 v109, 0\n\t" \
    "v_mov_b32 v110, 0\n\t" \
    "v_mov_b32 v111, 0\n\t" \
    "v_mov_b32 v112, 0\n\t" \
    "v_mov_b32 v113, 0\n\t" \
    "v_mov_b32 v114, 0\n\t" \
    "v_mov_b32 v115, 0\n\t" \
    "v_mov_b32 v116, 0\n\t" \
    "v_mov_b32 v117, 0\n\t" \
    "v_mov_b32 v118, 0\n\t" \
    "v_mov_b32 v119, 0\n\t" \
    "v_mov_b32 v120, 0\n\t" \
    "v_mov_b32 v121, 0\n\t" \
    "v_mov_b32 v122, 0\n\t" \
    "v_mov_b32 v123, 0\n\t" \
    "v_mov_b32 v124, 0\n\t" \
    "v_mov_b32 v125, 0\n\t" \
    "v_mov_b32 v126, 0\n\t" \
    "v_mov_b32 v127, 0\n\t" \
    "vv_go91_%=:\n\t" \
    "s_add_u32 s69, s65, 0\n\t" \
    "s_bitcmp1_b32 s69, 0\n\t" \
    "s_cselect_b32 s70, s63, s62\n\t" \
    "s_bfe_u32 s69, s69, 0x20001\n\t" \
    "s_lshl_b32 s69, s69, 19\n\t" \
    "s_add_u32 s69, s69, s70\n\t" \
    "s_add_u32 s46, s36, s69\n\t" \
    "s_addc_u32 s47, s37, 0\n\t" \
    "s_add_u32 s69, s65, 0\n\t" \
    "s_mul_i32 s70, s69, 171\n\t" \
    "s_lshr_b32 s70, s70, 9\n\t" \
    "s_mul_i32 s70, s70, 3\n\t" \
    "s_sub_u32 s69, s69, s70\n\t" \
    "s_mul_i32 s69, s69, 36\n\t" \
    "s_lshl_b32 s69, s69, 14\n\t" \
    "s_add_u32 s69, s69, s64\n\t" \
    "s_add_u32 s69, s69, 4194304\n\t" \
    "s_add_u32 s38, s36, s69\n\t" \
    "s_addc_u32 s39, s37, 0\n\t" \
    "s_mul_i32 s69, s65, 9216\n\t" \
    "s_add_u32 s69, s69, 123731968\n\t" \
    "s_add_u32 s40, s36, s69\n\t" \
    "s_addc_u32 s41, s37, 0\n\t" \
    "s_sub_u32 s70, s79, s74\n\t" \
    "s_max_i32 s70, s70, 0\n\t" \
    "s_min_i32 s70, s70, 16000\n\t" \
    "s_lshl_b32 s70, s70, 1\n\t" \
    "s_mul_hi_u32 s70, s70, s77\n\t" \
    "s_add_u32 s70, s70, s73\n\t" \
    "s_sub_u32 s70, s70, 100\n\t" \
    "s_getpc_b64 s[30:31]\n\t" \
    "vv_Wp93_%=:\n\t" \
    "s_add_u32 s30, s30, vv_We94_%=-vv_Wp93_%=\n\t" \
    "s_addc_u32 s31, s31, 0\n\t" \
    "vv_W92_%=:\n\t" \
    "s_memtime s[56:57]\n\t" \
    "s_waitcnt lgkmcnt(0)\n\t" \
    "s_sub_u32 s69, s70, s56\n\t" \
    "s_cmp_gt_i32 s69, 111\n\t" \
    "s_cbranch_scc1 vv_W92_%=\n\t" \
    "s_max_i32 s69, s69, 0\n\t" \
    "s_lshr_b32 s69, s69, 1\n\t" \
    "s_and_b32 s69, s69, 0x3c\n\t" \
    "s_sub_u32 s30, s30, s69\n\t" \
    "s_subb_u32 s31, s31, 0\n\t" \
    "s_setpc_b64 s[30:31]\n\t" \
    ".p2align 6\n\t" \
    "s_nop 0\n\t" \
    "s_nop 0\n\t" \
    "s_nop 0\n\t" \
    "s_nop 1\n\t" \
    "s_nop 1\n\t" \
    "s_nop 1\n\t" \
    "s_nop 1\n\t" \
    "s_nop 1\n\t" \
    "s_nop 1\n\t" \
    "s_nop 1\n\t" \
    "s_nop 1\n\t" \
    "s_nop 1\n\t" \
    "s_nop 1\n\t" \
    "s_nop 1\n\t" \
    "s_nop 1\n\t" \
    "s_nop 1\n\t" \
    "vv_We94_%=:\n\t" \
    "vv_G95_%=:\n\t" \
    "global_load_dwordx4 v[36:39], v1, s[46:47] sc1\n\t" \
    "s_nop 9\n\t" \
    "global_load_dwordx4 v[40:43], v1, s[46:47] offset:1024 sc1\n\t" \
    "s_nop 9\n\t" \
    "global_load_dwordx4 v[44:47], v2, s[46:47] sc1\n\t" \
    "s_waitcnt vmcnt(2)\n\t" \
    "v_smfmac_f32_16x16x64_bf16 v[72:75], v[36:39], a[48:55], v10\n\t" \
    "ds_write_b128 v0, v[36:39] offset:6144\n\t" \
    "v_smfmac_f32_16x16x64_bf16 v[76:79], v[36:39], a[120:127], v10\n\t" \
    "v_max3_u32 v140, v36, v37, v38\n\t" \
    "s_waitcnt vmcnt(1)\n\t" \
    "ds_write_b128 v0, v[40:43] offset:7168\n\t" \
    "v_smfmac_f32_16x16x64_bf16 v[72:75], v[40:43], a[56:63], v10\n\t" \
    "v_max3_u32 v140, v140, v39, v40\n\t" \
    "v_max3_u32 v140, v140, v41, v42\n\t" \
    "v_max_u32 v140, v140, v43\n\t" \
    "v_cmp_eq_u32 vcc, -1, v140\n\t" \
    "v_smfmac_f32_16x16x64_bf16 v[76:79], v[40:43], a[128:135], v10\n\t" \
    "s_cbranch_vccnz vv_Gbad96_%=\n\t" \
    "vv_Gc98_%=:\n\t" \
    "s_waitcnt lgkmcnt(0)\n\t" \
    "s_barrier\n\t" \
    "ds_read_b128 v[48:51], v0 offset:0\n\t" \
    "ds_read_b128 v[52:55], v0 offset:1024\n\t" \
    "s_waitcnt vmcnt(0)\n\t" \
    "v_smfmac_f32_16x16x64_bf16 v[72:75], v[44:47], a[64:71], v10\n\t" \
    "ds_read_b128 v[56:59], v0 offset:2048\n\t" \
    "ds_read_b128 v[60:63], v0 offset:3072\n\t" \
    "v_max3_u32 v140, v44, v45, v46\n\t" \
    "v_max_u32 v140, v140, v47\n\t" \
    "v_smfmac_f32_16x16x64_bf16 v[76:79], v[44:47], a[136:143], v10\n\t" \
    "ds_read_b128 v[64:67], v0 offset:4096\n\t" \
    "ds_read_b128 v[68:71], v0 offset:5120\n\t" \
    "v_cmp_eq_u32 vcc, -1, v140\n\t" \
    "s_cbranch_vccnz vv_Gbad297_%=\n\t" \
    "vv_Gc299_%=:\n\t" \
    "s_waitcnt lgkmcnt(5)\n\t" \
    "v_smfmac_f32_16x16x64_bf16 v[72:75], v[48:51], a[0:7], v10\n\t" \
    "s_waitcnt lgkmcnt(4)\n\t" \
    "v_smfmac_f32_16x16x64_bf16 v[72:75], v[52:55], a[8:15], v10\n\t" \
    "s_waitcnt lgkmcnt(3)\n\t" \
    "v_smfmac_f32_16x16x64_bf16 v[72:75], v[56:59], a[16:23], v10\n\t" \
    "s_waitcnt lgkmcnt(2)\n\t" \
    "v_smfmac_f32_16x16x64_bf16 v[72:75], v[60:63], a[24:31], v10\n\t" \
    "s_waitcnt lgkmcnt(1)\n\t" \
    "v_smfmac_f32_16x16x64_bf16 v[72:75], v[64:67], a[32:39], v10\n\t" \
    "s_waitcnt lgkmcnt(0)\n\t" \
    "v_smfmac_f32_16x16x64_bf16 v[72:75], v[68:71], a[40:47], v10\n\t" \
    "v_smfmac_f32_16x16x64_bf16 v[76:79], v[48:51], a[72:79], v10\n\t" \
    "s_nop 1\n\t" \
    "s_nop 0\n\t" \
    "v_smfmac_f32_16x16x64_bf16 v[76:79], v[52:55], a[80:87], v10\n\t" \
    "s_nop 1\n\t" \
    "s_nop 0\n\t" \
    "v_smfmac_f32_16x16x64_bf16 v[76:79], v[56:59], a[88:95], v10\n\t" \
    "v_add_f32 v72, v72, v74\n\t" \
    "v_smfmac_f32_16x16x64_bf16 v[76:79], v[60:63], a[96:103], v10\n\t" \
    "v_add_f32 v73, v73, v75\n\t" \
    "v_smfmac_f32_16x16x64_bf16 v[76:79], v[64:67], a[104:111], v10\n\t" \
    "v_cvt_pk_bf16_f32 v80, v72, v73\n\t" \
    "v_smfmac_f32_16x16x64_bf16 v[76:79], v[68:71], a[112:119], v10\n\t" \
    "ds_write_b32 v12, v80 offset:0\n\t" \
    "s_nop 6\n\t" \
    "v_add_f32 v76, v76, v78\n\t" \
    "v_add_f32 v77, v77, v79\n\t" \
    "v_cvt_pk_bf16_f32 v81, v76, v77\n\t" \
    "ds_write_b32 v12, v81 offset:64\n\t" \
    "s_waitcnt lgkmcnt(0)\n\t" \
    "s_barrier\n\t" \
    "ds_read_b64_tr_b16 v[84:85], v13 offset:0\n\t" \
    "ds_read_b64_tr_b16 v[86:87], v13 offset:128\n\t" \
    "ds_read_b64_tr_b16 v[88:89], v13 offset:1024\n\t" \
    "ds_read_b64_tr_b16 v[90:91], v13 offset:1152\n\t" \
    "s_waitcnt lgkmcnt(2)\n\t" \
    "v_smfmac_f32_16x16x64_bf16 v[92:95], v[84:87], a[144:151], v10\n\t" \
    "s_nop 2\n\t" \
    "s_waitcnt lgkmcnt(0)\n\t" \
    "v_smfmac_f32_16x16x64_bf16 v[92:95], v[88:91], a[152:159], v10\n\t" \
    "s_nop 2\n\t" \
    "v_smfmac_f32_16x16x64_bf16 v[96:99], v[84:87], a[160:167], v10\n\t" \
    "s_nop 2\n\t" \
    "v_smfmac_f32_16x16x64_bf16 v[96:99], v[88:91], a[168:175], v10\n\t" \
    "s_nop 2\n\t" \
    "v_smfmac_f32_16x16x64_bf16 v[100:103], v[84:87], a[176:183], v10\n\t" \
    "s_nop 0\n\t" \
    "v_add_f32 v92, v92, v94\n\t" \
    "v_add_f32 v93, v93, v95\n\t" \
    "v_smfmac_f32_16x16x64_bf16 v[100:103], v[88:91], a[184:191], v10\n\t" \
    "s_nop 1\n\t" \
    "v_cvt_pk_bf16_f32 v128, v92, v93\n\t" \
    "v_smfmac_f32_16x16x64_bf16 v[104:107], v[84:87], a[192:199], v10\n\t" \
    "s_nop 0\n\t" \
    "v_add_f32 v96, v96, v98\n\t" \
    "v_add_f32 v97, v97, v99\n\t" \
    "v_smfmac_f32_16x16x64_bf16 v[104:107], v[88:91], a[200:207], v10\n\t" \
    "s_nop 1\n\t" \
    "v_cvt_pk_bf16_f32 v129, v96, v97\n\t" \
    "v_smfmac_f32_16x16x64_bf16 v[124:127], v[84:87], v[240:247], v10\n\t" \
    "s_nop 0\n\t" \
    "v_add_f32 v100, v100, v102\n\t" \
    "v_add_f32 v101, v101, v103\n\t" \
    "v_smfmac_f32_16x16x64_bf16 v[124:127], v[88:91], v[248:255], v10\n\t" \
    "s_nop 1\n\t" \
    "global_load_dword v140, v20, s[46:47] sc1\n\t" \
    "v_cvt_pk_bf16_f32 v130, v100, v101\n\t" \
    "v_smfmac_f32_16x16x64_bf16 v[108:111], v[84:87], a[208:215], v10\n\t" \
    "s_nop 0\n\t" \
    "v_add_f32 v104, v104, v106\n\t" \
    "v_add_f32 v105, v105, v107\n\t" \
    "v_smfmac_f32_16x16x64_bf16 v[108:111], v[88:91], a[216:223], v10\n\t" \
    "s_nop 0\n\t" \
    "v_cvt_pk_bf16_f32 v131, v104, v105\n\t" \
    "v_permlane32_swap_b32 v128, v130\n\t" \
    "v_smfmac_f32_16x16x64_bf16 v[112:115], v[84:87], a[224:231], v10\n\t" \
    "s_nop 0\n\t" \
    "v_permlane32_swap_b32 v129, v131\n\t" \
    "s_nop 1\n\t" \
    "v_permlane16_swap_b32 v128, v129\n\t" \
    "v_smfmac_f32_16x16x64_bf16 v[112:115], v[88:91], a[232:239], v10\n\t" \
    "v_permlane16_swap_b32 v130, v131\n\t" \
    "global_store_dwordx4 v14, v[128:131], s[38:39] sc1\n\t" \
    "v_add_f32 v124, v124, v126\n\t" \
    "v_smfmac_f32_16x16x64_bf16 v[116:119], v[84:87], a[240:247], v10\n\t" \
    "s_nop 0\n\t" \
    "v_add_f32 v125, v125, v127\n\t" \
    "v_cvt_pk_bf16_f32 v136, v124, v125\n\t" \
    "v_smfmac_f32_16x16x64_bf16 v[116:119], v[88:91], a[248:255], v10\n\t" \
    "global_store_dword v16, v136, s[38:39] sc1\n\t" \
    "v_add_f32 v108, v108, v110\n\t" \
    "v_add_f32 v109, v109, v111\n\t" \
    "v_smfmac_f32_16x16x64_bf16 v[120:123], v[84:87], v[224:231], v10\n\t" \
    "s_nop 0\n\t" \
    "v_cvt_pk_bf16_f32 v132, v108, v109\n\t" \
    "v_add_f32 v112, v112, v114\n\t" \
    "v_smfmac_f32_16x16x64_bf16 v[120:123], v[88:91], v[232:239], v10\n\t" \
    "v_add_f32 v113, v113, v115\n\t" \
    "v_cvt_pk_bf16_f32 v133, v112, v113\n\t" \
    "v_add_f32 v116, v116, v118\n\t" \
    "v_add_f32 v117, v117, v119\n\t" \
    "v_cvt_pk_bf16_f32 v134, v116, v117\n\t" \
    "s_nop 2\n\t" \
    "v_pk_add_f32 v[120:121], v[120:121], v[122:123]\n\t" \
    "v_cvt_pk_bf16_f32 v135, v120, v121\n\t" \
    "v_permlane32_swap_b32 v132, v134\n\t" \
    "s_nop 0\n\t" \
    "v_permlane32_swap_b32 v133, v135\n\t" \
    "s_nop 1\n\t" \
    "v_permlane16_swap_b32 v132, v133\n\t" \
    "v_permlane16_swap_b32 v134, v135\n\t" \
    "global_store_dwordx4 v15, v[132:135], s[38:39] sc1\n\t" \
    "s_nop 1\n\t" \
    "global_store_dwordx4 v17, v[128:131], s[40:41] sc1\n\t" \
    "global_store_dwordx4 v17, v[132:135], s[40:41] offset:1024 sc1\n\t" \
    "global_store_dword v18, v136, s[40:41] sc1\n\t" \
    "s_cmp_eq_u32 s65, 63\n\t" \
    "s_cbranch_scc1 vv_end90_%=\n\t" \
    "s_mov_b32 s71, 3\n\t" \
    "vv_E101_%=:\n\t" \
    "s_memtime s[54:55]\n\t" \
    "s_nop 0\n\t" \
    "s_memrealtime s[4:5]\n\t" \
    "s_nop 0\n\t" \
    "s_memrealtime s[6:7]\n\t" \
    "s_nop 0\n\t" \
    "s_memrealtime s[8:9]\n\t" \
    "s_nop 0\n\t" \
    "s_memrealtime s[10:11]\n\t" \
    "s_nop 0\n\t" \
    "s_memrealtime s[12:13]\n\t" \
    "s_nop 0\n\t" \
    "s_memrealtime s[14:15]\n\t" \
    "s_nop 0\n\t" \
    "s_memrealtime s[16:17]\n\t" \
    "s_nop 0\n\t" \
    "s_memrealtime s[18:19]\n\t" \
    "s_nop 0\n\t" \
    "s_memrealtime s[20:21]\n\t" \
    "s_nop 0\n\t" \
    "s_memrealtime s[22:23]\n\t" \
    "s_nop 0\n\t" \
    "s_memrealtime s[24:25]\n\t" \
    "s_nop 0\n\t" \
    "s_memrealtime s[26:27]\n\t" \
    "s_nop 0\n\t" \
    "s_memrealtime s[28:29]\n\t" \
    "s_nop 0\n\t" \
    "s_memtime s[56:57]\n\t" \
    "s_mov_b32 s70, 1\n\t" \
    "s_sub_u32 s71, s71, 1\n\t" \
    "s_cselect_b32 s69, 0, -1\n\t" \
    "s_waitcnt lgkmcnt(0)\n\t" \
    "s_sub_u32 s54, s56, s54\n\t" \
    "s_and_b32 s54, s54, s69\n\t" \
    "s_cmp_gt_u32 s54, 124\n\t" \
    "s_cbranch_scc1 vv_E101_%=\n\t" \
    "s_cmp_eq_u32 s4, s28\n\t" \
    "s_addc_u32 s70, s70, 0\n\t" \
    "s_cmp_eq_u32 s6, s28\n\t" \
    "s_addc_u32 s70, s70, 0\n\t" \
    "s_cmp_eq_u32 s8, s28\n\t" \
    "s_addc_u32 s70, s70, 0\n\t" \
    "s_cmp_eq_u32 s10, s28\n\t" \
    "s_addc_u32 s70, s70, 0\n\t" \
    "s_cmp_eq_u32 s12, s28\n\t" \
    "s_addc_u32 s70, s70, 0\n\t" \
    "s_cmp_eq_u32 s14, s28\n\t" \
    "s_addc_u32 s70, s70, 0\n\t" \
    "s_cmp_eq_u32 s16, s28\n\t" \
    "s_addc_u32 s70, s70, 0\n\t" \
    "s_cmp_eq_u32 s18, s28\n\t" \
    "s_addc_u32 s70, s70, 0\n\t" \
    "s_cmp_eq_u32 s20, s28\n\t" \
    "s_addc_u32 s70, s70, 0\n\t" \
    "s_cmp_eq_u32 s22, s28\n\t" \
    "s_addc_u32 s70, s70, 0\n\t" \
    "s_cmp_eq_u32 s24, s28\n\t" \
    "s_addc_u32 s70, s70, 0\n\t" \
    "s_cmp_eq_u32 s26, s28\n\t" \
    "s_addc_u32 s70, s70, 0\n\t" \
    "s_mov_b32 s75, s73\n\t" \
    "s_mov_b32 s76, s74\n\t" \
    "s_lshl_b32 s70, s70, 3\n\t" \
    "s_sub_u32 s73, s56, s70\n\t" \
    "s_lshl_b32 s74, s28, 4\n\t" \
    "s_sub_u32 s69, s73, s75\n\t" \
    "s_sub_u32 s70, s74, s76\n\t" \
    "s_sub_u32 s71, s70, 1600\n\t" \
    "s_cmp_lt_u32 s71, 400000\n\t" \
    "s_cbranch_scc0 vv_K103_%=\n\t" \
    "v_cvt_f32_u32 v141, s69\n\t" \
    "v_cvt_f32_u32 v142, s70\n\t" \
    "v_rcp_f32 v143, v141\n\t" \
    "v_rcp_f32 v144, v142\n\t" \
    "v_mul_f32 v143, v143, v142\n\t" \
    "v_mul_f32 v144, v144, v141\n\t" \
    "v_mul_f32 v143, 0x4f800000, v143\n\t" \
    "v_mul_f32 v144, 0x4f000000, v144\n\t" \
    "v_cvt_u32_f32 v143, v143\n\t" \
    "v_cvt_u32_f32 v144, v144\n\t" \
    "v_readfirstlane_b32 s69, v143\n\t" \
    "v_readfirstlane_b32 s70, v144\n\t" \
    "s_nop 1\n\t" \
    "s_sub_u32 s69, s69, s78\n\t" \
    "s_ashr_i32 s69, s69, 2\n\t" \
    "s_add_u32 s78, s78, s69\n\t" \
    "s_sub_u32 s70, s70, s77\n\t" \
    "s_ashr_i32 s70, s70, 2\n\t" \
    "s_add_u32 s77, s77, s70\n\t" \
    "vv_K103_%=:\n\t" \
    "s_waitcnt vmcnt(6)\n\t" \
    "v_cmp_eq_u32 vcc, -1, v140\n\t" \
    "s_cbranch_vccz vv_Hok105_%=\n\t" \
    "vv_H104_%=:\n\t" \
    "global_load_dword v140, v20, s[46:47] sc1\n\t" \
    "s_waitcnt vmcnt(0)\n\t" \
    "v_cmp_eq_u32 vcc, -1, v140\n\t" \
    "s_cbranch_vccz vv_Hc106_%=\n\t" \
    "s_cmp_eq_u32 s68, 0\n\t" \
    "s_cbranch_scc1 vv_F107_%=\n\t" \
    "s_sub_u32 s68, s68, 1\n\t" \
    "s_sleep 5\n\t" \
    "s_branch vv_H104_%=\n\t" \
    "vv_F107_%=:\n\t" \
    "v_mov_b32 v141, 6095104\n\t" \
    "v_mov_b32 v142, 6\n\t" \
    "global_store_dword v141, v142, s[36:37] sc1\n\t" \
    "s_waitcnt vmcnt(0)\n\t" \
    "s_branch vv_Hc106_%=\n\t" \
    "vv_Hc106_%=:\n\t" \
    "vv_Hok105_%=:\n\t" \
    "v_subrev_u32 v140, s80, v140\n\t" \
    "v_and_b32 v20, -5, v20\n\t" \
    "s_nop 0\n\t" \
    "v_max_i32_dpp v140, v140, v140 row_ror:1 row_mask:0xf bank_mask:0xf\n\t" \
    "s_nop 1\n\t" \
    "v_max_i32_dpp v140, v140, v140 row_ror:2 row_mask:0xf bank_mask:0xf\n\t" \
    "s_nop 1\n\t" \
    "v_max_i32_dpp v140, v140, v140 row_ror:4 row_mask:0xf bank_mask:0xf\n\t" \
    "s_nop 1\n\t" \
    "v_max_i32_dpp v140, v140, v140 row_ror:8 row_mask:0xf bank_mask:0xf\n\t" \
    "s_nop 0\n\t" \
    "v_readlane_b32 s69, v140, 0\n\t" \
    "v_readlane_b32 s71, v140, 16\n\t" \
    "v_readlane_b32 s70, v140, 32\n\t" \
    "v_readlane_b32 s81, v140, 48\n\t" \
    "s_max_i32 s69, s69, s71\n\t" \
    "s_nop 0\n\t" \
    "s_max_i32 s71, s70, s81\n\t" \
    "s_max_i32 s69, s69, s71\n\t" \
    "s_add_u32 s81, s69, s80\n\t" \
    "v_mov_b32 v72, 0\n\t" \
    "v_mov_b32 v73, 0\n\t" \
    "v_mov_b32 v74, 0\n\t" \
    "v_mov_b32 v75, 0\n\t" \
    "v_mov_b32 v76, 0\n\t" \
    "v_mov_b32 v77, 0\n\t" \
    "v_mov_b32 v78, 0\n\t" \
    "v_mov_b32 v79, 0\n\t" \
    "v_mov_b32 v92, 0\n\t" \
    "v_mov_b32 v93, 0\n\t" \
    "v_mov_b32 v94, 0\n\t" \
    "v_mov_b32 v95, 0\n\t" \
    "v_mov_b32 v96, 0\n\t" \
    "v_mov_b32 v97, 0\n\t" \
    "v_mov_b32 v98, 0\n\t" \
    "v_mov_b32 v99, 0\n\t" \
    "v_mov_b32 v100, 0\n\t" \
    "v_mov_b32 v101, 0\n\t" \
    "v_mov_b32 v102, 0\n\t" \
    "v_mov_b32 v103, 0\n\t" \
    "v_mov_b32 v104, 0\n\t" \
    "v_mov_b32 v105, 0\n\t" \
    "v_mov_b32 v106, 0\n\t" \
    "v_mov_b32 v107, 0\n\t" \
    "v_mov_b32 v108, 0\n\t" \
    "v_mov_b32 v109, 0\n\t" \
    "v_mov_b32 v110, 0\n\t" \
    "v_mov_b32 v111, 0\n\t" \
    "v_mov_b32 v112, 0\n\t" \
    "v_mov_b32 v113, 0\n\t" \
    "v_mov_b32 v114, 0\n\t" \
    "v_mov_b32 v115, 0\n\t" \
    "v_mov_b32 v116, 0\n\t" \
    "v_mov_b32 v117, 0\n\t" \
    "v_mov_b32 v118, 0\n\t" \
    "v_mov_b32 v119, 0\n\t" \
    "v_mov_b32 v120, 0\n\t" \
    "v_mov_b32 v121, 0\n\t" \
    "v_mov_b32 v122, 0\n\t" \
    "v_mov_b32 v123, 0\n\t" \
    "v_mov_b32 v124, 0\n\t" \
    "v_mov_b32 v125, 0\n\t" \
    "v_mov_b32 v126, 0\n\t" \
    "v_mov_b32 v127, 0\n\t" \
    "s_cmp_lg_u32 s65, 0\n\t" \
    "s_cbranch_scc1 vv_Oc110_%=\n\t" \
    "s_add_u32 s79, s81, 240\n\t" \
    "s_sub_u32 s70, s79, s74\n\t" \
    "s_max_i32 s70, s70, 0\n\t" \
    "s_min_i32 s70, s70, 16000\n\t" \
    "s_lshl_b32 s70, s70, 1\n\t" \
    "s_mul_hi_u32 s70, s70, s77\n\t" \
    "s_add_u32 s70, s70, s73\n\t" \
    "vv_W111_%=:\n\t" \
    "s_memtime s[56:57]\n\t" \
    "s_waitcnt lgkmcnt(0)\n\t" \
    "s_sub_u32 s69, s70, s56\n\t" \
    "s_cmp_gt_i32 s69, 24\n\t" \
    "s_cbranch_scc1 vv_W111_%=\n\t" \
    "global_load_dword v140, v35, s[46:47] sc1\n\t" \
    "s_waitcnt vmcnt(0)\n\t" \
    "v_cmp_eq_u32 vcc, -1, v140\n\t" \
    "s_cbranch_vccz vv_Ook109_%=\n\t" \
    "vv_O108_%=:\n\t" \
    "global_load_dword v140, v35, s[46:47] offset:1536 sc1\n\t" \
    "s_waitcnt vmcnt(0)\n\t" \
    "v_cmp_eq_u32 vcc, -1, v140\n\t" \
    "s_cbranch_vccz vv_Ook109_%=\n\t" \
    "s_cmp_eq_u32 s68, 0\n\t" \
    "s_cbranch_scc1 vv_F112_%=\n\t" \
    "s_sub_u32 s68, s68, 1\n\t" \
    "s_sleep 3\n\t" \
    "s_branch vv_O108_%=\n\t" \
    "vv_F112_%=:\n\t" \
    "v_mov_b32 v141, 6095104\n\t" \
    "v_mov_b32 v142, 17\n\t" \
    "global_store_dword v141, v142, s[36:37] sc1\n\t" \
    "s_waitcnt vmcnt(0)\n\t" \
    "s_branch vv_Ook109_%=\n\t" \
    "vv_Ook109_%=:\n\t" \
    "v_subrev_u32 v140, s80, v140\n\t" \
    "s_nop 1\n\t" \
    "v_max_i32_dpp v140, v140, v140 row_ror:1 row_mask:0xf bank_mask:0xf\n\t" \
    "s_nop 1\n\t" \
    "v_max_i32_dpp v140, v140, v140 row_ror:2 row_mask:0xf bank_mask:0xf\n\t" \
    "s_nop 1\n\t" \
    "v_max_i32_dpp v140, v140, v140 row_ror:4 row_mask:0xf bank_mask:0xf\n\t" \
    "s_nop 1\n\t" \
    "v_max_i32_dpp v140, v140, v140 row_ror:8 row_mask:0xf bank_mask:0xf\n\t" \
    "s_nop 0\n\t" \
    "v_readlane_b32 s69, v140, 0\n\t" \
    "v_readlane_b32 s71, v140, 16\n\t" \
    "v_readlane_b32 s70, v140, 32\n\t" \
    "v_readlane_b32 s81, v140, 48\n\t" \
    "s_max_i32 s69, s69, s71\n\t" \
    "s_nop 0\n\t" \
    "s_max_i32 s71, s70, s81\n\t" \
    "s_max_i32 s69, s69, s71\n\t" \
    "s_add_u32 s81, s69, s80\n\t" \
    "vv_Oc110_%=:\n\t" \
    "s_mul_hi_u32 s69, s85, s78\n\t" \
    "s_add_u32 s79, s81, s69\n\t" \
    "s_add_u32 s79, s79, s87\n\t" \
    "s_cmp_ge_u32 s65, 36\n\t" \
    "s_cselect_b32 s69, 28, 0\n\t" \
    "s_sub_u32 s79, s79, s69\n\t" \
    "s_cmp_lt_u32 s65, 8\n\t" \
    "s_cselect_b32 s69, 96, 0\n\t" \
    "s_add_u32 s79, s79, s69\n\t" \
    "s_cmp_ge_u32 s65, 57\n\t" \
    "s_cselect_b32 s69, 96, 0\n\t" \
    "s_add_u32 s79, s79, s69\n\t" \
    "s_add_u32 s80, s79, s88\n\t" \
    "s_add_u32 s65, s65, 1\n\t" \
    "s_branch vv_go91_%=\n\t" \
    "vv_Gbad96_%=:\n\t" \
    "s_waitcnt vmcnt(0) lgkmcnt(0)\n\t" \
    "s_nop 7\n\t" \
    "v_mov_b32 v72, 0\n\t" \
    "v_mov_b32 v73, 0\n\t" \
    "v_mov_b32 v74, 0\n\t" \
    "v_mov_b32 v75, 0\n\t" \
    "v_mov_b32 v76, 0\n\t" \
    "v_mov_b32 v77, 0\n\t" \
    "v_mov_b32 v78, 0\n\t" \
    "v_mov_b32 v79, 0\n\t" \
    "s_cmp_eq_u32 s68, 0\n\t" \
    "s_cbranch_scc1 vv_F113_%=\n\t" \
    "s_sub_u32 s68, s68, 1\n\t" \
    "s_branch vv_G95_%=\n\t" \
    "vv_F113_%=:\n\t" \
    "v_mov_b32 v140, 6095104\n\t" \
    "v_mov_b32 v141, 1\n\t" \
    "global_store_dword v140, v141, s[36:37] sc1\n\t" \
    "s_waitcnt vmcnt(0)\n\t" \
    "s_branch vv_Gc98_%=\n\t" \
    "vv_Gbad297_%=:\n\t" \
    "global_load_dwordx4 v[44:47], v2, s[46:47] sc1\n\t" \
    "s_waitcnt vmcnt(0)\n\t" \
    "v_max3_u32 v140, v44, v45, v46\n\t" \
    "v_max_u32 v140, v140, v47\n\t" \
    "v_cmp_eq_u32 vcc, -1, v140\n\t" \
    "s_cbranch_vccz vv_Gre2100_%=\n\t" \
    "s_cmp_eq_u32 s68, 0\n\t" \
    "s_cbranch_scc1 vv_F114_%=\n\t" \
    "s_sub_u32 s68, s68, 1\n\t" \
    "s_sleep 1\n\t" \
    "s_branch vv_Gbad297_%=\n\t" \
    "vv_F114_%=:\n\t" \
    "v_mov_b32 v140, 6095104\n\t" \
    "v_mov_b32 v141, 2\n\t" \
    "global_store_dword v140, v141, s[36:37] sc1\n\t" \
    "s_waitcnt vmcnt(0)\n\t" \
    "s_branch vv_Gre2100_%=\n\t" \
    "vv_Gre2100_%=:\n\t" \
    "v_mov_b32 v72, 0\n\t" \
    "v_mov_b32 v73, 0\n\t" \
    "v_mov_b32 v74, 0\n\t" \
    "v_mov_b32 v75, 0\n\t" \
    "v_mov_b32 v76, 0\n\t" \
    "v_mov_b32 v77, 0\n\t" \
    "v_mov_b32 v78, 0\n\t" \
    "v_mov_b32 v79, 0\n\t" \
    "v_smfmac_f32_16x16x64_bf16 v[72:75], v[36:39], a[48:55], v10\n\t" \
    "s_nop 0\n\t" \
    "v_smfmac_f32_16x16x64_bf16 v[76:79], v[36:39], a[120:127], v10\n\t" \
    "v_smfmac_f32_16x16x64_bf16 v[72:75], v[40:43], a[56:63], v10\n\t" \
    "v_smfmac_f32_16x16x64_bf16 v[76:79], v[40:43], a[128:135], v10\n\t" \
    "v_smfmac_f32_16x16x64_bf16 v[72:75], v[44:47], a[64:71], v10\n\t" \
    "v_smfmac_f32_16x16x64_bf16 v[76:79], v[44:47], a[136:143], v10\n\t" \
    "s_branch vv_Gc299_%=\n\t" \
    "vv_end90_%=:\n\t" \
    "s_endpgm\n\t" \
    "gx_begin_%=:\n\t" \
    "v_mbcnt_lo_u32_b32 v223, -1, 0\n\t" \
    "v_mbcnt_hi_u32_b32 v223, -1, v223\n\t" \
    "s_mov_b32 s68, 262144\n\t" \
    "s_mov_b32 s72, 0\n\t" \
    "s_mov_b32 s83, -1\n\t" \
    "s_mov_b32 s65, 0\n\t" \
    "v_and_b32 v180, 3, v223\n\t" \
    "v_lshlrev_b32 v202, 3, v180\n\t" \
    "v_lshrrev_b32 v181, 5, v223\n\t" \
    "v_lshl_add_u32 v202, v181, 12, v202\n\t" \
    "v_bfe_u32 v182, v223, 2, 2\n\t" \
    "s_mov_b32 s70, 288\n\t" \
    "v_mad_u32_u24 v202, v182, s70, v202\n\t" \
    "v_bfe_u32 v183, v223, 4, 1\n\t" \
    "s_mov_b32 s70, 2176\n\t" \
    "v_mad_u32_u24 v202, v183, s70, v202\n\t" \
    "s_lshl_b32 s69, s66, 6\n\t" \
    "v_xor_b32 v202, s69, v202\n\t" \
    "v_xor_b32 v203, 32, v202\n\t" \
    "v_lshlrev_b32 v3, 4, v180\n\t" \
    "v_lshlrev_b32 v2, 5, v181\n\t" \
    "v_xor_b32 v3, v3, v2\n\t" \
    "s_bfe_u32 s71, s66, 0x10000\n\t" \
    "s_mul_i32 s69, s71, 576\n\t" \
    "s_bfe_u32 s71, s66, 0x10001\n\t" \
    "s_mul_i32 s71, s71, 18432\n\t" \
    "s_add_u32 s69, s69, s71\n\t" \
    "v_lshrrev_b32 v184, 2, v223\n\t" \
    "v_mov_b32 v1, s69\n\t" \
    "s_mov_b32 s70, 1152\n\t" \
    "v_mad_u32_u24 v1, v184, s70, v1\n\t" \
    "v_add_u32 v1, v1, v3\n\t" \
    "v_and_b32 v185, 15, v223\n\t" \
    "v_lshlrev_b32 v3, 6, v185\n\t" \
    "v_lshrrev_b32 v186, 4, v223\n\t" \
    "v_lshl_add_u32 v3, v186, 4, v3\n\t" \
    "v_bfe_u32 v187, v223, 3, 1\n\t" \
    "v_lshlrev_b32 v2, 5, v187\n\t" \
    "v_xor_b32 v3, v3, v2\n\t" \
    "v_mov_b32 v2, v3\n\t" \
    "s_mul_i32 s89, s66, 9216\n\t" \
    "s_mov_b32 s90, 0\n\t" \
    "s_waitcnt vmcnt(12) lgkmcnt(0)\n\t" \
    "s_barrier\n\t" \
    "s_add_u32 m0, s82, 65536\n\t" \
    "s_nop 0\n\t" \
    "global_load_lds_dwordx4 v200, s[42:43]\n\t" \
    "v_add_u32 v200, 131072, v200\n\t" \
    "s_add_u32 m0, s82, 69632\n\t" \
    "s_nop 0\n\t" \
    "global_load_lds_dwordx4 v200, s[42:43]\n\t" \
    "v_add_u32 v200, 131072, v200\n\t" \
    "s_add_u32 m0, s82, 73728\n\t" \
    "s_nop 0\n\t" \
    "global_load_lds_dwordx4 v200, s[42:43]\n\t" \
    "v_add_u32 v200, 131072, v200\n\t" \
    "s_add_u32 m0, s82, 77824\n\t" \
    "s_nop 0\n\t" \
    "global_load_lds_dwordx4 v200, s[42:43]\n\t" \
    "v_add_u32 v200, 131072, v200\n\t" \
    "v_add_u32 v202, 73728, v202\n\t" \
    "v_add_u32 v203, 73728, v203\n\t" \
    "ds_read_b64_tr_b16 a[144:145], v202 offset:0\n\t" \
    "ds_read_b64_tr_b16 a[146:147], v202 offset:1024\n\t" \
    "ds_read_b64_tr_b16 a[148:149], v202 offset:8192\n\t" \
    "ds_read_b64_tr_b16 a[150:151], v202 offset:9216\n\t" \
    "ds_read_b64_tr_b16 a[216:217], v203 offset:0\n\t" \
    "ds_read_b64_tr_b16 a[218:219], v203 offset:1024\n\t" \
    "ds_read_b64_tr_b16 a[220:221], v203 offset:8192\n\t" \
    "ds_read_b64_tr_b16 a[222:223], v203 offset:9216\n\t" \
    "v_lshlrev_b32 v0, 4, v223\n\t" \
    "s_mul_i32 s69, s66, 2048\n\t" \
    "s_add_u32 s69, s69, 147456\n\t" \
    "v_lshl_add_u32 v4, v223, 4, s69\n\t" \
    "s_mov_b32 s69, 147456\n\t" \
    "v_lshl_add_u32 v5, v223, 4, s69\n\t" \
    "v_lshlrev_b32 v6, 2, v223\n\t" \
    "v_mov_b32 v7, 0\n\t" \
    "s_mul_i32 s97, s67, 7\n\t" \
    "s_and_b32 s97, s97, 63\n\t" \
    "s_lshl_b32 s97, s97, 13\n\t" \
    "s_mul_i32 s71, s66, 2048\n\t" \
    "s_add_u32 s69, s97, s71\n\t" \
    "s_mul_i32 s71, s67, 524288\n\t" \
    "s_add_u32 s69, s69, s71\n\t" \
    "v_lshl_add_u32 v8, v223, 4, s69\n\t" \
    "s_add_u32 s38, s36, 6291456\n\t" \
    "s_addc_u32 s39, s37, 0\n\t" \
    "s_add_u32 s40, s36, 23068672\n\t" \
    "s_addc_u32 s41, s37, 0\n\t" \
    "s_mul_i32 s69, s67, 128\n\t" \
    "s_add_u32 s69, s69, 6098944\n\t" \
    "v_and_b32 v188, 1, v223\n\t" \
    "v_mov_b32 v10, s69\n\t" \
    "s_mov_b32 s70, 61440\n\t" \
    "v_mad_u32_u24 v10, v188, s70, v10\n\t" \
    "v_lshlrev_b32 v11, 0, v188\n\t" \
    "s_cmp_eq_u32 s66, 0\n\t" \
    "s_cselect_b32 s58, 3, 0\n\t" \
    "s_mov_b32 s59, 0\n\t" \
    "s_waitcnt vmcnt(12) lgkmcnt(0)\n\t" \
    "s_barrier\n\t" \
    "s_add_u32 m0, s82, 0\n\t" \
    "s_nop 0\n\t" \
    "global_load_lds_dwordx4 v200, s[42:43]\n\t" \
    "v_add_u32 v200, 131072, v200\n\t" \
    "s_add_u32 m0, s82, 4096\n\t" \
    "s_nop 0\n\t" \
    "global_load_lds_dwordx4 v200, s[42:43]\n\t" \
    "v_add_u32 v200, 131072, v200\n\t" \
    "s_add_u32 m0, s82, 8192\n\t" \
    "s_nop 0\n\t" \
    "global_load_lds_dwordx4 v200, s[42:43]\n\t" \
    "v_add_u32 v200, 131072, v200\n\t" \
    "s_add_u32 m0, s82, 12288\n\t" \
    "s_nop 0\n\t" \
    "global_load_lds_dwordx4 v200, s[42:43]\n\t" \
    "v_add_u32 v200, 131072, v200\n\t" \
    "ds_read_b64_tr_b16 a[152:153], v202 offset:16384\n\t" \
    "ds_read_b64_tr_b16 a[154:155], v202 offset:17408\n\t" \
    "ds_read_b64_tr_b16 a[156:157], v202 offset:24576\n\t" \
    "ds_read_b64_tr_b16 a[158:159], v202 offset:25600\n\t" \
    "ds_read_b64_tr_b16 a[224:225], v203 offset:16384\n\t" \
    "ds_read_b64_tr_b16 a[226:227], v203 offset:17408\n\t" \
    "ds_read_b64_tr_b16 a[228:229], v203 offset:24576\n\t" \
    "ds_read_b64_tr_b16 a[230:231], v203 offset:25600\n\t" \
    "s_waitcnt vmcnt(12) lgkmcnt(0)\n\t" \
    "s_barrier\n\t" \
    "s_add_u32 m0, s82, 16384\n\t" \
    "s_nop 0\n\t" \
    "global_load_lds_dwordx4 v200, s[42:43]\n\t" \
    "v_add_u32 v200, 131072, v200\n\t" \
    "s_add_u32 m0, s82, 20480\n\t" \
    "s_nop 0\n\t" \
    "global_load_lds_dwordx4 v200, s[42:43]\n\t" \
    "v_add_u32 v200, 131072, v200\n\t" \
    "s_add_u32 m0, s82, 24576\n\t" \
    "s_nop 0\n\t" \
    "global_load_lds_dwordx4 v200, s[42:43]\n\t" \
    "v_add_u32 v200, 131072, v200\n\t" \
    "s_add_u32 m0, s82, 28672\n\t" \
    "s_nop 0\n\t" \
    "global_load_lds_dwordx4 v200, s[42:43]\n\t" \
    "v_add_u32 v200, 131072, v200\n\t" \
    "s_add_u32 m0, s89, 0\n\t" \
    "s_nop 0\n\t" \
    "global_load_lds_dwordx4 v1, s[50:51] offset:0\n\t" \
    "s_add_u32 m0, s89, 960\n\t" \
    "s_nop 0\n\t" \
    "global_load_lds_dwordx4 v1, s[50:51] offset:64\n\t" \
    "s_add_u32 m0, s89, 1920\n\t" \
    "s_nop 0\n\t" \
    "global_load_lds_dwordx4 v1, s[50:51] offset:128\n\t" \
    "s_add_u32 m0, s89, 2880\n\t" \
    "s_nop 0\n\t" \
    "global_load_lds_dwordx4 v1, s[50:51] offset:192\n\t" \
    "s_add_u32 m0, s89, 3840\n\t" \
    "s_nop 0\n\t" \
    "global_load_lds_dwordx4 v1, s[50:51] offset:256\n\t" \
    "s_add_u32 m0, s89, 4800\n\t" \
    "s_nop 0\n\t" \
    "global_load_lds_dwordx4 v1, s[50:51] offset:320\n\t" \
    "s_add_u32 m0, s89, 5760\n\t" \
    "s_nop 0\n\t" \
    "global_load_lds_dwordx4 v1, s[50:51] offset:384\n\t" \
    "s_add_u32 m0, s89, 6720\n\t" \
    "s_nop 0\n\t" \
    "global_load_lds_dwordx4 v1, s[50:51] offset:448\n\t" \
    "s_add_u32 m0, s89, 7680\n\t" \
    "s_nop 0\n\t" \
    "global_load_lds_dwordx4 v1, s[50:51] offset:512\n\t" \
    "ds_read_b64_tr_b16 a[160:161], v202 offset:32768\n\t" \
    "ds_read_b64_tr_b16 a[162:163], v202 offset:33792\n\t" \
    "ds_read_b64_tr_b16 a[164:165], v202 offset:40960\n\t" \
    "ds_read_b64_tr_b16 a[166:167], v202 offset:41984\n\t" \
    "ds_read_b64_tr_b16 a[232:233], v203 offset:32768\n\t" \
    "ds_read_b64_tr_b16 a[234:235], v203 offset:33792\n\t" \
    "ds_read_b64_tr_b16 a[236:237], v203 offset:40960\n\t" \
    "ds_read_b64_tr_b16 a[238:239], v203 offset:41984\n\t" \
    "s_waitcnt vmcnt(21) lgkmcnt(0)\n\t" \
    "s_barrier\n\t" \
    "s_add_u32 m0, s82, 32768\n\t" \
    "s_nop 0\n\t" \
    "global_load_lds_dwordx4 v200, s[42:43]\n\t" \
    "v_add_u32 v200, 131072, v200\n\t" \
    "s_add_u32 m0, s82, 36864\n\t" \
    "s_nop 0\n\t" \
    "global_load_lds_dwordx4 v200, s[42:43]\n\t" \
    "v_add_u32 v200, 131072, v200\n\t" \
    "s_add_u32 m0, s82, 40960\n\t" \
    "s_nop 0\n\t" \
    "global_load_lds_dwordx4 v200, s[42:43]\n\t" \
    "v_add_u32 v200, 131072, v200\n\t" \
    "s_add_u32 m0, s82, 45056\n\t" \
    "s_nop 0\n\t" \
    "global_load_lds_dwordx4 v200, s[42:43]\n\t" \
    "v_add_u32 v200, 131072, v200\n\t" \
    "s_add_u32 s50, s50, 36864\n\t" \
    "s_addc_u32 s51, s51, 0\n\t" \
    "s_add_u32 m0, s89, 36864\n\t" \
    "s_nop 0\n\t" \
    "global_load_lds_dwordx4 v1, s[50:51] offset:0\n\t" \
    "s_add_u32 m0, s89, 37824\n\t" \
    "s_nop 0\n\t" \
    "global_load_lds_dwordx4 v1, s[50:51] offset:64\n\t" \
    "s_add_u32 m0, s89, 38784\n\t" \
    "s_nop 0\n\t" \
    "global_load_lds_dwordx4 v1, s[50:51] offset:128\n\t" \
    "s_add_u32 m0, s89, 39744\n\t" \
    "s_nop 0\n\t" \
    "global_load_lds_dwordx4 v1, s[50:51] offset:192\n\t" \
    "s_add_u32 m0, s89, 40704\n\t" \
    "s_nop 0\n\t" \
    "global_load_lds_dwordx4 v1, s[50:51] offset:256\n\t" \
    "s_add_u32 m0, s89, 41664\n\t" \
    "s_nop 0\n\t" \
    "global_load_lds_dwordx4 v1, s[50:51] offset:320\n\t" \
    "s_add_u32 m0, s89, 42624\n\t" \
    "s_nop 0\n\t" \
    "global_load_lds_dwordx4 v1, s[50:51] offset:384\n\t" \
    "s_add_u32 m0, s89, 43584\n\t" \
    "s_nop 0\n\t" \
    "global_load_lds_dwordx4 v1, s[50:51] offset:448\n\t" \
    "s_add_u32 m0, s89, 44544\n\t" \
    "s_nop 0\n\t" \
    "global_load_lds_dwordx4 v1, s[50:51] offset:512\n\t" \
    "ds_read_b64_tr_b16 a[168:169], v202 offset:49152\n\t" \
    "ds_read_b64_tr_b16 a[170:171], v202 offset:50176\n\t" \
    "ds_read_b64_tr_b16 a[172:173], v202 offset:57344\n\t" \
    "ds_read_b64_tr_b16 a[174:175], v202 offset:58368\n\t" \
    "ds_read_b64_tr_b16 a[240:241], v203 offset:49152\n\t" \
    "ds_read_b64_tr_b16 a[242:243], v203 offset:50176\n\t" \
    "ds_read_b64_tr_b16 a[244:245], v203 offset:57344\n\t" \
    "ds_read_b64_tr_b16 a[246:247], v203 offset:58368\n\t" \
    "s_waitcnt vmcnt(30) lgkmcnt(0)\n\t" \
    "s_barrier\n\t" \
    "s_add_u32 m0, s82, 49152\n\t" \
    "s_nop 0\n\t" \
    "global_load_lds_dwordx4 v200, s[42:43]\n\t" \
    "v_add_u32 v200, 131072, v200\n\t" \
    "s_add_u32 m0, s82, 53248\n\t" \
    "s_nop 0\n\t" \
    "global_load_lds_dwordx4 v200, s[42:43]\n\t" \
    "v_add_u32 v200, 131072, v200\n\t" \
    "s_add_u32 m0, s82, 57344\n\t" \
    "s_nop 0\n\t" \
    "global_load_lds_dwordx4 v200, s[42:43]\n\t" \
    "v_add_u32 v200, 131072, v200\n\t" \
    "s_add_u32 m0, s82, 61440\n\t" \
    "s_nop 0\n\t" \
    "global_load_lds_dwordx4 v200, s[42:43]\n\t" \
    "v_add_u32 v200, 131072, v200\n\t" \
    "v_add_u32 v202, 65536, v202\n\t" \
    "v_add_u32 v203, 65536, v203\n\t" \
    "ds_read_b64_tr_b16 a[176:177], v202 offset:0\n\t" \
    "ds_read_b64_tr_b16 a[178:179], v202 offset:1024\n\t" \
    "ds_read_b64_tr_b16 a[180:181], v202 offset:8192\n\t" \
    "ds_read_b64_tr_b16 a[182:183], v202 offset:9216\n\t" \
    "ds_read_b64_tr_b16 a[248:249], v203 offset:0\n\t" \
    "ds_read_b64_tr_b16 a[250:251], v203 offset:1024\n\t" \
    "ds_read_b64_tr_b16 a[252:253], v203 offset:8192\n\t" \
    "ds_read_b64_tr_b16 a[254:255], v203 offset:9216\n\t" \
    "s_waitcnt vmcnt(30) lgkmcnt(0)\n\t" \
    "s_barrier\n\t" \
    "v_subrev_u32 v200, 4718592, v200\n\t" \
    "s_add_u32 m0, s82, 65536\n\t" \
    "s_nop 0\n\t" \
    "global_load_lds_dwordx4 v200, s[44:45]\n\t" \
    "v_add_u32 v200, 131072, v200\n\t" \
    "s_add_u32 m0, s82, 69632\n\t" \
    "s_nop 0\n\t" \
    "global_load_lds_dwordx4 v200, s[44:45]\n\t" \
    "v_add_u32 v200, 131072, v200\n\t" \
    "s_add_u32 m0, s82, 73728\n\t" \
    "s_nop 0\n\t" \
    "global_load_lds_dwordx4 v200, s[44:45]\n\t" \
    "v_add_u32 v200, 131072, v200\n\t" \
    "s_add_u32 m0, s82, 77824\n\t" \
    "s_nop 0\n\t" \
    "global_load_lds_dwordx4 v200, s[44:45]\n\t" \
    "v_add_u32 v200, 131072, v200\n\t" \
    "v_add_u32 v202, -65536, v202\n\t" \
    "v_add_u32 v203, -65536, v203\n\t" \
    "ds_read_b64_tr_b16 a[184:185], v202 offset:0\n\t" \
    "ds_read_b64_tr_b16 a[186:187], v202 offset:1024\n\t" \
    "ds_read_b64_tr_b16 a[188:189], v202 offset:8192\n\t" \
    "ds_read_b64_tr_b16 a[190:191], v202 offset:9216\n\t" \
    "ds_read_b64_tr_b16 v[224:225], v203 offset:0\n\t" \
    "ds_read_b64_tr_b16 v[226:227], v203 offset:1024\n\t" \
    "ds_read_b64_tr_b16 v[228:229], v203 offset:8192\n\t" \
    "ds_read_b64_tr_b16 v[230:231], v203 offset:9216\n\t" \
    "s_waitcnt vmcnt(30) lgkmcnt(0)\n\t" \
    "s_barrier\n\t" \
    "s_add_u32 m0, s82, 0\n\t" \
    "s_nop 0\n\t" \
    "global_load_lds_dwordx4 v200, s[44:45]\n\t" \
    "v_add_u32 v200, 131072, v200\n\t" \
    "s_add_u32 m0, s82, 4096\n\t" \
    "s_nop 0\n\t" \
    "global_load_lds_dwordx4 v200, s[44:45]\n\t" \
    "v_add_u32 v200, 131072, v200\n\t" \
    "s_add_u32 m0, s82, 8192\n\t" \
    "s_nop 0\n\t" \
    "global_load_lds_dwordx4 v200, s[44:45]\n\t" \
    "v_add_u32 v200, 131072, v200\n\t" \
    "s_add_u32 m0, s82, 12288\n\t" \
    "s_nop 0\n\t" \
    "global_load_lds_dwordx4 v200, s[44:45]\n\t" \
    "v_add_u32 v200, 131072, v200\n\t" \
    "ds_read_b64_tr_b16 a[192:193], v202 offset:16384\n\t" \
    "ds_read_b64_tr_b16 a[194:195], v202 offset:17408\n\t" \
    "ds_read_b64_tr_b16 a[196:197], v202 offset:24576\n\t" \
    "ds_read_b64_tr_b16 a[198:199], v202 offset:25600\n\t" \
    "ds_read_b64_tr_b16 v[232:233], v203 offset:16384\n\t" \
    "ds_read_b64_tr_b16 v[234:235], v203 offset:17408\n\t" \
    "ds_read_b64_tr_b16 v[236:237], v203 offset:24576\n\t" \
    "ds_read_b64_tr_b16 v[238:239], v203 offset:25600\n\t" \
    "s_waitcnt vmcnt(21) lgkmcnt(0)\n\t" \
    "s_barrier\n\t" \
    "s_add_u32 m0, s82, 16384\n\t" \
    "s_nop 0\n\t" \
    "global_load_lds_dwordx4 v200, s[44:45]\n\t" \
    "v_add_u32 v200, 131072, v200\n\t" \
    "s_add_u32 m0, s82, 20480\n\t" \
    "s_nop 0\n\t" \
    "global_load_lds_dwordx4 v200, s[44:45]\n\t" \
    "v_add_u32 v200, 131072, v200\n\t" \
    "s_add_u32 m0, s82, 24576\n\t" \
    "s_nop 0\n\t" \
    "global_load_lds_dwordx4 v200, s[44:45]\n\t" \
    "v_add_u32 v200, 131072, v200\n\t" \
    "s_add_u32 m0, s82, 28672\n\t" \
    "s_nop 0\n\t" \
    "global_load_lds_dwordx4 v200, s[44:45]\n\t" \
    "v_add_u32 v200, 131072, v200\n\t" \
    "v_mbcnt_lo_u32_b32 v164, -1, 0\n\t" \
    "v_mbcnt_hi_u32_b32 v164, -1, v164\n\t" \
    "s_cmp_eq_u32 s66, 0\n\t" \
    "s_cselect_b32 s69, 59, 11\n\t" \
    "v_min_u32 v164, s69, v164\n\t" \
    "v_lshlrev_b32 v164, 6, v164\n\t" \
    "v_add_u32 v164, 6258688, v164\n\t" \
    "v_mov_b32 v165, 6266880\n\t" \
    "global_load_dword v165, v165, s[36:37] sc1\n\t" \
    "global_load_dwordx2 v[166:167], v164, s[36:37] sc1\n\t" \
    "ds_read_b64_tr_b16 a[200:201], v202 offset:32768\n\t" \
    "ds_read_b64_tr_b16 a[202:203], v202 offset:33792\n\t" \
    "ds_read_b64_tr_b16 a[204:205], v202 offset:40960\n\t" \
    "ds_read_b64_tr_b16 a[206:207], v202 offset:41984\n\t" \
    "ds_read_b64_tr_b16 v[240:241], v203 offset:32768\n\t" \
    "ds_read_b64_tr_b16 v[242:243], v203 offset:33792\n\t" \
    "ds_read_b64_tr_b16 v[244:245], v203 offset:40960\n\t" \
    "ds_read_b64_tr_b16 v[246:247], v203 offset:41984\n\t" \
    "s_waitcnt vmcnt(14) lgkmcnt(0)\n\t" \
    "s_barrier\n\t" \
    "s_add_u32 m0, s82, 32768\n\t" \
    "s_nop 0\n\t" \
    "global_load_lds_dwordx4 v200, s[44:45]\n\t" \
    "v_add_u32 v200, 131072, v200\n\t" \
    "s_add_u32 m0, s82, 36864\n\t" \
    "s_nop 0\n\t" \
    "global_load_lds_dwordx4 v200, s[44:45]\n\t" \
    "v_add_u32 v200, 131072, v200\n\t" \
    "s_add_u32 m0, s82, 40960\n\t" \
    "s_nop 0\n\t" \
    "global_load_lds_dwordx4 v200, s[44:45]\n\t" \
    "v_add_u32 v200, 131072, v200\n\t" \
    "s_add_u32 m0, s82, 45056\n\t" \
    "s_nop 0\n\t" \
    "global_load_lds_dwordx4 v200, s[44:45]\n\t" \
    "v_add_u32 v200, 131072, v200\n\t" \
    "ds_read_b64_tr_b16 a[208:209], v202 offset:49152\n\t" \
    "ds_read_b64_tr_b16 a[210:211], v202 offset:50176\n\t" \
    "ds_read_b64_tr_b16 a[212:213], v202 offset:57344\n\t" \
    "ds_read_b64_tr_b16 a[214:215], v202 offset:58368\n\t" \
    "ds_read_b64_tr_b16 v[248:249], v203 offset:49152\n\t" \
    "ds_read_b64_tr_b16 v[250:251], v203 offset:50176\n\t" \
    "ds_read_b64_tr_b16 v[252:253], v203 offset:57344\n\t" \
    "ds_read_b64_tr_b16 v[254:255], v203 offset:58368\n\t" \
    "s_waitcnt lgkmcnt(0)\n\t" \
    "s_waitcnt lgkmcnt(6)\n\t" \
    "v_add_u32 v2, 36864, v3\n\t" \
    "ds_read_b128 v[24:27], v2 offset:0\n\t" \
    "ds_read_b128 v[28:31], v2 offset:18432\n\t" \
    "ds_read_b128 v[32:35], v2 offset:1024\n\t" \
    "ds_read_b128 v[36:39], v2 offset:19456\n\t" \
    "s_waitcnt lgkmcnt(3)\n\t" \
    "v_mfma_f32_16x16x32_bf16 v[88:91], a[144:147], v[24:27], 0\n\t" \
    "ds_read_b128 v[40:43], v2 offset:2048\n\t" \
    "s_waitcnt lgkmcnt(3)\n\t" \
    "v_mfma_f32_16x16x32_bf16 v[92:95], a[144:147], v[28:31], 0\n\t" \
    "ds_read_b128 v[44:47], v2 offset:20480\n\t" \
    "v_mfma_f32_16x16x32_bf16 v[96:99], a[216:219], v[24:27], 0\n\t" \
    "v_mfma_f32_16x16x32_bf16 v[100:103], a[216:219], v[28:31], 0\n\t" \
    "s_waitcnt lgkmcnt(3)\n\t" \
    "v_mfma_f32_16x16x32_bf16 v[88:91], a[148:151], v[32:35], v[88:91]\n\t" \
    "ds_read_b128 v[48:51], v2 offset:3072\n\t" \
    "s_waitcnt lgkmcnt(3)\n\t" \
    "v_mfma_f32_16x16x32_bf16 v[92:95], a[148:151], v[36:39], v[92:95]\n\t" \
    "ds_read_b128 v[52:55], v2 offset:21504\n\t" \
    "v_mfma_f32_16x16x32_bf16 v[96:99], a[220:223], v[32:35], v[96:99]\n\t" \
    "v_mfma_f32_16x16x32_bf16 v[100:103], a[220:223], v[36:39], v[100:103]\n\t" \
    "s_waitcnt lgkmcnt(3)\n\t" \
    "v_mfma_f32_16x16x32_bf16 v[88:91], a[152:155], v[40:43], v[88:91]\n\t" \
    "ds_read_b128 v[56:59], v2 offset:4096\n\t" \
    "s_waitcnt lgkmcnt(3)\n\t" \
    "v_mfma_f32_16x16x32_bf16 v[92:95], a[152:155], v[44:47], v[92:95]\n\t" \
    "ds_read_b128 v[60:63], v2 offset:22528\n\t" \
    "v_mfma_f32_16x16x32_bf16 v[96:99], a[224:227], v[40:43], v[96:99]\n\t" \
    "v_mfma_f32_16x16x32_bf16 v[100:103], a[224:227], v[44:47], v[100:103]\n\t" \
    "s_waitcnt lgkmcnt(3)\n\t" \
    "v_mfma_f32_16x16x32_bf16 v[88:91], a[156:159], v[48:51], v[88:91]\n\t" \
    "ds_read_b128 v[64:67], v2 offset:5120\n\t" \
    "s_waitcnt lgkmcnt(3)\n\t" \
    "v_mfma_f32_16x16x32_bf16 v[92:95], a[156:159], v[52:55], v[92:95]\n\t" \
    "ds_read_b128 v[68:71], v2 offset:23552\n\t" \
    "v_mfma_f32_16x16x32_bf16 v[96:99], a[228:231], v[48:51], v[96:99]\n\t" \
    "v_mfma_f32_16x16x32_bf16 v[100:103], a[228:231], v[52:55], v[100:103]\n\t" \
    "s_waitcnt lgkmcnt(3)\n\t" \
    "v_mfma_f32_16x16x32_bf16 v[88:91], a[160:163], v[56:59], v[88:91]\n\t" \
    "ds_read_b128 v[24:27], v2 offset:6144\n\t" \
    "s_waitcnt lgkmcnt(3)\n\t" \
    "v_mfma_f32_16x16x32_bf16 v[92:95], a[160:163], v[60:63], v[92:95]\n\t" \
    "ds_read_b128 v[28:31], v2 offset:24576\n\t" \
    "v_mfma_f32_16x16x32_bf16 v[96:99], a[232:235], v[56:59], v[96:99]\n\t" \
    "v_mfma_f32_16x16x32_bf16 v[100:103], a[232:235], v[60:63], v[100:103]\n\t" \
    "s_waitcnt lgkmcnt(3)\n\t" \
    "v_mfma_f32_16x16x32_bf16 v[88:91], a[164:167], v[64:67], v[88:91]\n\t" \
    "ds_read_b128 v[32:35], v2 offset:7168\n\t" \
    "s_waitcnt lgkmcnt(3)\n\t" \
    "v_mfma_f32_16x16x32_bf16 v[92:95], a[164:167], v[68:71], v[92:95]\n\t" \
    "ds_read_b128 v[36:39], v2 offset:25600\n\t" \
    "v_mfma_f32_16x16x32_bf16 v[96:99], a[236:239], v[64:67], v[96:99]\n\t" \
    "v_mfma_f32_16x16x32_bf16 v[100:103], a[236:239], v[68:71], v[100:103]\n\t" \
    "s_waitcnt lgkmcnt(3)\n\t" \
    "v_mfma_f32_16x16x32_bf16 v[88:91], a[168:171], v[24:27], v[88:91]\n\t" \
    "ds_read_b128 v[40:43], v2 offset:8192\n\t" \
    "s_waitcnt lgkmcnt(3)\n\t" \
    "v_mfma_f32_16x16x32_bf16 v[92:95], a[168:171], v[28:31], v[92:95]\n\t" \
    "ds_read_b128 v[44:47], v2 offset:26624\n\t" \
    "v_mfma_f32_16x16x32_bf16 v[96:99], a[240:243], v[24:27], v[96:99]\n\t" \
    "v_mfma_f32_16x16x32_bf16 v[100:103], a[240:243], v[28:31], v[100:103]\n\t" \
    "s_waitcnt lgkmcnt(3)\n\t" \
    "v_mfma_f32_16x16x32_bf16 v[88:91], a[172:175], v[32:35], v[88:91]\n\t" \
    "ds_read_b128 v[48:51], v2 offset:9216\n\t" \
    "s_waitcnt lgkmcnt(3)\n\t" \
    "v_mfma_f32_16x16x32_bf16 v[92:95], a[172:175], v[36:39], v[92:95]\n\t" \
    "ds_read_b128 v[52:55], v2 offset:27648\n\t" \
    "v_mfma_f32_16x16x32_bf16 v[96:99], a[244:247], v[32:35], v[96:99]\n\t" \
    "v_mfma_f32_16x16x32_bf16 v[100:103], a[244:247], v[36:39], v[100:103]\n\t" \
    "s_waitcnt lgkmcnt(3)\n\t" \
    "v_mfma_f32_16x16x32_bf16 v[88:91], a[176:179], v[40:43], v[88:91]\n\t" \
    "ds_read_b128 v[56:59], v2 offset:10240\n\t" \
    "s_waitcnt lgkmcnt(3)\n\t" \
    "v_mfma_f32_16x16x32_bf16 v[92:95], a[176:179], v[44:47], v[92:95]\n\t" \
    "ds_read_b128 v[60:63], v2 offset:28672\n\t" \
    "v_mfma_f32_16x16x32_bf16 v[96:99], a[248:251], v[40:43], v[96:99]\n\t" \
    "v_mfma_f32_16x16x32_bf16 v[100:103], a[248:251], v[44:47], v[100:103]\n\t" \
    "s_waitcnt lgkmcnt(3)\n\t" \
    "v_mfma_f32_16x16x32_bf16 v[88:91], a[180:183], v[48:51], v[88:91]\n\t" \
    "ds_read_b128 v[64:67], v2 offset:11264\n\t" \
    "s_waitcnt lgkmcnt(3)\n\t" \
    "v_mfma_f32_16x16x32_bf16 v[92:95], a[180:183], v[52:55], v[92:95]\n\t" \
    "ds_read_b128 v[68:71], v2 offset:29696\n\t" \
    "v_mfma_f32_16x16x32_bf16 v[96:99], a[252:255], v[48:51], v[96:99]\n\t" \
    "v_mfma_f32_16x16x32_bf16 v[100:103], a[252:255], v[52:55], v[100:103]\n\t" \
    "s_waitcnt lgkmcnt(3)\n\t" \
    "v_mfma_f32_16x16x32_bf16 v[88:91], a[184:187], v[56:59], v[88:91]\n\t" \
    "ds_read_b128 v[24:27], v2 offset:12288\n\t" \
    "s_waitcnt lgkmcnt(3)\n\t" \
    "v_mfma_f32_16x16x32_bf16 v[92:95], a[184:187], v[60:63], v[92:95]\n\t" \
    "ds_read_b128 v[28:31], v2 offset:30720\n\t" \
    "v_mfma_f32_16x16x32_bf16 v[96:99], v[224:227], v[56:59], v[96:99]\n\t" \
    "v_mfma_f32_16x16x32_bf16 v[100:103], v[224:227], v[60:63], v[100:103]\n\t" \
    "s_waitcnt lgkmcnt(3)\n\t" \
    "v_mfma_f32_16x16x32_bf16 v[88:91], a[188:191], v[64:67], v[88:91]\n\t" \
    "ds_read_b128 v[32:35], v2 offset:13312\n\t" \
    "s_waitcnt lgkmcnt(3)\n\t" \
    "v_mfma_f32_16x16x32_bf16 v[92:95], a[188:191], v[68:71], v[92:95]\n\t" \
    "ds_read_b128 v[36:39], v2 offset:31744\n\t" \
    "v_mfma_f32_16x16x32_bf16 v[96:99], v[228:231], v[64:67], v[96:99]\n\t" \
    "v_mfma_f32_16x16x32_bf16 v[100:103], v[228:231], v[68:71], v[100:103]\n\t" \
    "s_waitcnt lgkmcnt(3)\n\t" \
    "v_mfma_f32_16x16x32_bf16 v[88:91], a[192:195], v[24:27], v[88:91]\n\t" \
    "ds_read_b128 v[40:43], v2 offset:14336\n\t" \
    "s_waitcnt lgkmcnt(3)\n\t" \
    "v_mfma_f32_16x16x32_bf16 v[92:95], a[192:195], v[28:31], v[92:95]\n\t" \
    "ds_read_b128 v[44:47], v2 offset:32768\n\t" \
    "v_mfma_f32_16x16x32_bf16 v[96:99], v[232:235], v[24:27], v[96:99]\n\t" \
    "v_mfma_f32_16x16x32_bf16 v[100:103], v[232:235], v[28:31], v[100:103]\n\t" \
    "s_waitcnt lgkmcnt(3)\n\t" \
    "v_mfma_f32_16x16x32_bf16 v[88:91], a[196:199], v[32:35], v[88:91]\n\t" \
    "ds_read_b128 v[48:51], v2 offset:15360\n\t" \
    "s_waitcnt lgkmcnt(3)\n\t" \
    "v_mfma_f32_16x16x32_bf16 v[92:95], a[196:199], v[36:39], v[92:95]\n\t" \
    "ds_read_b128 v[52:55], v2 offset:33792\n\t" \
    "v_mfma_f32_16x16x32_bf16 v[96:99], v[236:239], v[32:35], v[96:99]\n\t" \
    "v_mfma_f32_16x16x32_bf16 v[100:103], v[236:239], v[36:39], v[100:103]\n\t" \
    "s_waitcnt lgkmcnt(3)\n\t" \
    "v_mfma_f32_16x16x32_bf16 v[88:91], a[200:203], v[40:43], v[88:91]\n\t" \
    "ds_read_b128 v[56:59], v2 offset:16384\n\t" \
    "s_waitcnt lgkmcnt(3)\n\t" \
    "v_mfma_f32_16x16x32_bf16 v[92:95], a[200:203], v[44:47], v[92:95]\n\t" \
    "ds_read_b128 v[60:63], v2 offset:34816\n\t" \
    "v_mfma_f32_16x16x32_bf16 v[96:99], v[240:243], v[40:43], v[96:99]\n\t" \
    "v_mfma_f32_16x16x32_bf16 v[100:103], v[240:243], v[44:47], v[100:103]\n\t" \
    "s_waitcnt lgkmcnt(3)\n\t" \
    "v_mfma_f32_16x16x32_bf16 v[88:91], a[204:207], v[48:51], v[88:91]\n\t" \
    "ds_read_b128 v[64:67], v2 offset:17408\n\t" \
    "s_waitcnt lgkmcnt(3)\n\t" \
    "v_mfma_f32_16x16x32_bf16 v[92:95], a[204:207], v[52:55], v[92:95]\n\t" \
    "ds_read_b128 v[68:71], v2 offset:35840\n\t" \
    "v_mfma_f32_16x16x32_bf16 v[96:99], v[244:247], v[48:51], v[96:99]\n\t" \
    "v_mfma_f32_16x16x32_bf16 v[100:103], v[244:247], v[52:55], v[100:103]\n\t" \
    "s_waitcnt lgkmcnt(3)\n\t" \
    "v_mfma_f32_16x16x32_bf16 v[88:91], a[208:211], v[56:59], v[88:91]\n\t" \
    "s_waitcnt lgkmcnt(2)\n\t" \
    "v_mfma_f32_16x16x32_bf16 v[92:95], a[208:211], v[60:63], v[92:95]\n\t" \
    "v_mfma_f32_16x16x32_bf16 v[96:99], v[248:251], v[56:59], v[96:99]\n\t" \
    "v_mfma_f32_16x16x32_bf16 v[100:103], v[248:251], v[60:63], v[100:103]\n\t" \
    "s_waitcnt lgkmcnt(1)\n\t" \
    "v_mfma_f32_16x16x32_bf16 v[88:91], a[212:215], v[64:67], v[88:91]\n\t" \
    "s_waitcnt lgkmcnt(0)\n\t" \
    "v_mfma_f32_16x16x32_bf16 v[92:95], a[212:215], v[68:71], v[92:95]\n\t" \
    "v_mfma_f32_16x16x32_bf16 v[96:99], v[252:255], v[64:67], v[96:99]\n\t" \
    "v_mfma_f32_16x16x32_bf16 v[100:103], v[252:255], v[68:71], v[100:103]\n\t" \
    "v_xor_b32 v9, 8192, v8\n\t" \
    "s_nop 2\n\t" \
    "v_cvt_pk_bf16_f32 v112, v88, v89\n\t" \
    "v_cvt_pk_bf16_f32 v113, v90, v91\n\t" \
    "s_nop 0\n\t" \
    "v_cvt_pk_bf16_f32 v114, v96, v97\n\t" \
    "v_cvt_pk_bf16_f32 v115, v98, v99\n\t" \
    "global_store_dwordx4 v9, v[112:115], s[40:41] offset:0 sc1\n\t" \
    "v_cvt_pk_bf16_f32 v116, v92, v93\n\t" \
    "v_cvt_pk_bf16_f32 v117, v94, v95\n\t" \
    "v_cvt_pk_bf16_f32 v118, v100, v101\n\t" \
    "v_cvt_pk_bf16_f32 v119, v102, v103\n\t" \
    "global_store_dwordx4 v9, v[116:119], s[40:41] offset:1024 sc1\n\t" \
    "s_waitcnt lgkmcnt(6)\n\t" \
    "v_add_u32 v2, 0, v3\n\t" \
    "ds_read_b128 v[24:27], v2 offset:0\n\t" \
    "ds_read_b128 v[28:31], v2 offset:18432\n\t" \
    "ds_read_b128 v[32:35], v2 offset:1024\n\t" \
    "ds_read_b128 v[36:39], v2 offset:19456\n\t" \
    "s_waitcnt lgkmcnt(3)\n\t" \
    "v_mfma_f32_16x16x32_bf16 v[88:91], a[144:147], v[24:27], 0\n\t" \
    "ds_read_b128 v[40:43], v2 offset:2048\n\t" \
    "s_waitcnt lgkmcnt(3)\n\t" \
    "v_mfma_f32_16x16x32_bf16 v[92:95], a[144:147], v[28:31], 0\n\t" \
    "ds_read_b128 v[44:47], v2 offset:20480\n\t" \
    "v_mfma_f32_16x16x32_bf16 v[96:99], a[216:219], v[24:27], 0\n\t" \
    "v_mfma_f32_16x16x32_bf16 v[100:103], a[216:219], v[28:31], 0\n\t" \
    "s_waitcnt lgkmcnt(3)\n\t" \
    "v_mfma_f32_16x16x32_bf16 v[88:91], a[148:151], v[32:35], v[88:91]\n\t" \
    "ds_read_b128 v[48:51], v2 offset:3072\n\t" \
    "s_waitcnt lgkmcnt(3)\n\t" \
    "v_mfma_f32_16x16x32_bf16 v[92:95], a[148:151], v[36:39], v[92:95]\n\t" \
    "ds_read_b128 v[52:55], v2 offset:21504\n\t" \
    "v_mfma_f32_16x16x32_bf16 v[96:99], a[220:223], v[32:35], v[96:99]\n\t" \
    "v_mfma_f32_16x16x32_bf16 v[100:103], a[220:223], v[36:39], v[100:103]\n\t" \
    "s_waitcnt lgkmcnt(3)\n\t" \
    "v_mfma_f32_16x16x32_bf16 v[88:91], a[152:155], v[40:43], v[88:91]\n\t" \
    "ds_read_b128 v[56:59], v2 offset:4096\n\t" \
    "s_waitcnt lgkmcnt(3)\n\t" \
    "v_mfma_f32_16x16x32_bf16 v[92:95], a[152:155], v[44:47], v[92:95]\n\t" \
    "ds_read_b128 v[60:63], v2 offset:22528\n\t" \
    "v_mfma_f32_16x16x32_bf16 v[96:99], a[224:227], v[40:43], v[96:99]\n\t" \
    "v_mfma_f32_16x16x32_bf16 v[100:103], a[224:227], v[44:47], v[100:103]\n\t" \
    "s_waitcnt lgkmcnt(3)\n\t" \
    "v_mfma_f32_16x16x32_bf16 v[88:91], a[156:159], v[48:51], v[88:91]\n\t" \
    "ds_read_b128 v[64:67], v2 offset:5120\n\t" \
    "s_waitcnt lgkmcnt(3)\n\t" \
    "v_mfma_f32_16x16x32_bf16 v[92:95], a[156:159], v[52:55], v[92:95]\n\t" \
    "ds_read_b128 v[68:71], v2 offset:23552\n\t" \
    "v_mfma_f32_16x16x32_bf16 v[96:99], a[228:231], v[48:51], v[96:99]\n\t" \
    "v_mfma_f32_16x16x32_bf16 v[100:103], a[228:231], v[52:55], v[100:103]\n\t" \
    "s_waitcnt lgkmcnt(3)\n\t" \
    "v_mfma_f32_16x16x32_bf16 v[88:91], a[160:163], v[56:59], v[88:91]\n\t" \
    "ds_read_b128 v[24:27], v2 offset:6144\n\t" \
    "s_waitcnt lgkmcnt(3)\n\t" \
    "v_mfma_f32_16x16x32_bf16 v[92:95], a[160:163], v[60:63], v[92:95]\n\t" \
    "ds_read_b128 v[28:31], v2 offset:24576\n\t" \
    "v_mfma_f32_16x16x32_bf16 v[96:99], a[232:235], v[56:59], v[96:99]\n\t" \
    "v_mfma_f32_16x16x32_bf16 v[100:103], a[232:235], v[60:63], v[100:103]\n\t" \
    "s_waitcnt lgkmcnt(3)\n\t" \
    "v_mfma_f32_16x16x32_bf16 v[88:91], a[164:167], v[64:67], v[88:91]\n\t" \
    "ds_read_b128 v[32:35], v2 offset:7168\n\t" \
    "s_waitcnt lgkmcnt(3)\n\t" \
    "v_mfma_f32_16x16x32_bf16 v[92:95], a[164:167], v[68:71], v[92:95]\n\t" \
    "ds_read_b128 v[36:39], v2 offset:25600\n\t" \
    "v_mfma_f32_16x16x32_bf16 v[96:99], a[236:239], v[64:67], v[96:99]\n\t" \
    "v_mfma_f32_16x16x32_bf16 v[100:103], a[236:239], v[68:71], v[100:103]\n\t" \
    "s_waitcnt lgkmcnt(3)\n\t" \
    "v_mfma_f32_16x16x32_bf16 v[88:91], a[168:171], v[24:27], v[88:91]\n\t" \
    "ds_read_b128 v[40:43], v2 offset:8192\n\t" \
    "s_waitcnt lgkmcnt(3)\n\t" \
    "v_mfma_f32_16x16x32_bf16 v[92:95], a[168:171], v[28:31], v[92:95]\n\t" \
    "ds_read_b128 v[44:47], v2 offset:26624\n\t" \
    "v_mfma_f32_16x16x32_bf16 v[96:99], a[240:243], v[24:27], v[96:99]\n\t" \
    "v_mfma_f32_16x16x32_bf16 v[100:103], a[240:243], v[28:31], v[100:103]\n\t" \
    "s_waitcnt lgkmcnt(3)\n\t" \
    "v_mfma_f32_16x16x32_bf16 v[88:91], a[172:175], v[32:35], v[88:91]\n\t" \
    "ds_read_b128 v[48:51], v2 offset:9216\n\t" \
    "s_waitcnt lgkmcnt(3)\n\t" \
    "v_mfma_f32_16x16x32_bf16 v[92:95], a[172:175], v[36:39], v[92:95]\n\t" \
    "ds_read_b128 v[52:55], v2 offset:27648\n\t" \
    "v_mfma_f32_16x16x32_bf16 v[96:99], a[244:247], v[32:35], v[96:99]\n\t" \
    "v_mfma_f32_16x16x32_bf16 v[100:103], a[244:247], v[36:39], v[100:103]\n\t" \
    "s_waitcnt lgkmcnt(3)\n\t" \
    "v_mfma_f32_16x16x32_bf16 v[88:91], a[176:179], v[40:43], v[88:91]\n\t" \
    "ds_read_b128 v[56:59], v2 offset:10240\n\t" \
    "s_waitcnt lgkmcnt(3)\n\t" \
    "v_mfma_f32_16x16x32_bf16 v[92:95], a[176:179], v[44:47], v[92:95]\n\t" \
    "ds_read_b128 v[60:63], v2 offset:28672\n\t" \
    "v_mfma_f32_16x16x32_bf16 v[96:99], a[248:251], v[40:43], v[96:99]\n\t" \
    "v_mfma_f32_16x16x32_bf16 v[100:103], a[248:251], v[44:47], v[100:103]\n\t" \
    "s_waitcnt lgkmcnt(3)\n\t" \
    "v_mfma_f32_16x16x32_bf16 v[88:91], a[180:183], v[48:51], v[88:91]\n\t" \
    "ds_read_b128 v[64:67], v2 offset:11264\n\t" \
    "s_waitcnt lgkmcnt(3)\n\t" \
    "v_mfma_f32_16x16x32_bf16 v[92:95], a[180:183], v[52:55], v[92:95]\n\t" \
    "ds_read_b128 v[68:71], v2 offset:29696\n\t" \
    "v_mfma_f32_16x16x32_bf16 v[96:99], a[252:255], v[48:51], v[96:99]\n\t" \
    "v_mfma_f32_16x16x32_bf16 v[100:103], a[252:255], v[52:55], v[100:103]\n\t" \
    "s_waitcnt lgkmcnt(3)\n\t" \
    "v_mfma_f32_16x16x32_bf16 v[88:91], a[184:187], v[56:59], v[88:91]\n\t" \
    "ds_read_b128 v[24:27], v2 offset:12288\n\t" \
    "s_waitcnt lgkmcnt(3)\n\t" \
    "v_mfma_f32_16x16x32_bf16 v[92:95], a[184:187], v[60:63], v[92:95]\n\t" \
    "ds_read_b128 v[28:31], v2 offset:30720\n\t" \
    "v_mfma_f32_16x16x32_bf16 v[96:99], v[224:227], v[56:59], v[96:99]\n\t" \
    "v_mfma_f32_16x16x32_bf16 v[100:103], v[224:227], v[60:63], v[100:103]\n\t" \
    "s_waitcnt lgkmcnt(3)\n\t" \
    "v_mfma_f32_16x16x32_bf16 v[88:91], a[188:191], v[64:67], v[88:91]\n\t" \
    "ds_read_b128 v[32:35], v2 offset:13312\n\t" \
    "s_waitcnt lgkmcnt(3)\n\t" \
    "v_mfma_f32_16x16x32_bf16 v[92:95], a[188:191], v[68:71], v[92:95]\n\t" \
    "ds_read_b128 v[36:39], v2 offset:31744\n\t" \
    "v_mfma_f32_16x16x32_bf16 v[96:99], v[228:231], v[64:67], v[96:99]\n\t" \
    "v_mfma_f32_16x16x32_bf16 v[100:103], v[228:231], v[68:71], v[100:103]\n\t" \
    "s_waitcnt lgkmcnt(3)\n\t" \
    "v_mfma_f32_16x16x32_bf16 v[88:91], a[192:195], v[24:27], v[88:91]\n\t" \
    "ds_read_b128 v[40:43], v2 offset:14336\n\t" \
    "s_waitcnt lgkmcnt(3)\n\t" \
    "v_mfma_f32_16x16x32_bf16 v[92:95], a[192:195], v[28:31], v[92:95]\n\t" \
    "ds_read_b128 v[44:47], v2 offset:32768\n\t" \
    "v_mfma_f32_16x16x32_bf16 v[96:99], v[232:235], v[24:27], v[96:99]\n\t" \
    "v_mfma_f32_16x16x32_bf16 v[100:103], v[232:235], v[28:31], v[100:103]\n\t" \
    "s_waitcnt lgkmcnt(3)\n\t" \
    "v_mfma_f32_16x16x32_bf16 v[88:91], a[196:199], v[32:35], v[88:91]\n\t" \
    "ds_read_b128 v[48:51], v2 offset:15360\n\t" \
    "s_waitcnt lgkmcnt(3)\n\t" \
    "v_mfma_f32_16x16x32_bf16 v[92:95], a[196:199], v[36:39], v[92:95]\n\t" \
    "ds_read_b128 v[52:55], v2 offset:33792\n\t" \
    "v_mfma_f32_16x16x32_bf16 v[96:99], v[236:239], v[32:35], v[96:99]\n\t" \
    "v_mfma_f32_16x16x32_bf16 v[100:103], v[236:239], v[36:39], v[100:103]\n\t" \
    "s_waitcnt lgkmcnt(3)\n\t" \
    "v_mfma_f32_16x16x32_bf16 v[88:91], a[200:203], v[40:43], v[88:91]\n\t" \
    "ds_read_b128 v[56:59], v2 offset:16384\n\t" \
    "s_waitcnt lgkmcnt(3)\n\t" \
    "v_mfma_f32_16x16x32_bf16 v[92:95], a[200:203], v[44:47], v[92:95]\n\t" \
    "ds_read_b128 v[60:63], v2 offset:34816\n\t" \
    "v_mfma_f32_16x16x32_bf16 v[96:99], v[240:243], v[40:43], v[96:99]\n\t" \
    "v_mfma_f32_16x16x32_bf16 v[100:103], v[240:243], v[44:47], v[100:103]\n\t" \
    "s_waitcnt lgkmcnt(3)\n\t" \
    "v_mfma_f32_16x16x32_bf16 v[88:91], a[204:207], v[48:51], v[88:91]\n\t" \
    "ds_read_b128 v[64:67], v2 offset:17408\n\t" \
    "s_waitcnt lgkmcnt(3)\n\t" \
    "v_mfma_f32_16x16x32_bf16 v[92:95], a[204:207], v[52:55], v[92:95]\n\t" \
    "ds_read_b128 v[68:71], v2 offset:35840\n\t" \
    "v_mfma_f32_16x16x32_bf16 v[96:99], v[244:247], v[48:51], v[96:99]\n\t" \
    "v_mfma_f32_16x16x32_bf16 v[100:103], v[244:247], v[52:55], v[100:103]\n\t" \
    "s_waitcnt lgkmcnt(3)\n\t" \
    "v_mfma_f32_16x16x32_bf16 v[88:91], a[208:211], v[56:59], v[88:91]\n\t" \
    "s_waitcnt lgkmcnt(2)\n\t" \
    "v_mfma_f32_16x16x32_bf16 v[92:95], a[208:211], v[60:63], v[92:95]\n\t" \
    "v_mfma_f32_16x16x32_bf16 v[96:99], v[248:251], v[56:59], v[96:99]\n\t" \
    "v_mfma_f32_16x16x32_bf16 v[100:103], v[248:251], v[60:63], v[100:103]\n\t" \
    "s_waitcnt lgkmcnt(1)\n\t" \
    "v_mfma_f32_16x16x32_bf16 v[88:91], a[212:215], v[64:67], v[88:91]\n\t" \
    "s_waitcnt lgkmcnt(0)\n\t" \
    "v_mfma_f32_16x16x32_bf16 v[92:95], a[212:215], v[68:71], v[92:95]\n\t" \
    "v_mfma_f32_16x16x32_bf16 v[96:99], v[252:255], v[64:67], v[96:99]\n\t" \
    "v_mfma_f32_16x16x32_bf16 v[100:103], v[252:255], v[68:71], v[100:103]\n\t" \
    "v_xor_b32 v9, 0, v8\n\t" \
    "s_nop 2\n\t" \
    "v_cvt_pk_bf16_f32 v112, v88, v89\n\t" \
    "v_cvt_pk_bf16_f32 v113, v90, v91\n\t" \
    "s_nop 0\n\t" \
    "v_cvt_pk_bf16_f32 v114, v96, v97\n\t" \
    "v_cvt_pk_bf16_f32 v115, v98, v99\n\t" \
    "global_store_dwordx4 v9, v[112:115], s[40:41] offset:0 sc1\n\t" \
    "v_cvt_pk_bf16_f32 v116, v92, v93\n\t" \
    "v_cvt_pk_bf16_f32 v117, v94, v95\n\t" \
    "v_cvt_pk_bf16_f32 v118, v100, v101\n\t" \
    "v_cvt_pk_bf16_f32 v119, v102, v103\n\t" \
    "global_store_dwordx4 v9, v[116:119], s[40:41] offset:1024 sc1\n\t" \
    "s_waitcnt vmcnt(18) lgkmcnt(0)\n\t" \
    "s_barrier\n\t" \
    "s_add_u32 m0, s82, 49152\n\t" \
    "s_nop 0\n\t" \
    "global_load_lds_dwordx4 v200, s[44:45]\n\t" \
    "v_add_u32 v200, 131072, v200\n\t" \
    "s_add_u32 m0, s82, 53248\n\t" \
    "s_nop 0\n\t" \
    "global_load_lds_dwordx4 v200, s[44:45]\n\t" \
    "v_add_u32 v200, 131072, v200\n\t" \
    "s_add_u32 m0, s82, 57344\n\t" \
    "s_nop 0\n\t" \
    "global_load_lds_dwordx4 v200, s[44:45]\n\t" \
    "v_add_u32 v200, 131072, v200\n\t" \
    "s_add_u32 m0, s82, 61440\n\t" \
    "s_nop 0\n\t" \
    "global_load_lds_dwordx4 v200, s[44:45]\n\t" \
    "v_add_u32 v200, 131072, v200\n\t" \
    "v_add_u32 v202, 65536, v202\n\t" \
    "v_add_u32 v203, 65536, v203\n\t" \
    "ds_read_b64_tr_b16 a[0:1], v202 offset:0\n\t" \
    "ds_read_b64_tr_b16 a[2:3], v202 offset:1024\n\t" \
    "ds_read_b64_tr_b16 a[4:5], v202 offset:8192\n\t" \
    "ds_read_b64_tr_b16 a[6:7], v202 offset:9216\n\t" \
    "ds_read_b64_tr_b16 a[72:73], v203 offset:0\n\t" \
    "ds_read_b64_tr_b16 a[74:75], v203 offset:1024\n\t" \
    "ds_read_b64_tr_b16 a[76:77], v203 offset:8192\n\t" \
    "ds_read_b64_tr_b16 a[78:79], v203 offset:9216\n\t" \
    "s_waitcnt vmcnt(18) lgkmcnt(0)\n\t" \
    "s_barrier\n\t" \
    "s_add_u32 m0, s82, 65536\n\t" \
    "s_nop 0\n\t" \
    "global_load_lds_dwordx4 v200, s[44:45]\n\t" \
    "v_add_u32 v200, 131072, v200\n\t" \
    "s_add_u32 m0, s82, 69632\n\t" \
    "s_nop 0\n\t" \
    "global_load_lds_dwordx4 v200, s[44:45]\n\t" \
    "v_add_u32 v200, 131072, v200\n\t" \
    "s_add_u32 m0, s82, 73728\n\t" \
    "s_nop 0\n\t" \
    "global_load_lds_dwordx4 v200, s[44:45]\n\t" \
    "v_add_u32 v200, 131072, v200\n\t" \
    "s_add_u32 m0, s82, 77824\n\t" \
    "s_nop 0\n\t" \
    "global_load_lds_dwordx4 v200, s[44:45]\n\t" \
    "v_add_u32 v200, 131072, v200\n\t" \
    "v_add_u32 v202, -65536, v202\n\t" \
    "v_add_u32 v203, -65536, v203\n\t" \
    "ds_read_b64_tr_b16 a[8:9], v202 offset:0\n\t" \
    "ds_read_b64_tr_b16 a[10:11], v202 offset:1024\n\t" \
    "ds_read_b64_tr_b16 a[12:13], v202 offset:8192\n\t" \
    "ds_read_b64_tr_b16 a[14:15], v202 offset:9216\n\t" \
    "ds_read_b64_tr_b16 a[80:81], v203 offset:0\n\t" \
    "ds_read_b64_tr_b16 a[82:83], v203 offset:1024\n\t" \
    "ds_read_b64_tr_b16 a[84:85], v203 offset:8192\n\t" \
    "ds_read_b64_tr_b16 a[86:87], v203 offset:9216\n\t" \
    "s_waitcnt vmcnt(18) lgkmcnt(0)\n\t" \
    "s_barrier\n\t" \
    "s_add_u32 m0, s82, 0\n\t" \
    "s_nop 0\n\t" \
    "global_load_lds_dwordx4 v200, s[44:45]\n\t" \
    "v_add_u32 v200, 131072, v200\n\t" \
    "s_add_u32 m0, s82, 4096\n\t" \
    "s_nop 0\n\t" \
    "global_load_lds_dwordx4 v200, s[44:45]\n\t" \
    "v_add_u32 v200, 131072, v200\n\t" \
    "s_add_u32 m0, s82, 8192\n\t" \
    "s_nop 0\n\t" \
    "global_load_lds_dwordx4 v200, s[44:45]\n\t" \
    "v_add_u32 v200, 131072, v200\n\t" \
    "s_add_u32 m0, s82, 12288\n\t" \
    "s_nop 0\n\t" \
    "global_load_lds_dwordx4 v200, s[44:45]\n\t" \
    "v_add_u32 v200, 131072, v200\n\t" \
    "ds_read_b64_tr_b16 a[16:17], v202 offset:16384\n\t" \
    "ds_read_b64_tr_b16 a[18:19], v202 offset:17408\n\t" \
    "ds_read_b64_tr_b16 a[20:21], v202 offset:24576\n\t" \
    "ds_read_b64_tr_b16 a[22:23], v202 offset:25600\n\t" \
    "ds_read_b64_tr_b16 a[88:89], v203 offset:16384\n\t" \
    "ds_read_b64_tr_b16 a[90:91], v203 offset:17408\n\t" \
    "ds_read_b64_tr_b16 a[92:93], v203 offset:24576\n\t" \
    "ds_read_b64_tr_b16 a[94:95], v203 offset:25600\n\t" \
    "s_waitcnt vmcnt(16) lgkmcnt(0)\n\t" \
    "s_barrier\n\t" \
    "s_add_u32 m0, s82, 16384\n\t" \
    "s_nop 0\n\t" \
    "global_load_lds_dwordx4 v200, s[44:45]\n\t" \
    "v_add_u32 v200, 131072, v200\n\t" \
    "s_add_u32 m0, s82, 20480\n\t" \
    "s_nop 0\n\t" \
    "global_load_lds_dwordx4 v200, s[44:45]\n\t" \
    "v_add_u32 v200, 131072, v200\n\t" \
    "s_add_u32 m0, s82, 24576\n\t" \
    "s_nop 0\n\t" \
    "global_load_lds_dwordx4 v200, s[44:45]\n\t" \
    "v_add_u32 v200, 131072, v200\n\t" \
    "s_add_u32 m0, s82, 28672\n\t" \
    "s_nop 0\n\t" \
    "global_load_lds_dwordx4 v200, s[44:45]\n\t" \
    "v_add_u32 v200, 131072, v200\n\t" \
    "ds_read_b64_tr_b16 a[24:25], v202 offset:32768\n\t" \
    "ds_read_b64_tr_b16 a[26:27], v202 offset:33792\n\t" \
    "ds_read_b64_tr_b16 a[28:29], v202 offset:40960\n\t" \
    "ds_read_b64_tr_b16 a[30:31], v202 offset:41984\n\t" \
    "ds_read_b64_tr_b16 a[96:97], v203 offset:32768\n\t" \
    "ds_read_b64_tr_b16 a[98:99], v203 offset:33792\n\t" \
    "ds_read_b64_tr_b16 a[100:101], v203 offset:40960\n\t" \
    "ds_read_b64_tr_b16 a[102:103], v203 offset:41984\n\t" \
    "s_waitcnt vmcnt(12) lgkmcnt(0)\n\t" \
    "s_barrier\n\t" \
    "s_add_u32 m0, s82, 32768\n\t" \
    "s_nop 0\n\t" \
    "global_load_lds_dwordx4 v200, s[44:45]\n\t" \
    "v_add_u32 v200, 131072, v200\n\t" \
    "s_add_u32 m0, s82, 36864\n\t" \
    "s_nop 0\n\t" \
    "global_load_lds_dwordx4 v200, s[44:45]\n\t" \
    "v_add_u32 v200, 131072, v200\n\t" \
    "s_add_u32 m0, s82, 40960\n\t" \
    "s_nop 0\n\t" \
    "global_load_lds_dwordx4 v200, s[44:45]\n\t" \
    "v_add_u32 v200, 131072, v200\n\t" \
    "s_add_u32 m0, s82, 45056\n\t" \
    "s_nop 0\n\t" \
    "global_load_lds_dwordx4 v200, s[44:45]\n\t" \
    "v_add_u32 v200, 131072, v200\n\t" \
    "v_sub_u32 v166, v166, v165\n\t" \
    "v_cmp_ne_u32 vcc, 1, v166\n\t" \
    "s_cbranch_vccz gx_Cok2_%=\n\t" \
    "gx_C1_%=:\n\t" \
    "global_load_dwordx2 v[166:167], v164, s[36:37] sc1\n\t" \
    "s_waitcnt vmcnt(0)\n\t" \
    "v_sub_u32 v166, v166, v165\n\t" \
    "v_cmp_ne_u32 vcc, 1, v166\n\t" \
    "s_cbranch_vccz gx_Cok2_%=\n\t" \
    "s_cmp_eq_u32 s68, 0\n\t" \
    "s_cbranch_scc1 gx_F3_%=\n\t" \
    "s_sub_u32 s68, s68, 1\n\t" \
    "s_sleep 4\n\t" \
    "s_branch gx_C1_%=\n\t" \
    "gx_F3_%=:\n\t" \
    "v_mov_b32 v168, 6095104\n\t" \
    "v_mov_b32 v169, 4\n\t" \
    "global_store_dword v168, v169, s[36:37] sc1\n\t" \
    "s_waitcnt vmcnt(0)\n\t" \
    "s_branch gx_Cok2_%=\n\t" \
    "gx_Cok2_%=:\n\t" \
    "s_cmp_lg_u32 s66, 0\n\t" \
    "s_cbranch_scc1 gx_Ca4_%=\n\t" \
    "v_mov_b32 v164, 6176768\n\t" \
    "s_mov_b64 exec, 1\n\t" \
    "global_atomic_add v164, v166, s[36:37] sc1\n\t" \
    "s_mov_b64 exec, -1\n\t" \
    "gx_Ca4_%=:\n\t" \
    "v_min_u32_dpp v167, v167, v167 row_shl:6 row_mask:0xf bank_mask:0xf\n\t" \
    "s_nop 0\n\t" \
    "v_readlane_b32 s69, v167, 4\n\t" \
    "v_readlane_b32 s70, v167, 5\n\t" \
    "s_nop 1\n\t" \
    "s_cmp_lt_u32 s70, s69\n\t" \
    "s_cselect_b32 s52, 8192, 0\n\t" \
    "s_xor_b32 s52, s52, 8192\n\t" \
    "s_cmp_lg_u32 s66, 0\n\t" \
    "s_cbranch_scc1 gx_SF5_%=\n\t" \
    "s_lshl_b32 s69, s67, 7\n\t" \
    "s_add_u32 s69, s69, 6160384\n\t" \
    "s_add_u32 s69, s69, s52\n\t" \
    "s_add_u32 s70, s65, 1\n\t" \
    "v_mov_b32 v156, s69\n\t" \
    "v_mov_b32 v157, s70\n\t" \
    "s_mov_b64 exec, 1\n\t" \
    "global_store_dword v156, v157, s[36:37] sc1\n\t" \
    "s_mov_b64 exec, -1\n\t" \
    "gx_SF5_%=:\n\t" \
    "v_add_u32 v10, s52, v10\n\t" \
    "ds_read_b64_tr_b16 a[32:33], v202 offset:49152\n\t" \
    "ds_read_b64_tr_b16 a[34:35], v202 offset:50176\n\t" \
    "ds_read_b64_tr_b16 a[36:37], v202 offset:57344\n\t" \
    "ds_read_b64_tr_b16 a[38:39], v202 offset:58368\n\t" \
    "ds_read_b64_tr_b16 a[104:105], v203 offset:49152\n\t" \
    "ds_read_b64_tr_b16 a[106:107], v203 offset:50176\n\t" \
    "ds_read_b64_tr_b16 a[108:109], v203 offset:57344\n\t" \
    "ds_read_b64_tr_b16 a[110:111], v203 offset:58368\n\t" \
    "s_waitcnt vmcnt(12) lgkmcnt(0)\n\t" \
    "s_barrier\n\t" \
    "v_add_u32 v202, 65536, v202\n\t" \
    "v_add_u32 v203, 65536, v203\n\t" \
    "ds_read_b64_tr_b16 a[40:41], v202 offset:0\n\t" \
    "ds_read_b64_tr_b16 a[42:43], v202 offset:1024\n\t" \
    "ds_read_b64_tr_b16 a[44:45], v202 offset:8192\n\t" \
    "ds_read_b64_tr_b16 a[46:47], v202 offset:9216\n\t" \
    "ds_read_b64_tr_b16 a[112:113], v203 offset:0\n\t" \
    "ds_read_b64_tr_b16 a[114:115], v203 offset:1024\n\t" \
    "ds_read_b64_tr_b16 a[116:117], v203 offset:8192\n\t" \
    "ds_read_b64_tr_b16 a[118:119], v203 offset:9216\n\t" \
    "s_waitcnt vmcnt(8) lgkmcnt(0)\n\t" \
    "s_barrier\n\t" \
    "v_add_u32 v202, -65536, v202\n\t" \
    "v_add_u32 v203, -65536, v203\n\t" \
    "ds_read_b64_tr_b16 a[48:49], v202 offset:0\n\t" \
    "ds_read_b64_tr_b16 a[50:51], v202 offset:1024\n\t" \
    "ds_read_b64_tr_b16 a[52:53], v202 offset:8192\n\t" \
    "ds_read_b64_tr_b16 a[54:55], v202 offset:9216\n\t" \
    "ds_read_b64_tr_b16 a[120:121], v203 offset:0\n\t" \
    "ds_read_b64_tr_b16 a[122:123], v203 offset:1024\n\t" \
    "ds_read_b64_tr_b16 a[124:125], v203 offset:8192\n\t" \
    "ds_read_b64_tr_b16 a[126:127], v203 offset:9216\n\t" \
    "s_waitcnt vmcnt(4) lgkmcnt(0)\n\t" \
    "s_barrier\n\t" \
    "ds_read_b64_tr_b16 a[56:57], v202 offset:16384\n\t" \
    "ds_read_b64_tr_b16 a[58:59], v202 offset:17408\n\t" \
    "ds_read_b64_tr_b16 a[60:61], v202 offset:24576\n\t" \
    "ds_read_b64_tr_b16 a[62:63], v202 offset:25600\n\t" \
    "ds_read_b64_tr_b16 a[128:129], v203 offset:16384\n\t" \
    "ds_read_b64_tr_b16 a[130:131], v203 offset:17408\n\t" \
    "ds_read_b64_tr_b16 a[132:133], v203 offset:24576\n\t" \
    "ds_read_b64_tr_b16 a[134:135], v203 offset:25600\n\t" \
    "s_waitcnt vmcnt(0) lgkmcnt(0)\n\t" \
    "s_barrier\n\t" \
    "ds_read_b64_tr_b16 a[64:65], v202 offset:32768\n\t" \
    "ds_read_b64_tr_b16 a[66:67], v202 offset:33792\n\t" \
    "ds_read_b64_tr_b16 a[68:69], v202 offset:40960\n\t" \
    "ds_read_b64_tr_b16 a[70:71], v202 offset:41984\n\t" \
    "ds_read_b64_tr_b16 a[136:137], v203 offset:32768\n\t" \
    "ds_read_b64_tr_b16 a[138:139], v203 offset:33792\n\t" \
    "ds_read_b64_tr_b16 a[140:141], v203 offset:40960\n\t" \
    "ds_read_b64_tr_b16 a[142:143], v203 offset:41984\n\t" \
    "s_waitcnt lgkmcnt(0)\n\t" \
    "s_barrier\n\t" \
    "ds_read_b128 v[24:27], v2 offset:0\n\t" \
    "ds_read_b128 v[28:31], v2 offset:18432\n\t" \
    "ds_read_b128 v[32:35], v2 offset:1024\n\t" \
    "ds_read_b128 v[36:39], v2 offset:19456\n\t" \
    "ds_read_b128 v[40:43], v2 offset:2048\n\t" \
    "ds_read_b128 v[44:47], v2 offset:20480\n\t" \
    "s_cmp_lt_u32 s65, 62\n\t" \
    "s_cselect_b32 s69, 36864, 0\n\t" \
    "s_add_u32 s50, s50, s69\n\t" \
    "s_addc_u32 s51, s51, 0\n\t" \
    "s_add_u32 s90, s90, 36864\n\t" \
    "s_cmp_eq_u32 s90, 147456\n\t" \
    "s_cselect_b32 s90, 0, s90\n\t" \
    "s_add_u32 s91, s90, 36864\n\t" \
    "s_cmp_eq_u32 s91, 147456\n\t" \
    "s_cselect_b32 s91, 0, s91\n\t" \
    "s_add_u32 s91, s91, s89\n\t" \
    "v_add_u32 v17, s90, v3\n\t" \
    "s_branch gx_mid8_%=\n\t" \
    ".p2align 6\n\t" \
    "s_nop 0\n\t" \
    "s_nop 0\n\t" \
    "gx_loop6_%=:\n\t" \
    "s_waitcnt lgkmcnt(5)\n\t" \
    "v_mfma_f32_16x16x32_bf16 v[88:91], a[144:147], v[24:27], 0\n\t" \
    "ds_read_b128 v[48:51], v2 offset:3072\n\t" \
    "s_waitcnt lgkmcnt(5)\n\t" \
    "v_mfma_f32_16x16x32_bf16 v[92:95], a[144:147], v[28:31], 0\n\t" \
    "ds_read_b128 v[52:55], v2 offset:21504\n\t" \
    "v_mfma_f32_16x16x32_bf16 v[96:99], a[216:219], v[24:27], 0\n\t" \
    "s_sub_u32 s70, s65, 1\n\t" \
    "v_max_i32 v157, s70, v11\n\t" \
    "s_cmp_lt_u32 s65, 62\n\t" \
    "v_mfma_f32_16x16x32_bf16 v[100:103], a[216:219], v[28:31], 0\n\t" \
    "s_cselect_b32 s69, 36864, 0\n\t" \
    "s_add_u32 s50, s50, s69\n\t" \
    "s_addc_u32 s51, s51, 0\n\t" \
    "s_waitcnt lgkmcnt(5)\n\t" \
    "v_mfma_f32_16x16x32_bf16 v[88:91], a[148:151], v[32:35], v[88:91]\n\t" \
    "ds_read_b128 v[56:59], v2 offset:4096\n\t" \
    "s_waitcnt lgkmcnt(5)\n\t" \
    "v_mfma_f32_16x16x32_bf16 v[92:95], a[148:151], v[36:39], v[92:95]\n\t" \
    "ds_read_b128 v[60:63], v2 offset:22528\n\t" \
    "v_mfma_f32_16x16x32_bf16 v[96:99], a[220:223], v[32:35], v[96:99]\n\t" \
    "s_add_u32 s90, s90, 36864\n\t" \
    "s_cmp_eq_u32 s90, 147456\n\t" \
    "s_cselect_b32 s90, 0, s90\n\t" \
    "v_mfma_f32_16x16x32_bf16 v[100:103], a[220:223], v[36:39], v[100:103]\n\t" \
    "s_add_u32 s91, s90, 36864\n\t" \
    "s_cmp_eq_u32 s91, 147456\n\t" \
    "s_cselect_b32 s91, 0, s91\n\t" \
    "s_waitcnt lgkmcnt(5)\n\t" \
    "v_mfma_f32_16x16x32_bf16 v[88:91], a[152:155], v[40:43], v[88:91]\n\t" \
    "ds_read_b128 v[64:67], v2 offset:5120\n\t" \
    "s_waitcnt lgkmcnt(5)\n\t" \
    "v_mfma_f32_16x16x32_bf16 v[92:95], a[152:155], v[44:47], v[92:95]\n\t" \
    "ds_read_b128 v[68:71], v2 offset:23552\n\t" \
    "v_mfma_f32_16x16x32_bf16 v[96:99], a[224:227], v[40:43], v[96:99]\n\t" \
    "s_add_u32 s91, s91, s89\n\t" \
    "v_add_u32 v17, s90, v3\n\t" \
    "v_mfma_f32_16x16x32_bf16 v[100:103], a[224:227], v[44:47], v[100:103]\n\t" \
    "s_waitcnt lgkmcnt(5)\n\t" \
    "v_mfma_f32_16x16x32_bf16 v[88:91], a[156:159], v[48:51], v[88:91]\n\t" \
    "ds_read_b128 v[24:27], v2 offset:6144\n\t" \
    "s_waitcnt lgkmcnt(5)\n\t" \
    "v_mfma_f32_16x16x32_bf16 v[92:95], a[156:159], v[52:55], v[92:95]\n\t" \
    "ds_read_b128 v[28:31], v2 offset:24576\n\t" \
    "v_mfma_f32_16x16x32_bf16 v[96:99], a[228:231], v[48:51], v[96:99]\n\t" \
    "s_waitcnt vmcnt(0)\n\t" \
    "s_barrier\n\t" \
    "v_mfma_f32_16x16x32_bf16 v[100:103], a[228:231], v[52:55], v[100:103]\n\t" \
    "s_mov_b64 exec, s[58:59]\n\t" \
    "global_store_dword v10, v157, s[36:37] sc1\n\t" \
    "s_mov_b64 exec, -1\n\t" \
    "s_waitcnt lgkmcnt(5)\n\t" \
    "v_mfma_f32_16x16x32_bf16 v[88:91], a[160:163], v[56:59], v[88:91]\n\t" \
    "ds_read_b128 v[32:35], v2 offset:7168\n\t" \
    "s_waitcnt lgkmcnt(5)\n\t" \
    "v_mfma_f32_16x16x32_bf16 v[92:95], a[160:163], v[60:63], v[92:95]\n\t" \
    "ds_read_b128 v[36:39], v2 offset:25600\n\t" \
    "v_mfma_f32_16x16x32_bf16 v[96:99], a[232:235], v[56:59], v[96:99]\n\t" \
    "v_cvt_pk_bf16_f32 v104, v72, v73\n\t" \
    "v_mfma_f32_16x16x32_bf16 v[100:103], a[232:235], v[60:63], v[100:103]\n\t" \
    "v_cvt_pk_bf16_f32 v105, v74, v75\n\t" \
    "s_waitcnt lgkmcnt(5)\n\t" \
    "v_mfma_f32_16x16x32_bf16 v[88:91], a[164:167], v[64:67], v[88:91]\n\t" \
    "ds_read_b128 v[40:43], v2 offset:8192\n\t" \
    "s_waitcnt lgkmcnt(5)\n\t" \
    "v_mfma_f32_16x16x32_bf16 v[92:95], a[164:167], v[68:71], v[92:95]\n\t" \
    "ds_read_b128 v[44:47], v2 offset:26624\n\t" \
    "v_mfma_f32_16x16x32_bf16 v[96:99], a[236:239], v[64:67], v[96:99]\n\t" \
    "v_cvt_pk_bf16_f32 v106, v80, v81\n\t" \
    "v_mfma_f32_16x16x32_bf16 v[100:103], a[236:239], v[68:71], v[100:103]\n\t" \
    "v_cvt_pk_bf16_f32 v107, v82, v83\n\t" \
    "s_waitcnt lgkmcnt(5)\n\t" \
    "v_mfma_f32_16x16x32_bf16 v[88:91], a[168:171], v[24:27], v[88:91]\n\t" \
    "ds_read_b128 v[48:51], v2 offset:9216\n\t" \
    "s_waitcnt lgkmcnt(5)\n\t" \
    "v_mfma_f32_16x16x32_bf16 v[92:95], a[168:171], v[28:31], v[92:95]\n\t" \
    "ds_read_b128 v[52:55], v2 offset:27648\n\t" \
    "v_mfma_f32_16x16x32_bf16 v[96:99], a[240:243], v[24:27], v[96:99]\n\t" \
    "global_store_dwordx4 v9, v[104:107], s[38:39] offset:0 sc1\n\t" \
    "v_mfma_f32_16x16x32_bf16 v[100:103], a[240:243], v[28:31], v[100:103]\n\t" \
    "v_cvt_pk_bf16_f32 v108, v76, v77\n\t" \
    "s_waitcnt lgkmcnt(5)\n\t" \
    "v_mfma_f32_16x16x32_bf16 v[88:91], a[172:175], v[32:35], v[88:91]\n\t" \
    "ds_read_b128 v[56:59], v2 offset:10240\n\t" \
    "s_waitcnt lgkmcnt(5)\n\t" \
    "v_mfma_f32_16x16x32_bf16 v[92:95], a[172:175], v[36:39], v[92:95]\n\t" \
    "ds_read_b128 v[60:63], v2 offset:28672\n\t" \
    "v_mfma_f32_16x16x32_bf16 v[96:99], a[244:247], v[32:35], v[96:99]\n\t" \
    "v_cvt_pk_bf16_f32 v109, v78, v79\n\t" \
    "v_mfma_f32_16x16x32_bf16 v[100:103], a[244:247], v[36:39], v[100:103]\n\t" \
    "v_cvt_pk_bf16_f32 v110, v84, v85\n\t" \
    "s_waitcnt lgkmcnt(5)\n\t" \
    "v_mfma_f32_16x16x32_bf16 v[88:91], a[176:179], v[40:43], v[88:91]\n\t" \
    "ds_read_b128 v[64:67], v2 offset:11264\n\t" \
    "s_waitcnt lgkmcnt(5)\n\t" \
    "v_mfma_f32_16x16x32_bf16 v[92:95], a[176:179], v[44:47], v[92:95]\n\t" \
    "ds_read_b128 v[68:71], v2 offset:29696\n\t" \
    "v_mfma_f32_16x16x32_bf16 v[96:99], a[248:251], v[40:43], v[96:99]\n\t" \
    "v_cvt_pk_bf16_f32 v111, v86, v87\n\t" \
    "v_mfma_f32_16x16x32_bf16 v[100:103], a[248:251], v[44:47], v[100:103]\n\t" \
    "global_store_dwordx4 v9, v[108:111], s[38:39] offset:1024 sc1\n\t" \
    "s_waitcnt lgkmcnt(5)\n\t" \
    "v_mfma_f32_16x16x32_bf16 v[88:91], a[180:183], v[48:51], v[88:91]\n\t" \
    "ds_read_b128 v[24:27], v2 offset:12288\n\t" \
    "s_waitcnt lgkmcnt(5)\n\t" \
    "v_mfma_f32_16x16x32_bf16 v[92:95], a[180:183], v[52:55], v[92:95]\n\t" \
    "ds_read_b128 v[28:31], v2 offset:30720\n\t" \
    "v_mfma_f32_16x16x32_bf16 v[96:99], a[252:255], v[48:51], v[96:99]\n\t" \
    "s_add_u32 m0, s91, 0\n\t" \
    "v_mfma_f32_16x16x32_bf16 v[100:103], a[252:255], v[52:55], v[100:103]\n\t" \
    "global_load_lds_dwordx4 v1, s[50:51] offset:0\n\t" \
    "s_waitcnt lgkmcnt(5)\n\t" \
    "v_mfma_f32_16x16x32_bf16 v[88:91], a[184:187], v[56:59], v[88:91]\n\t" \
    "ds_read_b128 v[32:35], v2 offset:13312\n\t" \
    "s_waitcnt lgkmcnt(5)\n\t" \
    "v_mfma_f32_16x16x32_bf16 v[92:95], a[184:187], v[60:63], v[92:95]\n\t" \
    "ds_read_b128 v[36:39], v2 offset:31744\n\t" \
    "v_mfma_f32_16x16x32_bf16 v[96:99], v[224:227], v[56:59], v[96:99]\n\t" \
    "s_add_u32 m0, s91, 960\n\t" \
    "v_mfma_f32_16x16x32_bf16 v[100:103], v[224:227], v[60:63], v[100:103]\n\t" \
    "global_load_lds_dwordx4 v1, s[50:51] offset:64\n\t" \
    "s_waitcnt lgkmcnt(5)\n\t" \
    "v_mfma_f32_16x16x32_bf16 v[88:91], a[188:191], v[64:67], v[88:91]\n\t" \
    "ds_read_b128 v[40:43], v2 offset:14336\n\t" \
    "s_waitcnt lgkmcnt(5)\n\t" \
    "v_mfma_f32_16x16x32_bf16 v[92:95], a[188:191], v[68:71], v[92:95]\n\t" \
    "ds_read_b128 v[44:47], v2 offset:32768\n\t" \
    "v_mfma_f32_16x16x32_bf16 v[96:99], v[228:231], v[64:67], v[96:99]\n\t" \
    "s_add_u32 m0, s91, 1920\n\t" \
    "v_mfma_f32_16x16x32_bf16 v[100:103], v[228:231], v[68:71], v[100:103]\n\t" \
    "global_load_lds_dwordx4 v1, s[50:51] offset:128\n\t" \
    "s_waitcnt lgkmcnt(5)\n\t" \
    "v_mfma_f32_16x16x32_bf16 v[88:91], a[192:195], v[24:27], v[88:91]\n\t" \
    "ds_read_b128 v[48:51], v2 offset:15360\n\t" \
    "s_waitcnt lgkmcnt(5)\n\t" \
    "v_mfma_f32_16x16x32_bf16 v[92:95], a[192:195], v[28:31], v[92:95]\n\t" \
    "ds_read_b128 v[52:55], v2 offset:33792\n\t" \
    "v_mfma_f32_16x16x32_bf16 v[96:99], v[232:235], v[24:27], v[96:99]\n\t" \
    "s_add_u32 m0, s91, 2880\n\t" \
    "v_mfma_f32_16x16x32_bf16 v[100:103], v[232:235], v[28:31], v[100:103]\n\t" \
    "global_load_lds_dwordx4 v1, s[50:51] offset:192\n\t" \
    "s_waitcnt lgkmcnt(5)\n\t" \
    "v_mfma_f32_16x16x32_bf16 v[88:91], a[196:199], v[32:35], v[88:91]\n\t" \
    "ds_read_b128 v[56:59], v2 offset:16384\n\t" \
    "s_waitcnt lgkmcnt(5)\n\t" \
    "v_mfma_f32_16x16x32_bf16 v[92:95], a[196:199], v[36:39], v[92:95]\n\t" \
    "ds_read_b128 v[60:63], v2 offset:34816\n\t" \
    "v_mfma_f32_16x16x32_bf16 v[96:99], v[236:239], v[32:35], v[96:99]\n\t" \
    "s_add_u32 m0, s91, 3840\n\t" \
    "v_mfma_f32_16x16x32_bf16 v[100:103], v[236:239], v[36:39], v[100:103]\n\t" \
    "global_load_lds_dwordx4 v1, s[50:51] offset:256\n\t" \
    "s_waitcnt lgkmcnt(5)\n\t" \
    "v_mfma_f32_16x16x32_bf16 v[88:91], a[200:203], v[40:43], v[88:91]\n\t" \
    "ds_read_b128 v[64:67], v2 offset:17408\n\t" \
    "s_waitcnt lgkmcnt(5)\n\t" \
    "v_mfma_f32_16x16x32_bf16 v[92:95], a[200:203], v[44:47], v[92:95]\n\t" \
    "ds_read_b128 v[68:71], v2 offset:35840\n\t" \
    "v_mfma_f32_16x16x32_bf16 v[96:99], v[240:243], v[40:43], v[96:99]\n\t" \
    "s_add_u32 m0, s91, 4800\n\t" \
    "v_mfma_f32_16x16x32_bf16 v[100:103], v[240:243], v[44:47], v[100:103]\n\t" \
    "global_load_lds_dwordx4 v1, s[50:51] offset:320\n\t" \
    "s_waitcnt lgkmcnt(5)\n\t" \
    "v_mfma_f32_16x16x32_bf16 v[88:91], a[204:207], v[48:51], v[88:91]\n\t" \
    "ds_read_b128 v[24:27], v2 offset:0\n\t" \
    "s_waitcnt lgkmcnt(5)\n\t" \
    "v_mfma_f32_16x16x32_bf16 v[92:95], a[204:207], v[52:55], v[92:95]\n\t" \
    "ds_read_b128 v[28:31], v2 offset:18432\n\t" \
    "v_mfma_f32_16x16x32_bf16 v[96:99], v[244:247], v[48:51], v[96:99]\n\t" \
    "s_add_u32 m0, s91, 5760\n\t" \
    "v_mfma_f32_16x16x32_bf16 v[100:103], v[244:247], v[52:55], v[100:103]\n\t" \
    "global_load_lds_dwordx4 v1, s[50:51] offset:384\n\t" \
    "s_waitcnt lgkmcnt(5)\n\t" \
    "v_mfma_f32_16x16x32_bf16 v[88:91], a[208:211], v[56:59], v[88:91]\n\t" \
    "ds_read_b128 v[32:35], v2 offset:1024\n\t" \
    "s_waitcnt lgkmcnt(5)\n\t" \
    "v_mfma_f32_16x16x32_bf16 v[92:95], a[208:211], v[60:63], v[92:95]\n\t" \
    "ds_read_b128 v[36:39], v2 offset:19456\n\t" \
    "v_mfma_f32_16x16x32_bf16 v[96:99], v[248:251], v[56:59], v[96:99]\n\t" \
    "s_add_u32 m0, s91, 6720\n\t" \
    "v_mfma_f32_16x16x32_bf16 v[100:103], v[248:251], v[60:63], v[100:103]\n\t" \
    "global_load_lds_dwordx4 v1, s[50:51] offset:448\n\t" \
    "s_waitcnt lgkmcnt(5)\n\t" \
    "v_mfma_f32_16x16x32_bf16 v[88:91], a[212:215], v[64:67], v[88:91]\n\t" \
    "ds_read_b128 v[40:43], v2 offset:2048\n\t" \
    "s_waitcnt lgkmcnt(5)\n\t" \
    "v_mfma_f32_16x16x32_bf16 v[92:95], a[212:215], v[68:71], v[92:95]\n\t" \
    "ds_read_b128 v[44:47], v2 offset:20480\n\t" \
    "v_mfma_f32_16x16x32_bf16 v[96:99], v[252:255], v[64:67], v[96:99]\n\t" \
    "s_add_u32 m0, s91, 7680\n\t" \
    "v_mfma_f32_16x16x32_bf16 v[100:103], v[252:255], v[68:71], v[100:103]\n\t" \
    "global_load_lds_dwordx4 v1, s[50:51] offset:512\n\t" \
    "gx_mid8_%=:\n\t" \
    "s_waitcnt lgkmcnt(5)\n\t" \
    "v_mfma_f32_16x16x32_bf16 v[72:75], a[0:3], v[24:27], 0\n\t" \
    "ds_read_b128 v[48:51], v2 offset:3072\n\t" \
    "s_waitcnt lgkmcnt(5)\n\t" \
    "v_mfma_f32_16x16x32_bf16 v[76:79], a[0:3], v[28:31], 0\n\t" \
    "ds_read_b128 v[52:55], v2 offset:21504\n\t" \
    "v_mfma_f32_16x16x32_bf16 v[80:83], a[72:75], v[24:27], 0\n\t" \
    "s_lshl_b32 s69, s65, 13\n\t" \
    "v_xor_b32 v9, s69, v8\n\t" \
    "v_mfma_f32_16x16x32_bf16 v[84:87], a[72:75], v[28:31], 0\n\t" \
    "s_waitcnt lgkmcnt(5)\n\t" \
    "v_mfma_f32_16x16x32_bf16 v[72:75], a[4:7], v[32:35], v[72:75]\n\t" \
    "ds_read_b128 v[56:59], v2 offset:4096\n\t" \
    "s_waitcnt lgkmcnt(5)\n\t" \
    "v_mfma_f32_16x16x32_bf16 v[76:79], a[4:7], v[36:39], v[76:79]\n\t" \
    "ds_read_b128 v[60:63], v2 offset:22528\n\t" \
    "v_mfma_f32_16x16x32_bf16 v[80:83], a[76:79], v[32:35], v[80:83]\n\t" \
    "v_mfma_f32_16x16x32_bf16 v[84:87], a[76:79], v[36:39], v[84:87]\n\t" \
    "s_waitcnt lgkmcnt(5)\n\t" \
    "v_mfma_f32_16x16x32_bf16 v[72:75], a[8:11], v[40:43], v[72:75]\n\t" \
    "ds_read_b128 v[64:67], v2 offset:5120\n\t" \
    "s_waitcnt lgkmcnt(5)\n\t" \
    "v_mfma_f32_16x16x32_bf16 v[76:79], a[8:11], v[44:47], v[76:79]\n\t" \
    "ds_read_b128 v[68:71], v2 offset:23552\n\t" \
    "v_mfma_f32_16x16x32_bf16 v[80:83], a[80:83], v[40:43], v[80:83]\n\t" \
    "v_cvt_pk_bf16_f32 v112, v88, v89\n\t" \
    "v_mfma_f32_16x16x32_bf16 v[84:87], a[80:83], v[44:47], v[84:87]\n\t" \
    "v_cvt_pk_bf16_f32 v113, v90, v91\n\t" \
    "s_waitcnt lgkmcnt(5)\n\t" \
    "v_mfma_f32_16x16x32_bf16 v[72:75], a[12:15], v[48:51], v[72:75]\n\t" \
    "ds_read_b128 v[24:27], v2 offset:6144\n\t" \
    "s_waitcnt lgkmcnt(5)\n\t" \
    "v_mfma_f32_16x16x32_bf16 v[76:79], a[12:15], v[52:55], v[76:79]\n\t" \
    "ds_read_b128 v[28:31], v2 offset:24576\n\t" \
    "v_mfma_f32_16x16x32_bf16 v[80:83], a[84:87], v[48:51], v[80:83]\n\t" \
    "v_cvt_pk_bf16_f32 v114, v96, v97\n\t" \
    "v_mfma_f32_16x16x32_bf16 v[84:87], a[84:87], v[52:55], v[84:87]\n\t" \
    "v_cvt_pk_bf16_f32 v115, v98, v99\n\t" \
    "s_waitcnt lgkmcnt(5)\n\t" \
    "v_mfma_f32_16x16x32_bf16 v[72:75], a[16:19], v[56:59], v[72:75]\n\t" \
    "ds_read_b128 v[32:35], v2 offset:7168\n\t" \
    "s_waitcnt lgkmcnt(5)\n\t" \
    "v_mfma_f32_16x16x32_bf16 v[76:79], a[16:19], v[60:63], v[76:79]\n\t" \
    "ds_read_b128 v[36:39], v2 offset:25600\n\t" \
    "v_mfma_f32_16x16x32_bf16 v[80:83], a[88:91], v[56:59], v[80:83]\n\t" \
    "global_store_dwordx4 v9, v[112:115], s[40:41] offset:0 sc1\n\t" \
    "v_mfma_f32_16x16x32_bf16 v[84:87], a[88:91], v[60:63], v[84:87]\n\t" \
    "v_cvt_pk_bf16_f32 v116, v92, v93\n\t" \
    "s_waitcnt lgkmcnt(5)\n\t" \
    "v_mfma_f32_16x16x32_bf16 v[72:75], a[20:23], v[64:67], v[72:75]\n\t" \
    "ds_read_b128 v[40:43], v2 offset:8192\n\t" \
    "s_waitcnt lgkmcnt(5)\n\t" \
    "v_mfma_f32_16x16x32_bf16 v[76:79], a[20:23], v[68:71], v[76:79]\n\t" \
    "ds_read_b128 v[44:47], v2 offset:26624\n\t" \
    "v_mfma_f32_16x16x32_bf16 v[80:83], a[92:95], v[64:67], v[80:83]\n\t" \
    "v_cvt_pk_bf16_f32 v117, v94, v95\n\t" \
    "v_mfma_f32_16x16x32_bf16 v[84:87], a[92:95], v[68:71], v[84:87]\n\t" \
    "v_cvt_pk_bf16_f32 v118, v100, v101\n\t" \
    "s_waitcnt lgkmcnt(5)\n\t" \
    "v_mfma_f32_16x16x32_bf16 v[72:75], a[24:27], v[24:27], v[72:75]\n\t" \
    "ds_read_b128 v[48:51], v2 offset:9216\n\t" \
    "s_waitcnt lgkmcnt(5)\n\t" \
    "v_mfma_f32_16x16x32_bf16 v[76:79], a[24:27], v[28:31], v[76:79]\n\t" \
    "ds_read_b128 v[52:55], v2 offset:27648\n\t" \
    "v_mfma_f32_16x16x32_bf16 v[80:83], a[96:99], v[24:27], v[80:83]\n\t" \
    "v_cvt_pk_bf16_f32 v119, v102, v103\n\t" \
    "v_mfma_f32_16x16x32_bf16 v[84:87], a[96:99], v[28:31], v[84:87]\n\t" \
    "global_store_dwordx4 v9, v[116:119], s[40:41] offset:1024 sc1\n\t" \
    "s_waitcnt lgkmcnt(5)\n\t" \
    "v_mfma_f32_16x16x32_bf16 v[72:75], a[28:31], v[32:35], v[72:75]\n\t" \
    "ds_read_b128 v[56:59], v2 offset:10240\n\t" \
    "s_waitcnt lgkmcnt(5)\n\t" \
    "v_mfma_f32_16x16x32_bf16 v[76:79], a[28:31], v[36:39], v[76:79]\n\t" \
    "ds_read_b128 v[60:63], v2 offset:28672\n\t" \
    "v_mfma_f32_16x16x32_bf16 v[80:83], a[100:103], v[32:35], v[80:83]\n\t" \
    "v_mfma_f32_16x16x32_bf16 v[84:87], a[100:103], v[36:39], v[84:87]\n\t" \
    "s_waitcnt lgkmcnt(5)\n\t" \
    "v_mfma_f32_16x16x32_bf16 v[72:75], a[32:35], v[40:43], v[72:75]\n\t" \
    "ds_read_b128 v[64:67], v2 offset:11264\n\t" \
    "s_waitcnt lgkmcnt(5)\n\t" \
    "v_mfma_f32_16x16x32_bf16 v[76:79], a[32:35], v[44:47], v[76:79]\n\t" \
    "ds_read_b128 v[68:71], v2 offset:29696\n\t" \
    "v_mfma_f32_16x16x32_bf16 v[80:83], a[104:107], v[40:43], v[80:83]\n\t" \
    "v_mfma_f32_16x16x32_bf16 v[84:87], a[104:107], v[44:47], v[84:87]\n\t" \
    "s_waitcnt lgkmcnt(5)\n\t" \
    "v_mfma_f32_16x16x32_bf16 v[72:75], a[36:39], v[48:51], v[72:75]\n\t" \
    "ds_read_b128 v[24:27], v2 offset:12288\n\t" \
    "s_waitcnt lgkmcnt(5)\n\t" \
    "v_mfma_f32_16x16x32_bf16 v[76:79], a[36:39], v[52:55], v[76:79]\n\t" \
    "ds_read_b128 v[28:31], v2 offset:30720\n\t" \
    "v_mfma_f32_16x16x32_bf16 v[80:83], a[108:111], v[48:51], v[80:83]\n\t" \
    "v_mfma_f32_16x16x32_bf16 v[84:87], a[108:111], v[52:55], v[84:87]\n\t" \
    "s_waitcnt lgkmcnt(5)\n\t" \
    "v_mfma_f32_16x16x32_bf16 v[72:75], a[40:43], v[56:59], v[72:75]\n\t" \
    "ds_read_b128 v[32:35], v2 offset:13312\n\t" \
    "s_waitcnt lgkmcnt(5)\n\t" \
    "v_mfma_f32_16x16x32_bf16 v[76:79], a[40:43], v[60:63], v[76:79]\n\t" \
    "ds_read_b128 v[36:39], v2 offset:31744\n\t" \
    "v_mfma_f32_16x16x32_bf16 v[80:83], a[112:115], v[56:59], v[80:83]\n\t" \
    "v_mfma_f32_16x16x32_bf16 v[84:87], a[112:115], v[60:63], v[84:87]\n\t" \
    "s_waitcnt lgkmcnt(5)\n\t" \
    "v_mfma_f32_16x16x32_bf16 v[72:75], a[44:47], v[64:67], v[72:75]\n\t" \
    "ds_read_b128 v[40:43], v2 offset:14336\n\t" \
    "s_waitcnt lgkmcnt(5)\n\t" \
    "v_mfma_f32_16x16x32_bf16 v[76:79], a[44:47], v[68:71], v[76:79]\n\t" \
    "ds_read_b128 v[44:47], v2 offset:32768\n\t" \
    "v_mfma_f32_16x16x32_bf16 v[80:83], a[116:119], v[64:67], v[80:83]\n\t" \
    "v_mfma_f32_16x16x32_bf16 v[84:87], a[116:119], v[68:71], v[84:87]\n\t" \
    "s_waitcnt lgkmcnt(5)\n\t" \
    "v_mfma_f32_16x16x32_bf16 v[72:75], a[48:51], v[24:27], v[72:75]\n\t" \
    "ds_read_b128 v[48:51], v2 offset:15360\n\t" \
    "s_waitcnt lgkmcnt(5)\n\t" \
    "v_mfma_f32_16x16x32_bf16 v[76:79], a[48:51], v[28:31], v[76:79]\n\t" \
    "ds_read_b128 v[52:55], v2 offset:33792\n\t" \
    "v_mfma_f32_16x16x32_bf16 v[80:83], a[120:123], v[24:27], v[80:83]\n\t" \
    "v_mfma_f32_16x16x32_bf16 v[84:87], a[120:123], v[28:31], v[84:87]\n\t" \
    "s_waitcnt lgkmcnt(5)\n\t" \
    "v_mfma_f32_16x16x32_bf16 v[72:75], a[52:55], v[32:35], v[72:75]\n\t" \
    "ds_read_b128 v[56:59], v2 offset:16384\n\t" \
    "s_waitcnt lgkmcnt(5)\n\t" \
    "v_mfma_f32_16x16x32_bf16 v[76:79], a[52:55], v[36:39], v[76:79]\n\t" \
    "ds_read_b128 v[60:63], v2 offset:34816\n\t" \
    "v_mfma_f32_16x16x32_bf16 v[80:83], a[124:127], v[32:35], v[80:83]\n\t" \
    "v_mfma_f32_16x16x32_bf16 v[84:87], a[124:127], v[36:39], v[84:87]\n\t" \
    "s_cmp_eq_u32 s65, 0\n\t" \
    "s_cbranch_scc1 gx_g19_%=\n\t" \
    "gx_g1b10_%=:\n\t" \
    "s_waitcnt lgkmcnt(5)\n\t" \
    "v_mfma_f32_16x16x32_bf16 v[72:75], a[56:59], v[40:43], v[72:75]\n\t" \
    "ds_read_b128 v[64:67], v2 offset:17408\n\t" \
    "s_waitcnt lgkmcnt(5)\n\t" \
    "v_mfma_f32_16x16x32_bf16 v[76:79], a[56:59], v[44:47], v[76:79]\n\t" \
    "ds_read_b128 v[68:71], v2 offset:35840\n\t" \
    "v_mfma_f32_16x16x32_bf16 v[80:83], a[128:131], v[40:43], v[80:83]\n\t" \
    "v_mfma_f32_16x16x32_bf16 v[84:87], a[128:131], v[44:47], v[84:87]\n\t" \
    "s_waitcnt lgkmcnt(5)\n\t" \
    "v_mfma_f32_16x16x32_bf16 v[72:75], a[60:63], v[48:51], v[72:75]\n\t" \
    "ds_read_b128 v[24:27], v17 offset:0\n\t" \
    "s_waitcnt lgkmcnt(5)\n\t" \
    "v_mfma_f32_16x16x32_bf16 v[76:79], a[60:63], v[52:55], v[76:79]\n\t" \
    "ds_read_b128 v[28:31], v17 offset:18432\n\t" \
    "v_mfma_f32_16x16x32_bf16 v[80:83], a[132:135], v[48:51], v[80:83]\n\t" \
    "v_mov_b32 v2, v17\n\t" \
    "v_mfma_f32_16x16x32_bf16 v[84:87], a[132:135], v[52:55], v[84:87]\n\t" \
    "s_waitcnt lgkmcnt(5)\n\t" \
    "v_mfma_f32_16x16x32_bf16 v[72:75], a[64:67], v[56:59], v[72:75]\n\t" \
    "ds_read_b128 v[32:35], v17 offset:1024\n\t" \
    "s_waitcnt lgkmcnt(5)\n\t" \
    "v_mfma_f32_16x16x32_bf16 v[76:79], a[64:67], v[60:63], v[76:79]\n\t" \
    "ds_read_b128 v[36:39], v17 offset:19456\n\t" \
    "v_mfma_f32_16x16x32_bf16 v[80:83], a[136:139], v[56:59], v[80:83]\n\t" \
    "v_mfma_f32_16x16x32_bf16 v[84:87], a[136:139], v[60:63], v[84:87]\n\t" \
    "s_waitcnt lgkmcnt(5)\n\t" \
    "v_mfma_f32_16x16x32_bf16 v[72:75], a[68:71], v[64:67], v[72:75]\n\t" \
    "ds_read_b128 v[40:43], v17 offset:2048\n\t" \
    "s_waitcnt lgkmcnt(5)\n\t" \
    "v_mfma_f32_16x16x32_bf16 v[76:79], a[68:71], v[68:71], v[76:79]\n\t" \
    "ds_read_b128 v[44:47], v17 offset:20480\n\t" \
    "v_mfma_f32_16x16x32_bf16 v[80:83], a[140:143], v[64:67], v[80:83]\n\t" \
    "s_add_u32 s65, s65, 1\n\t" \
    "s_cmp_lt_u32 s65, 64\n\t" \
    "v_mfma_f32_16x16x32_bf16 v[84:87], a[140:143], v[68:71], v[84:87]\n\t" \
    "s_cbranch_scc1 gx_loop6_%=\n\t" \
    "s_nop 7\n\t" \
    "v_cvt_pk_bf16_f32 v104, v72, v73\n\t" \
    "v_cvt_pk_bf16_f32 v105, v74, v75\n\t" \
    "v_cvt_pk_bf16_f32 v106, v80, v81\n\t" \
    "v_cvt_pk_bf16_f32 v107, v82, v83\n\t" \
    "global_store_dwordx4 v9, v[104:107], s[38:39] offset:0 sc1\n\t" \
    "v_cvt_pk_bf16_f32 v108, v76, v77\n\t" \
    "v_cvt_pk_bf16_f32 v109, v78, v79\n\t" \
    "v_cvt_pk_bf16_f32 v110, v84, v85\n\t" \
    "v_cvt_pk_bf16_f32 v111, v86, v87\n\t" \
    "global_store_dwordx4 v9, v[108:111], s[38:39] offset:1024 sc1\n\t" \
    "s_waitcnt vmcnt(0) lgkmcnt(0)\n\t" \
    "s_barrier\n\t" \
    "s_cmp_lg_u32 s66, 0\n\t" \
    "s_cbranch_scc1 gx_SF11_%=\n\t" \
    "s_lshl_b32 s69, s67, 7\n\t" \
    "s_add_u32 s69, s69, 6098944\n\t" \
    "s_add_u32 s69, s69, s52\n\t" \
    "s_add_u32 s70, s65, 0\n\t" \
    "v_mov_b32 v156, s69\n\t" \
    "v_mov_b32 v157, s70\n\t" \
    "s_add_u32 s69, s69, 61440\n\t" \
    "s_max_i32 s70, s70, 1\n\t" \
    "v_writelane_b32 v156, s69, 1\n\t" \
    "v_writelane_b32 v157, s70, 1\n\t" \
    "s_mov_b64 exec, 3\n\t" \
    "global_store_dword v156, v157, s[36:37] sc1\n\t" \
    "s_mov_b64 exec, -1\n\t" \
    "gx_SF11_%=:\n\t" \
    "s_waitcnt vmcnt(0)\n\t" \
    "s_branch ea_done_%=\n\t" \
    "gx_g19_%=:\n\t" \
    "s_add_u32 m0, s91, 0\n\t" \
    "s_nop 0\n\t" \
    "global_load_lds_dwordx4 v1, s[50:51] offset:0\n\t" \
    "s_add_u32 m0, s91, 960\n\t" \
    "s_nop 0\n\t" \
    "global_load_lds_dwordx4 v1, s[50:51] offset:64\n\t" \
    "s_add_u32 m0, s91, 1920\n\t" \
    "s_nop 0\n\t" \
    "global_load_lds_dwordx4 v1, s[50:51] offset:128\n\t" \
    "s_add_u32 m0, s91, 2880\n\t" \
    "s_nop 0\n\t" \
    "global_load_lds_dwordx4 v1, s[50:51] offset:192\n\t" \
    "s_add_u32 m0, s91, 3840\n\t" \
    "s_nop 0\n\t" \
    "global_load_lds_dwordx4 v1, s[50:51] offset:256\n\t" \
    "s_add_u32 m0, s91, 4800\n\t" \
    "s_nop 0\n\t" \
    "global_load_lds_dwordx4 v1, s[50:51] offset:320\n\t" \
    "s_add_u32 m0, s91, 5760\n\t" \
    "s_nop 0\n\t" \
    "global_load_lds_dwordx4 v1, s[50:51] offset:384\n\t" \
    "s_add_u32 m0, s91, 6720\n\t" \
    "s_nop 0\n\t" \
    "global_load_lds_dwordx4 v1, s[50:51] offset:448\n\t" \
    "s_add_u32 m0, s91, 7680\n\t" \
    "s_nop 0\n\t" \
    "global_load_lds_dwordx4 v1, s[50:51] offset:512\n\t" \
    "s_branch gx_g1b10_%=\n\t" \
    "gv_begin_%=:\n\t" \
    "v_mbcnt_lo_u32_b32 v223, -1, 0\n\t" \
    "v_mbcnt_hi_u32_b32 v223, -1, v223\n\t" \
    "s_mov_b32 s68, 262144\n\t" \
    "s_mov_b32 s72, 0\n\t" \
    "s_mov_b32 s83, -1\n\t" \
    "s_mov_b32 s65, 0\n\t" \
    "v_and_b32 v180, 3, v223\n\t" \
    "v_lshlrev_b32 v202, 3, v180\n\t" \
    "v_lshrrev_b32 v181, 5, v223\n\t" \
    "v_lshl_add_u32 v202, v181, 12, v202\n\t" \
    "v_bfe_u32 v182, v223, 2, 2\n\t" \
    "s_mov_b32 s70, 288\n\t" \
    "v_mad_u32_u24 v202, v182, s70, v202\n\t" \
    "v_bfe_u32 v183, v223, 4, 1\n\t" \
    "s_mov_b32 s70, 2176\n\t" \
    "v_mad_u32_u24 v202, v183, s70, v202\n\t" \
    "s_lshl_b32 s69, s66, 6\n\t" \
    "v_xor_b32 v202, s69, v202\n\t" \
    "v_xor_b32 v203, 32, v202\n\t" \
    "s_mul_i32 s69, s66, 9216\n\t" \
    "v_and_b32 v184, 1, v223\n\t" \
    "v_lshl_add_u32 v201, v184, 4, s69\n\t" \
    "v_lshrrev_b32 v185, 3, v223\n\t" \
    "s_mov_b32 s70, 1152\n\t" \
    "v_mad_u32_u24 v201, v185, s70, v201\n\t" \
    "v_bfe_u32 v186, v223, 1, 2\n\t" \
    "v_lshlrev_b32 v204, 5, v186\n\t" \
    "v_lshrrev_b32 v187, 4, v223\n\t" \
    "v_lshlrev_b32 v205, 5, v187\n\t" \
    "v_xor_b32 v204, v204, v205\n\t" \
    "v_add_u32 v201, v201, v204\n\t" \
    "s_mul_i32 s96, s66, 9216\n\t" \
    "v_lshlrev_b32 v207, 3, v180\n\t" \
    "v_bfe_u32 v188, v223, 2, 1\n\t" \
    "v_lshl_add_u32 v207, v188, 7, v207\n\t" \
    "v_bfe_u32 v189, v223, 3, 2\n\t" \
    "s_mov_b32 s70, 288\n\t" \
    "v_mad_u32_u24 v207, v189, s70, v207\n\t" \
    "s_mov_b32 s70, 9216\n\t" \
    "v_mad_u32_u24 v207, v181, s70, v207\n\t" \
    "s_mul_i32 s69, s66, 9\n\t" \
    "s_add_u32 s69, s69, 0\n\t" \
    "s_and_b32 s70, s69, 3\n\t" \
    "s_lshl_b32 s70, s70, 5\n\t" \
    "s_lshr_b32 s69, s69, 2\n\t" \
    "s_lshl_b32 s69, s69, 10\n\t" \
    "v_xor_b32 v204, s70, v207\n\t" \
    "v_add_u32 v204, s69, v204\n\t" \
    "s_mul_i32 s69, s66, 9\n\t" \
    "s_add_u32 s69, s69, 1\n\t" \
    "s_and_b32 s70, s69, 3\n\t" \
    "s_lshl_b32 s70, s70, 5\n\t" \
    "s_lshr_b32 s69, s69, 2\n\t" \
    "s_lshl_b32 s69, s69, 10\n\t" \
    "v_xor_b32 v205, s70, v207\n\t" \
    "v_add_u32 v205, s69, v205\n\t" \
    "s_mul_i32 s69, s66, 9\n\t" \
    "s_add_u32 s69, s69, 2\n\t" \
    "s_and_b32 s70, s69, 3\n\t" \
    "s_lshl_b32 s70, s70, 5\n\t" \
    "s_lshr_b32 s69, s69, 2\n\t" \
    "s_lshl_b32 s69, s69, 10\n\t" \
    "v_xor_b32 v206, s70, v207\n\t" \
    "v_add_u32 v206, s69, v206\n\t" \
    "s_mul_i32 s69, s66, 9\n\t" \
    "s_add_u32 s69, s69, 3\n\t" \
    "s_and_b32 s70, s69, 3\n\t" \
    "s_lshl_b32 s70, s70, 5\n\t" \
    "s_lshr_b32 s69, s69, 2\n\t" \
    "s_lshl_b32 s69, s69, 10\n\t" \
    "v_xor_b32 v207, s70, v207\n\t" \
    "v_add_u32 v207, s69, v207\n\t" \
    "s_waitcnt vmcnt(12) lgkmcnt(0)\n\t" \
    "s_barrier\n\t" \
    "s_add_u32 m0, s82, 65536\n\t" \
    "s_nop 0\n\t" \
    "global_load_lds_dwordx4 v200, s[42:43]\n\t" \
    "v_add_u32 v200, 131072, v200\n\t" \
    "s_add_u32 m0, s82, 69632\n\t" \
    "s_nop 0\n\t" \
    "global_load_lds_dwordx4 v200, s[42:43]\n\t" \
    "v_add_u32 v200, 131072, v200\n\t" \
    "s_add_u32 m0, s82, 73728\n\t" \
    "s_nop 0\n\t" \
    "global_load_lds_dwordx4 v200, s[42:43]\n\t" \
    "v_add_u32 v200, 131072, v200\n\t" \
    "s_add_u32 m0, s82, 77824\n\t" \
    "s_nop 0\n\t" \
    "global_load_lds_dwordx4 v200, s[42:43]\n\t" \
    "v_add_u32 v200, 131072, v200\n\t" \
    "ds_read_b64_tr_b16 a[0:1], v202 offset:36864\n\t" \
    "ds_read_b64_tr_b16 a[2:3], v202 offset:37888\n\t" \
    "ds_read_b64_tr_b16 a[4:5], v202 offset:45056\n\t" \
    "ds_read_b64_tr_b16 a[6:7], v202 offset:46080\n\t" \
    "ds_read_b64_tr_b16 a[72:73], v203 offset:36864\n\t" \
    "ds_read_b64_tr_b16 a[74:75], v203 offset:37888\n\t" \
    "ds_read_b64_tr_b16 a[76:77], v203 offset:45056\n\t" \
    "ds_read_b64_tr_b16 a[78:79], v203 offset:46080\n\t" \
    "v_lshlrev_b32 v3, 4, v180\n\t" \
    "v_lshlrev_b32 v2, 5, v181\n\t" \
    "v_xor_b32 v3, v3, v2\n\t" \
    "s_bfe_u32 s71, s66, 0x10000\n\t" \
    "s_mul_i32 s69, s71, 576\n\t" \
    "s_bfe_u32 s71, s66, 0x10001\n\t" \
    "s_mul_i32 s71, s71, 18432\n\t" \
    "s_add_u32 s69, s69, s71\n\t" \
    "v_lshrrev_b32 v190, 2, v223\n\t" \
    "v_mov_b32 v1, s69\n\t" \
    "s_mov_b32 s70, 1152\n\t" \
    "v_mad_u32_u24 v1, v190, s70, v1\n\t" \
    "v_add_u32 v1, v1, v3\n\t" \
    "v_and_b32 v191, 15, v223\n\t" \
    "v_lshlrev_b32 v3, 6, v191\n\t" \
    "v_lshl_add_u32 v3, v187, 4, v3\n\t" \
    "v_bfe_u32 v192, v223, 3, 1\n\t" \
    "v_lshlrev_b32 v2, 5, v192\n\t" \
    "v_xor_b32 v3, v3, v2\n\t" \
    "v_mov_b32 v2, v3\n\t" \
    "s_lshr_b32 s65, s67, 5\n\t" \
    "s_mul_i32 s69, s65, 36864\n\t" \
    "s_add_u32 s50, s50, s69\n\t" \
    "s_addc_u32 s51, s51, 0\n\t" \
    "s_add_u32 s38, s36, 48234496\n\t" \
    "s_addc_u32 s39, s37, 0\n\t" \
    "s_mul_i32 s89, s66, 9216\n\t" \
    "s_mov_b32 s90, 0\n\t" \
    "s_waitcnt vmcnt(12) lgkmcnt(0)\n\t" \
    "s_barrier\n\t" \
    "s_add_u32 m0, s82, 81920\n\t" \
    "s_nop 0\n\t" \
    "global_load_lds_dwordx4 v200, s[42:43]\n\t" \
    "v_add_u32 v200, 131072, v200\n\t" \
    "s_add_u32 m0, s82, 86016\n\t" \
    "s_nop 0\n\t" \
    "global_load_lds_dwordx4 v200, s[42:43]\n\t" \
    "v_add_u32 v200, 131072, v200\n\t" \
    "s_add_u32 m0, s82, 90112\n\t" \
    "s_nop 0\n\t" \
    "global_load_lds_dwordx4 v200, s[42:43]\n\t" \
    "v_add_u32 v200, 131072, v200\n\t" \
    "s_add_u32 m0, s82, 94208\n\t" \
    "s_nop 0\n\t" \
    "global_load_lds_dwordx4 v200, s[42:43]\n\t" \
    "v_add_u32 v200, 131072, v200\n\t" \
    "ds_read_b64_tr_b16 a[8:9], v202 offset:53248\n\t" \
    "ds_read_b64_tr_b16 a[10:11], v202 offset:54272\n\t" \
    "ds_read_b64_tr_b16 a[12:13], v202 offset:61440\n\t" \
    "ds_read_b64_tr_b16 a[14:15], v202 offset:62464\n\t" \
    "ds_read_b64_tr_b16 a[80:81], v203 offset:53248\n\t" \
    "ds_read_b64_tr_b16 a[82:83], v203 offset:54272\n\t" \
    "ds_read_b64_tr_b16 a[84:85], v203 offset:61440\n\t" \
    "ds_read_b64_tr_b16 a[86:87], v203 offset:62464\n\t" \
    "v_lshlrev_b32 v0, 4, v223\n\t" \
    "s_mul_i32 s69, s66, 2048\n\t" \
    "s_add_u32 s69, s69, 147456\n\t" \
    "v_lshl_add_u32 v4, v223, 4, s69\n\t" \
    "s_mov_b32 s69, 147456\n\t" \
    "v_lshl_add_u32 v5, v223, 4, s69\n\t" \
    "v_lshlrev_b32 v6, 2, v223\n\t" \
    "v_mov_b32 v7, 0\n\t" \
    "s_cmp_eq_u32 s66, 0\n\t" \
    "s_cselect_b32 s58, 31, 0\n\t" \
    "s_mov_b32 s59, 0\n\t" \
    "s_waitcnt vmcnt(12) lgkmcnt(0)\n\t" \
    "s_barrier\n\t" \
    "s_add_u32 m0, s82, 98304\n\t" \
    "s_nop 0\n\t" \
    "global_load_lds_dwordx4 v200, s[42:43]\n\t" \
    "v_add_u32 v200, 131072, v200\n\t" \
    "s_add_u32 m0, s82, 102400\n\t" \
    "s_nop 0\n\t" \
    "global_load_lds_dwordx4 v200, s[42:43]\n\t" \
    "v_add_u32 v200, 131072, v200\n\t" \
    "s_add_u32 m0, s82, 106496\n\t" \
    "s_nop 0\n\t" \
    "global_load_lds_dwordx4 v200, s[42:43]\n\t" \
    "v_add_u32 v200, 131072, v200\n\t" \
    "s_add_u32 m0, s82, 110592\n\t" \
    "s_nop 0\n\t" \
    "global_load_lds_dwordx4 v200, s[42:43]\n\t" \
    "v_add_u32 v200, 131072, v200\n\t" \
    "v_add_u32 v202, 69632, v202\n\t" \
    "v_add_u32 v203, 69632, v203\n\t" \
    "ds_read_b64_tr_b16 a[16:17], v202 offset:0\n\t" \
    "ds_read_b64_tr_b16 a[18:19], v202 offset:1024\n\t" \
    "ds_read_b64_tr_b16 a[20:21], v202 offset:8192\n\t" \
    "ds_read_b64_tr_b16 a[22:23], v202 offset:9216\n\t" \
    "ds_read_b64_tr_b16 a[88:89], v203 offset:0\n\t" \
    "ds_read_b64_tr_b16 a[90:91], v203 offset:1024\n\t" \
    "ds_read_b64_tr_b16 a[92:93], v203 offset:8192\n\t" \
    "ds_read_b64_tr_b16 a[94:95], v203 offset:9216\n\t" \
    "s_bfe_u32 s71, s67, 0x50000\n\t" \
    "s_mul_i32 s69, s71, 32\n\t" \
    "s_add_u32 s69, s69, 262148\n\t" \
    "v_lshl_add_u32 v19, v180, 16, s69\n\t" \
    "v_lshl_add_u32 v19, v181, 13, v19\n\t" \
    "v_bfe_u32 v193, v223, 2, 3\n\t" \
    "v_lshlrev_b32 v199, 10, v193\n\t" \
    "v_min_u32 v199, 4096, v199\n\t" \
    "v_add_u32 v19, v199, v19\n\t" \
    "s_mul_i32 s69, s67, 32\n\t" \
    "s_add_u32 s69, s69, 6193152\n\t" \
    "v_lshl_add_u32 v20, v180, 11, s69\n\t" \
    "s_lshl_b32 s69, s67, 7\n\t" \
    "s_add_u32 s69, s69, 6111232\n\t" \
    "v_writelane_b32 v20, s69, 4\n\t" \
    "s_bfe_u32 s71, s67, 0x50000\n\t" \
    "s_mul_i32 s69, s71, 1024\n\t" \
    "v_lshl_add_u32 v17, v223, 4, s69\n\t" \
    "s_mul_i32 s69, s66, 9\n\t" \
    "s_add_u32 s69, s69, 0\n\t" \
    "s_mul_i32 s70, s69, 7\n\t" \
    "s_and_b32 s70, s70, 63\n\t" \
    "s_lshl_b32 s70, s70, 15\n\t" \
    "s_mul_i32 s69, s69, 2097152\n\t" \
    "s_add_u32 s69, s69, s70\n\t" \
    "v_add_u32 v8, s69, v17\n\t" \
    "s_mul_i32 s69, s66, 9\n\t" \
    "s_add_u32 s69, s69, 1\n\t" \
    "s_mul_i32 s70, s69, 7\n\t" \
    "s_and_b32 s70, s70, 63\n\t" \
    "s_lshl_b32 s70, s70, 15\n\t" \
    "s_mul_i32 s69, s69, 2097152\n\t" \
    "s_add_u32 s69, s69, s70\n\t" \
    "v_add_u32 v9, s69, v17\n\t" \
    "s_mul_i32 s69, s66, 9\n\t" \
    "s_add_u32 s69, s69, 2\n\t" \
    "s_mul_i32 s70, s69, 7\n\t" \
    "s_and_b32 s70, s70, 63\n\t" \
    "s_lshl_b32 s70, s70, 15\n\t" \
    "s_mul_i32 s69, s69, 2097152\n\t" \
    "s_add_u32 s69, s69, s70\n\t" \
    "v_add_u32 v10, s69, v17\n\t" \
    "s_mul_i32 s69, s66, 9\n\t" \
    "s_add_u32 s69, s69, 3\n\t" \
    "s_mul_i32 s70, s69, 7\n\t" \
    "s_and_b32 s70, s70, 63\n\t" \
    "s_lshl_b32 s70, s70, 15\n\t" \
    "s_mul_i32 s69, s69, 2097152\n\t" \
    "s_add_u32 s69, s69, s70\n\t" \
    "v_add_u32 v11, s69, v17\n\t" \
    "s_mul_i32 s69, s66, 9\n\t" \
    "s_add_u32 s69, s69, 4\n\t" \
    "s_mul_i32 s70, s69, 7\n\t" \
    "s_and_b32 s70, s70, 63\n\t" \
    "s_lshl_b32 s70, s70, 15\n\t" \
    "s_mul_i32 s69, s69, 2097152\n\t" \
    "s_add_u32 s69, s69, s70\n\t" \
    "v_add_u32 v12, s69, v17\n\t" \
    "s_mul_i32 s69, s66, 9\n\t" \
    "s_add_u32 s69, s69, 5\n\t" \
    "s_mul_i32 s70, s69, 7\n\t" \
    "s_and_b32 s70, s70, 63\n\t" \
    "s_lshl_b32 s70, s70, 15\n\t" \
    "s_mul_i32 s69, s69, 2097152\n\t" \
    "s_add_u32 s69, s69, s70\n\t" \
    "v_add_u32 v13, s69, v17\n\t" \
    "s_mul_i32 s69, s66, 9\n\t" \
    "s_add_u32 s69, s69, 6\n\t" \
    "s_mul_i32 s70, s69, 7\n\t" \
    "s_and_b32 s70, s70, 63\n\t" \
    "s_lshl_b32 s70, s70, 15\n\t" \
    "s_mul_i32 s69, s69, 2097152\n\t" \
    "s_add_u32 s69, s69, s70\n\t" \
    "v_add_u32 v14, s69, v17\n\t" \
    "s_mul_i32 s69, s66, 9\n\t" \
    "s_add_u32 s69, s69, 7\n\t" \
    "s_mul_i32 s70, s69, 7\n\t" \
    "s_and_b32 s70, s70, 63\n\t" \
    "s_lshl_b32 s70, s70, 15\n\t" \
    "s_mul_i32 s69, s69, 2097152\n\t" \
    "s_add_u32 s69, s69, s70\n\t" \
    "v_add_u32 v15, s69, v17\n\t" \
    "s_mul_i32 s69, s66, 9\n\t" \
    "s_add_u32 s69, s69, 8\n\t" \
    "s_mul_i32 s70, s69, 7\n\t" \
    "s_and_b32 s70, s70, 63\n\t" \
    "s_lshl_b32 s70, s70, 15\n\t" \
    "s_mul_i32 s69, s69, 2097152\n\t" \
    "s_add_u32 s69, s69, s70\n\t" \
    "v_add_u32 v16, s69, v17\n\t" \
    "s_waitcnt vmcnt(12) lgkmcnt(0)\n\t" \
    "s_barrier\n\t" \
    "s_add_u32 m0, s82, 0\n\t" \
    "s_nop 0\n\t" \
    "global_load_lds_dwordx4 v200, s[42:43]\n\t" \
    "v_add_u32 v200, 131072, v200\n\t" \
    "s_add_u32 m0, s82, 4096\n\t" \
    "s_nop 0\n\t" \
    "global_load_lds_dwordx4 v200, s[42:43]\n\t" \
    "v_add_u32 v200, 131072, v200\n\t" \
    "s_add_u32 m0, s82, 8192\n\t" \
    "s_nop 0\n\t" \
    "global_load_lds_dwordx4 v200, s[42:43]\n\t" \
    "v_add_u32 v200, 131072, v200\n\t" \
    "s_add_u32 m0, s82, 12288\n\t" \
    "s_nop 0\n\t" \
    "global_load_lds_dwordx4 v200, s[42:43]\n\t" \
    "v_add_u32 v200, 131072, v200\n\t" \
    "ds_read_b64_tr_b16 a[24:25], v202 offset:16384\n\t" \
    "ds_read_b64_tr_b16 a[26:27], v202 offset:17408\n\t" \
    "ds_read_b64_tr_b16 a[28:29], v202 offset:24576\n\t" \
    "ds_read_b64_tr_b16 a[30:31], v202 offset:25600\n\t" \
    "ds_read_b64_tr_b16 a[96:97], v203 offset:16384\n\t" \
    "ds_read_b64_tr_b16 a[98:99], v203 offset:17408\n\t" \
    "ds_read_b64_tr_b16 a[100:101], v203 offset:24576\n\t" \
    "ds_read_b64_tr_b16 a[102:103], v203 offset:25600\n\t" \
    "s_waitcnt vmcnt(12) lgkmcnt(0)\n\t" \
    "s_barrier\n\t" \
    "s_add_u32 m0, s82, 16384\n\t" \
    "s_nop 0\n\t" \
    "global_load_lds_dwordx4 v200, s[42:43]\n\t" \
    "v_add_u32 v200, 131072, v200\n\t" \
    "s_add_u32 m0, s82, 20480\n\t" \
    "s_nop 0\n\t" \
    "global_load_lds_dwordx4 v200, s[42:43]\n\t" \
    "v_add_u32 v200, 131072, v200\n\t" \
    "s_add_u32 m0, s82, 24576\n\t" \
    "s_nop 0\n\t" \
    "global_load_lds_dwordx4 v200, s[42:43]\n\t" \
    "v_add_u32 v200, 131072, v200\n\t" \
    "s_add_u32 m0, s82, 28672\n\t" \
    "s_nop 0\n\t" \
    "global_load_lds_dwordx4 v200, s[42:43]\n\t" \
    "v_add_u32 v200, 131072, v200\n\t" \
    "s_add_u32 m0, s89, 0\n\t" \
    "s_nop 0\n\t" \
    "global_load_lds_dwordx4 v1, s[50:51] offset:0\n\t" \
    "s_add_u32 m0, s89, 960\n\t" \
    "s_nop 0\n\t" \
    "global_load_lds_dwordx4 v1, s[50:51] offset:64\n\t" \
    "s_add_u32 m0, s89, 1920\n\t" \
    "s_nop 0\n\t" \
    "global_load_lds_dwordx4 v1, s[50:51] offset:128\n\t" \
    "s_add_u32 m0, s89, 2880\n\t" \
    "s_nop 0\n\t" \
    "global_load_lds_dwordx4 v1, s[50:51] offset:192\n\t" \
    "s_add_u32 m0, s89, 3840\n\t" \
    "s_nop 0\n\t" \
    "global_load_lds_dwordx4 v1, s[50:51] offset:256\n\t" \
    "s_add_u32 m0, s89, 4800\n\t" \
    "s_nop 0\n\t" \
    "global_load_lds_dwordx4 v1, s[50:51] offset:320\n\t" \
    "s_add_u32 m0, s89, 5760\n\t" \
    "s_nop 0\n\t" \
    "global_load_lds_dwordx4 v1, s[50:51] offset:384\n\t" \
    "s_add_u32 m0, s89, 6720\n\t" \
    "s_nop 0\n\t" \
    "global_load_lds_dwordx4 v1, s[50:51] offset:448\n\t" \
    "s_add_u32 m0, s89, 7680\n\t" \
    "s_nop 0\n\t" \
    "global_load_lds_dwordx4 v1, s[50:51] offset:512\n\t" \
    "ds_read_b64_tr_b16 a[32:33], v202 offset:32768\n\t" \
    "ds_read_b64_tr_b16 a[34:35], v202 offset:33792\n\t" \
    "ds_read_b64_tr_b16 a[36:37], v202 offset:40960\n\t" \
    "ds_read_b64_tr_b16 a[38:39], v202 offset:41984\n\t" \
    "ds_read_b64_tr_b16 a[104:105], v203 offset:32768\n\t" \
    "ds_read_b64_tr_b16 a[106:107], v203 offset:33792\n\t" \
    "ds_read_b64_tr_b16 a[108:109], v203 offset:40960\n\t" \
    "ds_read_b64_tr_b16 a[110:111], v203 offset:41984\n\t" \
    "s_waitcnt vmcnt(21) lgkmcnt(0)\n\t" \
    "s_barrier\n\t" \
    "s_add_u32 m0, s96, 73728\n\t" \
    "s_nop 0\n\t" \
    "global_load_lds_dwordx4 v201, s[44:45] offset:0\n\t" \
    "s_add_u32 m0, s96, 74624\n\t" \
    "s_nop 0\n\t" \
    "global_load_lds_dwordx4 v201, s[44:45] offset:128\n\t" \
    "s_add_u32 m0, s96, 75520\n\t" \
    "s_nop 0\n\t" \
    "global_load_lds_dwordx4 v201, s[44:45] offset:256\n\t" \
    "s_add_u32 m0, s96, 76416\n\t" \
    "s_nop 0\n\t" \
    "global_load_lds_dwordx4 v201, s[44:45] offset:384\n\t" \
    "s_add_u32 m0, s96, 77312\n\t" \
    "s_nop 0\n\t" \
    "global_load_lds_dwordx4 v201, s[44:45] offset:512\n\t" \
    "s_add_u32 m0, s96, 78208\n\t" \
    "s_nop 0\n\t" \
    "global_load_lds_dwordx4 v201, s[44:45] offset:640\n\t" \
    "s_add_u32 m0, s96, 79104\n\t" \
    "s_nop 0\n\t" \
    "global_load_lds_dwordx4 v201, s[44:45] offset:768\n\t" \
    "s_add_u32 m0, s96, 80000\n\t" \
    "s_nop 0\n\t" \
    "global_load_lds_dwordx4 v201, s[44:45] offset:896\n\t" \
    "s_add_u32 m0, s96, 80896\n\t" \
    "s_nop 0\n\t" \
    "global_load_lds_dwordx4 v201, s[44:45] offset:1024\n\t" \
    "v_add_u32 v201, 36864, v201\n\t" \
    "ds_read_b64_tr_b16 a[40:41], v202 offset:49152\n\t" \
    "ds_read_b64_tr_b16 a[42:43], v202 offset:50176\n\t" \
    "ds_read_b64_tr_b16 a[44:45], v202 offset:57344\n\t" \
    "ds_read_b64_tr_b16 a[46:47], v202 offset:58368\n\t" \
    "ds_read_b64_tr_b16 a[112:113], v203 offset:49152\n\t" \
    "ds_read_b64_tr_b16 a[114:115], v203 offset:50176\n\t" \
    "ds_read_b64_tr_b16 a[116:117], v203 offset:57344\n\t" \
    "ds_read_b64_tr_b16 a[118:119], v203 offset:58368\n\t" \
    "s_waitcnt vmcnt(26) lgkmcnt(0)\n\t" \
    "s_barrier\n\t" \
    "v_add_u32 v202, 65536, v202\n\t" \
    "v_add_u32 v203, 65536, v203\n\t" \
    "ds_read_b64_tr_b16 a[48:49], v202 offset:0\n\t" \
    "ds_read_b64_tr_b16 a[50:51], v202 offset:1024\n\t" \
    "ds_read_b64_tr_b16 a[52:53], v202 offset:8192\n\t" \
    "ds_read_b64_tr_b16 a[54:55], v202 offset:9216\n\t" \
    "ds_read_b64_tr_b16 a[120:121], v203 offset:0\n\t" \
    "ds_read_b64_tr_b16 a[122:123], v203 offset:1024\n\t" \
    "ds_read_b64_tr_b16 a[124:125], v203 offset:8192\n\t" \
    "ds_read_b64_tr_b16 a[126:127], v203 offset:9216\n\t" \
    "s_waitcnt vmcnt(22) lgkmcnt(0)\n\t" \
    "s_barrier\n\t" \
    "s_add_u32 m0, s96, 110592\n\t" \
    "s_nop 0\n\t" \
    "global_load_lds_dwordx4 v201, s[44:45] offset:0\n\t" \
    "s_add_u32 m0, s96, 111488\n\t" \
    "s_nop 0\n\t" \
    "global_load_lds_dwordx4 v201, s[44:45] offset:128\n\t" \
    "s_add_u32 m0, s96, 112384\n\t" \
    "s_nop 0\n\t" \
    "global_load_lds_dwordx4 v201, s[44:45] offset:256\n\t" \
    "s_add_u32 m0, s96, 113280\n\t" \
    "s_nop 0\n\t" \
    "global_load_lds_dwordx4 v201, s[44:45] offset:384\n\t" \
    "s_add_u32 m0, s96, 114176\n\t" \
    "s_nop 0\n\t" \
    "global_load_lds_dwordx4 v201, s[44:45] offset:512\n\t" \
    "s_add_u32 m0, s96, 115072\n\t" \
    "s_nop 0\n\t" \
    "global_load_lds_dwordx4 v201, s[44:45] offset:640\n\t" \
    "s_add_u32 m0, s96, 115968\n\t" \
    "s_nop 0\n\t" \
    "global_load_lds_dwordx4 v201, s[44:45] offset:768\n\t" \
    "s_add_u32 m0, s96, 116864\n\t" \
    "s_nop 0\n\t" \
    "global_load_lds_dwordx4 v201, s[44:45] offset:896\n\t" \
    "s_add_u32 m0, s96, 117760\n\t" \
    "s_nop 0\n\t" \
    "global_load_lds_dwordx4 v201, s[44:45] offset:1024\n\t" \
    "v_add_u32 v201, 36864, v201\n\t" \
    "v_mbcnt_lo_u32_b32 v164, -1, 0\n\t" \
    "v_mbcnt_hi_u32_b32 v164, -1, v164\n\t" \
    "s_cmp_eq_u32 s66, 0\n\t" \
    "s_cselect_b32 s69, 59, 11\n\t" \
    "v_min_u32 v164, s69, v164\n\t" \
    "v_lshlrev_b32 v164, 6, v164\n\t" \
    "v_add_u32 v164, 6258688, v164\n\t" \
    "v_mov_b32 v165, 6266880\n\t" \
    "global_load_dword v165, v165, s[36:37] sc1\n\t" \
    "global_load_dwordx2 v[166:167], v164, s[36:37] sc1\n\t" \
    "v_add_u32 v202, -98304, v202\n\t" \
    "v_add_u32 v203, -98304, v203\n\t" \
    "ds_read_b64_tr_b16 a[56:57], v202 offset:0\n\t" \
    "ds_read_b64_tr_b16 a[58:59], v202 offset:1024\n\t" \
    "ds_read_b64_tr_b16 a[60:61], v202 offset:8192\n\t" \
    "ds_read_b64_tr_b16 a[62:63], v202 offset:9216\n\t" \
    "ds_read_b64_tr_b16 a[128:129], v203 offset:0\n\t" \
    "ds_read_b64_tr_b16 a[130:131], v203 offset:1024\n\t" \
    "ds_read_b64_tr_b16 a[132:133], v203 offset:8192\n\t" \
    "ds_read_b64_tr_b16 a[134:135], v203 offset:9216\n\t" \
    "s_waitcnt vmcnt(29) lgkmcnt(0)\n\t" \
    "s_barrier\n\t" \
    "ds_read_b64_tr_b16 a[64:65], v202 offset:16384\n\t" \
    "ds_read_b64_tr_b16 a[66:67], v202 offset:17408\n\t" \
    "ds_read_b64_tr_b16 a[68:69], v202 offset:24576\n\t" \
    "ds_read_b64_tr_b16 a[70:71], v202 offset:25600\n\t" \
    "ds_read_b64_tr_b16 a[136:137], v203 offset:16384\n\t" \
    "ds_read_b64_tr_b16 a[138:139], v203 offset:17408\n\t" \
    "ds_read_b64_tr_b16 a[140:141], v203 offset:24576\n\t" \
    "ds_read_b64_tr_b16 a[142:143], v203 offset:25600\n\t" \
    "s_waitcnt vmcnt(11) lgkmcnt(0)\n\t" \
    "s_barrier\n\t" \
    "s_add_u32 m0, s96, 36864\n\t" \
    "s_nop 0\n\t" \
    "global_load_lds_dwordx4 v201, s[44:45] offset:0\n\t" \
    "s_add_u32 m0, s96, 37760\n\t" \
    "s_nop 0\n\t" \
    "global_load_lds_dwordx4 v201, s[44:45] offset:128\n\t" \
    "s_add_u32 m0, s96, 38656\n\t" \
    "s_nop 0\n\t" \
    "global_load_lds_dwordx4 v201, s[44:45] offset:256\n\t" \
    "s_add_u32 m0, s96, 39552\n\t" \
    "s_nop 0\n\t" \
    "global_load_lds_dwordx4 v201, s[44:45] offset:384\n\t" \
    "s_add_u32 m0, s96, 40448\n\t" \
    "s_nop 0\n\t" \
    "global_load_lds_dwordx4 v201, s[44:45] offset:512\n\t" \
    "s_add_u32 m0, s96, 41344\n\t" \
    "s_nop 0\n\t" \
    "global_load_lds_dwordx4 v201, s[44:45] offset:640\n\t" \
    "s_add_u32 m0, s96, 42240\n\t" \
    "s_nop 0\n\t" \
    "global_load_lds_dwordx4 v201, s[44:45] offset:768\n\t" \
    "s_add_u32 m0, s96, 43136\n\t" \
    "s_nop 0\n\t" \
    "global_load_lds_dwordx4 v201, s[44:45] offset:896\n\t" \
    "s_add_u32 m0, s96, 44032\n\t" \
    "s_nop 0\n\t" \
    "global_load_lds_dwordx4 v201, s[44:45] offset:1024\n\t" \
    "v_add_u32 v201, 36864, v201\n\t" \
    "v_add_u32 v204, 73728, v204\n\t" \
    "v_add_u32 v205, 73728, v205\n\t" \
    "v_add_u32 v206, 73728, v206\n\t" \
    "v_add_u32 v207, 73728, v207\n\t" \
    "ds_read_b64_tr_b16 a[144:145], v204 offset:0\n\t" \
    "ds_read_b64_tr_b16 a[146:147], v204 offset:18432\n\t" \
    "ds_read_b64_tr_b16 a[160:161], v205 offset:0\n\t" \
    "ds_read_b64_tr_b16 a[162:163], v205 offset:18432\n\t" \
    "ds_read_b64_tr_b16 a[176:177], v206 offset:0\n\t" \
    "ds_read_b64_tr_b16 a[178:179], v206 offset:18432\n\t" \
    "s_waitcnt lgkmcnt(6)\n\t" \
    "ds_read_b64_tr_b16 a[192:193], v207 offset:0\n\t" \
    "ds_read_b64_tr_b16 a[194:195], v207 offset:18432\n\t" \
    "ds_read_b64_tr_b16 a[208:209], v204 offset:1024\n\t" \
    "ds_read_b64_tr_b16 a[210:211], v204 offset:19456\n\t" \
    "ds_read_b64_tr_b16 a[224:225], v205 offset:1024\n\t" \
    "ds_read_b64_tr_b16 a[226:227], v205 offset:19456\n\t" \
    "s_waitcnt lgkmcnt(6)\n\t" \
    "ds_read_b64_tr_b16 a[240:241], v206 offset:1024\n\t" \
    "ds_read_b64_tr_b16 a[242:243], v206 offset:19456\n\t" \
    "ds_read_b64_tr_b16 v[224:225], v207 offset:1024\n\t" \
    "ds_read_b64_tr_b16 v[226:227], v207 offset:19456\n\t" \
    "ds_read_b64_tr_b16 v[240:241], v204 offset:2048\n\t" \
    "ds_read_b64_tr_b16 v[242:243], v204 offset:20480\n\t" \
    "s_waitcnt lgkmcnt(6)\n\t" \
    "s_waitcnt lgkmcnt(6)\n\t" \
    "ds_read_b128 v[24:27], v2 offset:0\n\t" \
    "ds_read_b128 v[28:31], v2 offset:18432\n\t" \
    "ds_read_b128 v[32:35], v2 offset:1024\n\t" \
    "ds_read_b128 v[36:39], v2 offset:19456\n\t" \
    "s_waitcnt lgkmcnt(3)\n\t" \
    "v_mfma_f32_16x16x32_bf16 v[72:75], a[0:3], v[24:27], 0\n\t" \
    "ds_read_b128 v[40:43], v2 offset:2048\n\t" \
    "s_waitcnt lgkmcnt(3)\n\t" \
    "v_mfma_f32_16x16x32_bf16 v[76:79], a[0:3], v[28:31], 0\n\t" \
    "ds_read_b128 v[44:47], v2 offset:20480\n\t" \
    "v_mfma_f32_16x16x32_bf16 v[80:83], a[72:75], v[24:27], 0\n\t" \
    "v_mfma_f32_16x16x32_bf16 v[84:87], a[72:75], v[28:31], 0\n\t" \
    "s_waitcnt lgkmcnt(3)\n\t" \
    "v_mfma_f32_16x16x32_bf16 v[72:75], a[4:7], v[32:35], v[72:75]\n\t" \
    "ds_read_b128 v[48:51], v2 offset:3072\n\t" \
    "s_waitcnt lgkmcnt(3)\n\t" \
    "v_mfma_f32_16x16x32_bf16 v[76:79], a[4:7], v[36:39], v[76:79]\n\t" \
    "ds_read_b128 v[52:55], v2 offset:21504\n\t" \
    "v_mfma_f32_16x16x32_bf16 v[80:83], a[76:79], v[32:35], v[80:83]\n\t" \
    "v_mfma_f32_16x16x32_bf16 v[84:87], a[76:79], v[36:39], v[84:87]\n\t" \
    "s_waitcnt lgkmcnt(3)\n\t" \
    "v_mfma_f32_16x16x32_bf16 v[72:75], a[8:11], v[40:43], v[72:75]\n\t" \
    "ds_read_b128 v[56:59], v2 offset:4096\n\t" \
    "s_waitcnt lgkmcnt(3)\n\t" \
    "v_mfma_f32_16x16x32_bf16 v[76:79], a[8:11], v[44:47], v[76:79]\n\t" \
    "ds_read_b128 v[60:63], v2 offset:22528\n\t" \
    "v_mfma_f32_16x16x32_bf16 v[80:83], a[80:83], v[40:43], v[80:83]\n\t" \
    "v_mfma_f32_16x16x32_bf16 v[84:87], a[80:83], v[44:47], v[84:87]\n\t" \
    "s_waitcnt lgkmcnt(3)\n\t" \
    "v_mfma_f32_16x16x32_bf16 v[72:75], a[12:15], v[48:51], v[72:75]\n\t" \
    "ds_read_b128 v[64:67], v2 offset:5120\n\t" \
    "s_waitcnt lgkmcnt(3)\n\t" \
    "v_mfma_f32_16x16x32_bf16 v[76:79], a[12:15], v[52:55], v[76:79]\n\t" \
    "ds_read_b128 v[68:71], v2 offset:23552\n\t" \
    "v_mfma_f32_16x16x32_bf16 v[80:83], a[84:87], v[48:51], v[80:83]\n\t" \
    "v_mfma_f32_16x16x32_bf16 v[84:87], a[84:87], v[52:55], v[84:87]\n\t" \
    "s_waitcnt lgkmcnt(3)\n\t" \
    "v_mfma_f32_16x16x32_bf16 v[72:75], a[16:19], v[56:59], v[72:75]\n\t" \
    "s_waitcnt lgkmcnt(2)\n\t" \
    "v_mfma_f32_16x16x32_bf16 v[76:79], a[16:19], v[60:63], v[76:79]\n\t" \
    "v_mfma_f32_16x16x32_bf16 v[80:83], a[88:91], v[56:59], v[80:83]\n\t" \
    "v_mfma_f32_16x16x32_bf16 v[84:87], a[88:91], v[60:63], v[84:87]\n\t" \
    "s_waitcnt lgkmcnt(1)\n\t" \
    "v_mfma_f32_16x16x32_bf16 v[72:75], a[20:23], v[64:67], v[72:75]\n\t" \
    "s_waitcnt lgkmcnt(0)\n\t" \
    "v_mfma_f32_16x16x32_bf16 v[76:79], a[20:23], v[68:71], v[76:79]\n\t" \
    "v_mfma_f32_16x16x32_bf16 v[80:83], a[92:95], v[64:67], v[80:83]\n\t" \
    "v_mfma_f32_16x16x32_bf16 v[84:87], a[92:95], v[68:71], v[84:87]\n\t" \
    "s_waitcnt vmcnt(11) lgkmcnt(0)\n\t" \
    "s_barrier\n\t" \
    "s_add_u32 m0, s96, 73728\n\t" \
    "s_nop 0\n\t" \
    "global_load_lds_dwordx4 v201, s[44:45] offset:0\n\t" \
    "s_add_u32 m0, s96, 74624\n\t" \
    "s_nop 0\n\t" \
    "global_load_lds_dwordx4 v201, s[44:45] offset:128\n\t" \
    "s_add_u32 m0, s96, 75520\n\t" \
    "s_nop 0\n\t" \
    "global_load_lds_dwordx4 v201, s[44:45] offset:256\n\t" \
    "s_add_u32 m0, s96, 76416\n\t" \
    "s_nop 0\n\t" \
    "global_load_lds_dwordx4 v201, s[44:45] offset:384\n\t" \
    "s_add_u32 m0, s96, 77312\n\t" \
    "s_nop 0\n\t" \
    "global_load_lds_dwordx4 v201, s[44:45] offset:512\n\t" \
    "s_add_u32 m0, s96, 78208\n\t" \
    "s_nop 0\n\t" \
    "global_load_lds_dwordx4 v201, s[44:45] offset:640\n\t" \
    "s_add_u32 m0, s96, 79104\n\t" \
    "s_nop 0\n\t" \
    "global_load_lds_dwordx4 v201, s[44:45] offset:768\n\t" \
    "s_add_u32 m0, s96, 80000\n\t" \
    "s_nop 0\n\t" \
    "global_load_lds_dwordx4 v201, s[44:45] offset:896\n\t" \
    "s_add_u32 m0, s96, 80896\n\t" \
    "s_nop 0\n\t" \
    "global_load_lds_dwordx4 v201, s[44:45] offset:1024\n\t" \
    "v_add_u32 v201, 36864, v201\n\t" \
    "ds_read_b64_tr_b16 a[148:149], v204 offset:36864\n\t" \
    "ds_read_b64_tr_b16 a[150:151], v204 offset:55296\n\t" \
    "ds_read_b64_tr_b16 a[164:165], v205 offset:36864\n\t" \
    "ds_read_b64_tr_b16 a[166:167], v205 offset:55296\n\t" \
    "ds_read_b64_tr_b16 a[180:181], v206 offset:36864\n\t" \
    "ds_read_b64_tr_b16 a[182:183], v206 offset:55296\n\t" \
    "s_waitcnt lgkmcnt(6)\n\t" \
    "ds_read_b64_tr_b16 a[196:197], v207 offset:36864\n\t" \
    "ds_read_b64_tr_b16 a[198:199], v207 offset:55296\n\t" \
    "ds_read_b64_tr_b16 a[212:213], v204 offset:37888\n\t" \
    "ds_read_b64_tr_b16 a[214:215], v204 offset:56320\n\t" \
    "ds_read_b64_tr_b16 a[228:229], v205 offset:37888\n\t" \
    "ds_read_b64_tr_b16 a[230:231], v205 offset:56320\n\t" \
    "s_waitcnt lgkmcnt(6)\n\t" \
    "ds_read_b64_tr_b16 a[244:245], v206 offset:37888\n\t" \
    "ds_read_b64_tr_b16 a[246:247], v206 offset:56320\n\t" \
    "ds_read_b64_tr_b16 v[228:229], v207 offset:37888\n\t" \
    "ds_read_b64_tr_b16 v[230:231], v207 offset:56320\n\t" \
    "ds_read_b64_tr_b16 v[244:245], v204 offset:38912\n\t" \
    "ds_read_b64_tr_b16 v[246:247], v204 offset:57344\n\t" \
    "s_waitcnt lgkmcnt(6)\n\t" \
    "s_waitcnt lgkmcnt(6)\n\t" \
    "ds_read_b128 v[24:27], v2 offset:6144\n\t" \
    "ds_read_b128 v[28:31], v2 offset:24576\n\t" \
    "ds_read_b128 v[32:35], v2 offset:7168\n\t" \
    "ds_read_b128 v[36:39], v2 offset:25600\n\t" \
    "s_waitcnt lgkmcnt(3)\n\t" \
    "v_mfma_f32_16x16x32_bf16 v[72:75], a[24:27], v[24:27], v[72:75]\n\t" \
    "ds_read_b128 v[40:43], v2 offset:8192\n\t" \
    "s_waitcnt lgkmcnt(3)\n\t" \
    "v_mfma_f32_16x16x32_bf16 v[76:79], a[24:27], v[28:31], v[76:79]\n\t" \
    "ds_read_b128 v[44:47], v2 offset:26624\n\t" \
    "v_mfma_f32_16x16x32_bf16 v[80:83], a[96:99], v[24:27], v[80:83]\n\t" \
    "v_mfma_f32_16x16x32_bf16 v[84:87], a[96:99], v[28:31], v[84:87]\n\t" \
    "s_waitcnt lgkmcnt(3)\n\t" \
    "v_mfma_f32_16x16x32_bf16 v[72:75], a[28:31], v[32:35], v[72:75]\n\t" \
    "ds_read_b128 v[48:51], v2 offset:9216\n\t" \
    "s_waitcnt lgkmcnt(3)\n\t" \
    "v_mfma_f32_16x16x32_bf16 v[76:79], a[28:31], v[36:39], v[76:79]\n\t" \
    "ds_read_b128 v[52:55], v2 offset:27648\n\t" \
    "v_mfma_f32_16x16x32_bf16 v[80:83], a[100:103], v[32:35], v[80:83]\n\t" \
    "v_mfma_f32_16x16x32_bf16 v[84:87], a[100:103], v[36:39], v[84:87]\n\t" \
    "s_waitcnt lgkmcnt(3)\n\t" \
    "v_mfma_f32_16x16x32_bf16 v[72:75], a[32:35], v[40:43], v[72:75]\n\t" \
    "ds_read_b128 v[56:59], v2 offset:10240\n\t" \
    "s_waitcnt lgkmcnt(3)\n\t" \
    "v_mfma_f32_16x16x32_bf16 v[76:79], a[32:35], v[44:47], v[76:79]\n\t" \
    "ds_read_b128 v[60:63], v2 offset:28672\n\t" \
    "v_mfma_f32_16x16x32_bf16 v[80:83], a[104:107], v[40:43], v[80:83]\n\t" \
    "v_mfma_f32_16x16x32_bf16 v[84:87], a[104:107], v[44:47], v[84:87]\n\t" \
    "s_waitcnt lgkmcnt(3)\n\t" \
    "v_mfma_f32_16x16x32_bf16 v[72:75], a[36:39], v[48:51], v[72:75]\n\t" \
    "ds_read_b128 v[64:67], v2 offset:11264\n\t" \
    "s_waitcnt lgkmcnt(3)\n\t" \
    "v_mfma_f32_16x16x32_bf16 v[76:79], a[36:39], v[52:55], v[76:79]\n\t" \
    "ds_read_b128 v[68:71], v2 offset:29696\n\t" \
    "v_mfma_f32_16x16x32_bf16 v[80:83], a[108:111], v[48:51], v[80:83]\n\t" \
    "v_mfma_f32_16x16x32_bf16 v[84:87], a[108:111], v[52:55], v[84:87]\n\t" \
    "s_waitcnt lgkmcnt(3)\n\t" \
    "v_mfma_f32_16x16x32_bf16 v[72:75], a[40:43], v[56:59], v[72:75]\n\t" \
    "s_waitcnt lgkmcnt(2)\n\t" \
    "v_mfma_f32_16x16x32_bf16 v[76:79], a[40:43], v[60:63], v[76:79]\n\t" \
    "v_mfma_f32_16x16x32_bf16 v[80:83], a[112:115], v[56:59], v[80:83]\n\t" \
    "v_mfma_f32_16x16x32_bf16 v[84:87], a[112:115], v[60:63], v[84:87]\n\t" \
    "s_waitcnt lgkmcnt(1)\n\t" \
    "v_mfma_f32_16x16x32_bf16 v[72:75], a[44:47], v[64:67], v[72:75]\n\t" \
    "s_waitcnt lgkmcnt(0)\n\t" \
    "v_mfma_f32_16x16x32_bf16 v[76:79], a[44:47], v[68:71], v[76:79]\n\t" \
    "v_mfma_f32_16x16x32_bf16 v[80:83], a[116:119], v[64:67], v[80:83]\n\t" \
    "v_mfma_f32_16x16x32_bf16 v[84:87], a[116:119], v[68:71], v[84:87]\n\t" \
    "s_waitcnt vmcnt(9) lgkmcnt(0)\n\t" \
    "s_barrier\n\t" \
    "s_add_u32 s50, s50, 73728\n\t" \
    "s_addc_u32 s51, s51, 0\n\t" \
    "s_add_u32 m0, s89, 110592\n\t" \
    "s_nop 0\n\t" \
    "global_load_lds_dwordx4 v1, s[50:51] offset:0\n\t" \
    "s_add_u32 m0, s89, 111552\n\t" \
    "s_nop 0\n\t" \
    "global_load_lds_dwordx4 v1, s[50:51] offset:64\n\t" \
    "s_add_u32 m0, s89, 112512\n\t" \
    "s_nop 0\n\t" \
    "global_load_lds_dwordx4 v1, s[50:51] offset:128\n\t" \
    "s_add_u32 m0, s89, 113472\n\t" \
    "s_nop 0\n\t" \
    "global_load_lds_dwordx4 v1, s[50:51] offset:192\n\t" \
    "s_add_u32 m0, s89, 114432\n\t" \
    "s_nop 0\n\t" \
    "global_load_lds_dwordx4 v1, s[50:51] offset:256\n\t" \
    "s_add_u32 m0, s89, 115392\n\t" \
    "s_nop 0\n\t" \
    "global_load_lds_dwordx4 v1, s[50:51] offset:320\n\t" \
    "s_add_u32 m0, s89, 116352\n\t" \
    "s_nop 0\n\t" \
    "global_load_lds_dwordx4 v1, s[50:51] offset:384\n\t" \
    "s_add_u32 m0, s89, 117312\n\t" \
    "s_nop 0\n\t" \
    "global_load_lds_dwordx4 v1, s[50:51] offset:448\n\t" \
    "s_add_u32 m0, s89, 118272\n\t" \
    "s_nop 0\n\t" \
    "global_load_lds_dwordx4 v1, s[50:51] offset:512\n\t" \
    "v_add_u32 v204, -36864, v204\n\t" \
    "v_add_u32 v205, -36864, v205\n\t" \
    "v_add_u32 v206, -36864, v206\n\t" \
    "v_add_u32 v207, -36864, v207\n\t" \
    "ds_read_b64_tr_b16 a[152:153], v204 offset:0\n\t" \
    "ds_read_b64_tr_b16 a[154:155], v204 offset:18432\n\t" \
    "ds_read_b64_tr_b16 a[168:169], v205 offset:0\n\t" \
    "ds_read_b64_tr_b16 a[170:171], v205 offset:18432\n\t" \
    "ds_read_b64_tr_b16 a[184:185], v206 offset:0\n\t" \
    "ds_read_b64_tr_b16 a[186:187], v206 offset:18432\n\t" \
    "s_waitcnt lgkmcnt(6)\n\t" \
    "ds_read_b64_tr_b16 a[200:201], v207 offset:0\n\t" \
    "ds_read_b64_tr_b16 a[202:203], v207 offset:18432\n\t" \
    "ds_read_b64_tr_b16 a[216:217], v204 offset:1024\n\t" \
    "ds_read_b64_tr_b16 a[218:219], v204 offset:19456\n\t" \
    "ds_read_b64_tr_b16 a[232:233], v205 offset:1024\n\t" \
    "ds_read_b64_tr_b16 a[234:235], v205 offset:19456\n\t" \
    "s_waitcnt lgkmcnt(6)\n\t" \
    "ds_read_b64_tr_b16 a[248:249], v206 offset:1024\n\t" \
    "ds_read_b64_tr_b16 a[250:251], v206 offset:19456\n\t" \
    "ds_read_b64_tr_b16 v[232:233], v207 offset:1024\n\t" \
    "ds_read_b64_tr_b16 v[234:235], v207 offset:19456\n\t" \
    "ds_read_b64_tr_b16 v[248:249], v204 offset:2048\n\t" \
    "ds_read_b64_tr_b16 v[250:251], v204 offset:20480\n\t" \
    "s_waitcnt lgkmcnt(6)\n\t" \
    "s_waitcnt lgkmcnt(6)\n\t" \
    "ds_read_b128 v[24:27], v2 offset:12288\n\t" \
    "ds_read_b128 v[28:31], v2 offset:30720\n\t" \
    "ds_read_b128 v[32:35], v2 offset:13312\n\t" \
    "ds_read_b128 v[36:39], v2 offset:31744\n\t" \
    "s_waitcnt lgkmcnt(3)\n\t" \
    "v_mfma_f32_16x16x32_bf16 v[72:75], a[48:51], v[24:27], v[72:75]\n\t" \
    "ds_read_b128 v[40:43], v2 offset:14336\n\t" \
    "s_waitcnt lgkmcnt(3)\n\t" \
    "v_mfma_f32_16x16x32_bf16 v[76:79], a[48:51], v[28:31], v[76:79]\n\t" \
    "ds_read_b128 v[44:47], v2 offset:32768\n\t" \
    "v_mfma_f32_16x16x32_bf16 v[80:83], a[120:123], v[24:27], v[80:83]\n\t" \
    "v_mfma_f32_16x16x32_bf16 v[84:87], a[120:123], v[28:31], v[84:87]\n\t" \
    "s_waitcnt lgkmcnt(3)\n\t" \
    "v_mfma_f32_16x16x32_bf16 v[72:75], a[52:55], v[32:35], v[72:75]\n\t" \
    "ds_read_b128 v[48:51], v2 offset:15360\n\t" \
    "s_waitcnt lgkmcnt(3)\n\t" \
    "v_mfma_f32_16x16x32_bf16 v[76:79], a[52:55], v[36:39], v[76:79]\n\t" \
    "ds_read_b128 v[52:55], v2 offset:33792\n\t" \
    "v_mfma_f32_16x16x32_bf16 v[80:83], a[124:127], v[32:35], v[80:83]\n\t" \
    "v_mfma_f32_16x16x32_bf16 v[84:87], a[124:127], v[36:39], v[84:87]\n\t" \
    "s_waitcnt lgkmcnt(3)\n\t" \
    "v_mfma_f32_16x16x32_bf16 v[72:75], a[56:59], v[40:43], v[72:75]\n\t" \
    "ds_read_b128 v[56:59], v2 offset:16384\n\t" \
    "s_waitcnt lgkmcnt(3)\n\t" \
    "v_mfma_f32_16x16x32_bf16 v[76:79], a[56:59], v[44:47], v[76:79]\n\t" \
    "ds_read_b128 v[60:63], v2 offset:34816\n\t" \
    "v_mfma_f32_16x16x32_bf16 v[80:83], a[128:131], v[40:43], v[80:83]\n\t" \
    "v_mfma_f32_16x16x32_bf16 v[84:87], a[128:131], v[44:47], v[84:87]\n\t" \
    "s_waitcnt lgkmcnt(3)\n\t" \
    "v_mfma_f32_16x16x32_bf16 v[72:75], a[60:63], v[48:51], v[72:75]\n\t" \
    "ds_read_b128 v[64:67], v2 offset:17408\n\t" \
    "s_waitcnt lgkmcnt(3)\n\t" \
    "v_mfma_f32_16x16x32_bf16 v[76:79], a[60:63], v[52:55], v[76:79]\n\t" \
    "ds_read_b128 v[68:71], v2 offset:35840\n\t" \
    "v_mfma_f32_16x16x32_bf16 v[80:83], a[132:135], v[48:51], v[80:83]\n\t" \
    "v_mfma_f32_16x16x32_bf16 v[84:87], a[132:135], v[52:55], v[84:87]\n\t" \
    "s_waitcnt lgkmcnt(3)\n\t" \
    "v_mfma_f32_16x16x32_bf16 v[72:75], a[64:67], v[56:59], v[72:75]\n\t" \
    "s_waitcnt lgkmcnt(2)\n\t" \
    "v_mfma_f32_16x16x32_bf16 v[76:79], a[64:67], v[60:63], v[76:79]\n\t" \
    "v_mfma_f32_16x16x32_bf16 v[80:83], a[136:139], v[56:59], v[80:83]\n\t" \
    "v_mfma_f32_16x16x32_bf16 v[84:87], a[136:139], v[60:63], v[84:87]\n\t" \
    "s_waitcnt lgkmcnt(1)\n\t" \
    "v_mfma_f32_16x16x32_bf16 v[72:75], a[68:71], v[64:67], v[72:75]\n\t" \
    "s_waitcnt lgkmcnt(0)\n\t" \
    "v_mfma_f32_16x16x32_bf16 v[76:79], a[68:71], v[68:71], v[76:79]\n\t" \
    "v_mfma_f32_16x16x32_bf16 v[80:83], a[140:143], v[64:67], v[80:83]\n\t" \
    "v_mfma_f32_16x16x32_bf16 v[84:87], a[140:143], v[68:71], v[84:87]\n\t" \
    "s_waitcnt vmcnt(9) lgkmcnt(0)\n\t" \
    "s_barrier\n\t" \
    "ds_read_b64_tr_b16 a[156:157], v204 offset:36864\n\t" \
    "ds_read_b64_tr_b16 a[158:159], v204 offset:55296\n\t" \
    "ds_read_b64_tr_b16 a[172:173], v205 offset:36864\n\t" \
    "ds_read_b64_tr_b16 a[174:175], v205 offset:55296\n\t" \
    "ds_read_b64_tr_b16 a[188:189], v206 offset:36864\n\t" \
    "ds_read_b64_tr_b16 a[190:191], v206 offset:55296\n\t" \
    "s_waitcnt lgkmcnt(6)\n\t" \
    "ds_read_b64_tr_b16 a[204:205], v207 offset:36864\n\t" \
    "ds_read_b64_tr_b16 a[206:207], v207 offset:55296\n\t" \
    "ds_read_b64_tr_b16 a[220:221], v204 offset:37888\n\t" \
    "ds_read_b64_tr_b16 a[222:223], v204 offset:56320\n\t" \
    "ds_read_b64_tr_b16 a[236:237], v205 offset:37888\n\t" \
    "ds_read_b64_tr_b16 a[238:239], v205 offset:56320\n\t" \
    "s_waitcnt lgkmcnt(6)\n\t" \
    "ds_read_b64_tr_b16 a[252:253], v206 offset:37888\n\t" \
    "ds_read_b64_tr_b16 a[254:255], v206 offset:56320\n\t" \
    "ds_read_b64_tr_b16 v[236:237], v207 offset:37888\n\t" \
    "ds_read_b64_tr_b16 v[238:239], v207 offset:56320\n\t" \
    "ds_read_b64_tr_b16 v[252:253], v204 offset:38912\n\t" \
    "ds_read_b64_tr_b16 v[254:255], v204 offset:57344\n\t" \
    "s_waitcnt lgkmcnt(6)\n\t" \
    "s_waitcnt lgkmcnt(0)\n\t" \
    "s_barrier\n\t" \
    "v_sub_u32 v166, v166, v165\n\t" \
    "v_cmp_ne_u32 vcc, 1, v166\n\t" \
    "s_cbranch_vccz gv_Cok2_%=\n\t" \
    "gv_C1_%=:\n\t" \
    "global_load_dwordx2 v[166:167], v164, s[36:37] sc1\n\t" \
    "s_waitcnt vmcnt(0)\n\t" \
    "v_sub_u32 v166, v166, v165\n\t" \
    "v_cmp_ne_u32 vcc, 1, v166\n\t" \
    "s_cbranch_vccz gv_Cok2_%=\n\t" \
    "s_cmp_eq_u32 s68, 0\n\t" \
    "s_cbranch_scc1 gv_F3_%=\n\t" \
    "s_sub_u32 s68, s68, 1\n\t" \
    "s_sleep 4\n\t" \
    "s_branch gv_C1_%=\n\t" \
    "gv_F3_%=:\n\t" \
    "v_mov_b32 v168, 6095104\n\t" \
    "v_mov_b32 v169, 4\n\t" \
    "global_store_dword v168, v169, s[36:37] sc1\n\t" \
    "s_waitcnt vmcnt(0)\n\t" \
    "s_branch gv_Cok2_%=\n\t" \
    "gv_Cok2_%=:\n\t" \
    "s_cmp_lg_u32 s66, 0\n\t" \
    "s_cbranch_scc1 gv_Ca4_%=\n\t" \
    "v_mov_b32 v164, 6176768\n\t" \
    "s_mov_b64 exec, 1\n\t" \
    "global_atomic_add v164, v166, s[36:37] sc1\n\t" \
    "s_mov_b64 exec, -1\n\t" \
    "gv_Ca4_%=:\n\t" \
    "v_min_u32_dpp v167, v167, v167 row_shl:6 row_mask:0xf bank_mask:0xf\n\t" \
    "s_nop 0\n\t" \
    "v_readlane_b32 s69, v167, 4\n\t" \
    "v_readlane_b32 s70, v167, 5\n\t" \
    "s_nop 1\n\t" \
    "s_cmp_lt_u32 s70, s69\n\t" \
    "s_cselect_b32 s52, 8192, 0\n\t" \
    "s_xor_b32 s52, s52, 8192\n\t" \
    "v_xor_b32 v20, s52, v20\n\t" \
    "s_lshr_b32 s69, s67, 5\n\t" \
    "s_or_b32 s69, s69, s66\n\t" \
    "s_cbranch_scc1 gv_npr5_%=\n\t" \
    "s_memrealtime s[54:55]\n\t" \
    "s_waitcnt lgkmcnt(0)\n\t" \
    "s_lshl_b32 s70, s54, 4\n\t" \
    "s_add_u32 s70, s70, 4160\n\t" \
    "v_mov_b32 v157, s70\n\t" \
    "global_store_dword v19, v157, s[36:37] sc1\n\t" \
    "gv_npr5_%=:\n\t" \
    "s_cmp_lt_u32 s65, 60\n\t" \
    "s_cselect_b32 s69, 73728, 0\n\t" \
    "s_add_u32 s50, s50, s69\n\t" \
    "s_addc_u32 s51, s51, 0\n\t" \
    "s_sub_u32 s90, s90, 36864\n\t" \
    "s_cselect_b32 s90, 110592, s90\n\t" \
    "s_sub_u32 s91, s90, 36864\n\t" \
    "s_cselect_b32 s91, 110592, s91\n\t" \
    "s_add_u32 s91, s91, s89\n\t" \
    "v_add_u32 v17, s90, v3\n\t" \
    "s_lshl_b32 s40, s65, 15\n\t" \
    "v_mov_b32 v2, v17\n\t" \
    "s_mov_b64 s[46:47], 0\n\t" \
    "s_branch gv_mid8_%=\n\t" \
    ".p2align 6\n\t" \
    "s_nop 0\n\t" \
    "s_nop 0\n\t" \
    "gv_loop6_%=:\n\t" \
    "s_waitcnt lgkmcnt(5)\n\t" \
    "v_mfma_f32_16x16x32_bf16 v[72:75], a[0:3], v[24:27], 0\n\t" \
    "ds_read_b128 v[48:51], v2 offset:3072\n\t" \
    "s_waitcnt lgkmcnt(5)\n\t" \
    "v_mfma_f32_16x16x32_bf16 v[76:79], a[0:3], v[28:31], 0\n\t" \
    "ds_read_b128 v[52:55], v2 offset:21504\n\t" \
    "v_mfma_f32_16x16x32_bf16 v[80:83], a[72:75], v[24:27], 0\n\t" \
    "s_cmp_lt_u32 s65, 60\n\t" \
    "s_cselect_b32 s69, 73728, 0\n\t" \
    "s_add_u32 s50, s50, s69\n\t" \
    "v_mfma_f32_16x16x32_bf16 v[84:87], a[72:75], v[28:31], 0\n\t" \
    "s_addc_u32 s51, s51, 0\n\t" \
    "s_sub_u32 s90, s90, 36864\n\t" \
    "s_cselect_b32 s90, 110592, s90\n\t" \
    "s_waitcnt lgkmcnt(5)\n\t" \
    "v_mfma_f32_16x16x32_bf16 v[72:75], a[4:7], v[32:35], v[72:75]\n\t" \
    "ds_read_b128 v[56:59], v2 offset:4096\n\t" \
    "s_waitcnt lgkmcnt(5)\n\t" \
    "v_mfma_f32_16x16x32_bf16 v[76:79], a[4:7], v[36:39], v[76:79]\n\t" \
    "ds_read_b128 v[60:63], v2 offset:22528\n\t" \
    "v_mfma_f32_16x16x32_bf16 v[80:83], a[76:79], v[32:35], v[80:83]\n\t" \
    "s_sub_u32 s91, s90, 36864\n\t" \
    "s_cselect_b32 s91, 110592, s91\n\t" \
    "s_add_u32 s91, s91, s89\n\t" \
    "v_mfma_f32_16x16x32_bf16 v[84:87], a[76:79], v[36:39], v[84:87]\n\t" \
    "v_add_u32 v17, s90, v3\n\t" \
    "s_lshl_b32 s40, s65, 15\n\t" \
    "s_sub_u32 s70, s65, 4\n\t" \
    "s_waitcnt lgkmcnt(5)\n\t" \
    "v_mfma_f32_16x16x32_bf16 v[72:75], a[8:11], v[40:43], v[72:75]\n\t" \
    "ds_read_b128 v[64:67], v2 offset:5120\n\t" \
    "s_waitcnt lgkmcnt(5)\n\t" \
    "v_mfma_f32_16x16x32_bf16 v[76:79], a[8:11], v[44:47], v[76:79]\n\t" \
    "ds_read_b128 v[68:71], v2 offset:23552\n\t" \
    "v_mfma_f32_16x16x32_bf16 v[80:83], a[80:83], v[40:43], v[80:83]\n\t" \
    "v_mov_b32 v157, s70\n\t" \
    "s_cmp_lt_u32 s65, 10\n\t" \
    "s_cselect_b64 s[46:47], 0, s[58:59]\n\t" \
    "v_mfma_f32_16x16x32_bf16 v[84:87], a[80:83], v[44:47], v[84:87]\n\t" \
    "s_waitcnt lgkmcnt(5)\n\t" \
    "v_mfma_f32_16x16x32_bf16 v[72:75], a[12:15], v[48:51], v[72:75]\n\t" \
    "ds_read_b128 v[24:27], v2 offset:6144\n\t" \
    "s_waitcnt lgkmcnt(5)\n\t" \
    "v_mfma_f32_16x16x32_bf16 v[76:79], a[12:15], v[52:55], v[76:79]\n\t" \
    "ds_read_b128 v[28:31], v2 offset:24576\n\t" \
    "v_mfma_f32_16x16x32_bf16 v[80:83], a[84:87], v[48:51], v[80:83]\n\t" \
    "s_add_u32 m0, s91, 0\n\t" \
    "v_mfma_f32_16x16x32_bf16 v[84:87], a[84:87], v[52:55], v[84:87]\n\t" \
    "global_load_lds_dwordx4 v1, s[50:51] offset:0\n\t" \
    "s_waitcnt lgkmcnt(5)\n\t" \
    "v_mfma_f32_16x16x32_bf16 v[72:75], a[16:19], v[56:59], v[72:75]\n\t" \
    "ds_read_b128 v[32:35], v2 offset:7168\n\t" \
    "s_waitcnt lgkmcnt(5)\n\t" \
    "v_mfma_f32_16x16x32_bf16 v[76:79], a[16:19], v[60:63], v[76:79]\n\t" \
    "ds_read_b128 v[36:39], v2 offset:25600\n\t" \
    "v_mfma_f32_16x16x32_bf16 v[80:83], a[88:91], v[56:59], v[80:83]\n\t" \
    "s_add_u32 m0, s91, 960\n\t" \
    "v_mfma_f32_16x16x32_bf16 v[84:87], a[88:91], v[60:63], v[84:87]\n\t" \
    "global_load_lds_dwordx4 v1, s[50:51] offset:64\n\t" \
    "s_waitcnt lgkmcnt(5)\n\t" \
    "v_mfma_f32_16x16x32_bf16 v[72:75], a[20:23], v[64:67], v[72:75]\n\t" \
    "ds_read_b128 v[40:43], v2 offset:8192\n\t" \
    "s_waitcnt lgkmcnt(5)\n\t" \
    "v_mfma_f32_16x16x32_bf16 v[76:79], a[20:23], v[68:71], v[76:79]\n\t" \
    "ds_read_b128 v[44:47], v2 offset:26624\n\t" \
    "v_mfma_f32_16x16x32_bf16 v[80:83], a[92:95], v[64:67], v[80:83]\n\t" \
    "s_add_u32 m0, s91, 1920\n\t" \
    "v_mfma_f32_16x16x32_bf16 v[84:87], a[92:95], v[68:71], v[84:87]\n\t" \
    "global_load_lds_dwordx4 v1, s[50:51] offset:128\n\t" \
    "s_waitcnt lgkmcnt(5)\n\t" \
    "v_mfma_f32_16x16x32_bf16 v[72:75], a[24:27], v[24:27], v[72:75]\n\t" \
    "ds_read_b128 v[48:51], v2 offset:9216\n\t" \
    "s_waitcnt lgkmcnt(5)\n\t" \
    "v_mfma_f32_16x16x32_bf16 v[76:79], a[24:27], v[28:31], v[76:79]\n\t" \
    "ds_read_b128 v[52:55], v2 offset:27648\n\t" \
    "v_mfma_f32_16x16x32_bf16 v[80:83], a[96:99], v[24:27], v[80:83]\n\t" \
    "s_add_u32 m0, s91, 2880\n\t" \
    "v_mfma_f32_16x16x32_bf16 v[84:87], a[96:99], v[28:31], v[84:87]\n\t" \
    "global_load_lds_dwordx4 v1, s[50:51] offset:192\n\t" \
    "s_waitcnt lgkmcnt(5)\n\t" \
    "v_mfma_f32_16x16x32_bf16 v[72:75], a[28:31], v[32:35], v[72:75]\n\t" \
    "ds_read_b128 v[56:59], v2 offset:10240\n\t" \
    "s_waitcnt lgkmcnt(5)\n\t" \
    "v_mfma_f32_16x16x32_bf16 v[76:79], a[28:31], v[36:39], v[76:79]\n\t" \
    "ds_read_b128 v[60:63], v2 offset:28672\n\t" \
    "v_mfma_f32_16x16x32_bf16 v[80:83], a[100:103], v[32:35], v[80:83]\n\t" \
    "s_add_u32 m0, s91, 3840\n\t" \
    "v_mfma_f32_16x16x32_bf16 v[84:87], a[100:103], v[36:39], v[84:87]\n\t" \
    "global_load_lds_dwordx4 v1, s[50:51] offset:256\n\t" \
    "s_waitcnt lgkmcnt(5)\n\t" \
    "v_mfma_f32_16x16x32_bf16 v[72:75], a[32:35], v[40:43], v[72:75]\n\t" \
    "ds_read_b128 v[64:67], v2 offset:11264\n\t" \
    "s_waitcnt lgkmcnt(5)\n\t" \
    "v_mfma_f32_16x16x32_bf16 v[76:79], a[32:35], v[44:47], v[76:79]\n\t" \
    "ds_read_b128 v[68:71], v2 offset:29696\n\t" \
    "v_mfma_f32_16x16x32_bf16 v[80:83], a[104:107], v[40:43], v[80:83]\n\t" \
    "s_add_u32 m0, s91, 4800\n\t" \
    "v_mfma_f32_16x16x32_bf16 v[84:87], a[104:107], v[44:47], v[84:87]\n\t" \
    "global_load_lds_dwordx4 v1, s[50:51] offset:320\n\t" \
    "s_waitcnt lgkmcnt(5)\n\t" \
    "v_mfma_f32_16x16x32_bf16 v[72:75], a[36:39], v[48:51], v[72:75]\n\t" \
    "ds_read_b128 v[24:27], v2 offset:12288\n\t" \
    "s_waitcnt lgkmcnt(5)\n\t" \
    "v_mfma_f32_16x16x32_bf16 v[76:79], a[36:39], v[52:55], v[76:79]\n\t" \
    "ds_read_b128 v[28:31], v2 offset:30720\n\t" \
    "v_mfma_f32_16x16x32_bf16 v[80:83], a[108:111], v[48:51], v[80:83]\n\t" \
    "s_add_u32 m0, s91, 5760\n\t" \
    "v_mfma_f32_16x16x32_bf16 v[84:87], a[108:111], v[52:55], v[84:87]\n\t" \
    "global_load_lds_dwordx4 v1, s[50:51] offset:384\n\t" \
    "s_waitcnt lgkmcnt(5)\n\t" \
    "v_mfma_f32_16x16x32_bf16 v[72:75], a[40:43], v[56:59], v[72:75]\n\t" \
    "ds_read_b128 v[32:35], v2 offset:13312\n\t" \
    "s_waitcnt lgkmcnt(5)\n\t" \
    "v_mfma_f32_16x16x32_bf16 v[76:79], a[40:43], v[60:63], v[76:79]\n\t" \
    "ds_read_b128 v[36:39], v2 offset:31744\n\t" \
    "v_mfma_f32_16x16x32_bf16 v[80:83], a[112:115], v[56:59], v[80:83]\n\t" \
    "s_add_u32 m0, s91, 6720\n\t" \
    "v_mfma_f32_16x16x32_bf16 v[84:87], a[112:115], v[60:63], v[84:87]\n\t" \
    "global_load_lds_dwordx4 v1, s[50:51] offset:448\n\t" \
    "s_waitcnt lgkmcnt(5)\n\t" \
    "v_mfma_f32_16x16x32_bf16 v[72:75], a[44:47], v[64:67], v[72:75]\n\t" \
    "ds_read_b128 v[40:43], v2 offset:14336\n\t" \
    "s_waitcnt lgkmcnt(5)\n\t" \
    "v_mfma_f32_16x16x32_bf16 v[76:79], a[44:47], v[68:71], v[76:79]\n\t" \
    "ds_read_b128 v[44:47], v2 offset:32768\n\t" \
    "v_mfma_f32_16x16x32_bf16 v[80:83], a[116:119], v[64:67], v[80:83]\n\t" \
    "s_add_u32 m0, s91, 7680\n\t" \
    "v_mfma_f32_16x16x32_bf16 v[84:87], a[116:119], v[68:71], v[84:87]\n\t" \
    "global_load_lds_dwordx4 v1, s[50:51] offset:512\n\t" \
    "s_waitcnt lgkmcnt(5)\n\t" \
    "v_mfma_f32_16x16x32_bf16 v[72:75], a[48:51], v[24:27], v[72:75]\n\t" \
    "ds_read_b128 v[48:51], v2 offset:15360\n\t" \
    "s_waitcnt lgkmcnt(5)\n\t" \
    "v_mfma_f32_16x16x32_bf16 v[76:79], a[48:51], v[28:31], v[76:79]\n\t" \
    "ds_read_b128 v[52:55], v2 offset:33792\n\t" \
    "v_mfma_f32_16x16x32_bf16 v[80:83], a[120:123], v[24:27], v[80:83]\n\t" \
    "v_mfma_f32_16x16x32_bf16 v[84:87], a[120:123], v[28:31], v[84:87]\n\t" \
    "s_waitcnt lgkmcnt(5)\n\t" \
    "v_mfma_f32_16x16x32_bf16 v[72:75], a[52:55], v[32:35], v[72:75]\n\t" \
    "ds_read_b128 v[56:59], v2 offset:16384\n\t" \
    "s_waitcnt lgkmcnt(5)\n\t" \
    "v_mfma_f32_16x16x32_bf16 v[76:79], a[52:55], v[36:39], v[76:79]\n\t" \
    "ds_read_b128 v[60:63], v2 offset:34816\n\t" \
    "v_mfma_f32_16x16x32_bf16 v[80:83], a[124:127], v[32:35], v[80:83]\n\t" \
    "v_mfma_f32_16x16x32_bf16 v[84:87], a[124:127], v[36:39], v[84:87]\n\t" \
    "s_waitcnt lgkmcnt(5)\n\t" \
    "v_mfma_f32_16x16x32_bf16 v[72:75], a[56:59], v[40:43], v[72:75]\n\t" \
    "ds_read_b128 v[64:67], v2 offset:17408\n\t" \
    "s_waitcnt lgkmcnt(5)\n\t" \
    "v_mfma_f32_16x16x32_bf16 v[76:79], a[56:59], v[44:47], v[76:79]\n\t" \
    "ds_read_b128 v[68:71], v2 offset:35840\n\t" \
    "v_mfma_f32_16x16x32_bf16 v[80:83], a[128:131], v[40:43], v[80:83]\n\t" \
    "v_mfma_f32_16x16x32_bf16 v[84:87], a[128:131], v[44:47], v[84:87]\n\t" \
    "s_waitcnt lgkmcnt(5)\n\t" \
    "v_mfma_f32_16x16x32_bf16 v[72:75], a[60:63], v[48:51], v[72:75]\n\t" \
    "s_waitcnt lgkmcnt(4)\n\t" \
    "v_mfma_f32_16x16x32_bf16 v[76:79], a[60:63], v[52:55], v[76:79]\n\t" \
    "v_mfma_f32_16x16x32_bf16 v[80:83], a[132:135], v[48:51], v[80:83]\n\t" \
    "v_mfma_f32_16x16x32_bf16 v[84:87], a[132:135], v[52:55], v[84:87]\n\t" \
    "s_waitcnt lgkmcnt(3)\n\t" \
    "v_mfma_f32_16x16x32_bf16 v[72:75], a[64:67], v[56:59], v[72:75]\n\t" \
    "s_waitcnt lgkmcnt(2)\n\t" \
    "v_mfma_f32_16x16x32_bf16 v[76:79], a[64:67], v[60:63], v[76:79]\n\t" \
    "v_mfma_f32_16x16x32_bf16 v[80:83], a[136:139], v[56:59], v[80:83]\n\t" \
    "v_mov_b32 v2, v17\n\t" \
    "v_mfma_f32_16x16x32_bf16 v[84:87], a[136:139], v[60:63], v[84:87]\n\t" \
    "s_waitcnt lgkmcnt(1)\n\t" \
    "v_mfma_f32_16x16x32_bf16 v[72:75], a[68:71], v[64:67], v[72:75]\n\t" \
    "s_waitcnt lgkmcnt(0)\n\t" \
    "v_mfma_f32_16x16x32_bf16 v[76:79], a[68:71], v[68:71], v[76:79]\n\t" \
    "v_mfma_f32_16x16x32_bf16 v[80:83], a[140:143], v[64:67], v[80:83]\n\t" \
    "v_mfma_f32_16x16x32_bf16 v[84:87], a[140:143], v[68:71], v[84:87]\n\t" \
    "gv_mid8_%=:\n\t" \
    "s_nop 3\n\t" \
    "v_cvt_pk_bf16_f32 v104, v72, v73\n\t" \
    "v_cvt_pk_bf16_f32 v105, v74, v75\n\t" \
    "s_nop 0\n\t" \
    "v_cvt_pk_bf16_f32 v106, v80, v81\n\t" \
    "v_cvt_pk_bf16_f32 v107, v82, v83\n\t" \
    "ds_write_b128 v4, v[104:107] offset:0\n\t" \
    "v_cvt_pk_bf16_f32 v108, v76, v77\n\t" \
    "v_cvt_pk_bf16_f32 v109, v78, v79\n\t" \
    "v_cvt_pk_bf16_f32 v110, v84, v85\n\t" \
    "v_cvt_pk_bf16_f32 v111, v86, v87\n\t" \
    "ds_write_b128 v4, v[108:111] offset:1024\n\t" \
    "s_waitcnt vmcnt(18) lgkmcnt(0)\n\t" \
    "s_barrier\n\t" \
    "ds_read_b128 v[164:167], v5 offset:0\n\t" \
    "ds_read_b128 v[168:171], v5 offset:1024\n\t" \
    "ds_read_b128 v[172:175], v5 offset:2048\n\t" \
    "ds_read_b128 v[176:179], v5 offset:3072\n\t" \
    "ds_read_b128 v[180:183], v5 offset:4096\n\t" \
    "ds_read_b128 v[184:187], v5 offset:5120\n\t" \
    "ds_read_b128 v[188:191], v5 offset:6144\n\t" \
    "ds_read_b128 v[192:195], v5 offset:7168\n\t" \
    "s_mov_b64 exec, s[46:47]\n\t" \
    "global_store_dword v20, v157, s[36:37] sc1\n\t" \
    "s_mov_b64 exec, -1\n\t" \
    "v_xor_b32 v4, 0x2000, v4\n\t" \
    "v_xor_b32 v5, 0x2000, v5\n\t" \
    "s_waitcnt lgkmcnt(7)\n\t" \
    "v_mfma_f32_16x16x32_bf16 v[72:75], v[164:167], a[144:147], 0\n\t" \
    "s_waitcnt lgkmcnt(6)\n\t" \
    "v_mfma_f32_16x16x32_bf16 v[76:79], v[168:171], a[144:147], 0\n\t" \
    "s_waitcnt lgkmcnt(5)\n\t" \
    "v_mfma_f32_16x16x32_bf16 v[72:75], v[172:175], a[148:151], v[72:75]\n\t" \
    "s_waitcnt lgkmcnt(4)\n\t" \
    "v_mfma_f32_16x16x32_bf16 v[76:79], v[176:179], a[148:151], v[76:79]\n\t" \
    "s_waitcnt lgkmcnt(3)\n\t" \
    "v_mfma_f32_16x16x32_bf16 v[72:75], v[180:183], a[152:155], v[72:75]\n\t" \
    "s_waitcnt lgkmcnt(2)\n\t" \
    "v_mfma_f32_16x16x32_bf16 v[76:79], v[184:187], a[152:155], v[76:79]\n\t" \
    "s_waitcnt lgkmcnt(1)\n\t" \
    "v_mfma_f32_16x16x32_bf16 v[72:75], v[188:191], a[156:159], v[72:75]\n\t" \
    "s_waitcnt lgkmcnt(0)\n\t" \
    "v_mfma_f32_16x16x32_bf16 v[76:79], v[192:195], a[156:159], v[76:79]\n\t" \
    "v_mfma_f32_16x16x32_bf16 v[80:83], v[164:167], a[160:163], 0\n\t" \
    "v_mfma_f32_16x16x32_bf16 v[84:87], v[168:171], a[160:163], 0\n\t" \
    "v_mfma_f32_16x16x32_bf16 v[80:83], v[172:175], a[164:167], v[80:83]\n\t" \
    "v_mfma_f32_16x16x32_bf16 v[84:87], v[176:179], a[164:167], v[84:87]\n\t" \
    "v_mfma_f32_16x16x32_bf16 v[80:83], v[180:183], a[168:171], v[80:83]\n\t" \
    "v_mfma_f32_16x16x32_bf16 v[84:87], v[184:187], a[168:171], v[84:87]\n\t" \
    "v_cvt_pk_bf16_f32 v104, v72, v73\n\t" \
    "v_mfma_f32_16x16x32_bf16 v[80:83], v[188:191], a[172:175], v[80:83]\n\t" \
    "v_cvt_pk_bf16_f32 v105, v74, v75\n\t" \
    "v_mfma_f32_16x16x32_bf16 v[84:87], v[192:195], a[172:175], v[84:87]\n\t" \
    "v_cvt_pk_bf16_f32 v106, v76, v77\n\t" \
    "v_mfma_f32_16x16x32_bf16 v[88:91], v[164:167], a[176:179], 0\n\t" \
    "v_cvt_pk_bf16_f32 v107, v78, v79\n\t" \
    "v_mfma_f32_16x16x32_bf16 v[92:95], v[168:171], a[176:179], 0\n\t" \
    "v_xor_b32 v18, s40, v8\n\t" \
    "v_mfma_f32_16x16x32_bf16 v[88:91], v[172:175], a[180:183], v[88:91]\n\t" \
    "global_store_dwordx4 v18, v[104:107], s[38:39] sc1\n\t" \
    "v_mfma_f32_16x16x32_bf16 v[92:95], v[176:179], a[180:183], v[92:95]\n\t" \
    "v_mfma_f32_16x16x32_bf16 v[88:91], v[180:183], a[184:187], v[88:91]\n\t" \
    "v_mfma_f32_16x16x32_bf16 v[92:95], v[184:187], a[184:187], v[92:95]\n\t" \
    "v_cvt_pk_bf16_f32 v108, v80, v81\n\t" \
    "v_mfma_f32_16x16x32_bf16 v[88:91], v[188:191], a[188:191], v[88:91]\n\t" \
    "v_cvt_pk_bf16_f32 v109, v82, v83\n\t" \
    "v_mfma_f32_16x16x32_bf16 v[92:95], v[192:195], a[188:191], v[92:95]\n\t" \
    "v_cvt_pk_bf16_f32 v110, v84, v85\n\t" \
    "v_mfma_f32_16x16x32_bf16 v[72:75], v[164:167], a[192:195], 0\n\t" \
    "v_cvt_pk_bf16_f32 v111, v86, v87\n\t" \
    "v_mfma_f32_16x16x32_bf16 v[76:79], v[168:171], a[192:195], 0\n\t" \
    "v_xor_b32 v18, s40, v9\n\t" \
    "v_mfma_f32_16x16x32_bf16 v[72:75], v[172:175], a[196:199], v[72:75]\n\t" \
    "global_store_dwordx4 v18, v[108:111], s[38:39] sc1\n\t" \
    "v_mfma_f32_16x16x32_bf16 v[76:79], v[176:179], a[196:199], v[76:79]\n\t" \
    "v_mfma_f32_16x16x32_bf16 v[72:75], v[180:183], a[200:203], v[72:75]\n\t" \
    "v_mfma_f32_16x16x32_bf16 v[76:79], v[184:187], a[200:203], v[76:79]\n\t" \
    "v_cvt_pk_bf16_f32 v104, v88, v89\n\t" \
    "v_mfma_f32_16x16x32_bf16 v[72:75], v[188:191], a[204:207], v[72:75]\n\t" \
    "v_cvt_pk_bf16_f32 v105, v90, v91\n\t" \
    "v_mfma_f32_16x16x32_bf16 v[76:79], v[192:195], a[204:207], v[76:79]\n\t" \
    "v_cvt_pk_bf16_f32 v106, v92, v93\n\t" \
    "v_mfma_f32_16x16x32_bf16 v[80:83], v[164:167], a[208:211], 0\n\t" \
    "v_cvt_pk_bf16_f32 v107, v94, v95\n\t" \
    "v_mfma_f32_16x16x32_bf16 v[84:87], v[168:171], a[208:211], 0\n\t" \
    "v_xor_b32 v18, s40, v10\n\t" \
    "v_mfma_f32_16x16x32_bf16 v[80:83], v[172:175], a[212:215], v[80:83]\n\t" \
    "global_store_dwordx4 v18, v[104:107], s[38:39] sc1\n\t" \
    "v_mfma_f32_16x16x32_bf16 v[84:87], v[176:179], a[212:215], v[84:87]\n\t" \
    "v_mfma_f32_16x16x32_bf16 v[80:83], v[180:183], a[216:219], v[80:83]\n\t" \
    "v_mfma_f32_16x16x32_bf16 v[84:87], v[184:187], a[216:219], v[84:87]\n\t" \
    "v_cvt_pk_bf16_f32 v108, v72, v73\n\t" \
    "v_mfma_f32_16x16x32_bf16 v[80:83], v[188:191], a[220:223], v[80:83]\n\t" \
    "v_cvt_pk_bf16_f32 v109, v74, v75\n\t" \
    "v_mfma_f32_16x16x32_bf16 v[84:87], v[192:195], a[220:223], v[84:87]\n\t" \
    "v_cvt_pk_bf16_f32 v110, v76, v77\n\t" \
    "v_mfma_f32_16x16x32_bf16 v[88:91], v[164:167], a[224:227], 0\n\t" \
    "v_cvt_pk_bf16_f32 v111, v78, v79\n\t" \
    "v_mfma_f32_16x16x32_bf16 v[92:95], v[168:171], a[224:227], 0\n\t" \
    "v_xor_b32 v18, s40, v11\n\t" \
    "v_mfma_f32_16x16x32_bf16 v[88:91], v[172:175], a[228:231], v[88:91]\n\t" \
    "global_store_dwordx4 v18, v[108:111], s[38:39] sc1\n\t" \
    "v_mfma_f32_16x16x32_bf16 v[92:95], v[176:179], a[228:231], v[92:95]\n\t" \
    "v_mfma_f32_16x16x32_bf16 v[88:91], v[180:183], a[232:235], v[88:91]\n\t" \
    "v_mfma_f32_16x16x32_bf16 v[92:95], v[184:187], a[232:235], v[92:95]\n\t" \
    "v_cvt_pk_bf16_f32 v104, v80, v81\n\t" \
    "v_mfma_f32_16x16x32_bf16 v[88:91], v[188:191], a[236:239], v[88:91]\n\t" \
    "v_cvt_pk_bf16_f32 v105, v82, v83\n\t" \
    "v_mfma_f32_16x16x32_bf16 v[92:95], v[192:195], a[236:239], v[92:95]\n\t" \
    "v_cvt_pk_bf16_f32 v106, v84, v85\n\t" \
    "v_mfma_f32_16x16x32_bf16 v[72:75], v[164:167], a[240:243], 0\n\t" \
    "v_cvt_pk_bf16_f32 v107, v86, v87\n\t" \
    "v_mfma_f32_16x16x32_bf16 v[76:79], v[168:171], a[240:243], 0\n\t" \
    "v_xor_b32 v18, s40, v12\n\t" \
    "v_mfma_f32_16x16x32_bf16 v[72:75], v[172:175], a[244:247], v[72:75]\n\t" \
    "global_store_dwordx4 v18, v[104:107], s[38:39] sc1\n\t" \
    "v_mfma_f32_16x16x32_bf16 v[76:79], v[176:179], a[244:247], v[76:79]\n\t" \
    "v_mfma_f32_16x16x32_bf16 v[72:75], v[180:183], a[248:251], v[72:75]\n\t" \
    "v_mfma_f32_16x16x32_bf16 v[76:79], v[184:187], a[248:251], v[76:79]\n\t" \
    "v_cvt_pk_bf16_f32 v108, v88, v89\n\t" \
    "v_mfma_f32_16x16x32_bf16 v[72:75], v[188:191], a[252:255], v[72:75]\n\t" \
    "v_cvt_pk_bf16_f32 v109, v90, v91\n\t" \
    "v_mfma_f32_16x16x32_bf16 v[76:79], v[192:195], a[252:255], v[76:79]\n\t" \
    "v_cvt_pk_bf16_f32 v110, v92, v93\n\t" \
    "v_mfma_f32_16x16x32_bf16 v[80:83], v[164:167], v[224:227], 0\n\t" \
    "v_cvt_pk_bf16_f32 v111, v94, v95\n\t" \
    "v_mfma_f32_16x16x32_bf16 v[84:87], v[168:171], v[224:227], 0\n\t" \
    "v_xor_b32 v18, s40, v13\n\t" \
    "v_mfma_f32_16x16x32_bf16 v[80:83], v[172:175], v[228:231], v[80:83]\n\t" \
    "global_store_dwordx4 v18, v[108:111], s[38:39] sc1\n\t" \
    "v_mfma_f32_16x16x32_bf16 v[84:87], v[176:179], v[228:231], v[84:87]\n\t" \
    "v_mfma_f32_16x16x32_bf16 v[80:83], v[180:183], v[232:235], v[80:83]\n\t" \
    "v_mfma_f32_16x16x32_bf16 v[84:87], v[184:187], v[232:235], v[84:87]\n\t" \
    "v_cvt_pk_bf16_f32 v104, v72, v73\n\t" \
    "v_mfma_f32_16x16x32_bf16 v[80:83], v[188:191], v[236:239], v[80:83]\n\t" \
    "v_cvt_pk_bf16_f32 v105, v74, v75\n\t" \
    "v_mfma_f32_16x16x32_bf16 v[84:87], v[192:195], v[236:239], v[84:87]\n\t" \
    "v_cvt_pk_bf16_f32 v106, v76, v77\n\t" \
    "s_cmp_lt_u32 s65, 4\n\t" \
    "s_cbranch_scc1 gv_f1st9_%=\n\t" \
    "gv_f1b10_%=:\n\t" \
    "v_mfma_f32_16x16x32_bf16 v[88:91], v[164:167], v[240:243], 0\n\t" \
    "v_cvt_pk_bf16_f32 v107, v78, v79\n\t" \
    "ds_read_b128 v[24:27], v17 offset:0\n\t" \
    "v_mfma_f32_16x16x32_bf16 v[92:95], v[168:171], v[240:243], 0\n\t" \
    "v_xor_b32 v18, s40, v14\n\t" \
    "ds_read_b128 v[28:31], v17 offset:18432\n\t" \
    "v_mfma_f32_16x16x32_bf16 v[88:91], v[172:175], v[244:247], v[88:91]\n\t" \
    "global_store_dwordx4 v18, v[104:107], s[38:39] sc1\n\t" \
    "ds_read_b128 v[32:35], v17 offset:1024\n\t" \
    "v_mfma_f32_16x16x32_bf16 v[92:95], v[176:179], v[244:247], v[92:95]\n\t" \
    "ds_read_b128 v[36:39], v17 offset:19456\n\t" \
    "v_mfma_f32_16x16x32_bf16 v[88:91], v[180:183], v[248:251], v[88:91]\n\t" \
    "ds_read_b128 v[40:43], v17 offset:2048\n\t" \
    "v_mfma_f32_16x16x32_bf16 v[92:95], v[184:187], v[248:251], v[92:95]\n\t" \
    "v_cvt_pk_bf16_f32 v108, v80, v81\n\t" \
    "ds_read_b128 v[44:47], v17 offset:20480\n\t" \
    "v_mfma_f32_16x16x32_bf16 v[88:91], v[188:191], v[252:255], v[88:91]\n\t" \
    "v_cvt_pk_bf16_f32 v109, v82, v83\n\t" \
    "v_mfma_f32_16x16x32_bf16 v[92:95], v[192:195], v[252:255], v[92:95]\n\t" \
    "v_cvt_pk_bf16_f32 v110, v84, v85\n\t" \
    "v_cvt_pk_bf16_f32 v111, v86, v87\n\t" \
    "v_xor_b32 v18, s40, v15\n\t" \
    "global_store_dwordx4 v18, v[108:111], s[38:39] sc1\n\t" \
    "s_nop 1\n\t" \
    "v_cvt_pk_bf16_f32 v104, v88, v89\n\t" \
    "v_cvt_pk_bf16_f32 v105, v90, v91\n\t" \
    "v_cvt_pk_bf16_f32 v106, v92, v93\n\t" \
    "v_cvt_pk_bf16_f32 v107, v94, v95\n\t" \
    "v_xor_b32 v18, s40, v16\n\t" \
    "global_store_dwordx4 v18, v[104:107], s[38:39] sc1\n\t" \
    "s_sub_u32 s69, s65, 6\n\t" \
    "s_cmp_ge_u32 s69, 56\n\t" \
    "s_cbranch_scc1 gv_fast11_%=\n\t" \
    "gv_nofast7_%=:\n\t" \
    "s_add_u32 s65, s65, 2\n\t" \
    "s_cmp_lt_u32 s65, 64\n\t" \
    "s_cbranch_scc1 gv_loop6_%=\n\t" \
    "s_waitcnt vmcnt(0) lgkmcnt(0)\n\t" \
    "s_barrier\n\t" \
    "s_branch ea_done_%=\n\t" \
    "gv_f1st9_%=:\n\t" \
    "s_waitcnt vmcnt(6)\n\t" \
    "s_barrier\n\t" \
    "s_branch gv_f1b10_%=\n\t" \
    "gv_fast11_%=:\n\t" \
    "s_waitcnt vmcnt(0)\n\t" \
    "s_barrier\n\t" \
    "s_cmp_lg_u32 s66, 0\n\t" \
    "s_cbranch_scc1 gv_SF12_%=\n\t" \
    "s_add_u32 s70, s65, 0\n\t" \
    "v_mov_b32 v157, s70\n\t" \
    "s_mov_b64 exec, 31\n\t" \
    "global_store_dword v20, v157, s[36:37] sc1\n\t" \
    "s_mov_b64 exec, -1\n\t" \
    "gv_SF12_%=:\n\t" \
    "s_cmp_ge_u32 s65, 2\n\t" \
    "s_cbranch_scc1 gv_nofast7_%=\n\t" \
    "s_add_u32 m0, s91, 0\n\t" \
    "s_nop 0\n\t" \
    "global_load_lds_dwordx4 v1, s[50:51] offset:0\n\t" \
    "s_add_u32 m0, s91, 960\n\t" \
    "s_nop 0\n\t" \
    "global_load_lds_dwordx4 v1, s[50:51] offset:64\n\t" \
    "s_add_u32 m0, s91, 1920\n\t" \
    "s_nop 0\n\t" \
    "global_load_lds_dwordx4 v1, s[50:51] offset:128\n\t" \
    "s_add_u32 m0, s91, 2880\n\t" \
    "s_nop 0\n\t" \
    "global_load_lds_dwordx4 v1, s[50:51] offset:192\n\t" \
    "s_add_u32 m0, s91, 3840\n\t" \
    "s_nop 0\n\t" \
    "global_load_lds_dwordx4 v1, s[50:51] offset:256\n\t" \
    "s_add_u32 m0, s91, 4800\n\t" \
    "s_nop 0\n\t" \
    "global_load_lds_dwordx4 v1, s[50:51] offset:320\n\t" \
    "s_add_u32 m0, s91, 5760\n\t" \
    "s_nop 0\n\t" \
    "global_load_lds_dwordx4 v1, s[50:51] offset:384\n\t" \
    "s_add_u32 m0, s91, 6720\n\t" \
    "s_nop 0\n\t" \
    "global_load_lds_dwordx4 v1, s[50:51] offset:448\n\t" \
    "s_add_u32 m0, s91, 7680\n\t" \
    "s_nop 0\n\t" \
    "global_load_lds_dwordx4 v1, s[50:51] offset:512\n\t" \
    "s_branch gv_nofast7_%=\n\t" \
    "ea_done_%=:\n\t" \
    "s_waitcnt lgkmcnt(0)\n\t" \
    "ea_last_%=:\n\t" \
    ""
#define ASM_R \
    "s_mov_b64 s[36:37], %0\n\t" \
    "s_mov_b64 s[42:43], %1\n\t" \
    "s_mov_b64 s[44:45], %2\n\t" \
    "s_mov_b32 s66, %3\n\t" \
    "s_mov_b32 s67, %4\n\t" \
    "s_mov_b64 s[50:51], %5\n\t" \
    "s_mov_b32 s82, %7\n\t" \
    "s_load_dwordx8 s[84:91], %6, 0\n\t" \
    "v_mbcnt_lo_u32_b32 v223, -1, 0\n\t" \
    "v_mbcnt_hi_u32_b32 v223, -1, v223\n\t" \
    "s_getpc_b64 s[46:47]\n\t" \
    "rr_pc_%=:\n\t" \
    "s_lshl_b32 s69, s66, 13\n\t" \
    "v_lshl_add_u32 v222, v223, 7, s69\n\t" \
    "v_min_u32 v222, rr_last_%=-rr_pc_%=-4, v222\n\t" \
    "global_load_dword v222, v222, s[46:47]\n\t" \
    "v_lshlrev_b32 v223, 2, v223\n\t" \
    "v_add_u32 v223, s82, v223\n\t" \
    "ds_read_b32 v0, v223 offset:0\n\t" \
    "ds_read_b32 v1, v223 offset:256\n\t" \
    "ds_read_b32 v2, v223 offset:512\n\t" \
    "ds_read_b32 v3, v223 offset:768\n\t" \
    "ds_read_b32 v4, v223 offset:1024\n\t" \
    "ds_read_b32 v5, v223 offset:1280\n\t" \
    "ds_read_b32 v6, v223 offset:1536\n\t" \
    "ds_read_b32 v7, v223 offset:1792\n\t" \
    "ds_read_b32 v8, v223 offset:2048\n\t" \
    "ds_read_b32 v9, v223 offset:2304\n\t" \
    "ds_read_b32 v10, v223 offset:2560\n\t" \
    "ds_read_b32 v11, v223 offset:2816\n\t" \
    "s_waitcnt lgkmcnt(0)\n\t" \
    "ds_read_b32 v12, v223 offset:3072\n\t" \
    "ds_read_b32 v13, v223 offset:3328\n\t" \
    "ds_read_b32 v14, v223 offset:3584\n\t" \
    "ds_read_b32 v15, v223 offset:3840\n\t" \
    "ds_read_b32 v16, v223 offset:4096\n\t" \
    "ds_read_b32 v17, v223 offset:4352\n\t" \
    "ds_read_b32 v18, v223 offset:4608\n\t" \
    "ds_read_b32 v19, v223 offset:4864\n\t" \
    "ds_read_b32 v20, v223 offset:5120\n\t" \
    "ds_read_b32 v21, v223 offset:5376\n\t" \
    "ds_read_b32 v22, v223 offset:5632\n\t" \
    "ds_read_b32 v23, v223 offset:5888\n\t" \
    "s_waitcnt lgkmcnt(0)\n\t" \
    "ds_read_b32 v24, v223 offset:6144\n\t" \
    "ds_read_b32 v25, v223 offset:6400\n\t" \
    "ds_read_b32 v26, v223 offset:6656\n\t" \
    "ds_read_b32 v27, v223 offset:6912\n\t" \
    "ds_read_b32 v28, v223 offset:7168\n\t" \
    "ds_read_b32 v29, v223 offset:7424\n\t" \
    "ds_read_b32 v30, v223 offset:7680\n\t" \
    "ds_read_b32 v31, v223 offset:7936\n\t" \
    "ds_read_b32 v32, v223 offset:8192\n\t" \
    "ds_read_b32 v33, v223 offset:8448\n\t" \
    "ds_read_b32 v34, v223 offset:8704\n\t" \
    "ds_read_b32 v35, v223 offset:8960\n\t" \
    "s_waitcnt lgkmcnt(0)\n\t" \
    "ds_read_b32 v36, v223 offset:9216\n\t" \
    "ds_read_b32 v37, v223 offset:9472\n\t" \
    "ds_read_b32 v38, v223 offset:9728\n\t" \
    "ds_read_b32 v39, v223 offset:9984\n\t" \
    "ds_read_b32 v40, v223 offset:10240\n\t" \
    "ds_read_b32 v41, v223 offset:10496\n\t" \
    "ds_read_b32 v42, v223 offset:10752\n\t" \
    "ds_read_b32 v43, v223 offset:11008\n\t" \
    "ds_read_b32 v44, v223 offset:11264\n\t" \
    "ds_read_b32 v45, v223 offset:11520\n\t" \
    "ds_read_b32 v46, v223 offset:11776\n\t" \
    "ds_read_b32 v164, v223 offset:12032\n\t" \
    "s_waitcnt lgkmcnt(0)\n\t" \
    "ds_read_b32 v165, v223 offset:12288\n\t" \
    "ds_read_b32 v166, v223 offset:12544\n\t" \
    "ds_read_b32 v167, v223 offset:12800\n\t" \
    "ds_read_b32 v196, v223 offset:13312\n\t" \
    "ds_read_b32 v197, v223 offset:13568\n\t" \
    "ds_read_b32 v198, v223 offset:13824\n\t" \
    "ds_read_b32 v199, v223 offset:14080\n\t" \
    "ds_read_b32 v200, v223 offset:14336\n\t" \
    "s_waitcnt lgkmcnt(0)\n\t" \
    "s_mov_b32 s68, 262144\n\t" \
    "s_mov_b32 s72, 0\n\t" \
    "s_mov_b32 s83, -1\n\t" \
    "s_mov_b32 s65, 0\n\t" \
    "v_add_u32 v181, 65536, v2\n\t" \
    "v_add_u32 v182, 131072, v2\n\t" \
    "v_add_u32 v183, 196608, v2\n\t" \
    "s_mov_b32 s77, 3019898880\n\t" \
    "s_mov_b32 s78, 3054198966\n\t" \
    "s_mov_b32 s71, 3\n\t" \
    "rr_E1_%=:\n\t" \
    "s_memtime s[54:55]\n\t" \
    "s_nop 0\n\t" \
    "s_memrealtime s[4:5]\n\t" \
    "s_nop 0\n\t" \
    "s_memrealtime s[6:7]\n\t" \
    "s_nop 0\n\t" \
    "s_memrealtime s[8:9]\n\t" \
    "s_nop 0\n\t" \
    "s_memrealtime s[10:11]\n\t" \
    "s_nop 0\n\t" \
    "s_memrealtime s[12:13]\n\t" \
    "s_nop 0\n\t" \
    "s_memrealtime s[14:15]\n\t" \
    "s_nop 0\n\t" \
    "s_memrealtime s[16:17]\n\t" \
    "s_nop 0\n\t" \
    "s_memrealtime s[18:19]\n\t" \
    "s_nop 0\n\t" \
    "s_memrealtime s[20:21]\n\t" \
    "s_nop 0\n\t" \
    "s_memrealtime s[22:23]\n\t" \
    "s_nop 0\n\t" \
    "s_memrealtime s[24:25]\n\t" \
    "s_nop 0\n\t" \
    "s_memrealtime s[26:27]\n\t" \
    "s_nop 0\n\t" \
    "s_memrealtime s[28:29]\n\t" \
    "s_nop 0\n\t" \
    "s_memtime s[56:57]\n\t" \
    "s_mov_b32 s70, 1\n\t" \
    "s_sub_u32 s71, s71, 1\n\t" \
    "s_cselect_b32 s69, 0, -1\n\t" \
    "s_waitcnt lgkmcnt(0)\n\t" \
    "s_sub_u32 s54, s56, s54\n\t" \
    "s_and_b32 s54, s54, s69\n\t" \
    "s_cmp_gt_u32 s54, 124\n\t" \
    "s_cbranch_scc1 rr_E1_%=\n\t" \
    "s_cmp_eq_u32 s4, s28\n\t" \
    "s_addc_u32 s70, s70, 0\n\t" \
    "s_cmp_eq_u32 s6, s28\n\t" \
    "s_addc_u32 s70, s70, 0\n\t" \
    "s_cmp_eq_u32 s8, s28\n\t" \
    "s_addc_u32 s70, s70, 0\n\t" \
    "s_cmp_eq_u32 s10, s28\n\t" \
    "s_addc_u32 s70, s70, 0\n\t" \
    "s_cmp_eq_u32 s12, s28\n\t" \
    "s_addc_u32 s70, s70, 0\n\t" \
    "s_cmp_eq_u32 s14, s28\n\t" \
    "s_addc_u32 s70, s70, 0\n\t" \
    "s_cmp_eq_u32 s16, s28\n\t" \
    "s_addc_u32 s70, s70, 0\n\t" \
    "s_cmp_eq_u32 s18, s28\n\t" \
    "s_addc_u32 s70, s70, 0\n\t" \
    "s_cmp_eq_u32 s20, s28\n\t" \
    "s_addc_u32 s70, s70, 0\n\t" \
    "s_cmp_eq_u32 s22, s28\n\t" \
    "s_addc_u32 s70, s70, 0\n\t" \
    "s_cmp_eq_u32 s24, s28\n\t" \
    "s_addc_u32 s70, s70, 0\n\t" \
    "s_cmp_eq_u32 s26, s28\n\t" \
    "s_addc_u32 s70, s70, 0\n\t" \
    "s_mov_b32 s75, s73\n\t" \
    "s_mov_b32 s76, s74\n\t" \
    "s_lshl_b32 s70, s70, 3\n\t" \
    "s_sub_u32 s73, s56, s70\n\t" \
    "s_lshl_b32 s74, s28, 4\n\t" \
    "v_mov_b32 v172, 0x1f0000\n\t" \
    "v_mov_b32 v173, 0x3f0000\n\t" \
    "global_load_dword v172, v172, s[36:37]\n\t" \
    "global_load_dword v173, v173, s[36:37]\n\t" \
    "s_mov_b32 s65, 63\n\t" \
    "s_mul_i32 s70, s67, 7\n\t" \
    "s_add_u32 s69, s65, 0\n\t" \
    "s_xor_b32 s69, s69, s70\n\t" \
    "s_and_b32 s69, s69, 63\n\t" \
    "s_lshl_b32 s69, s69, 15\n\t" \
    "s_mul_i32 s70, s67, 2097152\n\t" \
    "s_add_u32 s69, s69, s70\n\t" \
    "s_add_u32 s69, s69, 48234496\n\t" \
    "s_add_u32 s38, s36, s69\n\t" \
    "s_addc_u32 s39, s37, 0\n\t" \
    "v_mov_b32 v174, 0\n\t" \
    "global_load_dword v174, v174, s[38:39]\n\t" \
    "v_mbcnt_lo_u32_b32 v184, -1, 0\n\t" \
    "v_mbcnt_hi_u32_b32 v184, -1, v184\n\t" \
    "v_min_u32 v184, 59, v184\n\t" \
    "v_lshlrev_b32 v184, 6, v184\n\t" \
    "v_add_u32 v184, 6258688, v184\n\t" \
    "v_mov_b32 v185, 6266880\n\t" \
    "global_load_dword v185, v185, s[36:37] sc1\n\t" \
    "global_load_dwordx2 v[186:187], v184, s[36:37] sc1\n\t" \
    "s_waitcnt vmcnt(0)\n\t" \
    "v_sub_u32 v186, v186, v185\n\t" \
    "v_cmp_ne_u32 vcc, 1, v186\n\t" \
    "s_cbranch_vccz rr_Cok4_%=\n\t" \
    "rr_C3_%=:\n\t" \
    "global_load_dwordx2 v[186:187], v184, s[36:37] sc1\n\t" \
    "s_waitcnt vmcnt(0)\n\t" \
    "v_sub_u32 v186, v186, v185\n\t" \
    "v_cmp_ne_u32 vcc, 1, v186\n\t" \
    "s_cbranch_vccz rr_Cok4_%=\n\t" \
    "s_cmp_eq_u32 s68, 0\n\t" \
    "s_cbranch_scc1 rr_F5_%=\n\t" \
    "s_sub_u32 s68, s68, 1\n\t" \
    "s_sleep 4\n\t" \
    "s_branch rr_C3_%=\n\t" \
    "rr_F5_%=:\n\t" \
    "v_mov_b32 v188, 6095104\n\t" \
    "v_mov_b32 v189, 4\n\t" \
    "global_store_dword v188, v189, s[36:37] sc1\n\t" \
    "s_waitcnt vmcnt(0)\n\t" \
    "s_branch rr_Cok4_%=\n\t" \
    "rr_Cok4_%=:\n\t" \
    "v_min_u32_dpp v187, v187, v187 row_shl:6 row_mask:0xf bank_mask:0xf\n\t" \
    "s_nop 0\n\t" \
    "v_readlane_b32 s69, v187, 0\n\t" \
    "v_readlane_b32 s70, v187, 1\n\t" \
    "s_nop 1\n\t" \
    "s_cmp_lt_u32 s70, s69\n\t" \
    "s_cselect_b32 s62, 8192, 0\n\t" \
    "s_cselect_b32 s69, s70, s69\n\t" \
    "s_mov_b32 s71, s69\n\t" \
    "v_readlane_b32 s69, v187, 2\n\t" \
    "v_readlane_b32 s70, v187, 3\n\t" \
    "s_nop 1\n\t" \
    "s_cmp_lt_u32 s70, s69\n\t" \
    "s_cselect_b32 s63, 8192, 0\n\t" \
    "s_cselect_b32 s69, s70, s69\n\t" \
    "s_max_u32 s71, s69, s71\n\t" \
    "v_readlane_b32 s69, v187, 4\n\t" \
    "v_readlane_b32 s70, v187, 5\n\t" \
    "s_nop 1\n\t" \
    "s_cmp_lt_u32 s70, s69\n\t" \
    "s_cselect_b32 s64, 8192, 0\n\t" \
    "s_cselect_b32 s69, s70, s69\n\t" \
    "s_max_u32 s71, s69, s71\n\t" \
    "s_add_u32 s63, s63, 0x200000\n\t" \
    "s_xor_b32 s52, s64, 8192\n\t" \
    "s_cmp_gt_u32 s71, 625\n\t" \
    "s_cselect_b32 s69, 1, 0\n\t" \
    "s_mul_i32 s70, s69, 160\n\t" \
    "s_add_u32 s86, s86, s70\n\t" \
    "s_mul_i32 s70, s69, 536\n\t" \
    "s_add_u32 s87, s87, s70\n\t" \
    "s_mul_i32 s70, s69, 256\n\t" \
    "s_add_u32 s88, s88, s70\n\t" \
    "v_xor_b32 v6, s52, v6\n\t" \
    "v_lshlrev_b32 v178, 3, v0\n\t" \
    "v_and_b32 v178, 0xf80, v178\n\t" \
    "s_add_u32 s69, s52, 6148096\n\t" \
    "v_add_u32 v178, s69, v178\n\t" \
    "v_mov_b32 v172, -1\n\t" \
    "v_mov_b32 v173, -1\n\t" \
    "v_mov_b32 v174, -1\n\t" \
    "v_mov_b32 v175, -1\n\t" \
    "v_mov_b32 v170, 0\n\t" \
    "s_mov_b32 s65, s66\n\t" \
    "s_sub_u32 s80, s74, 32000\n\t" \
    "v_mov_b32 v177, -1\n\t" \
    "s_cmp_lg_u32 s51, 0\n\t" \
    "s_cselect_b32 s42, 0, 0\n\t" \
    "s_cselect_b32 s43, 32, 0\n\t" \
    "s_cmp_le_u32 s51, s65\n\t" \
    "s_cbranch_scc1 rr_n316_%=\n\t" \
    "s_add_u32 s69, s65, 2\n\t" \
    "s_mul_i32 s70, s69, 171\n\t" \
    "s_lshr_b32 s70, s70, 9\n\t" \
    "s_mul_i32 s70, s70, 3\n\t" \
    "s_sub_u32 s69, s69, s70\n\t" \
    "s_mul_i32 s69, s69, 36\n\t" \
    "s_lshl_b32 s69, s69, 14\n\t" \
    "s_add_u32 s69, s69, s64\n\t" \
    "s_add_u32 s69, s69, 4194304\n\t" \
    "s_add_u32 s40, s36, s69\n\t" \
    "s_addc_u32 s41, s37, 0\n\t" \
    "global_store_dwordx4 v196, v[172:175], s[40:41] sc1\n\t" \
    "global_store_dwordx4 v197, v[172:175], s[40:41] sc1\n\t" \
    "global_store_dwordx4 v198, v[172:175], s[40:41] sc1\n\t" \
    "s_add_u32 s69, s65, 2\n\t" \
    "s_mul_i32 s70, s69, 171\n\t" \
    "s_lshr_b32 s70, s70, 9\n\t" \
    "s_mul_i32 s70, s70, 3\n\t" \
    "s_sub_u32 s69, s69, s70\n\t" \
    "s_mul_i32 s69, s69, 36\n\t" \
    "s_add_u32 s69, s69, s67\n\t" \
    "s_lshl_b32 s69, s69, 14\n\t" \
    "s_add_u32 s69, s69, s64\n\t" \
    "s_add_u32 s69, s69, 4194304\n\t" \
    "s_add_u32 s40, s36, s69\n\t" \
    "s_addc_u32 s41, s37, 0\n\t" \
    "global_store_dwordx4 v0, v[172:175], s[40:41] offset:0 sc1\n\t" \
    "global_store_dwordx4 v0, v[172:175], s[40:41] offset:1024 sc1\n\t" \
    "global_store_dwordx4 v0, v[172:175], s[40:41] offset:2048 sc1\n\t" \
    "global_store_dwordx4 v0, v[172:175], s[40:41] offset:3072 sc1\n\t" \
    "global_store_dwordx4 v1, v[172:175], s[40:41] offset:0 sc1\n\t" \
    "global_store_dwordx4 v1, v[172:175], s[40:41] offset:1024 sc1\n\t" \
    "global_store_dwordx4 v1, v[172:175], s[40:41] offset:2048 sc1\n\t" \
    "global_store_dwordx4 v1, v[172:175], s[40:41] offset:3072 sc1\n\t" \
    "rr_n316_%=:\n\t" \
    "s_cmp_eq_u32 s51, 0\n\t" \
    "s_cbranch_scc1 rr_arm17_%=\n\t" \
    "s_cmp_gt_u32 s65, 61\n\t" \
    "s_cbranch_scc1 rr_arm17_%=\n\t" \
    "s_add_u32 s69, s65, 2\n\t" \
    "s_bitcmp1_b32 s69, 0\n\t" \
    "s_cselect_b32 s70, s63, s62\n\t" \
    "s_bfe_u32 s69, s69, 0x20001\n\t" \
    "s_lshl_b32 s69, s69, 19\n\t" \
    "s_add_u32 s69, s69, s70\n\t" \
    "s_add_u32 s46, s36, s69\n\t" \
    "s_addc_u32 s47, s37, 0\n\t" \
    "global_store_dwordx4 v164, v[172:175], s[46:47] sc1\n\t" \
    "global_store_dwordx4 v165, v[172:175], s[46:47] sc1\n\t" \
    "global_store_dwordx4 v166, v[172:175], s[46:47] sc1\n\t" \
    "global_store_dwordx4 v167, v[172:175], s[46:47] sc1\n\t" \
    "rr_arm17_%=:\n\t" \
    "s_waitcnt vmcnt(0)\n\t" \
    "s_add_u32 s52, s36, s62\n\t" \
    "s_addc_u32 s53, s37, 0\n\t" \
    "v_lshrrev_b32 v187, 9, v0\n\t" \
    "v_lshl_add_u32 v187, v187, 2, v7\n\t" \
    "v_add_u32 v187, 3072, v187\n\t" \
    "v_mov_b32 v189, -1\n\t" \
    "s_sleep 40\n\t" \
    "s_mov_b32 s71, 3\n\t" \
    "rr_E18_%=:\n\t" \
    "s_memtime s[54:55]\n\t" \
    "s_nop 0\n\t" \
    "s_memrealtime s[4:5]\n\t" \
    "s_nop 0\n\t" \
    "s_memrealtime s[6:7]\n\t" \
    "s_nop 0\n\t" \
    "s_memrealtime s[8:9]\n\t" \
    "s_nop 0\n\t" \
    "s_memrealtime s[10:11]\n\t" \
    "s_nop 0\n\t" \
    "s_memrealtime s[12:13]\n\t" \
    "s_nop 0\n\t" \
    "s_memrealtime s[14:15]\n\t" \
    "s_nop 0\n\t" \
    "s_memrealtime s[16:17]\n\t" \
    "s_nop 0\n\t" \
    "s_memrealtime s[18:19]\n\t" \
    "s_nop 0\n\t" \
    "s_memrealtime s[20:21]\n\t" \
    "s_nop 0\n\t" \
    "s_memrealtime s[22:23]\n\t" \
    "s_nop 0\n\t" \
    "s_memrealtime s[24:25]\n\t" \
    "s_nop 0\n\t" \
    "s_memrealtime s[26:27]\n\t" \
    "s_nop 0\n\t" \
    "s_memrealtime s[28:29]\n\t" \
    "s_nop 0\n\t" \
    "s_memtime s[56:57]\n\t" \
    "s_mov_b32 s70, 1\n\t" \
    "s_sub_u32 s71, s71, 1\n\t" \
    "s_cselect_b32 s69, 0, -1\n\t" \
    "s_waitcnt lgkmcnt(0)\n\t" \
    "s_sub_u32 s54, s56, s54\n\t" \
    "s_and_b32 s54, s54, s69\n\t" \
    "s_cmp_gt_u32 s54, 124\n\t" \
    "s_cbranch_scc1 rr_E18_%=\n\t" \
    "s_cmp_eq_u32 s4, s28\n\t" \
    "s_addc_u32 s70, s70, 0\n\t" \
    "s_cmp_eq_u32 s6, s28\n\t" \
    "s_addc_u32 s70, s70, 0\n\t" \
    "s_cmp_eq_u32 s8, s28\n\t" \
    "s_addc_u32 s70, s70, 0\n\t" \
    "s_cmp_eq_u32 s10, s28\n\t" \
    "s_addc_u32 s70, s70, 0\n\t" \
    "s_cmp_eq_u32 s12, s28\n\t" \
    "s_addc_u32 s70, s70, 0\n\t" \
    "s_cmp_eq_u32 s14, s28\n\t" \
    "s_addc_u32 s70, s70, 0\n\t" \
    "s_cmp_eq_u32 s16, s28\n\t" \
    "s_addc_u32 s70, s70, 0\n\t" \
    "s_cmp_eq_u32 s18, s28\n\t" \
    "s_addc_u32 s70, s70, 0\n\t" \
    "s_cmp_eq_u32 s20, s28\n\t" \
    "s_addc_u32 s70, s70, 0\n\t" \
    "s_cmp_eq_u32 s22, s28\n\t" \
    "s_addc_u32 s70, s70, 0\n\t" \
    "s_cmp_eq_u32 s24, s28\n\t" \
    "s_addc_u32 s70, s70, 0\n\t" \
    "s_cmp_eq_u32 s26, s28\n\t" \
    "s_addc_u32 s70, s70, 0\n\t" \
    "s_mov_b32 s75, s73\n\t" \
    "s_mov_b32 s76, s74\n\t" \
    "s_lshl_b32 s70, s70, 3\n\t" \
    "s_sub_u32 s73, s56, s70\n\t" \
    "s_lshl_b32 s74, s28, 4\n\t" \
    "s_sub_u32 s69, s73, s75\n\t" \
    "s_sub_u32 s70, s74, s76\n\t" \
    "s_sub_u32 s71, s70, 1600\n\t" \
    "s_cmp_lt_u32 s71, 400000\n\t" \
    "s_cbranch_scc0 rr_K20_%=\n\t" \
    "v_cvt_f32_u32 v190, s69\n\t" \
    "v_cvt_f32_u32 v191, s70\n\t" \
    "v_rcp_f32 v192, v190\n\t" \
    "v_rcp_f32 v193, v191\n\t" \
    "v_mul_f32 v192, v192, v191\n\t" \
    "v_mul_f32 v193, v193, v190\n\t" \
    "v_mul_f32 v192, 0x4f800000, v192\n\t" \
    "v_mul_f32 v193, 0x4f000000, v193\n\t" \
    "v_cvt_u32_f32 v192, v192\n\t" \
    "v_cvt_u32_f32 v193, v193\n\t" \
    "v_readfirstlane_b32 s69, v192\n\t" \
    "v_readfirstlane_b32 s70, v193\n\t" \
    "s_nop 1\n\t" \
    "s_mov_b32 s78, s69\n\t" \
    "s_mov_b32 s77, s70\n\t" \
    "rr_K20_%=:\n\t" \
    "s_add_u32 s69, s65, 0\n\t" \
    "s_bitcmp1_b32 s69, 0\n\t" \
    "s_cselect_b32 s70, s63, s62\n\t" \
    "s_bfe_u32 s69, s69, 0x20001\n\t" \
    "s_lshl_b32 s69, s69, 19\n\t" \
    "s_add_u32 s69, s69, s70\n\t" \
    "s_add_u32 s46, s36, s69\n\t" \
    "s_addc_u32 s47, s37, 0\n\t" \
    "s_add_u32 s69, s65, 1\n\t" \
    "s_bitcmp1_b32 s69, 0\n\t" \
    "s_cselect_b32 s70, s63, s62\n\t" \
    "s_bfe_u32 s69, s69, 0x20001\n\t" \
    "s_lshl_b32 s69, s69, 19\n\t" \
    "s_add_u32 s69, s69, s70\n\t" \
    "s_add_u32 s48, s36, s69\n\t" \
    "s_addc_u32 s49, s37, 0\n\t" \
    "s_mul_i32 s70, s67, 7\n\t" \
    "s_add_u32 s69, s65, 0\n\t" \
    "s_xor_b32 s69, s69, s70\n\t" \
    "s_and_b32 s69, s69, 63\n\t" \
    "s_lshl_b32 s69, s69, 15\n\t" \
    "s_mul_i32 s70, s67, 2097152\n\t" \
    "s_add_u32 s69, s69, s70\n\t" \
    "s_add_u32 s69, s69, 48234496\n\t" \
    "s_add_u32 s38, s36, s69\n\t" \
    "s_addc_u32 s39, s37, 0\n\t" \
    "s_add_u32 s69, s65, 0\n\t" \
    "s_mul_i32 s70, s69, 171\n\t" \
    "s_lshr_b32 s70, s70, 9\n\t" \
    "s_mul_i32 s70, s70, 3\n\t" \
    "s_sub_u32 s69, s69, s70\n\t" \
    "s_mul_i32 s69, s69, 36\n\t" \
    "s_add_u32 s69, s69, s67\n\t" \
    "s_lshl_b32 s69, s69, 14\n\t" \
    "s_add_u32 s69, s69, s64\n\t" \
    "s_add_u32 s69, s69, 4194304\n\t" \
    "s_add_u32 s40, s36, s69\n\t" \
    "s_addc_u32 s41, s37, 0\n\t" \
    "s_mov_b32 s91, 0\n\t" \
    "s_mov_b32 s96, 0x55555555\n\t" \
    "s_mov_b32 s58, 0\n\t" \
    "s_mov_b32 s59, -1\n\t" \
    "v_mov_b32 v160, 0\n\t" \
    "v_mov_b32 v161, 0\n\t" \
    "v_mov_b32 v162, 0\n\t" \
    "v_mov_b32 v163, 0\n\t" \
    "v_mov_b32 v185, 0\n\t" \
    "s_mov_b32 s91, 3\n\t" \
    "s_mov_b64 exec, 0\n\t" \
    "s_branch rr_h025_%=\n\t" \
    "rr_dry27_%=:\n\t" \
    "s_mov_b64 exec, -1\n\t" \
    "s_mov_b32 s91, 0\n\t" \
    "s_waitcnt vmcnt(0) lgkmcnt(0)\n\t" \
    "s_nop 7\n\t" \
    "v_mov_b32 v160, 0\n\t" \
    "v_mov_b32 v161, 0\n\t" \
    "v_mov_b32 v162, 0\n\t" \
    "v_mov_b32 v163, 0\n\t" \
    "rr_pg21_%=:\n\t" \
    "v_cmp_eq_u32 vcc, -1, v189\n\t" \
    "s_cmp_eq_u32 s96, 0\n\t" \
    "s_cselect_b32 s69, vcc_lo, 0\n\t" \
    "s_or_b32 s69, s69, vcc_hi\n\t" \
    "s_cbranch_scc0 rr_ph28_%=\n\t" \
    "s_sleep 10\n\t" \
    "global_load_dword v189, v187, s[52:53] sc1\n\t" \
    "s_mov_b32 s71, 3\n\t" \
    "rr_E29_%=:\n\t" \
    "s_memtime s[54:55]\n\t" \
    "s_nop 0\n\t" \
    "s_memrealtime s[4:5]\n\t" \
    "s_nop 0\n\t" \
    "s_memrealtime s[6:7]\n\t" \
    "s_nop 0\n\t" \
    "s_memrealtime s[8:9]\n\t" \
    "s_nop 0\n\t" \
    "s_memrealtime s[10:11]\n\t" \
    "s_nop 0\n\t" \
    "s_memrealtime s[12:13]\n\t" \
    "s_nop 0\n\t" \
    "s_memrealtime s[14:15]\n\t" \
    "s_nop 0\n\t" \
    "s_memrealtime s[16:17]\n\t" \
    "s_nop 0\n\t" \
    "s_memrealtime s[18:19]\n\t" \
    "s_nop 0\n\t" \
    "s_memrealtime s[20:21]\n\t" \
    "s_nop 0\n\t" \
    "s_memrealtime s[22:23]\n\t" \
    "s_nop 0\n\t" \
    "s_memrealtime s[24:25]\n\t" \
    "s_nop 0\n\t" \
    "s_memrealtime s[26:27]\n\t" \
    "s_nop 0\n\t" \
    "s_memrealtime s[28:29]\n\t" \
    "s_nop 0\n\t" \
    "s_memtime s[56:57]\n\t" \
    "s_mov_b32 s70, 1\n\t" \
    "s_sub_u32 s71, s71, 1\n\t" \
    "s_cselect_b32 s69, 0, -1\n\t" \
    "s_waitcnt lgkmcnt(0)\n\t" \
    "s_sub_u32 s54, s56, s54\n\t" \
    "s_and_b32 s54, s54, s69\n\t" \
    "s_cmp_gt_u32 s54, 124\n\t" \
    "s_cbranch_scc1 rr_E29_%=\n\t" \
    "s_cmp_eq_u32 s4, s28\n\t" \
    "s_addc_u32 s70, s70, 0\n\t" \
    "s_cmp_eq_u32 s6, s28\n\t" \
    "s_addc_u32 s70, s70, 0\n\t" \
    "s_cmp_eq_u32 s8, s28\n\t" \
    "s_addc_u32 s70, s70, 0\n\t" \
    "s_cmp_eq_u32 s10, s28\n\t" \
    "s_addc_u32 s70, s70, 0\n\t" \
    "s_cmp_eq_u32 s12, s28\n\t" \
    "s_addc_u32 s70, s70, 0\n\t" \
    "s_cmp_eq_u32 s14, s28\n\t" \
    "s_addc_u32 s70, s70, 0\n\t" \
    "s_cmp_eq_u32 s16, s28\n\t" \
    "s_addc_u32 s70, s70, 0\n\t" \
    "s_cmp_eq_u32 s18, s28\n\t" \
    "s_addc_u32 s70, s70, 0\n\t" \
    "s_cmp_eq_u32 s20, s28\n\t" \
    "s_addc_u32 s70, s70, 0\n\t" \
    "s_cmp_eq_u32 s22, s28\n\t" \
    "s_addc_u32 s70, s70, 0\n\t" \
    "s_cmp_eq_u32 s24, s28\n\t" \
    "s_addc_u32 s70, s70, 0\n\t" \
    "s_cmp_eq_u32 s26, s28\n\t" \
    "s_addc_u32 s70, s70, 0\n\t" \
    "s_mov_b32 s75, s73\n\t" \
    "s_mov_b32 s76, s74\n\t" \
    "s_lshl_b32 s70, s70, 3\n\t" \
    "s_sub_u32 s73, s56, s70\n\t" \
    "s_lshl_b32 s74, s28, 4\n\t" \
    "s_waitcnt vmcnt(0)\n\t" \
    "rr_ph28_%=:\n\t" \
    "v_cmp_ne_u32 s[34:35], -1, v189\n\t" \
    "s_memtime s[56:57]\n\t" \
    "s_waitcnt lgkmcnt(0)\n\t" \
    "s_sub_u32 s69, s56, s73\n\t" \
    "s_mul_hi_u32 s69, s69, s78\n\t" \
    "s_add_u32 s69, s69, s74\n\t" \
    "s_add_u32 s69, s69, 1280\n\t" \
    "v_sub_u32 v184, s69, v189\n\t" \
    "v_cmp_le_i32 vcc, 0, v184\n\t" \
    "s_nop 1\n\t" \
    "s_and_b64 s[54:55], vcc, s[34:35]\n\t" \
    "s_and_b64 s[54:55], s[54:55], s[58:59]\n\t" \
    "s_cbranch_scc0 rr_pn22_%=\n\t" \
    "s_mov_b64 exec, s[54:55]\n\t" \
    "global_load_dword v177, v6, s[36:37] sc1\n\t" \
    "s_mov_b64 exec, -1\n\t" \
    "s_waitcnt vmcnt(0)\n\t" \
    "v_cmp_le_i32 vcc, s65, v177\n\t" \
    "s_nop 1\n\t" \
    "s_andn2_b32 s59, s59, vcc_hi\n\t" \
    "s_lshr_b32 s69, vcc_hi, 1\n\t" \
    "s_and_b32 s69, s69, vcc_hi\n\t" \
    "s_and_b32 s54, s69, s96\n\t" \
    "s_andn2_b32 s96, s96, s54\n\t" \
    "s_mov_b32 s55, s54\n\t" \
    "s_lshr_b64 s[54:55], s[54:55], s50\n\t" \
    "s_bitcmp1_b32 s54, 0\n\t" \
    "s_cbranch_scc0 rr_pk31_%=\n\t" \
    "global_load_dwordx4 a[0:3], v26, s[38:39] offset:0 sc1\n\t" \
    "global_load_dwordx4 a[4:7], v26, s[38:39] offset:1024 sc1\n\t" \
    "rr_pk31_%=:\n\t" \
    "s_bitcmp1_b32 s54, 2\n\t" \
    "s_cbranch_scc0 rr_pk32_%=\n\t" \
    "global_load_dwordx4 a[8:11], v27, s[38:39] offset:0 sc1\n\t" \
    "global_load_dwordx4 a[12:15], v27, s[38:39] offset:1024 sc1\n\t" \
    "rr_pk32_%=:\n\t" \
    "s_bitcmp1_b32 s54, 4\n\t" \
    "s_cbranch_scc0 rr_pk33_%=\n\t" \
    "global_load_dwordx4 a[16:19], v28, s[38:39] offset:0 sc1\n\t" \
    "global_load_dwordx4 a[20:23], v28, s[38:39] offset:1024 sc1\n\t" \
    "rr_pk33_%=:\n\t" \
    "s_bitcmp1_b32 s54, 6\n\t" \
    "s_cbranch_scc0 rr_pk34_%=\n\t" \
    "global_load_dwordx4 a[24:27], v29, s[38:39] offset:0 sc1\n\t" \
    "global_load_dwordx4 a[28:31], v29, s[38:39] offset:1024 sc1\n\t" \
    "rr_pk34_%=:\n\t" \
    "s_bitcmp1_b32 s54, 8\n\t" \
    "s_cbranch_scc0 rr_pk35_%=\n\t" \
    "global_load_dwordx4 a[32:35], v30, s[38:39] offset:0 sc1\n\t" \
    "global_load_dwordx4 a[36:39], v30, s[38:39] offset:1024 sc1\n\t" \
    "rr_pk35_%=:\n\t" \
    "s_bitcmp1_b32 s54, 10\n\t" \
    "s_cbranch_scc0 rr_pk36_%=\n\t" \
    "global_load_dwordx4 a[40:43], v31, s[38:39] offset:0 sc1\n\t" \
    "global_load_dwordx4 a[44:47], v31, s[38:39] offset:1024 sc1\n\t" \
    "rr_pk36_%=:\n\t" \
    "s_bitcmp1_b32 s54, 12\n\t" \
    "s_cbranch_scc0 rr_pk37_%=\n\t" \
    "global_load_dwordx4 a[48:51], v32, s[38:39] offset:0 sc1\n\t" \
    "global_load_dwordx4 a[52:55], v32, s[38:39] offset:1024 sc1\n\t" \
    "rr_pk37_%=:\n\t" \
    "s_bitcmp1_b32 s54, 14\n\t" \
    "s_cbranch_scc0 rr_pk38_%=\n\t" \
    "global_load_dwordx4 a[56:59], v33, s[38:39] offset:0 sc1\n\t" \
    "global_load_dwordx4 a[60:63], v33, s[38:39] offset:1024 sc1\n\t" \
    "rr_pk38_%=:\n\t" \
    "s_bitcmp1_b32 s54, 16\n\t" \
    "s_cbranch_scc0 rr_pk39_%=\n\t" \
    "global_load_dwordx4 a[64:67], v34, s[38:39] offset:0 sc1\n\t" \
    "global_load_dwordx4 a[68:71], v34, s[38:39] offset:1024 sc1\n\t" \
    "rr_pk39_%=:\n\t" \
    "s_bitcmp1_b32 s54, 18\n\t" \
    "s_cbranch_scc0 rr_pk40_%=\n\t" \
    "global_load_dwordx4 a[72:75], v35, s[38:39] offset:0 sc1\n\t" \
    "global_load_dwordx4 a[76:79], v35, s[38:39] offset:1024 sc1\n\t" \
    "rr_pk40_%=:\n\t" \
    "s_bitcmp1_b32 s54, 20\n\t" \
    "s_cbranch_scc0 rr_pk41_%=\n\t" \
    "global_load_dwordx4 a[80:83], v36, s[38:39] offset:0 sc1\n\t" \
    "global_load_dwordx4 a[84:87], v36, s[38:39] offset:1024 sc1\n\t" \
    "rr_pk41_%=:\n\t" \
    "s_bitcmp1_b32 s54, 22\n\t" \
    "s_cbranch_scc0 rr_pk42_%=\n\t" \
    "global_load_dwordx4 a[88:91], v37, s[38:39] offset:0 sc1\n\t" \
    "global_load_dwordx4 a[92:95], v37, s[38:39] offset:1024 sc1\n\t" \
    "rr_pk42_%=:\n\t" \
    "s_bitcmp1_b32 s54, 24\n\t" \
    "s_cbranch_scc0 rr_pk43_%=\n\t" \
    "global_load_dwordx4 a[96:99], v38, s[38:39] offset:0 sc1\n\t" \
    "global_load_dwordx4 a[100:103], v38, s[38:39] offset:1024 sc1\n\t" \
    "rr_pk43_%=:\n\t" \
    "s_bitcmp1_b32 s54, 26\n\t" \
    "s_cbranch_scc0 rr_pk44_%=\n\t" \
    "global_load_dwordx4 a[104:107], v39, s[38:39] offset:0 sc1\n\t" \
    "global_load_dwordx4 a[108:111], v39, s[38:39] offset:1024 sc1\n\t" \
    "rr_pk44_%=:\n\t" \
    "s_bitcmp1_b32 s54, 28\n\t" \
    "s_cbranch_scc0 rr_pk45_%=\n\t" \
    "global_load_dwordx4 a[112:115], v40, s[38:39] offset:0 sc1\n\t" \
    "global_load_dwordx4 a[116:119], v40, s[38:39] offset:1024 sc1\n\t" \
    "rr_pk45_%=:\n\t" \
    "s_bitcmp1_b32 s54, 30\n\t" \
    "s_cbranch_scc0 rr_pk46_%=\n\t" \
    "global_load_dwordx4 a[120:123], v41, s[38:39] offset:0 sc1\n\t" \
    "global_load_dwordx4 a[124:127], v41, s[38:39] offset:1024 sc1\n\t" \
    "rr_pk46_%=:\n\t" \
    "rr_pn22_%=:\n\t" \
    "s_orn2_b32 s69, s96, s34\n\t" \
    "s_cbranch_scc0 rr_pe23_%=\n\t" \
    "s_cmp_eq_u32 s68, 0\n\t" \
    "s_cbranch_scc1 rr_F47_%=\n\t" \
    "s_sub_u32 s68, s68, 1\n\t" \
    "s_branch rr_pg21_%=\n\t" \
    "rr_F47_%=:\n\t" \
    "v_mov_b32 v184, 6095104\n\t" \
    "v_mov_b32 v185, 10\n\t" \
    "global_store_dword v184, v185, s[36:37] sc1\n\t" \
    "s_waitcnt vmcnt(0)\n\t" \
    "s_branch rr_pe23_%=\n\t" \
    "rr_pe23_%=:\n\t" \
    "s_mov_b32 s83, s65\n\t" \
    "v_mov_b32 v184, v189\n\t" \
    "v_subrev_u32 v184, s80, v184\n\t" \
    "s_nop 1\n\t" \
    "v_max_i32_dpp v184, v184, v184 row_ror:1 row_mask:0xf bank_mask:0xf\n\t" \
    "s_nop 1\n\t" \
    "v_max_i32_dpp v184, v184, v184 row_ror:2 row_mask:0xf bank_mask:0xf\n\t" \
    "s_nop 1\n\t" \
    "v_max_i32_dpp v184, v184, v184 row_ror:4 row_mask:0xf bank_mask:0xf\n\t" \
    "s_nop 1\n\t" \
    "v_max_i32_dpp v184, v184, v184 row_ror:8 row_mask:0xf bank_mask:0xf\n\t" \
    "s_nop 0\n\t" \
    "v_readlane_b32 s69, v184, 0\n\t" \
    "v_readlane_b32 s71, v184, 16\n\t" \
    "v_readlane_b32 s70, v184, 32\n\t" \
    "v_readlane_b32 s81, v184, 48\n\t" \
    "s_max_i32 s69, s69, s71\n\t" \
    "s_nop 0\n\t" \
    "s_max_i32 s71, s70, s81\n\t" \
    "s_max_i32 s69, s69, s71\n\t" \
    "s_add_u32 s81, s69, s80\n\t" \
    "s_cmp_lg_u32 s65, 0\n\t" \
    "s_cbranch_scc1 rr_u226_%=\n\t" \
    "s_waitcnt vmcnt(0)\n\t" \
    "global_load_dword v177, v6, s[36:37] sc1\n\t" \
    "v_mov_b32 v184, v189\n\t" \
    "v_subrev_u32 v184, s80, v184\n\t" \
    "s_nop 1\n\t" \
    "v_max_i32_dpp v184, v184, v184 row_ror:1 row_mask:0xf bank_mask:0xf\n\t" \
    "s_nop 1\n\t" \
    "v_max_i32_dpp v184, v184, v184 row_ror:2 row_mask:0xf bank_mask:0xf\n\t" \
    "s_nop 1\n\t" \
    "v_max_i32_dpp v184, v184, v184 row_ror:4 row_mask:0xf bank_mask:0xf\n\t" \
    "s_nop 1\n\t" \
    "v_max_i32_dpp v184, v184, v184 row_ror:8 row_mask:0xf bank_mask:0xf\n\t" \
    "s_nop 0\n\t" \
    "v_readlane_b32 s69, v184, 0\n\t" \
    "v_readlane_b32 s71, v184, 16\n\t" \
    "s_nop 1\n\t" \
    "s_max_i32 s69, s69, s71\n\t" \
    "s_add_u32 s81, s69, s80\n\t" \
    "s_memtime s[56:57]\n\t" \
    "s_waitcnt lgkmcnt(0)\n\t" \
    "s_sub_u32 s69, s56, s73\n\t" \
    "s_mul_hi_u32 s69, s69, s78\n\t" \
    "s_add_u32 s69, s69, s74\n\t" \
    "s_add_u32 s69, s69, 480\n\t" \
    "s_sub_u32 s69, s69, s81\n\t" \
    "s_max_i32 s69, s69, 0\n\t" \
    "s_add_u32 s69, s69, s81\n\t" \
    "v_mov_b32 v184, s69\n\t" \
    "s_add_u32 s69, s69, 160\n\t" \
    "v_and_b32_e64 v185, -2, s69\n\t" \
    "global_store_dword v3, v185, s[46:47] sc1\n\t" \
    "s_branch rr_h025_%=\n\t" \
    "rr_pf13_%=:\n\t" \
    "s_cmp_gt_u32 s65, 62\n\t" \
    "s_cbranch_scc1 rr_end8_%=\n\t" \
    "v_min_i32_dpp v177, v177, v177 row_ror:1 row_mask:0xf bank_mask:0xf\n\t" \
    "s_nop 1\n\t" \
    "v_min_i32_dpp v177, v177, v177 row_ror:2 row_mask:0xf bank_mask:0xf\n\t" \
    "s_nop 1\n\t" \
    "v_min_i32_dpp v177, v177, v177 row_ror:4 row_mask:0xf bank_mask:0xf\n\t" \
    "s_nop 1\n\t" \
    "v_min_i32_dpp v177, v177, v177 row_ror:8 row_mask:0xf bank_mask:0xf\n\t" \
    "s_nop 0\n\t" \
    "v_readlane_b32 s69, v177, 0\n\t" \
    "v_readlane_b32 s70, v177, 16\n\t" \
    "s_nop 1\n\t" \
    "s_min_i32 s69, s69, s70\n\t" \
    "s_max_i32 s83, s83, s69\n\t" \
    "rr_WF48_%=:\n\t" \
    "s_cmp_ge_i32 s83, s65\n\t" \
    "s_cbranch_scc1 rr_WFok49_%=\n\t" \
    "global_load_dword v184, v6, s[36:37] sc1\n\t" \
    "s_waitcnt vmcnt(0)\n\t" \
    "v_min_i32_dpp v184, v184, v184 row_ror:1 row_mask:0xf bank_mask:0xf\n\t" \
    "s_nop 1\n\t" \
    "v_min_i32_dpp v184, v184, v184 row_ror:2 row_mask:0xf bank_mask:0xf\n\t" \
    "s_nop 1\n\t" \
    "v_min_i32_dpp v184, v184, v184 row_ror:4 row_mask:0xf bank_mask:0xf\n\t" \
    "s_nop 1\n\t" \
    "v_min_i32_dpp v184, v184, v184 row_ror:8 row_mask:0xf bank_mask:0xf\n\t" \
    "s_nop 0\n\t" \
    "v_readlane_b32 s69, v184, 0\n\t" \
    "v_readlane_b32 s70, v184, 16\n\t" \
    "s_nop 1\n\t" \
    "s_min_i32 s83, s69, s70\n\t" \
    "s_cmp_ge_i32 s83, s65\n\t" \
    "s_cbranch_scc1 rr_WFok49_%=\n\t" \
    "s_cmp_eq_u32 s68, 0\n\t" \
    "s_cbranch_scc1 rr_F50_%=\n\t" \
    "s_sub_u32 s68, s68, 1\n\t" \
    "s_sleep 2\n\t" \
    "s_branch rr_WF48_%=\n\t" \
    "rr_F50_%=:\n\t" \
    "v_mov_b32 v184, 6095104\n\t" \
    "v_mov_b32 v185, 10\n\t" \
    "global_store_dword v184, v185, s[36:37] sc1\n\t" \
    "s_waitcnt vmcnt(0)\n\t" \
    "s_branch rr_WFok49_%=\n\t" \
    "rr_WFok49_%=:\n\t" \
    "s_mul_i32 s70, s67, 7\n\t" \
    "s_add_u32 s69, s65, 0\n\t" \
    "s_xor_b32 s69, s69, s70\n\t" \
    "s_and_b32 s69, s69, 63\n\t" \
    "s_lshl_b32 s69, s69, 15\n\t" \
    "s_mul_i32 s70, s67, 2097152\n\t" \
    "s_add_u32 s69, s69, s70\n\t" \
    "s_add_u32 s69, s69, 48234496\n\t" \
    "s_add_u32 s38, s36, s69\n\t" \
    "s_addc_u32 s39, s37, 0\n\t" \
    "s_mul_hi_u32 s69, s85, s78\n\t" \
    "s_add_u32 s79, s81, s69\n\t" \
    "s_add_u32 s79, s79, s87\n\t" \
    "s_cmp_ge_u32 s65, 38\n\t" \
    "s_cselect_b32 s69, 28, 0\n\t" \
    "s_sub_u32 s79, s79, s69\n\t" \
    "s_cmp_lt_u32 s65, 10\n\t" \
    "s_cselect_b32 s69, 96, 0\n\t" \
    "s_add_u32 s79, s79, s69\n\t" \
    "s_cmp_ge_u32 s65, 59\n\t" \
    "s_cselect_b32 s69, 96, 0\n\t" \
    "s_add_u32 s79, s79, s69\n\t" \
    "s_add_u32 s80, s79, s88\n\t" \
    "s_add_u32 s79, s79, -96\n\t" \
    "s_add_u32 s79, s79, s42\n\t" \
    "s_sub_u32 s70, s79, s74\n\t" \
    "s_max_i32 s70, s70, 0\n\t" \
    "s_min_i32 s70, s70, 16000\n\t" \
    "s_lshl_b32 s70, s70, 1\n\t" \
    "s_mul_hi_u32 s70, s70, s77\n\t" \
    "s_add_u32 s70, s70, s73\n\t" \
    "rr_W51_%=:\n\t" \
    "s_memtime s[56:57]\n\t" \
    "s_waitcnt lgkmcnt(0)\n\t" \
    "s_sub_u32 s69, s70, s56\n\t" \
    "s_cmp_gt_i32 s69, 24\n\t" \
    "s_cbranch_scc1 rr_W51_%=\n\t" \
    "s_add_u32 s69, s65, 4\n\t" \
    "s_min_i32 s69, s69, 62\n\t" \
    "s_cmp_ge_i32 s83, s69\n\t" \
    "s_cbranch_scc1 rr_nfl52_%=\n\t" \
    "global_load_dword v177, v6, s[36:37] sc1\n\t" \
    "rr_nfl52_%=:\n\t" \
    "global_load_dwordx4 a[0:3], v26, s[38:39] offset:0 sc1\n\t" \
    "global_load_dwordx4 a[4:7], v26, s[38:39] offset:1024 sc1\n\t" \
    "global_load_dwordx4 a[8:11], v27, s[38:39] offset:0 sc1\n\t" \
    "global_load_dwordx4 a[12:15], v27, s[38:39] offset:1024 sc1\n\t" \
    "global_load_dwordx4 a[16:19], v28, s[38:39] offset:0 sc1\n\t" \
    "global_load_dwordx4 a[20:23], v28, s[38:39] offset:1024 sc1\n\t" \
    "global_load_dwordx4 a[24:27], v29, s[38:39] offset:0 sc1\n\t" \
    "global_load_dwordx4 a[28:31], v29, s[38:39] offset:1024 sc1\n\t" \
    "global_load_dwordx4 a[32:35], v30, s[38:39] offset:0 sc1\n\t" \
    "global_load_dwordx4 a[36:39], v30, s[38:39] offset:1024 sc1\n\t" \
    "global_load_dwordx4 a[40:43], v31, s[38:39] offset:0 sc1\n\t" \
    "global_load_dwordx4 a[44:47], v31, s[38:39] offset:1024 sc1\n\t" \
    "global_load_dwordx4 a[48:51], v32, s[38:39] offset:0 sc1\n\t" \
    "global_load_dwordx4 a[52:55], v32, s[38:39] offset:1024 sc1\n\t" \
    "global_load_dwordx4 a[56:59], v33, s[38:39] offset:0 sc1\n\t" \
    "global_load_dwordx4 a[60:63], v33, s[38:39] offset:1024 sc1\n\t" \
    "global_load_dwordx4 a[64:67], v34, s[38:39] offset:0 sc1\n\t" \
    "global_load_dwordx4 a[68:71], v34, s[38:39] offset:1024 sc1\n\t" \
    "global_load_dwordx4 a[72:75], v35, s[38:39] offset:0 sc1\n\t" \
    "global_load_dwordx4 a[76:79], v35, s[38:39] offset:1024 sc1\n\t" \
    "global_load_dwordx4 a[80:83], v36, s[38:39] offset:0 sc1\n\t" \
    "global_load_dwordx4 a[84:87], v36, s[38:39] offset:1024 sc1\n\t" \
    "global_load_dwordx4 a[88:91], v37, s[38:39] offset:0 sc1\n\t" \
    "global_load_dwordx4 a[92:95], v37, s[38:39] offset:1024 sc1\n\t" \
    "global_load_dwordx4 a[96:99], v38, s[38:39] offset:0 sc1\n\t" \
    "global_load_dwordx4 a[100:103], v38, s[38:39] offset:1024 sc1\n\t" \
    "global_load_dwordx4 a[104:107], v39, s[38:39] offset:0 sc1\n\t" \
    "global_load_dwordx4 a[108:111], v39, s[38:39] offset:1024 sc1\n\t" \
    "global_load_dwordx4 a[112:115], v40, s[38:39] offset:0 sc1\n\t" \
    "global_load_dwordx4 a[116:119], v40, s[38:39] offset:1024 sc1\n\t" \
    "global_load_dwordx4 a[120:123], v41, s[38:39] offset:0 sc1\n\t" \
    "global_load_dwordx4 a[124:127], v41, s[38:39] offset:1024 sc1\n\t" \
    "s_cmp_eq_u32 s51, 0\n\t" \
    "s_cbranch_scc1 rr_nb53_%=\n\t" \
    "s_add_u32 s69, s65, 2\n\t" \
    "s_cmp_lt_i32 s83, s69\n\t" \
    "s_cbranch_scc1 rr_nb53_%=\n\t" \
    "s_add_u32 s69, s65, 1\n\t" \
    "s_lshl_b32 s69, s69, 15\n\t" \
    "v_xor_b32 v201, s69, v199\n\t" \
    "v_xor_b32 v202, s69, v200\n\t" \
    "global_load_dword v201, v201, s[36:37] sc1\n\t" \
    "global_load_dword v202, v202, s[36:37] sc1\n\t" \
    "rr_nb53_%=:\n\t" \
    "s_mul_hi_u32 s69, s85, s78\n\t" \
    "s_add_u32 s79, s81, s69\n\t" \
    "s_add_u32 s79, s79, s87\n\t" \
    "s_cmp_ge_u32 s65, 38\n\t" \
    "s_cselect_b32 s69, 28, 0\n\t" \
    "s_sub_u32 s79, s79, s69\n\t" \
    "s_cmp_lt_u32 s65, 10\n\t" \
    "s_cselect_b32 s69, 96, 0\n\t" \
    "s_add_u32 s79, s79, s69\n\t" \
    "s_cmp_ge_u32 s65, 59\n\t" \
    "s_cselect_b32 s69, 96, 0\n\t" \
    "s_add_u32 s79, s79, s69\n\t" \
    "s_add_u32 s80, s79, s88\n\t" \
    "s_add_u32 s79, s79, 320\n\t" \
    "s_add_u32 s79, s79, s43\n\t" \
    "s_sub_u32 s70, s79, s74\n\t" \
    "s_max_i32 s70, s70, 0\n\t" \
    "s_min_i32 s70, s70, 16000\n\t" \
    "s_lshl_b32 s70, s70, 1\n\t" \
    "s_mul_hi_u32 s70, s70, s77\n\t" \
    "s_add_u32 s70, s70, s73\n\t" \
    "rr_W54_%=:\n\t" \
    "s_memtime s[56:57]\n\t" \
    "s_waitcnt lgkmcnt(0)\n\t" \
    "s_sub_u32 s69, s70, s56\n\t" \
    "s_cmp_gt_i32 s69, 24\n\t" \
    "s_cbranch_scc1 rr_W54_%=\n\t" \
    "s_cmp_eq_u32 s51, 0\n\t" \
    "s_cbranch_scc1 rr_arm55_%=\n\t" \
    "global_store_dwordx4 v0, v[172:175], s[40:41] offset:0 sc1\n\t" \
    "global_store_dwordx4 v0, v[172:175], s[40:41] offset:1024 sc1\n\t" \
    "global_store_dwordx4 v0, v[172:175], s[40:41] offset:2048 sc1\n\t" \
    "global_store_dwordx4 v0, v[172:175], s[40:41] offset:3072 sc1\n\t" \
    "global_store_dwordx4 v1, v[172:175], s[40:41] offset:0 sc1\n\t" \
    "global_store_dwordx4 v1, v[172:175], s[40:41] offset:1024 sc1\n\t" \
    "global_store_dwordx4 v1, v[172:175], s[40:41] offset:2048 sc1\n\t" \
    "global_store_dwordx4 v1, v[172:175], s[40:41] offset:3072 sc1\n\t" \
    "s_cmp_gt_u32 s65, 61\n\t" \
    "s_cbranch_scc1 rr_arm55_%=\n\t" \
    "s_add_u32 s69, s65, 2\n\t" \
    "s_bitcmp1_b32 s69, 0\n\t" \
    "s_cselect_b32 s70, s63, s62\n\t" \
    "s_bfe_u32 s69, s69, 0x20001\n\t" \
    "s_lshl_b32 s69, s69, 19\n\t" \
    "s_add_u32 s69, s69, s70\n\t" \
    "s_add_u32 s46, s36, s69\n\t" \
    "s_addc_u32 s47, s37, 0\n\t" \
    "global_store_dwordx4 v164, v[172:175], s[46:47] sc1\n\t" \
    "global_store_dwordx4 v165, v[172:175], s[46:47] sc1\n\t" \
    "global_store_dwordx4 v166, v[172:175], s[46:47] sc1\n\t" \
    "global_store_dwordx4 v167, v[172:175], s[46:47] sc1\n\t" \
    "rr_arm55_%=:\n\t" \
    "s_waitcnt vmcnt(0)\n\t" \
    "v_min_i32_dpp v177, v177, v177 row_ror:1 row_mask:0xf bank_mask:0xf\n\t" \
    "s_nop 1\n\t" \
    "v_min_i32_dpp v177, v177, v177 row_ror:2 row_mask:0xf bank_mask:0xf\n\t" \
    "s_nop 1\n\t" \
    "v_min_i32_dpp v177, v177, v177 row_ror:4 row_mask:0xf bank_mask:0xf\n\t" \
    "s_nop 1\n\t" \
    "v_min_i32_dpp v177, v177, v177 row_ror:8 row_mask:0xf bank_mask:0xf\n\t" \
    "s_nop 0\n\t" \
    "v_readlane_b32 s69, v177, 0\n\t" \
    "v_readlane_b32 s70, v177, 16\n\t" \
    "s_nop 1\n\t" \
    "s_min_i32 s69, s69, s70\n\t" \
    "s_max_i32 s83, s83, s69\n\t" \
    "s_cmp_lg_u32 s65, 40\n\t" \
    "s_cbranch_scc1 rr_f256_%=\n\t" \
    "s_cmp_lg_u32 s67, 35\n\t" \
    "s_cbranch_scc1 rr_f256_%=\n\t" \
    "rr_f2l57_%=:\n\t" \
    "global_load_dword v186, v178, s[36:37] sc1\n\t" \
    "s_waitcnt vmcnt(0)\n\t" \
    "v_cmp_ne_u32 vcc, 1, v186\n\t" \
    "s_cbranch_vccz rr_f256_%=\n\t" \
    "s_cmp_eq_u32 s68, 0\n\t" \
    "s_cbranch_scc1 rr_F58_%=\n\t" \
    "s_sub_u32 s68, s68, 1\n\t" \
    "s_sleep 2\n\t" \
    "s_branch rr_f2l57_%=\n\t" \
    "rr_F58_%=:\n\t" \
    "v_mov_b32 v187, 6095104\n\t" \
    "v_mov_b32 v188, 11\n\t" \
    "global_store_dword v187, v188, s[36:37] sc1\n\t" \
    "s_waitcnt vmcnt(0)\n\t" \
    "s_branch rr_f256_%=\n\t" \
    "rr_f256_%=:\n\t" \
    "s_cmp_eq_u32 s51, 0\n\t" \
    "s_cbranch_scc1 rr_sl24_%=\n\t" \
    "s_add_u32 s69, s65, -1\n\t" \
    "s_bitcmp1_b32 s69, 0\n\t" \
    "s_cselect_b32 s70, s63, s62\n\t" \
    "s_bfe_u32 s69, s69, 0x20001\n\t" \
    "s_lshl_b32 s69, s69, 19\n\t" \
    "s_add_u32 s69, s69, s70\n\t" \
    "s_add_u32 s46, s36, s69\n\t" \
    "s_addc_u32 s47, s37, 0\n\t" \
    "s_add_u32 s79, s80, 480\n\t" \
    "s_sub_u32 s70, s79, s74\n\t" \
    "s_max_i32 s70, s70, 0\n\t" \
    "s_min_i32 s70, s70, 16000\n\t" \
    "s_lshl_b32 s70, s70, 1\n\t" \
    "s_mul_hi_u32 s70, s70, s77\n\t" \
    "s_add_u32 s70, s70, s73\n\t" \
    "rr_W59_%=:\n\t" \
    "s_memtime s[56:57]\n\t" \
    "s_waitcnt lgkmcnt(0)\n\t" \
    "s_sub_u32 s69, s70, s56\n\t" \
    "s_cmp_gt_i32 s69, 24\n\t" \
    "s_cbranch_scc1 rr_W59_%=\n\t" \
    "v_add_u32 v187, 4096, v7\n\t" \
    "global_load_dword v184, v187, s[46:47] sc1\n\t" \
    "s_waitcnt vmcnt(0)\n\t" \
    "v_cmp_eq_u32 vcc, -1, v184\n\t" \
    "s_cbranch_vccz rr_Hok61_%=\n\t" \
    "rr_H60_%=:\n\t" \
    "global_load_dword v184, v187, s[46:47] sc1\n\t" \
    "s_waitcnt vmcnt(0)\n\t" \
    "v_cmp_eq_u32 vcc, -1, v184\n\t" \
    "s_cbranch_vccz rr_Hc62_%=\n\t" \
    "s_cmp_eq_u32 s68, 0\n\t" \
    "s_cbranch_scc1 rr_F63_%=\n\t" \
    "s_sub_u32 s68, s68, 1\n\t" \
    "s_sleep 4\n\t" \
    "s_branch rr_H60_%=\n\t" \
    "rr_F63_%=:\n\t" \
    "v_mov_b32 v185, 6095104\n\t" \
    "v_mov_b32 v186, 19\n\t" \
    "global_store_dword v185, v186, s[36:37] sc1\n\t" \
    "s_waitcnt vmcnt(0)\n\t" \
    "s_branch rr_Hc62_%=\n\t" \
    "rr_Hc62_%=:\n\t" \
    "rr_Hok61_%=:\n\t" \
    "v_subrev_u32 v184, s80, v184\n\t" \
    "s_nop 1\n\t" \
    "v_max_i32_dpp v184, v184, v184 row_ror:1 row_mask:0xf bank_mask:0xf\n\t" \
    "s_nop 1\n\t" \
    "v_max_i32_dpp v184, v184, v184 row_ror:2 row_mask:0xf bank_mask:0xf\n\t" \
    "s_nop 1\n\t" \
    "v_max_i32_dpp v184, v184, v184 row_ror:4 row_mask:0xf bank_mask:0xf\n\t" \
    "s_nop 1\n\t" \
    "v_max_i32_dpp v184, v184, v184 row_ror:8 row_mask:0xf bank_mask:0xf\n\t" \
    "s_nop 0\n\t" \
    "v_readlane_b32 s69, v184, 0\n\t" \
    "v_readlane_b32 s71, v184, 16\n\t" \
    "s_nop 1\n\t" \
    "s_max_i32 s69, s69, s71\n\t" \
    "s_add_u32 s81, s69, s80\n\t" \
    "s_mul_hi_u32 s69, s85, s78\n\t" \
    "s_add_u32 s79, s81, s69\n\t" \
    "s_add_u32 s79, s79, s87\n\t" \
    "s_cmp_ge_u32 s65, 37\n\t" \
    "s_cselect_b32 s69, 28, 0\n\t" \
    "s_sub_u32 s79, s79, s69\n\t" \
    "s_cmp_lt_u32 s65, 9\n\t" \
    "s_cselect_b32 s69, 96, 0\n\t" \
    "s_add_u32 s79, s79, s69\n\t" \
    "s_cmp_ge_u32 s65, 58\n\t" \
    "s_cselect_b32 s69, 96, 0\n\t" \
    "s_add_u32 s79, s79, s69\n\t" \
    "s_add_u32 s80, s79, s88\n\t" \
    "s_add_u32 s79, s79, 96\n\t" \
    "s_add_u32 s69, s65, 1\n\t" \
    "s_mul_i32 s70, s69, 171\n\t" \
    "s_lshr_b32 s70, s70, 9\n\t" \
    "s_mul_i32 s70, s70, 3\n\t" \
    "s_sub_u32 s69, s69, s70\n\t" \
    "s_mul_i32 s69, s69, 36\n\t" \
    "s_lshl_b32 s69, s69, 14\n\t" \
    "s_add_u32 s69, s69, s64\n\t" \
    "s_add_u32 s69, s69, 4194304\n\t" \
    "s_add_u32 s40, s36, s69\n\t" \
    "s_addc_u32 s41, s37, 0\n\t" \
    "s_sub_u32 s70, s79, s74\n\t" \
    "s_max_i32 s70, s70, 0\n\t" \
    "s_min_i32 s70, s70, 16000\n\t" \
    "s_lshl_b32 s70, s70, 1\n\t" \
    "s_mul_hi_u32 s70, s70, s77\n\t" \
    "s_add_u32 s70, s70, s73\n\t" \
    "rr_W64_%=:\n\t" \
    "s_memtime s[56:57]\n\t" \
    "s_waitcnt lgkmcnt(0)\n\t" \
    "s_sub_u32 s69, s70, s56\n\t" \
    "s_cmp_gt_i32 s69, 24\n\t" \
    "s_cbranch_scc1 rr_W64_%=\n\t" \
    "global_store_dwordx4 v196, v[172:175], s[40:41] sc1\n\t" \
    "global_store_dwordx4 v197, v[172:175], s[40:41] sc1\n\t" \
    "global_store_dwordx4 v198, v[172:175], s[40:41] sc1\n\t" \
    "s_branch rr_u226_%=\n\t" \
    "rr_sl24_%=:\n\t" \
    "ds_read_b64 v[184:185], v9\n\t" \
    "s_sub_u32 s70, s65, 1\n\t" \
    "s_waitcnt lgkmcnt(0)\n\t" \
    "v_readfirstlane_b32 s69, v185\n\t" \
    "v_readfirstlane_b32 s81, v184\n\t" \
    "s_nop 0\n\t" \
    "s_cmp_eq_u32 s69, s70\n\t" \
    "s_cbranch_scc1 rr_u226_%=\n\t" \
    "s_cmp_eq_u32 s68, 0\n\t" \
    "s_cbranch_scc1 rr_F65_%=\n\t" \
    "s_sub_u32 s68, s68, 1\n\t" \
    "s_sleep 2\n\t" \
    "s_branch rr_sl24_%=\n\t" \
    "rr_F65_%=:\n\t" \
    "v_mov_b32 v184, 6095104\n\t" \
    "v_mov_b32 v185, 14\n\t" \
    "global_store_dword v184, v185, s[36:37] sc1\n\t" \
    "s_waitcnt vmcnt(0)\n\t" \
    "s_branch rr_u226_%=\n\t" \
    "rr_u226_%=:\n\t" \
    "s_add_u32 s69, s65, -1\n\t" \
    "s_bitcmp1_b32 s69, 0\n\t" \
    "s_cselect_b32 s70, s63, s62\n\t" \
    "s_bfe_u32 s69, s69, 0x20001\n\t" \
    "s_lshl_b32 s69, s69, 19\n\t" \
    "s_add_u32 s69, s69, s70\n\t" \
    "s_add_u32 s46, s36, s69\n\t" \
    "s_addc_u32 s47, s37, 0\n\t" \
    "s_cmp_lg_u32 s65, 1\n\t" \
    "s_cbranch_scc1 rr_Oc68_%=\n\t" \
    "s_add_u32 s79, s81, 480\n\t" \
    "s_sub_u32 s70, s79, s74\n\t" \
    "s_max_i32 s70, s70, 0\n\t" \
    "s_min_i32 s70, s70, 16000\n\t" \
    "s_lshl_b32 s70, s70, 1\n\t" \
    "s_mul_hi_u32 s70, s70, s77\n\t" \
    "s_add_u32 s70, s70, s73\n\t" \
    "rr_W69_%=:\n\t" \
    "s_memtime s[56:57]\n\t" \
    "s_waitcnt lgkmcnt(0)\n\t" \
    "s_sub_u32 s69, s70, s56\n\t" \
    "s_cmp_gt_i32 s69, 24\n\t" \
    "s_cbranch_scc1 rr_W69_%=\n\t" \
    "global_load_dword v184, v46, s[46:47] sc1\n\t" \
    "s_waitcnt vmcnt(0)\n\t" \
    "v_cmp_eq_u32 vcc, -1, v184\n\t" \
    "s_cbranch_vccz rr_Ook67_%=\n\t" \
    "rr_O66_%=:\n\t" \
    "global_load_dword v184, v46, s[46:47] offset:1536 sc1\n\t" \
    "s_waitcnt vmcnt(0)\n\t" \
    "v_cmp_eq_u32 vcc, -1, v184\n\t" \
    "s_cbranch_vccz rr_Ook67_%=\n\t" \
    "s_cmp_eq_u32 s68, 0\n\t" \
    "s_cbranch_scc1 rr_F70_%=\n\t" \
    "s_sub_u32 s68, s68, 1\n\t" \
    "s_sleep 3\n\t" \
    "s_branch rr_O66_%=\n\t" \
    "rr_F70_%=:\n\t" \
    "v_mov_b32 v185, 6095104\n\t" \
    "v_mov_b32 v186, 18\n\t" \
    "global_store_dword v185, v186, s[36:37] sc1\n\t" \
    "s_waitcnt vmcnt(0)\n\t" \
    "s_branch rr_Ook67_%=\n\t" \
    "rr_Ook67_%=:\n\t" \
    "v_subrev_u32 v184, s80, v184\n\t" \
    "s_nop 1\n\t" \
    "v_max_i32_dpp v184, v184, v184 row_ror:1 row_mask:0xf bank_mask:0xf\n\t" \
    "s_nop 1\n\t" \
    "v_max_i32_dpp v184, v184, v184 row_ror:2 row_mask:0xf bank_mask:0xf\n\t" \
    "s_nop 1\n\t" \
    "v_max_i32_dpp v184, v184, v184 row_ror:4 row_mask:0xf bank_mask:0xf\n\t" \
    "s_nop 1\n\t" \
    "v_max_i32_dpp v184, v184, v184 row_ror:8 row_mask:0xf bank_mask:0xf\n\t" \
    "s_nop 0\n\t" \
    "v_readlane_b32 s69, v184, 0\n\t" \
    "v_readlane_b32 s71, v184, 16\n\t" \
    "v_readlane_b32 s70, v184, 32\n\t" \
    "v_readlane_b32 s81, v184, 48\n\t" \
    "s_max_i32 s69, s69, s71\n\t" \
    "s_nop 0\n\t" \
    "s_max_i32 s71, s70, s81\n\t" \
    "s_max_i32 s69, s69, s71\n\t" \
    "s_add_u32 s81, s69, s80\n\t" \
    "rr_Oc68_%=:\n\t" \
    "s_mul_hi_u32 s69, s85, s78\n\t" \
    "s_add_u32 s79, s81, s69\n\t" \
    "s_add_u32 s79, s79, s87\n\t" \
    "s_cmp_ge_u32 s65, 37\n\t" \
    "s_cselect_b32 s69, 28, 0\n\t" \
    "s_sub_u32 s79, s79, s69\n\t" \
    "s_cmp_lt_u32 s65, 9\n\t" \
    "s_cselect_b32 s69, 96, 0\n\t" \
    "s_add_u32 s79, s79, s69\n\t" \
    "s_cmp_ge_u32 s65, 58\n\t" \
    "s_cselect_b32 s69, 96, 0\n\t" \
    "s_add_u32 s79, s79, s69\n\t" \
    "s_add_u32 s80, s79, s88\n\t" \
    "s_sub_u32 s70, s79, s74\n\t" \
    "s_max_i32 s70, s70, 0\n\t" \
    "s_min_i32 s70, s70, 16000\n\t" \
    "s_lshl_b32 s70, s70, 1\n\t" \
    "s_mul_hi_u32 s70, s70, s77\n\t" \
    "s_add_u32 s70, s70, s73\n\t" \
    "rr_W71_%=:\n\t" \
    "s_memtime s[56:57]\n\t" \
    "s_waitcnt lgkmcnt(0)\n\t" \
    "s_sub_u32 s69, s70, s56\n\t" \
    "s_cmp_gt_i32 s69, 24\n\t" \
    "s_cbranch_scc1 rr_W71_%=\n\t" \
    "rr_act7_%=:\n\t" \
    "s_add_u32 s69, s65, 0\n\t" \
    "s_bitcmp1_b32 s69, 0\n\t" \
    "s_cselect_b32 s70, s63, s62\n\t" \
    "s_bfe_u32 s69, s69, 0x20001\n\t" \
    "s_lshl_b32 s69, s69, 19\n\t" \
    "s_add_u32 s69, s69, s70\n\t" \
    "s_add_u32 s46, s36, s69\n\t" \
    "s_addc_u32 s47, s37, 0\n\t" \
    "s_add_u32 s69, s65, 1\n\t" \
    "s_bitcmp1_b32 s69, 0\n\t" \
    "s_cselect_b32 s70, s63, s62\n\t" \
    "s_bfe_u32 s69, s69, 0x20001\n\t" \
    "s_lshl_b32 s69, s69, 19\n\t" \
    "s_add_u32 s69, s69, s70\n\t" \
    "s_add_u32 s48, s36, s69\n\t" \
    "s_addc_u32 s49, s37, 0\n\t" \
    "s_add_u32 s69, s65, 0\n\t" \
    "s_mul_i32 s70, s69, 171\n\t" \
    "s_lshr_b32 s70, s70, 9\n\t" \
    "s_mul_i32 s70, s70, 3\n\t" \
    "s_sub_u32 s69, s69, s70\n\t" \
    "s_mul_i32 s69, s69, 36\n\t" \
    "s_add_u32 s69, s69, s67\n\t" \
    "s_lshl_b32 s69, s69, 14\n\t" \
    "s_add_u32 s69, s69, s64\n\t" \
    "s_add_u32 s69, s69, 4194304\n\t" \
    "s_add_u32 s40, s36, s69\n\t" \
    "s_addc_u32 s41, s37, 0\n\t" \
    "s_add_u32 s69, s65, 2\n\t" \
    "s_cmp_ge_i32 s83, s69\n\t" \
    "s_cbranch_scc1 rr_nf72_%=\n\t" \
    "global_load_dword v177, v6, s[36:37] sc1\n\t" \
    "rr_nf72_%=:\n\t" \
    "s_mov_b32 s71, 3\n\t" \
    "rr_E73_%=:\n\t" \
    "s_memtime s[54:55]\n\t" \
    "s_nop 0\n\t" \
    "s_memrealtime s[4:5]\n\t" \
    "s_nop 0\n\t" \
    "s_memrealtime s[6:7]\n\t" \
    "s_nop 0\n\t" \
    "s_memrealtime s[8:9]\n\t" \
    "s_nop 0\n\t" \
    "s_memrealtime s[10:11]\n\t" \
    "s_nop 0\n\t" \
    "s_memrealtime s[12:13]\n\t" \
    "s_nop 0\n\t" \
    "s_memrealtime s[14:15]\n\t" \
    "s_nop 0\n\t" \
    "s_memrealtime s[16:17]\n\t" \
    "s_nop 0\n\t" \
    "s_memrealtime s[18:19]\n\t" \
    "s_nop 0\n\t" \
    "s_memrealtime s[20:21]\n\t" \
    "s_nop 0\n\t" \
    "s_memrealtime s[22:23]\n\t" \
    "s_nop 0\n\t" \
    "s_memrealtime s[24:25]\n\t" \
    "s_nop 0\n\t" \
    "s_memrealtime s[26:27]\n\t" \
    "s_nop 0\n\t" \
    "s_memrealtime s[28:29]\n\t" \
    "s_nop 0\n\t" \
    "s_memtime s[56:57]\n\t" \
    "s_mov_b32 s70, 1\n\t" \
    "s_sub_u32 s71, s71, 1\n\t" \
    "s_cselect_b32 s69, 0, -1\n\t" \
    "s_waitcnt lgkmcnt(0)\n\t" \
    "s_sub_u32 s54, s56, s54\n\t" \
    "s_and_b32 s54, s54, s69\n\t" \
    "s_cmp_gt_u32 s54, 124\n\t" \
    "s_cbranch_scc1 rr_E73_%=\n\t" \
    "s_cmp_eq_u32 s4, s28\n\t" \
    "s_addc_u32 s70, s70, 0\n\t" \
    "s_cmp_eq_u32 s6, s28\n\t" \
    "s_addc_u32 s70, s70, 0\n\t" \
    "s_cmp_eq_u32 s8, s28\n\t" \
    "s_addc_u32 s70, s70, 0\n\t" \
    "s_cmp_eq_u32 s10, s28\n\t" \
    "s_addc_u32 s70, s70, 0\n\t" \
    "s_cmp_eq_u32 s12, s28\n\t" \
    "s_addc_u32 s70, s70, 0\n\t" \
    "s_cmp_eq_u32 s14, s28\n\t" \
    "s_addc_u32 s70, s70, 0\n\t" \
    "s_cmp_eq_u32 s16, s28\n\t" \
    "s_addc_u32 s70, s70, 0\n\t" \
    "s_cmp_eq_u32 s18, s28\n\t" \
    "s_addc_u32 s70, s70, 0\n\t" \
    "s_cmp_eq_u32 s20, s28\n\t" \
    "s_addc_u32 s70, s70, 0\n\t" \
    "s_cmp_eq_u32 s22, s28\n\t" \
    "s_addc_u32 s70, s70, 0\n\t" \
    "s_cmp_eq_u32 s24, s28\n\t" \
    "s_addc_u32 s70, s70, 0\n\t" \
    "s_cmp_eq_u32 s26, s28\n\t" \
    "s_addc_u32 s70, s70, 0\n\t" \
    "s_mov_b32 s75, s73\n\t" \
    "s_mov_b32 s76, s74\n\t" \
    "s_lshl_b32 s70, s70, 3\n\t" \
    "s_sub_u32 s73, s56, s70\n\t" \
    "s_lshl_b32 s74, s28, 4\n\t" \
    "s_sub_u32 s69, s73, s75\n\t" \
    "s_sub_u32 s70, s74, s76\n\t" \
    "s_sub_u32 s71, s70, 1600\n\t" \
    "s_cmp_lt_u32 s71, 400000\n\t" \
    "s_cbranch_scc0 rr_K75_%=\n\t" \
    "v_cvt_f32_u32 v184, s69\n\t" \
    "v_cvt_f32_u32 v185, s70\n\t" \
    "v_rcp_f32 v186, v184\n\t" \
    "v_rcp_f32 v187, v185\n\t" \
    "v_mul_f32 v186, v186, v185\n\t" \
    "v_mul_f32 v187, v187, v184\n\t" \
    "v_mul_f32 v186, 0x4f800000, v186\n\t" \
    "v_mul_f32 v187, 0x4f000000, v187\n\t" \
    "v_cvt_u32_f32 v186, v186\n\t" \
    "v_cvt_u32_f32 v187, v187\n\t" \
    "v_readfirstlane_b32 s69, v186\n\t" \
    "v_readfirstlane_b32 s70, v187\n\t" \
    "s_nop 1\n\t" \
    "s_sub_u32 s69, s69, s78\n\t" \
    "s_ashr_i32 s69, s69, 2\n\t" \
    "s_add_u32 s78, s78, s69\n\t" \
    "s_sub_u32 s70, s70, s77\n\t" \
    "s_ashr_i32 s70, s70, 2\n\t" \
    "s_add_u32 s77, s77, s70\n\t" \
    "rr_K75_%=:\n\t" \
    "s_mov_b32 s91, 0\n\t" \
    "v_mov_b32 v160, 0\n\t" \
    "v_mov_b32 v161, 0\n\t" \
    "v_mov_b32 v162, 0\n\t" \
    "v_mov_b32 v163, 0\n\t" \
    "s_sub_u32 s70, s80, s74\n\t" \
    "s_max_i32 s70, s70, 0\n\t" \
    "s_min_i32 s70, s70, 16000\n\t" \
    "s_lshl_b32 s70, s70, 1\n\t" \
    "s_mul_hi_u32 s70, s70, s77\n\t" \
    "s_add_u32 s70, s70, s73\n\t" \
    "s_sub_u32 s70, s70, 100\n\t" \
    "s_getpc_b64 s[30:31]\n\t" \
    "rr_Wp82_%=:\n\t" \
    "s_add_u32 s30, s30, rr_We83_%=-rr_Wp82_%=\n\t" \
    "s_addc_u32 s31, s31, 0\n\t" \
    "rr_W81_%=:\n\t" \
    "s_memtime s[56:57]\n\t" \
    "s_waitcnt lgkmcnt(0)\n\t" \
    "s_sub_u32 s69, s70, s56\n\t" \
    "s_cmp_gt_i32 s69, 111\n\t" \
    "s_cbranch_scc1 rr_W81_%=\n\t" \
    "s_max_i32 s69, s69, 0\n\t" \
    "s_lshr_b32 s69, s69, 1\n\t" \
    "s_and_b32 s69, s69, 0x3c\n\t" \
    "s_sub_u32 s30, s30, s69\n\t" \
    "s_subb_u32 s31, s31, 0\n\t" \
    "s_setpc_b64 s[30:31]\n\t" \
    ".p2align 6\n\t" \
    "s_nop 0\n\t" \
    "s_nop 0\n\t" \
    "s_nop 0\n\t" \
    "s_nop 1\n\t" \
    "s_nop 1\n\t" \
    "s_nop 1\n\t" \
    "s_nop 1\n\t" \
    "s_nop 1\n\t" \
    "s_nop 1\n\t" \
    "s_nop 1\n\t" \
    "s_nop 1\n\t" \
    "s_nop 1\n\t" \
    "s_nop 1\n\t" \
    "s_nop 1\n\t" \
    "s_nop 1\n\t" \
    "s_nop 1\n\t" \
    "rr_We83_%=:\n\t" \
    "global_load_dword v184, v7, s[46:47] sc1\n\t" \
    "s_sub_u32 s97, s74, s86\n\t" \
    "s_sub_u32 s71, s73, 190\n\t" \
    "s_getpc_b64 s[30:31]\n\t" \
    "rr_wp84_%=:\n\t" \
    "s_add_u32 s30, s30, rr_again9_%=-rr_wp84_%=\n\t" \
    "s_addc_u32 s31, s31, (rr_again9_%=-rr_wp84_%=)>>32\n\t" \
    "s_waitcnt vmcnt(0) lgkmcnt(0)\n\t" \
    "rr_hok76_%=:\n\t" \
    "s_memtime s[56:57]\n\t" \
    "v_cmp_eq_u32 vcc, -1, v184\n\t" \
    "v_subrev_u32 v185, s97, v184\n\t" \
    "s_nop 1\n\t" \
    "v_max_i32_dpp v185, v185, v185 row_ror:1 row_mask:0xf bank_mask:0xf\n\t" \
    "s_cbranch_vccnz rr_hlate77_%=\n\t" \
    "s_nop 0\n\t" \
    "v_max_i32_dpp v185, v185, v185 row_ror:2 row_mask:0xf bank_mask:0xf\n\t" \
    "s_nop 1\n\t" \
    "v_max_i32_dpp v185, v185, v185 row_ror:4 row_mask:0xf bank_mask:0xf\n\t" \
    "s_nop 1\n\t" \
    "v_max_i32_dpp v185, v185, v185 row_ror:8 row_mask:0xf bank_mask:0xf\n\t" \
    "s_nop 0\n\t" \
    "v_readlane_b32 s69, v185, 0\n\t" \
    "v_readlane_b32 s70, v185, 16\n\t" \
    "s_waitcnt lgkmcnt(0)\n\t" \
    "s_sub_u32 s71, s71, s56\n\t" \
    "s_max_i32 s81, s69, s70\n\t" \
    "s_max_i32 s69, s81, 0\n\t" \
    "s_lshl_b32 s69, s69, 1\n\t" \
    "s_mul_hi_u32 s69, s69, s77\n\t" \
    "s_add_u32 s69, s69, s71\n\t" \
    "s_cmp_gt_i32 s69, 111\n\t" \
    "s_cbranch_scc1 rr_wl78_%=\n\t" \
    "rr_wf80_%=:\n\t" \
    "s_max_i32 s69, s69, 0\n\t" \
    "s_lshr_b32 s69, s69, 1\n\t" \
    "s_and_b32 s69, s69, 0x3c\n\t" \
    "s_sub_u32 s30, s30, s69\n\t" \
    "s_subb_u32 s31, s31, 0\n\t" \
    "s_setpc_b64 s[30:31]\n\t" \
    "rr_wl78_%=:\n\t" \
    "s_min_i32 s69, s69, 24000\n\t" \
    "s_add_u32 s70, s69, s56\n\t" \
    "s_add_u32 s70, s70, 62\n\t" \
    "rr_wp79_%=:\n\t" \
    "s_memtime s[56:57]\n\t" \
    "s_waitcnt lgkmcnt(0)\n\t" \
    "s_sub_u32 s69, s70, s56\n\t" \
    "s_cmp_gt_i32 s69, 111\n\t" \
    "s_cbranch_scc1 rr_wp79_%=\n\t" \
    "s_branch rr_wf80_%=\n\t" \
    ".p2align 6\n\t" \
    "s_nop 0\n\t" \
    "s_nop 0\n\t" \
    "s_nop 0\n\t" \
    "s_nop 1\n\t" \
    "s_nop 1\n\t" \
    "s_nop 1\n\t" \
    "s_nop 1\n\t" \
    "s_nop 1\n\t" \
    "s_nop 1\n\t" \
    "s_nop 1\n\t" \
    "s_nop 1\n\t" \
    "s_nop 1\n\t" \
    "s_nop 1\n\t" \
    "s_nop 1\n\t" \
    "s_nop 1\n\t" \
    "s_nop 1\n\t" \
    "rr_again9_%=:\n\t" \
    "global_load_dwordx4 v[48:51], v10, s[46:47] sc1\n\t" \
    "global_load_dwordx4 v[52:55], v11, s[46:47] sc1\n\t" \
    "global_load_dwordx4 v[56:59], v12, s[46:47] sc1\n\t" \
    "global_load_dwordx4 v[60:63], v13, s[46:47] sc1\n\t" \
    "global_load_dwordx4 v[64:67], v14, s[46:47] sc1\n\t" \
    "global_load_dwordx4 v[68:71], v15, s[46:47] sc1\n\t" \
    "global_load_dwordx4 v[72:75], v16, s[46:47] sc1\n\t" \
    "global_load_dwordx4 v[76:79], v17, s[46:47] sc1\n\t" \
    "global_load_dwordx4 v[80:83], v18, s[46:47] sc1\n\t" \
    "global_load_dwordx4 v[84:87], v19, s[46:47] sc1\n\t" \
    "global_load_dwordx4 v[88:91], v20, s[46:47] sc1\n\t" \
    "global_load_dwordx4 v[92:95], v21, s[46:47] sc1\n\t" \
    "global_load_dwordx4 v[96:99], v22, s[46:47] sc1\n\t" \
    "global_load_dwordx4 v[100:103], v23, s[46:47] sc1\n\t" \
    "global_load_dwordx4 v[104:107], v24, s[46:47] sc1\n\t" \
    "global_load_dwordx4 v[108:111], v25, s[46:47] sc1\n\t" \
    "global_load_dwordx4 v[112:115], v42, s[46:47] sc1\n\t" \
    "global_load_dwordx4 v[116:119], v43, s[46:47] sc1\n\t" \
    "global_load_dwordx4 v[120:123], v44, s[46:47] sc1\n\t" \
    "global_load_dwordx4 v[124:127], v45, s[46:47] sc1\n\t" \
    "global_load_dwordx4 v[128:131], v0, s[40:41] offset:0 sc1\n\t" \
    "global_load_dwordx4 v[132:135], v0, s[40:41] offset:1024 sc1\n\t" \
    "global_load_dwordx4 v[136:139], v0, s[40:41] offset:2048 sc1\n\t" \
    "global_load_dwordx4 v[140:143], v0, s[40:41] offset:3072 sc1\n\t" \
    "global_load_dwordx4 v[144:147], v1, s[40:41] offset:0 sc1\n\t" \
    "global_load_dwordx4 v[148:151], v1, s[40:41] offset:1024 sc1\n\t" \
    "global_load_dwordx4 v[152:155], v1, s[40:41] offset:2048 sc1\n\t" \
    "global_load_dwordx4 v[156:159], v1, s[40:41] offset:3072 sc1\n\t" \
    "s_cmp_lg_u32 s91, 0\n\t" \
    "s_cbranch_scc1 rr_nob12_%=\n\t" \
    "s_add_u32 s81, s81, s97\n\t" \
    "v_mov_b32 v184, s81\n\t" \
    "v_mov_b32 v185, s65\n\t" \
    "ds_write_b64 v9, v[184:185]\n\t" \
    "rr_nob12_%=:\n\t" \
    "s_waitcnt vmcnt(27)\n\t" \
    "v_smfmac_f32_16x16x64_bf16 v[160:163], v[48:51], a[0:7], v4\n\t" \
    "s_waitcnt vmcnt(26)\n\t" \
    "v_smfmac_f32_16x16x64_bf16 v[160:163], v[52:55], a[8:15], v4\n\t" \
    "s_waitcnt vmcnt(25)\n\t" \
    "v_smfmac_f32_16x16x64_bf16 v[160:163], v[56:59], a[16:23], v4\n\t" \
    "s_waitcnt vmcnt(24)\n\t" \
    "v_smfmac_f32_16x16x64_bf16 v[160:163], v[60:63], a[24:31], v4\n\t" \
    "s_waitcnt vmcnt(23)\n\t" \
    "v_smfmac_f32_16x16x64_bf16 v[160:163], v[64:67], a[32:39], v4\n\t" \
    "s_waitcnt vmcnt(22)\n\t" \
    "v_smfmac_f32_16x16x64_bf16 v[160:163], v[68:71], a[40:47], v4\n\t" \
    "s_waitcnt vmcnt(21)\n\t" \
    "v_smfmac_f32_16x16x64_bf16 v[160:163], v[72:75], a[48:55], v4\n\t" \
    "s_waitcnt vmcnt(20)\n\t" \
    "v_smfmac_f32_16x16x64_bf16 v[160:163], v[76:79], a[56:63], v4\n\t" \
    "s_waitcnt vmcnt(19)\n\t" \
    "v_smfmac_f32_16x16x64_bf16 v[160:163], v[80:83], a[64:71], v4\n\t" \
    "s_waitcnt vmcnt(18)\n\t" \
    "v_smfmac_f32_16x16x64_bf16 v[160:163], v[84:87], a[72:79], v4\n\t" \
    "s_waitcnt vmcnt(17)\n\t" \
    "v_smfmac_f32_16x16x64_bf16 v[160:163], v[88:91], a[80:87], v4\n\t" \
    "s_waitcnt vmcnt(16)\n\t" \
    "v_smfmac_f32_16x16x64_bf16 v[160:163], v[92:95], a[88:95], v4\n\t" \
    "s_waitcnt vmcnt(15)\n\t" \
    "v_smfmac_f32_16x16x64_bf16 v[160:163], v[96:99], a[96:103], v4\n\t" \
    "s_waitcnt vmcnt(14)\n\t" \
    "v_smfmac_f32_16x16x64_bf16 v[160:163], v[100:103], a[104:111], v4\n\t" \
    "s_waitcnt vmcnt(13)\n\t" \
    "v_smfmac_f32_16x16x64_bf16 v[160:163], v[104:107], a[112:119], v4\n\t" \
    "s_waitcnt vmcnt(12)\n\t" \
    "v_smfmac_f32_16x16x64_bf16 v[160:163], v[108:111], a[120:127], v4\n\t" \
    "s_waitcnt vmcnt(6)\n\t" \
    "v_smfmac_f32_16x16x64_bf16 v[160:163], v[112:115], v[128:135], v4\n\t" \
    "s_waitcnt vmcnt(4)\n\t" \
    "v_smfmac_f32_16x16x64_bf16 v[160:163], v[116:119], v[136:143], v4\n\t" \
    "s_waitcnt vmcnt(2)\n\t" \
    "v_smfmac_f32_16x16x64_bf16 v[160:163], v[120:123], v[144:151], v4\n\t" \
    "s_waitcnt vmcnt(0)\n\t" \
    "v_smfmac_f32_16x16x64_bf16 v[160:163], v[124:127], v[152:159], v4\n\t" \
    "s_nop 7\n\t" \
    "v_pk_add_f32 v[160:161], v[160:161], v[162:163]\n\t" \
    "v_cvt_pk_bf16_f32 v184, v160, v161\n\t" \
    "v_cmp_u_f32 vcc, v160, v161\n\t" \
    "s_cbranch_vccnz rr_bad10_%=\n\t" \
    "rr_cont11_%=:\n\t" \
    "v_mov_b32_dpp v185, v184 quad_perm:[1,0,3,2] row_mask:0xf bank_mask:0xf\n\t" \
    "v_perm_b32 v184, v185, v184, v5\n\t" \
    "global_store_dword v2, v184, s[48:49] sc1\n\t" \
    "global_store_dword v181, v184, s[48:49] sc1\n\t" \
    "global_store_dword v182, v184, s[48:49] sc1\n\t" \
    "global_store_dword v183, v184, s[48:49] sc1\n\t" \
    "s_cmp_eq_u32 s91, 3\n\t" \
    "s_cbranch_scc1 rr_dry27_%=\n\t" \
    "s_cmp_eq_u32 s65, 62\n\t" \
    "s_cbranch_scc1 rr_end8_%=\n\t" \
    "s_add_u32 s65, s65, 2\n\t" \
    "s_branch rr_pf13_%=\n\t" \
    "rr_bad10_%=:\n\t" \
    "s_waitcnt lgkmcnt(0)\n\t" \
    "v_max_u32 v185, v48, v49\n\t" \
    "v_max3_u32 v185, v185, v50, v51\n\t" \
    "v_max3_u32 v185, v185, v52, v53\n\t" \
    "v_max3_u32 v185, v185, v54, v55\n\t" \
    "v_max3_u32 v185, v185, v56, v57\n\t" \
    "v_max3_u32 v185, v185, v58, v59\n\t" \
    "v_max3_u32 v185, v185, v60, v61\n\t" \
    "v_max3_u32 v185, v185, v62, v63\n\t" \
    "v_max3_u32 v185, v185, v64, v65\n\t" \
    "v_max3_u32 v185, v185, v66, v67\n\t" \
    "v_max3_u32 v185, v185, v68, v69\n\t" \
    "v_max3_u32 v185, v185, v70, v71\n\t" \
    "v_max3_u32 v185, v185, v72, v73\n\t" \
    "v_max3_u32 v185, v185, v74, v75\n\t" \
    "v_max3_u32 v185, v185, v76, v77\n\t" \
    "v_max3_u32 v185, v185, v78, v79\n\t" \
    "v_max3_u32 v185, v185, v80, v81\n\t" \
    "v_max3_u32 v185, v185, v82, v83\n\t" \
    "v_max3_u32 v185, v185, v84, v85\n\t" \
    "v_max3_u32 v185, v185, v86, v87\n\t" \
    "v_max3_u32 v185, v185, v88, v89\n\t" \
    "v_max3_u32 v185, v185, v90, v91\n\t" \
    "v_max3_u32 v185, v185, v92, v93\n\t" \
    "v_max3_u32 v185, v185, v94, v95\n\t" \
    "v_max3_u32 v185, v185, v96, v97\n\t" \
    "v_max3_u32 v185, v185, v98, v99\n\t" \
    "v_max3_u32 v185, v185, v100, v101\n\t" \
    "v_max3_u32 v185, v185, v102, v103\n\t" \
    "v_max3_u32 v185, v185, v104, v105\n\t" \
    "v_max3_u32 v185, v185, v106, v107\n\t" \
    "v_max3_u32 v185, v185, v108, v109\n\t" \
    "v_max3_u32 v185, v185, v110, v111\n\t" \
    "v_max3_u32 v185, v185, v112, v113\n\t" \
    "v_max3_u32 v185, v185, v114, v115\n\t" \
    "v_max3_u32 v185, v185, v116, v117\n\t" \
    "v_max3_u32 v185, v185, v118, v119\n\t" \
    "v_max3_u32 v185, v185, v120, v121\n\t" \
    "v_max3_u32 v185, v185, v122, v123\n\t" \
    "v_max3_u32 v185, v185, v124, v125\n\t" \
    "v_max3_u32 v185, v185, v126, v127\n\t" \
    "v_max_u32 v186, v128, v129\n\t" \
    "v_max3_u32 v186, v186, v130, v131\n\t" \
    "v_max3_u32 v186, v186, v132, v133\n\t" \
    "v_max3_u32 v186, v186, v134, v135\n\t" \
    "v_max3_u32 v186, v186, v136, v137\n\t" \
    "v_max3_u32 v186, v186, v138, v139\n\t" \
    "v_max3_u32 v186, v186, v140, v141\n\t" \
    "v_max3_u32 v186, v186, v142, v143\n\t" \
    "v_max3_u32 v186, v186, v144, v145\n\t" \
    "v_max3_u32 v186, v186, v146, v147\n\t" \
    "v_max3_u32 v186, v186, v148, v149\n\t" \
    "v_max3_u32 v186, v186, v150, v151\n\t" \
    "v_max3_u32 v186, v186, v152, v153\n\t" \
    "v_max3_u32 v186, v186, v154, v155\n\t" \
    "v_max3_u32 v186, v186, v156, v157\n\t" \
    "v_max3_u32 v186, v186, v158, v159\n\t" \
    "v_max_u32 v185, v185, v186\n\t" \
    "v_cmp_eq_u32 vcc, -1, v185\n\t" \
    "s_cbranch_vccz rr_cont11_%=\n\t" \
    "s_mov_b32 s91, 1\n\t" \
    "v_mov_b32 v160, 0\n\t" \
    "v_mov_b32 v161, 0\n\t" \
    "v_mov_b32 v162, 0\n\t" \
    "v_mov_b32 v163, 0\n\t" \
    "s_cmp_eq_u32 s68, 0\n\t" \
    "s_cbranch_scc1 rr_F85_%=\n\t" \
    "s_sub_u32 s68, s68, 1\n\t" \
    "s_branch rr_again9_%=\n\t" \
    "rr_F85_%=:\n\t" \
    "v_mov_b32 v186, 6095104\n\t" \
    "v_mov_b32 v187, 3\n\t" \
    "global_store_dword v186, v187, s[36:37] sc1\n\t" \
    "s_waitcnt vmcnt(0)\n\t" \
    "s_branch rr_cont11_%=\n\t" \
    "rr_hlate77_%=:\n\t" \
    "s_waitcnt lgkmcnt(0)\n\t" \
    "v_add_u32 v189, 1024, v7\n\t" \
    "global_load_dword v186, v189, s[46:47] sc1\n\t" \
    "s_sleep 4\n\t" \
    "v_add_u32 v189, 2048, v7\n\t" \
    "global_load_dword v187, v189, s[46:47] sc1\n\t" \
    "s_waitcnt vmcnt(1)\n\t" \
    "v_cmp_eq_u32 vcc, -1, v186\n\t" \
    "s_cbranch_vccz rr_hg86_%=\n\t" \
    "v_add_u32 v189, 3072, v7\n\t" \
    "global_load_dword v186, v189, s[46:47] sc1\n\t" \
    "s_waitcnt vmcnt(1)\n\t" \
    "v_cmp_eq_u32 vcc, -1, v187\n\t" \
    "s_cbranch_vccz rr_hg87_%=\n\t" \
    "s_waitcnt vmcnt(0)\n\t" \
    "v_cmp_eq_u32 vcc, -1, v186\n\t" \
    "s_cbranch_vccz rr_hg86_%=\n\t" \
    "rr_hp88_%=:\n\t" \
    "global_load_dword v184, v189, s[46:47] sc1\n\t" \
    "s_waitcnt vmcnt(0)\n\t" \
    "v_cmp_eq_u32 vcc, -1, v184\n\t" \
    "s_cbranch_vccz rr_hpd89_%=\n\t" \
    "s_cmp_eq_u32 s68, 0\n\t" \
    "s_cbranch_scc1 rr_F90_%=\n\t" \
    "s_sub_u32 s68, s68, 1\n\t" \
    "s_sleep 1\n\t" \
    "s_branch rr_hp88_%=\n\t" \
    "rr_F90_%=:\n\t" \
    "v_mov_b32 v186, 6095104\n\t" \
    "v_mov_b32 v187, 7\n\t" \
    "global_store_dword v186, v187, s[36:37] sc1\n\t" \
    "s_waitcnt vmcnt(0)\n\t" \
    "s_branch rr_hpd89_%=\n\t" \
    "rr_hpd89_%=:\n\t" \
    "rr_h025_%=:\n\t" \
    "v_and_b32 v184, -2, v184\n\t" \
    "s_mov_b32 s71, 3\n\t" \
    "rr_E91_%=:\n\t" \
    "s_memtime s[54:55]\n\t" \
    "s_nop 0\n\t" \
    "s_memrealtime s[4:5]\n\t" \
    "s_nop 0\n\t" \
    "s_memrealtime s[6:7]\n\t" \
    "s_nop 0\n\t" \
    "s_memrealtime s[8:9]\n\t" \
    "s_nop 0\n\t" \
    "s_memrealtime s[10:11]\n\t" \
    "s_nop 0\n\t" \
    "s_memrealtime s[12:13]\n\t" \
    "s_nop 0\n\t" \
    "s_memrealtime s[14:15]\n\t" \
    "s_nop 0\n\t" \
    "s_memrealtime s[16:17]\n\t" \
    "s_nop 0\n\t" \
    "s_memrealtime s[18:19]\n\t" \
    "s_nop 0\n\t" \
    "s_memrealtime s[20:21]\n\t" \
    "s_nop 0\n\t" \
    "s_memrealtime s[22:23]\n\t" \
    "s_nop 0\n\t" \
    "s_memrealtime s[24:25]\n\t" \
    "s_nop 0\n\t" \
    "s_memrealtime s[26:27]\n\t" \
    "s_nop 0\n\t" \
    "s_memrealtime s[28:29]\n\t" \
    "s_nop 0\n\t" \
    "s_memtime s[56:57]\n\t" \
    "s_mov_b32 s70, 1\n\t" \
    "s_sub_u32 s71, s71, 1\n\t" \
    "s_cselect_b32 s69, 0, -1\n\t" \
    "s_waitcnt lgkmcnt(0)\n\t" \
    "s_sub_u32 s54, s56, s54\n\t" \
    "s_and_b32 s54, s54, s69\n\t" \
    "s_cmp_gt_u32 s54, 124\n\t" \
    "s_cbranch_scc1 rr_E91_%=\n\t" \
    "s_cmp_eq_u32 s4, s28\n\t" \
    "s_addc_u32 s70, s70, 0\n\t" \
    "s_cmp_eq_u32 s6, s28\n\t" \
    "s_addc_u32 s70, s70, 0\n\t" \
    "s_cmp_eq_u32 s8, s28\n\t" \
    "s_addc_u32 s70, s70, 0\n\t" \
    "s_cmp_eq_u32 s10, s28\n\t" \
    "s_addc_u32 s70, s70, 0\n\t" \
    "s_cmp_eq_u32 s12, s28\n\t" \
    "s_addc_u32 s70, s70, 0\n\t" \
    "s_cmp_eq_u32 s14, s28\n\t" \
    "s_addc_u32 s70, s70, 0\n\t" \
    "s_cmp_eq_u32 s16, s28\n\t" \
    "s_addc_u32 s70, s70, 0\n\t" \
    "s_cmp_eq_u32 s18, s28\n\t" \
    "s_addc_u32 s70, s70, 0\n\t" \
    "s_cmp_eq_u32 s20, s28\n\t" \
    "s_addc_u32 s70, s70, 0\n\t" \
    "s_cmp_eq_u32 s22, s28\n\t" \
    "s_addc_u32 s70, s70, 0\n\t" \
    "s_cmp_eq_u32 s24, s28\n\t" \
    "s_addc_u32 s70, s70, 0\n\t" \
    "s_cmp_eq_u32 s26, s28\n\t" \
    "s_addc_u32 s70, s70, 0\n\t" \
    "s_mov_b32 s75, s73\n\t" \
    "s_mov_b32 s76, s74\n\t" \
    "s_lshl_b32 s70, s70, 3\n\t" \
    "s_sub_u32 s73, s56, s70\n\t" \
    "s_lshl_b32 s74, s28, 4\n\t" \
    "s_sub_u32 s97, s74, s86\n\t" \
    "s_sub_u32 s71, s73, 190\n\t" \
    "s_getpc_b64 s[30:31]\n\t" \
    "rr_wp93_%=:\n\t" \
    "s_add_u32 s30, s30, rr_again9_%=-rr_wp93_%=\n\t" \
    "s_addc_u32 s31, s31, (rr_again9_%=-rr_wp93_%=)>>32\n\t" \
    "s_branch rr_hok76_%=\n\t" \
    "rr_hg86_%=:\n\t" \
    "v_mov_b32 v184, v186\n\t" \
    "s_branch rr_hok76_%=\n\t" \
    "rr_hg87_%=:\n\t" \
    "v_mov_b32 v184, v187\n\t" \
    "s_branch rr_hok76_%=\n\t" \
    "rr_end8_%=:\n\t" \
    "s_waitcnt vmcnt(0) lgkmcnt(0)\n\t" \
    "s_endpgm\n\t" \
    "rr_last_%=:\n\t" \
    ""
#define ASM_O \
    "s_mov_b64 s[36:37], %0\n\t" \
    "s_mov_b64 s[42:43], %1\n\t" \
    "s_mov_b64 s[44:45], %2\n\t" \
    "s_mov_b32 s66, %3\n\t" \
    "s_mov_b32 s67, %4\n\t" \
    "s_mov_b64 s[50:51], %5\n\t" \
    "s_mov_b32 s82, %7\n\t" \
    "v_mbcnt_lo_u32_b32 v223, -1, 0\n\t" \
    "v_mbcnt_hi_u32_b32 v223, -1, v223\n\t" \
    "s_getpc_b64 s[46:47]\n\t" \
    "oo_pc_%=:\n\t" \
    "s_lshl_b32 s69, s66, 13\n\t" \
    "v_lshl_add_u32 v222, v223, 7, s69\n\t" \
    "v_min_u32 v222, oo_last_%=-oo_pc_%=-4, v222\n\t" \
    "global_load_dword v222, v222, s[46:47]\n\t" \
    "v_lshlrev_b32 v223, 2, v223\n\t" \
    "v_add_u32 v223, s82, v223\n\t" \
    "ds_read_b32 v0, v223 offset:0\n\t" \
    "ds_read_b32 v1, v223 offset:256\n\t" \
    "ds_read_b32 v2, v223 offset:512\n\t" \
    "ds_read_b32 v3, v223 offset:768\n\t" \
    "ds_read_b32 v4, v223 offset:1024\n\t" \
    "ds_read_b32 v5, v223 offset:1280\n\t" \
    "ds_read_b32 v6, v223 offset:1536\n\t" \
    "ds_read_b32 v7, v223 offset:1792\n\t" \
    "ds_read_b32 v8, v223 offset:2048\n\t" \
    "ds_read_b32 v9, v223 offset:2304\n\t" \
    "ds_read_b32 v10, v223 offset:2560\n\t" \
    "ds_read_b32 v11, v223 offset:2816\n\t" \
    "s_waitcnt lgkmcnt(0)\n\t" \
    "ds_read_b32 v12, v223 offset:3072\n\t" \
    "ds_read_b32 v13, v223 offset:3328\n\t" \
    "ds_read_b32 v14, v223 offset:3584\n\t" \
    "ds_read_b32 v15, v223 offset:3840\n\t" \
    "ds_read_b32 v16, v223 offset:4096\n\t" \
    "ds_read_b32 v17, v223 offset:4352\n\t" \
    "s_waitcnt lgkmcnt(0)\n\t" \
    "s_mov_b32 s68, 262144\n\t" \
    "s_mov_b32 s72, 0\n\t" \
    "s_mov_b32 s83, -1\n\t" \
    "s_mov_b32 s65, 0\n\t" \
    "s_barrier\n\t" \
    "s_mov_b32 s65, s67\n\t" \
    "s_mov_b32 s96, s67\n\t" \
    "s_add_u32 s38, s36, 48234496\n\t" \
    "s_addc_u32 s39, s37, 0\n\t" \
    "oo_poll1_%=:\n\t" \
    "s_cmp_gt_u32 s96, 26\n\t" \
    "s_cselect_b64 vcc, -1, 0\n\t" \
    "v_cndmask_b32 v25, v12, v13, vcc\n\t" \
    "global_load_dword v24, v25, s[36:37] sc1\n\t" \
    "s_waitcnt vmcnt(0)\n\t" \
    "v_cmp_gt_i32 vcc, s96, v24\n\t" \
    "s_cbranch_vccz oo_go2_%=\n\t" \
    "s_cmp_eq_u32 s68, 0\n\t" \
    "s_cbranch_scc1 oo_F7_%=\n\t" \
    "s_sub_u32 s68, s68, 1\n\t" \
    "s_sleep 10\n\t" \
    "s_branch oo_poll1_%=\n\t" \
    "oo_F7_%=:\n\t" \
    "v_mov_b32 v25, 6095104\n\t" \
    "v_mov_b32 v26, 40\n\t" \
    "global_store_dword v25, v26, s[36:37] sc1\n\t" \
    "s_waitcnt vmcnt(0)\n\t" \
    "s_branch oo_go2_%=\n\t" \
    "oo_go2_%=:\n\t" \
    "s_mul_i32 s69, s96, 65536\n\t" \
    "s_add_u32 s69, s69, 146800640\n\t" \
    "s_add_u32 s40, s36, s69\n\t" \
    "s_addc_u32 s41, s37, 0\n\t" \
    "s_lshl_b32 s69, s96, 15\n\t" \
    "v_xor_b32 v18, s69, v0\n\t" \
    "v_xor_b32 v19, s69, v1\n\t" \
    "v_xor_b32 v20, s69, v2\n\t" \
    "v_xor_b32 v21, s69, v3\n\t" \
    "v_xor_b32 v22, s69, v4\n\t" \
    "v_xor_b32 v23, s69, v5\n\t" \
    "global_load_dwordx4 v[32:35], v6, s[40:41] offset:-4096 sc1\n\t" \
    "global_load_dwordx4 v[36:39], v6, s[40:41] offset:-3072 sc1\n\t" \
    "global_load_dwordx4 a[0:3], v18, s[38:39] offset:-4096 sc1\n\t" \
    "global_load_dwordx4 a[4:7], v19, s[38:39] offset:-4096 sc1\n\t" \
    "global_load_dwordx4 a[8:11], v20, s[38:39] offset:-4096 sc1\n\t" \
    "global_load_dwordx4 a[12:15], v21, s[38:39] offset:-4096 sc1\n\t" \
    "global_load_dwordx4 a[16:19], v22, s[38:39] offset:-4096 sc1\n\t" \
    "global_load_dwordx4 a[20:23], v23, s[38:39] offset:-4096 sc1\n\t" \
    "global_load_dwordx4 v[40:43], v6, s[40:41] offset:-2048 sc1\n\t" \
    "global_load_dwordx4 v[44:47], v6, s[40:41] offset:-1024 sc1\n\t" \
    "global_load_dwordx4 a[24:27], v18, s[38:39] offset:-3072 sc1\n\t" \
    "global_load_dwordx4 a[28:31], v19, s[38:39] offset:-3072 sc1\n\t" \
    "global_load_dwordx4 a[32:35], v20, s[38:39] offset:-3072 sc1\n\t" \
    "global_load_dwordx4 a[36:39], v21, s[38:39] offset:-3072 sc1\n\t" \
    "global_load_dwordx4 a[40:43], v22, s[38:39] offset:-3072 sc1\n\t" \
    "global_load_dwordx4 a[44:47], v23, s[38:39] offset:-3072 sc1\n\t" \
    "global_load_dwordx4 v[48:51], v6, s[40:41] offset:0 sc1\n\t" \
    "global_load_dwordx4 v[52:55], v6, s[40:41] offset:1024 sc1\n\t" \
    "global_load_dwordx4 a[48:51], v18, s[38:39] offset:-2048 sc1\n\t" \
    "global_load_dwordx4 a[52:55], v19, s[38:39] offset:-2048 sc1\n\t" \
    "global_load_dwordx4 a[56:59], v20, s[38:39] offset:-2048 sc1\n\t" \
    "global_load_dwordx4 a[60:63], v21, s[38:39] offset:-2048 sc1\n\t" \
    "global_load_dwordx4 a[64:67], v22, s[38:39] offset:-2048 sc1\n\t" \
    "global_load_dwordx4 a[68:71], v23, s[38:39] offset:-2048 sc1\n\t" \
    "global_load_dwordx4 v[56:59], v6, s[40:41] offset:2048 sc1\n\t" \
    "global_load_dwordx4 v[60:63], v6, s[40:41] offset:3072 sc1\n\t" \
    "global_load_dwordx4 a[72:75], v18, s[38:39] offset:-1024 sc1\n\t" \
    "global_load_dwordx4 a[76:79], v19, s[38:39] offset:-1024 sc1\n\t" \
    "global_load_dwordx4 a[80:83], v20, s[38:39] offset:-1024 sc1\n\t" \
    "global_load_dwordx4 a[84:87], v21, s[38:39] offset:-1024 sc1\n\t" \
    "global_load_dwordx4 a[88:91], v22, s[38:39] offset:-1024 sc1\n\t" \
    "global_load_dwordx4 a[92:95], v23, s[38:39] offset:-1024 sc1\n\t" \
    "oo_loop3_%=:\n\t" \
    "s_mul_i32 s69, s96, 16384\n\t" \
    "s_add_u32 s69, s69, 150994944\n\t" \
    "s_add_u32 s46, s36, s69\n\t" \
    "s_addc_u32 s47, s37, 0\n\t" \
    "s_mul_i32 s69, s96, 9216\n\t" \
    "s_add_u32 s69, s69, 0\n\t" \
    "s_add_u32 s48, s36, s69\n\t" \
    "s_addc_u32 s49, s37, 0\n\t" \
    "s_waitcnt vmcnt(29)\n\t" \
    "v_mfma_f32_16x16x32_bf16 v[72:75], a[0:3], v[32:35], 0\n\t" \
    "global_load_dwordx4 v[64:67], v7, s[40:41] offset:-4096 sc1\n\t" \
    "v_mfma_f32_16x16x32_bf16 v[76:79], a[0:3], v[36:39], 0\n\t" \
    "global_load_dwordx4 v[68:71], v7, s[40:41] offset:-3072 sc1\n\t" \
    "s_waitcnt vmcnt(30)\n\t" \
    "v_mfma_f32_16x16x32_bf16 v[80:83], a[4:7], v[32:35], 0\n\t" \
    "global_load_dwordx4 a[96:99], v18, s[38:39] offset:0 sc1\n\t" \
    "v_mfma_f32_16x16x32_bf16 v[84:87], a[4:7], v[36:39], 0\n\t" \
    "global_load_dwordx4 a[100:103], v19, s[38:39] offset:0 sc1\n\t" \
    "s_waitcnt vmcnt(31)\n\t" \
    "v_mfma_f32_16x16x32_bf16 v[88:91], a[8:11], v[32:35], 0\n\t" \
    "global_load_dwordx4 a[104:107], v20, s[38:39] offset:0 sc1\n\t" \
    "v_mfma_f32_16x16x32_bf16 v[92:95], a[8:11], v[36:39], 0\n\t" \
    "global_load_dwordx4 a[108:111], v21, s[38:39] offset:0 sc1\n\t" \
    "s_waitcnt vmcnt(32)\n\t" \
    "v_mfma_f32_16x16x32_bf16 v[96:99], a[12:15], v[32:35], 0\n\t" \
    "global_load_dwordx4 a[112:115], v22, s[38:39] offset:0 sc1\n\t" \
    "v_mfma_f32_16x16x32_bf16 v[100:103], a[12:15], v[36:39], 0\n\t" \
    "global_load_dwordx4 a[116:119], v23, s[38:39] offset:0 sc1\n\t" \
    "s_waitcnt vmcnt(33)\n\t" \
    "v_mfma_f32_16x16x32_bf16 v[104:107], a[16:19], v[32:35], 0\n\t" \
    "v_mfma_f32_16x16x32_bf16 v[108:111], a[16:19], v[36:39], 0\n\t" \
    "s_waitcnt vmcnt(32)\n\t" \
    "v_mfma_f32_16x16x32_bf16 v[112:115], a[20:23], v[32:35], 0\n\t" \
    "v_mfma_f32_16x16x32_bf16 v[116:119], a[20:23], v[36:39], 0\n\t" \
    "s_add_u32 s96, s96, 4\n\t" \
    "s_cmp_gt_u32 s96, 26\n\t" \
    "s_cselect_b64 vcc, -1, 0\n\t" \
    "v_cndmask_b32 v25, v12, v13, vcc\n\t" \
    "global_load_dword v24, v25, s[36:37] sc1\n\t" \
    "s_waitcnt vmcnt(30)\n\t" \
    "v_mfma_f32_16x16x32_bf16 v[72:75], a[24:27], v[40:43], v[72:75]\n\t" \
    "global_load_dwordx4 v[32:35], v7, s[40:41] offset:-2048 sc1\n\t" \
    "v_mfma_f32_16x16x32_bf16 v[76:79], a[24:27], v[44:47], v[76:79]\n\t" \
    "global_load_dwordx4 v[36:39], v7, s[40:41] offset:-1024 sc1\n\t" \
    "s_waitcnt vmcnt(31)\n\t" \
    "v_mfma_f32_16x16x32_bf16 v[80:83], a[28:31], v[40:43], v[80:83]\n\t" \
    "global_load_dwordx4 a[0:3], v18, s[38:39] offset:1024 sc1\n\t" \
    "v_mfma_f32_16x16x32_bf16 v[84:87], a[28:31], v[44:47], v[84:87]\n\t" \
    "global_load_dwordx4 a[4:7], v19, s[38:39] offset:1024 sc1\n\t" \
    "s_waitcnt vmcnt(32)\n\t" \
    "v_mfma_f32_16x16x32_bf16 v[88:91], a[32:35], v[40:43], v[88:91]\n\t" \
    "global_load_dwordx4 a[8:11], v20, s[38:39] offset:1024 sc1\n\t" \
    "v_mfma_f32_16x16x32_bf16 v[92:95], a[32:35], v[44:47], v[92:95]\n\t" \
    "global_load_dwordx4 a[12:15], v21, s[38:39] offset:1024 sc1\n\t" \
    "s_waitcnt vmcnt(33)\n\t" \
    "v_mfma_f32_16x16x32_bf16 v[96:99], a[36:39], v[40:43], v[96:99]\n\t" \
    "global_load_dwordx4 a[16:19], v22, s[38:39] offset:1024 sc1\n\t" \
    "v_mfma_f32_16x16x32_bf16 v[100:103], a[36:39], v[44:47], v[100:103]\n\t" \
    "global_load_dwordx4 a[20:23], v23, s[38:39] offset:1024 sc1\n\t" \
    "s_waitcnt vmcnt(34)\n\t" \
    "v_mfma_f32_16x16x32_bf16 v[104:107], a[40:43], v[40:43], v[104:107]\n\t" \
    "v_mfma_f32_16x16x32_bf16 v[108:111], a[40:43], v[44:47], v[108:111]\n\t" \
    "s_waitcnt vmcnt(33)\n\t" \
    "v_mfma_f32_16x16x32_bf16 v[112:115], a[44:47], v[40:43], v[112:115]\n\t" \
    "v_mfma_f32_16x16x32_bf16 v[116:119], a[44:47], v[44:47], v[116:119]\n\t" \
    "s_waitcnt vmcnt(30)\n\t" \
    "v_mfma_f32_16x16x32_bf16 v[72:75], a[48:51], v[48:51], v[72:75]\n\t" \
    "global_load_dwordx4 v[40:43], v7, s[40:41] offset:0 sc1\n\t" \
    "v_mfma_f32_16x16x32_bf16 v[76:79], a[48:51], v[52:55], v[76:79]\n\t" \
    "global_load_dwordx4 v[44:47], v7, s[40:41] offset:1024 sc1\n\t" \
    "s_waitcnt vmcnt(31)\n\t" \
    "v_mfma_f32_16x16x32_bf16 v[80:83], a[52:55], v[48:51], v[80:83]\n\t" \
    "global_load_dwordx4 a[24:27], v18, s[38:39] offset:2048 sc1\n\t" \
    "v_mfma_f32_16x16x32_bf16 v[84:87], a[52:55], v[52:55], v[84:87]\n\t" \
    "global_load_dwordx4 a[28:31], v19, s[38:39] offset:2048 sc1\n\t" \
    "s_waitcnt vmcnt(32)\n\t" \
    "v_mfma_f32_16x16x32_bf16 v[88:91], a[56:59], v[48:51], v[88:91]\n\t" \
    "global_load_dwordx4 a[32:35], v20, s[38:39] offset:2048 sc1\n\t" \
    "v_mfma_f32_16x16x32_bf16 v[92:95], a[56:59], v[52:55], v[92:95]\n\t" \
    "global_load_dwordx4 a[36:39], v21, s[38:39] offset:2048 sc1\n\t" \
    "s_waitcnt vmcnt(33)\n\t" \
    "v_mfma_f32_16x16x32_bf16 v[96:99], a[60:63], v[48:51], v[96:99]\n\t" \
    "global_load_dwordx4 a[40:43], v22, s[38:39] offset:2048 sc1\n\t" \
    "v_mfma_f32_16x16x32_bf16 v[100:103], a[60:63], v[52:55], v[100:103]\n\t" \
    "global_load_dwordx4 a[44:47], v23, s[38:39] offset:2048 sc1\n\t" \
    "s_waitcnt vmcnt(34)\n\t" \
    "v_mfma_f32_16x16x32_bf16 v[104:107], a[64:67], v[48:51], v[104:107]\n\t" \
    "v_mfma_f32_16x16x32_bf16 v[108:111], a[64:67], v[52:55], v[108:111]\n\t" \
    "s_waitcnt vmcnt(33)\n\t" \
    "v_mfma_f32_16x16x32_bf16 v[112:115], a[68:71], v[48:51], v[112:115]\n\t" \
    "v_mfma_f32_16x16x32_bf16 v[116:119], a[68:71], v[52:55], v[116:119]\n\t" \
    "s_waitcnt vmcnt(30)\n\t" \
    "v_mfma_f32_16x16x32_bf16 v[72:75], a[72:75], v[56:59], v[72:75]\n\t" \
    "global_load_dwordx4 v[48:51], v7, s[40:41] offset:2048 sc1\n\t" \
    "v_mfma_f32_16x16x32_bf16 v[76:79], a[72:75], v[60:63], v[76:79]\n\t" \
    "global_load_dwordx4 v[52:55], v7, s[40:41] offset:3072 sc1\n\t" \
    "s_waitcnt vmcnt(31)\n\t" \
    "v_mfma_f32_16x16x32_bf16 v[80:83], a[76:79], v[56:59], v[80:83]\n\t" \
    "global_load_dwordx4 a[48:51], v18, s[38:39] offset:3072 sc1\n\t" \
    "v_mfma_f32_16x16x32_bf16 v[84:87], a[76:79], v[60:63], v[84:87]\n\t" \
    "global_load_dwordx4 a[52:55], v19, s[38:39] offset:3072 sc1\n\t" \
    "s_waitcnt vmcnt(32)\n\t" \
    "v_mfma_f32_16x16x32_bf16 v[88:91], a[80:83], v[56:59], v[88:91]\n\t" \
    "global_load_dwordx4 a[56:59], v20, s[38:39] offset:3072 sc1\n\t" \
    "v_mfma_f32_16x16x32_bf16 v[92:95], a[80:83], v[60:63], v[92:95]\n\t" \
    "global_load_dwordx4 a[60:63], v21, s[38:39] offset:3072 sc1\n\t" \
    "s_waitcnt vmcnt(33)\n\t" \
    "v_mfma_f32_16x16x32_bf16 v[96:99], a[84:87], v[56:59], v[96:99]\n\t" \
    "global_load_dwordx4 a[64:67], v22, s[38:39] offset:3072 sc1\n\t" \
    "v_mfma_f32_16x16x32_bf16 v[100:103], a[84:87], v[60:63], v[100:103]\n\t" \
    "global_load_dwordx4 a[68:71], v23, s[38:39] offset:3072 sc1\n\t" \
    "s_waitcnt vmcnt(34)\n\t" \
    "v_mfma_f32_16x16x32_bf16 v[104:107], a[88:91], v[56:59], v[104:107]\n\t" \
    "v_mfma_f32_16x16x32_bf16 v[108:111], a[88:91], v[60:63], v[108:111]\n\t" \
    "s_waitcnt vmcnt(33)\n\t" \
    "v_mfma_f32_16x16x32_bf16 v[112:115], a[92:95], v[56:59], v[112:115]\n\t" \
    "v_mfma_f32_16x16x32_bf16 v[116:119], a[92:95], v[60:63], v[116:119]\n\t" \
    "s_waitcnt vmcnt(30)\n\t" \
    "v_mfma_f32_16x16x32_bf16 v[72:75], a[96:99], v[64:67], v[72:75]\n\t" \
    "global_load_dwordx4 v[56:59], v8, s[46:47] offset:0 sc1\n\t" \
    "v_mfma_f32_16x16x32_bf16 v[76:79], a[96:99], v[68:71], v[76:79]\n\t" \
    "global_load_dwordx4 v[60:63], v9, s[46:47] offset:0 sc1\n\t" \
    "s_waitcnt vmcnt(31)\n\t" \
    "v_mfma_f32_16x16x32_bf16 v[80:83], a[100:103], v[64:67], v[80:83]\n\t" \
    "global_load_dwordx4 a[72:75], v10, s[48:49] offset:0 sc1\n\t" \
    "v_mfma_f32_16x16x32_bf16 v[84:87], a[100:103], v[68:71], v[84:87]\n\t" \
    "global_load_dwordx4 a[76:79], v10, s[48:49] offset:256 sc1\n\t" \
    "s_waitcnt vmcnt(32)\n\t" \
    "v_mfma_f32_16x16x32_bf16 v[88:91], a[104:107], v[64:67], v[88:91]\n\t" \
    "global_load_dwordx4 a[80:83], v10, s[48:49] offset:512 sc1\n\t" \
    "v_mfma_f32_16x16x32_bf16 v[92:95], a[104:107], v[68:71], v[92:95]\n\t" \
    "global_load_dwordx4 a[84:87], v10, s[48:49] offset:768 sc1\n\t" \
    "s_waitcnt vmcnt(33)\n\t" \
    "v_mfma_f32_16x16x32_bf16 v[96:99], a[108:111], v[64:67], v[96:99]\n\t" \
    "global_load_dwordx4 a[88:91], v10, s[48:49] offset:1024 sc1\n\t" \
    "v_mfma_f32_16x16x32_bf16 v[100:103], a[108:111], v[68:71], v[100:103]\n\t" \
    "global_load_dwordx4 a[92:95], v10, s[48:49] offset:1280 sc1\n\t" \
    "s_waitcnt vmcnt(34)\n\t" \
    "v_mfma_f32_16x16x32_bf16 v[104:107], a[112:115], v[64:67], v[104:107]\n\t" \
    "v_mfma_f32_16x16x32_bf16 v[108:111], a[112:115], v[68:71], v[108:111]\n\t" \
    "s_waitcnt vmcnt(33)\n\t" \
    "v_mfma_f32_16x16x32_bf16 v[112:115], a[116:119], v[64:67], v[112:115]\n\t" \
    "v_mfma_f32_16x16x32_bf16 v[116:119], a[116:119], v[68:71], v[116:119]\n\t" \
    "s_waitcnt vmcnt(29)\n\t" \
    "v_mfma_f32_16x16x32_bf16 v[72:75], a[0:3], v[32:35], v[72:75]\n\t" \
    "global_load_dwordx4 v[64:67], v8, s[46:47] offset:1024 sc1\n\t" \
    "v_mfma_f32_16x16x32_bf16 v[76:79], a[0:3], v[36:39], v[76:79]\n\t" \
    "global_load_dwordx4 v[68:71], v9, s[46:47] offset:1024 sc1\n\t" \
    "s_waitcnt vmcnt(30)\n\t" \
    "v_mfma_f32_16x16x32_bf16 v[80:83], a[4:7], v[32:35], v[80:83]\n\t" \
    "global_load_dwordx4 a[96:99], v11, s[48:49] offset:0 sc1\n\t" \
    "v_mfma_f32_16x16x32_bf16 v[84:87], a[4:7], v[36:39], v[84:87]\n\t" \
    "global_load_dwordx4 a[100:103], v11, s[48:49] offset:256 sc1\n\t" \
    "s_waitcnt vmcnt(31)\n\t" \
    "v_mfma_f32_16x16x32_bf16 v[88:91], a[8:11], v[32:35], v[88:91]\n\t" \
    "global_load_dwordx4 a[104:107], v11, s[48:49] offset:512 sc1\n\t" \
    "v_mfma_f32_16x16x32_bf16 v[92:95], a[8:11], v[36:39], v[92:95]\n\t" \
    "global_load_dwordx4 a[108:111], v11, s[48:49] offset:768 sc1\n\t" \
    "s_waitcnt vmcnt(32)\n\t" \
    "v_mfma_f32_16x16x32_bf16 v[96:99], a[12:15], v[32:35], v[96:99]\n\t" \
    "global_load_dwordx4 a[112:115], v11, s[48:49] offset:1024 sc1\n\t" \
    "v_mfma_f32_16x16x32_bf16 v[100:103], a[12:15], v[36:39], v[100:103]\n\t" \
    "global_load_dwordx4 a[116:119], v11, s[48:49] offset:1280 sc1\n\t" \
    "s_waitcnt vmcnt(33)\n\t" \
    "v_mfma_f32_16x16x32_bf16 v[104:107], a[16:19], v[32:35], v[104:107]\n\t" \
    "v_mfma_f32_16x16x32_bf16 v[108:111], a[16:19], v[36:39], v[108:111]\n\t" \
    "s_waitcnt vmcnt(32)\n\t" \
    "v_mfma_f32_16x16x32_bf16 v[112:115], a[20:23], v[32:35], v[112:115]\n\t" \
    "v_mfma_f32_16x16x32_bf16 v[116:119], a[20:23], v[36:39], v[116:119]\n\t" \
    "v_cmp_gt_i32 vcc, s96, v24\n\t" \
    "s_cmp_ge_u32 s96, 58\n\t" \
    "s_cbranch_scc1 oo_stop4_%=\n\t" \
    "s_cbranch_vccnz oo_stop4_%=\n\t" \
    "s_mul_i32 s69, s96, 65536\n\t" \
    "s_add_u32 s69, s69, 146800640\n\t" \
    "s_add_u32 s40, s36, s69\n\t" \
    "s_addc_u32 s41, s37, 0\n\t" \
    "s_lshl_b32 s69, s96, 15\n\t" \
    "v_xor_b32 v18, s69, v0\n\t" \
    "v_xor_b32 v19, s69, v1\n\t" \
    "v_xor_b32 v20, s69, v2\n\t" \
    "v_xor_b32 v21, s69, v3\n\t" \
    "v_xor_b32 v22, s69, v4\n\t" \
    "v_xor_b32 v23, s69, v5\n\t" \
    "s_waitcnt vmcnt(29)\n\t" \
    "v_mfma_f32_16x16x32_bf16 v[72:75], a[24:27], v[40:43], v[72:75]\n\t" \
    "global_load_dwordx4 v[32:35], v6, s[40:41] offset:-4096 sc1\n\t" \
    "v_mfma_f32_16x16x32_bf16 v[76:79], a[24:27], v[44:47], v[76:79]\n\t" \
    "global_load_dwordx4 v[36:39], v6, s[40:41] offset:-3072 sc1\n\t" \
    "s_waitcnt vmcnt(30)\n\t" \
    "v_mfma_f32_16x16x32_bf16 v[80:83], a[28:31], v[40:43], v[80:83]\n\t" \
    "global_load_dwordx4 a[0:3], v18, s[38:39] offset:-4096 sc1\n\t" \
    "v_mfma_f32_16x16x32_bf16 v[84:87], a[28:31], v[44:47], v[84:87]\n\t" \
    "global_load_dwordx4 a[4:7], v19, s[38:39] offset:-4096 sc1\n\t" \
    "s_waitcnt vmcnt(31)\n\t" \
    "v_mfma_f32_16x16x32_bf16 v[88:91], a[32:35], v[40:43], v[88:91]\n\t" \
    "global_load_dwordx4 a[8:11], v20, s[38:39] offset:-4096 sc1\n\t" \
    "v_mfma_f32_16x16x32_bf16 v[92:95], a[32:35], v[44:47], v[92:95]\n\t" \
    "global_load_dwordx4 a[12:15], v21, s[38:39] offset:-4096 sc1\n\t" \
    "s_waitcnt vmcnt(32)\n\t" \
    "v_mfma_f32_16x16x32_bf16 v[96:99], a[36:39], v[40:43], v[96:99]\n\t" \
    "global_load_dwordx4 a[16:19], v22, s[38:39] offset:-4096 sc1\n\t" \
    "v_mfma_f32_16x16x32_bf16 v[100:103], a[36:39], v[44:47], v[100:103]\n\t" \
    "global_load_dwordx4 a[20:23], v23, s[38:39] offset:-4096 sc1\n\t" \
    "s_waitcnt vmcnt(33)\n\t" \
    "v_mfma_f32_16x16x32_bf16 v[104:107], a[40:43], v[40:43], v[104:107]\n\t" \
    "v_mfma_f32_16x16x32_bf16 v[108:111], a[40:43], v[44:47], v[108:111]\n\t" \
    "s_waitcnt vmcnt(32)\n\t" \
    "v_mfma_f32_16x16x32_bf16 v[112:115], a[44:47], v[40:43], v[112:115]\n\t" \
    "v_mfma_f32_16x16x32_bf16 v[116:119], a[44:47], v[44:47], v[116:119]\n\t" \
    "s_waitcnt vmcnt(29)\n\t" \
    "v_mfma_f32_16x16x32_bf16 v[72:75], a[48:51], v[48:51], v[72:75]\n\t" \
    "global_load_dwordx4 v[40:43], v6, s[40:41] offset:-2048 sc1\n\t" \
    "v_mfma_f32_16x16x32_bf16 v[76:79], a[48:51], v[52:55], v[76:79]\n\t" \
    "global_load_dwordx4 v[44:47], v6, s[40:41] offset:-1024 sc1\n\t" \
    "s_waitcnt vmcnt(30)\n\t" \
    "v_mfma_f32_16x16x32_bf16 v[80:83], a[52:55], v[48:51], v[80:83]\n\t" \
    "global_load_dwordx4 a[24:27], v18, s[38:39] offset:-3072 sc1\n\t" \
    "v_mfma_f32_16x16x32_bf16 v[84:87], a[52:55], v[52:55], v[84:87]\n\t" \
    "global_load_dwordx4 a[28:31], v19, s[38:39] offset:-3072 sc1\n\t" \
    "s_waitcnt vmcnt(31)\n\t" \
    "v_mfma_f32_16x16x32_bf16 v[88:91], a[56:59], v[48:51], v[88:91]\n\t" \
    "global_load_dwordx4 a[32:35], v20, s[38:39] offset:-3072 sc1\n\t" \
    "v_mfma_f32_16x16x32_bf16 v[92:95], a[56:59], v[52:55], v[92:95]\n\t" \
    "global_load_dwordx4 a[36:39], v21, s[38:39] offset:-3072 sc1\n\t" \
    "s_waitcnt vmcnt(32)\n\t" \
    "v_mfma_f32_16x16x32_bf16 v[96:99], a[60:63], v[48:51], v[96:99]\n\t" \
    "global_load_dwordx4 a[40:43], v22, s[38:39] offset:-3072 sc1\n\t" \
    "v_mfma_f32_16x16x32_bf16 v[100:103], a[60:63], v[52:55], v[100:103]\n\t" \
    "global_load_dwordx4 a[44:47], v23, s[38:39] offset:-3072 sc1\n\t" \
    "s_waitcnt vmcnt(33)\n\t" \
    "v_mfma_f32_16x16x32_bf16 v[104:107], a[64:67], v[48:51], v[104:107]\n\t" \
    "v_mfma_f32_16x16x32_bf16 v[108:111], a[64:67], v[52:55], v[108:111]\n\t" \
    "s_waitcnt vmcnt(32)\n\t" \
    "v_mfma_f32_16x16x32_bf16 v[112:115], a[68:71], v[48:51], v[112:115]\n\t" \
    "v_mfma_f32_16x16x32_bf16 v[116:119], a[68:71], v[52:55], v[116:119]\n\t" \
    "s_waitcnt vmcnt(29)\n\t" \
    "v_mfma_f32_16x16x32_bf16 v[72:75], a[72:75], v[56:59], v[72:75]\n\t" \
    "global_load_dwordx4 v[48:51], v6, s[40:41] offset:0 sc1\n\t" \
    "v_mfma_f32_16x16x32_bf16 v[76:79], a[72:75], v[60:63], v[76:79]\n\t" \
    "global_load_dwordx4 v[52:55], v6, s[40:41] offset:1024 sc1\n\t" \
    "s_waitcnt vmcnt(30)\n\t" \
    "v_mfma_f32_16x16x32_bf16 v[80:83], a[76:79], v[56:59], v[80:83]\n\t" \
    "global_load_dwordx4 a[48:51], v18, s[38:39] offset:-2048 sc1\n\t" \
    "v_mfma_f32_16x16x32_bf16 v[84:87], a[76:79], v[60:63], v[84:87]\n\t" \
    "global_load_dwordx4 a[52:55], v19, s[38:39] offset:-2048 sc1\n\t" \
    "s_waitcnt vmcnt(31)\n\t" \
    "v_mfma_f32_16x16x32_bf16 v[88:91], a[80:83], v[56:59], v[88:91]\n\t" \
    "global_load_dwordx4 a[56:59], v20, s[38:39] offset:-2048 sc1\n\t" \
    "v_mfma_f32_16x16x32_bf16 v[92:95], a[80:83], v[60:63], v[92:95]\n\t" \
    "global_load_dwordx4 a[60:63], v21, s[38:39] offset:-2048 sc1\n\t" \
    "s_waitcnt vmcnt(32)\n\t" \
    "v_mfma_f32_16x16x32_bf16 v[96:99], a[84:87], v[56:59], v[96:99]\n\t" \
    "global_load_dwordx4 a[64:67], v22, s[38:39] offset:-2048 sc1\n\t" \
    "v_mfma_f32_16x16x32_bf16 v[100:103], a[84:87], v[60:63], v[100:103]\n\t" \
    "global_load_dwordx4 a[68:71], v23, s[38:39] offset:-2048 sc1\n\t" \
    "s_waitcnt vmcnt(33)\n\t" \
    "v_mfma_f32_16x16x32_bf16 v[104:107], a[88:91], v[56:59], v[104:107]\n\t" \
    "v_mfma_f32_16x16x32_bf16 v[108:111], a[88:91], v[60:63], v[108:111]\n\t" \
    "s_waitcnt vmcnt(32)\n\t" \
    "v_mfma_f32_16x16x32_bf16 v[112:115], a[92:95], v[56:59], v[112:115]\n\t" \
    "v_mfma_f32_16x16x32_bf16 v[116:119], a[92:95], v[60:63], v[116:119]\n\t" \
    "s_waitcnt vmcnt(29)\n\t" \
    "v_mfma_f32_16x16x32_bf16 v[72:75], a[96:99], v[64:67], v[72:75]\n\t" \
    "global_load_dwordx4 v[56:59], v6, s[40:41] offset:2048 sc1\n\t" \
    "v_mfma_f32_16x16x32_bf16 v[76:79], a[96:99], v[68:71], v[76:79]\n\t" \
    "global_load_dwordx4 v[60:63], v6, s[40:41] offset:3072 sc1\n\t" \
    "s_waitcnt vmcnt(30)\n\t" \
    "v_mfma_f32_16x16x32_bf16 v[80:83], a[100:103], v[64:67], v[80:83]\n\t" \
    "global_load_dwordx4 a[72:75], v18, s[38:39] offset:-1024 sc1\n\t" \
    "v_mfma_f32_16x16x32_bf16 v[84:87], a[100:103], v[68:71], v[84:87]\n\t" \
    "global_load_dwordx4 a[76:79], v19, s[38:39] offset:-1024 sc1\n\t" \
    "s_waitcnt vmcnt(31)\n\t" \
    "v_mfma_f32_16x16x32_bf16 v[88:91], a[104:107], v[64:67], v[88:91]\n\t" \
    "global_load_dwordx4 a[80:83], v20, s[38:39] offset:-1024 sc1\n\t" \
    "v_mfma_f32_16x16x32_bf16 v[92:95], a[104:107], v[68:71], v[92:95]\n\t" \
    "global_load_dwordx4 a[84:87], v21, s[38:39] offset:-1024 sc1\n\t" \
    "s_waitcnt vmcnt(32)\n\t" \
    "v_mfma_f32_16x16x32_bf16 v[96:99], a[108:111], v[64:67], v[96:99]\n\t" \
    "global_load_dwordx4 a[88:91], v22, s[38:39] offset:-1024 sc1\n\t" \
    "v_mfma_f32_16x16x32_bf16 v[100:103], a[108:111], v[68:71], v[100:103]\n\t" \
    "global_load_dwordx4 a[92:95], v23, s[38:39] offset:-1024 sc1\n\t" \
    "s_waitcnt vmcnt(33)\n\t" \
    "v_mfma_f32_16x16x32_bf16 v[104:107], a[112:115], v[64:67], v[104:107]\n\t" \
    "v_mfma_f32_16x16x32_bf16 v[108:111], a[112:115], v[68:71], v[108:111]\n\t" \
    "s_waitcnt vmcnt(32)\n\t" \
    "v_mfma_f32_16x16x32_bf16 v[112:115], a[116:119], v[64:67], v[112:115]\n\t" \
    "v_mfma_f32_16x16x32_bf16 v[116:119], a[116:119], v[68:71], v[116:119]\n\t" \
    "s_mov_b32 s97, 1\n\t" \
    "s_branch oo_epi5_%=\n\t" \
    "oo_stop4_%=:\n\t" \
    "s_waitcnt vmcnt(29)\n\t" \
    "v_mfma_f32_16x16x32_bf16 v[72:75], a[24:27], v[40:43], v[72:75]\n\t" \
    "v_mfma_f32_16x16x32_bf16 v[76:79], a[24:27], v[44:47], v[76:79]\n\t" \
    "s_waitcnt vmcnt(28)\n\t" \
    "v_mfma_f32_16x16x32_bf16 v[80:83], a[28:31], v[40:43], v[80:83]\n\t" \
    "v_mfma_f32_16x16x32_bf16 v[84:87], a[28:31], v[44:47], v[84:87]\n\t" \
    "s_waitcnt vmcnt(27)\n\t" \
    "v_mfma_f32_16x16x32_bf16 v[88:91], a[32:35], v[40:43], v[88:91]\n\t" \
    "v_mfma_f32_16x16x32_bf16 v[92:95], a[32:35], v[44:47], v[92:95]\n\t" \
    "s_waitcnt vmcnt(26)\n\t" \
    "v_mfma_f32_16x16x32_bf16 v[96:99], a[36:39], v[40:43], v[96:99]\n\t" \
    "v_mfma_f32_16x16x32_bf16 v[100:103], a[36:39], v[44:47], v[100:103]\n\t" \
    "s_waitcnt vmcnt(25)\n\t" \
    "v_mfma_f32_16x16x32_bf16 v[104:107], a[40:43], v[40:43], v[104:107]\n\t" \
    "v_mfma_f32_16x16x32_bf16 v[108:111], a[40:43], v[44:47], v[108:111]\n\t" \
    "s_waitcnt vmcnt(24)\n\t" \
    "v_mfma_f32_16x16x32_bf16 v[112:115], a[44:47], v[40:43], v[112:115]\n\t" \
    "v_mfma_f32_16x16x32_bf16 v[116:119], a[44:47], v[44:47], v[116:119]\n\t" \
    "s_waitcnt vmcnt(21)\n\t" \
    "v_mfma_f32_16x16x32_bf16 v[72:75], a[48:51], v[48:51], v[72:75]\n\t" \
    "v_mfma_f32_16x16x32_bf16 v[76:79], a[48:51], v[52:55], v[76:79]\n\t" \
    "s_waitcnt vmcnt(20)\n\t" \
    "v_mfma_f32_16x16x32_bf16 v[80:83], a[52:55], v[48:51], v[80:83]\n\t" \
    "v_mfma_f32_16x16x32_bf16 v[84:87], a[52:55], v[52:55], v[84:87]\n\t" \
    "s_waitcnt vmcnt(19)\n\t" \
    "v_mfma_f32_16x16x32_bf16 v[88:91], a[56:59], v[48:51], v[88:91]\n\t" \
    "v_mfma_f32_16x16x32_bf16 v[92:95], a[56:59], v[52:55], v[92:95]\n\t" \
    "s_waitcnt vmcnt(18)\n\t" \
    "v_mfma_f32_16x16x32_bf16 v[96:99], a[60:63], v[48:51], v[96:99]\n\t" \
    "v_mfma_f32_16x16x32_bf16 v[100:103], a[60:63], v[52:55], v[100:103]\n\t" \
    "s_waitcnt vmcnt(17)\n\t" \
    "v_mfma_f32_16x16x32_bf16 v[104:107], a[64:67], v[48:51], v[104:107]\n\t" \
    "v_mfma_f32_16x16x32_bf16 v[108:111], a[64:67], v[52:55], v[108:111]\n\t" \
    "s_waitcnt vmcnt(16)\n\t" \
    "v_mfma_f32_16x16x32_bf16 v[112:115], a[68:71], v[48:51], v[112:115]\n\t" \
    "v_mfma_f32_16x16x32_bf16 v[116:119], a[68:71], v[52:55], v[116:119]\n\t" \
    "s_waitcnt vmcnt(13)\n\t" \
    "v_mfma_f32_16x16x32_bf16 v[72:75], a[72:75], v[56:59], v[72:75]\n\t" \
    "v_mfma_f32_16x16x32_bf16 v[76:79], a[72:75], v[60:63], v[76:79]\n\t" \
    "s_waitcnt vmcnt(12)\n\t" \
    "v_mfma_f32_16x16x32_bf16 v[80:83], a[76:79], v[56:59], v[80:83]\n\t" \
    "v_mfma_f32_16x16x32_bf16 v[84:87], a[76:79], v[60:63], v[84:87]\n\t" \
    "s_waitcnt vmcnt(11)\n\t" \
    "v_mfma_f32_16x16x32_bf16 v[88:91], a[80:83], v[56:59], v[88:91]\n\t" \
    "v_mfma_f32_16x16x32_bf16 v[92:95], a[80:83], v[60:63], v[92:95]\n\t" \
    "s_waitcnt vmcnt(10)\n\t" \
    "v_mfma_f32_16x16x32_bf16 v[96:99], a[84:87], v[56:59], v[96:99]\n\t" \
    "v_mfma_f32_16x16x32_bf16 v[100:103], a[84:87], v[60:63], v[100:103]\n\t" \
    "s_waitcnt vmcnt(9)\n\t" \
    "v_mfma_f32_16x16x32_bf16 v[104:107], a[88:91], v[56:59], v[104:107]\n\t" \
    "v_mfma_f32_16x16x32_bf16 v[108:111], a[88:91], v[60:63], v[108:111]\n\t" \
    "s_waitcnt vmcnt(8)\n\t" \
    "v_mfma_f32_16x16x32_bf16 v[112:115], a[92:95], v[56:59], v[112:115]\n\t" \
    "v_mfma_f32_16x16x32_bf16 v[116:119], a[92:95], v[60:63], v[116:119]\n\t" \
    "s_waitcnt vmcnt(5)\n\t" \
    "v_mfma_f32_16x16x32_bf16 v[72:75], a[96:99], v[64:67], v[72:75]\n\t" \
    "v_mfma_f32_16x16x32_bf16 v[76:79], a[96:99], v[68:71], v[76:79]\n\t" \
    "s_waitcnt vmcnt(4)\n\t" \
    "v_mfma_f32_16x16x32_bf16 v[80:83], a[100:103], v[64:67], v[80:83]\n\t" \
    "v_mfma_f32_16x16x32_bf16 v[84:87], a[100:103], v[68:71], v[84:87]\n\t" \
    "s_waitcnt vmcnt(3)\n\t" \
    "v_mfma_f32_16x16x32_bf16 v[88:91], a[104:107], v[64:67], v[88:91]\n\t" \
    "v_mfma_f32_16x16x32_bf16 v[92:95], a[104:107], v[68:71], v[92:95]\n\t" \
    "s_waitcnt vmcnt(2)\n\t" \
    "v_mfma_f32_16x16x32_bf16 v[96:99], a[108:111], v[64:67], v[96:99]\n\t" \
    "v_mfma_f32_16x16x32_bf16 v[100:103], a[108:111], v[68:71], v[100:103]\n\t" \
    "s_waitcnt vmcnt(1)\n\t" \
    "v_mfma_f32_16x16x32_bf16 v[104:107], a[112:115], v[64:67], v[104:107]\n\t" \
    "v_mfma_f32_16x16x32_bf16 v[108:111], a[112:115], v[68:71], v[108:111]\n\t" \
    "s_waitcnt vmcnt(0)\n\t" \
    "v_mfma_f32_16x16x32_bf16 v[112:115], a[116:119], v[64:67], v[112:115]\n\t" \
    "v_mfma_f32_16x16x32_bf16 v[116:119], a[116:119], v[68:71], v[116:119]\n\t" \
    "s_mov_b32 s97, 0\n\t" \
    "oo_epi5_%=:\n\t" \
    "s_mul_i32 s69, s65, 73728\n\t" \
    "s_add_u32 s44, s42, s69\n\t" \
    "s_addc_u32 s45, s43, 0\n\t" \
    "s_nop 7\n\t" \
    "ds_write_b128 v14, v[72:75] offset:0\n\t" \
    "ds_write_b128 v14, v[76:79] offset:1024\n\t" \
    "ds_write_b128 v14, v[80:83] offset:2048\n\t" \
    "ds_write_b128 v14, v[84:87] offset:3072\n\t" \
    "ds_write_b128 v14, v[88:91] offset:4096\n\t" \
    "ds_write_b128 v14, v[92:95] offset:5120\n\t" \
    "ds_write_b128 v14, v[96:99] offset:6144\n\t" \
    "ds_write_b128 v14, v[100:103] offset:7168\n\t" \
    "ds_write_b128 v14, v[104:107] offset:8192\n\t" \
    "ds_write_b128 v14, v[108:111] offset:9216\n\t" \
    "ds_write_b128 v14, v[112:115] offset:10240\n\t" \
    "ds_write_b128 v14, v[116:119] offset:11264\n\t" \
    "s_waitcnt lgkmcnt(0)\n\t" \
    "s_barrier\n\t" \
    "ds_read_b128 v[120:123], v15 offset:0\n\t" \
    "ds_read_b128 v[124:127], v15 offset:12288\n\t" \
    "ds_read_b128 v[128:131], v15 offset:24576\n\t" \
    "ds_read_b128 v[132:135], v15 offset:36864\n\t" \
    "ds_read_b128 v[136:139], v15 offset:4096\n\t" \
    "ds_read_b128 v[140:143], v15 offset:16384\n\t" \
    "ds_read_b128 v[144:147], v15 offset:28672\n\t" \
    "ds_read_b128 v[148:151], v15 offset:40960\n\t" \
    "ds_read_b128 v[152:155], v15 offset:8192\n\t" \
    "ds_read_b128 v[156:159], v15 offset:20480\n\t" \
    "ds_read_b128 v[160:163], v15 offset:32768\n\t" \
    "ds_read_b128 v[164:167], v15 offset:45056\n\t" \
    "v_xor_b32 v14, 0x10000, v14\n\t" \
    "v_xor_b32 v15, 0x10000, v15\n\t" \
    "s_waitcnt lgkmcnt(10)\n\t" \
    "v_pk_add_f32 v[120:121], v[120:121], v[124:125]\n\t" \
    "v_pk_add_f32 v[122:123], v[122:123], v[126:127]\n\t" \
    "s_waitcnt lgkmcnt(8)\n\t" \
    "v_pk_add_f32 v[128:129], v[128:129], v[132:133]\n\t" \
    "v_pk_add_f32 v[130:131], v[130:131], v[134:135]\n\t" \
    "v_pk_add_f32 v[120:121], v[120:121], v[128:129]\n\t" \
    "v_pk_add_f32 v[122:123], v[122:123], v[130:131]\n\t" \
    "v_cvt_pk_bf16_f32 v120, v120, v121\n\t" \
    "v_cvt_pk_bf16_f32 v121, v122, v123\n\t" \
    "global_store_dwordx2 v16, v[120:121], s[44:45] offset:0\n\t" \
    "s_waitcnt lgkmcnt(6)\n\t" \
    "v_pk_add_f32 v[136:137], v[136:137], v[140:141]\n\t" \
    "v_pk_add_f32 v[138:139], v[138:139], v[142:143]\n\t" \
    "s_waitcnt lgkmcnt(4)\n\t" \
    "v_pk_add_f32 v[144:145], v[144:145], v[148:149]\n\t" \
    "v_pk_add_f32 v[146:147], v[146:147], v[150:151]\n\t" \
    "v_pk_add_f32 v[136:137], v[136:137], v[144:145]\n\t" \
    "v_pk_add_f32 v[138:139], v[138:139], v[146:147]\n\t" \
    "v_cvt_pk_bf16_f32 v136, v136, v137\n\t" \
    "v_cvt_pk_bf16_f32 v137, v138, v139\n\t" \
    "global_store_dwordx2 v16, v[136:137], s[44:45] offset:64\n\t" \
    "s_waitcnt lgkmcnt(2)\n\t" \
    "v_pk_add_f32 v[152:153], v[152:153], v[156:157]\n\t" \
    "v_pk_add_f32 v[154:155], v[154:155], v[158:159]\n\t" \
    "s_waitcnt lgkmcnt(0)\n\t" \
    "v_pk_add_f32 v[160:161], v[160:161], v[164:165]\n\t" \
    "v_pk_add_f32 v[162:163], v[162:163], v[166:167]\n\t" \
    "v_pk_add_f32 v[152:153], v[152:153], v[160:161]\n\t" \
    "v_pk_add_f32 v[154:155], v[154:155], v[162:163]\n\t" \
    "v_cvt_pk_bf16_f32 v152, v152, v153\n\t" \
    "v_cvt_pk_bf16_f32 v153, v154, v155\n\t" \
    "global_store_dwordx2 v16, v[152:153], s[44:45] offset:128\n\t" \
    "s_mov_b32 s65, s96\n\t" \
    "s_cmp_lg_u32 s97, 0\n\t" \
    "s_cbranch_scc1 oo_loop3_%=\n\t" \
    "s_cmp_lt_u32 s65, 58\n\t" \
    "s_cbranch_scc1 oo_poll1_%=\n\t" \
    "s_waitcnt vmcnt(0) lgkmcnt(0)\n\t" \
    "s_barrier\n\t" \
    "oo_last_%=:\n\t" \
    ""
#define ASM_CV \
    "v_mov_b32 v0, %0\n\t" \
    "v_mov_b32 v2, %2\n\t" \
    "v_subrev_u32 v1, 768, %1\n\t" \
    "v_mov_b32 v4, 0\n\t" \
    "v_mov_b32 v5, 0\n\t" \
    "v_mov_b32 v6, 0\n\t" \
    "v_mov_b32 v7, 0\n\t" \
    "v_mov_b32 v8, 0\n\t" \
    "v_mov_b32 v9, 0\n\t" \
    "v_mov_b32 v10, 0\n\t" \
    "v_mov_b32 v11, 0\n\t" \
    "v_mov_b32 v12, 0\n\t" \
    "v_mov_b32 v13, 0\n\t" \
    "v_mov_b32 v14, 0\n\t" \
    "v_mov_b32 v15, 0\n\t" \
    "v_mov_b32 v16, 0\n\t" \
    "v_mov_b32 v17, 0\n\t" \
    "v_mov_b32 v18, 0\n\t" \
    "v_mov_b32 v19, 0\n\t" \
    "v_mov_b32 v20, 0\n\t" \
    "v_mov_b32 v21, 0\n\t" \
    "v_mov_b32 v22, 0\n\t" \
    "v_mov_b32 v23, 0\n\t" \
    "v_mov_b32 v24, 0\n\t" \
    "v_mov_b32 v25, 0\n\t" \
    "v_mov_b32 v26, 0\n\t" \
    "v_mov_b32 v27, 0\n\t" \
    "v_mov_b32 v28, 0\n\t" \
    "v_mov_b32 v29, 0\n\t" \
    "v_mov_b32 v30, 0\n\t" \
    "v_mov_b32 v31, 0\n\t" \
    "v_mov_b32 v32, 0\n\t" \
    "v_mov_b32 v33, 0\n\t" \
    "v_mov_b32 v34, 0\n\t" \
    "v_mov_b32 v35, 0\n\t" \
    "s_waitcnt lgkmcnt(0)\n\t" \
    "ds_read_b64 v[36:37], v0 offset:0\n\t" \
    "ds_read_b64 v[38:39], v0 offset:8\n\t" \
    "ds_read_b128 v[44:47], v1 offset:768\n\t" \
    "ds_read_b64 v[40:41], v0 offset:32\n\t" \
    "ds_read_b64 v[42:43], v0 offset:40\n\t" \
    "ds_read_b128 v[48:51], v1 offset:2304\n\t" \
    "ds_read_b128 v[52:55], v1 offset:3840\n\t" \
    "ds_read_b128 v[56:59], v1 offset:5376\n\t" \
    "s_mov_b32 s69, 4\n\t" \
    "cv_c1_%=:\n\t" \
    "s_waitcnt lgkmcnt(5)\n\t" \
    "v_mfma_f32_16x16x32_f16 v[4:7], v[36:39], v[44:47], v[4:7]\n\t" \
    "ds_read_b64 v[60:61], v0 offset:64\n\t" \
    "s_waitcnt lgkmcnt(4)\n\t" \
    "v_mfma_f32_16x16x32_f16 v[20:23], v[40:43], v[44:47], v[20:23]\n\t" \
    "ds_read_b64 v[62:63], v0 offset:72\n\t" \
    "s_waitcnt lgkmcnt(4)\n\t" \
    "v_mfma_f32_16x16x32_f16 v[8:11], v[36:39], v[48:51], v[8:11]\n\t" \
    "ds_read_b128 v[68:71], v1 offset:672\n\t" \
    "v_mfma_f32_16x16x32_f16 v[24:27], v[40:43], v[48:51], v[24:27]\n\t" \
    "ds_read_b64 v[64:65], v0 offset:96\n\t" \
    "s_waitcnt lgkmcnt(5)\n\t" \
    "v_mfma_f32_16x16x32_f16 v[12:15], v[36:39], v[52:55], v[12:15]\n\t" \
    "ds_read_b64 v[66:67], v0 offset:104\n\t" \
    "v_mfma_f32_16x16x32_f16 v[28:31], v[40:43], v[52:55], v[28:31]\n\t" \
    "ds_read_b128 v[72:75], v1 offset:2208\n\t" \
    "s_waitcnt lgkmcnt(6)\n\t" \
    "v_mfma_f32_16x16x32_f16 v[16:19], v[36:39], v[56:59], v[16:19]\n\t" \
    "ds_read_b128 v[76:79], v1 offset:3744\n\t" \
    "v_mfma_f32_16x16x32_f16 v[32:35], v[40:43], v[56:59], v[32:35]\n\t" \
    "ds_read_b128 v[80:83], v1 offset:5280\n\t" \
    "s_waitcnt lgkmcnt(5)\n\t" \
    "v_mfma_f32_16x16x32_f16 v[4:7], v[60:63], v[68:71], v[4:7]\n\t" \
    "ds_read_b64 v[84:85], v0 offset:128\n\t" \
    "s_waitcnt lgkmcnt(4)\n\t" \
    "v_mfma_f32_16x16x32_f16 v[20:23], v[64:67], v[68:71], v[20:23]\n\t" \
    "ds_read_b64 v[86:87], v0 offset:136\n\t" \
    "s_waitcnt lgkmcnt(4)\n\t" \
    "v_mfma_f32_16x16x32_f16 v[8:11], v[60:63], v[72:75], v[8:11]\n\t" \
    "ds_read_b128 v[92:95], v1 offset:576\n\t" \
    "v_mfma_f32_16x16x32_f16 v[24:27], v[64:67], v[72:75], v[24:27]\n\t" \
    "ds_read_b64 v[88:89], v0 offset:160\n\t" \
    "s_waitcnt lgkmcnt(5)\n\t" \
    "v_mfma_f32_16x16x32_f16 v[12:15], v[60:63], v[76:79], v[12:15]\n\t" \
    "ds_read_b64 v[90:91], v0 offset:168\n\t" \
    "v_mfma_f32_16x16x32_f16 v[28:31], v[64:67], v[76:79], v[28:31]\n\t" \
    "ds_read_b128 v[96:99], v1 offset:2112\n\t" \
    "s_waitcnt lgkmcnt(6)\n\t" \
    "v_mfma_f32_16x16x32_f16 v[16:19], v[60:63], v[80:83], v[16:19]\n\t" \
    "ds_read_b128 v[100:103], v1 offset:3648\n\t" \
    "v_mfma_f32_16x16x32_f16 v[32:35], v[64:67], v[80:83], v[32:35]\n\t" \
    "ds_read_b128 v[104:107], v1 offset:5184\n\t" \
    "s_waitcnt lgkmcnt(5)\n\t" \
    "v_mfma_f32_16x16x32_f16 v[4:7], v[84:87], v[92:95], v[4:7]\n\t" \
    "ds_read_b64 v[108:109], v0 offset:192\n\t" \
    "s_waitcnt lgkmcnt(4)\n\t" \
    "v_mfma_f32_16x16x32_f16 v[20:23], v[88:91], v[92:95], v[20:23]\n\t" \
    "ds_read_b64 v[110:111], v0 offset:200\n\t" \
    "s_waitcnt lgkmcnt(4)\n\t" \
    "v_mfma_f32_16x16x32_f16 v[8:11], v[84:87], v[96:99], v[8:11]\n\t" \
    "ds_read_b128 v[116:119], v1 offset:480\n\t" \
    "v_mfma_f32_16x16x32_f16 v[24:27], v[88:91], v[96:99], v[24:27]\n\t" \
    "ds_read_b64 v[112:113], v0 offset:224\n\t" \
    "s_waitcnt lgkmcnt(5)\n\t" \
    "v_mfma_f32_16x16x32_f16 v[12:15], v[84:87], v[100:103], v[12:15]\n\t" \
    "ds_read_b64 v[114:115], v0 offset:232\n\t" \
    "v_mfma_f32_16x16x32_f16 v[28:31], v[88:91], v[100:103], v[28:31]\n\t" \
    "ds_read_b128 v[120:123], v1 offset:2016\n\t" \
    "s_waitcnt lgkmcnt(6)\n\t" \
    "v_mfma_f32_16x16x32_f16 v[16:19], v[84:87], v[104:107], v[16:19]\n\t" \
    "ds_read_b128 v[124:127], v1 offset:3552\n\t" \
    "v_mfma_f32_16x16x32_f16 v[32:35], v[88:91], v[104:107], v[32:35]\n\t" \
    "ds_read_b128 v[128:131], v1 offset:5088\n\t" \
    "s_waitcnt lgkmcnt(5)\n\t" \
    "v_mfma_f32_16x16x32_f16 v[4:7], v[108:111], v[116:119], v[4:7]\n\t" \
    "ds_read_b64 v[36:37], v0 offset:256\n\t" \
    "s_waitcnt lgkmcnt(4)\n\t" \
    "v_mfma_f32_16x16x32_f16 v[20:23], v[112:115], v[116:119], v[20:23]\n\t" \
    "ds_read_b64 v[38:39], v0 offset:264\n\t" \
    "s_waitcnt lgkmcnt(4)\n\t" \
    "v_mfma_f32_16x16x32_f16 v[8:11], v[108:111], v[120:123], v[8:11]\n\t" \
    "ds_read_b128 v[44:47], v1 offset:384\n\t" \
    "v_mfma_f32_16x16x32_f16 v[24:27], v[112:115], v[120:123], v[24:27]\n\t" \
    "ds_read_b64 v[40:41], v0 offset:288\n\t" \
    "s_waitcnt lgkmcnt(5)\n\t" \
    "v_mfma_f32_16x16x32_f16 v[12:15], v[108:111], v[124:127], v[12:15]\n\t" \
    "ds_read_b64 v[42:43], v0 offset:296\n\t" \
    "v_mfma_f32_16x16x32_f16 v[28:31], v[112:115], v[124:127], v[28:31]\n\t" \
    "ds_read_b128 v[48:51], v1 offset:1920\n\t" \
    "s_waitcnt lgkmcnt(6)\n\t" \
    "v_mfma_f32_16x16x32_f16 v[16:19], v[108:111], v[128:131], v[16:19]\n\t" \
    "ds_read_b128 v[52:55], v1 offset:3456\n\t" \
    "v_mfma_f32_16x16x32_f16 v[32:35], v[112:115], v[128:131], v[32:35]\n\t" \
    "ds_read_b128 v[56:59], v1 offset:4992\n\t" \
    "v_add_u32 v0, 256, v0\n\t" \
    "v_subrev_u32 v1, 384, v1\n\t" \
    "s_sub_u32 s69, s69, 1\n\t" \
    "s_cmp_lg_u32 s69, 0\n\t" \
    "s_cbranch_scc1 cv_c1_%=\n\t" \
    "s_waitcnt lgkmcnt(0)\n\t" \
    "ds_read_b64 v[36:37], v0 offset:0\n\t" \
    "ds_read_b64 v[38:39], v0 offset:8\n\t" \
    "ds_read_b128 v[48:51], v1 offset:2304\n\t" \
    "ds_read_b64 v[40:41], v0 offset:32\n\t" \
    "ds_read_b64 v[42:43], v0 offset:40\n\t" \
    "ds_read_b128 v[52:55], v1 offset:3840\n\t" \
    "ds_read_b128 v[56:59], v1 offset:5376\n\t" \
    "ds_read_b64 v[60:61], v0 offset:64\n\t" \
    "ds_read_b64 v[62:63], v0 offset:72\n\t" \
    "ds_read_b128 v[72:75], v1 offset:2208\n\t" \
    "ds_read_b64 v[64:65], v0 offset:96\n\t" \
    "ds_read_b64 v[66:67], v0 offset:104\n\t" \
    "ds_read_b128 v[76:79], v1 offset:3744\n\t" \
    "ds_read_b128 v[80:83], v1 offset:5280\n\t" \
    "s_mov_b32 s69, 4\n\t" \
    "cv_c2_%=:\n\t" \
    "s_waitcnt lgkmcnt(11)\n\t" \
    "v_mfma_f32_16x16x32_f16 v[8:11], v[36:39], v[48:51], v[8:11]\n\t" \
    "ds_read_b64 v[84:85], v0 offset:128\n\t" \
    "ds_read_b64 v[86:87], v0 offset:136\n\t" \
    "s_waitcnt lgkmcnt(11)\n\t" \
    "v_mfma_f32_16x16x32_f16 v[24:27], v[40:43], v[48:51], v[24:27]\n\t" \
    "ds_read_b128 v[96:99], v1 offset:2112\n\t" \
    "ds_read_b64 v[88:89], v0 offset:160\n\t" \
    "s_waitcnt lgkmcnt(12)\n\t" \
    "v_mfma_f32_16x16x32_f16 v[12:15], v[36:39], v[52:55], v[12:15]\n\t" \
    "ds_read_b64 v[90:91], v0 offset:168\n\t" \
    "ds_read_b128 v[100:103], v1 offset:3648\n\t" \
    "v_mfma_f32_16x16x32_f16 v[28:31], v[40:43], v[52:55], v[28:31]\n\t" \
    "ds_read_b128 v[104:107], v1 offset:5184\n\t" \
    "s_waitcnt lgkmcnt(14)\n\t" \
    "v_mfma_f32_16x16x32_f16 v[16:19], v[36:39], v[56:59], v[16:19]\n\t" \
    "v_mfma_f32_16x16x32_f16 v[32:35], v[40:43], v[56:59], v[32:35]\n\t" \
    "s_waitcnt lgkmcnt(11)\n\t" \
    "v_mfma_f32_16x16x32_f16 v[8:11], v[60:63], v[72:75], v[8:11]\n\t" \
    "ds_read_b64 v[108:109], v0 offset:192\n\t" \
    "ds_read_b64 v[110:111], v0 offset:200\n\t" \
    "s_waitcnt lgkmcnt(11)\n\t" \
    "v_mfma_f32_16x16x32_f16 v[24:27], v[64:67], v[72:75], v[24:27]\n\t" \
    "ds_read_b128 v[120:123], v1 offset:2016\n\t" \
    "ds_read_b64 v[112:113], v0 offset:224\n\t" \
    "s_waitcnt lgkmcnt(12)\n\t" \
    "v_mfma_f32_16x16x32_f16 v[12:15], v[60:63], v[76:79], v[12:15]\n\t" \
    "ds_read_b64 v[114:115], v0 offset:232\n\t" \
    "ds_read_b128 v[124:127], v1 offset:3552\n\t" \
    "v_mfma_f32_16x16x32_f16 v[28:31], v[64:67], v[76:79], v[28:31]\n\t" \
    "ds_read_b128 v[128:131], v1 offset:5088\n\t" \
    "s_waitcnt lgkmcnt(14)\n\t" \
    "v_mfma_f32_16x16x32_f16 v[16:19], v[60:63], v[80:83], v[16:19]\n\t" \
    "v_mfma_f32_16x16x32_f16 v[32:35], v[64:67], v[80:83], v[32:35]\n\t" \
    "s_waitcnt lgkmcnt(11)\n\t" \
    "v_mfma_f32_16x16x32_f16 v[8:11], v[84:87], v[96:99], v[8:11]\n\t" \
    "ds_read_b64 v[36:37], v0 offset:256\n\t" \
    "ds_read_b64 v[38:39], v0 offset:264\n\t" \
    "s_waitcnt lgkmcnt(11)\n\t" \
    "v_mfma_f32_16x16x32_f16 v[24:27], v[88:91], v[96:99], v[24:27]\n\t" \
    "ds_read_b128 v[48:51], v1 offset:1920\n\t" \
    "ds_read_b64 v[40:41], v0 offset:288\n\t" \
    "s_waitcnt lgkmcnt(12)\n\t" \
    "v_mfma_f32_16x16x32_f16 v[12:15], v[84:87], v[100:103], v[12:15]\n\t" \
    "ds_read_b64 v[42:43], v0 offset:296\n\t" \
    "ds_read_b128 v[52:55], v1 offset:3456\n\t" \
    "v_mfma_f32_16x16x32_f16 v[28:31], v[88:91], v[100:103], v[28:31]\n\t" \
    "ds_read_b128 v[56:59], v1 offset:4992\n\t" \
    "s_waitcnt lgkmcnt(14)\n\t" \
    "v_mfma_f32_16x16x32_f16 v[16:19], v[84:87], v[104:107], v[16:19]\n\t" \
    "v_mfma_f32_16x16x32_f16 v[32:35], v[88:91], v[104:107], v[32:35]\n\t" \
    "s_waitcnt lgkmcnt(11)\n\t" \
    "v_mfma_f32_16x16x32_f16 v[8:11], v[108:111], v[120:123], v[8:11]\n\t" \
    "ds_read_b64 v[60:61], v0 offset:320\n\t" \
    "ds_read_b64 v[62:63], v0 offset:328\n\t" \
    "s_waitcnt lgkmcnt(11)\n\t" \
    "v_mfma_f32_16x16x32_f16 v[24:27], v[112:115], v[120:123], v[24:27]\n\t" \
    "ds_read_b128 v[72:75], v1 offset:1824\n\t" \
    "ds_read_b64 v[64:65], v0 offset:352\n\t" \
    "s_waitcnt lgkmcnt(12)\n\t" \
    "v_mfma_f32_16x16x32_f16 v[12:15], v[108:111], v[124:127], v[12:15]\n\t" \
    "ds_read_b64 v[66:67], v0 offset:360\n\t" \
    "ds_read_b128 v[76:79], v1 offset:3360\n\t" \
    "v_mfma_f32_16x16x32_f16 v[28:31], v[112:115], v[124:127], v[28:31]\n\t" \
    "ds_read_b128 v[80:83], v1 offset:4896\n\t" \
    "s_waitcnt lgkmcnt(14)\n\t" \
    "v_mfma_f32_16x16x32_f16 v[16:19], v[108:111], v[128:131], v[16:19]\n\t" \
    "v_mfma_f32_16x16x32_f16 v[32:35], v[112:115], v[128:131], v[32:35]\n\t" \
    "v_add_u32 v0, 256, v0\n\t" \
    "v_subrev_u32 v1, 384, v1\n\t" \
    "s_sub_u32 s69, s69, 1\n\t" \
    "s_cmp_lg_u32 s69, 0\n\t" \
    "s_cbranch_scc1 cv_c2_%=\n\t" \
    "s_waitcnt lgkmcnt(0)\n\t" \
    "ds_read_b64 v[36:37], v0 offset:0\n\t" \
    "ds_read_b64 v[38:39], v0 offset:8\n\t" \
    "ds_read_b128 v[52:55], v1 offset:3840\n\t" \
    "ds_read_b64 v[40:41], v0 offset:32\n\t" \
    "ds_read_b64 v[42:43], v0 offset:40\n\t" \
    "ds_read_b128 v[56:59], v1 offset:5376\n\t" \
    "ds_read_b64 v[60:61], v0 offset:64\n\t" \
    "ds_read_b64 v[62:63], v0 offset:72\n\t" \
    "ds_read_b128 v[76:79], v1 offset:3744\n\t" \
    "ds_read_b64 v[64:65], v0 offset:96\n\t" \
    "ds_read_b64 v[66:67], v0 offset:104\n\t" \
    "ds_read_b128 v[80:83], v1 offset:5280\n\t" \
    "s_mov_b32 s69, 4\n\t" \
    "cv_c3_%=:\n\t" \
    "s_waitcnt lgkmcnt(9)\n\t" \
    "v_mfma_f32_16x16x32_f16 v[12:15], v[36:39], v[52:55], v[12:15]\n\t" \
    "ds_read_b64 v[84:85], v0 offset:128\n\t" \
    "ds_read_b64 v[86:87], v0 offset:136\n\t" \
    "s_waitcnt lgkmcnt(9)\n\t" \
    "v_mfma_f32_16x16x32_f16 v[28:31], v[40:43], v[52:55], v[28:31]\n\t" \
    "ds_read_b128 v[100:103], v1 offset:3648\n\t" \
    "ds_read_b64 v[88:89], v0 offset:160\n\t" \
    "s_waitcnt lgkmcnt(10)\n\t" \
    "v_mfma_f32_16x16x32_f16 v[16:19], v[36:39], v[56:59], v[16:19]\n\t" \
    "ds_read_b64 v[90:91], v0 offset:168\n\t" \
    "ds_read_b128 v[104:107], v1 offset:5184\n\t" \
    "v_mfma_f32_16x16x32_f16 v[32:35], v[40:43], v[56:59], v[32:35]\n\t" \
    "s_waitcnt lgkmcnt(9)\n\t" \
    "v_mfma_f32_16x16x32_f16 v[12:15], v[60:63], v[76:79], v[12:15]\n\t" \
    "ds_read_b64 v[108:109], v0 offset:192\n\t" \
    "ds_read_b64 v[110:111], v0 offset:200\n\t" \
    "s_waitcnt lgkmcnt(9)\n\t" \
    "v_mfma_f32_16x16x32_f16 v[28:31], v[64:67], v[76:79], v[28:31]\n\t" \
    "ds_read_b128 v[124:127], v1 offset:3552\n\t" \
    "ds_read_b64 v[112:113], v0 offset:224\n\t" \
    "s_waitcnt lgkmcnt(10)\n\t" \
    "v_mfma_f32_16x16x32_f16 v[16:19], v[60:63], v[80:83], v[16:19]\n\t" \
    "ds_read_b64 v[114:115], v0 offset:232\n\t" \
    "ds_read_b128 v[128:131], v1 offset:5088\n\t" \
    "v_mfma_f32_16x16x32_f16 v[32:35], v[64:67], v[80:83], v[32:35]\n\t" \
    "s_waitcnt lgkmcnt(9)\n\t" \
    "v_mfma_f32_16x16x32_f16 v[12:15], v[84:87], v[100:103], v[12:15]\n\t" \
    "ds_read_b64 v[36:37], v0 offset:256\n\t" \
    "ds_read_b64 v[38:39], v0 offset:264\n\t" \
    "s_waitcnt lgkmcnt(9)\n\t" \
    "v_mfma_f32_16x16x32_f16 v[28:31], v[88:91], v[100:103], v[28:31]\n\t" \
    "ds_read_b128 v[52:55], v1 offset:3456\n\t" \
    "ds_read_b64 v[40:41], v0 offset:288\n\t" \
    "s_waitcnt lgkmcnt(10)\n\t" \
    "v_mfma_f32_16x16x32_f16 v[16:19], v[84:87], v[104:107], v[16:19]\n\t" \
    "ds_read_b64 v[42:43], v0 offset:296\n\t" \
    "ds_read_b128 v[56:59], v1 offset:4992\n\t" \
    "v_mfma_f32_16x16x32_f16 v[32:35], v[88:91], v[104:107], v[32:35]\n\t" \
    "s_waitcnt lgkmcnt(9)\n\t" \
    "v_mfma_f32_16x16x32_f16 v[12:15], v[108:111], v[124:127], v[12:15]\n\t" \
    "ds_read_b64 v[60:61], v0 offset:320\n\t" \
    "ds_read_b64 v[62:63], v0 offset:328\n\t" \
    "s_waitcnt lgkmcnt(9)\n\t" \
    "v_mfma_f32_16x16x32_f16 v[28:31], v[112:115], v[124:127], v[28:31]\n\t" \
    "ds_read_b128 v[76:79], v1 offset:3360\n\t" \
    "ds_read_b64 v[64:65], v0 offset:352\n\t" \
    "s_waitcnt lgkmcnt(10)\n\t" \
    "v_mfma_f32_16x16x32_f16 v[16:19], v[108:111], v[128:131], v[16:19]\n\t" \
    "ds_read_b64 v[66:67], v0 offset:360\n\t" \
    "ds_read_b128 v[80:83], v1 offset:4896\n\t" \
    "v_mfma_f32_16x16x32_f16 v[32:35], v[112:115], v[128:131], v[32:35]\n\t" \
    "v_add_u32 v0, 256, v0\n\t" \
    "v_subrev_u32 v1, 384, v1\n\t" \
    "s_sub_u32 s69, s69, 1\n\t" \
    "s_cmp_lg_u32 s69, 0\n\t" \
    "s_cbranch_scc1 cv_c3_%=\n\t" \
    "s_waitcnt lgkmcnt(0)\n\t" \
    "ds_read_b64 v[36:37], v0 offset:0\n\t" \
    "ds_read_b64 v[38:39], v0 offset:8\n\t" \
    "ds_read_b128 v[56:59], v1 offset:5376\n\t" \
    "ds_read_b64 v[40:41], v0 offset:32\n\t" \
    "ds_read_b64 v[42:43], v0 offset:40\n\t" \
    "ds_read_b64 v[60:61], v0 offset:64\n\t" \
    "ds_read_b64 v[62:63], v0 offset:72\n\t" \
    "ds_read_b128 v[80:83], v1 offset:5280\n\t" \
    "ds_read_b64 v[64:65], v0 offset:96\n\t" \
    "ds_read_b64 v[66:67], v0 offset:104\n\t" \
    "ds_read_b64 v[84:85], v0 offset:128\n\t" \
    "ds_read_b64 v[86:87], v0 offset:136\n\t" \
    "ds_read_b128 v[104:107], v1 offset:5184\n\t" \
    "ds_read_b64 v[88:89], v0 offset:160\n\t" \
    "ds_read_b64 v[90:91], v0 offset:168\n\t" \
    "s_mov_b32 s69, 4\n\t" \
    "cv_c4_%=:\n\t" \
    "s_waitcnt lgkmcnt(12)\n\t" \
    "v_mfma_f32_16x16x32_f16 v[16:19], v[36:39], v[56:59], v[16:19]\n\t" \
    "ds_read_b64 v[108:109], v0 offset:192\n\t" \
    "ds_read_b64 v[110:111], v0 offset:200\n\t" \
    "ds_read_b128 v[128:131], v1 offset:5088\n\t" \
    "s_waitcnt lgkmcnt(13)\n\t" \
    "v_mfma_f32_16x16x32_f16 v[32:35], v[40:43], v[56:59], v[32:35]\n\t" \
    "ds_read_b64 v[112:113], v0 offset:224\n\t" \
    "ds_read_b64 v[114:115], v0 offset:232\n\t" \
    "s_waitcnt lgkmcnt(12)\n\t" \
    "v_mfma_f32_16x16x32_f16 v[16:19], v[60:63], v[80:83], v[16:19]\n\t" \
    "ds_read_b64 v[36:37], v0 offset:256\n\t" \
    "ds_read_b64 v[38:39], v0 offset:264\n\t" \
    "ds_read_b128 v[56:59], v1 offset:4992\n\t" \
    "s_waitcnt lgkmcnt(13)\n\t" \
    "v_mfma_f32_16x16x32_f16 v[32:35], v[64:67], v[80:83], v[32:35]\n\t" \
    "ds_read_b64 v[40:41], v0 offset:288\n\t" \
    "ds_read_b64 v[42:43], v0 offset:296\n\t" \
    "s_waitcnt lgkmcnt(12)\n\t" \
    "v_mfma_f32_16x16x32_f16 v[16:19], v[84:87], v[104:107], v[16:19]\n\t" \
    "ds_read_b64 v[60:61], v0 offset:320\n\t" \
    "ds_read_b64 v[62:63], v0 offset:328\n\t" \
    "ds_read_b128 v[80:83], v1 offset:4896\n\t" \
    "s_waitcnt lgkmcnt(13)\n\t" \
    "v_mfma_f32_16x16x32_f16 v[32:35], v[88:91], v[104:107], v[32:35]\n\t" \
    "ds_read_b64 v[64:65], v0 offset:352\n\t" \
    "ds_read_b64 v[66:67], v0 offset:360\n\t" \
    "s_waitcnt lgkmcnt(12)\n\t" \
    "v_mfma_f32_16x16x32_f16 v[16:19], v[108:111], v[128:131], v[16:19]\n\t" \
    "ds_read_b64 v[84:85], v0 offset:384\n\t" \
    "ds_read_b64 v[86:87], v0 offset:392\n\t" \
    "ds_read_b128 v[104:107], v1 offset:4800\n\t" \
    "s_waitcnt lgkmcnt(13)\n\t" \
    "v_mfma_f32_16x16x32_f16 v[32:35], v[112:115], v[128:131], v[32:35]\n\t" \
    "ds_read_b64 v[88:89], v0 offset:416\n\t" \
    "ds_read_b64 v[90:91], v0 offset:424\n\t" \
    "v_add_u32 v0, 256, v0\n\t" \
    "v_subrev_u32 v1, 384, v1\n\t" \
    "s_sub_u32 s69, s69, 1\n\t" \
    "s_cmp_lg_u32 s69, 0\n\t" \
    "s_cbranch_scc1 cv_c4_%=\n\t" \
    "s_waitcnt lgkmcnt(0)\n\t" \
    "s_nop 7\n\t" \
    "ds_write_b128 v2, v[4:7] offset:0\n\t" \
    "ds_write_b128 v2, v[8:11] offset:1024\n\t" \
    "ds_write_b128 v2, v[12:15] offset:2048\n\t" \
    "ds_write_b128 v2, v[16:19] offset:3072\n\t" \
    "ds_write_b128 v2, v[20:23] offset:4096\n\t" \
    "ds_write_b128 v2, v[24:27] offset:5120\n\t" \
    "ds_write_b128 v2, v[28:31] offset:6144\n\t" \
    "ds_write_b128 v2, v[32:35] offset:7168\n\t" \
    "s_waitcnt lgkmcnt(0)\n\t" \
    "cv_last_%=:\n\t" \
    ""
#define ASM_CLOB "v0", "v1", "v2", "v3", "v4", "v5", "v6", "v7", "v8", "v9", "v10", "v11", "v12", "v13", "v14", "v15", "v16", "v17", "v18", "v19", "v20", "v21", "v22", "v23", "v24", "v25", "v26", "v27", "v28", "v29", "v30", "v31", "v32", "v33", "v34", "v35", "v36", "v37", "v38", "v39", "v40", "v41", "v42", "v43", "v44", "v45", "v46", "v47", "v48", "v49", "v50", "v51", "v52", "v53", "v54", "v55", "v56", "v57", "v58", "v59", "v60", "v61", "v62", "v63", "v64", "v65", "v66", "v67", "v68", "v69", "v70", "v71", "v72", "v73", "v74", "v75", "v76", "v77", "v78", "v79", "v80", "v81", "v82", "v83", "v84", "v85", "v86", "v87", "v88", "v89", "v90", "v91", "v92", "v93", "v94", "v95", "v96", "v97", "v98", "v99", "v100", "v101", "v102", "v103", "v104", "v105", "v106", "v107", "v108", "v109", "v110", "v111", "v112", "v113", "v114", "v115", "v116", "v117", "v118", "v119", "v120", "v121", "v122", "v123", "v124", "v125", "v126", "v127", "v128", "v129", "v130", "v131", "v132", "v133", "v134", "v135", "v136", "v137", "v138", "v139", "v140", "v141", "v142", "v143", "v144", "v145", "v146", "v147", "v148", "v149", "v150", "v151", "v152", "v153", "v154", "v155", "v156", "v157", "v158", "v159", "v160", "v161", "v162", "v163", "v164", "v165", "v166", "v167", "v168", "v169", "v170", "v171", "v172", "v173", "v174", "v175", "v176", "v177", "v178", "v179", "v180", "v181", "v182", "v183", "v184", "v185", "v186", "v187", "v188", "v189", "v190", "v191", "v192", "v193", "v194", "v195", "v196", "v197", "v198", "v199", "v200", "v201", "v202", "v203", "v204", "v205", "v206", "v207", "v208", "v209", "v210", "v211", "v212", "v213", "v214", "v215", "v222", "v223", "v224", "v225", "v226", "v227", "v228", "v229", "v230", "v231", "v232", "v233", "v234", "v235", "v236", "v237", "v238", "v239", "v240", "v241", "v242", "v243", "v244", "v245", "v246", "v247", "v248", "v249", "v250", "v251", "v252", "v253", "v254", "v255", "a0", "a1", "a2", "a3", "a4", "a5", "a6", "a7", "a8", "a9", "a10", "a11", "a12", "a13", "a14", "a15", "a16", "a17", "a18", "a19", "a20", "a21", "a22", "a23", "a24", "a25", "a26", "a27", "a28", "a29", "a30", "a31", "a32", "a33", "a34", "a35", "a36", "a37", "a38", "a39", "a40", "a41", "a42", "a43", "a44", "a45", "a46", "a47", "a48", "a49", "a50", "a51", "a52", "a53", "a54", "a55", "a56", "a57", "a58", "a59", "a60", "a61", "a62", "a63", "a64", "a65", "a66", "a67", "a68", "a69", "a70", "a71", "a72", "a73", "a74", "a75", "a76", "a77", "a78", "a79", "a80", "a81", "a82", "a83", "a84", "a85", "a86", "a87", "a88", "a89", "a90", "a91", "a92", "a93", "a94", "a95", "a96", "a97", "a98", "a99", "a100", "a101", "a102", "a103", "a104", "a105", "a106", "a107", "a108", "a109", "a110", "a111", "a112", "a113", "a114", "a115", "a116", "a117", "a118", "a119", "a120", "a121", "a122", "a123", "a124", "a125", "a126", "a127", "a128", "a129", "a130", "a131", "a132", "a133", "a134", "a135", "a136", "a137", "a138", "a139", "a140", "a141", "a142", "a143", "a144", "a145", "a146", "a147", "a148", "a149", "a150", "a151", "a152", "a153", "a154", "a155", "a156", "a157", "a158", "a159", "a160", "a161", "a162", "a163", "a164", "a165", "a166", "a167", "a168", "a169", "a170", "a171", "a172", "a173", "a174", "a175", "a176", "a177", "a178", "a179", "a180", "a181", "a182", "a183", "a184", "a185", "a186", "a187", "a188", "a189", "a190", "a191", "a192", "a193", "a194", "a195", "a196", "a197", "a198", "a199", "a200", "a201", "a202", "a203", "a204", "a205", "a206", "a207", "a208", "a209", "a210", "a211", "a212", "a213", "a214", "a215", "a216", "a217", "a218", "a219", "a220", "a221", "a222", "a223", "a224", "a225", "a226", "a227", "a228", "a229", "a230", "a231", "a232", "a233", "a234", "a235", "a236", "a237", "a238", "a239", "a240", "a241", "a242", "a243", "a244", "a245", "a246", "a247", "a248", "a249", "a250", "a251", "a252", "a253", "a254", "a255", "s33", "s34", "s35", "s36", "s37", "s38", "s39", "s40", "s41", "s42", "s43", "s44", "s45", "s46", "s47", "s48", "s49", "s50", "s51", "s52", "s53", "s54", "s55", "s56", "s57", "s58", "s59", "s60", "s61", "s62", "s63", "s64", "s65", "s66", "s67", "s68", "s69", "s70", "s71", "s72", "s73", "s74", "s75", "s76", "s77", "s78", "s79", "s80", "s81", "s82", "s83", "s84", "s85", "s86", "s87", "s88", "s89", "s90", "s91", "s92", "s93", "s94", "s95", "s96", "s97", "s98", "s99", "vcc", "scc", "memory"
#define CV_CLOB "v0", "v1", "v2", "v3", "v4", "v5", "v6", "v7", "v8", "v9", "v10", "v11", "v12", "v13", "v14", "v15", "v16", "v17", "v18", "v19", "v20", "v21", "v22", "v23", "v24", "v25", "v26", "v27", "v28", "v29", "v30", "v31", "v32", "v33", "v34", "v35", "v36", "v37", "v38", "v39", "v40", "v41", "v42", "v43", "v44", "v45", "v46", "v47", "v48", "v49", "v50", "v51", "v52", "v53", "v54", "v55", "v56", "v57", "v58", "v59", "v60", "v61", "v62", "v63", "v64", "v65", "v66", "v67", "v68", "v69", "v70", "v71", "v72", "v73", "v74", "v75", "v76", "v77", "v78", "v79", "v80", "v81", "v82", "v83", "v84", "v85", "v86", "v87", "v88", "v89", "v90", "v91", "v92", "v93", "v94", "v95", "v96", "v97", "v98", "v99", "v100", "v101", "v102", "v103", "v104", "v105", "v106", "v107", "v108", "v109", "v110", "v111", "v112", "v113", "v114", "v115", "v116", "v117", "v118", "v119", "v120", "v121", "v122", "v123", "v124", "v125", "v126", "v127", "v128", "v129", "v130", "v131", "s69", "scc", "memory"
static constexpr unsigned G_STEP = 0x80000;
static constexpr unsigned G_CPY = 0x10000;
static constexpr unsigned G_HOFF = 0x40000;
static constexpr unsigned G_RM = 0x4;
static constexpr unsigned G_RP = 0x2;
static constexpr unsigned G_HP = 0x20;
static constexpr unsigned G_HKB = 0x8;
static constexpr unsigned G_RHO = 0x1400;
static constexpr unsigned G_RHS = 0x600;
static constexpr unsigned G_RARM = 0x1;
static constexpr unsigned G_NARM = 0x4;
static constexpr unsigned G_WS_UM = 0x400000;
static constexpr unsigned G_WS_CTL = 0x5d0000;
static constexpr unsigned G_WS_DONE = 0x5f8000;
static constexpr unsigned G_WS_EP = 0x5fa000;
static constexpr unsigned G_WS_STAMPS = 0xc000000;
static constexpr unsigned G_C_TASK = 0x200;
static constexpr unsigned G_C_GX = 0x1000;
static constexpr unsigned G_C_GV = 0x4000;
static constexpr unsigned G_C_KM = 0x8000;
static constexpr unsigned G_C_UM = 0x9000;
static constexpr unsigned G_C_PC = 0xc000;
static constexpr unsigned G_C_UT = 0xd000;
static constexpr unsigned G_C_GK = 0x10000;
static constexpr unsigned G_C_YC = 0x11000;
static constexpr unsigned G_C_ERR = 0x100;
static constexpr unsigned G_C_ARR = 0x14000;
static constexpr unsigned G_C_KG = 0x1c000;
static constexpr unsigned G_C_END = 0x22000;
static constexpr unsigned G_CTF = 0x2000;
static constexpr unsigned G_GVC = 0x2;
static constexpr unsigned G_GVH = 0x14000;
static constexpr unsigned G_HS8 = 0x80000;
static constexpr unsigned G_HS4 = 0x40000;
static constexpr unsigned G_UXS = 0x200000;
static constexpr unsigned G_UMH = 0xa0000;
static constexpr unsigned G_D_QX = 0x600000;
static constexpr unsigned G_D_KX = 0x1600000;
static constexpr unsigned G_D_KM = 0x2600000;
static constexpr unsigned G_D_UX = 0x2e00000;
static constexpr unsigned G_D_UMX = 0x7600000;
static constexpr unsigned G_D_PX = 0x8c00000;
static constexpr unsigned G_D_PM = 0x9000000;
static constexpr unsigned G_D_KT = 0x9100000;
static constexpr unsigned G_D_UT = 0x9600000;
static constexpr unsigned G_D_YB = 0x9900000;
static constexpr unsigned G_D_END = 0x9c00000;
static constexpr unsigned G_SKM = 0x7;
static constexpr unsigned G_GVN = 0x2;
static constexpr unsigned G_STG = 0x9000;
static constexpr unsigned G_NA_ = 0x1a;
static constexpr unsigned G_KROW = 0x30;
#define FLG(x_) (kDefault[5])
#define FLG2(x_) (kDefault[7])
static constexpr int kDefault[8] = {PRM_DEFAULT};
#define MFMA32(a_, b_, c_) __builtin_amdgcn_mfma_f32_16x16x32_bf16(__builtin_bit_cast(bf16x8, a_), __builtin_bit_cast(bf16x8, b_), c_, 0, 0, 0)
#define MFMA16(a_, b_, c_) __builtin_amdgcn_mfma_f32_16x16x16bf16_1k(__builtin_bit_cast(s16x4, a_), __builtin_bit_cast(s16x4, b_), c_, 0, 0, 0)
#define WLD8(d_, voff_, base_, off_) asm volatile("s_nop 4\n\tglobal_load_dwordx2 %0, %1, %2 offset:%3 sc1" : "=v"(d_) : "v"(voff_), "s"(base_), "i"(off_) : "memory")
#define WLD16(d_, voff_, base_, off_) asm volatile("s_nop 4\n\tglobal_load_dwordx4 %0, %1, %2 offset:%3 sc1" : "=v"(d_) : "v"(voff_), "s"(base_), "i"(off_) : "memory")
#define WST16(voff_, d_, base_, off_) asm volatile("s_nop 4\n\tglobal_store_dwordx4 %0, %1, %2 offset:%3 sc1\n\ts_nop 1" :: "v"(voff_), "v"(d_), "s"(base_), "i"(off_) : "memory")
#define WLD16X4(d0, d1, d2, d3, voff_, base_) asm volatile("s_nop 4\n\tglobal_load_dwordx4 %0, %4, %5 sc1\n\tglobal_load_dwordx4 %1, %4, %5 offset:1024 sc1\n\t" \
    "global_load_dwordx4 %2, %4, %5 offset:2048 sc1\n\tglobal_load_dwordx4 %3, %4, %5 offset:3072 sc1" \
    : "=&v"(d0), "=&v"(d1), "=&v"(d2), "=&v"(d3) : "v"(voff_), "s"(base_) : "memory")

__device__ __forceinline__ u32x4 pack8(const f32x4& a, const f32x4& b) {
    return u32x4{pk2bf(a[0], a[1]), pk2bf(a[2], a[3]), pk2bf(b[0], b[1]), pk2bf(b[2], b[3])};
}
template <class T> __device__ __forceinline__ T* uni(T* q) {
    const u64 v = (u64)q;
    unsigned lo = __builtin_amdgcn_readfirstlane((unsigned)v), hi = __builtin_amdgcn_readfirstlane((unsigned)(v >> 32));
    asm volatile("s_nop 4" : "+s"(lo), "+s"(hi));
    return (T*)(((u64)hi << 32) | lo);
}
#define CTLO (G_WS_CTL + (unsigned)p.prm[0])
template <bool FAST = false>
__device__ __forceinline__ void wait_word(const MainP& p, const char* f, int want, unsigned code) {
    unsigned tries = 0;
    while (__any((int)__hip_atomic_load((const gu32*)f, RLX_AGENT) < want)) {
        if (++tries > kWaitCap) { __hip_atomic_store((gu32*)(p.ws + G_WS_CTL + G_C_ERR), code, RLX_AGENT); break; }
        if ((tries & 1023u) == 0u && __hip_atomic_load((const gu32*)(p.ws + G_WS_CTL + G_C_ERR), RLX_AGENT) != 0xffffffffu) break;
        if (FAST) __builtin_amdgcn_s_sleep(1); else __builtin_amdgcn_s_sleep(10);
    }
}

__device__ __forceinline__ int skew(int s, int id) { return (s ^ (G_SKM * id)) & 63; }
__device__ __forceinline__ unsigned umx_base(int h) { return G_D_UMX + (h / 3) * 0x200000 + (h % 3) * G_UMH; }
typedef GAS unsigned long long gu64;
__device__ __forceinline__ void init_done(char* ws, int slot, unsigned val) {
    const unsigned e = __hip_atomic_load((const gu32*)(ws + G_WS_EP), RLX_AGENT);
    __hip_atomic_store((gu64*)(ws + G_WS_DONE + slot * 64), ((u64)val << 32) | (e + 1u), RLX_AGENT);
}
__device__ __forceinline__ void init_seen(char* ws, int lane, unsigned e) {
    for (unsigned tries = 0; tries < kWaitCap; ++tries) {
        if (!__any(__hip_atomic_load((const gu32*)(ws + G_WS_DONE + min(lane, 59) * 64), RLX_AGENT) != e + 1u)) break;
        __builtin_amdgcn_s_sleep(8);
    }
}
__device__ __forceinline__ void init_count(char* ws, int lane) { if (lane == 0) __hip_atomic_fetch_add((gu32*)(ws + G_WS_CTL + G_C_ARR), 1u, RLX_AGENT); }
__device__ __forceinline__ void fill_block(char* ws, int b, int tid, const u32x4& v0, const u32x4& v1) {
    const unsigned o = b * 8192 + tid * 16, o2 = o + 4096;
    asm volatile("global_store_dwordx4 %0, %1, %2 sc1\n\ts_nop 1" :: "v"(o), "v"(v0), "s"(ws) : "memory");
    asm volatile("global_store_dwordx4 %0, %1, %2 sc1\n\ts_nop 1" :: "v"(o2), "v"(v1), "s"(ws) : "memory");
}
__device__ __forceinline__ void init_fill(char* ws, const u16* mem0, int f, int tid) {
    const u32x4 sent = {0xffffffffu, 0xffffffffu, 0xffffffffu, 0xffffffffu};
    u32x4 v0 = sent, v1 = sent;
    if (f < 16) {
#pragma unroll
        for (int k = 0; k < 2; ++k) {
            const int q = tid + 256 * k + ((f & 2) ? 256 : 0), m = q & 15;
            if (!(f & 2) || (q >= 512 && q < 576)) {
                const unsigned* src = (const unsigned*)(mem0 + (2 * (m >> 2) + (m & 1)) * DM + 16 * (q >> 4) + (m & 2));
                const u32x4 o = {min(src[0], 0xfffffffeu), min(src[2], 0xfffffffeu), min(src[4], 0xfffffffeu), min(src[6], 0xfffffffeu)};
                if (k) v1 = o; else v0 = o;
            }
        }
    }
    constexpr int HB2 = 2 * ((G_HKB + 7) / 8), NH = 32 + 8 * G_RP, NP = NH + 8 * HB2, NU = 144;
#pragma unroll 1
    for (int q = f; q < NP + NU + (int)(G_C_END >> 13); q += 48) {
        const int u = q - 32, y = q - NH;
        fill_block(ws, q < 32 ? (q >> 4) * 256 + ((q >> 2) & 3) * 8 + 4 + (q & 3) : q < NH ? ((u >> 2) & 1) * 256 + (u >> 3) * 8 + (u & 3) : q < NP ? (y & 1) * 256 + 32 + ((y >> 1) & 3) * 8 + (y >> 3) : q < NP + NU ? q - NP + 512 : q - NP - NU + (int)(G_WS_CTL >> 13), tid, v0, v1);
        v0 = sent; v1 = sent;
    }
    const unsigned e = __hip_atomic_load((const gu32*)(ws + G_WS_EP), RLX_AGENT);
    asm volatile("s_waitcnt vmcnt(0)" :: "v"(e) : "memory");
    __syncthreads();
    if (tid == 0) __hip_atomic_store((gu64*)(ws + G_WS_DONE + (12 + f) * 64), (u64)(e + 1u), RLX_AGENT);
}
__device__ __forceinline__ void init_cal(char* ws, char* smem, int id, int tid, int n) {
    const int sl = id % 6, w = tid >> 6;
    char* a = ws + (size_t)(sl >> 1) * 0x200000 + 0x1B0000 + (id / 6) * 16384 + (sl & 1) * 8192 + w * 2048;
    volatile unsigned* lds = (volatile unsigned*)smem;
    const u32x4 z = {0u, 0u, 0u, 0u};
    if (tid == 0) lds[4] = 0u;
    asm volatile("global_store_dwordx4 %0, %1, off sc1\n\tglobal_store_dwordx4 %0, %1, off offset:1024 sc1\n\ts_waitcnt vmcnt(0)" :: "v"(a + (tid & 63) * 16), "v"(z) : "memory");
    __syncthreads();
    if ((tid & 63) == 0) {
        unsigned best = 0xfffffffeu;
#pragma unroll 1
        for (int it = 0; it < 2 * n; ++it) {
            const unsigned c0 = (unsigned)__builtin_amdgcn_s_memtime();
            const unsigned v = __hip_atomic_fetch_or((gu32*)(a + it * 128), 0u, RLX_AGENT);
            asm volatile("s_waitcnt vmcnt(0)" :: "v"(v) : "memory");
            best = min(best, (unsigned)__builtin_amdgcn_s_memtime() - c0);
            if (best < 500u) lds[4] = 1u;
            if (it + 1 >= n && lds[4]) break;
        }
        lds[w] = best;
    }
    __syncthreads();
    if (tid == 0) init_done(ws, id, min(min(lds[0], lds[1]), min(lds[2], lds[3])));
    __syncthreads();
}

#define TST(slot_, i_)
#define PIN8(a) asm volatile("" : "+v"(a[0]), "+v"(a[1]), "+v"(a[2]), "+v"(a[3]), "+v"(a[4]), "+v"(a[5]), "+v"(a[6]), "+v"(a[7]))
struct AOps { u32x4 q[4], k[8]; int fl; };
struct APtr { const char* q; const char* k; const char* m; unsigned voff, vkm, voffm, vfl; };
__device__ __forceinline__ void attn_batch(const MainP& p, AOps& o, const APtr& a) {
    const char* q2 = a.q + 4096; const char* k2 = a.k + 4096;
    WLD16(o.q[0], a.voff, a.q, 0); WLD16(o.q[1], a.voff, a.q, 2048); WLD16(o.q[2], a.voff, q2, 0); WLD16(o.q[3], a.voff, q2, 2048);
    WLD16X4(o.k[0], o.k[1], o.k[2], o.k[3], a.voff, a.k); WLD16X4(o.k[4], o.k[5], o.k[6], o.k[7], a.voff, k2);
    asm volatile("global_load_dword %0, %1, %2 sc1" : "=v"(o.fl) : "v"(a.vfl), "s"(p.ws) : "memory");
}
__device__ __forceinline__ void attn_pin(AOps& o) {
    asm volatile("" : "+v"(o.fl), "+v"(o.q[0]), "+v"(o.q[1]), "+v"(o.q[2]), "+v"(o.q[3])); PIN8(o.k);
}
static constexpr int NFINE = 6;
static constexpr int NG1 = 64 - NFINE;
__device__ __forceinline__ void score_round4(float* r, const f32x4& x) {
    asm volatile("v_cvt_pk_bf16_f32 %0, 0, %4\n\tv_cvt_pk_bf16_f32 %1, 0, %5\n\tv_cvt_pk_bf16_f32 %2, 0, %6\n\tv_cvt_pk_bf16_f32 %3, 0, %7\n\t"
                 "v_mul_f32 %0, 0x3db504f3, %0\n\tv_mul_f32 %1, 0x3db504f3, %1\n\tv_mul_f32 %2, 0x3db504f3, %2\n\tv_mul_f32 %3, 0x3db504f3, %3\n\t"
                 "v_cvt_pk_bf16_f32 %0, 0, %0\n\tv_cvt_pk_bf16_f32 %1, 0, %1\n\tv_cvt_pk_bf16_f32 %2, 0, %2\n\tv_cvt_pk_bf16_f32 %3, 0, %3"
                 : "=&v"(r[0]), "=&v"(r[1]), "=&v"(r[2]), "=&v"(r[3]) : "v"(x[0]), "v"(x[1]), "v"(x[2]), "v"(x[3]));
}
__device__ __forceinline__ float vmax3(float a, float b, float c) { float r; asm("v_max3_f32 %0, %1, %2, %3" : "=v"(r) : "v"(a), "v"(b), "v"(c)); return r; }
template <int N>
__device__ __forceinline__ void attn_soft(float (&q)[12], u32x2& Pm, u32x4& Px) {
    float m = vmax3(q[0], q[1], q[2]);
#pragma unroll
    for (int i = 3; i < N; i += 2) m = vmax3(m, q[i], q[i + 1 < N ? i + 1 : i]);
    { auto a = __builtin_amdgcn_permlane16_swap(__float_as_uint(m), __float_as_uint(m), false, false); m = vmax3(m, __uint_as_float(a[0]), __uint_as_float(a[1])); }
    { auto a = __builtin_amdgcn_permlane32_swap(__float_as_uint(m), __float_as_uint(m), false, false); m = vmax3(m, __uint_as_float(a[0]), __uint_as_float(a[1])); }
    const float ml = -m * 1.4426950408889634f;
#pragma unroll
    for (int i = 0; i < N; ++i) q[i] = __builtin_amdgcn_exp2f(fmaf(q[i], 1.4426950408889634f, ml));
    float sum = 0.5f * ((q[0] + q[1]) + (q[2] + q[3])) + ((q[4] + q[5]) + (q[6] + q[7]));
    if (N > 8) sum += (q[8] + q[9]) + (q[10] + q[11]);
    sum = rowsum4(sum);
    const float inv = __builtin_amdgcn_rcpf(sum);
    Pm = u32x2{pk2bf(q[0] * inv, q[1] * inv), pk2bf(q[2] * inv, q[3] * inv)};
    Px = u32x4{pk2bf(q[4] * inv, q[5] * inv), pk2bf(q[6] * inv, q[7] * inv), N > 8 ? pk2bf(q[8] * inv, q[9] * inv) : 0u, N > 8 ? pk2bf(q[10] * inv, q[11] * inv) : 0u};
}
static constexpr int NA = G_NA_;
__device__ __forceinline__ int attn_next(int s) { return s < NA ? s + 1 : s + 2; }
__device__ __forceinline__ unsigned attn_flag(const MainP& p, int s, int hq) { return CTLO + G_C_PC + ((s > NA && (s & 1)) ? 1024 : 0) + hq * 128; }
struct BOps { u32x4 q[8], k[8]; };
#define WLD16X4A(d0, d1, d2, d3, voff_, base_) asm volatile("s_nop 4\n\tglobal_load_dwordx4 %0, %4, %5 sc1\n\tglobal_load_dwordx4 %1, %4, %5 offset:1024 sc1\n\t" \
    "global_load_dwordx4 %2, %4, %5 offset:2048 sc1\n\tglobal_load_dwordx4 %3, %4, %5 offset:3072 sc1" \
    : "=&a"(d0), "=&a"(d1), "=&a"(d2), "=&a"(d3) : "v"(voff_), "s"(base_) : "memory")
#define PIN8A(a) asm volatile("" : "+a"(a[0]), "+a"(a[1]), "+a"(a[2]), "+a"(a[3]), "+a"(a[4]), "+a"(a[5]), "+a"(a[6]), "+a"(a[7]))
#define MFMA_AV0(acc, a_, b_) asm volatile("v_mfma_f32_16x16x32_bf16 %0, %1, %2, 0" : "=&v"(acc) : "a"(a_), "v"(b_))
#define MFMA_AV(acc, a_, b_) asm volatile("v_mfma_f32_16x16x32_bf16 %0, %1, %2, %0" : "+v"(acc) : "a"(a_), "v"(b_))
__device__ __forceinline__ void attn_batch8(const MainP& p, BOps& o, const APtr& a) {
    const char* q2 = a.q + 4096; const char* k2 = a.k + 4096;
    WLD16X4(o.q[0], o.q[1], o.q[2], o.q[3], a.voff, a.q); WLD16X4(o.q[4], o.q[5], o.q[6], o.q[7], a.voff, q2);
    WLD16X4A(o.k[0], o.k[1], o.k[2], o.k[3], a.voff, a.k); WLD16X4A(o.k[4], o.k[5], o.k[6], o.k[7], a.voff, k2);
}
__device__ __forceinline__ void attn_step(const MainP& p, BOps& o, BOps& n1, BOps& n2, APtr& a, int& kg, int& kk, int s, int h, int w, int lane) {
    const int g = lane >> 4, c = lane & 15;
    const float scale = 0.08838834764831845f;
    TST(s * 64 + h * 2, 0);
    const int s2 = attn_next(attn_next(s));
    if (kg <= s2 || kk < s) {
        VMC0();
        wait_word<true>(p, p.ws + a.vfl, (lane & 1) ? s : s2 + 1, 13u);
    }
    TST(s * 64 + h * 2, 1);
    a.q = p.ws + G_D_QX + (size_t)h * G_HS8 + skew(s2, h) * 8192; a.k = a.q + (G_D_KX - G_D_QX); a.m = p.ws + G_D_KM + (size_t)h * G_HS4 + skew(s, h) * 4096;
    __builtin_amdgcn_sched_barrier(0);
    u32x4 km[4];
    int fl;
    WLD16X4A(km[0], km[1], km[2], km[3], a.vkm, a.m);
    asm volatile("global_load_dword %0, %1, %2 sc1" : "=v"(fl) : "v"(a.vfl), "s"(p.ws) : "memory");
    attn_batch8(p, n2, a);
    f32x4 y[5];
#pragma unroll
    for (int wc = 0; wc < 4; ++wc)
#pragma unroll
        for (int i = 0; i < 3; ++i) { if (wc == 0) MFMA_AV0(y[i], o.k[i >> 1], o.q[(i + 1) >> 1]); else MFMA_AV(y[i], o.k[wc * 2 + (i >> 1)], o.q[wc * 2 + ((i + 1) >> 1)]); }
    asm volatile("s_nop 7" : "+v"(y[0]), "+v"(y[1]), "+v"(y[2]));
    float q0[12], q1[12];
    score_round4(q0 + 4, y[0]); score_round4(q1 + 4, y[1]); score_round4(q1 + 8, y[2]);
#pragma unroll
    for (int i = 0; i < 4; ++i) {
        const bool vis = 4 * g + i <= c;
        q0[4 + i] = vis ? q0[4 + i] : -INFINITY; q1[8 + i] = vis ? q1[8 + i] : -INFINITY;
    }
    __builtin_amdgcn_sched_barrier(0);
    asm volatile("s_waitcnt vmcnt(16)\n\ts_barrier" : "+a"(km[0]), "+a"(km[1]), "+a"(km[2]), "+a"(km[3]), "+v"(fl) :: "memory");
    PIN8(n1.q); PIN8A(n1.k);
    { const unsigned fo = attn_flag(p, s, h >> 2); const int fv = s - 1;
      asm volatile("s_cmp_lg_u32 %3, 0\n\ts_cbranch_scc1 1f\n\tglobal_store_dword %0, %1, %2 sc1\n1:" :: "v"(fo), "v"(fv), "s"(p.ws), "s"(w) : "scc", "memory"); }
#pragma unroll
    for (int wc = 0; wc < 4; ++wc)
#pragma unroll
        for (int i = 0; i < 2; ++i) { if (wc == 0) MFMA_AV0(y[3 + i], km[0], o.q[i]); else MFMA_AV(y[3 + i], km[wc], o.q[wc * 2 + i]); }
    asm volatile("s_nop 7" : "+v"(y[3]), "+v"(y[4]));
    kg = __builtin_amdgcn_readlane(fl, 0); kk = __builtin_amdgcn_readlane(fl, 1);
    score_round4(q0, y[3]); score_round4(q1, y[4]);
    u32x2 Pm0, Pm1; u32x4 Px0, Px1;
    attn_soft<8>(q0, Pm0, Px0); attn_soft<12>(q1, Pm1, Px1);
    char* pxb = p.ws + G_D_PX + (size_t)(s * 32 + h) * 2048;
    char* pmb = p.ws + G_D_PM + (size_t)s * 16384 + h * 256; char* pm2 = pmb + 8192;
    WST16(a.voff, Px0, pxb, 0); WST16(a.voff, Px1, pxb, 1024);
    asm volatile("global_store_dwordx2 %0, %1, %3 sc1\n\tglobal_store_dwordx2 %0, %2, %4 sc1" :: "v"(a.voffm), "v"(Pm0), "v"(Pm1), "s"(pmb), "s"(pm2) : "memory");
    { const unsigned fo = attn_flag(p, s, h >> 2);
      asm volatile("s_cmp_lt_u32 %4, 46\n\ts_cbranch_scc1 1f\n\ts_waitcnt vmcnt(0)\n\ts_barrier\n\ts_cmp_lg_u32 %3, 0\n\ts_cbranch_scc1 1f\n\tglobal_store_dword %0, %1, %2 sc1\n1:"
                   :: "v"(fo), "v"(s), "s"(p.ws), "s"(w), "s"(s) : "scc", "memory"); }
    TST(s * 64 + h * 2, 2);
}
__device__ __forceinline__ void attn_role(const MainP& p, int s, int h, int w, int lane) {
    APtr a;
    a.q = p.ws + G_D_QX + (size_t)h * G_HS8 + skew(s, h) * 8192; a.k = a.q + (G_D_KX - G_D_QX);
    a.voff = lane * 16; a.vkm = (lane & 55) * 16; a.voffm = (lane & 15) * 16 + ((lane >> 4) & 1) * 8;
    a.vfl = CTLO + ((lane & 1) ? G_C_KM : G_C_GX) + h * 128;
    BOps o0, o1, o2;
    wait_word(p, p.ws + CTLO + G_C_GX + h * 128, attn_next(s) + 1, 10u);
    attn_batch8(p, o0, a);
    a.q = p.ws + G_D_QX + (size_t)h * G_HS8 + skew(attn_next(s), h) * 8192; a.k = a.q + (G_D_KX - G_D_QX);
    attn_batch8(p, o1, a);
    VMC0();
    PIN8(o0.q); PIN8A(o0.k); PIN8(o1.q); PIN8A(o1.k);
    int kg = -1, kk = -1;
    static_assert(NA % 2 == 0 && NG1 + 3 < NSEG, "ranges");
#pragma unroll 1
    for (;;) {
        attn_step(p, o0, o1, o2, a, kg, kk, s, h, w, lane);
        if ((s = attn_next(s)) >= NG1) break;
        attn_step(p, o1, o2, o0, a, kg, kk, s, h, w, lane);
        if ((s = attn_next(s)) >= NG1) break;
        attn_step(p, o2, o0, o1, a, kg, kk, s, h, w, lane);
        if ((s = attn_next(s)) >= NG1) break;
    }
    VMC0();
}
__device__ __forceinline__ unsigned umax4(const u32x4& v) { return max(max(v[0], v[1]), max(v[2], v[3])); }
__device__ __forceinline__ bool give_up(const MainP& p, unsigned& tries, unsigned code) {
    if (++tries <= kWaitCap / 4) return false;
    __hip_atomic_store((gu32*)(p.ws + G_WS_CTL + G_C_ERR), code, RLX_AGENT);
    return true;
}
__device__ __forceinline__ void task_attn_tail(const MainP& p, int s, int h, int w, int lane) {
    const int g = lane >> 4, c = lane & 15, qt = w & 1;
    const float scale = 0.08838834764831845f;
    APtr a;
    a.q = p.ws + G_D_QX + (size_t)h * G_HS8 + skew(s, h) * 8192 + qt * 1024; a.k = p.ws + G_D_KX + (size_t)h * G_HS8 + skew(s, h) * 8192; a.m = p.ws + G_D_KM + (size_t)h * G_HS4 + skew(s, h) * 4096;
    a.voff = lane * 16; a.vkm = (lane & 55) * 16; a.voffm = c * 16 + (g & 1) * 8; a.vfl = G_WS_CTL;
    AOps o;
    wait_word(p, p.ws + CTLO + G_C_GX + h * 128, s + 1, 14u);
    attn_batch(p, o, a);
    VMC0();
    attn_pin(o);
    float q[12];
    u32x4 km[4];
#pragma unroll
    for (int kt = 1; kt >= -1; --kt) {
        if (kt < 0) {
            TST(s * 64 + h * 2 + qt, 0);
            wait_word(p, p.ws + CTLO + G_C_KM + h * 128, s - 2, 15u);
            if (FLG(p.prm[5]) & 2) wait_word<true>(p, p.ws + CTLO + G_C_KM + h * 128, s - 1, 15u);
            for (unsigned tries = 0;;) {
                WLD16X4(km[0], km[1], km[2], km[3], a.vkm, a.m);
                asm volatile("s_waitcnt vmcnt(0)" : "+v"(km[0]), "+v"(km[1]), "+v"(km[2]), "+v"(km[3]) :: "memory");
                if (!__any(max(max(umax4(km[0]), umax4(km[1])), max(umax4(km[2]), umax4(km[3]))) == 0xffffffffu) || give_up(p, tries, 16u)) break;
            }
            TST(s * 64 + h * 2 + qt, 1);
        }
        f32x4 y = {0.f, 0.f, 0.f, 0.f};
#pragma unroll
        for (int wc = 0; wc < 4; ++wc) y = MFMA32(kt < 0 ? km[wc] : o.k[wc * 2 + (kt < 0 ? 0 : kt)], o.q[wc], y);
#pragma unroll
        for (int i = 0; i < 4; ++i) q[4 + 4 * kt + i] = (16 * kt + 4 * g + i <= 16 * qt + c) ? bfr(bfr(y[i]) * scale) : -INFINITY;
    }
    u32x2 Pm; u32x4 Px;
    attn_soft<12>(q, Pm, Px);
    char* pxb = p.ws + G_D_PX + (size_t)(s * 32 + h) * 2048 + qt * 1024;
    char* pmb = p.ws + G_D_PM + (size_t)s * 16384 + h * 256 + qt * 8192;
    WST16(a.voff, Px, pxb, 0);
    asm volatile("global_store_dwordx2 %0, %1, %2 sc1" :: "v"(a.voffm), "v"(Pm), "s"(pmb) : "memory");
    TST(s * 64 + h * 2 + qt, 2);
}

#define RLD(d_, voff_, base_, off_) asm volatile("global_load_dwordx4 %0, %1, %2 offset:%3 sc1" : "=v"(d_) : "v"(voff_), "s"(base_), "i"(off_) : "memory")
#define RLDA(d_, voff_, base_, off_) asm volatile("global_load_dwordx4 %0, %1, %2 offset:%3 sc1" : "=a"(d_) : "v"(voff_), "s"(base_), "i"(off_) : "memory")
#define MFMA_ACC_A(acc, a_, b_) asm volatile("v_mfma_f32_16x16x32_bf16 %0, %1, %2, %0" : "+v"(acc) : "a"(a_), "v"(b_))
#define VMW(n_) asm volatile("s_waitcnt vmcnt(%0)" :: "i"(n_) : "memory")
template <int NT, int DEPTH, bool TAIL>
__device__ __forceinline__ void task_out(const MainP& p, char* smem, int s, int ct0, int w, int lane) {
    const int g = lane >> 4, c = lane & 15;
    const unsigned voff = lane * 16;
    f32x4 acc[NT][2];
#pragma unroll
    for (int t = 0; t < NT; ++t) { acc[t][0] = f32x4{0.f, 0.f, 0.f, 0.f}; acc[t][1] = acc[t][0]; asm volatile("" : "+v"(acc[t][0]), "+v"(acc[t][1])); }
    const char* ub[NT];
#pragma unroll
    for (int t = 0; t < NT; ++t) ub[t] = uni(p.ws + G_D_UX + (size_t)(ct0 + t) * G_UXS + skew(s, ct0 + t) * 32768 + w * 8192);
    const char* pb = uni(p.ws + G_D_PX + (size_t)(s * 32 + w * 8) * 2048);
    const char* um = uni(p.ws + s * 9216 + ct0 * 256);
    const unsigned vu0 = umx_base(w * 8 + g) + c * 16, vu1 = umx_base(w * 8 + 4 + g) + c * 16;
    const char* pm = uni(p.ws + G_D_PM + (size_t)s * 16384 + w * 2048);
    constexpr int NR = DEPTH + 1 > 10 ? 10 : DEPTH + 1;
    u32x4 a[NR][NT], b[10][2];
    unsigned vb = voff, va = voff;
    TST(4096 + (s * 36 + ct0) * 4 + w, 0);
    const int f0 = (int)__hip_atomic_load((const gu32*)(p.ws + CTLO + (lane < 8 ? G_C_UM + (w * 8 + lane) * 128 : lane < 16 ? G_C_GV + ((s & (G_GVN - 1)) * 32 + w * 8 + lane - 8) * 128
                                                                                                                                : attn_flag(p, s, w * 2 + (lane & 1)) - CTLO)), RLX_AGENT);
    const bool early = !TAIL && !__any(f0 < s);
    if (!early) wait_word(p, p.ws + CTLO + (lane < 8 ? G_C_GV + ((s & (G_GVN - 1)) * 32 + w * 8 + lane) * 128 : G_C_UM + w * 8 * 128), lane < 8 ? s : s - 3, 21u);
#define ALOAD(q) do { if ((q) < 8) { _Pragma("unroll") for (int t = 0; t < NT; ++t) RLDA(a[(q) % NR][t], va, ub[t], 0);  \
                                     asm volatile("v_add_u32 %0, 0x400, %0" : "+v"(va)); }                                            \
                      else { _Pragma("unroll") for (int t = 0; t < NT; ++t) RLDA(a[(q) % NR][t], ((q) & 1) ? vu1 : vu0, um, t * 256); } } while (0)
    constexpr int D0 = DEPTH < 8 ? DEPTH : 8;
    asm volatile("s_nop 4");
#pragma unroll
    for (int q = 0; q < D0; ++q) ALOAD(q);
    asm volatile("s_cmp_lg_u32 %0, 0\n\ts_cbranch_scc1 1f\n\ts_waitcnt vmcnt(0)\n1:\n\ts_waitcnt vmcnt(30)" :: "s"(__builtin_amdgcn_readfirstlane((int)early)) : "scc", "memory");
    unsigned tries = 0;
    if (TAIL) {
        const char* sp = pm + (lane & 7) * 256 + ((lane >> 3) & 1) * 8192;
        const int dl = (FLG(p.prm[5]) >> 26) & 31;
        if (dl) {
            wait_word<true>(p, p.ws + CTLO + G_C_KM + ((ct0 >> 1) + w) * 128, s - 1, 25u);
            for (int i = 0; i < dl + (s < 63 ? 5 : 0); ++i) __builtin_amdgcn_s_sleep(2);
        } else
        while (__any(__hip_atomic_load((const gu32*)sp, RLX_AGENT) == 0xffffffffu) && !give_up(p, tries, 23u)) __builtin_amdgcn_s_sleep(1);
    } else if (!early) wait_word(p, p.ws + CTLO + (lane < 8 ? G_C_UM + (w * 8 + lane) * 128 : attn_flag(p, s, w * 2 + (lane & 1)) - CTLO), s, 22u);
    if (!early) TST(4096 + (s * 36 + ct0) * 4 + w, 1);
    for (;;) {
    asm volatile("s_nop 4");
#pragma unroll
    for (int q = 0; q < 8; ++q) { RLD(b[q][0], vb, pb, 0); RLD(b[q][1], vb, pb, 1024); asm volatile("v_add_u32 %0, 0x800, %0" : "+v"(vb)); }
#pragma unroll
    for (int q = 8; q < 10; ++q) { RLD(b[q][0], voff, pm, (q & 1) * 1024); RLD(b[q][1], voff, pm + 8192, (q & 1) * 1024); }
#pragma unroll
    for (int q = D0; q < NR; ++q) ALOAD(q);
#pragma unroll
    for (int q = 0; q < 10; ++q) {
        const int issuedA = (NR + q < 10 ? NR + q : 10) - D0;
        if (q < D0) VMW(2 * (9 - q) + issuedA * NT); else VMW((issuedA - (q - D0 + 1)) * NT);
#pragma unroll
        for (int t = 0; t < NT; ++t) { MFMA_ACC_A(acc[t][0], a[q % NR][t], b[q][0]); MFMA_ACC_A(acc[t][1], a[q % NR][t], b[q][1]); }
        if (NR + q < 10) ALOAD(NR + q);
    }
    asm volatile("s_nop 7" ::: "memory");
#pragma unroll
    for (int t = 0; t < NT; ++t) asm volatile("" : "+v"(acc[t][0]), "+v"(acc[t][1]));
    if (!TAIL) break;
    f32x4 chk = acc[0][0];
#pragma unroll
    for (int t = 0; t < NT; ++t) chk += acc[t][1] + acc[t][0];
    const float ck = (chk[0] + chk[1]) + (chk[2] + chk[3]);
    if (!__any(ck != ck)) break;
    unsigned mx = 0;
#pragma unroll
    for (int q = 0; q < 10; ++q) { asm volatile("" : "+v"(b[q][0]), "+v"(b[q][1])); mx = max(mx, max(umax4(b[q][0]), umax4(b[q][1]))); }
#pragma unroll
    for (int t = 0; t < NT; ++t) { asm volatile("" : "+a"(a[8 % NR][t]), "+a"(a[9 % NR][t])); mx = max(mx, max(umax4(a[8 % NR][t]), umax4(a[9 % NR][t]))); }
    if (!__any(mx == 0xffffffffu) || give_up(p, tries, 24u)) break;
    vb = voff;
#pragma unroll
    for (int t = 0; t < NT; ++t) { acc[t][0] = f32x4{0.f, 0.f, 0.f, 0.f}; acc[t][1] = acc[t][0]; asm volatile("" : "+v"(acc[t][0]), "+v"(acc[t][1])); }
    }
    TST(4096 + (s * 36 + ct0) * 4 + w, 2);
    f32x4* red = (f32x4*)smem;
#pragma unroll
    for (int t = 0; t < NT; ++t) { red[((w * NT + t) * 2 + 0) * 64 + lane] = acc[t][0]; red[((w * NT + t) * 2 + 1) * 64 + lane] = acc[t][1]; }
    __syncthreads();
    u16* orow = p.out + (size_t)(32 * s + 16 * (w & 1) + c) * OUTW + 16 * ct0 + 4 * g;
#pragma unroll
    for (int t = 0; t < NT; ++t)
        if ((t & 1) == (w >> 1)) {
            const int m = w & 1;
            const f32x4 v = (red[((0 * NT + t) * 2 + m) * 64 + lane] + red[((1 * NT + t) * 2 + m) * 64 + lane]) +
                            (red[((2 * NT + t) * 2 + m) * 64 + lane] + red[((3 * NT + t) * 2 + m) * 64 + lane]);
            *(u32x2*)(orow + 16 * t) = u32x2{pk2bf(v[0], v[1]), pk2bf(v[2], v[3])};
        }
}

static constexpr int KROW = G_KROW;
static constexpr int KARR = 80 * KROW;
static constexpr int UCS = 2208;
struct BCh {
    u16 uc[4][UCS];
    f32x4 cst[NST];
    u16 Kh[KARR + 256];
    u16 ub[SL];
};
static_assert(4 * sizeof(BCh) <= LDS_TOTAL - 32, "LDS");
__device__ __forceinline__ unsigned wave_umax(unsigned m) {
#pragma unroll
    for (int o = 32; o; o >>= 1) m = max(m, (unsigned)__shfl_xor((int)m, o));
    return m;
}
__device__ __forceinline__ unsigned scale_exp(unsigned absbits) { return min(max(absbits >> 23, 15u), 254u); }
__device__ __forceinline__ unsigned pk2h(float a, float b) { unsigned r; asm("v_cvt_pk_f16_f32 %0, %1, %2" : "=v"(r) : "v"(a), "v"(b)); return r; }
__device__ __forceinline__ unsigned prm(unsigned hi, unsigned lo, unsigned sel) { return __builtin_amdgcn_perm(hi, lo, sel); }
#define BST(i)
#define BST2(i)
__device__ __forceinline__ void task_ut(const MainP& p, char* smem, int rb, int w, int lane) {
    const int tid = w * 64 + lane, g = lane >> 4, c = lane & 15;
    const u32x4* src = (const u32x4*)(p.U + (size_t)rb * 64 * DM);
    u32x4 v[18];
#pragma unroll
    for (int i = 0; i < 18; ++i) {
        v[i] = src[tid + 256 * i];
    }
    BST2(0);
#pragma unroll
    for (int i = 0; i < 18; ++i) { const int ch = tid + 256 * i, row = ch / 72; *(u32x4*)(smem + row * 1184 + (ch - 72 * row) * 16) = v[i]; }
    __syncthreads();
    const unsigned ad = (unsigned)(uintptr_t)smem + (16 * g + (c >> 2)) * 1184 + (w * 144 + 4 * (c & 3)) * 2;
#pragma unroll
    for (int i = 0; i < 9; ++i) {
        u32x2 x0, x1, x2, x3;
        asm volatile("ds_read_b64_tr_b16 %0, %4 offset:%5\n\tds_read_b64_tr_b16 %1, %4 offset:%6\n\tds_read_b64_tr_b16 %2, %4 offset:%7\n\tds_read_b64_tr_b16 %3, %4 offset:%8\n\ts_waitcnt lgkmcnt(0)"
                     : "=&v"(x0), "=&v"(x1), "=&v"(x2), "=&v"(x3) : "v"(ad), "i"(i * 32), "i"(4 * 1184 + i * 32), "i"(8 * 1184 + i * 32), "i"(12 * 1184 + i * 32) : "memory");
        v[2 * i] = u32x4{x0[0], x0[1], x1[0], x1[1]}; v[2 * i + 1] = u32x4{x2[0], x2[1], x3[0], x3[1]};
    }
    __syncthreads();
    BST2(1);
    char* tw = smem + w * (144 * 144);
#pragma unroll
    for (int i = 0; i < 9; ++i) { *(u32x4*)(tw + (16 * i + c) * 144 + g * 32) = v[2 * i]; *(u32x4*)(tw + (16 * i + c) * 144 + g * 32 + 16) = v[2 * i + 1]; }
    const char* dst = uni(p.ws + G_D_UT + (size_t)rb * 73728 + w * 18432);
#pragma unroll
    for (int i = 0; i < 18; ++i) {
        const u32x4 o = *(const u32x4*)(tw + (8 * i + (lane >> 3)) * 144 + (lane & 7) * 16);
        const unsigned vi = lane * 16 + i * 1024;
        WST16(vi, o, dst, 0);
    }
    BST2(2);
    const u32x4 sent = {0xffffffffu, 0xffffffffu, 0xffffffffu, 0xffffffffu};
#pragma unroll 1
    for (int sg = NG1; sg < 64; ++sg) {
        const unsigned ov = umx_base(rb) + sg * 9216 + tid * 16, ok = G_D_KM + rb * G_HS4 + skew(sg, rb) * 4096 + tid * 16;
        const unsigned op = tid < 128 ? G_D_PX + (sg * 32 + rb) * 2048 + tid * 16
                          : tid < 192 ? ov + 6144 : G_D_PM + sg * 16384 + ((tid >> 4) & 1) * 8192 + rb * 256 + (tid & 15) * 16;
        asm volatile("global_store_dwordx4 %0, %4, %5 sc1\n\tglobal_store_dwordx4 %1, %4, %5 sc1\n\tglobal_store_dwordx4 %2, %4, %5 sc1\n\t"
                     "global_store_dwordx4 %3, %4, %5 sc1\n\ts_nop 1" :: "v"(ov), "v"(ov + 4096), "v"(ok), "v"(op), "v"(sent), "s"(p.ws) : "memory");
    }
    BST2(3);
    VMC0();
    BST2(4);
    __syncthreads();
    if (tid == 0) __hip_atomic_store((gu32*)(p.ws + CTLO + G_C_UT + rb * 128), 1u, RLX_AGENT);
}
struct KPar { u16 ldt, pa[2][4]; };
__device__ __forceinline__ KPar kgen_par(const MainP& p, int d, int lane) {
    KPar k;
    k.ldt = p.logdt[d];
#pragma unroll
    for (int q = 0; q < 2; ++q) { const int i = d * NST + lane + 64 * q; k.pa[q][0] = p.logA[i]; k.pa[q][1] = p.Aim[i]; k.pa[q][2] = p.Cre[i]; k.pa[q][3] = p.Cim[i]; }
    return k;
}
struct Pull { const char* a; const char* b; const char* c; unsigned at; };
__device__ __forceinline__ void pull_inputs(Pull& q, int w, bool now) {
    if (q.a && (now || (int)((unsigned)__builtin_amdgcn_s_memrealtime() - q.at) >= 0)) {
#pragma unroll
        for (int i = 0; i < 3; ++i)
            if (i < 2 || w == 0) asm volatile("s_mov_b32 m0, %3\n\ts_nop 0\n\tglobal_load_lds_dword %0, %1\n\tglobal_load_lds_dword %0, %2" :: "v"(threadIdx.x * 128 + i * 32768), "s"(q.a), "s"(q.b), "s"(155648) : "memory");
        asm volatile("global_load_lds_dword %0, %1" :: "v"(min(threadIdx.x, 143u) * 128), "s"(q.c) : "memory");
        q.a = nullptr;
    }
}
__device__ __forceinline__ void task_kgen(const MainP& p, char* smem, int d, int w, int lane, const KPar& kp, bool first, unsigned ep, Pull& pl) {
    BCh& L = reinterpret_cast<BCh*>(smem)[w];
    const int g = lane >> 4, c = lane & 15, d0 = d;
    const int tid = (d & 3) * 64 + lane;
    unsigned* Kd = (unsigned*)L.uc;
    unsigned ek;
    BST(10);
    const auto& pa = kp.pa;
    const float dt = bfr(expf(bf2f(kp.ldt)));
#pragma unroll
    for (int q = 0; q < 2; ++q) {
        const float ar = -bfr(expf(bf2f(pa[q][0])));
        const float cr = bf2f(pa[q][2]) * dt, ci = bf2f(pa[q][3]) * dt;
        L.cst[lane + 64 * q] = f32x4{ar * dt * 1.4426950408889634f, bf2f(pa[q][1]) * dt * 0.15915494309189535f,
                                     __log2f(hypotf(cr, ci)), atan2f(ci, cr) * 0.15915494309189535f};
    }
    BST(11);
    {
        f32x4 acc[3];
#pragma unroll
        for (int v = 0; v < 3; ++v) acc[v] = f32x4{0.f, 0.f, 0.f, 0.f};
        auto base_of = [](int v, int r) { return v == 0 ? (r & 7) + 128 * (r >> 3) : v == 1 ? (r < 8 ? 256 + 2 * (r & 3) + 128 * (r >> 2) : 512 + 4 * (r & 1) + 128 * ((r - 8) >> 1))
                                                                                           : 1024 + 128 * min(r, 8); };
        const float b0 = (float)base_of(0, c), b1 = (float)base_of(1, c), b2 = (float)base_of(2, c), off = (float)(8 * c);
#define KG_POLAR(h_, l_, lg_, f_, a_, b_) do { const float m_ = __builtin_amdgcn_exp2f(fmaf(cc[0], b_, lg_)), p_ = __builtin_amdgcn_fractf(fmaf(a_, b_, f_)); \
            const float re_ = m_ * __builtin_amdgcn_cosf(p_), im_ = m_ * __builtin_amdgcn_sinf(p_);                                                            \
            h_ = pk2bf(re_, im_); l_ = pk2bf(re_ - lo2f(h_), im_ - hi2f(h_)); } while (0)
#pragma unroll 1
        for (int t = 0; t < NST / 16; ++t) {
            u32x4 fh, fl, ah[3], al[3];
            pull_inputs(pl, w, false);
#pragma unroll
            for (int j = 0; j < 4; ++j) {
                const f32x4 cc = L.cst[16 * t + 4 * g + j];
                KG_POLAR(fh[j], fl[j], 0.f, 0.f, -cc[1], off);
                KG_POLAR(ah[0][j], al[0][j], cc[2], cc[3], cc[1], b0); KG_POLAR(ah[1][j], al[1][j], cc[2], cc[3], cc[1], b1); KG_POLAR(ah[2][j], al[2][j], cc[2], cc[3], cc[1], b2);
            }
#pragma unroll
            for (int v = 0; v < 3; ++v) { acc[v] = MFMA32(al[v], fh, acc[v]); acc[v] = MFMA32(ah[v], fl, acc[v]); acc[v] = MFMA32(ah[v], fh, acc[v]); }
        }
        BST(12);
        unsigned mx = 0;
#pragma unroll
        for (int v = 0; v < 3; ++v)
#pragma unroll
            for (int i = 0; i < 4; ++i)
                if (base_of(v, 4 * g + i) + 8 * c <= 2048) mx = max(mx, __float_as_uint(acc[v][i]) & 0x7fffffffu);
        ek = scale_exp(wave_umax(mx));
        const float sk = __uint_as_float((268u - ek) << 23);
#pragma unroll
        for (int v = 0; v < 3; ++v)
#pragma unroll
            for (int i = 0; i < 4; ++i) {
                const int pos = base_of(v, 4 * g + i) + 8 * c;
                if (pos <= 2048) Kd[pos] = pk2h(acc[v][i] * sk, 0.f);
            }
    }
    char* kt = p.ws + G_D_KT + (size_t)d * 8192;
    char* kt2 = kt + 4096;
    const unsigned voff = lane * 16;
#pragma unroll
    for (int q = 0; q < 4; ++q) {
        const int e = lane + 64 * q;
        unsigned t[8], k[8];
        const unsigned k0 = Kd[8 * e], k8 = Kd[8 * e + 8];
        if (q == 0) {
            const u32x4 a = *(const u32x4*)&Kd[8 * e], b = *(const u32x4*)&Kd[8 * e + 4];
            const bool two = lane >= 32;
            t[0] = a[0]; t[1] = two ? a[0] : a[1]; t[2] = a[2]; t[3] = two ? b[0] : a[3]; t[4] = b[0]; t[5] = two ? b[0] : b[1]; t[6] = b[2]; t[7] = two ? k8 : b[3];
        } else if (q == 1) {
            const unsigned k4 = Kd[8 * e + 4];
            t[0] = t[1] = t[2] = k0; t[3] = t[4] = t[5] = k4; t[6] = t[7] = k8;
        } else {
            t[0] = t[1] = t[2] = t[3] = k0; t[4] = (lane & 1) ? k8 : k0; t[5] = t[6] = t[7] = k8;
        }
#pragma unroll
        for (int j = 0; j < 8; ++j) k[j] = t[7 - j];
        u32x4 kh;
#pragma unroll
        for (int j = 0; j < 4; ++j) kh[j] = prm(k[2 * j + 1], k[2 * j], 0x05040100u);
        WST16(voff, kh, kt, q * 1024);
    }
    if (lane < 8) { const u32x4 e4 = {ek, ek, ek, ek}; WST16(voff, e4, kt2, 0); }
    BST(13);
    VMC0();
    BST(14);
    if (first) init_seen(p.ws, lane, ep);
    BST(15);
    if (lane == 0) __hip_atomic_fetch_add((gu32*)(p.ws + G_WS_CTL + G_C_KG + (d >> 2) * 128), 1u, RLX_AGENT);
}
__device__ __forceinline__ void task_s4d(const MainP& p, char* smem, int d0, int w, int lane, int& nb, bool more) {
    BCh* LL = reinterpret_cast<BCh*>(smem);
    BCh& L = LL[w];
    const int tid = w * 64 + lane, g = lane >> 4, c = lane & 15, d = d0 + w;
    BST(0);
    wait_word(p, p.ws + (lane < 32 ? CTLO + G_C_UT + lane * 128 : G_WS_CTL + G_C_KG + (d0 >> 2) * 128), lane < 32 ? 1 : 3, 30u);
    const float dsk = bf2f(p.Dskip[d]);
    const char* kt = p.ws + G_D_KT + (size_t)d * 8192;
    const char* kt2 = kt + 4096;
    const char* ut = p.ws + G_D_UT + d * 128;
    const unsigned voff = lane * 16;
    u32x4 kh[4], uu[4];
    unsigned ek;
#pragma unroll
    for (int q = 0; q < 4; ++q) { const unsigned vu = (8 * q + (lane >> 3)) * 73728 + (lane & 7) * 16; WLD16(uu[q], vu, ut, 0); }
    WLD16X4(kh[0], kh[1], kh[2], kh[3], voff, kt);
    asm volatile("global_load_dword %0, %1, %2 sc1" : "=v"(ek) : "v"(0u), "s"(kt2) : "memory");
    const u32x4 z4 = {0u, 0u, 0u, 0u};
    *(u32x4*)&L.Kh[8 * lane] = z4;
    *(u32x4*)&L.Kh[512 + 8 * (lane & 31)] = z4;
    *(u32x4*)&L.uc[0][64 + SL] = z4;
    *(u32x4*)&L.uc[(lane >> 3) & 3][8 * (lane & 7)] = z4;
    asm volatile("s_waitcnt vmcnt(0)" : "+v"(kh[0]), "+v"(kh[1]), "+v"(kh[2]), "+v"(kh[3]), "+v"(ek),
                 "+v"(uu[0]), "+v"(uu[1]), "+v"(uu[2]), "+v"(uu[3]) :: "memory");
    BST(1);
    unsigned mu = 0;
#pragma unroll
    for (int q = 0; q < 4; ++q)
#pragma unroll
        for (int j = 0; j < 4; ++j) mu = max(mu, max(uu[q][j] & 0x7fff0000u, (uu[q][j] << 16) & 0x7fff0000u));
    const unsigned eu = scale_exp(wave_umax(mu));
    const float su = __uint_as_float((268u - eu) << 23), iu = __uint_as_float((eu - 14u) << 23), ik = __uint_as_float((ek - 13u) << 23);
#pragma unroll
    for (int q = 0; q < 4; ++q) {
        const int e = lane + 64 * q, at = (16 + (e >> 2)) * KROW + 24 - 8 * (e & 3);
        u32x4 h;
#pragma unroll
        for (int j = 0; j < 4; ++j) h[j] = pk2h(lo2f(uu[q][j]) * su, hi2f(uu[q][j]) * su);
        *(u32x4*)&L.uc[0][64 + 8 * e] = h;
        *(u32x4*)&L.ub[8 * e] = uu[q];
        *(u32x4*)&L.Kh[at] = kh[q];
    }
    BST(2);
    BST(3);
#pragma unroll
    for (int q = 0; q < 4; ++q) {
        const int e = lane + 64 * q;
        const int ue = 56 + 8 * e;
        const u32x4 a = *(const u32x4*)&L.uc[0][ue];
        const u32x2 b = *(const u32x2*)&L.uc[0][ue + 8];
        const unsigned s0 = __builtin_amdgcn_alignbit(a[1], a[0], 16), s1 = __builtin_amdgcn_alignbit(a[2], a[1], 16), s2 = __builtin_amdgcn_alignbit(a[3], a[2], 16),
                       s3 = __builtin_amdgcn_alignbit(b[0], a[3], 16), s4 = __builtin_amdgcn_alignbit(b[1], b[0], 16);
        *(u32x4*)&L.uc[1][ue] = u32x4{s0, s1, s2, s3};
        *(u32x4*)&L.uc[2][ue] = u32x4{a[1], a[2], a[3], b[0]};
        *(u32x4*)&L.uc[3][ue] = u32x4{s1, s2, s3, s4};
    }
    if (lane == 0) {
        const u32x4 a = *(const u32x4*)&L.uc[0][56 + SL];
        const unsigned s0 = __builtin_amdgcn_alignbit(a[1], a[0], 16), s1 = __builtin_amdgcn_alignbit(a[2], a[1], 16), s2 = __builtin_amdgcn_alignbit(a[3], a[2], 16), s3 = a[3] >> 16;
        *(u32x4*)&L.uc[1][56 + SL] = u32x4{s0, s1, s2, s3};
        *(u32x4*)&L.uc[2][56 + SL] = u32x4{a[1], a[2], a[3], 0u};
        *(u32x4*)&L.uc[3][56 + SL] = u32x4{s1, s2, s3, 0u};
    }
    BST(4);
    {
        const int o = 33 + c + 8 * g;
        const unsigned ua = (unsigned)(uintptr_t)&L.uc[o & 3][o & ~3], ka = (unsigned)(uintptr_t)&L.Kh[(16 + c) * KROW + 8 * g];
        const f32x4* res = (const f32x4*)L.Kh + lane;
        asm volatile(ASM_CV :: "v"(ua), "v"(ka), "v"((unsigned)(uintptr_t)res) : CV_CLOB);
        BST(5);
#pragma unroll
        for (int par = 0; par < 2; ++par)
#pragma unroll
            for (int t = 0; t < 4; ++t) {
                const int n = 16 * (par + 2 * (16 * t + c)) + 4 * g;
                const u32x2 uu = *(const u32x2*)&L.ub[n];
                const f32x4 y4 = res[(par * 4 + t) * 64] * fminf(ik, iu) * fmaxf(ik, iu);
                const float r0 = bfr(y4[0]) + bfr(lo2f(uu[0]) * dsk), r1 = bfr(y4[1]) + bfr(hi2f(uu[0]) * dsk);
                const float r2 = bfr(y4[2]) + bfr(lo2f(uu[1]) * dsk), r3 = bfr(y4[3]) + bfr(hi2f(uu[1]) * dsk);
                *(u32x2*)&L.uc[1][n] = u32x2{pk2bf(r0, r1), pk2bf(r2, r3)};
            }
    }
    __syncthreads();
    BST(6);
    u32x4 y[4];
#pragma unroll
    for (int ch = 0; ch < 4; ++ch) y[ch] = *(const u32x4*)&LL[ch].uc[1][8 * tid];
    const unsigned vt = tid * 16;
    char* yb = p.ws + G_D_YB + (size_t)(d0 >> 2) * 16384;
#pragma unroll
    for (int i = 0; i < 4; ++i) {
        const u32x4 o = {prm(y[1][i], y[0][i], 0x05040100u), prm(y[3][i], y[2][i], 0x05040100u), prm(y[1][i], y[0][i], 0x07060302u), prm(y[3][i], y[2][i], 0x07060302u)};
        char* yi = yb + i * 4096;
        WST16(vt, o, yi, 0);
    }
    BST(7);
    if (tid == 0 && more) nb = (int)(__hip_atomic_fetch_add((gu32*)(p.ws + CTLO + G_C_TASK + 128), 1u, RLX_AGENT) + 1u);
    VMC0();
    __syncthreads();
    if (tid == 0) __hip_atomic_fetch_add((gu32*)(p.ws + CTLO + G_C_YC + (d0 >> 6) * 128), 1u, RLX_AGENT);
    BST(8);
}
__device__ __forceinline__ void task_merge(const MainP& p, char* smem, int d0, int i, int w, int lane) {
    const unsigned vt = (w * 64 + lane) * 16;
    wait_word(p, p.ws + CTLO + G_C_YC + (d0 >> 6) * 128, 15, 31u);
    {
        const char* sb = p.ws + G_D_YB + (size_t)(d0 >> 6) * 262144 + i * 4096;
        u32x4 z[16];
#pragma unroll
        for (int j = 0; j < 16; ++j) { const char* sj = sb + j * 16384; WLD16(z[j], vt, sj, 0); }
        asm volatile("s_waitcnt vmcnt(0)" : "+v"(z[0]), "+v"(z[1]), "+v"(z[2]), "+v"(z[3]), "+v"(z[4]), "+v"(z[5]), "+v"(z[6]), "+v"(z[7]),
                     "+v"(z[8]), "+v"(z[9]), "+v"(z[10]), "+v"(z[11]), "+v"(z[12]), "+v"(z[13]), "+v"(z[14]), "+v"(z[15]) :: "memory");
        char* tw = smem + w * 18432;
#pragma unroll
        for (int j = 0; j < 8; ++j) {
            *(u32x4*)(tw + lane * 144 + j * 16) = u32x4{z[2 * j][0], z[2 * j][1], z[2 * j + 1][0], z[2 * j + 1][1]};
            *(u32x4*)(tw + (64 + lane) * 144 + j * 16) = u32x4{z[2 * j][2], z[2 * j][3], z[2 * j + 1][2], z[2 * j + 1][3]};
        }
#pragma unroll
        for (int k = 0; k < 16; ++k) {
            const int q = 8 * k + (lane >> 3);
            const int row = 8 * (64 * w + (q & 63)) + 2 * i + (q >> 6);
            *(u32x4*)(p.out + (size_t)row * OUTW + DM + (d0 & ~63) + 8 * (lane & 7)) = *(const u32x4*)(tw + q * 144 + (lane & 7) * 16);
        }
    }
}

static constexpr int NKG = 144, E_B = NKG + 36;
static constexpr int Q1 = 0, Q2 = Q1 + (NFINE - 2) * 25;
static constexpr int NTASK = E_B + Q2 + 68;

__device__ __forceinline__ void worker_body(const MainP& p, char* smem, const int w, const int lane, int quota) {
    const int tid = w * 64 + lane;
    int* slot = (int*)(smem + LDS_TOTAL - 16);
    int nb = -1;
    int nst = 0;
    gu32* qa = (gu32*)(p.ws + CTLO + G_C_TASK);
    for (;;) {
        if (tid == 0) {
            int t = E_B;
            if (quota > 0) {
                const int k = nb >= 0 ? nb : (int)(__hip_atomic_fetch_add(qa + 32, 1u, RLX_AGENT) + 1u);
                if (k < E_B) t = k;
                nb = -1;
            }
            if (t >= E_B) t = E_B + (int)(__hip_atomic_fetch_add(qa, 1u, RLX_AGENT) + 1u);
            *slot = t;
        }
        __syncthreads();
        const int t = __builtin_amdgcn_readfirstlane(*slot);
        if (t >= NTASK) break;
        quota = t < E_B ? quota - 1 : 0;
        if (t < E_B) {
            if (t < 128 || (t >= 144 && t < 160)) task_s4d(p, smem, 4 * (t < 128 ? t : t - 16), w, lane, nb, quota > 0);
            else task_merge(p, smem, 64 * ((t < 144 ? t - 128 : t - 144) >> 2), t & 3, w, lane);
        } else {
            const int u = t - E_B;
            const bool last2 = u >= Q2;
            const int sg = last2 ? 62 + (u - Q2) / 34 : NG1 + (u - Q1) / 25, r = last2 ? (u - Q2) % 34 : (u - Q1) % 25;
            if (r < 16) task_attn_tail(p, sg, 2 * r + (w >> 1), w, lane);
            else if (last2) task_out<2, 9, true>(p, smem, sg, 2 * (r - 16), w, lane);
            else task_out<4, 9, true>(p, smem, sg, 4 * (r - 16), w, lane);
            if (last2) break;
        }
        __syncthreads();
    }
}

__host__ __device__ constexpr unsigned phys(unsigned x) { return (x >> 13) * 16384 + (x & 8191); }
__host__ __device__ constexpr unsigned aMB(int f, int b) { return phys(f < 8 ? 16384 + f * 1024 + b : 28672 + b); }
__host__ __device__ constexpr unsigned aPX(int pr, int b) { return phys(pr * 1024 + b); }
__host__ __device__ constexpr unsigned aPM(int u, int b) { return phys(24576 + u * 1024 + b); }
#define IST(k)

static constexpr int L_RW = 160000;
__device__ __forceinline__ int lane_id() { int l; asm volatile("v_mbcnt_lo_u32_b32 %0, -1, 0\n\tv_mbcnt_hi_u32_b32 %0, -1, %0" : "=v"(l)); return l; }
typedef const __attribute__((address_space(4))) MainP* KArg;

__global__ __launch_bounds__(256, 1) void main_kernel(MainP p_) {
    __shared__ __attribute__((aligned(16))) char smem[LDS_TOTAL];
    KArg ka = (KArg)__builtin_amdgcn_kernarg_segment_ptr();
    static_assert(offsetof(MainP, W) == 0 && offsetof(MainP, ws) == 0x20 && offsetof(MainP, X) == 0x28 && offsetof(MainP, prm) == 0x40, "argument layout");
    asm volatile(ASM_EA :: "s"(ka), "s"(blockIdx.x), "v"(threadIdx.x) : ASM_CLOB);
    asm volatile("" : "+s"(ka));
    const unsigned t00 = (unsigned)__builtin_amdgcn_s_memrealtime();
    const int b = blockIdx.x, x = b & 7, ix = b >> 3;
    const int w = __builtin_amdgcn_readfirstlane(threadIdx.x >> 6);
    const bool isr = x < 4 && ix >= 16;
    if (ix == (x < 4 ? 24 : 31) && (FLG(ka->prm[5]) & 8)) {
        const unsigned t = threadIdx.x;
        const char* pg = t < (G_D_END >> 21) ? ka->ws + (size_t)t * 0x200000 + 0x1FFF80 : t < 128 ? ka->ws : (const char*)(t < 192 ? ka->out : t < 224 ? ka->X : ka->U) + min((t & 31) * 0x100000u, t < 192 ? 4718588u : 2359292u);
        asm volatile("s_mov_b32 m0, %1\n\ts_nop 0\n\tglobal_load_lds_dword %0, off" :: "v"(pg), "s"(155648) : "memory");
    }
    if (x < 4 && ix >= 29) init_cal(ka->ws, smem, x * 3 + ix - 29, threadIdx.x, (FLG(ka->prm[5]) >> 16) & 7);
    if (x >= 4 ? ix >= 24 : (ix >= 25 && ix < 29)) init_fill(ka->ws, ka->mem0, x >= 4 ? (ix - 24) * 4 + (x & 3) : 32 + (ix - 25) * 4 + x, threadIdx.x);
    IST(0);
    const int NS = (FLG(ka->prm[5]) >> 8) & 3, NF = (FLG(ka->prm[5]) >> 21) & 7, RK = (FLG(ka->prm[5]) >> 10) & 3;
    const int NSP = G_RARM ? 7 : FLG2(ka->prm[7]) & 7;
    const int NA1 = 18 + 4 * NSP, NB1 = NA1 - 3 * max(NSP - 4, 0), N1 = RK ? NA1 + (RK - 1) * NB1 : 0, N2 = N1 + (28 - 4 * NSP) * NS, NKE = min(N2 + max(24 * NF - 8, 0), NKG);
    if (ix >= (x < 4 ? 16 : 24)) {
        MainP p;
        p.ws = ka->ws; p.logA = ka->logA; p.Aim = ka->Aim; p.Cre = ka->Cre; p.Cim = ka->Cim; p.logdt = ka->logdt;
        Pull pl = {nullptr, nullptr, nullptr, t00 + 400 + 50 * ((FLG(ka->prm[5]) >> 4) & 15)};
        const bool own = x < 4 && ix < 25 + NSP;
        int d0, nr, dd;
        if (own) {
            const int jt = ix < 25 ? ix - 16 : ix - 25, solo = jt < NSP;
            const int k = ix >= 25 ? 18 + NSP + 3 * jt + w - 1 : solo ? 3 * jt + w - 1 : NSP + 2 * jt + w - 2;
            d0 = k < NB1 ? x * NB1 + k : 4 * NB1 * RK + x * (NA1 - NB1) + k - NB1; nr = w < 2 - solo ? 0 : k < NB1 ? RK : min(RK, 1); dd = 4 * NB1;
            if (threadIdx.x < 8) ((unsigned*)(smem + L_RW))[threadIdx.x] = 0xffffffffu;
        } else if (x < 4) { d0 = 4 * (N1 + (ix - 25 - NSP) * 4 + x) + w; nr = NS; dd = 4 * (28 - 4 * NSP); }
        else {
            d0 = 4 * (N2 + (ix - 24) * 4 + (x & 3)) + w; nr = ix < 28 ? NF : ix < 30 ? max(NF - 1, 0) : 0; dd = 96;
            const size_t o = (size_t)((ix - 24) * 4 + (x & 3)) * 73728; pl.a = (const char*)ka->X + o; pl.b = (const char*)ka->U + o;
            pl.c = (const char*)((x & 2) ? ((x & 1) ? ka->Cim : ka->Cre) : ((x & 1) ? ka->Aim : ka->logA)) + (ix - 24) * 18432;
        }
        while (nr > 0 && d0 + dd * (nr - 1) >= 4 * NKG) --nr;
        KPar kn;
        if (nr > 0) kn = kgen_par(p, d0, threadIdx.x & 63);
        __builtin_amdgcn_sched_barrier(0);
        const unsigned ep = __hip_atomic_load((const gu32*)(p.ws + G_WS_EP), RLX_AGENT);
        asm volatile("s_waitcnt vmcnt(0)" :: "v"(ep) : "memory");
        __syncthreads();
        if (x >= 4 && ix == 24) {
            const char* xs = (const char*)ka->X + threadIdx.x * 128;
#pragma unroll
            for (int i = 0; i < 7; ++i) asm volatile("s_mov_b32 m0, %1\n\ts_nop 0\n\tglobal_load_lds_dword %0, off" :: "v"(xs + i * 32768), "s"(155648) : "memory");
        }
        {
            const int q = x < 4 ? ix - 25 : ix - 26;
            if (q >= 0 && q < 4 && (FLG(ka->prm[5]) & 1)) {
                const int hs = (x & 3) * 8 + 4 * (q & 1) + ((1 - (int)((u64)ka->W[q & 2] >> 8)) & 3);
                const char* src = (q & 2) ? (const char*)ka->W[3] + (size_t)hs * 147456 + threadIdx.x * 128 : (const char*)ka->W[x < 4] + hs * 256 + (threadIdx.x >> 1) * 8192 + (threadIdx.x & 1) * 128;
                const unsigned st = (q & 2) ? 32768u : 1048576u;
#pragma unroll
                for (int i = 0; i < 5; ++i)
                    if (i < 4 || w < 2) {
                        if (FLG(ka->prm[5]) & 4) asm volatile("s_mov_b32 m0, %1\n\ts_nop 0\n\tglobal_load_lds_dword %0, off nt" :: "v"(src + (size_t)i * st), "s"(155648) : "memory");
                        else asm volatile("s_mov_b32 m0, %1\n\ts_nop 0\n\tglobal_load_lds_dword %0, off" :: "v"(src + (size_t)i * st), "s"(155648) : "memory");
                    }
            }
        }
        if (nr == 0) init_seen(p.ws, threadIdx.x & 63, ep);
#pragma unroll 1
        for (int r = 0; r < nr; ++r) {
            const KPar kp = kn;
            kn = kgen_par(p, d0 + dd * min(r + 1, nr - 1), threadIdx.x & 63);
            __builtin_amdgcn_sched_barrier(0);
            task_kgen(p, smem, d0 + dd * r, w, threadIdx.x & 63, kp, r == 0, ep, pl);
        }
        if (w == 0) init_count(p.ws, threadIdx.x & 63);
        while (pl.a) { pull_inputs(pl, w, false); __builtin_amdgcn_s_sleep(8); }
        if (x < 4 && !own) return;
        asm volatile("" : "+s"(ka));
    }
    if (isr) {
        const int lane = threadIdx.x & 63, g = lane >> 4, c = lane & 15;
        const int jt = ix < 25 ? ix - 16 : ix - 25, solo = jt < NSP, par = solo ? ix >= 25 : w;
        const unsigned lb = w * G_STG;
        unsigned* ct = (unsigned*)(smem + lb) + lane;
        asm volatile("" :: "v"(ct) : "memory");
#define CTW(k, val) ct[(k) * 64] = (unsigned)(val)
        const u64 ws = (u64)ka->ws, prm = (u64)ka + 0x40;
        const u64 stb = 0;
        IST(1);
        CTW(0, lane * 16);
        const int j = x * 9 + jt;
        const int rot = (jt * 16) / 9;
        if (w >= 2 - solo) return;
        CTW(1, lane * 16 + 4096);
        CTW(2, aMB(j >> 2, (j & 3) * 256 + (4 * g + 2 * ((c >> 1) & 1) + (c & 1)) * 16 + (c >> 2) * 4));
        for (int k = 0; k < (int)G_NARM; ++k) {
            constexpr int N0 = G_HKB, N1 = N0 + (9 * G_RM + 3) / 4, N2 = N1 + 5 * G_RP;
            constexpr int NO = G_RARM ? 7 : 9;
            static_assert(N2 <= NO * G_NARM && G_NARM <= 5, "shares");
            const int q = min(jt + NO * k, N2 - 1), qm = min(x * (N1 - N0) + q - N0, (int)(9 * G_RM) - 1), qp = x * 5 * G_RP + q - N1;
            const int cp = q < N0 ? 4 + x : q < N1 ? qm / 9 : qp / 20, kb = q < N0 ? q : q < N1 ? (qm % 9 == 8 ? 28 : 16 + qm % 9) : (qp % 20 < 16 ? qp % 20 : 8 + qp % 20);
            CTW(47 + k, cp * G_CPY + phys(kb * 1024 + lane * 16));
        }
        for (int k = 0; k < 3; ++k) { const int q = min(jt + 7 * k, 15); CTW(52 + k, (x * 9 + 7 + (q >> 3)) * 16384 + (q & 7) * 1024 + lane * 16); }
        for (int k = 0; k < 2; ++k) { const int L = min(jt * 74 + min(k * 64 + lane, 73), 511), tl = x * 9 + 7 + (L >> 8); CTW(55 + k, G_D_UX + tl * G_UXS + skew(0, tl) * 32768 + (L & 255) * 128); }
        const unsigned pc = x * G_RP / 4 * G_CPY;
        CTW(3, (lane & 3) * G_CPY + G_HOFF + G_RHO + ((lane >> 2) & 1) * G_RHS + j * 32); CTW(46, x * G_CPY + G_HOFF + G_RHO + min(lane, 35) * 32);
        CTW(4, (c & 2) ? 0xEEEE : 0x4444); CTW(5, (c & 1) ? 0x03020706u : 0x05040100u);
        CTW(6, G_GVC ? G_WS_CTL + G_C_GV + G_GVH + x * 2048 + (par * 32 + (lane & 31)) * 32 : G_WS_CTL + G_C_GV + (par * 32 + (lane & 31)) * 128); CTW(7, x * G_CPY + G_HOFF + (lane & 31) * G_HP); CTW(8, lane * 4); CTW(9, L_RW);
        for (int k = 0; k < 16; ++k) { const int i = (k + rot) & 15; CTW(10 + k, pc + aPX(i, lane * 16)); CTW(26 + k, i * 2048 + lane * 16); }
        for (int k = 0; k < 4; ++k) CTW(42 + k, pc + aPM(k, lane * 16));
        IST(2);
        asm volatile(ASM_R :: "s"(ws), "s"(ws), "s"((u64)(t00 << 4)), "s"(__builtin_amdgcn_readfirstlane(par)), "s"(__builtin_amdgcn_readfirstlane(j)), "s"((u64)__builtin_amdgcn_readfirstlane(2 * rot) + ((u64)__builtin_amdgcn_readfirstlane(solo) << 32)), "s"(prm), "s"(lb), "s"(stb) : ASM_CLOB);
    }
    asm volatile("" : "+s"(ka));
    MainP p;
    p.prm[5] = ka->prm[5];
    p.U = ka->U; p.ws = ka->ws; p.logA = ka->logA; p.Aim = ka->Aim; p.Cre = ka->Cre; p.Cim = ka->Cim; p.logdt = ka->logdt; p.Dskip = ka->Dskip; p.out = ka->out;
    {
        auto cal = [&](int k) { return __hip_atomic_load((const gu32*)(p.ws + G_WS_DONE + k * 64 + 4), RLX_AGENT); };
        p.prm[0] = __builtin_amdgcn_readfirstlane((min(cal(5), cal(11)) < min(cal(4), cal(10)) ? 8192 : 0) ^ G_CTF);
    }
    const bool s4 = ix < 22 - ((FLG(p.prm[5]) >> 12) & 15);
    if (s4 && ix >= 8 && ix < 24) for (int i = 0; i < (int)(((ix & 7) * 4 + (x & 3)) * ((FLG(p.prm[5]) >> 24) & 3)); ++i) __builtin_amdgcn_s_sleep(3);
    if (x >= 4 && ix >= 8 && ix < 16) {
#pragma unroll 1
        for (int q = NKE + (ix - 8) * 4 + (x & 3); q < NKG; q += 32) {
            Pull pl = {nullptr, nullptr, nullptr, 0u};
            task_kgen(p, smem, 4 * q + w, w, lane_id(), kgen_par(p, 4 * q + w, lane_id()), false, 0u, pl);
        }
    }
    if (ix >= 24 && ix < 30) {
#pragma unroll 1
        for (int rb = (ix - 24) * 4 + (x & 3); rb < 32; rb += 8) {
            __syncthreads();
            task_ut(p, smem, rb, w, lane_id());
            if (ix < 28) break;
        }
        __syncthreads();
        const int lane = lane_id(), g = lane >> 4, c = lane & 15, ct0 = 6 * (ix - 24), s0 = x & 3;
        const unsigned lb = w * G_STG;
        unsigned* ct = (unsigned*)(smem + lb) + lane;
        asm volatile("" :: "v"(ct) : "memory");
        for (int t = 0; t < 6; ++t) CTW(t, (ct0 + t) * G_UXS + skew(0, ct0 + t) * 32768 + w * 8192 + 4096 + lane * 16);
        CTW(6, w * 16384 + 4096 + lane * 16); CTW(7, w * 16384 + 12288 + lane * 16); CTW(8, w * 2048 + lane * 16); CTW(9, w * 2048 + 8192 + lane * 16);
        CTW(10, umx_base(w * 8 + g) + ct0 * 256 + c * 16); CTW(11, umx_base(w * 8 + 4 + g) + ct0 * 256 + c * 16);
        const unsigned fa = lane < 8 ? CTLO + G_C_UM + (w * 8 + lane) * 128 : CTLO + G_C_GV + ((s0 & 1) * 32 + w * 8 + (lane & 7)) * 128;
        CTW(12, lane < 16 ? fa : attn_flag(p, s0, w * 2 + (lane & 1))); CTW(13, lane < 16 ? fa : attn_flag(p, s0 + 60, w * 2 + (lane & 1)));
        CTW(14, w * 12288 + lane * 16); CTW(15, w * 1024 + lane * 16);
        CTW(16, (16 * (w & 1) + c) * (OUTW * 2) + (16 * ct0 + 16 * (w >> 1) + 4 * g) * 2); CTW(17, lane * 4);
        static_assert(NG1 == 58, "as in the generated block");
        const u64 ws = (u64)p.ws;
        const u64 stb = 0;
        asm volatile(ASM_O :: "s"(ws), "s"((u64)p.out), "s"(ws), "s"(w), "s"(s0), "s"(ws), "s"(ws), "s"(lb), "s"(stb) : ASM_CLOB);
        asm volatile("" : "+s"(ka));
        p.prm[5] = ka->prm[5];
        p.U = ka->U; p.ws = ka->ws; p.logA = ka->logA; p.Aim = ka->Aim; p.Cre = ka->Cre; p.Cim = ka->Cim; p.logdt = ka->logdt; p.Dskip = ka->Dskip; p.out = ka->out;
    }
    if ((ix & 22) == 22 && x >= 4) { attn_role(p, ix >= 30 ? 0 : NA + 1, (x & 3) * 8 + (ix & 1) * 4 + w, w, lane_id());
        if (b == 255) {
            wait_word(p, p.ws + G_WS_CTL + G_C_ARR, 255, 50u);
            if (w == 0 && lane_id() == 0) __hip_atomic_fetch_add((gu32*)(p.ws + G_WS_EP), 1u, RLX_AGENT);
        }
        __syncthreads();
    }
    worker_body(p, smem, w, lane_id(), s4 ? 1000 : 0);
}

extern "C" __global__ void code_end_marker() {}

static constexpr int g_prm[8] = {PRM_DEFAULT};

extern "C" void kernel_launch(void* const* d_in, const int* in_sizes, int n_in, void* d_out, int out_size,
                              void* d_ws, size_t ws_size, hipStream_t stream) {
    char* ws = (char*)(((uintptr_t)d_ws + 0x1FFFFF) & ~(uintptr_t)0x1FFFFF);
    MainP p;
    for (int i = 0; i < 8; ++i) p.prm[i] = g_prm[i];
    p.mem0 = (const u16*)d_in[1];
    for (int i = 0; i < 4; ++i) p.W[i] = (const u16*)d_in[2 + i];
    p.X = (const u16*)d_in[0]; p.U = (const u16*)d_in[7]; p.ws = ws;
    p.logA = (const u16*)d_in[8]; p.Aim = (const u16*)d_in[9]; p.Cre = (const u16*)d_in[10]; p.Cim = (const u16*)d_in[11];
    p.logdt = (const u16*)d_in[12]; p.Dskip = (const u16*)d_in[13]; p.out = (u16*)d_out;
    main_kernel<<<256, 256, 0, stream>>>(p);
}
```

```cpp
#include <hip/hip_runtime.h>
#include <hip/hip_bf16.h>
#include <stdint.h>
#include <stddef.h>

typedef __attribute__((ext_vector_type(8))) __bf16 bf16x8;
typedef __attribute__((ext_vector_type(4))) short s16x4;
typedef __attribute__((ext_vector_type(4))) float f32x4;
typedef __attribute__((ext_vector_type(4))) unsigned u32x4;
typedef __attribute__((ext_vector_type(2))) unsigned u32x2;
typedef __attribute__((ext_vector_type(2))) float f32x2;
typedef unsigned short u16;
typedef unsigned long long u64;

#define GAS __attribute__((address_space(1)))
typedef GAS unsigned gu32;
#define RLX_AGENT __ATOMIC_RELAXED, __HIP_MEMORY_SCOPE_AGENT

static constexpr int DM = 576, NH = 32, DH = 128, MM = 8, SEG = 32, NSEG = 64, SL = 2048;
static constexpr int HD = NH * DH;
static constexpr int OUTW = 2 * DM;
static constexpr int NST = 128;

__device__ __forceinline__ float bf2f(u16 h) { return __uint_as_float(((unsigned)h) << 16); }
typedef __attribute__((ext_vector_type(2))) __bf16 bf16x2_t;
__device__ __forceinline__ unsigned pk2bf(float a, float b) {
    bf16x2_t v; v[0] = (__bf16)a; v[1] = (__bf16)b;
    return __builtin_bit_cast(unsigned, v);
}
__device__ __forceinline__ float bfr(float f) { return __uint_as_float(pk2bf(f, f) << 16); }
__device__ __forceinline__ float lo2f(unsigned u) { return __uint_as_float(u << 16); }
__device__ __forceinline__ float hi2f(unsigned u) { return __uint_as_float(u & 0xffff0000u); }
__device__ __forceinline__ float rowmax4(float x) {
    auto a = __builtin_amdgcn_permlane16_swap(__float_as_uint(x), __float_as_uint(x), false, false);
    x = fmaxf(__uint_as_float(a[0]), __uint_as_float(a[1]));
    auto b = __builtin_amdgcn_permlane32_swap(__float_as_uint(x), __float_as_uint(x), false, false);
    return fmaxf(__uint_as_float(b[0]), __uint_as_float(b[1]));
}
__device__ __forceinline__ float rowsum4(float x) {
    auto a = __builtin_amdgcn_permlane16_swap(__float_as_uint(x), __float_as_uint(x), false, false);
    x = __uint_as_float(a[0]) + __uint_as_float(a[1]);
    auto b = __builtin_amdgcn_permlane32_swap(__float_as_uint(x), __float_as_uint(x), false, false);
    return __uint_as_float(b[0]) + __uint_as_float(b[1]);
}

struct MainP {
    const u16* W[4];
    char* ws; const u16* X; const u16* mem0; const u16* U;
    int prm[8];
    const u16* logA; const u16* Aim; const u16* Cre; const u16* Cim; const u16* logdt; const u16* Dskip;
    u16* out;
};
static constexpr int LDS_TOTAL = 163840;
static constexpr unsigned kWaitCap = 1u << 18;
#define VMC0() asm volatile("s_waitcnt vmcnt(0)" ::: "memory")
#define PRM_DEFAULT 1090, 600, 108, 659, 800, 1615005475, 15728816, 7
#define ASM_EA \
    "s_load_dwordx8 s[40:47], %0, 0\n\t" \
    "s_load_dwordx4 s[36:39], %0, 0x20\n\t" \
    "s_load_dwordx8 s[84:91], %0, 0x40\n\t" \
    "s_getpc_b64 s[54:55]\n\t" \
    "ea_pc_%=:\n\t" \
    "s_bfe_u32 s69, %1, 0x20003\n\t" \
    "s_lshl_b32 s69, s69, 15\n\t" \
    "v_lshl_add_u32 v1, %2, 7, s69\n\t" \
    "v_min_u32 v1, code_end_marker-ea_pc_%=, v1\n\t" \
    "s_mov_b32 m0, 155648\n\t" \
    "v_readfirstlane_b32 s66, %2\n\t" \
    "global_load_lds_dword v1, s[54:55]\n\t" \
    "s_bitcmp1_b32 %1, 2\n\t" \
    "s_cselect_b32 s69, 24, 16\n\t" \
    "s_lshr_b32 s70, %1, 3\n\t" \
    "s_cmp_ge_u32 s70, s69\n\t" \
    "s_cbranch_scc1 ea_done_%=\n\t" \
    "s_lshr_b32 s66, s66, 6\n\t" \
    "s_and_b32 s69, %1, 0xc4\n\t" \
    "s_cmp_eq_u32 s69, 4\n\t" \
    "s_cselect_b32 s69, 72, 36\n\t" \
    "s_add_u32 s82, s66, s69\n\t" \
    "s_lshl_b32 s82, s82, 10\n\t" \
    "s_mov_b32 m0, s82\n\t" \
    "s_and_b32 s67, %1, 3\n\t" \
    "s_lshl_b32 s67, s67, 3\n\t" \
    "s_and_b32 s69, s70, 7\n\t" \
    "s_add_u32 s67, s67, s69\n\t" \
    "s_lshl_b32 s69, s67, 8\n\t" \
    "v_lshrrev_b32 v1, 4, %2\n\t" \
    "v_lshrrev_b32 v2, 3, %2\n\t" \
    "v_and_b32 v2, 6, v2\n\t" \
    "v_and_or_b32 v2, v1, 8, v2\n\t" \
    "v_xor_b32 v2, v2, %2\n\t" \
    "v_and_b32 v2, 15, v2\n\t" \
    "v_lshl_add_u32 v2, v2, 4, s69\n\t" \
    "v_lshl_add_u32 v200, v1, 13, v2\n\t" \
    "s_mov_b32 s71, 16\n\t" \
    "s_bitcmp1_b32 %1, 2\n\t" \
    "s_waitcnt lgkmcnt(0)\n\t" \
    "s_cselect_b64 s[48:49], s[42:43], s[40:41]\n\t" \
    "s_cselect_b64 s[40:41], s[40:41], s[42:43]\n\t" \
    "s_cmp_ge_u32 s70, 8\n\t" \
    "s_cselect_b64 s[42:43], s[44:45], s[48:49]\n\t" \
    "s_cselect_b64 s[44:45], s[46:47], s[40:41]\n\t" \
    "ea_lp1_%=:\n\t" \
    "global_load_lds_dwordx4 v200, s[42:43]\n\t" \
    "v_add_u32 v200, 131072, v200\n\t" \
    "s_add_u32 m0, m0, 4096\n\t" \
    "s_sub_u32 s71, s71, 1\n\t" \
    "s_cmp_lg_u32 s71, 0\n\t" \
    "s_cbranch_scc1 ea_lp1_%=\n\t" \
    "s_mov_b64 s[50:51], s[38:39]\n\t" \
    "s_cmp_lt_u32 s70, 8\n\t" \
    "s_cbranch_scc1 ea_w_%=\n\t" \
    "s_mul_i32 s69, s67, 147456\n\t" \
    "s_add_u32 s44, s44, s69\n\t" \
    "s_addc_u32 s45, s45, 0\n\t" \
    "s_and_b32 s69, s70, 16\n\t" \
    "s_lshl_b32 s69, s69, 1\n\t" \
    "s_add_u32 s67, s67, s69\n\t" \
    "s_bitcmp1_b32 %1, 2\n\t" \
    "s_cbranch_scc1 gv_begin_%=\n\t" \
    "s_branch vv_begin_%=\n\t" \
    "ea_w_%=:\n\t" \
    "s_bitcmp1_b32 %1, 2\n\t" \
    "s_cbranch_scc1 gx_begin_%=\n\t" \
    "qk_begin_%=:\n\t" \
    "v_mbcnt_lo_u32_b32 v223, -1, 0\n\t" \
    "v_mbcnt_hi_u32_b32 v223, -1, v223\n\t" \
    "s_mov_b32 s68, 262144\n\t" \
    "s_mov_b32 s72, 0\n\t" \
    "s_mov_b32 s83, -1\n\t" \
    "s_mov_b32 s65, 0\n\t" \
    "v_and_b32 v180, 3, v223\n\t" \
    "v_lshlrev_b32 v202, 3, v180\n\t" \
    "v_lshrrev_b32 v181, 5, v223\n\t" \
    "v_lshl_add_u32 v202, v181, 12, v202\n\t" \
    "v_bfe_u32 v182, v223, 2, 2\n\t" \
    "s_mov_b32 s70, 288\n\t" \
    "v_mad_u32_u24 v202, v182, s70, v202\n\t" \
    "v_bfe_u32 v183, v223, 4, 1\n\t" \
    "s_mov_b32 s70, 2176\n\t" \
    "v_mad_u32_u24 v202, v183, s70, v202\n\t" \
    "s_lshl_b32 s69, s66, 6\n\t" \
    "v_xor_b32 v202, s69, v202\n\t" \
    "v_xor_b32 v203, 32, v202\n\t" \
    "v_lshlrev_b32 v0, 4, v223\n\t" \
    "s_mul_i32 s69, s66, 2048\n\t" \
    "s_bfe_u32 s71, s67, 0x10004\n\t" \
    "s_mul_i32 s71, s71, 131072\n\t" \
    "s_add_u32 s69, s69, s71\n\t" \
    "s_add_u32 s69, s69, 65536\n\t" \
    "v_lshl_add_u32 v1, v223, 4, s69\n\t" \
    "s_bfe_u32 s71, s67, 0x10004\n\t" \
    "s_mul_i32 s69, s71, 131072\n\t" \
    "s_add_u32 s69, s69, 102400\n\t" \
    "v_lshl_add_u32 v2, v223, 4, s69\n\t" \
    "s_bfe_u32 s71, s67, 0x10004\n\t" \
    "s_mul_i32 s69, s71, 131072\n\t" \
    "s_add_u32 s69, s69, 65536\n\t" \
    "v_lshl_add_u32 v34, v223, 4, s69\n\t" \
    "s_mov_b32 s69, 17476\n\t" \
    "v_bfe_u32 v184, v223, 1, 1\n\t" \
    "v_mov_b32 v10, s69\n\t" \
    "s_mov_b32 s70, 43690\n\t" \
    "v_mad_u32_u24 v10, v184, s70, v10\n\t" \
    "v_mov_b32 v11, 0\n\t" \
    "s_waitcnt vmcnt(12) lgkmcnt(0)\n\t" \
    "s_barrier\n\t" \
    "s_add_u32 m0, s82, 65536\n\t" \
    "s_nop 0\n\t" \
    "global_load_lds_dwordx4 v200, s[42:43]\n\t" \
    "v_add_u32 v200, 131072, v200\n\t" \
    "s_add_u32 m0, s82, 69632\n\t" \
    "s_nop 0\n\t" \
    "global_load_lds_dwordx4 v200, s[42:43]\n\t" \
    "v_add_u32 v200, 131072, v200\n\t" \
    "s_add_u32 m0, s82, 73728\n\t" \
    "s_nop 0\n\t" \
    "global_load_lds_dwordx4 v200, s[42:43]\n\t" \
    "v_add_u32 v200, 131072, v200\n\t" \
    "s_add_u32 m0, s82, 77824\n\t" \
    "s_nop 0\n\t" \
    "global_load_lds_dwordx4 v200, s[42:43]\n\t" \
    "v_add_u32 v200, 131072, v200\n\t" \
    "ds_read_b64_tr_b16 a[0:1], v202 offset:36864\n\t" \
    "ds_read_b64_tr_b16 a[2:3], v202 offset:37888\n\t" \
    "ds_read_b64_tr_b16 a[4:5], v202 offset:45056\n\t" \
    "ds_read_b64_tr_b16 a[6:7], v202 offset:46080\n\t" \
    "ds_read_b64_tr_b16 a[72:73], v203 offset:36864\n\t" \
    "ds_read_b64_tr_b16 a[74:75], v203 offset:37888\n\t" \
    "ds_read_b64_tr_b16 a[76:77], v203 offset:45056\n\t" \
    "ds_read_b64_tr_b16 a[78:79], v203 offset:46080\n\t" \
    "s_mul_i32 s69, s66, 512\n\t" \
    "s_add_u32 s69, s69, 16384\n\t" \
    "v_and_b32 v185, 13, v223\n\t" \
    "v_lshl_add_u32 v12, v185, 5, s69\n\t" \
    "v_lshl_add_u32 v12, v184, 2, v12\n\t" \
    "v_lshrrev_b32 v186, 4, v223\n\t" \
    "v_lshl_add_u32 v12, v186, 3, v12\n\t" \
    "s_mov_b32 s69, 16384\n\t" \
    "v_and_b32 v187, 15, v223\n\t" \
    "v_lshl_add_u32 v13, v187, 3, s69\n\t" \
    "v_lshl_add_u32 v13, v186, 8, v13\n\t" \
    "s_bfe_u32 s71, s67, 0x10004\n\t" \
    "s_mul_i32 s69, s71, 32768\n\t" \
    "s_add_u32 s69, s69, 266240\n\t" \
    "v_and_b32 v188, 31, v223\n\t" \
    "v_lshl_add_u32 v20, v188, 5, s69\n\t" \
    "v_lshl_add_u32 v20, v181, 2, v20\n\t" \
    "s_mul_i32 s69, s67, 32\n\t" \
    "s_add_u32 s69, s69, 262144\n\t" \
    "v_lshl_add_u32 v21, v180, 14, s69\n\t" \
    "v_bfe_u32 v189, v223, 2, 3\n\t" \
    "v_lshlrev_b32 v199, 10, v189\n\t" \
    "v_min_u32 v199, 4096, v199\n\t" \
    "v_add_u32 v21, v199, v21\n\t" \
    "s_bfe_u32 s71, s67, 0x10004\n\t" \
    "s_mul_i32 s69, s71, 32768\n\t" \
    "s_add_u32 s69, s69, 267264\n\t" \
    "v_min_u32 v35, 35, v223\n\t" \
    "v_lshl_add_u32 v35, v35, 5, s69\n\t" \
    "v_lshlrev_b32 v22, 2, v223\n\t" \
    "s_waitcnt vmcnt(12) lgkmcnt(0)\n\t" \
    "s_barrier\n\t" \
    "s_add_u32 m0, s82, 81920\n\t" \
    "s_nop 0\n\t" \
    "global_load_lds_dwordx4 v200, s[42:43]\n\t" \
    "v_add_u32 v200, 131072, v200\n\t" \
    "s_add_u32 m0, s82, 86016\n\t" \
    "s_nop 0\n\t" \
    "global_load_lds_dwordx4 v200, s[42:43]\n\t" \
    "v_add_u32 v200, 131072, v200\n\t" \
    "s_add_u32 m0, s82, 90112\n\t" \
    "s_nop 0\n\t" \
    "global_load_lds_dwordx4 v200, s[42:43]\n\t" \
    "v_add_u32 v200, 131072, v200\n\t" \
    "s_add_u32 m0, s82, 94208\n\t" \
    "s_nop 0\n\t" \
    "global_load_lds_dwordx4 v200, s[42:43]\n\t" \
    "v_add_u32 v200, 131072, v200\n\t" \
    "ds_read_b64_tr_b16 a[8:9], v202 offset:53248\n\t" \
    "ds_read_b64_tr_b16 a[10:11], v202 offset:54272\n\t" \
    "ds_read_b64_tr_b16 a[12:13], v202 offset:61440\n\t" \
    "ds_read_b64_tr_b16 a[14:15], v202 offset:62464\n\t" \
    "ds_read_b64_tr_b16 a[80:81], v203 offset:53248\n\t" \
    "ds_read_b64_tr_b16 a[82:83], v203 offset:54272\n\t" \
    "ds_read_b64_tr_b16 a[84:85], v203 offset:61440\n\t" \
    "ds_read_b64_tr_b16 a[86:87], v203 offset:62464\n\t" \
    "s_mul_i32 s69, s67, 128\n\t" \
    "s_add_u32 s69, s69, 6127616\n\t" \
    "v_mov_b32 v14, s69\n\t" \
    "v_and_b32 v190, 57, v223\n\t" \
    "v_lshlrev_b32 v23, 4, v190\n\t" \
    "v_lshl_add_u32 v23, v184, 6, v23\n\t" \
    "v_bfe_u32 v191, v223, 2, 1\n\t" \
    "v_lshl_add_u32 v23, v191, 5, v23\n\t" \
    "v_add_u32 v15, 4096, v23\n\t" \
    "s_mul_i32 s69, s66, 512\n\t" \
    "s_add_u32 s69, s69, 8192\n\t" \
    "v_lshl_add_u32 v16, v187, 4, s69\n\t" \
    "v_lshl_add_u32 v16, v186, 2, v16\n\t" \
    "s_mov_b32 s69, 8192\n\t" \
    "v_and_b32 v192, 1, v223\n\t" \
    "v_lshl_add_u32 v17, v192, 3, s69\n\t" \
    "v_lshrrev_b32 v193, 2, v223\n\t" \
    "v_lshl_add_u32 v17, v193, 4, v17\n\t" \
    "s_mul_i32 s69, s67, 128\n\t" \
    "s_add_u32 s69, s69, 6160384\n\t" \
    "v_mov_b32 v24, s69\n\t" \
    "s_waitcnt vmcnt(12) lgkmcnt(0)\n\t" \
    "s_barrier\n\t" \
    "s_add_u32 m0, s82, 98304\n\t" \
    "s_nop 0\n\t" \
    "global_load_lds_dwordx4 v200, s[42:43]\n\t" \
    "v_add_u32 v200, 131072, v200\n\t" \
    "s_add_u32 m0, s82, 102400\n\t" \
    "s_nop 0\n\t" \
    "global_load_lds_dwordx4 v200, s[42:43]\n\t" \
    "v_add_u32 v200, 131072, v200\n\t" \
    "s_add_u32 m0, s82, 106496\n\t" \
    "s_nop 0\n\t" \
    "global_load_lds_dwordx4 v200, s[42:43]\n\t" \
    "v_add_u32 v200, 131072, v200\n\t" \
    "s_add_u32 m0, s82, 110592\n\t" \
    "s_nop 0\n\t" \
    "global_load_lds_dwordx4 v200, s[42:43]\n\t" \
    "v_add_u32 v200, 131072, v200\n\t" \
    "v_add_u32 v202, 69632, v202\n\t" \
    "v_add_u32 v203, 69632, v203\n\t" \
    "ds_read_b64_tr_b16 a[16:17], v202 offset:0\n\t" \
    "ds_read_b64_tr_b16 a[18:19], v202 offset:1024\n\t" \
    "ds_read_b64_tr_b16 a[20:21], v202 offset:8192\n\t" \
    "ds_read_b64_tr_b16 a[22:23], v202 offset:9216\n\t" \
    "ds_read_b64_tr_b16 a[88:89], v203 offset:0\n\t" \
    "ds_read_b64_tr_b16 a[90:91], v203 offset:1024\n\t" \
    "ds_read_b64_tr_b16 a[92:93], v203 offset:8192\n\t" \
    "ds_read_b64_tr_b16 a[94:95], v203 offset:9216\n\t" \
    "s_bfe_u32 s71, s66, 0x10000\n\t" \
    "s_mul_i32 s69, s71, 128\n\t" \
    "s_bfe_u32 s71, s67, 0x40000\n\t" \
    "s_mul_i32 s71, s71, 512\n\t" \
    "s_add_u32 s69, s69, s71\n\t" \
    "s_bfe_u32 s71, s67, 0x10004\n\t" \
    "s_mul_i32 s71, s71, 32768\n\t" \
    "s_add_u32 s69, s69, s71\n\t" \
    "v_lshl_add_u32 v18, v180, 2, s69\n\t" \
    "v_lshl_add_u32 v18, v191, 5, v18\n\t" \
    "v_bfe_u32 v194, v223, 3, 1\n\t" \
    "v_lshl_add_u32 v18, v194, 8, v18\n\t" \
    "v_lshl_add_u32 v18, v183, 4, v18\n\t" \
    "v_lshl_add_u32 v18, v181, 6, v18\n\t" \
    "s_bfe_u32 s71, s66, 0x10000\n\t" \
    "s_mul_i32 s69, s71, 128\n\t" \
    "s_bfe_u32 s71, s67, 0x10000\n\t" \
    "s_mul_i32 s71, s71, 8\n\t" \
    "s_add_u32 s69, s69, s71\n\t" \
    "s_bfe_u32 s71, s67, 0x40001\n\t" \
    "s_mul_i32 s71, s71, 256\n\t" \
    "s_add_u32 s69, s69, s71\n\t" \
    "s_add_u32 s69, s69, 98304\n\t" \
    "v_lshl_add_u32 v19, v184, 5, s69\n\t" \
    "v_and_b32 v195, 5, v193\n\t" \
    "v_lshl_add_u32 v19, v195, 2, v19\n\t" \
    "v_lshl_add_u32 v19, v181, 6, v19\n\t" \
    "v_add_u32 v3, 16384, v18\n\t" \
    "v_add_u32 v4, 16384, v19\n\t" \
    "s_waitcnt vmcnt(12) lgkmcnt(0)\n\t" \
    "s_barrier\n\t" \
    "s_add_u32 m0, s82, 0\n\t" \
    "s_nop 0\n\t" \
    "global_load_lds_dwordx4 v200, s[42:43]\n\t" \
    "v_add_u32 v200, 131072, v200\n\t" \
    "s_add_u32 m0, s82, 4096\n\t" \
    "s_nop 0\n\t" \
    "global_load_lds_dwordx4 v200, s[42:43]\n\t" \
    "v_add_u32 v200, 131072, v200\n\t" \
    "s_add_u32 m0, s82, 8192\n\t" \
    "s_nop 0\n\t" \
    "global_load_lds_dwordx4 v200, s[42:43]\n\t" \
    "v_add_u32 v200, 131072, v200\n\t" \
    "s_add_u32 m0, s82, 12288\n\t" \
    "s_nop 0\n\t" \
    "global_load_lds_dwordx4 v200, s[42:43]\n\t" \
    "v_add_u32 v200, 131072, v200\n\t" \
    "ds_read_b64_tr_b16 a[24:25], v202 offset:16384\n\t" \
    "ds_read_b64_tr_b16 a[26:27], v202 offset:17408\n\t" \
    "ds_read_b64_tr_b16 a[28:29], v202 offset:24576\n\t" \
    "ds_read_b64_tr_b16 a[30:31], v202 offset:25600\n\t" \
    "ds_read_b64_tr_b16 a[96:97], v203 offset:16384\n\t" \
    "ds_read_b64_tr_b16 a[98:99], v203 offset:17408\n\t" \
    "ds_read_b64_tr_b16 a[100:101], v203 offset:24576\n\t" \
    "ds_read_b64_tr_b16 a[102:103], v203 offset:25600\n\t" \
    "s_mov_b32 s77, 3019898880\n\t" \
    "s_mov_b32 s78, 3054198966\n\t" \
    "s_mov_b32 s71, 3\n\t" \
    "qk_E1_%=:\n\t" \
    "s_memtime s[54:55]\n\t" \
    "s_nop 0\n\t" \
    "s_memrealtime s[4:5]\n\t" \
    "s_nop 0\n\t" \
    "s_memrealtime s[6:7]\n\t" \
    "s_nop 0\n\t" \
    "s_memrealtime s[8:9]\n\t" \
    "s_nop 0\n\t" \
    "s_memrealtime s[10:11]\n\t" \
    "s_nop 0\n\t" \
    "s_memrealtime s[12:13]\n\t" \
    "s_nop 0\n\t" \
    "s_memrealtime s[14:15]\n\t" \
    "s_nop 0\n\t" \
    "s_memrealtime s[16:17]\n\t" \
    "s_nop 0\n\t" \
    "s_memrealtime s[18:19]\n\t" \
    "s_nop 0\n\t" \
    "s_memrealtime s[20:21]\n\t" \
    "s_nop 0\n\t" \
    "s_memrealtime s[22:23]\n\t" \
    "s_nop 0\n\t" \
    "s_memrealtime s[24:25]\n\t" \
    "s_nop 0\n\t" \
    "s_memrealtime s[26:27]\n\t" \
    "s_nop 0\n\t" \
    "s_memrealtime s[28:29]\n\t" \
    "s_nop 0\n\t" \
    "s_memtime s[56:57]\n\t" \
    "s_mov_b32 s70, 1\n\t" \
    "s_sub_u32 s71, s71, 1\n\t" \
    "s_cselect_b32 s69, 0, -1\n\t" \
    "s_waitcnt lgkmcnt(0)\n\t" \
    "s_sub_u32 s54, s56, s54\n\t" \
    "s_and_b32 s54, s54, s69\n\t" \
    "s_cmp_gt_u32 s54, 124\n\t" \
    "s_cbranch_scc1 qk_E1_%=\n\t" \
    "s_cmp_eq_u32 s4, s28\n\t" \
    "s_addc_u32 s70, s70, 0\n\t" \
    "s_cmp_eq_u32 s6, s28\n\t" \
    "s_addc_u32 s70, s70, 0\n\t" \
    "s_cmp_eq_u32 s8, s28\n\t" \
    "s_addc_u32 s70, s70, 0\n\t" \
    "s_cmp_eq_u32 s10, s28\n\t" \
    "s_addc_u32 s70, s70, 0\n\t" \
    "s_cmp_eq_u32 s12, s28\n\t" \
    "s_addc_u32 s70, s70, 0\n\t" \
    "s_cmp_eq_u32 s14, s28\n\t" \
    "s_addc_u32 s70, s70, 0\n\t" \
    "s_cmp_eq_u32 s16, s28\n\t" \
    "s_addc_u32 s70, s70, 0\n\t" \
    "s_cmp_eq_u32 s18, s28\n\t" \
    "s_addc_u32 s70, s70, 0\n\t" \
    "s_cmp_eq_u32 s20, s28\n\t" \
    "s_addc_u32 s70, s70, 0\n\t" \
    "s_cmp_eq_u32 s22, s28\n\t" \
    "s_addc_u32 s70, s70, 0\n\t" \
    "s_cmp_eq_u32 s24, s28\n\t" \
    "s_addc_u32 s70, s70, 0\n\t" \
    "s_cmp_eq_u32 s26, s28\n\t" \
    "s_addc_u32 s70, s70, 0\n\t" \
    "s_mov_b32 s75, s73\n\t" \
    "s_mov_b32 s76, s74\n\t" \
    "s_lshl_b32 s70, s70, 3\n\t" \
    "s_sub_u32 s73, s56, s70\n\t" \
    "s_lshl_b32 s74, s28, 4\n\t" \
    "s_waitcnt vmcnt(12) lgkmcnt(0)\n\t" \
    "s_barrier\n\t" \
    "s_add_u32 m0, s82, 16384\n\t" \
    "s_nop 0\n\t" \
    "global_load_lds_dwordx4 v200, s[42:43]\n\t" \
    "v_add_u32 v200, 131072, v200\n\t" \
    "s_add_u32 m0, s82, 20480\n\t" \
    "s_nop 0\n\t" \
    "global_load_lds_dwordx4 v200, s[42:43]\n\t" \
    "v_add_u32 v200, 131072, v200\n\t" \
    "s_add_u32 m0, s82, 24576\n\t" \
    "s_nop 0\n\t" \
    "global_load_lds_dwordx4 v200, s[42:43]\n\t" \
    "v_add_u32 v200, 131072, v200\n\t" \
    "s_add_u32 m0, s82, 28672\n\t" \
    "s_nop 0\n\t" \
    "global_load_lds_dwordx4 v200, s[42:43]\n\t" \
    "v_add_u32 v200, 131072, v200\n\t" \
    "ds_read_b64_tr_b16 a[32:33], v202 offset:32768\n\t" \
    "ds_read_b64_tr_b16 a[34:35], v202 offset:33792\n\t" \
    "ds_read_b64_tr_b16 a[36:37], v202 offset:40960\n\t" \
    "ds_read_b64_tr_b16 a[38:39], v202 offset:41984\n\t" \
    "ds_read_b64_tr_b16 a[104:105], v203 offset:32768\n\t" \
    "ds_read_b64_tr_b16 a[106:107], v203 offset:33792\n\t" \
    "ds_read_b64_tr_b16 a[108:109], v203 offset:40960\n\t" \
    "ds_read_b64_tr_b16 a[110:111], v203 offset:41984\n\t" \
    "s_waitcnt vmcnt(12) lgkmcnt(0)\n\t" \
    "s_barrier\n\t" \
    "v_subrev_u32 v200, 4718592, v200\n\t" \
    "s_add_u32 m0, s82, 32768\n\t" \
    "s_nop 0\n\t" \
    "global_load_lds_dwordx4 v200, s[44:45]\n\t" \
    "v_add_u32 v200, 131072, v200\n\t" \
    "s_add_u32 m0, s82, 36864\n\t" \
    "s_nop 0\n\t" \
    "global_load_lds_dwordx4 v200, s[44:45]\n\t" \
    "v_add_u32 v200, 131072, v200\n\t" \
    "s_add_u32 m0, s82, 40960\n\t" \
    "s_nop 0\n\t" \
    "global_load_lds_dwordx4 v200, s[44:45]\n\t" \
    "v_add_u32 v200, 131072, v200\n\t" \
    "s_add_u32 m0, s82, 45056\n\t" \
    "s_nop 0\n\t" \
    "global_load_lds_dwordx4 v200, s[44:45]\n\t" \
    "v_add_u32 v200, 131072, v200\n\t" \
    "ds_read_b64_tr_b16 a[40:41], v202 offset:49152\n\t" \
    "ds_read_b64_tr_b16 a[42:43], v202 offset:50176\n\t" \
    "ds_read_b64_tr_b16 a[44:45], v202 offset:57344\n\t" \
    "ds_read_b64_tr_b16 a[46:47], v202 offset:58368\n\t" \
    "ds_read_b64_tr_b16 a[112:113], v203 offset:49152\n\t" \
    "ds_read_b64_tr_b16 a[114:115], v203 offset:50176\n\t" \
    "ds_read_b64_tr_b16 a[116:117], v203 offset:57344\n\t" \
    "ds_read_b64_tr_b16 a[118:119], v203 offset:58368\n\t" \
    "s_waitcnt vmcnt(12) lgkmcnt(0)\n\t" \
    "s_barrier\n\t" \
    "s_add_u32 m0, s82, 49152\n\t" \
    "s_nop 0\n\t" \
    "global_load_lds_dwordx4 v200, s[44:45]\n\t" \
    "v_add_u32 v200, 131072, v200\n\t" \
    "s_add_u32 m0, s82, 53248\n\t" \
    "s_nop 0\n\t" \
    "global_load_lds_dwordx4 v200, s[44:45]\n\t" \
    "v_add_u32 v200, 131072, v200\n\t" \
    "s_add_u32 m0, s82, 57344\n\t" \
    "s_nop 0\n\t" \
    "global_load_lds_dwordx4 v200, s[44:45]\n\t" \
    "v_add_u32 v200, 131072, v200\n\t" \
    "s_add_u32 m0, s82, 61440\n\t" \
    "s_nop 0\n\t" \
    "global_load_lds_dwordx4 v200, s[44:45]\n\t" \
    "v_add_u32 v200, 131072, v200\n\t" \
    "v_add_u32 v202, 65536, v202\n\t" \
    "v_add_u32 v203, 65536, v203\n\t" \
    "ds_read_b64_tr_b16 a[48:49], v202 offset:0\n\t" \
    "ds_read_b64_tr_b16 a[50:51], v202 offset:1024\n\t" \
    "ds_read_b64_tr_b16 a[52:53], v202 offset:8192\n\t" \
    "ds_read_b64_tr_b16 a[54:55], v202 offset:9216\n\t" \
    "ds_read_b64_tr_b16 a[120:121], v203 offset:0\n\t" \
    "ds_read_b64_tr_b16 a[122:123], v203 offset:1024\n\t" \
    "ds_read_b64_tr_b16 a[124:125], v203 offset:8192\n\t" \
    "ds_read_b64_tr_b16 a[126:127], v203 offset:9216\n\t" \
    "s_waitcnt vmcnt(12) lgkmcnt(0)\n\t" \
    "s_barrier\n\t" \
    "s_add_u32 m0, s82, 65536\n\t" \
    "s_nop 0\n\t" \
    "global_load_lds_dwordx4 v200, s[44:45]\n\t" \
    "v_add_u32 v200, 131072, v200\n\t" \
    "s_add_u32 m0, s82, 69632\n\t" \
    "s_nop 0\n\t" \
    "global_load_lds_dwordx4 v200, s[44:45]\n\t" \
    "v_add_u32 v200, 131072, v200\n\t" \
    "s_add_u32 m0, s82, 73728\n\t" \
    "s_nop 0\n\t" \
    "global_load_lds_dwordx4 v200, s[44:45]\n\t" \
    "v_add_u32 v200, 131072, v200\n\t" \
    "s_add_u32 m0, s82, 77824\n\t" \
    "s_nop 0\n\t" \
    "global_load_lds_dwordx4 v200, s[44:45]\n\t" \
    "v_add_u32 v200, 131072, v200\n\t" \
    "v_add_u32 v202, -98304, v202\n\t" \
    "v_add_u32 v203, -98304, v203\n\t" \
    "ds_read_b64_tr_b16 a[56:57], v202 offset:0\n\t" \
    "ds_read_b64_tr_b16 a[58:59], v202 offset:1024\n\t" \
    "ds_read_b64_tr_b16 a[60:61], v202 offset:8192\n\t" \
    "ds_read_b64_tr_b16 a[62:63], v202 offset:9216\n\t" \
    "ds_read_b64_tr_b16 a[128:129], v203 offset:0\n\t" \
    "ds_read_b64_tr_b16 a[130:131], v203 offset:1024\n\t" \
    "ds_read_b64_tr_b16 a[132:133], v203 offset:8192\n\t" \
    "ds_read_b64_tr_b16 a[134:135], v203 offset:9216\n\t" \
    "s_waitcnt vmcnt(12) lgkmcnt(0)\n\t" \
    "s_barrier\n\t" \
    "s_add_u32 m0, s82, 81920\n\t" \
    "s_nop 0\n\t" \
    "global_load_lds_dwordx4 v200, s[44:45]\n\t" \
    "v_add_u32 v200, 131072, v200\n\t" \
    "s_add_u32 m0, s82, 86016\n\t" \
    "s_nop 0\n\t" \
    "global_load_lds_dwordx4 v200, s[44:45]\n\t" \
    "v_add_u32 v200, 131072, v200\n\t" \
    "s_add_u32 m0, s82, 90112\n\t" \
    "s_nop 0\n\t" \
    "global_load_lds_dwordx4 v200, s[44:45]\n\t" \
    "v_add_u32 v200, 131072, v200\n\t" \
    "s_add_u32 m0, s82, 94208\n\t" \
    "s_nop 0\n\t" \
    "global_load_lds_dwordx4 v200, s[44:45]\n\t" \
    "v_add_u32 v200, 131072, v200\n\t" \
    "ds_read_b64_tr_b16 a[64:65], v202 offset:16384\n\t" \
    "ds_read_b64_tr_b16 a[66:67], v202 offset:17408\n\t" \
    "ds_read_b64_tr_b16 a[68:69], v202 offset:24576\n\t" \
    "ds_read_b64_tr_b16 a[70:71], v202 offset:25600\n\t" \
    "ds_read_b64_tr_b16 a[136:137], v203 offset:16384\n\t" \
    "ds_read_b64_tr_b16 a[138:139], v203 offset:17408\n\t" \
    "ds_read_b64_tr_b16 a[140:141], v203 offset:24576\n\t" \
    "ds_read_b64_tr_b16 a[142:143], v203 offset:25600\n\t" \
    "s_waitcnt vmcnt(12) lgkmcnt(0)\n\t" \
    "s_barrier\n\t" \
    "s_add_u32 m0, s82, 98304\n\t" \
    "s_nop 0\n\t" \
    "global_load_lds_dwordx4 v200, s[44:45]\n\t" \
    "v_add_u32 v200, 131072, v200\n\t" \
    "s_add_u32 m0, s82, 102400\n\t" \
    "s_nop 0\n\t" \
    "global_load_lds_dwordx4 v200, s[44:45]\n\t" \
    "v_add_u32 v200, 131072, v200\n\t" \
    "s_add_u32 m0, s82, 106496\n\t" \
    "s_nop 0\n\t" \
    "global_load_lds_dwordx4 v200, s[44:45]\n\t" \
    "v_add_u32 v200, 131072, v200\n\t" \
    "s_add_u32 m0, s82, 110592\n\t" \
    "s_nop 0\n\t" \
    "global_load_lds_dwordx4 v200, s[44:45]\n\t" \
    "v_add_u32 v200, 131072, v200\n\t" \
    "ds_read_b64_tr_b16 a[144:145], v202 offset:32768\n\t" \
    "ds_read_b64_tr_b16 a[146:147], v202 offset:33792\n\t" \
    "ds_read_b64_tr_b16 a[148:149], v202 offset:40960\n\t" \
    "ds_read_b64_tr_b16 a[150:151], v202 offset:41984\n\t" \
    "ds_read_b64_tr_b16 a[216:217], v203 offset:32768\n\t" \
    "ds_read_b64_tr_b16 a[218:219], v203 offset:33792\n\t" \
    "ds_read_b64_tr_b16 a[220:221], v203 offset:40960\n\t" \
    "ds_read_b64_tr_b16 a[222:223], v203 offset:41984\n\t" \
    "s_waitcnt vmcnt(12) lgkmcnt(0)\n\t" \
    "s_barrier\n\t" \
    "s_add_u32 m0, s82, 0\n\t" \
    "s_nop 0\n\t" \
    "global_load_lds_dwordx4 v200, s[44:45]\n\t" \
    "v_add_u32 v200, 131072, v200\n\t" \
    "s_add_u32 m0, s82, 4096\n\t" \
    "s_nop 0\n\t" \
    "global_load_lds_dwordx4 v200, s[44:45]\n\t" \
    "v_add_u32 v200, 131072, v200\n\t" \
    "s_add_u32 m0, s82, 8192\n\t" \
    "s_nop 0\n\t" \
    "global_load_lds_dwordx4 v200, s[44:45]\n\t" \
    "v_add_u32 v200, 131072, v200\n\t" \
    "s_add_u32 m0, s82, 12288\n\t" \
    "s_nop 0\n\t" \
    "global_load_lds_dwordx4 v200, s[44:45]\n\t" \
    "v_add_u32 v200, 131072, v200\n\t" \
    "ds_read_b64_tr_b16 a[152:153], v202 offset:49152\n\t" \
    "ds_read_b64_tr_b16 a[154:155], v202 offset:50176\n\t" \
    "ds_read_b64_tr_b16 a[156:157], v202 offset:57344\n\t" \
    "ds_read_b64_tr_b16 a[158:159], v202 offset:58368\n\t" \
    "ds_read_b64_tr_b16 a[224:225], v203 offset:49152\n\t" \
    "ds_read_b64_tr_b16 a[226:227], v203 offset:50176\n\t" \
    "ds_read_b64_tr_b16 a[228:229], v203 offset:57344\n\t" \
    "ds_read_b64_tr_b16 a[230:231], v203 offset:58368\n\t" \
    "s_waitcnt vmcnt(12) lgkmcnt(0)\n\t" \
    "s_barrier\n\t" \
    "s_add_u32 m0, s82, 16384\n\t" \
    "s_nop 0\n\t" \
    "global_load_lds_dwordx4 v200, s[44:45]\n\t" \
    "v_add_u32 v200, 131072, v200\n\t" \
    "s_add_u32 m0, s82, 20480\n\t" \
    "s_nop 0\n\t" \
    "global_load_lds_dwordx4 v200, s[44:45]\n\t" \
    "v_add_u32 v200, 131072, v200\n\t" \
    "s_add_u32 m0, s82, 24576\n\t" \
    "s_nop 0\n\t" \
    "global_load_lds_dwordx4 v200, s[44:45]\n\t" \
    "v_add_u32 v200, 131072, v200\n\t" \
    "s_add_u32 m0, s82, 28672\n\t" \
    "s_nop 0\n\t" \
    "global_load_lds_dwordx4 v200, s[44:45]\n\t" \
    "v_add_u32 v200, 131072, v200\n\t" \
    "v_add_u32 v202, 65536, v202\n\t" \
    "v_add_u32 v203, 65536, v203\n\t" \
    "ds_read_b64_tr_b16 a[160:161], v202 offset:0\n\t" \
    "ds_read_b64_tr_b16 a[162:163], v202 offset:1024\n\t" \
    "ds_read_b64_tr_b16 a[164:165], v202 offset:8192\n\t" \
    "ds_read_b64_tr_b16 a[166:167], v202 offset:9216\n\t" \
    "ds_read_b64_tr_b16 a[232:233], v203 offset:0\n\t" \
    "ds_read_b64_tr_b16 a[234:235], v203 offset:1024\n\t" \
    "ds_read_b64_tr_b16 a[236:237], v203 offset:8192\n\t" \
    "ds_read_b64_tr_b16 a[238:239], v203 offset:9216\n\t" \
    "s_waitcnt vmcnt(12) lgkmcnt(0)\n\t" \
    "s_barrier\n\t" \
    "s_add_u32 m0, s82, 32768\n\t" \
    "s_nop 0\n\t" \
    "global_load_lds_dwordx4 v200, s[44:45]\n\t" \
    "v_add_u32 v200, 131072, v200\n\t" \
    "s_add_u32 m0, s82, 36864\n\t" \
    "s_nop 0\n\t" \
    "global_load_lds_dwordx4 v200, s[44:45]\n\t" \
    "v_add_u32 v200, 131072, v200\n\t" \
    "s_add_u32 m0, s82, 40960\n\t" \
    "s_nop 0\n\t" \
    "global_load_lds_dwordx4 v200, s[44:45]\n\t" \
    "v_add_u32 v200, 131072, v200\n\t" \
    "s_add_u32 m0, s82, 45056\n\t" \
    "s_nop 0\n\t" \
    "global_load_lds_dwordx4 v200, s[44:45]\n\t" \
    "v_add_u32 v200, 131072, v200\n\t" \
    "ds_read_b64_tr_b16 a[168:169], v202 offset:16384\n\t" \
    "ds_read_b64_tr_b16 a[170:171], v202 offset:17408\n\t" \
    "ds_read_b64_tr_b16 a[172:173], v202 offset:24576\n\t" \
    "ds_read_b64_tr_b16 a[174:175], v202 offset:25600\n\t" \
    "ds_read_b64_tr_b16 a[240:241], v203 offset:16384\n\t" \
    "ds_read_b64_tr_b16 a[242:243], v203 offset:17408\n\t" \
    "ds_read_b64_tr_b16 a[244:245], v203 offset:24576\n\t" \
    "ds_read_b64_tr_b16 a[246:247], v203 offset:25600\n\t" \
    "s_waitcnt vmcnt(12) lgkmcnt(0)\n\t" \
    "s_barrier\n\t" \
    "s_add_u32 m0, s82, 49152\n\t" \
    "s_nop 0\n\t" \
    "global_load_lds_dwordx4 v200, s[44:45]\n\t" \
    "v_add_u32 v200, 131072, v200\n\t" \
    "s_add_u32 m0, s82, 53248\n\t" \
    "s_nop 0\n\t" \
    "global_load_lds_dwordx4 v200, s[44:45]\n\t" \
    "v_add_u32 v200, 131072, v200\n\t" \
    "s_add_u32 m0, s82, 57344\n\t" \
    "s_nop 0\n\t" \
    "global_load_lds_dwordx4 v200, s[44:45]\n\t" \
    "v_add_u32 v200, 131072, v200\n\t" \
    "s_add_u32 m0, s82, 61440\n\t" \
    "s_nop 0\n\t" \
    "global_load_lds_dwordx4 v200, s[44:45]\n\t" \
    "v_add_u32 v200, 131072, v200\n\t" \
    "ds_read_b64_tr_b16 a[176:177], v202 offset:32768\n\t" \
    "ds_read_b64_tr_b16 a[178:179], v202 offset:33792\n\t" \
    "ds_read_b64_tr_b16 a[180:181], v202 offset:40960\n\t" \
    "ds_read_b64_tr_b16 a[182:183], v202 offset:41984\n\t" \
    "ds_read_b64_tr_b16 a[248:249], v203 offset:32768\n\t" \
    "ds_read_b64_tr_b16 a[250:251], v203 offset:33792\n\t" \
    "ds_read_b64_tr_b16 a[252:253], v203 offset:40960\n\t" \
    "ds_read_b64_tr_b16 a[254:255], v203 offset:41984\n\t" \
    "s_waitcnt vmcnt(12) lgkmcnt(0)\n\t" \
    "s_barrier\n\t" \
    "v_mbcnt_lo_u32_b32 v160, -1, 0\n\t" \
    "v_mbcnt_hi_u32_b32 v160, -1, v160\n\t" \
    "v_min_u32 v160, 59, v160\n\t" \
    "v_lshlrev_b32 v160, 6, v160\n\t" \
    "v_add_u32 v160, 6258688, v160\n\t" \
    "v_mov_b32 v161, 6266880\n\t" \
    "global_load_dword v161, v161, s[36:37] sc1\n\t" \
    "global_load_dwordx2 v[162:163], v160, s[36:37] sc1\n\t" \
    "v_add_u32 v202, -65536, v202\n\t" \
    "v_add_u32 v203, -65536, v203\n\t" \
    "ds_read_b64_tr_b16 a[184:185], v202 offset:0\n\t" \
    "ds_read_b64_tr_b16 a[186:187], v202 offset:1024\n\t" \
    "ds_read_b64_tr_b16 a[188:189], v202 offset:8192\n\t" \
    "ds_read_b64_tr_b16 a[190:191], v202 offset:9216\n\t" \
    "ds_read_b64_tr_b16 v[224:225], v203 offset:0\n\t" \
    "ds_read_b64_tr_b16 v[226:227], v203 offset:1024\n\t" \
    "ds_read_b64_tr_b16 v[228:229], v203 offset:8192\n\t" \
    "ds_read_b64_tr_b16 v[230:231], v203 offset:9216\n\t" \
    "s_waitcnt vmcnt(10) lgkmcnt(0)\n\t" \
    "s_barrier\n\t" \
    "ds_read_b64_tr_b16 a[192:193], v202 offset:16384\n\t" \
    "ds_read_b64_tr_b16 a[194:195], v202 offset:17408\n\t" \
    "ds_read_b64_tr_b16 a[196:197], v202 offset:24576\n\t" \
    "ds_read_b64_tr_b16 a[198:199], v202 offset:25600\n\t" \
    "ds_read_b64_tr_b16 v[232:233], v203 offset:16384\n\t" \
    "ds_read_b64_tr_b16 v[234:235], v203 offset:17408\n\t" \
    "ds_read_b64_tr_b16 v[236:237], v203 offset:24576\n\t" \
    "ds_read_b64_tr_b16 v[238:239], v203 offset:25600\n\t" \
    "s_waitcnt vmcnt(6) lgkmcnt(0)\n\t" \
    "s_barrier\n\t" \
    "ds_read_b64_tr_b16 a[200:201], v202 offset:32768\n\t" \
    "ds_read_b64_tr_b16 a[202:203], v202 offset:33792\n\t" \
    "ds_read_b64_tr_b16 a[204:205], v202 offset:40960\n\t" \
    "ds_read_b64_tr_b16 a[206:207], v202 offset:41984\n\t" \
    "ds_read_b64_tr_b16 v[240:241], v203 offset:32768\n\t" \
    "ds_read_b64_tr_b16 v[242:243], v203 offset:33792\n\t" \
    "ds_read_b64_tr_b16 v[244:245], v203 offset:40960\n\t" \
    "ds_read_b64_tr_b16 v[246:247], v203 offset:41984\n\t" \
    "s_waitcnt vmcnt(2) lgkmcnt(0)\n\t" \
    "s_barrier\n\t" \
    "ds_read_b64_tr_b16 a[208:209], v202 offset:49152\n\t" \
    "ds_read_b64_tr_b16 a[210:211], v202 offset:50176\n\t" \
    "ds_read_b64_tr_b16 a[212:213], v202 offset:57344\n\t" \
    "ds_read_b64_tr_b16 a[214:215], v202 offset:58368\n\t" \
    "ds_read_b64_tr_b16 v[248:249], v203 offset:49152\n\t" \
    "ds_read_b64_tr_b16 v[250:251], v203 offset:50176\n\t" \
    "ds_read_b64_tr_b16 v[252:253], v203 offset:57344\n\t" \
    "ds_read_b64_tr_b16 v[254:255], v203 offset:58368\n\t" \
    "s_waitcnt vmcnt(0) lgkmcnt(0)\n\t" \
    "s_barrier\n\t" \
    "v_sub_u32 v162, v162, v161\n\t" \
    "v_cmp_ne_u32 vcc, 1, v162\n\t" \
    "s_cbranch_vccz qk_Cok4_%=\n\t" \
    "qk_C3_%=:\n\t" \
    "global_load_dwordx2 v[162:163], v160, s[36:37] sc1\n\t" \
    "s_waitcnt vmcnt(0)\n\t" \
    "v_sub_u32 v162, v162, v161\n\t" \
    "v_cmp_ne_u32 vcc, 1, v162\n\t" \
    "s_cbranch_vccz qk_Cok4_%=\n\t" \
    "s_cmp_eq_u32 s68, 0\n\t" \
    "s_cbranch_scc1 qk_F5_%=\n\t" \
    "s_sub_u32 s68, s68, 1\n\t" \
    "s_sleep 4\n\t" \
    "s_branch qk_C3_%=\n\t" \
    "qk_F5_%=:\n\t" \
    "v_mov_b32 v164, 6095104\n\t" \
    "v_mov_b32 v165, 4\n\t" \
    "global_store_dword v164, v165, s[36:37] sc1\n\t" \
    "s_waitcnt vmcnt(0)\n\t" \
    "s_branch qk_Cok4_%=\n\t" \
    "qk_Cok4_%=:\n\t" \
    "s_cmp_lg_u32 s66, 0\n\t" \
    "s_cbranch_scc1 qk_Ca6_%=\n\t" \
    "v_mov_b32 v160, 6176768\n\t" \
    "s_mov_b64 exec, 1\n\t" \
    "global_atomic_add v160, v162, s[36:37] sc1\n\t" \
    "s_mov_b64 exec, -1\n\t" \
    "qk_Ca6_%=:\n\t" \
    "v_sub_u32_dpp v162, v163, v163 row_shl:6 row_mask:0xf bank_mask:0xf\n\t" \
    "v_min_u32_dpp v163, v163, v163 row_shl:6 row_mask:0xf bank_mask:0xf\n\t" \
    "v_min_i32 v162, 64, v162\n\t" \
    "v_max_i32 v162, -64, v162\n\t" \
    "s_mov_b32 s97, 0\n\t" \
    "v_readlane_b32 s69, v163, 0\n\t" \
    "v_readlane_b32 s70, v163, 1\n\t" \
    "v_readlane_b32 s54, v162, 0\n\t" \
    "v_readlane_b32 s55, v162, 1\n\t" \
    "s_nop 1\n\t" \
    "s_cmp_lt_u32 s70, s69\n\t" \
    "s_cselect_b32 s62, 8192, 0\n\t" \
    "s_cselect_b32 s54, s55, s54\n\t" \
    "s_cselect_b32 s69, s70, s69\n\t" \
    "s_mov_b32 s71, s69\n\t" \
    "s_add_u32 s97, s97, s54\n\t" \
    "v_readlane_b32 s69, v163, 2\n\t" \
    "v_readlane_b32 s70, v163, 3\n\t" \
    "v_readlane_b32 s54, v162, 2\n\t" \
    "v_readlane_b32 s55, v162, 3\n\t" \
    "s_nop 1\n\t" \
    "s_sub_u32 s55, 0, s55\n\t" \
    "s_cmp_lt_u32 s70, s69\n\t" \
    "s_cselect_b32 s63, 8192, 0\n\t" \
    "s_cselect_b32 s54, s55, s54\n\t" \
    "s_cselect_b32 s69, s70, s69\n\t" \
    "s_max_u32 s71, s69, s71\n\t" \
    "s_add_u32 s97, s97, s54\n\t" \
    "v_readlane_b32 s69, v163, 4\n\t" \
    "v_readlane_b32 s70, v163, 5\n\t" \
    "v_readlane_b32 s54, v162, 4\n\t" \
    "v_readlane_b32 s55, v162, 5\n\t" \
    "s_nop 1\n\t" \
    "s_sub_u32 s54, 0, s54\n\t" \
    "s_sub_u32 s55, 0, s55\n\t" \
    "s_cmp_lt_u32 s70, s69\n\t" \
    "s_cselect_b32 s64, 8192, 0\n\t" \
    "s_cselect_b32 s54, s55, s54\n\t" \
    "s_cselect_b32 s69, s70, s69\n\t" \
    "s_max_u32 s71, s69, s71\n\t" \
    "s_add_u32 s97, s97, s54\n\t" \
    "s_add_u32 s63, s63, 0x200000\n\t" \
    "s_xor_b32 s52, s64, 8192\n\t" \
    "s_cmp_gt_u32 s71, 625\n\t" \
    "s_cselect_b32 s69, 1, 0\n\t" \
    "s_mul_i32 s70, s69, 160\n\t" \
    "s_add_u32 s86, s86, s70\n\t" \
    "s_mul_i32 s70, s69, 536\n\t" \
    "s_add_u32 s87, s87, s70\n\t" \
    "s_mul_i32 s70, s69, 256\n\t" \
    "s_add_u32 s88, s88, s70\n\t" \
    "s_mul_i32 s70, s67, 7\n\t" \
    "s_add_u32 s69, s65, 0\n\t" \
    "s_xor_b32 s69, s69, s70\n\t" \
    "s_and_b32 s69, s69, 63\n\t" \
    "s_lshl_b32 s69, s69, 13\n\t" \
    "s_mul_i32 s70, s67, 524288\n\t" \
    "s_add_u32 s69, s69, s70\n\t" \
    "s_add_u32 s69, s69, 23068672\n\t" \
    "s_add_u32 s50, s36, s69\n\t" \
    "s_addc_u32 s51, s37, 0\n\t" \
    "v_add_u32 v14, s52, v14\n\t" \
    "v_add_u32 v24, s52, v24\n\t" \
    "v_mov_b32 v26, 0x3db504f3\n\t" \
    "v_mov_b32 v28, 0x3fb8aa3b\n\t" \
    "v_mov_b32 v27, 0x3db504f3\n\t" \
    "v_mov_b32 v29, 0x3fb8aa3b\n\t" \
    "v_mov_b32 v29, 0x3e0293ee\n\t" \
    "s_mov_b32 s48, 0xaaaaaaaa\n\t" \
    "s_mov_b32 s49, 0xaaaaaaaa\n\t" \
    "s_mov_b32 s58, 0x00ff00ff\n\t" \
    "s_mov_b32 s59, 0x00ff00ff\n\t" \
    "s_lshl_b32 s69, s66, 2\n\t" \
    "v_mov_b32 v30, s69\n\t" \
    "v_mov_b32 v31, 0\n\t" \
    "v_and_b32 v33, 12, v22\n\t" \
    "ds_write_b32 v30, v31 offset:20496\n\t" \
    "s_waitcnt lgkmcnt(0)\n\t" \
    "s_mov_b64 s[40:41], s[36:37]\n\t" \
    "s_cmp_gt_i32 s97, 0\n\t" \
    "s_cselect_b32 s70, 1, 0\n\t" \
    "s_lshr_b32 s69, s67, 3\n\t" \
    "s_xor_b32 s69, s69, s70\n\t" \
    "s_and_b32 s97, s69, 1\n\t" \
    "s_mov_b64 s[44:45], 0\n\t" \
    "s_barrier\n\t" \
    "s_mov_b32 s71, 3\n\t" \
    "qk_E7_%=:\n\t" \
    "s_memtime s[54:55]\n\t" \
    "s_nop 0\n\t" \
    "s_memrealtime s[4:5]\n\t" \
    "s_nop 0\n\t" \
    "s_memrealtime s[6:7]\n\t" \
    "s_nop 0\n\t" \
    "s_memrealtime s[8:9]\n\t" \
    "s_nop 0\n\t" \
    "s_memrealtime s[10:11]\n\t" \
    "s_nop 0\n\t" \
    "s_memrealtime s[12:13]\n\t" \
    "s_nop 0\n\t" \
    "s_memrealtime s[14:15]\n\t" \
    "s_nop 0\n\t" \
    "s_memrealtime s[16:17]\n\t" \
    "s_nop 0\n\t" \
    "s_memrealtime s[18:19]\n\t" \
    "s_nop 0\n\t" \
    "s_memrealtime s[20:21]\n\t" \
    "s_nop 0\n\t" \
    "s_memrealtime s[22:23]\n\t" \
    "s_nop 0\n\t" \
    "s_memrealtime s[24:25]\n\t" \
    "s_nop 0\n\t" \
    "s_memrealtime s[26:27]\n\t" \
    "s_nop 0\n\t" \
    "s_memrealtime s[28:29]\n\t" \
    "s_nop 0\n\t" \
    "s_memtime s[56:57]\n\t" \
    "s_mov_b32 s70, 1\n\t" \
    "s_sub_u32 s71, s71, 1\n\t" \
    "s_cselect_b32 s69, 0, -1\n\t" \
    "s_waitcnt lgkmcnt(0)\n\t" \
    "s_sub_u32 s54, s56, s54\n\t" \
    "s_and_b32 s54, s54, s69\n\t" \
    "s_cmp_gt_u32 s54, 124\n\t" \
    "s_cbranch_scc1 qk_E7_%=\n\t" \
    "s_cmp_eq_u32 s4, s28\n\t" \
    "s_addc_u32 s70, s70, 0\n\t" \
    "s_cmp_eq_u32 s6, s28\n\t" \
    "s_addc_u32 s70, s70, 0\n\t" \
    "s_cmp_eq_u32 s8, s28\n\t" \
    "s_addc_u32 s70, s70, 0\n\t" \
    "s_cmp_eq_u32 s10, s28\n\t" \
    "s_addc_u32 s70, s70, 0\n\t" \
    "s_cmp_eq_u32 s12, s28\n\t" \
    "s_addc_u32 s70, s70, 0\n\t" \
    "s_cmp_eq_u32 s14, s28\n\t" \
    "s_addc_u32 s70, s70, 0\n\t" \
    "s_cmp_eq_u32 s16, s28\n\t" \
    "s_addc_u32 s70, s70, 0\n\t" \
    "s_cmp_eq_u32 s18, s28\n\t" \
    "s_addc_u32 s70, s70, 0\n\t" \
    "s_cmp_eq_u32 s20, s28\n\t" \
    "s_addc_u32 s70, s70, 0\n\t" \
    "s_cmp_eq_u32 s22, s28\n\t" \
    "s_addc_u32 s70, s70, 0\n\t" \
    "s_cmp_eq_u32 s24, s28\n\t" \
    "s_addc_u32 s70, s70, 0\n\t" \
    "s_cmp_eq_u32 s26, s28\n\t" \
    "s_addc_u32 s70, s70, 0\n\t" \
    "s_mov_b32 s75, s73\n\t" \
    "s_mov_b32 s76, s74\n\t" \
    "s_lshl_b32 s70, s70, 3\n\t" \
    "s_sub_u32 s73, s56, s70\n\t" \
    "s_lshl_b32 s74, s28, 4\n\t" \
    "s_sub_u32 s69, s73, s75\n\t" \
    "s_sub_u32 s70, s74, s76\n\t" \
    "s_sub_u32 s71, s70, 1600\n\t" \
    "s_cmp_lt_u32 s71, 400000\n\t" \
    "s_cbranch_scc0 qk_K9_%=\n\t" \
    "v_cvt_f32_u32 v160, s69\n\t" \
    "v_cvt_f32_u32 v161, s70\n\t" \
    "v_rcp_f32 v162, v160\n\t" \
    "v_rcp_f32 v163, v161\n\t" \
    "v_mul_f32 v162, v162, v161\n\t" \
    "v_mul_f32 v163, v163, v160\n\t" \
    "v_mul_f32 v162, 0x4f800000, v162\n\t" \
    "v_mul_f32 v163, 0x4f000000, v163\n\t" \
    "v_cvt_u32_f32 v162, v162\n\t" \
    "v_cvt_u32_f32 v163, v163\n\t" \
    "v_readfirstlane_b32 s69, v162\n\t" \
    "v_readfirstlane_b32 s70, v163\n\t" \
    "s_nop 1\n\t" \
    "s_mov_b32 s78, s69\n\t" \
    "s_mov_b32 s77, s70\n\t" \
    "qk_K9_%=:\n\t" \
    "s_mov_b32 s79, s73\n\t" \
    "s_add_u32 s80, s74, 2400\n\t" \
    "s_cmp_eq_u32 s66, 1\n\t" \
    "s_cbranch_scc1 qk_loop11_%=\n\t" \
    "s_cmp_eq_u32 s66, 2\n\t" \
    "s_cbranch_scc1 qk_loop12_%=\n\t" \
    "s_cmp_eq_u32 s66, 3\n\t" \
    "s_cbranch_scc1 qk_loop13_%=\n\t" \
    "qk_loop10_%=:\n\t" \
    "v_mov_b32 v72, 0\n\t" \
    "v_mov_b32 v73, 0\n\t" \
    "v_mov_b32 v74, 0\n\t" \
    "v_mov_b32 v75, 0\n\t" \
    "v_mov_b32 v76, 0\n\t" \
    "v_mov_b32 v77, 0\n\t" \
    "v_mov_b32 v78, 0\n\t" \
    "v_mov_b32 v79, 0\n\t" \
    "v_mov_b32 v80, 0\n\t" \
    "v_mov_b32 v81, 0\n\t" \
    "v_mov_b32 v82, 0\n\t" \
    "v_mov_b32 v83, 0\n\t" \
    "v_mov_b32 v84, 0\n\t" \
    "v_mov_b32 v85, 0\n\t" \
    "v_mov_b32 v86, 0\n\t" \
    "v_mov_b32 v87, 0\n\t" \
    "v_mov_b32 v88, 0\n\t" \
    "v_mov_b32 v89, 0\n\t" \
    "v_mov_b32 v90, 0\n\t" \
    "v_mov_b32 v91, 0\n\t" \
    "v_mov_b32 v92, 0\n\t" \
    "v_mov_b32 v93, 0\n\t" \
    "v_mov_b32 v94, 0\n\t" \
    "v_mov_b32 v95, 0\n\t" \
    "v_mov_b32 v96, 0\n\t" \
    "v_mov_b32 v97, 0\n\t" \
    "v_mov_b32 v98, 0\n\t" \
    "v_mov_b32 v99, 0\n\t" \
    "v_add_u32 v31, 1, v31\n\t" \
    "s_add_u32 s69, s65, 0\n\t" \
    "s_bitcmp1_b32 s69, 0\n\t" \
    "s_cselect_b32 s70, s63, s62\n\t" \
    "s_bfe_u32 s69, s69, 0x20001\n\t" \
    "s_lshl_b32 s69, s69, 19\n\t" \
    "s_add_u32 s69, s69, s70\n\t" \
    "s_add_u32 s46, s36, s69\n\t" \
    "s_addc_u32 s47, s37, 0\n\t" \
    "s_mov_b64 s[42:43], s[46:47]\n\t" \
    "s_add_u32 s69, s65, 0\n\t" \
    "s_lshr_b32 s69, s69, 1\n\t" \
    "s_xor_b32 s69, s69, s97\n\t" \
    "s_and_b32 s69, s69, 1\n\t" \
    "s_lshl_b32 s69, s69, 14\n\t" \
    "s_or_b32 s46, s46, s69\n\t" \
    "s_add_u32 s69, s65, 3\n\t" \
    "s_cmp_ge_i32 s83, s69\n\t" \
    "s_cselect_b64 vcc, -1, 0\n\t" \
    "v_mov_b32 v9, 6233984\n\t" \
    "v_cndmask_b32 v9, v24, v9, vcc\n\t" \
    "s_mul_i32 s70, s67, 7\n\t" \
    "s_add_u32 s69, s65, 1\n\t" \
    "s_xor_b32 s69, s69, s70\n\t" \
    "s_and_b32 s69, s69, 63\n\t" \
    "s_lshl_b32 s69, s69, 13\n\t" \
    "s_mul_i32 s70, s67, 524288\n\t" \
    "s_add_u32 s69, s69, s70\n\t" \
    "s_add_u32 s69, s69, 23068672\n\t" \
    "s_add_u32 s38, s36, s69\n\t" \
    "s_addc_u32 s39, s37, 0\n\t" \
    "qk_go19_%=:\n\t" \
    "s_mov_b32 s70, s79\n\t" \
    "s_sub_u32 s70, s70, 100\n\t" \
    "s_getpc_b64 s[30:31]\n\t" \
    "qk_Wp21_%=:\n\t" \
    "s_add_u32 s30, s30, qk_We22_%=-qk_Wp21_%=\n\t" \
    "s_addc_u32 s31, s31, 0\n\t" \
    "qk_W20_%=:\n\t" \
    "s_memtime s[56:57]\n\t" \
    "s_waitcnt lgkmcnt(0)\n\t" \
    "s_sub_u32 s69, s70, s56\n\t" \
    "s_cmp_gt_i32 s69, 111\n\t" \
    "s_cbranch_scc1 qk_W20_%=\n\t" \
    "s_max_i32 s69, s69, 0\n\t" \
    "s_lshr_b32 s69, s69, 1\n\t" \
    "s_and_b32 s69, s69, 0x3c\n\t" \
    "s_sub_u32 s30, s30, s69\n\t" \
    "s_subb_u32 s31, s31, 0\n\t" \
    "s_setpc_b64 s[30:31]\n\t" \
    ".p2align 6\n\t" \
    "s_nop 0\n\t" \
    "s_nop 0\n\t" \
    "s_nop 0\n\t" \
    "s_nop 1\n\t" \
    "s_nop 1\n\t" \
    "s_nop 1\n\t" \
    "s_nop 1\n\t" \
    "s_nop 1\n\t" \
    "s_nop 1\n\t" \
    "s_nop 1\n\t" \
    "s_nop 1\n\t" \
    "s_nop 1\n\t" \
    "s_nop 1\n\t" \
    "s_nop 1\n\t" \
    "s_nop 1\n\t" \
    "s_nop 1\n\t" \
    "qk_We22_%=:\n\t" \
    "qk_G23_%=:\n\t" \
    "global_load_dwordx4 v[36:39], v1, s[46:47] sc1\n\t" \
    "s_nop 9\n\t" \
    "global_load_dwordx4 v[40:43], v1, s[46:47] offset:1024 sc1\n\t" \
    "s_nop 9\n\t" \
    "global_load_dwordx4 v[44:47], v2, s[46:47] sc1\n\t" \
    "s_waitcnt vmcnt(2)\n\t" \
    "v_smfmac_f32_16x16x64_bf16 v[72:75], v[36:39], a[0:7], v10\n\t" \
    "ds_write_b128 v0, v[36:39] offset:0\n\t" \
    "v_smfmac_f32_16x16x64_bf16 v[76:79], v[36:39], a[72:79], v10\n\t" \
    "v_max3_u32 v160, v36, v37, v38\n\t" \
    "v_smfmac_f32_16x16x64_bf16 v[80:83], v[36:39], a[144:151], v10\n\t" \
    "v_smfmac_f32_16x16x64_bf16 v[84:87], v[36:39], a[216:223], v10\n\t" \
    "s_waitcnt vmcnt(1)\n\t" \
    "ds_write_b128 v0, v[40:43] offset:1024\n\t" \
    "v_smfmac_f32_16x16x64_bf16 v[72:75], v[40:43], a[8:15], v10\n\t" \
    "v_max3_u32 v160, v160, v39, v40\n\t" \
    "v_max3_u32 v160, v160, v41, v42\n\t" \
    "v_smfmac_f32_16x16x64_bf16 v[76:79], v[40:43], a[80:87], v10\n\t" \
    "v_max_u32 v160, v160, v43\n\t" \
    "v_cmp_eq_u32 vcc, -1, v160\n\t" \
    "v_smfmac_f32_16x16x64_bf16 v[80:83], v[40:43], a[152:159], v10\n\t" \
    "v_smfmac_f32_16x16x64_bf16 v[84:87], v[40:43], a[224:231], v10\n\t" \
    "s_cbranch_vccnz qk_Gbad24_%=\n\t" \
    "qk_Gc26_%=:\n\t" \
    "s_waitcnt lgkmcnt(0)\n\t" \
    "s_barrier\n\t" \
    "ds_read_b128 v[48:51], v0 offset:2048\n\t" \
    "ds_read_b128 v[52:55], v0 offset:3072\n\t" \
    "s_waitcnt vmcnt(0)\n\t" \
    "v_smfmac_f32_16x16x64_bf16 v[72:75], v[44:47], a[64:71], v10\n\t" \
    "ds_read_b128 v[56:59], v0 offset:4096\n\t" \
    "v_max3_u32 v160, v44, v45, v46\n\t" \
    "v_smfmac_f32_16x16x64_bf16 v[76:79], v[44:47], a[136:143], v10\n\t" \
    "ds_read_b128 v[60:63], v0 offset:5120\n\t" \
    "v_max_u32 v160, v160, v47\n\t" \
    "v_smfmac_f32_16x16x64_bf16 v[80:83], v[44:47], a[208:215], v10\n\t" \
    "ds_read_b128 v[64:67], v0 offset:6144\n\t" \
    "v_cmp_eq_u32 vcc, -1, v160\n\t" \
    "v_smfmac_f32_16x16x64_bf16 v[84:87], v[44:47], v[248:255], v10\n\t" \
    "ds_read_b128 v[68:71], v0 offset:7168\n\t" \
    "s_cbranch_vccnz qk_Gbad225_%=\n\t" \
    "qk_Gc227_%=:\n\t" \
    "s_waitcnt lgkmcnt(5)\n\t" \
    "v_smfmac_f32_16x16x64_bf16 v[72:75], v[48:51], a[16:23], v10\n\t" \
    "s_waitcnt lgkmcnt(4)\n\t" \
    "v_smfmac_f32_16x16x64_bf16 v[72:75], v[52:55], a[24:31], v10\n\t" \
    "s_waitcnt lgkmcnt(3)\n\t" \
    "v_smfmac_f32_16x16x64_bf16 v[72:75], v[56:59], a[32:39], v10\n\t" \
    "global_load_dword v25, v9, s[36:37] sc1\n\t" \
    "s_waitcnt lgkmcnt(2)\n\t" \
    "v_smfmac_f32_16x16x64_bf16 v[72:75], v[60:63], a[40:47], v10\n\t" \
    "s_waitcnt lgkmcnt(1)\n\t" \
    "v_smfmac_f32_16x16x64_bf16 v[72:75], v[64:67], a[48:55], v10\n\t" \
    "s_waitcnt lgkmcnt(0)\n\t" \
    "v_smfmac_f32_16x16x64_bf16 v[72:75], v[68:71], a[56:63], v10\n\t" \
    "v_smfmac_f32_16x16x64_bf16 v[76:79], v[48:51], a[88:95], v10\n\t" \
    "s_nop 1\n\t" \
    "s_nop 0\n\t" \
    "v_smfmac_f32_16x16x64_bf16 v[76:79], v[52:55], a[96:103], v10\n\t" \
    "s_nop 1\n\t" \
    "s_nop 0\n\t" \
    "v_smfmac_f32_16x16x64_bf16 v[76:79], v[56:59], a[104:111], v10\n\t" \
    "v_add_f32 v72, v72, v74\n\t" \
    "v_smfmac_f32_16x16x64_bf16 v[76:79], v[60:63], a[112:119], v10\n\t" \
    "v_add_f32 v73, v73, v75\n\t" \
    "v_smfmac_f32_16x16x64_bf16 v[76:79], v[64:67], a[120:127], v10\n\t" \
    "v_cvt_pk_bf16_f32 v156, v72, v73\n\t" \
    "v_smfmac_f32_16x16x64_bf16 v[76:79], v[68:71], a[128:135], v10\n\t" \
    "ds_write_b32 v12, v156 offset:0\n\t" \
    "v_smfmac_f32_16x16x64_bf16 v[80:83], v[48:51], a[160:167], v10\n\t" \
    "s_nop 1\n\t" \
    "s_nop 0\n\t" \
    "v_smfmac_f32_16x16x64_bf16 v[80:83], v[52:55], a[168:175], v10\n\t" \
    "s_nop 1\n\t" \
    "s_nop 0\n\t" \
    "v_smfmac_f32_16x16x64_bf16 v[80:83], v[56:59], a[176:183], v10\n\t" \
    "v_add_f32 v76, v76, v78\n\t" \
    "v_smfmac_f32_16x16x64_bf16 v[80:83], v[60:63], a[184:191], v10\n\t" \
    "v_add_f32 v77, v77, v79\n\t" \
    "v_smfmac_f32_16x16x64_bf16 v[80:83], v[64:67], a[192:199], v10\n\t" \
    "v_cvt_pk_bf16_f32 v157, v76, v77\n\t" \
    "v_smfmac_f32_16x16x64_bf16 v[80:83], v[68:71], a[200:207], v10\n\t" \
    "ds_write_b32 v12, v157 offset:64\n\t" \
    "v_smfmac_f32_16x16x64_bf16 v[84:87], v[48:51], a[232:239], v10\n\t" \
    "s_nop 1\n\t" \
    "s_nop 0\n\t" \
    "v_smfmac_f32_16x16x64_bf16 v[84:87], v[52:55], a[240:247], v10\n\t" \
    "s_nop 1\n\t" \
    "s_nop 0\n\t" \
    "v_smfmac_f32_16x16x64_bf16 v[84:87], v[56:59], a[248:255], v10\n\t" \
    "v_add_f32 v80, v80, v82\n\t" \
    "v_smfmac_f32_16x16x64_bf16 v[84:87], v[60:63], v[224:231], v10\n\t" \
    "v_add_f32 v81, v81, v83\n\t" \
    "v_smfmac_f32_16x16x64_bf16 v[84:87], v[64:67], v[232:239], v10\n\t" \
    "v_cvt_pk_bf16_f32 v158, v80, v81\n\t" \
    "v_smfmac_f32_16x16x64_bf16 v[84:87], v[68:71], v[240:247], v10\n\t" \
    "ds_write_b32 v16, v158 offset:0\n\t" \
    "s_nop 6\n\t" \
    "v_pk_add_f32 v[84:85], v[84:85], v[86:87]\n\t" \
    "v_cvt_pk_bf16_f32 v159, v84, v85\n\t" \
    "ds_write_b32 v16, v159 offset:256\n\t" \
    "s_cmp_eq_u32 s65, 0\n\t" \
    "s_cbranch_scc1 qk_k017_%=\n\t" \
    "qk_k0b18_%=:\n\t" \
    "s_waitcnt lgkmcnt(0)\n\t" \
    "s_barrier\n\t" \
    "ds_read_b64_tr_b16 v[132:133], v13 offset:0\n\t" \
    "ds_read_b64_tr_b16 v[134:135], v13 offset:128\n\t" \
    "ds_read_b64_tr_b16 v[136:137], v13 offset:1024\n\t" \
    "ds_read_b64_tr_b16 v[138:139], v13 offset:1152\n\t" \
    "ds_read_b64_tr_b16 v[140:141], v17 offset:0\n\t" \
    "ds_read_b64_tr_b16 v[142:143], v17 offset:256\n\t" \
    "ds_read_b64_tr_b16 v[144:145], v17 offset:512\n\t" \
    "ds_read_b64_tr_b16 v[146:147], v17 offset:768\n\t" \
    "ds_read_b64_tr_b16 v[148:149], v17 offset:1024\n\t" \
    "ds_read_b64_tr_b16 v[150:151], v17 offset:1280\n\t" \
    "ds_read_b64_tr_b16 v[152:153], v17 offset:1536\n\t" \
    "ds_read_b64_tr_b16 v[154:155], v17 offset:1792\n\t" \
    "s_waitcnt lgkmcnt(10)\n\t" \
    "v_smfmac_f32_16x16x64_bf16 v[88:91], v[132:135], v[100:107], v10\n\t" \
    "v_smfmac_f32_16x16x64_bf16 v[92:95], v[132:135], v[116:123], v10\n\t" \
    "s_waitcnt lgkmcnt(8)\n\t" \
    "v_smfmac_f32_16x16x64_bf16 v[88:91], v[136:139], v[108:115], v10\n\t" \
    "v_smfmac_f32_16x16x64_bf16 v[92:95], v[136:139], v[124:131], v10\n\t" \
    "s_waitcnt lgkmcnt(4)\n\t" \
    "v_smfmac_f32_16x16x64_bf16 v[96:99], v[132:135], v[140:147], v10\n\t" \
    "s_waitcnt lgkmcnt(0)\n\t" \
    "s_nop 1\n\t" \
    "v_smfmac_f32_16x16x64_bf16 v[96:99], v[136:139], v[148:155], v10\n\t" \
    "s_nop 0\n\t" \
    "v_add_f32 v168, v88, v90\n\t" \
    "v_add_f32 v169, v92, v94\n\t" \
    "v_max_f32 v178, v168, v169\n\t" \
    "v_mul_f32 v168, v168, v29\n\t" \
    "v_mul_f32 v169, v169, v29\n\t" \
    "v_max_f32_dpp v178, v178, v178 row_mirror row_mask:0xf bank_mask:0xf\n\t" \
    "s_nop 0\n\t" \
    "v_add_f32 v172, v96, v98\n\t" \
    "v_max_f32 v173, v178, v172\n\t" \
    "v_mul_f32 v172, v172, v29\n\t" \
    "s_nop 0\n\t" \
    "v_max_f32_dpp v173, v173, v173 quad_perm:[1,0,3,2] row_mask:0xf bank_mask:0xf\n\t" \
    "s_nop 1\n\t" \
    "v_max_f32_dpp v173, v173, v173 quad_perm:[2,3,0,1] row_mask:0xf bank_mask:0xf\n\t" \
    "s_nop 1\n\t" \
    "v_max_f32_dpp v173, v173, v173 row_half_mirror row_mask:0xf bank_mask:0xf\n\t" \
    "v_fma_f32 v172, -v173, v29, v172\n\t" \
    "v_fma_f32 v168, -v173, v29, v168\n\t" \
    "v_fma_f32 v169, -v173, v29, v169\n\t" \
    "v_exp_f32 v172, v172\n\t" \
    "v_exp_f32 v168, v168\n\t" \
    "v_exp_f32 v169, v169\n\t" \
    "v_fma_f32 v173, v172, 0.5, v168\n\t" \
    "v_add_f32 v173, v173, v169\n\t" \
    "v_mov_b32_dpp v174, v172 quad_perm:[0,0,2,2] row_mask:0xf bank_mask:0xf\n\t" \
    "v_mov_b32_dpp v175, v172 quad_perm:[1,1,3,3] row_mask:0xf bank_mask:0xf\n\t" \
    "v_add_f32_dpp v173, v173, v173 quad_perm:[1,0,3,2] row_mask:0xf bank_mask:0xf\n\t" \
    "v_mov_b32_dpp v176, v168 quad_perm:[1,0,3,2] row_mask:0xf bank_mask:0xf\n\t" \
    "v_mov_b32_dpp v177, v169 quad_perm:[1,0,3,2] row_mask:0xf bank_mask:0xf\n\t" \
    "v_add_f32_dpp v173, v173, v173 quad_perm:[2,3,0,1] row_mask:0xf bank_mask:0xf\n\t" \
    "v_cndmask_b32 v168, v168, v177, s[48:49]\n\t" \
    "v_cndmask_b32 v169, v176, v169, s[48:49]\n\t" \
    "v_add_f32_dpp v173, v173, v173 row_half_mirror row_mask:0xf bank_mask:0xf\n\t" \
    "s_nop 1\n\t" \
    "v_add_f32_dpp v173, v173, v173 row_mirror row_mask:0xf bank_mask:0xf\n\t" \
    "v_rcp_f32 v173, v173\n\t" \
    "s_nop 0\n\t" \
    "v_pk_mul_f32 v[168:169], v[168:169], v[172:173] op_sel:[0,1] op_sel_hi:[1,1]\n\t" \
    "v_pk_mul_f32 v[174:175], v[174:175], v[172:173] op_sel:[0,1] op_sel_hi:[1,1]\n\t" \
    "v_cvt_pk_bf16_f32 v176, v168, v169\n\t" \
    "global_store_dword v18, v176, s[42:43] sc1\n\t" \
    "global_store_dword v3, v176, s[42:43] sc1\n\t" \
    "v_cvt_pk_bf16_f32 v177, v174, v175\n\t" \
    "global_store_dword v19, v177, s[42:43] sc1\n\t" \
    "global_store_dword v4, v177, s[42:43] sc1\n\t" \
    "s_cmp_eq_u32 s65, 63\n\t" \
    "s_cbranch_scc1 qk_last14_%=\n\t" \
    "s_memtime s[54:55]\n\t" \
    "s_waitcnt vmcnt(4) lgkmcnt(0)\n\t" \
    "s_add_u32 s79, s54, 728\n\t" \
    "v_readfirstlane_b32 s69, v25\n\t" \
    "s_add_u32 s71, s65, 1\n\t" \
    "s_max_i32 s83, s83, s69\n\t" \
    "qk_WF29_%=:\n\t" \
    "s_cmp_ge_i32 s83, s71\n\t" \
    "s_cbranch_scc1 qk_WFok30_%=\n\t" \
    "global_load_dword v160, v24, s[36:37] sc1\n\t" \
    "s_waitcnt vmcnt(0)\n\t" \
    "v_readfirstlane_b32 s83, v160\n\t" \
    "s_nop 1\n\t" \
    "s_cmp_ge_i32 s83, s71\n\t" \
    "s_cbranch_scc1 qk_WFok30_%=\n\t" \
    "s_cmp_eq_u32 s68, 0\n\t" \
    "s_cbranch_scc1 qk_F31_%=\n\t" \
    "s_sub_u32 s68, s68, 1\n\t" \
    "s_sleep 2\n\t" \
    "s_branch qk_WF29_%=\n\t" \
    "qk_F31_%=:\n\t" \
    "v_mov_b32 v160, 6095104\n\t" \
    "v_mov_b32 v161, 9\n\t" \
    "global_store_dword v160, v161, s[36:37] sc1\n\t" \
    "s_waitcnt vmcnt(0)\n\t" \
    "s_branch qk_WFok30_%=\n\t" \
    "qk_WFok30_%=:\n\t" \
    "v_mov_b32 v72, 0\n\t" \
    "v_mov_b32 v73, 0\n\t" \
    "v_mov_b32 v74, 0\n\t" \
    "v_mov_b32 v75, 0\n\t" \
    "v_mov_b32 v76, 0\n\t" \
    "v_mov_b32 v77, 0\n\t" \
    "v_mov_b32 v78, 0\n\t" \
    "v_mov_b32 v79, 0\n\t" \
    "v_mov_b32 v80, 0\n\t" \
    "v_mov_b32 v81, 0\n\t" \
    "v_mov_b32 v82, 0\n\t" \
    "v_mov_b32 v83, 0\n\t" \
    "v_mov_b32 v84, 0\n\t" \
    "v_mov_b32 v85, 0\n\t" \
    "v_mov_b32 v86, 0\n\t" \
    "v_mov_b32 v87, 0\n\t" \
    "v_mov_b32 v88, 0\n\t" \
    "v_mov_b32 v89, 0\n\t" \
    "v_mov_b32 v90, 0\n\t" \
    "v_mov_b32 v91, 0\n\t" \
    "v_mov_b32 v92, 0\n\t" \
    "v_mov_b32 v93, 0\n\t" \
    "v_mov_b32 v94, 0\n\t" \
    "v_mov_b32 v95, 0\n\t" \
    "v_mov_b32 v96, 0\n\t" \
    "v_mov_b32 v97, 0\n\t" \
    "v_mov_b32 v98, 0\n\t" \
    "v_mov_b32 v99, 0\n\t" \
    "s_mov_b32 s70, s79\n\t" \
    "qk_W32_%=:\n\t" \
    "s_memtime s[56:57]\n\t" \
    "s_waitcnt lgkmcnt(0)\n\t" \
    "s_sub_u32 s69, s70, s56\n\t" \
    "s_cmp_gt_i32 s69, 24\n\t" \
    "s_cbranch_scc1 qk_W32_%=\n\t" \
    "global_load_dwordx4 v[100:103], v23, s[38:39] offset:0 sc1\n\t" \
    "global_load_dwordx4 v[104:107], v23, s[38:39] offset:2048 sc1\n\t" \
    "global_load_dwordx4 v[108:111], v15, s[38:39] offset:0 sc1\n\t" \
    "global_load_dwordx4 v[112:115], v15, s[38:39] offset:2048 sc1\n\t" \
    "global_load_dwordx4 v[116:119], v23, s[38:39] offset:1024 sc1\n\t" \
    "global_load_dwordx4 v[120:123], v23, s[38:39] offset:3072 sc1\n\t" \
    "global_load_dwordx4 v[124:127], v15, s[38:39] offset:1024 sc1\n\t" \
    "global_load_dwordx4 v[128:131], v15, s[38:39] offset:3072 sc1\n\t" \
    "s_add_u32 s65, s65, 1\n\t" \
    "v_add_u32 v31, 1, v31\n\t" \
    "s_add_u32 s69, s65, 0\n\t" \
    "s_bitcmp1_b32 s69, 0\n\t" \
    "s_cselect_b32 s70, s63, s62\n\t" \
    "s_bfe_u32 s69, s69, 0x20001\n\t" \
    "s_lshl_b32 s69, s69, 19\n\t" \
    "s_add_u32 s69, s69, s70\n\t" \
    "s_add_u32 s46, s36, s69\n\t" \
    "s_addc_u32 s47, s37, 0\n\t" \
    "s_mov_b64 s[42:43], s[46:47]\n\t" \
    "s_add_u32 s69, s65, 0\n\t" \
    "s_lshr_b32 s69, s69, 1\n\t" \
    "s_xor_b32 s69, s69, s97\n\t" \
    "s_and_b32 s69, s69, 1\n\t" \
    "s_lshl_b32 s69, s69, 14\n\t" \
    "s_or_b32 s46, s46, s69\n\t" \
    "s_add_u32 s69, s65, 3\n\t" \
    "s_cmp_ge_i32 s83, s69\n\t" \
    "s_cselect_b64 vcc, -1, 0\n\t" \
    "v_mov_b32 v9, 6233984\n\t" \
    "v_cndmask_b32 v9, v24, v9, vcc\n\t" \
    "s_mul_i32 s70, s67, 7\n\t" \
    "s_add_u32 s69, s65, 1\n\t" \
    "s_xor_b32 s69, s69, s70\n\t" \
    "s_and_b32 s69, s69, 63\n\t" \
    "s_lshl_b32 s69, s69, 13\n\t" \
    "s_mul_i32 s70, s67, 524288\n\t" \
    "s_add_u32 s69, s69, s70\n\t" \
    "s_add_u32 s69, s69, 23068672\n\t" \
    "s_add_u32 s38, s36, s69\n\t" \
    "s_addc_u32 s39, s37, 0\n\t" \
    "s_waitcnt lgkmcnt(0)\n\t" \
    "s_barrier\n\t" \
    "ds_read_b32 v160, v11 offset:20480\n\t" \
    "s_waitcnt lgkmcnt(0)\n\t" \
    "v_readfirstlane_b32 s79, v160\n\t" \
    "s_nop 1\n\t" \
    "s_branch qk_go19_%=\n\t" \
    "qk_Gbad24_%=:\n\t" \
    "s_waitcnt vmcnt(0) lgkmcnt(0)\n\t" \
    "s_nop 7\n\t" \
    "v_mov_b32 v72, 0\n\t" \
    "v_mov_b32 v73, 0\n\t" \
    "v_mov_b32 v74, 0\n\t" \
    "v_mov_b32 v75, 0\n\t" \
    "v_mov_b32 v76, 0\n\t" \
    "v_mov_b32 v77, 0\n\t" \
    "v_mov_b32 v78, 0\n\t" \
    "v_mov_b32 v79, 0\n\t" \
    "v_mov_b32 v80, 0\n\t" \
    "v_mov_b32 v81, 0\n\t" \
    "v_mov_b32 v82, 0\n\t" \
    "v_mov_b32 v83, 0\n\t" \
    "v_mov_b32 v84, 0\n\t" \
    "v_mov_b32 v85, 0\n\t" \
    "v_mov_b32 v86, 0\n\t" \
    "v_mov_b32 v87, 0\n\t" \
    "s_cmp_eq_u32 s68, 0\n\t" \
    "s_cbranch_scc1 qk_F33_%=\n\t" \
    "s_sub_u32 s68, s68, 1\n\t" \
    "s_branch qk_G23_%=\n\t" \
    "qk_F33_%=:\n\t" \
    "v_mov_b32 v160, 6095104\n\t" \
    "v_mov_b32 v161, 1\n\t" \
    "global_store_dword v160, v161, s[36:37] sc1\n\t" \
    "s_waitcnt vmcnt(0)\n\t" \
    "s_branch qk_Gc26_%=\n\t" \
    "qk_Gbad225_%=:\n\t" \
    "global_load_dwordx4 v[44:47], v2, s[46:47] sc1\n\t" \
    "s_waitcnt vmcnt(0)\n\t" \
    "v_max3_u32 v160, v44, v45, v46\n\t" \
    "v_max_u32 v160, v160, v47\n\t" \
    "v_cmp_eq_u32 vcc, -1, v160\n\t" \
    "s_cbranch_vccz qk_Gre228_%=\n\t" \
    "s_cmp_eq_u32 s68, 0\n\t" \
    "s_cbranch_scc1 qk_F34_%=\n\t" \
    "s_sub_u32 s68, s68, 1\n\t" \
    "s_sleep 1\n\t" \
    "s_branch qk_Gbad225_%=\n\t" \
    "qk_F34_%=:\n\t" \
    "v_mov_b32 v160, 6095104\n\t" \
    "v_mov_b32 v161, 2\n\t" \
    "global_store_dword v160, v161, s[36:37] sc1\n\t" \
    "s_waitcnt vmcnt(0)\n\t" \
    "s_branch qk_Gre228_%=\n\t" \
    "qk_Gre228_%=:\n\t" \
    "v_mov_b32 v72, 0\n\t" \
    "v_mov_b32 v73, 0\n\t" \
    "v_mov_b32 v74, 0\n\t" \
    "v_mov_b32 v75, 0\n\t" \
    "v_mov_b32 v76, 0\n\t" \
    "v_mov_b32 v77, 0\n\t" \
    "v_mov_b32 v78, 0\n\t" \
    "v_mov_b32 v79, 0\n\t" \
    "v_mov_b32 v80, 0\n\t" \
    "v_mov_b32 v81, 0\n\t" \
    "v_mov_b32 v82, 0\n\t" \
    "v_mov_b32 v83, 0\n\t" \
    "v_mov_b32 v84, 0\n\t" \
    "v_mov_b32 v85, 0\n\t" \
    "v_mov_b32 v86, 0\n\t" \
    "v_mov_b32 v87, 0\n\t" \
    "v_smfmac_f32_16x16x64_bf16 v[72:75], v[36:39], a[0:7], v10\n\t" \
    "v_smfmac_f32_16x16x64_bf16 v[76:79], v[36:39], a[72:79], v10\n\t" \
    "v_smfmac_f32_16x16x64_bf16 v[80:83], v[36:39], a[144:151], v10\n\t" \
    "v_smfmac_f32_16x16x64_bf16 v[84:87], v[36:39], a[216:223], v10\n\t" \
    "v_smfmac_f32_16x16x64_bf16 v[72:75], v[40:43], a[8:15], v10\n\t" \
    "v_smfmac_f32_16x16x64_bf16 v[76:79], v[40:43], a[80:87], v10\n\t" \
    "v_smfmac_f32_16x16x64_bf16 v[80:83], v[40:43], a[152:159], v10\n\t" \
    "v_smfmac_f32_16x16x64_bf16 v[84:87], v[40:43], a[224:231], v10\n\t" \
    "v_smfmac_f32_16x16x64_bf16 v[72:75], v[44:47], a[64:71], v10\n\t" \
    "v_smfmac_f32_16x16x64_bf16 v[76:79], v[44:47], a[136:143], v10\n\t" \
    "v_smfmac_f32_16x16x64_bf16 v[80:83], v[44:47], a[208:215], v10\n\t" \
    "v_smfmac_f32_16x16x64_bf16 v[84:87], v[44:47], v[248:255], v10\n\t" \
    "s_branch qk_Gc227_%=\n\t" \
    "qk_k017_%=:\n\t" \
    "qk_WF35_%=:\n\t" \
    "s_cmp_ge_i32 s83, s65\n\t" \
    "s_cbranch_scc1 qk_WFok36_%=\n\t" \
    "global_load_dword v160, v24, s[36:37] sc1\n\t" \
    "s_waitcnt vmcnt(0)\n\t" \
    "v_readfirstlane_b32 s83, v160\n\t" \
    "s_nop 1\n\t" \
    "s_cmp_ge_i32 s83, s65\n\t" \
    "s_cbranch_scc1 qk_WFok36_%=\n\t" \
    "s_cmp_eq_u32 s68, 0\n\t" \
    "s_cbranch_scc1 qk_F37_%=\n\t" \
    "s_sub_u32 s68, s68, 1\n\t" \
    "s_sleep 2\n\t" \
    "s_branch qk_WF35_%=\n\t" \
    "qk_F37_%=:\n\t" \
    "v_mov_b32 v160, 6095104\n\t" \
    "v_mov_b32 v161, 8\n\t" \
    "global_store_dword v160, v161, s[36:37] sc1\n\t" \
    "s_waitcnt vmcnt(0)\n\t" \
    "s_branch qk_WFok36_%=\n\t" \
    "qk_WFok36_%=:\n\t" \
    "s_memrealtime s[54:55]\n\t" \
    "s_waitcnt lgkmcnt(0)\n\t" \
    "s_lshl_b32 s69, s54, 4\n\t" \
    "s_add_u32 s69, s69, 1280\n\t" \
    "v_mov_b32 v160, s69\n\t" \
    "global_load_dwordx4 v[100:103], v23, s[50:51] offset:0 sc1\n\t" \
    "global_load_dwordx4 v[104:107], v23, s[50:51] offset:2048 sc1\n\t" \
    "global_load_dwordx4 v[108:111], v15, s[50:51] offset:0 sc1\n\t" \
    "global_load_dwordx4 v[112:115], v15, s[50:51] offset:2048 sc1\n\t" \
    "global_load_dwordx4 v[116:119], v23, s[50:51] offset:1024 sc1\n\t" \
    "global_load_dwordx4 v[120:123], v23, s[50:51] offset:3072 sc1\n\t" \
    "global_load_dwordx4 v[124:127], v15, s[50:51] offset:1024 sc1\n\t" \
    "global_load_dwordx4 v[128:131], v15, s[50:51] offset:3072 sc1\n\t" \
    "s_mov_b64 exec, 0xff000\n\t" \
    "global_store_dword v21, v160, s[42:43] sc1\n\t" \
    "s_mov_b64 exec, -1\n\t" \
    "s_waitcnt vmcnt(1)\n\t" \
    "s_branch qk_k0b18_%=\n\t" \
    "qk_last14_%=:\n\t" \
    "s_endpgm\n\t" \
    "qk_loop11_%=:\n\t" \
    "v_mov_b32 v72, 0\n\t" \
    "v_mov_b32 v73, 0\n\t" \
    "v_mov_b32 v74, 0\n\t" \
    "v_mov_b32 v75, 0\n\t" \
    "v_mov_b32 v76, 0\n\t" \
    "v_mov_b32 v77, 0\n\t" \
    "v_mov_b32 v78, 0\n\t" \
    "v_mov_b32 v79, 0\n\t" \
    "v_mov_b32 v80, 0\n\t" \
    "v_mov_b32 v81, 0\n\t" \
    "v_mov_b32 v82, 0\n\t" \
    "v_mov_b32 v83, 0\n\t" \
    "v_mov_b32 v84, 0\n\t" \
    "v_mov_b32 v85, 0\n\t" \
    "v_mov_b32 v86, 0\n\t" \
    "v_mov_b32 v87, 0\n\t" \
    "v_mov_b32 v88, 0\n\t" \
    "v_mov_b32 v89, 0\n\t" \
    "v_mov_b32 v90, 0\n\t" \
    "v_mov_b32 v91, 0\n\t" \
    "v_mov_b32 v92, 0\n\t" \
    "v_mov_b32 v93, 0\n\t" \
    "v_mov_b32 v94, 0\n\t" \
    "v_mov_b32 v95, 0\n\t" \
    "v_mov_b32 v96, 0\n\t" \
    "v_mov_b32 v97, 0\n\t" \
    "v_mov_b32 v98, 0\n\t" \
    "v_mov_b32 v99, 0\n\t" \
    "v_add_u32 v31, 1, v31\n\t" \
    "s_add_u32 s69, s65, 0\n\t" \
    "s_bitcmp1_b32 s69, 0\n\t" \
    "s_cselect_b32 s70, s63, s62\n\t" \
    "s_bfe_u32 s69, s69, 0x20001\n\t" \
    "s_lshl_b32 s69, s69, 19\n\t" \
    "s_add_u32 s69, s69, s70\n\t" \
    "s_add_u32 s46, s36, s69\n\t" \
    "s_addc_u32 s47, s37, 0\n\t" \
    "s_mov_b64 s[42:43], s[46:47]\n\t" \
    "s_add_u32 s69, s65, 0\n\t" \
    "s_lshr_b32 s69, s69, 1\n\t" \
    "s_xor_b32 s69, s69, s97\n\t" \
    "s_and_b32 s69, s69, 1\n\t" \
    "s_lshl_b32 s69, s69, 14\n\t" \
    "s_or_b32 s46, s46, s69\n\t" \
    "s_add_u32 s69, s65, 3\n\t" \
    "s_cmp_ge_i32 s83, s69\n\t" \
    "s_cselect_b64 vcc, -1, 0\n\t" \
    "v_mov_b32 v9, 6233984\n\t" \
    "v_cndmask_b32 v9, v24, v9, vcc\n\t" \
    "s_mul_i32 s70, s67, 7\n\t" \
    "s_add_u32 s69, s65, 1\n\t" \
    "s_xor_b32 s69, s69, s70\n\t" \
    "s_and_b32 s69, s69, 63\n\t" \
    "s_lshl_b32 s69, s69, 13\n\t" \
    "s_mul_i32 s70, s67, 524288\n\t" \
    "s_add_u32 s69, s69, s70\n\t" \
    "s_add_u32 s69, s69, 23068672\n\t" \
    "s_add_u32 s38, s36, s69\n\t" \
    "s_addc_u32 s39, s37, 0\n\t" \
    "qk_go43_%=:\n\t" \
    "s_mov_b32 s70, s79\n\t" \
    "s_sub_u32 s70, s70, 100\n\t" \
    "s_getpc_b64 s[30:31]\n\t" \
    "qk_Wp45_%=:\n\t" \
    "s_add_u32 s30, s30, qk_We46_%=-qk_Wp45_%=\n\t" \
    "s_addc_u32 s31, s31, 0\n\t" \
    "qk_W44_%=:\n\t" \
    "s_memtime s[56:57]\n\t" \
    "s_waitcnt lgkmcnt(0)\n\t" \
    "s_sub_u32 s69, s70, s56\n\t" \
    "s_cmp_gt_i32 s69, 111\n\t" \
    "s_cbranch_scc1 qk_W44_%=\n\t" \
    "s_max_i32 s69, s69, 0\n\t" \
    "s_lshr_b32 s69, s69, 1\n\t" \
    "s_and_b32 s69, s69, 0x3c\n\t" \
    "s_sub_u32 s30, s30, s69\n\t" \
    "s_subb_u32 s31, s31, 0\n\t" \
    "s_setpc_b64 s[30:31]\n\t" \
    ".p2align 6\n\t" \
    "s_nop 0\n\t" \
    "s_nop 0\n\t" \
    "s_nop 0\n\t" \
    "s_nop 1\n\t" \
    "s_nop 1\n\t" \
    "s_nop 1\n\t" \
    "s_nop 1\n\t" \
    "s_nop 1\n\t" \
    "s_nop 1\n\t" \
    "s_nop 1\n\t" \
    "s_nop 1\n\t" \
    "s_nop 1\n\t" \
    "s_nop 1\n\t" \
    "s_nop 1\n\t" \
    "s_nop 1\n\t" \
    "s_nop 1\n\t" \
    "qk_We46_%=:\n\t" \
    "qk_G47_%=:\n\t" \
    "global_load_dwordx4 v[36:39], v1, s[46:47] sc1\n\t" \
    "s_nop 9\n\t" \
    "global_load_dwordx4 v[40:43], v1, s[46:47] offset:1024 sc1\n\t" \
    "s_nop 9\n\t" \
    "global_load_dwordx4 v[44:47], v2, s[46:47] sc1\n\t" \
    "s_waitcnt vmcnt(2)\n\t" \
    "v_smfmac_f32_16x16x64_bf16 v[72:75], v[36:39], a[16:23], v10\n\t" \
    "ds_write_b128 v0, v[36:39] offset:2048\n\t" \
    "v_smfmac_f32_16x16x64_bf16 v[76:79], v[36:39], a[88:95], v10\n\t" \
    "v_max3_u32 v160, v36, v37, v38\n\t" \
    "v_smfmac_f32_16x16x64_bf16 v[80:83], v[36:39], a[160:167], v10\n\t" \
    "v_smfmac_f32_16x16x64_bf16 v[84:87], v[36:39], a[232:239], v10\n\t" \
    "s_waitcnt vmcnt(1)\n\t" \
    "ds_write_b128 v0, v[40:43] offset:3072\n\t" \
    "v_smfmac_f32_16x16x64_bf16 v[72:75], v[40:43], a[24:31], v10\n\t" \
    "v_max3_u32 v160, v160, v39, v40\n\t" \
    "v_max3_u32 v160, v160, v41, v42\n\t" \
    "v_smfmac_f32_16x16x64_bf16 v[76:79], v[40:43], a[96:103], v10\n\t" \
    "v_max_u32 v160, v160, v43\n\t" \
    "v_cmp_eq_u32 vcc, -1, v160\n\t" \
    "v_smfmac_f32_16x16x64_bf16 v[80:83], v[40:43], a[168:175], v10\n\t" \
    "v_smfmac_f32_16x16x64_bf16 v[84:87], v[40:43], a[240:247], v10\n\t" \
    "s_cbranch_vccnz qk_Gbad48_%=\n\t" \
    "qk_Gc50_%=:\n\t" \
    "s_waitcnt lgkmcnt(0)\n\t" \
    "s_barrier\n\t" \
    "ds_read_b128 v[48:51], v0 offset:4096\n\t" \
    "ds_read_b128 v[52:55], v0 offset:5120\n\t" \
    "s_waitcnt vmcnt(0)\n\t" \
    "v_smfmac_f32_16x16x64_bf16 v[72:75], v[44:47], a[64:71], v10\n\t" \
    "ds_read_b128 v[56:59], v0 offset:6144\n\t" \
    "v_max3_u32 v160, v44, v45, v46\n\t" \
    "v_smfmac_f32_16x16x64_bf16 v[76:79], v[44:47], a[136:143], v10\n\t" \
    "ds_read_b128 v[60:63], v0 offset:7168\n\t" \
    "v_max_u32 v160, v160, v47\n\t" \
    "v_smfmac_f32_16x16x64_bf16 v[80:83], v[44:47], a[208:215], v10\n\t" \
    "ds_read_b128 v[64:67], v0 offset:0\n\t" \
    "v_cmp_eq_u32 vcc, -1, v160\n\t" \
    "v_smfmac_f32_16x16x64_bf16 v[84:87], v[44:47], v[248:255], v10\n\t" \
    "ds_read_b128 v[68:71], v0 offset:1024\n\t" \
    "s_cbranch_vccnz qk_Gbad249_%=\n\t" \
    "qk_Gc251_%=:\n\t" \
    "s_waitcnt lgkmcnt(5)\n\t" \
    "v_smfmac_f32_16x16x64_bf16 v[72:75], v[48:51], a[32:39], v10\n\t" \
    "s_waitcnt lgkmcnt(4)\n\t" \
    "v_smfmac_f32_16x16x64_bf16 v[72:75], v[52:55], a[40:47], v10\n\t" \
    "s_waitcnt lgkmcnt(3)\n\t" \
    "v_smfmac_f32_16x16x64_bf16 v[72:75], v[56:59], a[48:55], v10\n\t" \
    "global_load_dword v25, v9, s[36:37] sc1\n\t" \
    "s_waitcnt lgkmcnt(2)\n\t" \
    "v_smfmac_f32_16x16x64_bf16 v[72:75], v[60:63], a[56:63], v10\n\t" \
    "s_waitcnt lgkmcnt(1)\n\t" \
    "v_smfmac_f32_16x16x64_bf16 v[72:75], v[64:67], a[0:7], v10\n\t" \
    "s_waitcnt lgkmcnt(0)\n\t" \
    "v_smfmac_f32_16x16x64_bf16 v[72:75], v[68:71], a[8:15], v10\n\t" \
    "v_smfmac_f32_16x16x64_bf16 v[76:79], v[48:51], a[104:111], v10\n\t" \
    "s_nop 1\n\t" \
    "s_nop 0\n\t" \
    "v_smfmac_f32_16x16x64_bf16 v[76:79], v[52:55], a[112:119], v10\n\t" \
    "s_nop 1\n\t" \
    "s_nop 0\n\t" \
    "v_smfmac_f32_16x16x64_bf16 v[76:79], v[56:59], a[120:127], v10\n\t" \
    "v_add_f32 v72, v72, v74\n\t" \
    "v_smfmac_f32_16x16x64_bf16 v[76:79], v[60:63], a[128:135], v10\n\t" \
    "v_add_f32 v73, v73, v75\n\t" \
    "v_smfmac_f32_16x16x64_bf16 v[76:79], v[64:67], a[72:79], v10\n\t" \
    "v_cvt_pk_bf16_f32 v156, v72, v73\n\t" \
    "v_smfmac_f32_16x16x64_bf16 v[76:79], v[68:71], a[80:87], v10\n\t" \
    "ds_write_b32 v12, v156 offset:0\n\t" \
    "v_smfmac_f32_16x16x64_bf16 v[80:83], v[48:51], a[176:183], v10\n\t" \
    "s_nop 1\n\t" \
    "s_nop 0\n\t" \
    "v_smfmac_f32_16x16x64_bf16 v[80:83], v[52:55], a[184:191], v10\n\t" \
    "s_nop 1\n\t" \
    "s_nop 0\n\t" \
    "v_smfmac_f32_16x16x64_bf16 v[80:83], v[56:59], a[192:199], v10\n\t" \
    "v_add_f32 v76, v76, v78\n\t" \
    "v_smfmac_f32_16x16x64_bf16 v[80:83], v[60:63], a[200:207], v10\n\t" \
    "v_add_f32 v77, v77, v79\n\t" \
    "v_smfmac_f32_16x16x64_bf16 v[80:83], v[64:67], a[144:151], v10\n\t" \
    "v_cvt_pk_bf16_f32 v157, v76, v77\n\t" \
    "v_smfmac_f32_16x16x64_bf16 v[80:83], v[68:71], a[152:159], v10\n\t" \
    "ds_write_b32 v12, v157 offset:64\n\t" \
    "v_smfmac_f32_16x16x64_bf16 v[84:87], v[48:51], a[248:255], v10\n\t" \
    "s_nop 1\n\t" \
    "s_nop 0\n\t" \
    "v_smfmac_f32_16x16x64_bf16 v[84:87], v[52:55], v[224:231], v10\n\t" \
    "s_nop 1\n\t" \
    "s_nop 0\n\t" \
    "v_smfmac_f32_16x16x64_bf16 v[84:87], v[56:59], v[232:239], v10\n\t" \
    "v_add_f32 v80, v80, v82\n\t" \
    "v_smfmac_f32_16x16x64_bf16 v[84:87], v[60:63], v[240:247], v10\n\t" \
    "v_add_f32 v81, v81, v83\n\t" \
    "v_smfmac_f32_16x16x64_bf16 v[84:87], v[64:67], a[216:223], v10\n\t" \
    "v_cvt_pk_bf16_f32 v158, v80, v81\n\t" \
    "v_smfmac_f32_16x16x64_bf16 v[84:87], v[68:71], a[224:231], v10\n\t" \
    "ds_write_b32 v16, v158 offset:0\n\t" \
    "s_nop 6\n\t" \
    "v_pk_add_f32 v[84:85], v[84:85], v[86:87]\n\t" \
    "v_cvt_pk_bf16_f32 v159, v84, v85\n\t" \
    "ds_write_b32 v16, v159 offset:256\n\t" \
    "s_cmp_eq_u32 s65, 0\n\t" \
    "s_cbranch_scc1 qk_k041_%=\n\t" \
    "qk_k0b42_%=:\n\t" \
    "s_waitcnt lgkmcnt(0)\n\t" \
    "s_barrier\n\t" \
    "ds_read_b64_tr_b16 v[132:133], v13 offset:0\n\t" \
    "ds_read_b64_tr_b16 v[134:135], v13 offset:128\n\t" \
    "ds_read_b64_tr_b16 v[136:137], v13 offset:1024\n\t" \
    "ds_read_b64_tr_b16 v[138:139], v13 offset:1152\n\t" \
    "ds_read_b64_tr_b16 v[140:141], v17 offset:0\n\t" \
    "ds_read_b64_tr_b16 v[142:143], v17 offset:256\n\t" \
    "ds_read_b64_tr_b16 v[144:145], v17 offset:512\n\t" \
    "ds_read_b64_tr_b16 v[146:147], v17 offset:768\n\t" \
    "ds_read_b64_tr_b16 v[148:149], v17 offset:1024\n\t" \
    "ds_read_b64_tr_b16 v[150:151], v17 offset:1280\n\t" \
    "ds_read_b64_tr_b16 v[152:153], v17 offset:1536\n\t" \
    "ds_read_b64_tr_b16 v[154:155], v17 offset:1792\n\t" \
    "s_waitcnt lgkmcnt(10)\n\t" \
    "v_smfmac_f32_16x16x64_bf16 v[88:91], v[132:135], v[100:107], v10\n\t" \
    "v_smfmac_f32_16x16x64_bf16 v[92:95], v[132:135], v[116:123], v10\n\t" \
    "s_waitcnt lgkmcnt(8)\n\t" \
    "v_smfmac_f32_16x16x64_bf16 v[88:91], v[136:139], v[108:115], v10\n\t" \
    "v_smfmac_f32_16x16x64_bf16 v[92:95], v[136:139], v[124:131], v10\n\t" \
    "s_waitcnt lgkmcnt(4)\n\t" \
    "v_smfmac_f32_16x16x64_bf16 v[96:99], v[132:135], v[140:147], v10\n\t" \
    "s_waitcnt lgkmcnt(0)\n\t" \
    "s_nop 1\n\t" \
    "v_smfmac_f32_16x16x64_bf16 v[96:99], v[136:139], v[148:155], v10\n\t" \
    "s_nop 0\n\t" \
    "v_add_f32 v168, v89, v91\n\t" \
    "v_add_f32 v169, v93, v95\n\t" \
    "v_max_f32 v178, v168, v169\n\t" \
    "v_mul_f32 v168, v168, v29\n\t" \
    "v_mul_f32 v169, v169, v29\n\t" \
    "v_max_f32_dpp v178, v178, v178 row_mirror row_mask:0xf bank_mask:0xf\n\t" \
    "s_nop 0\n\t" \
    "v_add_f32 v172, v97, v99\n\t" \
    "v_max_f32 v173, v178, v172\n\t" \
    "v_mul_f32 v172, v172, v29\n\t" \
    "s_nop 0\n\t" \
    "v_max_f32_dpp v173, v173, v173 quad_perm:[1,0,3,2] row_mask:0xf bank_mask:0xf\n\t" \
    "s_nop 1\n\t" \
    "v_max_f32_dpp v173, v173, v173 quad_perm:[2,3,0,1] row_mask:0xf bank_mask:0xf\n\t" \
    "s_nop 1\n\t" \
    "v_max_f32_dpp v173, v173, v173 row_half_mirror row_mask:0xf bank_mask:0xf\n\t" \
    "v_fma_f32 v172, -v173, v29, v172\n\t" \
    "v_fma_f32 v168, -v173, v29, v168\n\t" \
    "v_fma_f32 v169, -v173, v29, v169\n\t" \
    "v_exp_f32 v172, v172\n\t" \
    "v_exp_f32 v168, v168\n\t" \
    "v_exp_f32 v169, v169\n\t" \
    "v_fma_f32 v173, v172, 0.5, v168\n\t" \
    "v_add_f32 v173, v173, v169\n\t" \
    "v_mov_b32_dpp v174, v172 quad_perm:[0,0,2,2] row_mask:0xf bank_mask:0xf\n\t" \
    "v_mov_b32_dpp v175, v172 quad_perm:[1,1,3,3] row_mask:0xf bank_mask:0xf\n\t" \
    "v_add_f32_dpp v173, v173, v173 quad_perm:[1,0,3,2] row_mask:0xf bank_mask:0xf\n\t" \
    "v_mov_b32_dpp v176, v168 quad_perm:[1,0,3,2] row_mask:0xf bank_mask:0xf\n\t" \
    "v_mov_b32_dpp v177, v169 quad_perm:[1,0,3,2] row_mask:0xf bank_mask:0xf\n\t" \
    "v_add_f32_dpp v173, v173, v173 quad_perm:[2,3,0,1] row_mask:0xf bank_mask:0xf\n\t" \
    "v_cndmask_b32 v168, v168, v177, s[48:49]\n\t" \
    "v_cndmask_b32 v169, v176, v169, s[48:49]\n\t" \
    "v_add_f32_dpp v173, v173, v173 row_half_mirror row_mask:0xf bank_mask:0xf\n\t" \
    "s_nop 1\n\t" \
    "v_add_f32_dpp v173, v173, v173 row_mirror row_mask:0xf bank_mask:0xf\n\t" \
    "v_rcp_f32 v173, v173\n\t" \
    "s_nop 0\n\t" \
    "v_pk_mul_f32 v[168:169], v[168:169], v[172:173] op_sel:[0,1] op_sel_hi:[1,1]\n\t" \
    "v_pk_mul_f32 v[174:175], v[174:175], v[172:173] op_sel:[0,1] op_sel_hi:[1,1]\n\t" \
    "v_cvt_pk_bf16_f32 v176, v168, v169\n\t" \
    "global_store_dword v18, v176, s[42:43] sc1\n\t" \
    "global_store_dword v3, v176, s[42:43] sc1\n\t" \
    "v_cvt_pk_bf16_f32 v177, v174, v175\n\t" \
    "global_store_dword v19, v177, s[42:43] sc1\n\t" \
    "global_store_dword v4, v177, s[42:43] sc1\n\t" \
    "s_cmp_eq_u32 s65, 63\n\t" \
    "s_cbranch_scc1 qk_last38_%=\n\t" \
    "s_memtime s[54:55]\n\t" \
    "s_waitcnt vmcnt(4) lgkmcnt(0)\n\t" \
    "s_add_u32 s79, s54, 728\n\t" \
    "v_readfirstlane_b32 s69, v25\n\t" \
    "s_add_u32 s71, s65, 1\n\t" \
    "s_max_i32 s83, s83, s69\n\t" \
    "qk_WF53_%=:\n\t" \
    "s_cmp_ge_i32 s83, s71\n\t" \
    "s_cbranch_scc1 qk_WFok54_%=\n\t" \
    "global_load_dword v160, v24, s[36:37] sc1\n\t" \
    "s_waitcnt vmcnt(0)\n\t" \
    "v_readfirstlane_b32 s83, v160\n\t" \
    "s_nop 1\n\t" \
    "s_cmp_ge_i32 s83, s71\n\t" \
    "s_cbranch_scc1 qk_WFok54_%=\n\t" \
    "s_cmp_eq_u32 s68, 0\n\t" \
    "s_cbranch_scc1 qk_F55_%=\n\t" \
    "s_sub_u32 s68, s68, 1\n\t" \
    "s_sleep 2\n\t" \
    "s_branch qk_WF53_%=\n\t" \
    "qk_F55_%=:\n\t" \
    "v_mov_b32 v160, 6095104\n\t" \
    "v_mov_b32 v161, 9\n\t" \
    "global_store_dword v160, v161, s[36:37] sc1\n\t" \
    "s_waitcnt vmcnt(0)\n\t" \
    "s_branch qk_WFok54_%=\n\t" \
    "qk_WFok54_%=:\n\t" \
    "v_mov_b32 v72, 0\n\t" \
    "v_mov_b32 v73, 0\n\t" \
    "v_mov_b32 v74, 0\n\t" \
    "v_mov_b32 v75, 0\n\t" \
    "v_mov_b32 v76, 0\n\t" \
    "v_mov_b32 v77, 0\n\t" \
    "v_mov_b32 v78, 0\n\t" \
    "v_mov_b32 v79, 0\n\t" \
    "v_mov_b32 v80, 0\n\t" \
    "v_mov_b32 v81, 0\n\t" \
    "v_mov_b32 v82, 0\n\t" \
    "v_mov_b32 v83, 0\n\t" \
    "v_mov_b32 v84, 0\n\t" \
    "v_mov_b32 v85, 0\n\t" \
    "v_mov_b32 v86, 0\n\t" \
    "v_mov_b32 v87, 0\n\t" \
    "v_mov_b32 v88, 0\n\t" \
    "v_mov_b32 v89, 0\n\t" \
    "v_mov_b32 v90, 0\n\t" \
    "v_mov_b32 v91, 0\n\t" \
    "v_mov_b32 v92, 0\n\t" \
    "v_mov_b32 v93, 0\n\t" \
    "v_mov_b32 v94, 0\n\t" \
    "v_mov_b32 v95, 0\n\t" \
    "v_mov_b32 v96, 0\n\t" \
    "v_mov_b32 v97, 0\n\t" \
    "v_mov_b32 v98, 0\n\t" \
    "v_mov_b32 v99, 0\n\t" \
    "s_mov_b32 s70, s79\n\t" \
    "qk_W56_%=:\n\t" \
    "s_memtime s[56:57]\n\t" \
    "s_waitcnt lgkmcnt(0)\n\t" \
    "s_sub_u32 s69, s70, s56\n\t" \
    "s_cmp_gt_i32 s69, 24\n\t" \
    "s_cbranch_scc1 qk_W56_%=\n\t" \
    "global_load_dwordx4 v[100:103], v23, s[38:39] offset:0 sc1\n\t" \
    "global_load_dwordx4 v[104:107], v23, s[38:39] offset:2048 sc1\n\t" \
    "global_load_dwordx4 v[108:111], v15, s[38:39] offset:0 sc1\n\t" \
    "global_load_dwordx4 v[112:115], v15, s[38:39] offset:2048 sc1\n\t" \
    "global_load_dwordx4 v[116:119], v23, s[38:39] offset:1024 sc1\n\t" \
    "global_load_dwordx4 v[120:123], v23, s[38:39] offset:3072 sc1\n\t" \
    "global_load_dwordx4 v[124:127], v15, s[38:39] offset:1024 sc1\n\t" \
    "global_load_dwordx4 v[128:131], v15, s[38:39] offset:3072 sc1\n\t" \
    "s_add_u32 s65, s65, 1\n\t" \
    "v_add_u32 v31, 1, v31\n\t" \
    "s_add_u32 s69, s65, 0\n\t" \
    "s_bitcmp1_b32 s69, 0\n\t" \
    "s_cselect_b32 s70, s63, s62\n\t" \
    "s_bfe_u32 s69, s69, 0x20001\n\t" \
    "s_lshl_b32 s69, s69, 19\n\t" \
    "s_add_u32 s69, s69, s70\n\t" \
    "s_add_u32 s46, s36, s69\n\t" \
    "s_addc_u32 s47, s37, 0\n\t" \
    "s_mov_b64 s[42:43], s[46:47]\n\t" \
    "s_add_u32 s69, s65, 0\n\t" \
    "s_lshr_b32 s69, s69, 1\n\t" \
    "s_xor_b32 s69, s69, s97\n\t" \
    "s_and_b32 s69, s69, 1\n\t" \
    "s_lshl_b32 s69, s69, 14\n\t" \
    "s_or_b32 s46, s46, s69\n\t" \
    "s_add_u32 s69, s65, 3\n\t" \
    "s_cmp_ge_i32 s83, s69\n\t" \
    "s_cselect_b64 vcc, -1, 0\n\t" \
    "v_mov_b32 v9, 6233984\n\t" \
    "v_cndmask_b32 v9, v24, v9, vcc\n\t" \
    "s_mul_i32 s70, s67, 7\n\t" \
    "s_add_u32 s69, s65, 1\n\t" \
    "s_xor_b32 s69, s69, s70\n\t" \
    "s_and_b32 s69, s69, 63\n\t" \
    "s_lshl_b32 s69, s69, 13\n\t" \
    "s_mul_i32 s70, s67, 524288\n\t" \
    "s_add_u32 s69, s69, s70\n\t" \
    "s_add_u32 s69, s69, 23068672\n\t" \
    "s_add_u32 s38, s36, s69\n\t" \
    "s_addc_u32 s39, s37, 0\n\t" \
    "s_waitcnt lgkmcnt(0)\n\t" \
    "s_barrier\n\t" \
    "ds_read_b32 v160, v11 offset:20480\n\t" \
    "s_waitcnt lgkmcnt(0)\n\t" \
    "v_readfirstlane_b32 s79, v160\n\t" \
    "s_nop 1\n\t" \
    "s_branch qk_go43_%=\n\t" \
    "qk_Gbad48_%=:\n\t" \
    "s_waitcnt vmcnt(0) lgkmcnt(0)\n\t" \
    "s_nop 7\n\t" \
    "v_mov_b32 v72, 0\n\t" \
    "v_mov_b32 v73, 0\n\t" \
    "v_mov_b32 v74, 0\n\t" \
    "v_mov_b32 v75, 0\n\t" \
    "v_mov_b32 v76, 0\n\t" \
    "v_mov_b32 v77, 0\n\t" \
    "v_mov_b32 v78, 0\n\t" \
    "v_mov_b32 v79, 0\n\t" \
    "v_mov_b32 v80, 0\n\t" \
    "v_mov_b32 v81, 0\n\t" \
    "v_mov_b32 v82, 0\n\t" \
    "v_mov_b32 v83, 0\n\t" \
    "v_mov_b32 v84, 0\n\t" \
    "v_mov_b32 v85, 0\n\t" \
    "v_mov_b32 v86, 0\n\t" \
    "v_mov_b32 v87, 0\n\t" \
    "s_cmp_eq_u32 s68, 0\n\t" \
    "s_cbranch_scc1 qk_F57_%=\n\t" \
    "s_sub_u32 s68, s68, 1\n\t" \
    "s_branch qk_G47_%=\n\t" \
    "qk_F57_%=:\n\t" \
    "v_mov_b32 v160, 6095104\n\t" \
    "v_mov_b32 v161, 1\n\t" \
    "global_store_dword v160, v161, s[36:37] sc1\n\t" \
    "s_waitcnt vmcnt(0)\n\t" \
    "s_branch qk_Gc50_%=\n\t" \
    "qk_Gbad249_%=:\n\t" \
    "global_load_dwordx4 v[44:47], v2, s[46:47] sc1\n\t" \
    "s_waitcnt vmcnt(0)\n\t" \
    "v_max3_u32 v160, v44, v45, v46\n\t" \
    "v_max_u32 v160, v160, v47\n\t" \
    "v_cmp_eq_u32 vcc, -1, v160\n\t" \
    "s_cbranch_vccz qk_Gre252_%=\n\t" \
    "s_cmp_eq_u32 s68, 0\n\t" \
    "s_cbranch_scc1 qk_F58_%=\n\t" \
    "s_sub_u32 s68, s68, 1\n\t" \
    "s_sleep 1\n\t" \
    "s_branch qk_Gbad249_%=\n\t" \
    "qk_F58_%=:\n\t" \
    "v_mov_b32 v160, 6095104\n\t" \
    "v_mov_b32 v161, 2\n\t" \
    "global_store_dword v160, v161, s[36:37] sc1\n\t" \
    "s_waitcnt vmcnt(0)\n\t" \
    "s_branch qk_Gre252_%=\n\t" \
    "qk_Gre252_%=:\n\t" \
    "v_mov_b32 v72, 0\n\t" \
    "v_mov_b32 v73, 0\n\t" \
    "v_mov_b32 v74, 0\n\t" \
    "v_mov_b32 v75, 0\n\t" \
    "v_mov_b32 v76, 0\n\t" \
    "v_mov_b32 v77, 0\n\t" \
    "v_mov_b32 v78, 0\n\t" \
    "v_mov_b32 v79, 0\n\t" \
    "v_mov_b32 v80, 0\n\t" \
    "v_mov_b32 v81, 0\n\t" \
    "v_mov_b32 v82, 0\n\t" \
    "v_mov_b32 v83, 0\n\t" \
    "v_mov_b32 v84, 0\n\t" \
    "v_mov_b32 v85, 0\n\t" \
    "v_mov_b32 v86, 0\n\t" \
    "v_mov_b32 v87, 0\n\t" \
    "v_smfmac_f32_16x16x64_bf16 v[72:75], v[36:39], a[16:23], v10\n\t" \
    "v_smfmac_f32_16x16x64_bf16 v[76:79], v[36:39], a[88:95], v10\n\t" \
    "v_smfmac_f32_16x16x64_bf16 v[80:83], v[36:39], a[160:167], v10\n\t" \
    "v_smfmac_f32_16x16x64_bf16 v[84:87], v[36:39], a[232:239], v10\n\t" \
    "v_smfmac_f32_16x16x64_bf16 v[72:75], v[40:43], a[24:31], v10\n\t" \
    "v_smfmac_f32_16x16x64_bf16 v[76:79], v[40:43], a[96:103], v10\n\t" \
    "v_smfmac_f32_16x16x64_bf16 v[80:83], v[40:43], a[168:175], v10\n\t" \
    "v_smfmac_f32_16x16x64_bf16 v[84:87], v[40:43], a[240:247], v10\n\t" \
    "v_smfmac_f32_16x16x64_bf16 v[72:75], v[44:47], a[64:71], v10\n\t" \
    "v_smfmac_f32_16x16x64_bf16 v[76:79], v[44:47], a[136:143], v10\n\t" \
    "v_smfmac_f32_16x16x64_bf16 v[80:83], v[44:47], a[208:215], v10\n\t" \
    "v_smfmac_f32_16x16x64_bf16 v[84:87], v[44:47], v[248:255], v10\n\t" \
    "s_branch qk_Gc251_%=\n\t" \
    "qk_k041_%=:\n\t" \
    "qk_WF59_%=:\n\t" \
    "s_cmp_ge_i32 s83, s65\n\t" \
    "s_cbranch_scc1 qk_WFok60_%=\n\t" \
    "global_load_dword v160, v24, s[36:37] sc1\n\t" \
    "s_waitcnt vmcnt(0)\n\t" \
    "v_readfirstlane_b32 s83, v160\n\t" \
    "s_nop 1\n\t" \
    "s_cmp_ge_i32 s83, s65\n\t" \
    "s_cbranch_scc1 qk_WFok60_%=\n\t" \
    "s_cmp_eq_u32 s68, 0\n\t" \
    "s_cbranch_scc1 qk_F61_%=\n\t" \
    "s_sub_u32 s68, s68, 1\n\t" \
    "s_sleep 2\n\t" \
    "s_branch qk_WF59_%=\n\t" \
    "qk_F61_%=:\n\t" \
    "v_mov_b32 v160, 6095104\n\t" \
    "v_mov_b32 v161, 8\n\t" \
    "global_store_dword v160, v161, s[36:37] sc1\n\t" \
    "s_waitcnt vmcnt(0)\n\t" \
    "s_branch qk_WFok60_%=\n\t" \
    "qk_WFok60_%=:\n\t" \
    "global_load_dwordx4 v[100:103], v23, s[50:51] offset:0 sc1\n\t" \
    "global_load_dwordx4 v[104:107], v23, s[50:51] offset:2048 sc1\n\t" \
    "global_load_dwordx4 v[108:111], v15, s[50:51] offset:0 sc1\n\t" \
    "global_load_dwordx4 v[112:115], v15, s[50:51] offset:2048 sc1\n\t" \
    "global_load_dwordx4 v[116:119], v23, s[50:51] offset:1024 sc1\n\t" \
    "global_load_dwordx4 v[120:123], v23, s[50:51] offset:3072 sc1\n\t" \
    "global_load_dwordx4 v[124:127], v15, s[50:51] offset:1024 sc1\n\t" \
    "global_load_dwordx4 v[128:131], v15, s[50:51] offset:3072 sc1\n\t" \
    "s_waitcnt vmcnt(0)\n\t" \
    "s_branch qk_k0b42_%=\n\t" \
    "qk_last38_%=:\n\t" \
    "s_endpgm\n\t" \
    "qk_loop12_%=:\n\t" \
    "v_mov_b32 v72, 0\n\t" \
    "v_mov_b32 v73, 0\n\t" \
    "v_mov_b32 v74, 0\n\t" \
    "v_mov_b32 v75, 0\n\t" \
    "v_mov_b32 v76, 0\n\t" \
    "v_mov_b32 v77, 0\n\t" \
    "v_mov_b32 v78, 0\n\t" \
    "v_mov_b32 v79, 0\n\t" \
    "v_mov_b32 v80, 0\n\t" \
    "v_mov_b32 v81, 0\n\t" \
    "v_mov_b32 v82, 0\n\t" \
    "v_mov_b32 v83, 0\n\t" \
    "v_mov_b32 v84, 0\n\t" \
    "v_mov_b32 v85, 0\n\t" \
    "v_mov_b32 v86, 0\n\t" \
    "v_mov_b32 v87, 0\n\t" \
    "v_add_u32 v31, 1, v31\n\t" \
    "s_add_u32 s69, s65, 0\n\t" \
    "s_bitcmp1_b32 s69, 0\n\t" \
    "s_cselect_b32 s70, s63, s62\n\t" \
    "s_bfe_u32 s69, s69, 0x20001\n\t" \
    "s_lshl_b32 s69, s69, 19\n\t" \
    "s_add_u32 s69, s69, s70\n\t" \
    "s_add_u32 s46, s36, s69\n\t" \
    "s_addc_u32 s47, s37, 0\n\t" \
    "s_mov_b64 s[42:43], s[46:47]\n\t" \
    "s_add_u32 s69, s65, 0\n\t" \
    "s_lshr_b32 s69, s69, 1\n\t" \
    "s_xor_b32 s69, s69, s97\n\t" \
    "s_and_b32 s69, s69, 1\n\t" \
    "s_lshl_b32 s69, s69, 14\n\t" \
    "s_or_b32 s46, s46, s69\n\t" \
    "s_mul_i32 s70, s67, 7\n\t" \
    "s_add_u32 s69, s65, 0\n\t" \
    "s_xor_b32 s69, s69, s70\n\t" \
    "s_and_b32 s69, s69, 63\n\t" \
    "s_lshl_b32 s69, s69, 12\n\t" \
    "s_mul_i32 s70, s67, 262144\n\t" \
    "s_add_u32 s69, s69, s70\n\t" \
    "s_add_u32 s69, s69, 39845888\n\t" \
    "s_add_u32 s40, s36, s69\n\t" \
    "s_addc_u32 s41, s37, 0\n\t" \
    "s_sub_u32 s69, s65, 1\n\t" \
    "v_mov_b32 v25, s69\n\t" \
    "qk_go67_%=:\n\t" \
    "s_mov_b32 s70, s79\n\t" \
    "s_sub_u32 s70, s70, 100\n\t" \
    "s_getpc_b64 s[30:31]\n\t" \
    "qk_Wp69_%=:\n\t" \
    "s_add_u32 s30, s30, qk_We70_%=-qk_Wp69_%=\n\t" \
    "s_addc_u32 s31, s31, 0\n\t" \
    "qk_W68_%=:\n\t" \
    "s_memtime s[56:57]\n\t" \
    "s_waitcnt lgkmcnt(0)\n\t" \
    "s_sub_u32 s69, s70, s56\n\t" \
    "s_cmp_gt_i32 s69, 111\n\t" \
    "s_cbranch_scc1 qk_W68_%=\n\t" \
    "s_max_i32 s69, s69, 0\n\t" \
    "s_lshr_b32 s69, s69, 1\n\t" \
    "s_and_b32 s69, s69, 0x3c\n\t" \
    "s_sub_u32 s30, s30, s69\n\t" \
    "s_subb_u32 s31, s31, 0\n\t" \
    "s_setpc_b64 s[30:31]\n\t" \
    ".p2align 6\n\t" \
    "s_nop 0\n\t" \
    "s_nop 0\n\t" \
    "s_nop 0\n\t" \
    "s_nop 1\n\t" \
    "s_nop 1\n\t" \
    "s_nop 1\n\t" \
    "s_nop 1\n\t" \
    "s_nop 1\n\t" \
    "s_nop 1\n\t" \
    "s_nop 1\n\t" \
    "s_nop 1\n\t" \
    "s_nop 1\n\t" \
    "s_nop 1\n\t" \
    "s_nop 1\n\t" \
    "s_nop 1\n\t" \
    "s_nop 1\n\t" \
    "qk_We70_%=:\n\t" \
    "qk_G71_%=:\n\t" \
    "global_load_dwordx4 v[36:39], v1, s[46:47] sc1\n\t" \
    "s_nop 9\n\t" \
    "global_load_dwordx4 v[40:43], v1, s[46:47] offset:1024 sc1\n\t" \
    "s_nop 9\n\t" \
    "global_load_dwordx4 v[44:47], v2, s[46:47] sc1\n\t" \
    "s_waitcnt vmcnt(2)\n\t" \
    "v_smfmac_f32_16x16x64_bf16 v[72:75], v[36:39], a[32:39], v10\n\t" \
    "ds_write_b128 v0, v[36:39] offset:4096\n\t" \
    "v_smfmac_f32_16x16x64_bf16 v[76:79], v[36:39], a[104:111], v10\n\t" \
    "v_max3_u32 v160, v36, v37, v38\n\t" \
    "v_smfmac_f32_16x16x64_bf16 v[80:83], v[36:39], a[176:183], v10\n\t" \
    "v_smfmac_f32_16x16x64_bf16 v[84:87], v[36:39], a[248:255], v10\n\t" \
    "s_waitcnt vmcnt(1)\n\t" \
    "ds_write_b128 v0, v[40:43] offset:5120\n\t" \
    "v_smfmac_f32_16x16x64_bf16 v[72:75], v[40:43], a[40:47], v10\n\t" \
    "v_max3_u32 v160, v160, v39, v40\n\t" \
    "v_max3_u32 v160, v160, v41, v42\n\t" \
    "v_smfmac_f32_16x16x64_bf16 v[76:79], v[40:43], a[112:119], v10\n\t" \
    "v_max_u32 v160, v160, v43\n\t" \
    "v_cmp_eq_u32 vcc, -1, v160\n\t" \
    "v_smfmac_f32_16x16x64_bf16 v[80:83], v[40:43], a[184:191], v10\n\t" \
    "v_smfmac_f32_16x16x64_bf16 v[84:87], v[40:43], v[224:231], v10\n\t" \
    "s_cbranch_vccnz qk_Gbad72_%=\n\t" \
    "qk_Gc74_%=:\n\t" \
    "s_waitcnt lgkmcnt(0)\n\t" \
    "s_barrier\n\t" \
    "ds_read_b128 v[48:51], v0 offset:6144\n\t" \
    "ds_read_b128 v[52:55], v0 offset:7168\n\t" \
    "s_waitcnt vmcnt(0)\n\t" \
    "v_smfmac_f32_16x16x64_bf16 v[72:75], v[44:47], a[64:71], v10\n\t" \
    "ds_read_b128 v[56:59], v0 offset:0\n\t" \
    "v_max3_u32 v160, v44, v45, v46\n\t" \
    "v_smfmac_f32_16x16x64_bf16 v[76:79], v[44:47], a[136:143], v10\n\t" \
    "ds_read_b128 v[60:63], v0 offset:1024\n\t" \
    "v_max_u32 v160, v160, v47\n\t" \
    "v_smfmac_f32_16x16x64_bf16 v[80:83], v[44:47], a[208:215], v10\n\t" \
    "ds_read_b128 v[64:67], v0 offset:2048\n\t" \
    "v_cmp_eq_u32 vcc, -1, v160\n\t" \
    "v_smfmac_f32_16x16x64_bf16 v[84:87], v[44:47], v[248:255], v10\n\t" \
    "ds_read_b128 v[68:71], v0 offset:3072\n\t" \
    "s_cbranch_vccnz qk_Gbad273_%=\n\t" \
    "qk_Gc275_%=:\n\t" \
    "s_waitcnt lgkmcnt(5)\n\t" \
    "v_smfmac_f32_16x16x64_bf16 v[72:75], v[48:51], a[48:55], v10\n\t" \
    "global_store_dword v14, v25, s[36:37] sc1\n\t" \
    "s_waitcnt lgkmcnt(4)\n\t" \
    "v_smfmac_f32_16x16x64_bf16 v[72:75], v[52:55], a[56:63], v10\n\t" \
    "s_waitcnt lgkmcnt(3)\n\t" \
    "v_smfmac_f32_16x16x64_bf16 v[72:75], v[56:59], a[0:7], v10\n\t" \
    "s_waitcnt lgkmcnt(2)\n\t" \
    "v_smfmac_f32_16x16x64_bf16 v[72:75], v[60:63], a[8:15], v10\n\t" \
    "s_waitcnt lgkmcnt(1)\n\t" \
    "v_smfmac_f32_16x16x64_bf16 v[72:75], v[64:67], a[16:23], v10\n\t" \
    "s_waitcnt lgkmcnt(0)\n\t" \
    "v_smfmac_f32_16x16x64_bf16 v[72:75], v[68:71], a[24:31], v10\n\t" \
    "v_smfmac_f32_16x16x64_bf16 v[76:79], v[48:51], a[120:127], v10\n\t" \
    "s_nop 1\n\t" \
    "s_nop 0\n\t" \
    "v_smfmac_f32_16x16x64_bf16 v[76:79], v[52:55], a[128:135], v10\n\t" \
    "s_nop 1\n\t" \
    "s_nop 0\n\t" \
    "v_smfmac_f32_16x16x64_bf16 v[76:79], v[56:59], a[72:79], v10\n\t" \
    "v_add_f32 v72, v72, v74\n\t" \
    "v_smfmac_f32_16x16x64_bf16 v[76:79], v[60:63], a[80:87], v10\n\t" \
    "v_add_f32 v73, v73, v75\n\t" \
    "v_smfmac_f32_16x16x64_bf16 v[76:79], v[64:67], a[88:95], v10\n\t" \
    "v_cvt_pk_bf16_f32 v156, v72, v73\n\t" \
    "v_smfmac_f32_16x16x64_bf16 v[76:79], v[68:71], a[96:103], v10\n\t" \
    "ds_write_b32 v12, v156 offset:0\n\t" \
    "v_smfmac_f32_16x16x64_bf16 v[80:83], v[48:51], a[192:199], v10\n\t" \
    "s_nop 1\n\t" \
    "s_nop 0\n\t" \
    "v_smfmac_f32_16x16x64_bf16 v[80:83], v[52:55], a[200:207], v10\n\t" \
    "s_nop 1\n\t" \
    "s_nop 0\n\t" \
    "v_smfmac_f32_16x16x64_bf16 v[80:83], v[56:59], a[144:151], v10\n\t" \
    "v_add_f32 v76, v76, v78\n\t" \
    "v_smfmac_f32_16x16x64_bf16 v[80:83], v[60:63], a[152:159], v10\n\t" \
    "v_add_f32 v77, v77, v79\n\t" \
    "v_smfmac_f32_16x16x64_bf16 v[80:83], v[64:67], a[160:167], v10\n\t" \
    "v_cvt_pk_bf16_f32 v157, v76, v77\n\t" \
    "v_smfmac_f32_16x16x64_bf16 v[80:83], v[68:71], a[168:175], v10\n\t" \
    "ds_write_b32 v12, v157 offset:64\n\t" \
    "v_smfmac_f32_16x16x64_bf16 v[84:87], v[48:51], v[232:239], v10\n\t" \
    "s_nop 1\n\t" \
    "s_nop 0\n\t" \
    "v_smfmac_f32_16x16x64_bf16 v[84:87], v[52:55], v[240:247], v10\n\t" \
    "s_nop 1\n\t" \
    "s_nop 0\n\t" \
    "v_smfmac_f32_16x16x64_bf16 v[84:87], v[56:59], a[216:223], v10\n\t" \
    "v_add_f32 v80, v80, v82\n\t" \
    "v_smfmac_f32_16x16x64_bf16 v[84:87], v[60:63], a[224:231], v10\n\t" \
    "v_add_f32 v81, v81, v83\n\t" \
    "v_smfmac_f32_16x16x64_bf16 v[84:87], v[64:67], a[232:239], v10\n\t" \
    "v_cvt_pk_bf16_f32 v158, v80, v81\n\t" \
    "v_smfmac_f32_16x16x64_bf16 v[84:87], v[68:71], a[240:247], v10\n\t" \
    "ds_write_b32 v16, v158 offset:0\n\t" \
    "s_nop 6\n\t" \
    "v_pk_add_f32 v[84:85], v[84:85], v[86:87]\n\t" \
    "v_cvt_pk_bf16_f32 v159, v84, v85\n\t" \
    "ds_write_b32 v16, v159 offset:256\n\t" \
    "s_waitcnt lgkmcnt(0)\n\t" \
    "s_barrier\n\t" \
    "s_sleep 1\n\t" \
    "ds_read_b64_tr_b16 v[140:141], v17 offset:0\n\t" \
    "ds_read_b64_tr_b16 v[142:143], v17 offset:256\n\t" \
    "ds_read_b64_tr_b16 v[144:145], v17 offset:512\n\t" \
    "ds_read_b64_tr_b16 v[146:147], v17 offset:768\n\t" \
    "ds_read_b64_tr_b16 v[148:149], v17 offset:1024\n\t" \
    "ds_read_b64_tr_b16 v[150:151], v17 offset:1280\n\t" \
    "ds_read_b64_tr_b16 v[152:153], v17 offset:1536\n\t" \
    "ds_read_b64_tr_b16 v[154:155], v17 offset:1792\n\t" \
    "s_waitcnt lgkmcnt(0)\n\t" \
    "s_cmp_eq_u32 s65, 63\n\t" \
    "s_cbranch_scc1 qk_last62_%=\n\t" \
    "s_memtime s[54:55]\n\t" \
    "s_waitcnt lgkmcnt(0)\n\t" \
    "s_cmp_ge_u32 s65, 58\n\t" \
    "s_cselect_b32 s69, 0, 940\n\t" \
    "s_add_u32 s79, s54, s69\n\t" \
    "v_mov_b32 v72, 0\n\t" \
    "v_mov_b32 v73, 0\n\t" \
    "v_mov_b32 v74, 0\n\t" \
    "v_mov_b32 v75, 0\n\t" \
    "v_mov_b32 v76, 0\n\t" \
    "v_mov_b32 v77, 0\n\t" \
    "v_mov_b32 v78, 0\n\t" \
    "v_mov_b32 v79, 0\n\t" \
    "v_mov_b32 v80, 0\n\t" \
    "v_mov_b32 v81, 0\n\t" \
    "v_mov_b32 v82, 0\n\t" \
    "v_mov_b32 v83, 0\n\t" \
    "v_mov_b32 v84, 0\n\t" \
    "v_mov_b32 v85, 0\n\t" \
    "v_mov_b32 v86, 0\n\t" \
    "v_mov_b32 v87, 0\n\t" \
    "s_mov_b32 s70, s79\n\t" \
    "qk_W77_%=:\n\t" \
    "s_memtime s[56:57]\n\t" \
    "s_waitcnt lgkmcnt(0)\n\t" \
    "s_sub_u32 s69, s70, s56\n\t" \
    "s_cmp_gt_i32 s69, 24\n\t" \
    "s_cbranch_scc1 qk_W77_%=\n\t" \
    "s_mov_b64 exec, s[58:59]\n\t" \
    "global_store_dwordx4 v0, v[140:143], s[40:41] offset:0 sc1\n\t" \
    "global_store_dwordx4 v0, v[144:147], s[40:41] offset:1024 sc1\n\t" \
    "global_store_dwordx4 v0, v[148:151], s[40:41] offset:2048 sc1\n\t" \
    "global_store_dwordx4 v0, v[152:155], s[40:41] offset:3072 sc1\n\t" \
    "s_mov_b64 exec, -1\n\t" \
    "s_add_u32 s65, s65, 1\n\t" \
    "v_add_u32 v31, 1, v31\n\t" \
    "s_add_u32 s69, s65, 0\n\t" \
    "s_bitcmp1_b32 s69, 0\n\t" \
    "s_cselect_b32 s70, s63, s62\n\t" \
    "s_bfe_u32 s69, s69, 0x20001\n\t" \
    "s_lshl_b32 s69, s69, 19\n\t" \
    "s_add_u32 s69, s69, s70\n\t" \
    "s_add_u32 s46, s36, s69\n\t" \
    "s_addc_u32 s47, s37, 0\n\t" \
    "s_mov_b64 s[42:43], s[46:47]\n\t" \
    "s_add_u32 s69, s65, 0\n\t" \
    "s_lshr_b32 s69, s69, 1\n\t" \
    "s_xor_b32 s69, s69, s97\n\t" \
    "s_and_b32 s69, s69, 1\n\t" \
    "s_lshl_b32 s69, s69, 14\n\t" \
    "s_or_b32 s46, s46, s69\n\t" \
    "s_mul_i32 s70, s67, 7\n\t" \
    "s_add_u32 s69, s65, 0\n\t" \
    "s_xor_b32 s69, s69, s70\n\t" \
    "s_and_b32 s69, s69, 63\n\t" \
    "s_lshl_b32 s69, s69, 12\n\t" \
    "s_mul_i32 s70, s67, 262144\n\t" \
    "s_add_u32 s69, s69, s70\n\t" \
    "s_add_u32 s69, s69, 39845888\n\t" \
    "s_add_u32 s40, s36, s69\n\t" \
    "s_addc_u32 s41, s37, 0\n\t" \
    "s_sub_u32 s69, s65, 1\n\t" \
    "v_mov_b32 v25, s69\n\t" \
    "s_waitcnt lgkmcnt(0)\n\t" \
    "s_barrier\n\t" \
    "ds_read_b32 v160, v11 offset:20480\n\t" \
    "s_waitcnt lgkmcnt(0)\n\t" \
    "v_readfirstlane_b32 s79, v160\n\t" \
    "s_nop 1\n\t" \
    "s_branch qk_go67_%=\n\t" \
    "qk_Gbad72_%=:\n\t" \
    "s_waitcnt vmcnt(0) lgkmcnt(0)\n\t" \
    "s_nop 7\n\t" \
    "v_mov_b32 v72, 0\n\t" \
    "v_mov_b32 v73, 0\n\t" \
    "v_mov_b32 v74, 0\n\t" \
    "v_mov_b32 v75, 0\n\t" \
    "v_mov_b32 v76, 0\n\t" \
    "v_mov_b32 v77, 0\n\t" \
    "v_mov_b32 v78, 0\n\t" \
    "v_mov_b32 v79, 0\n\t" \
    "v_mov_b32 v80, 0\n\t" \
    "v_mov_b32 v81, 0\n\t" \
    "v_mov_b32 v82, 0\n\t" \
    "v_mov_b32 v83, 0\n\t" \
    "v_mov_b32 v84, 0\n\t" \
    "v_mov_b32 v85, 0\n\t" \
    "v_mov_b32 v86, 0\n\t" \
    "v_mov_b32 v87, 0\n\t" \
    "s_cmp_eq_u32 s68, 0\n\t" \
    "s_cbranch_scc1 qk_F78_%=\n\t" \
    "s_sub_u32 s68, s68, 1\n\t" \
    "s_branch qk_G71_%=\n\t" \
    "qk_F78_%=:\n\t" \
    "v_mov_b32 v160, 6095104\n\t" \
    "v_mov_b32 v161, 1\n\t" \
    "global_store_dword v160, v161, s[36:37] sc1\n\t" \
    "s_waitcnt vmcnt(0)\n\t" \
    "s_branch qk_Gc74_%=\n\t" \
    "qk_Gbad273_%=:\n\t" \
    "global_load_dwordx4 v[44:47], v2, s[46:47] sc1\n\t" \
    "s_waitcnt vmcnt(0)\n\t" \
    "v_max3_u32 v160, v44, v45, v46\n\t" \
    "v_max_u32 v160, v160, v47\n\t" \
    "v_cmp_eq_u32 vcc, -1, v160\n\t" \
    "s_cbranch_vccz qk_Gre276_%=\n\t" \
    "s_cmp_eq_u32 s68, 0\n\t" \
    "s_cbranch_scc1 qk_F79_%=\n\t" \
    "s_sub_u32 s68, s68, 1\n\t" \
    "s_sleep 1\n\t" \
    "s_branch qk_Gbad273_%=\n\t" \
    "qk_F79_%=:\n\t" \
    "v_mov_b32 v160, 6095104\n\t" \
    "v_mov_b32 v161, 2\n\t" \
    "global_store_dword v160, v161, s[36:37] sc1\n\t" \
    "s_waitcnt vmcnt(0)\n\t" \
    "s_branch qk_Gre276_%=\n\t" \
    "qk_Gre276_%=:\n\t" \
    "v_mov_b32 v72, 0\n\t" \
    "v_mov_b32 v73, 0\n\t" \
    "v_mov_b32 v74, 0\n\t" \
    "v_mov_b32 v75, 0\n\t" \
    "v_mov_b32 v76, 0\n\t" \
    "v_mov_b32 v77, 0\n\t" \
    "v_mov_b32 v78, 0\n\t" \
    "v_mov_b32 v79, 0\n\t" \
    "v_mov_b32 v80, 0\n\t" \
    "v_mov_b32 v81, 0\n\t" \
    "v_mov_b32 v82, 0\n\t" \
    "v_mov_b32 v83, 0\n\t" \
    "v_mov_b32 v84, 0\n\t" \
    "v_mov_b32 v85, 0\n\t" \
    "v_mov_b32 v86, 0\n\t" \
    "v_mov_b32 v87, 0\n\t" \
    "v_smfmac_f32_16x16x64_bf16 v[72:75], v[36:39], a[32:39], v10\n\t" \
    "v_smfmac_f32_16x16x64_bf16 v[76:79], v[36:39], a[104:111], v10\n\t" \
    "v_smfmac_f32_16x16x64_bf16 v[80:83], v[36:39], a[176:183], v10\n\t" \
    "v_smfmac_f32_16x16x64_bf16 v[84:87], v[36:39], a[248:255], v10\n\t" \
    "v_smfmac_f32_16x16x64_bf16 v[72:75], v[40:43], a[40:47], v10\n\t" \
    "v_smfmac_f32_16x16x64_bf16 v[76:79], v[40:43], a[112:119], v10\n\t" \
    "v_smfmac_f32_16x16x64_bf16 v[80:83], v[40:43], a[184:191], v10\n\t" \
    "v_smfmac_f32_16x16x64_bf16 v[84:87], v[40:43], v[224:231], v10\n\t" \
    "v_smfmac_f32_16x16x64_bf16 v[72:75], v[44:47], a[64:71], v10\n\t" \
    "v_smfmac_f32_16x16x64_bf16 v[76:79], v[44:47], a[136:143], v10\n\t" \
    "v_smfmac_f32_16x16x64_bf16 v[80:83], v[44:47], a[208:215], v10\n\t" \
    "v_smfmac_f32_16x16x64_bf16 v[84:87], v[44:47], v[248:255], v10\n\t" \
    "s_branch qk_Gc275_%=\n\t" \
    "qk_last62_%=:\n\t" \
    "s_mov_b64 exec, s[58:59]\n\t" \
    "global_store_dwordx4 v0, v[140:143], s[40:41] offset:0 sc1\n\t" \
    "global_store_dwordx4 v0, v[144:147], s[40:41] offset:1024 sc1\n\t" \
    "global_store_dwordx4 v0, v[148:151], s[40:41] offset:2048 sc1\n\t" \
    "global_store_dwordx4 v0, v[152:155], s[40:41] offset:3072 sc1\n\t" \
    "s_mov_b64 exec, -1\n\t" \
    "s_endpgm\n\t" \
    "qk_loop13_%=:\n\t" \
    "v_mov_b32 v72, 0\n\t" \
    "v_mov_b32 v73, 0\n\t" \
    "v_mov_b32 v74, 0\n\t" \
    "v_mov_b32 v75, 0\n\t" \
    "v_mov_b32 v76, 0\n\t" \
    "v_mov_b32 v77, 0\n\t" \
    "v_mov_b32 v78, 0\n\t" \
    "v_mov_b32 v79, 0\n\t" \
    "v_mov_b32 v80, 0\n\t" \
    "v_mov_b32 v81, 0\n\t" \
    "v_mov_b32 v82, 0\n\t" \
    "v_mov_b32 v83, 0\n\t" \
    "v_mov_b32 v84, 0\n\t" \
    "v_mov_b32 v85, 0\n\t" \
    "v_mov_b32 v86, 0\n\t" \
    "v_mov_b32 v87, 0\n\t" \
    "v_add_u32 v31, 1, v31\n\t" \
    "s_add_u32 s69, s65, 0\n\t" \
    "s_bitcmp1_b32 s69, 0\n\t" \
    "s_cselect_b32 s70, s63, s62\n\t" \
    "s_bfe_u32 s69, s69, 0x20001\n\t" \
    "s_lshl_b32 s69, s69, 19\n\t" \
    "s_add_u32 s69, s69, s70\n\t" \
    "s_add_u32 s46, s36, s69\n\t" \
    "s_addc_u32 s47, s37, 0\n\t" \
    "s_mov_b64 s[42:43], s[46:47]\n\t" \
    "s_add_u32 s69, s65, 0\n\t" \
    "s_lshr_b32 s69, s69, 1\n\t" \
    "s_xor_b32 s69, s69, s97\n\t" \
    "s_and_b32 s69, s69, 1\n\t" \
    "s_lshl_b32 s69, s69, 14\n\t" \
    "s_or_b32 s46, s46, s69\n\t" \
    "qk_go85_%=:\n\t" \
    "s_mov_b32 s70, s79\n\t" \
    "s_sub_u32 s70, s70, 100\n\t" \
    "s_getpc_b64 s[30:31]\n\t" \
    "qk_Wp87_%=:\n\t" \
    "s_add_u32 s30, s30, qk_We88_%=-qk_Wp87_%=\n\t" \
    "s_addc_u32 s31, s31, 0\n\t" \
    "qk_W86_%=:\n\t" \
    "s_memtime s[56:57]\n\t" \
    "s_waitcnt lgkmcnt(0)\n\t" \
    "s_sub_u32 s69, s70, s56\n\t" \
    "s_cmp_gt_i32 s69, 111\n\t" \
    "s_cbranch_scc1 qk_W86_%=\n\t" \
    "s_max_i32 s69, s69, 0\n\t" \
    "s_lshr_b32 s69, s69, 1\n\t" \
    "s_and_b32 s69, s69, 0x3c\n\t" \
    "s_sub_u32 s30, s30, s69\n\t" \
    "s_subb_u32 s31, s31, 0\n\t" \
    "s_setpc_b64 s[30:31]\n\t" \
    ".p2align 6\n\t" \
    "s_nop 0\n\t" \
    "s_nop 0\n\t" \
    "s_nop 0\n\t" \
    "s_nop 1\n\t" \
    "s_nop 1\n\t" \
    "s_nop 1\n\t" \
    "s_nop 1\n\t" \
    "s_nop 1\n\t" \
    "s_nop 1\n\t" \
    "s_nop 1\n\t" \
    "s_nop 1\n\t" \
    "s_nop 1\n\t" \
    "s_nop 1\n\t" \
    "s_nop 1\n\t" \
    "s_nop 1\n\t" \
    "s_nop 1\n\t" \
    "qk_We88_%=:\n\t" \
    "qk_G89_%=:\n\t" \
    "global_load_dwordx4 v[36:39], v1, s[46:47] sc1\n\t" \
    "s_nop 9\n\t" \
    "global_load_dwordx4 v[40:43], v1, s[46:47] offset:1024 sc1\n\t" \
    "s_nop 9\n\t" \
    "global_load_dwordx4 v[44:47], v2, s[46:47] sc1\n\t" \
    "s_mul_hi_u32 s69, s84, s78\n\t" \
    "s_add_u32 s33, s74, s69\n\t" \
    "s_lshr_b32 s69, s90, 16\n\t" \
    "s_add_u32 s34, s33, s69\n\t" \
    "s_and_b32 s69, s90, 0xffff\n\t" \
    "s_sub_u32 s35, s80, s74\n\t" \
    "s_sub_u32 s35, s35, s69\n\t" \
    "s_add_u32 s99, s35, 1280\n\t" \
    "s_add_u32 s98, s34, 600\n\t" \
    "s_waitcnt vmcnt(2)\n\t" \
    "v_smfmac_f32_16x16x64_bf16 v[72:75], v[36:39], a[48:55], v10\n\t" \
    "ds_write_b128 v0, v[36:39] offset:6144\n\t" \
    "v_smfmac_f32_16x16x64_bf16 v[76:79], v[36:39], a[120:127], v10\n\t" \
    "v_max3_u32 v160, v36, v37, v38\n\t" \
    "v_smfmac_f32_16x16x64_bf16 v[80:83], v[36:39], a[192:199], v10\n\t" \
    "v_smfmac_f32_16x16x64_bf16 v[84:87], v[36:39], v[232:239], v10\n\t" \
    "s_waitcnt vmcnt(1)\n\t" \
    "ds_write_b128 v0, v[40:43] offset:7168\n\t" \
    "v_smfmac_f32_16x16x64_bf16 v[72:75], v[40:43], a[56:63], v10\n\t" \
    "v_max3_u32 v160, v160, v39, v40\n\t" \
    "v_max3_u32 v160, v160, v41, v42\n\t" \
    "v_smfmac_f32_16x16x64_bf16 v[76:79], v[40:43], a[128:135], v10\n\t" \
    "v_max_u32 v160, v160, v43\n\t" \
    "v_cmp_eq_u32 vcc, -1, v160\n\t" \
    "v_smfmac_f32_16x16x64_bf16 v[80:83], v[40:43], a[200:207], v10\n\t" \
    "v_smfmac_f32_16x16x64_bf16 v[84:87], v[40:43], v[240:247], v10\n\t" \
    "s_cbranch_vccnz qk_Gbad90_%=\n\t" \
    "qk_Gc92_%=:\n\t" \
    "s_waitcnt lgkmcnt(0)\n\t" \
    "s_barrier\n\t" \
    "s_memtime s[56:57]\n\t" \
    "ds_read_b128 v[48:51], v0 offset:0\n\t" \
    "ds_read_b128 v[52:55], v0 offset:1024\n\t" \
    "s_waitcnt vmcnt(0)\n\t" \
    "v_smfmac_f32_16x16x64_bf16 v[72:75], v[44:47], a[64:71], v10\n\t" \
    "ds_read_b128 v[56:59], v0 offset:2048\n\t" \
    "v_max3_u32 v160, v44, v45, v46\n\t" \
    "v_smfmac_f32_16x16x64_bf16 v[76:79], v[44:47], a[136:143], v10\n\t" \
    "ds_read_b128 v[60:63], v0 offset:3072\n\t" \
    "v_max_u32 v160, v160, v47\n\t" \
    "v_smfmac_f32_16x16x64_bf16 v[80:83], v[44:47], a[208:215], v10\n\t" \
    "ds_read_b128 v[64:67], v0 offset:4096\n\t" \
    "v_cmp_eq_u32 vcc, -1, v160\n\t" \
    "v_smfmac_f32_16x16x64_bf16 v[84:87], v[44:47], v[248:255], v10\n\t" \
    "ds_read_b128 v[68:71], v0 offset:5120\n\t" \
    "s_cbranch_vccnz qk_Gbad291_%=\n\t" \
    "qk_Gc293_%=:\n\t" \
    "s_sub_u32 s69, s56, s73\n\t" \
    "s_mul_hi_u32 s69, s69, s78\n\t" \
    "s_cmp_gt_i32 s69, s35\n\t" \
    "s_waitcnt lgkmcnt(5)\n\t" \
    "v_smfmac_f32_16x16x64_bf16 v[72:75], v[48:51], a[0:7], v10\n\t" \
    "s_cselect_b32 s70, s34, s33\n\t" \
    "s_cmp_gt_i32 s69, s99\n\t" \
    "s_cselect_b32 s70, s98, s70\n\t" \
    "s_waitcnt lgkmcnt(4)\n\t" \
    "v_smfmac_f32_16x16x64_bf16 v[72:75], v[52:55], a[8:15], v10\n\t" \
    "s_add_u32 s70, s69, s70\n\t" \
    "v_and_b32_e64 v162, -2, s70\n\t" \
    "s_waitcnt lgkmcnt(3)\n\t" \
    "v_smfmac_f32_16x16x64_bf16 v[72:75], v[56:59], a[16:23], v10\n\t" \
    "s_mov_b64 exec, s[44:45]\n\t" \
    "global_store_dword v21, v162, s[42:43] sc1\n\t" \
    "s_mov_b64 exec, -1\n\t" \
    "s_waitcnt lgkmcnt(2)\n\t" \
    "v_smfmac_f32_16x16x64_bf16 v[72:75], v[60:63], a[24:31], v10\n\t" \
    "s_waitcnt lgkmcnt(1)\n\t" \
    "v_smfmac_f32_16x16x64_bf16 v[72:75], v[64:67], a[32:39], v10\n\t" \
    "s_waitcnt lgkmcnt(0)\n\t" \
    "v_smfmac_f32_16x16x64_bf16 v[72:75], v[68:71], a[40:47], v10\n\t" \
    "v_smfmac_f32_16x16x64_bf16 v[76:79], v[48:51], a[72:79], v10\n\t" \
    "s_nop 1\n\t" \
    "s_nop 0\n\t" \
    "v_smfmac_f32_16x16x64_bf16 v[76:79], v[52:55], a[80:87], v10\n\t" \
    "s_nop 1\n\t" \
    "s_nop 0\n\t" \
    "v_smfmac_f32_16x16x64_bf16 v[76:79], v[56:59], a[88:95], v10\n\t" \
    "v_add_f32 v72, v72, v74\n\t" \
    "v_smfmac_f32_16x16x64_bf16 v[76:79], v[60:63], a[96:103], v10\n\t" \
    "v_add_f32 v73, v73, v75\n\t" \
    "v_smfmac_f32_16x16x64_bf16 v[76:79], v[64:67], a[104:111], v10\n\t" \
    "v_cvt_pk_bf16_f32 v156, v72, v73\n\t" \
    "v_smfmac_f32_16x16x64_bf16 v[76:79], v[68:71], a[112:119], v10\n\t" \
    "ds_write_b32 v12, v156 offset:0\n\t" \
    "v_smfmac_f32_16x16x64_bf16 v[80:83], v[48:51], a[144:151], v10\n\t" \
    "s_nop 1\n\t" \
    "s_nop 0\n\t" \
    "v_smfmac_f32_16x16x64_bf16 v[80:83], v[52:55], a[152:159], v10\n\t" \
    "s_nop 1\n\t" \
    "s_nop 0\n\t" \
    "v_smfmac_f32_16x16x64_bf16 v[80:83], v[56:59], a[160:167], v10\n\t" \
    "v_add_f32 v76, v76, v78\n\t" \
    "v_smfmac_f32_16x16x64_bf16 v[80:83], v[60:63], a[168:175], v10\n\t" \
    "v_add_f32 v77, v77, v79\n\t" \
    "v_smfmac_f32_16x16x64_bf16 v[80:83], v[64:67], a[176:183], v10\n\t" \
    "v_cvt_pk_bf16_f32 v157, v76, v77\n\t" \
    "v_smfmac_f32_16x16x64_bf16 v[80:83], v[68:71], a[184:191], v10\n\t" \
    "ds_write_b32 v12, v157 offset:64\n\t" \
    "v_smfmac_f32_16x16x64_bf16 v[84:87], v[48:51], a[216:223], v10\n\t" \
    "s_nop 1\n\t" \
    "s_nop 0\n\t" \
    "v_smfmac_f32_16x16x64_bf16 v[84:87], v[52:55], a[224:231], v10\n\t" \
    "s_nop 1\n\t" \
    "s_nop 0\n\t" \
    "v_smfmac_f32_16x16x64_bf16 v[84:87], v[56:59], a[232:239], v10\n\t" \
    "v_add_f32 v80, v80, v82\n\t" \
    "v_smfmac_f32_16x16x64_bf16 v[84:87], v[60:63], a[240:247], v10\n\t" \
    "v_add_f32 v81, v81, v83\n\t" \
    "v_smfmac_f32_16x16x64_bf16 v[84:87], v[64:67], a[248:255], v10\n\t" \
    "v_cvt_pk_bf16_f32 v158, v80, v81\n\t" \
    "v_smfmac_f32_16x16x64_bf16 v[84:87], v[68:71], v[224:231], v10\n\t" \
    "ds_write_b32 v16, v158 offset:0\n\t" \
    "s_nop 6\n\t" \
    "v_pk_add_f32 v[84:85], v[84:85], v[86:87]\n\t" \
    "v_cvt_pk_bf16_f32 v159, v84, v85\n\t" \
    "ds_write_b32 v16, v159 offset:256\n\t" \
    "s_waitcnt lgkmcnt(0)\n\t" \
    "s_barrier\n\t" \
    "s_cmp_eq_u32 s65, 63\n\t" \
    "s_cbranch_scc1 qk_last80_%=\n\t" \
    "s_mov_b64 s[44:45], 0xfffff\n\t" \
    "s_sub_u32 s70, s80, s74\n\t" \
    "s_max_i32 s70, s70, 0\n\t" \
    "s_min_i32 s70, s70, 16000\n\t" \
    "s_lshl_b32 s70, s70, 1\n\t" \
    "s_mul_hi_u32 s70, s70, s77\n\t" \
    "s_add_u32 s70, s70, s73\n\t" \
    "qk_W95_%=:\n\t" \
    "s_memtime s[56:57]\n\t" \
    "s_waitcnt lgkmcnt(0)\n\t" \
    "s_sub_u32 s69, s70, s56\n\t" \
    "s_cmp_gt_i32 s69, 24\n\t" \
    "s_cbranch_scc1 qk_W95_%=\n\t" \
    "global_load_dword v160, v20, s[46:47] sc1\n\t" \
    "s_mov_b32 s71, 3\n\t" \
    "qk_E99_%=:\n\t" \
    "s_memtime s[54:55]\n\t" \
    "s_nop 0\n\t" \
    "s_memrealtime s[4:5]\n\t" \
    "s_nop 0\n\t" \
    "s_memrealtime s[6:7]\n\t" \
    "s_nop 0\n\t" \
    "s_memrealtime s[8:9]\n\t" \
    "s_nop 0\n\t" \
    "s_memrealtime s[10:11]\n\t" \
    "s_nop 0\n\t" \
    "s_memrealtime s[12:13]\n\t" \
    "s_nop 0\n\t" \
    "s_memrealtime s[14:15]\n\t" \
    "s_nop 0\n\t" \
    "s_memrealtime s[16:17]\n\t" \
    "s_nop 0\n\t" \
    "s_memrealtime s[18:19]\n\t" \
    "s_nop 0\n\t" \
    "s_memrealtime s[20:21]\n\t" \
    "s_nop 0\n\t" \
    "s_memrealtime s[22:23]\n\t" \
    "s_nop 0\n\t" \
    "s_memrealtime s[24:25]\n\t" \
    "s_nop 0\n\t" \
    "s_memrealtime s[26:27]\n\t" \
    "s_nop 0\n\t" \
    "s_memrealtime s[28:29]\n\t" \
    "s_nop 0\n\t" \
    "s_memtime s[56:57]\n\t" \
    "s_mov_b32 s70, 1\n\t" \
    "s_sub_u32 s71, s71, 1\n\t" \
    "s_cselect_b32 s69, 0, -1\n\t" \
    "s_waitcnt lgkmcnt(0)\n\t" \
    "s_sub_u32 s54, s56, s54\n\t" \
    "s_and_b32 s54, s54, s69\n\t" \
    "s_cmp_gt_u32 s54, 124\n\t" \
    "s_cbranch_scc1 qk_E99_%=\n\t" \
    "s_cmp_eq_u32 s4, s28\n\t" \
    "s_addc_u32 s70, s70, 0\n\t" \
    "s_cmp_eq_u32 s6, s28\n\t" \
    "s_addc_u32 s70, s70, 0\n\t" \
    "s_cmp_eq_u32 s8, s28\n\t" \
    "s_addc_u32 s70, s70, 0\n\t" \
    "s_cmp_eq_u32 s10, s28\n\t" \
    "s_addc_u32 s70, s70, 0\n\t" \
    "s_cmp_eq_u32 s12, s28\n\t" \
    "s_addc_u32 s70, s70, 0\n\t" \
    "s_cmp_eq_u32 s14, s28\n\t" \
    "s_addc_u32 s70, s70, 0\n\t" \
    "s_cmp_eq_u32 s16, s28\n\t" \
    "s_addc_u32 s70, s70, 0\n\t" \
    "s_cmp_eq_u32 s18, s28\n\t" \
    "s_addc_u32 s70, s70, 0\n\t" \
    "s_cmp_eq_u32 s20, s28\n\t" \
    "s_addc_u32 s70, s70, 0\n\t" \
    "s_cmp_eq_u32 s22, s28\n\t" \
    "s_addc_u32 s70, s70, 0\n\t" \
    "s_cmp_eq_u32 s24, s28\n\t" \
    "s_addc_u32 s70, s70, 0\n\t" \
    "s_cmp_eq_u32 s26, s28\n\t" \
    "s_addc_u32 s70, s70, 0\n\t" \
    "s_mov_b32 s75, s73\n\t" \
    "s_mov_b32 s76, s74\n\t" \
    "s_lshl_b32 s70, s70, 3\n\t" \
    "s_sub_u32 s73, s56, s70\n\t" \
    "s_lshl_b32 s74, s28, 4\n\t" \
    "s_sub_u32 s69, s73, s75\n\t" \
    "s_sub_u32 s70, s74, s76\n\t" \
    "s_sub_u32 s71, s70, 1600\n\t" \
    "s_cmp_lt_u32 s71, 400000\n\t" \
    "s_cbranch_scc0 qk_K101_%=\n\t" \
    "v_cvt_f32_u32 v161, s69\n\t" \
    "v_cvt_f32_u32 v162, s70\n\t" \
    "v_rcp_f32 v163, v161\n\t" \
    "v_rcp_f32 v164, v162\n\t" \
    "v_mul_f32 v163, v163, v162\n\t" \
    "v_mul_f32 v164, v164, v161\n\t" \
    "v_mul_f32 v163, 0x4f800000, v163\n\t" \
    "v_mul_f32 v164, 0x4f000000, v164\n\t" \
    "v_cvt_u32_f32 v163, v163\n\t" \
    "v_cvt_u32_f32 v164, v164\n\t" \
    "v_readfirstlane_b32 s69, v163\n\t" \
    "v_readfirstlane_b32 s70, v164\n\t" \
    "s_nop 1\n\t" \
    "s_sub_u32 s69, s69, s78\n\t" \
    "s_ashr_i32 s69, s69, 2\n\t" \
    "s_add_u32 s78, s78, s69\n\t" \
    "s_sub_u32 s70, s70, s77\n\t" \
    "s_ashr_i32 s70, s70, 2\n\t" \
    "s_add_u32 s77, s77, s70\n\t" \
    "qk_K101_%=:\n\t" \
    "s_waitcnt vmcnt(0)\n\t" \
    "v_cmp_eq_u32 vcc, -1, v160\n\t" \
    "s_cbranch_vccz qk_Hok97_%=\n\t" \
    "qk_H96_%=:\n\t" \
    "global_load_dword v160, v20, s[46:47] sc1\n\t" \
    "s_waitcnt vmcnt(0)\n\t" \
    "v_cmp_eq_u32 vcc, -1, v160\n\t" \
    "s_cbranch_vccz qk_Hc98_%=\n\t" \
    "s_cmp_eq_u32 s68, 0\n\t" \
    "s_cbranch_scc1 qk_F102_%=\n\t" \
    "s_sub_u32 s68, s68, 1\n\t" \
    "s_sleep 1\n\t" \
    "s_branch qk_H96_%=\n\t" \
    "qk_F102_%=:\n\t" \
    "v_mov_b32 v161, 6095104\n\t" \
    "v_mov_b32 v162, 5\n\t" \
    "global_store_dword v161, v162, s[36:37] sc1\n\t" \
    "s_waitcnt vmcnt(0)\n\t" \
    "s_branch qk_Hc98_%=\n\t" \
    "qk_Hc98_%=:\n\t" \
    "qk_Hok97_%=:\n\t" \
    "v_subrev_u32 v160, s80, v160\n\t" \
    "v_and_b32 v20, -5, v20\n\t" \
    "s_nop 0\n\t" \
    "v_max_i32_dpp v160, v160, v160 row_ror:1 row_mask:0xf bank_mask:0xf\n\t" \
    "s_nop 1\n\t" \
    "v_max_i32_dpp v160, v160, v160 row_ror:2 row_mask:0xf bank_mask:0xf\n\t" \
    "s_nop 1\n\t" \
    "v_max_i32_dpp v160, v160, v160 row_ror:4 row_mask:0xf bank_mask:0xf\n\t" \
    "s_nop 1\n\t" \
    "v_max_i32_dpp v160, v160, v160 row_ror:8 row_mask:0xf bank_mask:0xf\n\t" \
    "s_nop 0\n\t" \
    "v_readlane_b32 s69, v160, 0\n\t" \
    "v_readlane_b32 s71, v160, 16\n\t" \
    "v_readlane_b32 s70, v160, 32\n\t" \
    "v_readlane_b32 s81, v160, 48\n\t" \
    "s_max_i32 s69, s69, s71\n\t" \
    "s_nop 0\n\t" \
    "s_max_i32 s71, s70, s81\n\t" \
    "s_max_i32 s69, s69, s71\n\t" \
    "s_add_u32 s81, s69, s80\n\t" \
    "v_mov_b32 v72, 0\n\t" \
    "v_mov_b32 v73, 0\n\t" \
    "v_mov_b32 v74, 0\n\t" \
    "v_mov_b32 v75, 0\n\t" \
    "v_mov_b32 v76, 0\n\t" \
    "v_mov_b32 v77, 0\n\t" \
    "v_mov_b32 v78, 0\n\t" \
    "v_mov_b32 v79, 0\n\t" \
    "v_mov_b32 v80, 0\n\t" \
    "v_mov_b32 v81, 0\n\t" \
    "v_mov_b32 v82, 0\n\t" \
    "v_mov_b32 v83, 0\n\t" \
    "v_mov_b32 v84, 0\n\t" \
    "v_mov_b32 v85, 0\n\t" \
    "v_mov_b32 v86, 0\n\t" \
    "v_mov_b32 v87, 0\n\t" \
    "s_cmp_lg_u32 s65, 0\n\t" \
    "s_cbranch_scc1 qk_Oc105_%=\n\t" \
    "s_add_u32 s79, s81, 240\n\t" \
    "s_sub_u32 s70, s79, s74\n\t" \
    "s_max_i32 s70, s70, 0\n\t" \
    "s_min_i32 s70, s70, 16000\n\t" \
    "s_lshl_b32 s70, s70, 1\n\t" \
    "s_mul_hi_u32 s70, s70, s77\n\t" \
    "s_add_u32 s70, s70, s73\n\t" \
    "qk_W106_%=:\n\t" \
    "s_memtime s[56:57]\n\t" \
    "s_waitcnt lgkmcnt(0)\n\t" \
    "s_sub_u32 s69, s70, s56\n\t" \
    "s_cmp_gt_i32 s69, 24\n\t" \
    "s_cbranch_scc1 qk_W106_%=\n\t" \
    "global_load_dword v160, v35, s[46:47] sc1\n\t" \
    "s_waitcnt vmcnt(0)\n\t" \
    "v_cmp_eq_u32 vcc, -1, v160\n\t" \
    "s_cbranch_vccz qk_Ook104_%=\n\t" \
    "qk_O103_%=:\n\t" \
    "global_load_dword v160, v35, s[46:47] offset:1536 sc1\n\t" \
    "s_waitcnt vmcnt(0)\n\t" \
    "v_cmp_eq_u32 vcc, -1, v160\n\t" \
    "s_cbranch_vccz qk_Ook104_%=\n\t" \
    "s_cmp_eq_u32 s68, 0\n\t" \
    "s_cbranch_scc1 qk_F107_%=\n\t" \
    "s_sub_u32 s68, s68, 1\n\t" \
    "s_sleep 3\n\t" \
    "s_branch qk_O103_%=\n\t" \
    "qk_F107_%=:\n\t" \
    "v_mov_b32 v161, 6095104\n\t" \
    "v_mov_b32 v162, 16\n\t" \
    "global_store_dword v161, v162, s[36:37] sc1\n\t" \
    "s_waitcnt vmcnt(0)\n\t" \
    "s_branch qk_Ook104_%=\n\t" \
    "qk_Ook104_%=:\n\t" \
    "v_subrev_u32 v160, s80, v160\n\t" \
    "s_nop 1\n\t" \
    "v_max_i32_dpp v160, v160, v160 row_ror:1 row_mask:0xf bank_mask:0xf\n\t" \
    "s_nop 1\n\t" \
    "v_max_i32_dpp v160, v160, v160 row_ror:2 row_mask:0xf bank_mask:0xf\n\t" \
    "s_nop 1\n\t" \
    "v_max_i32_dpp v160, v160, v160 row_ror:4 row_mask:0xf bank_mask:0xf\n\t" \
    "s_nop 1\n\t" \
    "v_max_i32_dpp v160, v160, v160 row_ror:8 row_mask:0xf bank_mask:0xf\n\t" \
    "s_nop 0\n\t" \
    "v_readlane_b32 s69, v160, 0\n\t" \
    "v_readlane_b32 s71, v160, 16\n\t" \
    "v_readlane_b32 s70, v160, 32\n\t" \
    "v_readlane_b32 s81, v160, 48\n\t" \
    "s_max_i32 s69, s69, s71\n\t" \
    "s_nop 0\n\t" \
    "s_max_i32 s71, s70, s81\n\t" \
    "s_max_i32 s69, s69, s71\n\t" \
    "s_add_u32 s81, s69, s80\n\t" \
    "qk_Oc105_%=:\n\t" \
    "s_mul_hi_u32 s69, s85, s78\n\t" \
    "s_add_u32 s79, s81, s69\n\t" \
    "s_add_u32 s79, s79, s87\n\t" \
    "s_cmp_ge_u32 s65, 36\n\t" \
    "s_cselect_b32 s69, 28, 0\n\t" \
    "s_sub_u32 s79, s79, s69\n\t" \
    "s_cmp_lt_u32 s65, 8\n\t" \
    "s_cselect_b32 s69, 96, 0\n\t" \
    "s_add_u32 s79, s79, s69\n\t" \
    "s_cmp_ge_u32 s65, 57\n\t" \
    "s_cselect_b32 s69, 96, 0\n\t" \
    "s_add_u32 s79, s79, s69\n\t" \
    "s_add_u32 s80, s79, s88\n\t" \
    "s_sub_u32 s79, s79, s74\n\t" \
    "s_max_i32 s79, s79, 0\n\t" \
    "s_min_i32 s79, s79, 16000\n\t" \
    "s_lshl_b32 s79, s79, 1\n\t" \
    "s_mul_hi_u32 s79, s79, s77\n\t" \
    "s_add_u32 s79, s79, s73\n\t" \
    "v_mov_b32 v160, s79\n\t" \
    "ds_write_b32 v11, v160 offset:20480\n\t" \
    "s_add_u32 s65, s65, 1\n\t" \
    "v_add_u32 v31, 1, v31\n\t" \
    "s_add_u32 s69, s65, 0\n\t" \
    "s_bitcmp1_b32 s69, 0\n\t" \
    "s_cselect_b32 s70, s63, s62\n\t" \
    "s_bfe_u32 s69, s69, 0x20001\n\t" \
    "s_lshl_b32 s69, s69, 19\n\t" \
    "s_add_u32 s69, s69, s70\n\t" \
    "s_add_u32 s46, s36, s69\n\t" \
    "s_addc_u32 s47, s37, 0\n\t" \
    "s_mov_b64 s[42:43], s[46:47]\n\t" \
    "s_add_u32 s69, s65, 0\n\t" \
    "s_lshr_b32 s69, s69, 1\n\t" \
    "s_xor_b32 s69, s69, s97\n\t" \
    "s_and_b32 s69, s69, 1\n\t" \
    "s_lshl_b32 s69, s69, 14\n\t" \
    "s_or_b32 s46, s46, s69\n\t" \
    "s_waitcnt lgkmcnt(0)\n\t" \
    "s_barrier\n\t" \
    "s_branch qk_go85_%=\n\t" \
    "qk_Gbad90_%=:\n\t" \
    "s_waitcnt vmcnt(0) lgkmcnt(0)\n\t" \
    "s_nop 7\n\t" \
    "v_mov_b32 v72, 0\n\t" \
    "v_mov_b32 v73, 0\n\t" \
    "v_mov_b32 v74, 0\n\t" \
    "v_mov_b32 v75, 0\n\t" \
    "v_mov_b32 v76, 0\n\t" \
    "v_mov_b32 v77, 0\n\t" \
    "v_mov_b32 v78, 0\n\t" \
    "v_mov_b32 v79, 0\n\t" \
    "v_mov_b32 v80, 0\n\t" \
    "v_mov_b32 v81, 0\n\t" \
    "v_mov_b32 v82, 0\n\t" \
    "v_mov_b32 v83, 0\n\t" \
    "v_mov_b32 v84, 0\n\t" \
    "v_mov_b32 v85, 0\n\t" \
    "v_mov_b32 v86, 0\n\t" \
    "v_mov_b32 v87, 0\n\t" \
    "s_cmp_eq_u32 s68, 0\n\t" \
    "s_cbranch_scc1 qk_F108_%=\n\t" \
    "s_sub_u32 s68, s68, 1\n\t" \
    "s_branch qk_G89_%=\n\t" \
    "qk_F108_%=:\n\t" \
    "v_mov_b32 v160, 6095104\n\t" \
    "v_mov_b32 v161, 1\n\t" \
    "global_store_dword v160, v161, s[36:37] sc1\n\t" \
    "s_waitcnt vmcnt(0)\n\t" \
    "s_branch qk_Gc92_%=\n\t" \
    "qk_Gbad291_%=:\n\t" \
    "global_load_dwordx4 v[44:47], v2, s[46:47] sc1\n\t" \
    "s_waitcnt vmcnt(0)\n\t" \
    "v_max3_u32 v160, v44, v45, v46\n\t" \
    "v_max_u32 v160, v160, v47\n\t" \
    "v_cmp_eq_u32 vcc, -1, v160\n\t" \
    "s_cbranch_vccz qk_Gre294_%=\n\t" \
    "s_cmp_eq_u32 s68, 0\n\t" \
    "s_cbranch_scc1 qk_F109_%=\n\t" \
    "s_sub_u32 s68, s68, 1\n\t" \
    "s_sleep 1\n\t" \
    "s_branch qk_Gbad291_%=\n\t" \
    "qk_F109_%=:\n\t" \
    "v_mov_b32 v160, 6095104\n\t" \
    "v_mov_b32 v161, 2\n\t" \
    "global_store_dword v160, v161, s[36:37] sc1\n\t" \
    "s_waitcnt vmcnt(0)\n\t" \
    "s_branch qk_Gre294_%=\n\t" \
    "qk_Gre294_%=:\n\t" \
    "v_mov_b32 v72, 0\n\t" \
    "v_mov_b32 v73, 0\n\t" \
    "v_mov_b32 v74, 0\n\t" \
    "v_mov_b32 v75, 0\n\t" \
    "v_mov_b32 v76, 0\n\t" \
    "v_mov_b32 v77, 0\n\t" \
    "v_mov_b32 v78, 0\n\t" \
    "v_mov_b32 v79, 0\n\t" \
    "v_mov_b32 v80, 0\n\t" \
    "v_mov_b32 v81, 0\n\t" \
    "v_mov_b32 v82, 0\n\t" \
    "v_mov_b32 v83, 0\n\t" \
    "v_mov_b32 v84, 0\n\t" \
    "v_mov_b32 v85, 0\n\t" \
    "v_mov_b32 v86, 0\n\t" \
    "v_mov_b32 v87, 0\n\t" \
    "v_smfmac_f32_16x16x64_bf16 v[72:75], v[36:39], a[48:55], v10\n\t" \
    "v_smfmac_f32_16x16x64_bf16 v[76:79], v[36:39], a[120:127], v10\n\t" \
    "v_smfmac_f32_16x16x64_bf16 v[80:83], v[36:39], a[192:199], v10\n\t" \
    "v_smfmac_f32_16x16x64_bf16 v[84:87], v[36:39], v[232:239], v10\n\t" \
    "v_smfmac_f32_16x16x64_bf16 v[72:75], v[40:43], a[56:63], v10\n\t" \
    "v_smfmac_f32_16x16x64_bf16 v[76:79], v[40:43], a[128:135], v10\n\t" \
    "v_smfmac_f32_16x16x64_bf16 v[80:83], v[40:43], a[200:207], v10\n\t" \
    "v_smfmac_f32_16x16x64_bf16 v[84:87], v[40:43], v[240:247], v10\n\t" \
    "v_smfmac_f32_16x16x64_bf16 v[72:75], v[44:47], a[64:71], v10\n\t" \
    "v_smfmac_f32_16x16x64_bf16 v[76:79], v[44:47], a[136:143], v10\n\t" \
    "v_smfmac_f32_16x16x64_bf16 v[80:83], v[44:47], a[208:215], v10\n\t" \
    "v_smfmac_f32_16x16x64_bf16 v[84:87], v[44:47], v[248:255], v10\n\t" \
    "s_waitcnt lgkmcnt(0)\n\t" \
    "s_memtime s[56:57]\n\t" \
    "s_waitcnt lgkmcnt(0)\n\t" \
    "s_branch qk_Gc293_%=\n\t" \
    "qk_last80_%=:\n\t" \
    "s_endpgm\n\t" \
    "vv_begin_%=:\n\t" \
    "v_mbcnt_lo_u32_b32 v223, -1, 0\n\t" \
    "v_mbcnt_hi_u32_b32 v223, -1, v223\n\t" \
    "s_mov_b32 s68, 262144\n\t" \
    "s_mov_b32 s72, 0\n\t" \
    "s_mov_b32 s83, -1\n\t" \
    "s_mov_b32 s65, 0\n\t" \
    "v_and_b32 v180, 3, v223\n\t" \
    "v_lshlrev_b32 v202, 3, v180\n\t" \
    "v_lshrrev_b32 v181, 5, v223\n\t" \
    "v_lshl_add_u32 v202, v181, 12, v202\n\t" \
    "v_bfe_u32 v182, v223, 2, 2\n\t" \
    "s_mov_b32 s70, 288\n\t" \
    "v_mad_u32_u24 v202, v182, s70, v202\n\t" \
    "v_bfe_u32 v183, v223, 4, 1\n\t" \
    "s_mov_b32 s70, 2176\n\t" \
    "v_mad_u32_u24 v202, v183, s70, v202\n\t" \
    "s_lshl_b32 s69, s66, 6\n\t" \
    "v_xor_b32 v202, s69, v202\n\t" \
    "v_xor_b32 v203, 32, v202\n\t" \
    "s_mul_i32 s69, s66, 9216\n\t" \
    "v_and_b32 v184, 1, v223\n\t" \
    "v_lshl_add_u32 v201, v184, 4, s69\n\t" \
    "v_lshrrev_b32 v185, 3, v223\n\t" \
    "s_mov_b32 s70, 1152\n\t" \
    "v_mad_u32_u24 v201, v185, s70, v201\n\t" \
    "v_bfe_u32 v186, v223, 1, 2\n\t" \
    "v_lshlrev_b32 v204, 5, v186\n\t" \
    "v_lshrrev_b32 v187, 4, v223\n\t" \
    "v_lshlrev_b32 v205, 5, v187\n\t" \
    "v_xor_b32 v204, v204, v205\n\t" \
    "v_add_u32 v201, v201, v204\n\t" \
    "s_mul_i32 s96, s66, 9216\n\t" \
    "v_lshlrev_b32 v207, 3, v180\n\t" \
    "v_bfe_u32 v188, v223, 2, 1\n\t" \
    "v_lshl_add_u32 v207, v188, 7, v207\n\t" \
    "v_bfe_u32 v189, v223, 3, 2\n\t" \
    "s_mov_b32 s70, 288\n\t" \
    "v_mad_u32_u24 v207, v189, s70, v207\n\t" \
    "s_mov_b32 s70, 9216\n\t" \
    "v_mad_u32_u24 v207, v181, s70, v207\n\t" \
    "s_mul_i32 s69, s66, 9\n\t" \
    "s_add_u32 s69, s69, 0\n\t" \
    "s_and_b32 s70, s69, 3\n\t" \
    "s_lshl_b32 s70, s70, 5\n\t" \
    "s_lshr_b32 s69, s69, 2\n\t" \
    "s_lshl_b32 s69, s69, 10\n\t" \
    "v_xor_b32 v204, s70, v207\n\t" \
    "v_add_u32 v204, s69, v204\n\t" \
    "s_mul_i32 s69, s66, 9\n\t" \
    "s_add_u32 s69, s69, 1\n\t" \
    "s_and_b32 s70, s69, 3\n\t" \
    "s_lshl_b32 s70, s70, 5\n\t" \
    "s_lshr_b32 s69, s69, 2\n\t" \
    "s_lshl_b32 s69, s69, 10\n\t" \
    "v_xor_b32 v205, s70, v207\n\t" \
    "v_add_u32 v205, s69, v205\n\t" \
    "s_mul_i32 s69, s66, 9\n\t" \
    "s_add_u32 s69, s69, 2\n\t" \
    "s_and_b32 s70, s69, 3\n\t" \
    "s_lshl_b32 s70, s70, 5\n\t" \
    "s_lshr_b32 s69, s69, 2\n\t" \
    "s_lshl_b32 s69, s69, 10\n\t" \
    "v_xor_b32 v206, s70, v207\n\t" \
    "v_add_u32 v206, s69, v206\n\t" \
    "s_mul_i32 s69, s66, 9\n\t" \
    "s_add_u32 s69, s69, 3\n\t" \
    "s_and_b32 s70, s69, 3\n\t" \
    "s_lshl_b32 s70, s70, 5\n\t" \
    "s_lshr_b32 s69, s69, 2\n\t" \
    "s_lshl_b32 s69, s69, 10\n\t" \
    "v_xor_b32 v207, s70, v207\n\t" \
    "v_add_u32 v207, s69, v207\n\t" \
    "s_waitcnt vmcnt(12) lgkmcnt(0)\n\t" \
    "s_barrier\n\t" \
    "s_add_u32 m0, s82, 65536\n\t" \
    "s_nop 0\n\t" \
    "global_load_lds_dwordx4 v200, s[42:43]\n\t" \
    "v_add_u32 v200, 131072, v200\n\t" \
    "s_add_u32 m0, s82, 69632\n\t" \
    "s_nop 0\n\t" \
    "global_load_lds_dwordx4 v200, s[42:43]\n\t" \
    "v_add_u32 v200, 131072, v200\n\t" \
    "s_add_u32 m0, s82, 73728\n\t" \
    "s_nop 0\n\t" \
    "global_load_lds_dwordx4 v200, s[42:43]\n\t" \
    "v_add_u32 v200, 131072, v200\n\t" \
    "s_add_u32 m0, s82, 77824\n\t" \
    "s_nop 0\n\t" \
    "global_load_lds_dwordx4 v200, s[42:43]\n\t" \
    "v_add_u32 v200, 131072, v200\n\t" \
    "ds_read_b64_tr_b16 a[0:1], v202 offset:36864\n\t" \
    "ds_read_b64_tr_b16 a[2:3], v202 offset:37888\n\t" \
    "ds_read_b64_tr_b16 a[4:5], v202 offset:45056\n\t" \
    "ds_read_b64_tr_b16 a[6:7], v202 offset:46080\n\t" \
    "ds_read_b64_tr_b16 a[72:73], v203 offset:36864\n\t" \
    "ds_read_b64_tr_b16 a[74:75], v203 offset:37888\n\t" \
    "ds_read_b64_tr_b16 a[76:77], v203 offset:45056\n\t" \
    "ds_read_b64_tr_b16 a[78:79], v203 offset:46080\n\t" \
    "v_lshlrev_b32 v0, 4, v223\n\t" \
    "s_mul_i32 s69, s66, 2048\n\t" \
    "s_bfe_u32 s71, s67, 0x10004\n\t" \
    "s_mul_i32 s71, s71, 131072\n\t" \
    "s_add_u32 s69, s69, s71\n\t" \
    "s_add_u32 s69, s69, 65536\n\t" \
    "v_lshl_add_u32 v1, v223, 4, s69\n\t" \
    "s_bfe_u32 s71, s67, 0x10004\n\t" \
    "s_mul_i32 s69, s71, 131072\n\t" \
    "s_add_u32 s69, s69, 102400\n\t" \
    "v_lshl_add_u32 v2, v223, 4, s69\n\t" \
    "s_bfe_u32 s71, s67, 0x10004\n\t" \
    "s_mul_i32 s69, s71, 131072\n\t" \
    "s_add_u32 s69, s69, 65536\n\t" \
    "v_lshl_add_u32 v34, v223, 4, s69\n\t" \
    "s_mov_b32 s69, 17476\n\t" \
    "v_bfe_u32 v190, v223, 1, 1\n\t" \
    "v_mov_b32 v10, s69\n\t" \
    "s_mov_b32 s70, 43690\n\t" \
    "v_mad_u32_u24 v10, v190, s70, v10\n\t" \
    "v_mov_b32 v11, 0\n\t" \
    "s_waitcnt vmcnt(12) lgkmcnt(0)\n\t" \
    "s_barrier\n\t" \
    "s_add_u32 m0, s82, 81920\n\t" \
    "s_nop 0\n\t" \
    "global_load_lds_dwordx4 v200, s[42:43]\n\t" \
    "v_add_u32 v200, 131072, v200\n\t" \
    "s_add_u32 m0, s82, 86016\n\t" \
    "s_nop 0\n\t" \
    "global_load_lds_dwordx4 v200, s[42:43]\n\t" \
    "v_add_u32 v200, 131072, v200\n\t" \
    "s_add_u32 m0, s82, 90112\n\t" \
    "s_nop 0\n\t" \
    "global_load_lds_dwordx4 v200, s[42:43]\n\t" \
    "v_add_u32 v200, 131072, v200\n\t" \
    "s_add_u32 m0, s82, 94208\n\t" \
    "s_nop 0\n\t" \
    "global_load_lds_dwordx4 v200, s[42:43]\n\t" \
    "v_add_u32 v200, 131072, v200\n\t" \
    "ds_read_b64_tr_b16 a[8:9], v202 offset:53248\n\t" \
    "ds_read_b64_tr_b16 a[10:11], v202 offset:54272\n\t" \
    "ds_read_b64_tr_b16 a[12:13], v202 offset:61440\n\t" \
    "ds_read_b64_tr_b16 a[14:15], v202 offset:62464\n\t" \
    "ds_read_b64_tr_b16 a[80:81], v203 offset:53248\n\t" \
    "ds_read_b64_tr_b16 a[82:83], v203 offset:54272\n\t" \
    "ds_read_b64_tr_b16 a[84:85], v203 offset:61440\n\t" \
    "ds_read_b64_tr_b16 a[86:87], v203 offset:62464\n\t" \
    "s_mul_i32 s69, s66, 512\n\t" \
    "s_add_u32 s69, s69, 16384\n\t" \
    "v_and_b32 v191, 13, v223\n\t" \
    "v_lshl_add_u32 v12, v191, 5, s69\n\t" \
    "v_lshl_add_u32 v12, v190, 2, v12\n\t" \
    "v_lshl_add_u32 v12, v187, 3, v12\n\t" \
    "s_mov_b32 s69, 16384\n\t" \
    "v_and_b32 v192, 15, v223\n\t" \
    "v_lshl_add_u32 v13, v192, 3, s69\n\t" \
    "v_lshl_add_u32 v13, v187, 8, v13\n\t" \
    "s_bfe_u32 s71, s67, 0x10004\n\t" \
    "s_mul_i32 s69, s71, 32768\n\t" \
    "s_add_u32 s69, s69, 266240\n\t" \
    "v_and_b32 v193, 31, v223\n\t" \
    "v_lshl_add_u32 v20, v193, 5, s69\n\t" \
    "v_lshl_add_u32 v20, v181, 2, v20\n\t" \
    "s_mul_i32 s69, s67, 32\n\t" \
    "s_add_u32 s69, s69, 262144\n\t" \
    "v_lshl_add_u32 v21, v180, 14, s69\n\t" \
    "v_bfe_u32 v194, v223, 2, 3\n\t" \
    "v_lshlrev_b32 v199, 10, v194\n\t" \
    "v_min_u32 v199, 4096, v199\n\t" \
    "v_add_u32 v21, v199, v21\n\t" \
    "s_bfe_u32 s71, s67, 0x10004\n\t" \
    "s_mul_i32 s69, s71, 32768\n\t" \
    "s_add_u32 s69, s69, 267264\n\t" \
    "v_min_u32 v35, 35, v223\n\t" \
    "v_lshl_add_u32 v35, v35, 5, s69\n\t" \
    "v_lshlrev_b32 v22, 2, v223\n\t" \
    "s_waitcnt vmcnt(12) lgkmcnt(0)\n\t" \
    "s_barrier\n\t" \
    "s_add_u32 m0, s82, 98304\n\t" \
    "s_nop 0\n\t" \
    "global_load_lds_dwordx4 v200, s[42:43]\n\t" \
    "v_add_u32 v200, 131072, v200\n\t" \
    "s_add_u32 m0, s82, 102400\n\t" \
    "s_nop 0\n\t" \
    "global_load_lds_dwordx4 v200, s[42:43]\n\t" \
    "v_add_u32 v200, 131072, v200\n\t" \
    "s_add_u32 m0, s82, 106496\n\t" \
    "s_nop 0\n\t" \
    "global_load_lds_dwordx4 v200, s[42:43]\n\t" \
    "v_add_u32 v200, 131072, v200\n\t" \
    "s_add_u32 m0, s82, 110592\n\t" \
    "s_nop 0\n\t" \
    "global_load_lds_dwordx4 v200, s[42:43]\n\t" \
    "v_add_u32 v200, 131072, v200\n\t" \
    "v_add_u32 v202, 69632, v202\n\t" \
    "v_add_u32 v203, 69632, v203\n\t" \
    "ds_read_b64_tr_b16 a[16:17], v202 offset:0\n\t" \
    "ds_read_b64_tr_b16 a[18:19], v202 offset:1024\n\t" \
    "ds_read_b64_tr_b16 a[20:21], v202 offset:8192\n\t" \
    "ds_read_b64_tr_b16 a[22:23], v202 offset:9216\n\t" \
    "ds_read_b64_tr_b16 a[88:89], v203 offset:0\n\t" \
    "ds_read_b64_tr_b16 a[90:91], v203 offset:1024\n\t" \
    "ds_read_b64_tr_b16 a[92:93], v203 offset:8192\n\t" \
    "ds_read_b64_tr_b16 a[94:95], v203 offset:9216\n\t" \
    "s_mul_i32 s69, s66, 147456\n\t" \
    "s_mul_i32 s71, s67, 256\n\t" \
    "s_add_u32 s69, s69, s71\n\t" \
    "v_lshl_add_u32 v14, v192, 4, s69\n\t" \
    "v_lshl_add_u32 v14, v187, 14, v14\n\t" \
    "s_mul_i32 s69, s66, 147456\n\t" \
    "s_mul_i32 s71, s67, 256\n\t" \
    "s_add_u32 s69, s69, s71\n\t" \
    "s_add_u32 s69, s69, 65536\n\t" \
    "v_lshl_add_u32 v15, v192, 4, s69\n\t" \
    "v_lshl_add_u32 v15, v187, 14, v15\n\t" \
    "s_mul_i32 s69, s66, 147456\n\t" \
    "s_mul_i32 s71, s67, 256\n\t" \
    "s_add_u32 s69, s69, s71\n\t" \
    "s_add_u32 s69, s69, 131072\n\t" \
    "v_lshl_add_u32 v16, v192, 4, s69\n\t" \
    "v_lshl_add_u32 v16, v187, 2, v16\n\t" \
    "s_mul_i32 s97, s67, 171\n\t" \
    "s_lshr_b32 s97, s97, 9\n\t" \
    "s_mul_i32 s97, s97, 131072\n\t" \
    "s_mul_i32 s71, s66, 2304\n\t" \
    "s_add_u32 s69, s97, s71\n\t" \
    "s_mul_i32 s71, s67, 655360\n\t" \
    "s_add_u32 s69, s69, s71\n\t" \
    "v_lshl_add_u32 v17, v223, 4, s69\n\t" \
    "s_mul_i32 s71, s66, 2304\n\t" \
    "s_add_u32 s69, s97, s71\n\t" \
    "s_mul_i32 s71, s67, 655360\n\t" \
    "s_add_u32 s69, s69, s71\n\t" \
    "s_add_u32 s69, s69, 2048\n\t" \
    "v_lshl_add_u32 v18, v192, 4, s69\n\t" \
    "v_lshl_add_u32 v18, v187, 2, v18\n\t" \
    "s_waitcnt vmcnt(12) lgkmcnt(0)\n\t" \
    "s_barrier\n\t" \
    "s_add_u32 m0, s82, 0\n\t" \
    "s_nop 0\n\t" \
    "global_load_lds_dwordx4 v200, s[42:43]\n\t" \
    "v_add_u32 v200, 131072, v200\n\t" \
    "s_add_u32 m0, s82, 4096\n\t" \
    "s_nop 0\n\t" \
    "global_load_lds_dwordx4 v200, s[42:43]\n\t" \
    "v_add_u32 v200, 131072, v200\n\t" \
    "s_add_u32 m0, s82, 8192\n\t" \
    "s_nop 0\n\t" \
    "global_load_lds_dwordx4 v200, s[42:43]\n\t" \
    "v_add_u32 v200, 131072, v200\n\t" \
    "s_add_u32 m0, s82, 12288\n\t" \
    "s_nop 0\n\t" \
    "global_load_lds_dwordx4 v200, s[42:43]\n\t" \
    "v_add_u32 v200, 131072, v200\n\t" \
    "ds_read_b64_tr_b16 a[24:25], v202 offset:16384\n\t" \
    "ds_read_b64_tr_b16 a[26:27], v202 offset:17408\n\t" \
    "ds_read_b64_tr_b16 a[28:29], v202 offset:24576\n\t" \
    "ds_read_b64_tr_b16 a[30:31], v202 offset:25600\n\t" \
    "ds_read_b64_tr_b16 a[96:97], v203 offset:16384\n\t" \
    "ds_read_b64_tr_b16 a[98:99], v203 offset:17408\n\t" \
    "ds_read_b64_tr_b16 a[100:101], v203 offset:24576\n\t" \
    "ds_read_b64_tr_b16 a[102:103], v203 offset:25600\n\t" \
    "s_mov_b32 s77, 3019898880\n\t" \
    "s_mov_b32 s78, 3054198966\n\t" \
    "s_mov_b32 s71, 3\n\t" \
    "vv_E1_%=:\n\t" \
    "s_memtime s[54:55]\n\t" \
    "s_nop 0\n\t" \
    "s_memrealtime s[4:5]\n\t" \
    "s_nop 0\n\t" \
    "s_memrealtime s[6:7]\n\t" \
    "s_nop 0\n\t" \
    "s_memrealtime s[8:9]\n\t" \
    "s_nop 0\n\t" \
    "s_memrealtime s[10:11]\n\t" \
    "s_nop 0\n\t" \
    "s_memrealtime s[12:13]\n\t" \
    "s_nop 0\n\t" \
    "s_memrealtime s[14:15]\n\t" \
    "s_nop 0\n\t" \
    "s_memrealtime s[16:17]\n\t" \
    "s_nop 0\n\t" \
    "s_memrealtime s[18:19]\n\t" \
    "s_nop 0\n\t" \
    "s_memrealtime s[20:21]\n\t" \
    "s_nop 0\n\t" \
    "s_memrealtime s[22:23]\n\t" \
    "s_nop 0\n\t" \
    "s_memrealtime s[24:25]\n\t" \
    "s_nop 0\n\t" \
    "s_memrealtime s[26:27]\n\t" \
    "s_nop 0\n\t" \
    "s_memrealtime s[28:29]\n\t" \
    "s_nop 0\n\t" \
    "s_memtime s[56:57]\n\t" \
    "s_mov_b32 s70, 1\n\t" \
    "s_sub_u32 s71, s71, 1\n\t" \
    "s_cselect_b32 s69, 0, -1\n\t" \
    "s_waitcnt lgkmcnt(0)\n\t" \
    "s_sub_u32 s54, s56, s54\n\t" \
    "s_and_b32 s54, s54, s69\n\t" \
    "s_cmp_gt_u32 s54, 124\n\t" \
    "s_cbranch_scc1 vv_E1_%=\n\t" \
    "s_cmp_eq_u32 s4, s28\n\t" \
    "s_addc_u32 s70, s70, 0\n\t" \
    "s_cmp_eq_u32 s6, s28\n\t" \
    "s_addc_u32 s70, s70, 0\n\t" \
    "s_cmp_eq_u32 s8, s28\n\t" \
    "s_addc_u32 s70, s70, 0\n\t" \
    "s_cmp_eq_u32 s10, s28\n\t" \
    "s_addc_u32 s70, s70, 0\n\t" \
    "s_cmp_eq_u32 s12, s28\n\t" \
    "s_addc_u32 s70, s70, 0\n\t" \
    "s_cmp_eq_u32 s14, s28\n\t" \
    "s_addc_u32 s70, s70, 0\n\t" \
    "s_cmp_eq_u32 s16, s28\n\t" \
    "s_addc_u32 s70, s70, 0\n\t" \
    "s_cmp_eq_u32 s18, s28\n\t" \
    "s_addc_u32 s70, s70, 0\n\t" \
    "s_cmp_eq_u32 s20, s28\n\t" \
    "s_addc_u32 s70, s70, 0\n\t" \
    "s_cmp_eq_u32 s22, s28\n\t" \
    "s_addc_u32 s70, s70, 0\n\t" \
    "s_cmp_eq_u32 s24, s28\n\t" \
    "s_addc_u32 s70, s70, 0\n\t" \
    "s_cmp_eq_u32 s26, s28\n\t" \
    "s_addc_u32 s70, s70, 0\n\t" \
    "s_mov_b32 s75, s73\n\t" \
    "s_mov_b32 s76, s74\n\t" \
    "s_lshl_b32 s70, s70, 3\n\t" \
    "s_sub_u32 s73, s56, s70\n\t" \
    "s_lshl_b32 s74, s28, 4\n\t" \
    "s_waitcnt vmcnt(12) lgkmcnt(0)\n\t" \
    "s_barrier\n\t" \
    "s_add_u32 m0, s82, 16384\n\t" \
    "s_nop 0\n\t" \
    "global_load_lds_dwordx4 v200, s[42:43]\n\t" \
    "v_add_u32 v200, 131072, v200\n\t" \
    "s_add_u32 m0, s82, 20480\n\t" \
    "s_nop 0\n\t" \
    "global_load_lds_dwordx4 v200, s[42:43]\n\t" \
    "v_add_u32 v200, 131072, v200\n\t" \
    "s_add_u32 m0, s82, 24576\n\t" \
    "s_nop 0\n\t" \
    "global_load_lds_dwordx4 v200, s[42:43]\n\t" \
    "v_add_u32 v200, 131072, v200\n\t" \
    "s_add_u32 m0, s82, 28672\n\t" \
    "s_nop 0\n\t" \
    "global_load_lds_dwordx4 v200, s[42:43]\n\t" \
    "v_add_u32 v200, 131072, v200\n\t" \
    "ds_read_b64_tr_b16 a[32:33], v202 offset:32768\n\t" \
    "ds_read_b64_tr_b16 a[34:35], v202 offset:33792\n\t" \
    "ds_read_b64_tr_b16 a[36:37], v202 offset:40960\n\t" \
    "ds_read_b64_tr_b16 a[38:39], v202 offset:41984\n\t" \
    "ds_read_b64_tr_b16 a[104:105], v203 offset:32768\n\t" \
    "ds_read_b64_tr_b16 a[106:107], v203 offset:33792\n\t" \
    "ds_read_b64_tr_b16 a[108:109], v203 offset:40960\n\t" \
    "ds_read_b64_tr_b16 a[110:111], v203 offset:41984\n\t" \
    "s_waitcnt vmcnt(12) lgkmcnt(0)\n\t" \
    "s_barrier\n\t" \
    "s_add_u32 m0, s96, 0\n\t" \
    "s_nop 0\n\t" \
    "global_load_lds_dwordx4 v201, s[44:45] offset:0\n\t" \
    "s_add_u32 m0, s96, 896\n\t" \
    "s_nop 0\n\t" \
    "global_load_lds_dwordx4 v201, s[44:45] offset:128\n\t" \
    "s_add_u32 m0, s96, 1792\n\t" \
    "s_nop 0\n\t" \
    "global_load_lds_dwordx4 v201, s[44:45] offset:256\n\t" \
    "s_add_u32 m0, s96, 2688\n\t" \
    "s_nop 0\n\t" \
    "global_load_lds_dwordx4 v201, s[44:45] offset:384\n\t" \
    "s_add_u32 m0, s96, 3584\n\t" \
    "s_nop 0\n\t" \
    "global_load_lds_dwordx4 v201, s[44:45] offset:512\n\t" \
    "s_add_u32 m0, s96, 4480\n\t" \
    "s_nop 0\n\t" \
    "global_load_lds_dwordx4 v201, s[44:45] offset:640\n\t" \
    "s_add_u32 m0, s96, 5376\n\t" \
    "s_nop 0\n\t" \
    "global_load_lds_dwordx4 v201, s[44:45] offset:768\n\t" \
    "s_add_u32 m0, s96, 6272\n\t" \
    "s_nop 0\n\t" \
    "global_load_lds_dwordx4 v201, s[44:45] offset:896\n\t" \
    "s_add_u32 m0, s96, 7168\n\t" \
    "s_nop 0\n\t" \
    "global_load_lds_dwordx4 v201, s[44:45] offset:1024\n\t" \
    "v_add_u32 v201, 36864, v201\n\t" \
    "ds_read_b64_tr_b16 a[40:41], v202 offset:49152\n\t" \
    "ds_read_b64_tr_b16 a[42:43], v202 offset:50176\n\t" \
    "ds_read_b64_tr_b16 a[44:45], v202 offset:57344\n\t" \
    "ds_read_b64_tr_b16 a[46:47], v202 offset:58368\n\t" \
    "ds_read_b64_tr_b16 a[112:113], v203 offset:49152\n\t" \
    "ds_read_b64_tr_b16 a[114:115], v203 offset:50176\n\t" \
    "ds_read_b64_tr_b16 a[116:117], v203 offset:57344\n\t" \
    "ds_read_b64_tr_b16 a[118:119], v203 offset:58368\n\t" \
    "s_waitcnt vmcnt(17) lgkmcnt(0)\n\t" \
    "s_barrier\n\t" \
    "v_add_u32 v202, 65536, v202\n\t" \
    "v_add_u32 v203, 65536, v203\n\t" \
    "ds_read_b64_tr_b16 a[48:49], v202 offset:0\n\t" \
    "ds_read_b64_tr_b16 a[50:51], v202 offset:1024\n\t" \
    "ds_read_b64_tr_b16 a[52:53], v202 offset:8192\n\t" \
    "ds_read_b64_tr_b16 a[54:55], v202 offset:9216\n\t" \
    "ds_read_b64_tr_b16 a[120:121], v203 offset:0\n\t" \
    "ds_read_b64_tr_b16 a[122:123], v203 offset:1024\n\t" \
    "ds_read_b64_tr_b16 a[124:125], v203 offset:8192\n\t" \
    "ds_read_b64_tr_b16 a[126:127], v203 offset:9216\n\t" \
    "s_waitcnt vmcnt(13) lgkmcnt(0)\n\t" \
    "s_barrier\n\t" \
    "s_add_u32 m0, s96, 73728\n\t" \
    "s_nop 0\n\t" \
    "global_load_lds_dwordx4 v201, s[44:45] offset:0\n\t" \
    "s_add_u32 m0, s96, 74624\n\t" \
    "s_nop 0\n\t" \
    "global_load_lds_dwordx4 v201, s[44:45] offset:128\n\t" \
    "s_add_u32 m0, s96, 75520\n\t" \
    "s_nop 0\n\t" \
    "global_load_lds_dwordx4 v201, s[44:45] offset:256\n\t" \
    "s_add_u32 m0, s96, 76416\n\t" \
    "s_nop 0\n\t" \
    "global_load_lds_dwordx4 v201, s[44:45] offset:384\n\t" \
    "s_add_u32 m0, s96, 77312\n\t" \
    "s_nop 0\n\t" \
    "global_load_lds_dwordx4 v201, s[44:45] offset:512\n\t" \
    "s_add_u32 m0, s96, 78208\n\t" \
    "s_nop 0\n\t" \
    "global_load_lds_dwordx4 v201, s[44:45] offset:640\n\t" \
    "s_add_u32 m0, s96, 79104\n\t" \
    "s_nop 0\n\t" \
    "global_load_lds_dwordx4 v201, s[44:45] offset:768\n\t" \
    "s_add_u32 m0, s96, 80000\n\t" \
    "s_nop 0\n\t" \
    "global_load_lds_dwordx4 v201, s[44:45] offset:896\n\t" \
    "s_add_u32 m0, s96, 80896\n\t" \
    "s_nop 0\n\t" \
    "global_load_lds_dwordx4 v201, s[44:45] offset:1024\n\t" \
    "v_add_u32 v201, 36864, v201\n\t" \
    "v_add_u32 v202, -98304, v202\n\t" \
    "v_add_u32 v203, -98304, v203\n\t" \
    "ds_read_b64_tr_b16 a[56:57], v202 offset:0\n\t" \
    "ds_read_b64_tr_b16 a[58:59], v202 offset:1024\n\t" \
    "ds_read_b64_tr_b16 a[60:61], v202 offset:8192\n\t" \
    "ds_read_b64_tr_b16 a[62:63], v202 offset:9216\n\t" \
    "ds_read_b64_tr_b16 a[128:129], v203 offset:0\n\t" \
    "ds_read_b64_tr_b16 a[130:131], v203 offset:1024\n\t" \
    "ds_read_b64_tr_b16 a[132:133], v203 offset:8192\n\t" \
    "ds_read_b64_tr_b16 a[134:135], v203 offset:9216\n\t" \
    "s_waitcnt vmcnt(18) lgkmcnt(0)\n\t" \
    "s_barrier\n\t" \
    "s_add_u32 m0, s96, 110592\n\t" \
    "s_nop 0\n\t" \
    "global_load_lds_dwordx4 v201, s[44:45] offset:0\n\t" \
    "s_add_u32 m0, s96, 111488\n\t" \
    "s_nop 0\n\t" \
    "global_load_lds_dwordx4 v201, s[44:45] offset:128\n\t" \
    "s_add_u32 m0, s96, 112384\n\t" \
    "s_nop 0\n\t" \
    "global_load_lds_dwordx4 v201, s[44:45] offset:256\n\t" \
    "s_add_u32 m0, s96, 113280\n\t" \
    "s_nop 0\n\t" \
    "global_load_lds_dwordx4 v201, s[44:45] offset:384\n\t" \
    "s_add_u32 m0, s96, 114176\n\t" \
    "s_nop 0\n\t" \
    "global_load_lds_dwordx4 v201, s[44:45] offset:512\n\t" \
    "s_add_u32 m0, s96, 115072\n\t" \
    "s_nop 0\n\t" \
    "global_load_lds_dwordx4 v201, s[44:45] offset:640\n\t" \
    "s_add_u32 m0, s96, 115968\n\t" \
    "s_nop 0\n\t" \
    "global_load_lds_dwordx4 v201, s[44:45] offset:768\n\t" \
    "s_add_u32 m0, s96, 116864\n\t" \
    "s_nop 0\n\t" \
    "global_load_lds_dwordx4 v201, s[44:45] offset:896\n\t" \
    "s_add_u32 m0, s96, 117760\n\t" \
    "s_nop 0\n\t" \
    "global_load_lds_dwordx4 v201, s[44:45] offset:1024\n\t" \
    "v_add_u32 v201, 36864, v201\n\t" \
    "ds_read_b64_tr_b16 a[64:65], v202 offset:16384\n\t" \
    "ds_read_b64_tr_b16 a[66:67], v202 offset:17408\n\t" \
    "ds_read_b64_tr_b16 a[68:69], v202 offset:24576\n\t" \
    "ds_read_b64_tr_b16 a[70:71], v202 offset:25600\n\t" \
    "ds_read_b64_tr_b16 a[136:137], v203 offset:16384\n\t" \
    "ds_read_b64_tr_b16 a[138:139], v203 offset:17408\n\t" \
    "ds_read_b64_tr_b16 a[140:141], v203 offset:24576\n\t" \
    "ds_read_b64_tr_b16 a[142:143], v203 offset:25600\n\t" \
    "s_waitcnt vmcnt(18) lgkmcnt(0)\n\t" \
    "s_barrier\n\t" \
    "s_add_u32 m0, s96, 36864\n\t" \
    "s_nop 0\n\t" \
    "global_load_lds_dwordx4 v201, s[44:45] offset:0\n\t" \
    "s_add_u32 m0, s96, 37760\n\t" \
    "s_nop 0\n\t" \
    "global_load_lds_dwordx4 v201, s[44:45] offset:128\n\t" \
    "s_add_u32 m0, s96, 38656\n\t" \
    "s_nop 0\n\t" \
    "global_load_lds_dwordx4 v201, s[44:45] offset:256\n\t" \
    "s_add_u32 m0, s96, 39552\n\t" \
    "s_nop 0\n\t" \
    "global_load_lds_dwordx4 v201, s[44:45] offset:384\n\t" \
    "s_add_u32 m0, s96, 40448\n\t" \
    "s_nop 0\n\t" \
    "global_load_lds_dwordx4 v201, s[44:45] offset:512\n\t" \
    "s_add_u32 m0, s96, 41344\n\t" \
    "s_nop 0\n\t" \
    "global_load_lds_dwordx4 v201, s[44:45] offset:640\n\t" \
    "s_add_u32 m0, s96, 42240\n\t" \
    "s_nop 0\n\t" \
    "global_load_lds_dwordx4 v201, s[44:45] offset:768\n\t" \
    "s_add_u32 m0, s96, 43136\n\t" \
    "s_nop 0\n\t" \
    "global_load_lds_dwordx4 v201, s[44:45] offset:896\n\t" \
    "s_add_u32 m0, s96, 44032\n\t" \
    "s_nop 0\n\t" \
    "global_load_lds_dwordx4 v201, s[44:45] offset:1024\n\t" \
    "v_add_u32 v201, 36864, v201\n\t" \
    "ds_read_b64_tr_b16 a[144:145], v204 offset:0\n\t" \
    "ds_read_b64_tr_b16 a[146:147], v204 offset:18432\n\t" \
    "ds_read_b64_tr_b16 a[160:161], v205 offset:0\n\t" \
    "ds_read_b64_tr_b16 a[162:163], v205 offset:18432\n\t" \
    "ds_read_b64_tr_b16 a[176:177], v206 offset:0\n\t" \
    "ds_read_b64_tr_b16 a[178:179], v206 offset:18432\n\t" \
    "s_waitcnt lgkmcnt(6)\n\t" \
    "ds_read_b64_tr_b16 a[192:193], v207 offset:0\n\t" \
    "ds_read_b64_tr_b16 a[194:195], v207 offset:18432\n\t" \
    "ds_read_b64_tr_b16 a[208:209], v204 offset:1024\n\t" \
    "ds_read_b64_tr_b16 a[210:211], v204 offset:19456\n\t" \
    "ds_read_b64_tr_b16 a[224:225], v205 offset:1024\n\t" \
    "ds_read_b64_tr_b16 a[226:227], v205 offset:19456\n\t" \
    "s_waitcnt lgkmcnt(6)\n\t" \
    "ds_read_b64_tr_b16 a[240:241], v206 offset:1024\n\t" \
    "ds_read_b64_tr_b16 a[242:243], v206 offset:19456\n\t" \
    "ds_read_b64_tr_b16 v[224:225], v207 offset:1024\n\t" \
    "ds_read_b64_tr_b16 v[226:227], v207 offset:19456\n\t" \
    "ds_read_b64_tr_b16 v[240:241], v204 offset:2048\n\t" \
    "ds_read_b64_tr_b16 v[242:243], v204 offset:20480\n\t" \
    "s_waitcnt lgkmcnt(6)\n\t" \
    "s_waitcnt vmcnt(18) lgkmcnt(0)\n\t" \
    "s_barrier\n\t" \
    "v_mbcnt_lo_u32_b32 v140, -1, 0\n\t" \
    "v_mbcnt_hi_u32_b32 v140, -1, v140\n\t" \
    "v_min_u32 v140, 59, v140\n\t" \
    "v_lshlrev_b32 v140, 6, v140\n\t" \
    "v_add_u32 v140, 6258688, v140\n\t" \
    "v_mov_b32 v141, 6266880\n\t" \
    "global_load_dword v141, v141, s[36:37] sc1\n\t" \
    "global_load_dwordx2 v[142:143], v140, s[36:37] sc1\n\t" \
    "v_add_u32 v204, 73728, v204\n\t" \
    "v_add_u32 v205, 73728, v205\n\t" \
    "v_add_u32 v206, 73728, v206\n\t" \
    "v_add_u32 v207, 73728, v207\n\t" \
    "ds_read_b64_tr_b16 a[148:149], v204 offset:0\n\t" \
    "ds_read_b64_tr_b16 a[150:151], v204 offset:18432\n\t" \
    "ds_read_b64_tr_b16 a[164:165], v205 offset:0\n\t" \
    "ds_read_b64_tr_b16 a[166:167], v205 offset:18432\n\t" \
    "ds_read_b64_tr_b16 a[180:181], v206 offset:0\n\t" \
    "ds_read_b64_tr_b16 a[182:183], v206 offset:18432\n\t" \
    "s_waitcnt lgkmcnt(6)\n\t" \
    "ds_read_b64_tr_b16 a[196:197], v207 offset:0\n\t" \
    "ds_read_b64_tr_b16 a[198:199], v207 offset:18432\n\t" \
    "ds_read_b64_tr_b16 a[212:213], v204 offset:1024\n\t" \
    "ds_read_b64_tr_b16 a[214:215], v204 offset:19456\n\t" \
    "ds_read_b64_tr_b16 a[228:229], v205 offset:1024\n\t" \
    "ds_read_b64_tr_b16 a[230:231], v205 offset:19456\n\t" \
    "s_waitcnt lgkmcnt(6)\n\t" \
    "ds_read_b64_tr_b16 a[244:245], v206 offset:1024\n\t" \
    "ds_read_b64_tr_b16 a[246:247], v206 offset:19456\n\t" \
    "ds_read_b64_tr_b16 v[228:229], v207 offset:1024\n\t" \
    "ds_read_b64_tr_b16 v[230:231], v207 offset:19456\n\t" \
    "ds_read_b64_tr_b16 v[244:245], v204 offset:2048\n\t" \
    "ds_read_b64_tr_b16 v[246:247], v204 offset:20480\n\t" \
    "s_waitcnt lgkmcnt(6)\n\t" \
    "s_waitcnt vmcnt(11) lgkmcnt(0)\n\t" \
    "s_barrier\n\t" \
    "ds_read_b64_tr_b16 a[152:153], v204 offset:36864\n\t" \
    "ds_read_b64_tr_b16 a[154:155], v204 offset:55296\n\t" \
    "ds_read_b64_tr_b16 a[168:169], v205 offset:36864\n\t" \
    "ds_read_b64_tr_b16 a[170:171], v205 offset:55296\n\t" \
    "ds_read_b64_tr_b16 a[184:185], v206 offset:36864\n\t" \
    "ds_read_b64_tr_b16 a[186:187], v206 offset:55296\n\t" \
    "s_waitcnt lgkmcnt(6)\n\t" \
    "ds_read_b64_tr_b16 a[200:201], v207 offset:36864\n\t" \
    "ds_read_b64_tr_b16 a[202:203], v207 offset:55296\n\t" \
    "ds_read_b64_tr_b16 a[216:217], v204 offset:37888\n\t" \
    "ds_read_b64_tr_b16 a[218:219], v204 offset:56320\n\t" \
    "ds_read_b64_tr_b16 a[232:233], v205 offset:37888\n\t" \
    "ds_read_b64_tr_b16 a[234:235], v205 offset:56320\n\t" \
    "s_waitcnt lgkmcnt(6)\n\t" \
    "ds_read_b64_tr_b16 a[248:249], v206 offset:37888\n\t" \
    "ds_read_b64_tr_b16 a[250:251], v206 offset:56320\n\t" \
    "ds_read_b64_tr_b16 v[232:233], v207 offset:37888\n\t" \
    "ds_read_b64_tr_b16 v[234:235], v207 offset:56320\n\t" \
    "ds_read_b64_tr_b16 v[248:249], v204 offset:38912\n\t" \
    "ds_read_b64_tr_b16 v[250:251], v204 offset:57344\n\t" \
    "s_waitcnt lgkmcnt(6)\n\t" \
    "s_waitcnt vmcnt(2) lgkmcnt(0)\n\t" \
    "s_barrier\n\t" \
    "v_add_u32 v204, -36864, v204\n\t" \
    "v_add_u32 v205, -36864, v205\n\t" \
    "v_add_u32 v206, -36864, v206\n\t" \
    "v_add_u32 v207, -36864, v207\n\t" \
    "ds_read_b64_tr_b16 a[156:157], v204 offset:0\n\t" \
    "ds_read_b64_tr_b16 a[158:159], v204 offset:18432\n\t" \
    "ds_read_b64_tr_b16 a[172:173], v205 offset:0\n\t" \
    "ds_read_b64_tr_b16 a[174:175], v205 offset:18432\n\t" \
    "ds_read_b64_tr_b16 a[188:189], v206 offset:0\n\t" \
    "ds_read_b64_tr_b16 a[190:191], v206 offset:18432\n\t" \
    "s_waitcnt lgkmcnt(6)\n\t" \
    "ds_read_b64_tr_b16 a[204:205], v207 offset:0\n\t" \
    "ds_read_b64_tr_b16 a[206:207], v207 offset:18432\n\t" \
    "ds_read_b64_tr_b16 a[220:221], v204 offset:1024\n\t" \
    "ds_read_b64_tr_b16 a[222:223], v204 offset:19456\n\t" \
    "ds_read_b64_tr_b16 a[236:237], v205 offset:1024\n\t" \
    "ds_read_b64_tr_b16 a[238:239], v205 offset:19456\n\t" \
    "s_waitcnt lgkmcnt(6)\n\t" \
    "ds_read_b64_tr_b16 a[252:253], v206 offset:1024\n\t" \
    "ds_read_b64_tr_b16 a[254:255], v206 offset:19456\n\t" \
    "ds_read_b64_tr_b16 v[236:237], v207 offset:1024\n\t" \
    "ds_read_b64_tr_b16 v[238:239], v207 offset:19456\n\t" \
    "ds_read_b64_tr_b16 v[252:253], v204 offset:2048\n\t" \
    "ds_read_b64_tr_b16 v[254:255], v204 offset:20480\n\t" \
    "s_waitcnt lgkmcnt(6)\n\t" \
    "s_waitcnt vmcnt(0) lgkmcnt(0)\n\t" \
    "s_barrier\n\t" \
    "v_sub_u32 v142, v142, v141\n\t" \
    "v_cmp_ne_u32 vcc, 1, v142\n\t" \
    "s_cbranch_vccz vv_Cok4_%=\n\t" \
    "vv_C3_%=:\n\t" \
    "global_load_dwordx2 v[142:143], v140, s[36:37] sc1\n\t" \
    "s_waitcnt vmcnt(0)\n\t" \
    "v_sub_u32 v142, v142, v141\n\t" \
    "v_cmp_ne_u32 vcc, 1, v142\n\t" \
    "s_cbranch_vccz vv_Cok4_%=\n\t" \
    "s_cmp_eq_u32 s68, 0\n\t" \
    "s_cbranch_scc1 vv_F5_%=\n\t" \
    "s_sub_u32 s68, s68, 1\n\t" \
    "s_sleep 4\n\t" \
    "s_branch vv_C3_%=\n\t" \
    "vv_F5_%=:\n\t" \
    "v_mov_b32 v144, 6095104\n\t" \
    "v_mov_b32 v145, 4\n\t" \
    "global_store_dword v144, v145, s[36:37] sc1\n\t" \
    "s_waitcnt vmcnt(0)\n\t" \
    "s_branch vv_Cok4_%=\n\t" \
    "vv_Cok4_%=:\n\t" \
    "s_cmp_lg_u32 s66, 0\n\t" \
    "s_cbranch_scc1 vv_Ca6_%=\n\t" \
    "v_mov_b32 v140, 6176768\n\t" \
    "s_mov_b64 exec, 1\n\t" \
    "global_atomic_add v140, v142, s[36:37] sc1\n\t" \
    "s_mov_b64 exec, -1\n\t" \
    "vv_Ca6_%=:\n\t" \
    "v_sub_u32_dpp v142, v143, v143 row_shl:6 row_mask:0xf bank_mask:0xf\n\t" \
    "v_min_u32_dpp v143, v143, v143 row_shl:6 row_mask:0xf bank_mask:0xf\n\t" \
    "v_min_i32 v142, 64, v142\n\t" \
    "v_max_i32 v142, -64, v142\n\t" \
    "s_mov_b32 s97, 0\n\t" \
    "v_readlane_b32 s69, v143, 0\n\t" \
    "v_readlane_b32 s70, v143, 1\n\t" \
    "v_readlane_b32 s54, v142, 0\n\t" \
    "v_readlane_b32 s55, v142, 1\n\t" \
    "s_nop 1\n\t" \
    "s_cmp_lt_u32 s70, s69\n\t" \
    "s_cselect_b32 s62, 8192, 0\n\t" \
    "s_cselect_b32 s54, s55, s54\n\t" \
    "s_cselect_b32 s69, s70, s69\n\t" \
    "s_mov_b32 s71, s69\n\t" \
    "s_add_u32 s97, s97, s54\n\t" \
    "v_readlane_b32 s69, v143, 2\n\t" \
    "v_readlane_b32 s70, v143, 3\n\t" \
    "v_readlane_b32 s54, v142, 2\n\t" \
    "v_readlane_b32 s55, v142, 3\n\t" \
    "s_nop 1\n\t" \
    "s_sub_u32 s55, 0, s55\n\t" \
    "s_cmp_lt_u32 s70, s69\n\t" \
    "s_cselect_b32 s63, 8192, 0\n\t" \
    "s_cselect_b32 s54, s55, s54\n\t" \
    "s_cselect_b32 s69, s70, s69\n\t" \
    "s_max_u32 s71, s69, s71\n\t" \
    "s_add_u32 s97, s97, s54\n\t" \
    "v_readlane_b32 s69, v143, 4\n\t" \
    "v_readlane_b32 s70, v143, 5\n\t" \
    "v_readlane_b32 s54, v142, 4\n\t" \
    "v_readlane_b32 s55, v142, 5\n\t" \
    "s_nop 1\n\t" \
    "s_sub_u32 s54, 0, s54\n\t" \
    "s_sub_u32 s55, 0, s55\n\t" \
    "s_cmp_lt_u32 s70, s69\n\t" \
    "s_cselect_b32 s64, 8192, 0\n\t" \
    "s_cselect_b32 s54, s55, s54\n\t" \
    "s_cselect_b32 s69, s70, s69\n\t" \
    "s_max_u32 s71, s69, s71\n\t" \
    "s_add_u32 s97, s97, s54\n\t" \
    "s_add_u32 s63, s63, 0x200000\n\t" \
    "s_xor_b32 s52, s64, 8192\n\t" \
    "s_cmp_gt_u32 s71, 625\n\t" \
    "s_cselect_b32 s69, 1, 0\n\t" \
    "s_mul_i32 s70, s69, 160\n\t" \
    "s_add_u32 s86, s86, s70\n\t" \
    "s_mul_i32 s70, s69, 536\n\t" \
    "s_add_u32 s87, s87, s70\n\t" \
    "s_mul_i32 s70, s69, 256\n\t" \
    "s_add_u32 s88, s88, s70\n\t" \
    "s_cmp_gt_i32 s97, 0\n\t" \
    "s_cselect_b32 s70, 1, 0\n\t" \
    "s_lshr_b32 s69, s67, 3\n\t" \
    "s_xor_b32 s69, s69, s70\n\t" \
    "s_and_b32 s97, s69, 1\n\t" \
    "v_mov_b32 v148, -1\n\t" \
    "v_mov_b32 v149, -1\n\t" \
    "v_mov_b32 v150, -1\n\t" \
    "v_mov_b32 v151, -1\n\t" \
    "s_mov_b32 s71, 3\n\t" \
    "vv_E7_%=:\n\t" \
    "s_memtime s[54:55]\n\t" \
    "s_nop 0\n\t" \
    "s_memrealtime s[4:5]\n\t" \
    "s_nop 0\n\t" \
    "s_memrealtime s[6:7]\n\t" \
    "s_nop 0\n\t" \
    "s_memrealtime s[8:9]\n\t" \
    "s_nop 0\n\t" \
    "s_memrealtime s[10:11]\n\t" \
    "s_nop 0\n\t" \
    "s_memrealtime s[12:13]\n\t" \
    "s_nop 0\n\t" \
    "s_memrealtime s[14:15]\n\t" \
    "s_nop 0\n\t" \
    "s_memrealtime s[16:17]\n\t" \
    "s_nop 0\n\t" \
    "s_memrealtime s[18:19]\n\t" \
    "s_nop 0\n\t" \
    "s_memrealtime s[20:21]\n\t" \
    "s_nop 0\n\t" \
    "s_memrealtime s[22:23]\n\t" \
    "s_nop 0\n\t" \
    "s_memrealtime s[24:25]\n\t" \
    "s_nop 0\n\t" \
    "s_memrealtime s[26:27]\n\t" \
    "s_nop 0\n\t" \
    "s_memrealtime s[28:29]\n\t" \
    "s_nop 0\n\t" \
    "s_memtime s[56:57]\n\t" \
    "s_mov_b32 s70, 1\n\t" \
    "s_sub_u32 s71, s71, 1\n\t" \
    "s_cselect_b32 s69, 0, -1\n\t" \
    "s_waitcnt lgkmcnt(0)\n\t" \
    "s_sub_u32 s54, s56, s54\n\t" \
    "s_and_b32 s54, s54, s69\n\t" \
    "s_cmp_gt_u32 s54, 124\n\t" \
    "s_cbranch_scc1 vv_E7_%=\n\t" \
    "s_cmp_eq_u32 s4, s28\n\t" \
    "s_addc_u32 s70, s70, 0\n\t" \
    "s_cmp_eq_u32 s6, s28\n\t" \
    "s_addc_u32 s70, s70, 0\n\t" \
    "s_cmp_eq_u32 s8, s28\n\t" \
    "s_addc_u32 s70, s70, 0\n\t" \
    "s_cmp_eq_u32 s10, s28\n\t" \
    "s_addc_u32 s70, s70, 0\n\t" \
    "s_cmp_eq_u32 s12, s28\n\t" \
    "s_addc_u32 s70, s70, 0\n\t" \
    "s_cmp_eq_u32 s14, s28\n\t" \
    "s_addc_u32 s70, s70, 0\n\t" \
    "s_cmp_eq_u32 s16, s28\n\t" \
    "s_addc_u32 s70, s70, 0\n\t" \
    "s_cmp_eq_u32 s18, s28\n\t" \
    "s_addc_u32 s70, s70, 0\n\t" \
    "s_cmp_eq_u32 s20, s28\n\t" \
    "s_addc_u32 s70, s70, 0\n\t" \
    "s_cmp_eq_u32 s22, s28\n\t" \
    "s_addc_u32 s70, s70, 0\n\t" \
    "s_cmp_eq_u32 s24, s28\n\t" \
    "s_addc_u32 s70, s70, 0\n\t" \
    "s_cmp_eq_u32 s26, s28\n\t" \
    "s_addc_u32 s70, s70, 0\n\t" \
    "s_mov_b32 s75, s73\n\t" \
    "s_mov_b32 s76, s74\n\t" \
    "s_lshl_b32 s70, s70, 3\n\t" \
    "s_sub_u32 s73, s56, s70\n\t" \
    "s_lshl_b32 s74, s28, 4\n\t" \
    "s_sub_u32 s69, s73, s75\n\t" \
    "s_sub_u32 s70, s74, s76\n\t" \
    "s_sub_u32 s71, s70, 1600\n\t" \
    "s_cmp_lt_u32 s71, 400000\n\t" \
    "s_cbranch_scc0 vv_K9_%=\n\t" \
    "v_cvt_f32_u32 v140, s69\n\t" \
    "v_cvt_f32_u32 v141, s70\n\t" \
    "v_rcp_f32 v142, v140\n\t" \
    "v_rcp_f32 v143, v141\n\t" \
    "v_mul_f32 v142, v142, v141\n\t" \
    "v_mul_f32 v143, v143, v140\n\t" \
    "v_mul_f32 v142, 0x4f800000, v142\n\t" \
    "v_mul_f32 v143, 0x4f000000, v143\n\t" \
    "v_cvt_u32_f32 v142, v142\n\t" \
    "v_cvt_u32_f32 v143, v143\n\t" \
    "v_readfirstlane_b32 s69, v142\n\t" \
    "v_readfirstlane_b32 s70, v143\n\t" \
    "s_nop 1\n\t" \
    "s_mov_b32 s78, s69\n\t" \
    "s_mov_b32 s77, s70\n\t" \
    "vv_K9_%=:\n\t" \
    "s_mov_b32 s79, s74\n\t" \
    "s_sub_u32 s80, s74, 32000\n\t" \
    "s_cmp_eq_u32 s66, 1\n\t" \
    "s_cbranch_scc1 vv_loop11_%=\n\t" \
    "s_cmp_eq_u32 s66, 2\n\t" \
    "s_cbranch_scc1 vv_loop12_%=\n\t" \
    "s_cmp_eq_u32 s66, 3\n\t" \
    "s_cbranch_scc1 vv_loop13_%=\n\t" \
    "vv_loop10_%=:\n\t" \
    "v_mov_b32 v72, 0\n\t" \
    "v_mov_b32 v73, 0\n\t" \
    "v_mov_b32 v74, 0\n\t" \
    "v_mov_b32 v75, 0\n\t" \
    "v_mov_b32 v76, 0\n\t" \
    "v_mov_b32 v77, 0\n\t" \
    "v_mov_b32 v78, 0\n\t" \
    "v_mov_b32 v79, 0\n\t" \
    "v_mov_b32 v92, 0\n\t" \
    "v_mov_b32 v93, 0\n\t" \
    "v_mov_b32 v94, 0\n\t" \
    "v_mov_b32 v95, 0\n\t" \
    "v_mov_b32 v96, 0\n\t" \
    "v_mov_b32 v97, 0\n\t" \
    "v_mov_b32 v98, 0\n\t" \
    "v_mov_b32 v99, 0\n\t" \
    "v_mov_b32 v100, 0\n\t" \
    "v_mov_b32 v101, 0\n\t" \
    "v_mov_b32 v102, 0\n\t" \
    "v_mov_b32 v103, 0\n\t" \
    "v_mov_b32 v104, 0\n\t" \
    "v_mov_b32 v105, 0\n\t" \
    "v_mov_b32 v106, 0\n\t" \
    "v_mov_b32 v107, 0\n\t" \
    "v_mov_b32 v108, 0\n\t" \
    "v_mov_b32 v109, 0\n\t" \
    "v_mov_b32 v110, 0\n\t" \
    "v_mov_b32 v111, 0\n\t" \
    "v_mov_b32 v112, 0\n\t" \
    "v_mov_b32 v113, 0\n\t" \
    "v_mov_b32 v114, 0\n\t" \
    "v_mov_b32 v115, 0\n\t" \
    "v_mov_b32 v116, 0\n\t" \
    "v_mov_b32 v117, 0\n\t" \
    "v_mov_b32 v118, 0\n\t" \
    "v_mov_b32 v119, 0\n\t" \
    "v_mov_b32 v120, 0\n\t" \
    "v_mov_b32 v121, 0\n\t" \
    "v_mov_b32 v122, 0\n\t" \
    "v_mov_b32 v123, 0\n\t" \
    "v_mov_b32 v124, 0\n\t" \
    "v_mov_b32 v125, 0\n\t" \
    "v_mov_b32 v126, 0\n\t" \
    "v_mov_b32 v127, 0\n\t" \
    "vv_go15_%=:\n\t" \
    "s_add_u32 s69, s65, 0\n\t" \
    "s_bitcmp1_b32 s69, 0\n\t" \
    "s_cselect_b32 s70, s63, s62\n\t" \
    "s_bfe_u32 s69, s69, 0x20001\n\t" \
    "s_lshl_b32 s69, s69, 19\n\t" \
    "s_add_u32 s69, s69, s70\n\t" \
    "s_add_u32 s46, s36, s69\n\t" \
    "s_addc_u32 s47, s37, 0\n\t" \
    "s_add_u32 s69, s65, 0\n\t" \
    "s_lshr_b32 s69, s69, 1\n\t" \
    "s_xor_b32 s69, s69, s97\n\t" \
    "s_and_b32 s69, s69, 1\n\t" \
    "s_lshl_b32 s69, s69, 14\n\t" \
    "s_or_b32 s46, s46, s69\n\t" \
    "s_add_u32 s69, s65, 0\n\t" \
    "s_mul_i32 s70, s69, 171\n\t" \
    "s_lshr_b32 s70, s70, 9\n\t" \
    "s_mul_i32 s70, s70, 3\n\t" \
    "s_sub_u32 s69, s69, s70\n\t" \
    "s_mul_i32 s69, s69, 36\n\t" \
    "s_lshl_b32 s69, s69, 14\n\t" \
    "s_add_u32 s69, s69, s64\n\t" \
    "s_add_u32 s69, s69, 4194304\n\t" \
    "s_add_u32 s38, s36, s69\n\t" \
    "s_addc_u32 s39, s37, 0\n\t" \
    "s_mul_i32 s69, s65, 9216\n\t" \
    "s_add_u32 s69, s69, 123731968\n\t" \
    "s_add_u32 s40, s36, s69\n\t" \
    "s_addc_u32 s41, s37, 0\n\t" \
    "s_sub_u32 s70, s79, s74\n\t" \
    "s_max_i32 s70, s70, 0\n\t" \
    "s_min_i32 s70, s70, 16000\n\t" \
    "s_lshl_b32 s70, s70, 1\n\t" \
    "s_mul_hi_u32 s70, s70, s77\n\t" \
    "s_add_u32 s70, s70, s73\n\t" \
    "s_sub_u32 s70, s70, 100\n\t" \
    "s_getpc_b64 s[30:31]\n\t" \
    "vv_Wp17_%=:\n\t" \
    "s_add_u32 s30, s30, vv_We18_%=-vv_Wp17_%=\n\t" \
    "s_addc_u32 s31, s31, 0\n\t" \
    "vv_W16_%=:\n\t" \
    "s_memtime s[56:57]\n\t" \
    "s_waitcnt lgkmcnt(0)\n\t" \
    "s_sub_u32 s69, s70, s56\n\t" \
    "s_cmp_gt_i32 s69, 111\n\t" \
    "s_cbranch_scc1 vv_W16_%=\n\t" \
    "s_max_i32 s69, s69, 0\n\t" \
    "s_lshr_b32 s69, s69, 1\n\t" \
    "s_and_b32 s69, s69, 0x3c\n\t" \
    "s_sub_u32 s30, s30, s69\n\t" \
    "s_subb_u32 s31, s31, 0\n\t" \
    "s_setpc_b64 s[30:31]\n\t" \
    ".p2align 6\n\t" \
    "s_nop 0\n\t" \
    "s_nop 0\n\t" \
    "s_nop 0\n\t" \
    "s_nop 1\n\t" \
    "s_nop 1\n\t" \
    "s_nop 1\n\t" \
    "s_nop 1\n\t" \
    "s_nop 1\n\t" \
    "s_nop 1\n\t" \
    "s_nop 1\n\t" \
    "s_nop 1\n\t" \
    "s_nop 1\n\t" \
    "s_nop 1\n\t" \
    "s_nop 1\n\t" \
    "s_nop 1\n\t" \
    "s_nop 1\n\t" \
    "vv_We18_%=:\n\t" \
    "vv_G19_%=:\n\t" \
    "global_load_dwordx4 v[36:39], v1, s[46:47] sc1\n\t" \
    "s_nop 9\n\t" \
    "global_load_dwordx4 v[40:43], v1, s[46:47] offset:1024 sc1\n\t" \
    "s_nop 9\n\t" \
    "global_load_dwordx4 v[44:47], v2, s[46:47] sc1\n\t" \
    "s_waitcnt vmcnt(2)\n\t" \
    "v_smfmac_f32_16x16x64_bf16 v[72:75], v[36:39], a[0:7], v10\n\t" \
    "ds_write_b128 v0, v[36:39] offset:0\n\t" \
    "v_smfmac_f32_16x16x64_bf16 v[76:79], v[36:39], a[72:79], v10\n\t" \
    "v_max3_u32 v140, v36, v37, v38\n\t" \
    "s_waitcnt vmcnt(1)\n\t" \
    "ds_write_b128 v0, v[40:43] offset:1024\n\t" \
    "v_smfmac_f32_16x16x64_bf16 v[72:75], v[40:43], a[8:15], v10\n\t" \
    "v_max3_u32 v140, v140, v39, v40\n\t" \
    "v_max3_u32 v140, v140, v41, v42\n\t" \
    "v_max_u32 v140, v140, v43\n\t" \
    "v_cmp_eq_u32 vcc, -1, v140\n\t" \
    "v_smfmac_f32_16x16x64_bf16 v[76:79], v[40:43], a[80:87], v10\n\t" \
    "s_cbranch_vccnz vv_Gbad20_%=\n\t" \
    "vv_Gc22_%=:\n\t" \
    "s_waitcnt lgkmcnt(0)\n\t" \
    "s_barrier\n\t" \
    "ds_read_b128 v[48:51], v0 offset:2048\n\t" \
    "ds_read_b128 v[52:55], v0 offset:3072\n\t" \
    "s_waitcnt vmcnt(0)\n\t" \
    "v_smfmac_f32_16x16x64_bf16 v[72:75], v[44:47], a[64:71], v10\n\t" \
    "ds_read_b128 v[56:59], v0 offset:4096\n\t" \
    "ds_read_b128 v[60:63], v0 offset:5120\n\t" \
    "v_max3_u32 v140, v44, v45, v46\n\t" \
    "v_max_u32 v140, v140, v47\n\t" \
    "v_smfmac_f32_16x16x64_bf16 v[76:79], v[44:47], a[136:143], v10\n\t" \
    "ds_read_b128 v[64:67], v0 offset:6144\n\t" \
    "ds_read_b128 v[68:71], v0 offset:7168\n\t" \
    "v_cmp_eq_u32 vcc, -1, v140\n\t" \
    "s_cbranch_vccnz vv_Gbad221_%=\n\t" \
    "vv_Gc223_%=:\n\t" \
    "s_waitcnt lgkmcnt(5)\n\t" \
    "v_smfmac_f32_16x16x64_bf16 v[72:75], v[48:51], a[16:23], v10\n\t" \
    "s_waitcnt lgkmcnt(4)\n\t" \
    "v_smfmac_f32_16x16x64_bf16 v[72:75], v[52:55], a[24:31], v10\n\t" \
    "s_waitcnt lgkmcnt(3)\n\t" \
    "v_smfmac_f32_16x16x64_bf16 v[72:75], v[56:59], a[32:39], v10\n\t" \
    "s_waitcnt lgkmcnt(2)\n\t" \
    "v_smfmac_f32_16x16x64_bf16 v[72:75], v[60:63], a[40:47], v10\n\t" \
    "s_waitcnt lgkmcnt(1)\n\t" \
    "v_smfmac_f32_16x16x64_bf16 v[72:75], v[64:67], a[48:55], v10\n\t" \
    "s_waitcnt lgkmcnt(0)\n\t" \
    "v_smfmac_f32_16x16x64_bf16 v[72:75], v[68:71], a[56:63], v10\n\t" \
    "v_smfmac_f32_16x16x64_bf16 v[76:79], v[48:51], a[88:95], v10\n\t" \
    "s_nop 1\n\t" \
    "s_nop 0\n\t" \
    "v_smfmac_f32_16x16x64_bf16 v[76:79], v[52:55], a[96:103], v10\n\t" \
    "s_nop 1\n\t" \
    "s_nop 0\n\t" \
    "v_smfmac_f32_16x16x64_bf16 v[76:79], v[56:59], a[104:111], v10\n\t" \
    "v_add_f32 v72, v72, v74\n\t" \
    "v_smfmac_f32_16x16x64_bf16 v[76:79], v[60:63], a[112:119], v10\n\t" \
    "v_add_f32 v73, v73, v75\n\t" \
    "v_smfmac_f32_16x16x64_bf16 v[76:79], v[64:67], a[120:127], v10\n\t" \
    "v_cvt_pk_bf16_f32 v80, v72, v73\n\t" \
    "v_smfmac_f32_16x16x64_bf16 v[76:79], v[68:71], a[128:135], v10\n\t" \
    "ds_write_b32 v12, v80 offset:0\n\t" \
    "s_nop 6\n\t" \
    "v_add_f32 v76, v76, v78\n\t" \
    "v_add_f32 v77, v77, v79\n\t" \
    "v_cvt_pk_bf16_f32 v81, v76, v77\n\t" \
    "ds_write_b32 v12, v81 offset:64\n\t" \
    "s_waitcnt lgkmcnt(0)\n\t" \
    "s_barrier\n\t" \
    "ds_read_b64_tr_b16 v[84:85], v13 offset:0\n\t" \
    "ds_read_b64_tr_b16 v[86:87], v13 offset:128\n\t" \
    "ds_read_b64_tr_b16 v[88:89], v13 offset:1024\n\t" \
    "ds_read_b64_tr_b16 v[90:91], v13 offset:1152\n\t" \
    "s_waitcnt lgkmcnt(2)\n\t" \
    "v_smfmac_f32_16x16x64_bf16 v[92:95], v[84:87], a[144:151], v10\n\t" \
    "s_nop 2\n\t" \
    "s_waitcnt lgkmcnt(0)\n\t" \
    "v_smfmac_f32_16x16x64_bf16 v[92:95], v[88:91], a[152:159], v10\n\t" \
    "s_nop 2\n\t" \
    "v_smfmac_f32_16x16x64_bf16 v[96:99], v[84:87], a[160:167], v10\n\t" \
    "s_nop 2\n\t" \
    "v_smfmac_f32_16x16x64_bf16 v[96:99], v[88:91], a[168:175], v10\n\t" \
    "s_nop 2\n\t" \
    "v_smfmac_f32_16x16x64_bf16 v[100:103], v[84:87], a[176:183], v10\n\t" \
    "s_nop 0\n\t" \
    "v_add_f32 v92, v92, v94\n\t" \
    "v_add_f32 v93, v93, v95\n\t" \
    "v_smfmac_f32_16x16x64_bf16 v[100:103], v[88:91], a[184:191], v10\n\t" \
    "s_nop 1\n\t" \
    "v_cvt_pk_bf16_f32 v128, v92, v93\n\t" \
    "v_smfmac_f32_16x16x64_bf16 v[104:107], v[84:87], a[192:199], v10\n\t" \
    "s_nop 0\n\t" \
    "v_add_f32 v96, v96, v98\n\t" \
    "v_add_f32 v97, v97, v99\n\t" \
    "v_smfmac_f32_16x16x64_bf16 v[104:107], v[88:91], a[200:207], v10\n\t" \
    "s_nop 1\n\t" \
    "v_cvt_pk_bf16_f32 v129, v96, v97\n\t" \
    "v_smfmac_f32_16x16x64_bf16 v[124:127], v[84:87], v[240:247], v10\n\t" \
    "s_nop 0\n\t" \
    "v_add_f32 v100, v100, v102\n\t" \
    "v_add_f32 v101, v101, v103\n\t" \
    "v_smfmac_f32_16x16x64_bf16 v[124:127], v[88:91], v[248:255], v10\n\t" \
    "s_nop 1\n\t" \
    "global_load_dword v140, v20, s[46:47] sc1\n\t" \
    "v_cvt_pk_bf16_f32 v130, v100, v101\n\t" \
    "v_smfmac_f32_16x16x64_bf16 v[108:111], v[84:87], a[208:215], v10\n\t" \
    "s_nop 0\n\t" \
    "v_add_f32 v104, v104, v106\n\t" \
    "v_add_f32 v105, v105, v107\n\t" \
    "v_smfmac_f32_16x16x64_bf16 v[108:111], v[88:91], a[216:223], v10\n\t" \
    "s_nop 0\n\t" \
    "v_cvt_pk_bf16_f32 v131, v104, v105\n\t" \
    "v_permlane32_swap_b32 v128, v130\n\t" \
    "v_smfmac_f32_16x16x64_bf16 v[112:115], v[84:87], a[224:231], v10\n\t" \
    "s_nop 0\n\t" \
    "v_permlane32_swap_b32 v129, v131\n\t" \
    "s_nop 1\n\t" \
    "v_permlane16_swap_b32 v128, v129\n\t" \
    "v_smfmac_f32_16x16x64_bf16 v[112:115], v[88:91], a[232:239], v10\n\t" \
    "v_permlane16_swap_b32 v130, v131\n\t" \
    "global_store_dwordx4 v14, v[128:131], s[38:39] sc1\n\t" \
    "v_add_f32 v124, v124, v126\n\t" \
    "v_smfmac_f32_16x16x64_bf16 v[116:119], v[84:87], a[240:247], v10\n\t" \
    "s_nop 0\n\t" \
    "v_add_f32 v125, v125, v127\n\t" \
    "v_cvt_pk_bf16_f32 v136, v124, v125\n\t" \
    "v_smfmac_f32_16x16x64_bf16 v[116:119], v[88:91], a[248:255], v10\n\t" \
    "global_store_dword v16, v136, s[38:39] sc1\n\t" \
    "v_add_f32 v108, v108, v110\n\t" \
    "v_add_f32 v109, v109, v111\n\t" \
    "v_smfmac_f32_16x16x64_bf16 v[120:123], v[84:87], v[224:231], v10\n\t" \
    "s_nop 0\n\t" \
    "v_cvt_pk_bf16_f32 v132, v108, v109\n\t" \
    "v_add_f32 v112, v112, v114\n\t" \
    "v_smfmac_f32_16x16x64_bf16 v[120:123], v[88:91], v[232:239], v10\n\t" \
    "v_add_f32 v113, v113, v115\n\t" \
    "v_cvt_pk_bf16_f32 v133, v112, v113\n\t" \
    "v_add_f32 v116, v116, v118\n\t" \
    "v_add_f32 v117, v117, v119\n\t" \
    "v_cvt_pk_bf16_f32 v134, v116, v117\n\t" \
    "s_nop 2\n\t" \
    "v_pk_add_f32 v[120:121], v[120:121], v[122:123]\n\t" \
    "v_cvt_pk_bf16_f32 v135, v120, v121\n\t" \
    "v_permlane32_swap_b32 v132, v134\n\t" \
    "s_nop 0\n\t" \
    "v_permlane32_swap_b32 v133, v135\n\t" \
    "s_nop 1\n\t" \
    "v_permlane16_swap_b32 v132, v133\n\t" \
    "v_permlane16_swap_b32 v134, v135\n\t" \
    "global_store_dwordx4 v15, v[132:135], s[38:39] sc1\n\t" \
    "s_nop 1\n\t" \
    "global_store_dwordx4 v17, v[128:131], s[40:41] sc1\n\t" \
    "global_store_dwordx4 v17, v[132:135], s[40:41] offset:1024 sc1\n\t" \
    "global_store_dword v18, v136, s[40:41] sc1\n\t" \
    "s_cmp_eq_u32 s65, 63\n\t" \
    "s_cbranch_scc1 vv_end14_%=\n\t" \
    "s_mov_b32 s71, 3\n\t" \
    "vv_E25_%=:\n\t" \
    "s_memtime s[54:55]\n\t" \
    "s_nop 0\n\t" \
    "s_memrealtime s[4:5]\n\t" \
    "s_nop 0\n\t" \
    "s_memrealtime s[6:7]\n\t" \
    "s_nop 0\n\t" \
    "s_memrealtime s[8:9]\n\t" \
    "s_nop 0\n\t" \
    "s_memrealtime s[10:11]\n\t" \
    "s_nop 0\n\t" \
    "s_memrealtime s[12:13]\n\t" \
    "s_nop 0\n\t" \
    "s_memrealtime s[14:15]\n\t" \
    "s_nop 0\n\t" \
    "s_memrealtime s[16:17]\n\t" \
    "s_nop 0\n\t" \
    "s_memrealtime s[18:19]\n\t" \
    "s_nop 0\n\t" \
    "s_memrealtime s[20:21]\n\t" \
    "s_nop 0\n\t" \
    "s_memrealtime s[22:23]\n\t" \
    "s_nop 0\n\t" \
    "s_memrealtime s[24:25]\n\t" \
    "s_nop 0\n\t" \
    "s_memrealtime s[26:27]\n\t" \
    "s_nop 0\n\t" \
    "s_memrealtime s[28:29]\n\t" \
    "s_nop 0\n\t" \
    "s_memtime s[56:57]\n\t" \
    "s_mov_b32 s70, 1\n\t" \
    "s_sub_u32 s71, s71, 1\n\t" \
    "s_cselect_b32 s69, 0, -1\n\t" \
    "s_waitcnt lgkmcnt(0)\n\t" \
    "s_sub_u32 s54, s56, s54\n\t" \
    "s_and_b32 s54, s54, s69\n\t" \
    "s_cmp_gt_u32 s54, 124\n\t" \
    "s_cbranch_scc1 vv_E25_%=\n\t" \
    "s_cmp_eq_u32 s4, s28\n\t" \
    "s_addc_u32 s70, s70, 0\n\t" \
    "s_cmp_eq_u32 s6, s28\n\t" \
    "s_addc_u32 s70, s70, 0\n\t" \
    "s_cmp_eq_u32 s8, s28\n\t" \
    "s_addc_u32 s70, s70, 0\n\t" \
    "s_cmp_eq_u32 s10, s28\n\t" \
    "s_addc_u32 s70, s70, 0\n\t" \
    "s_cmp_eq_u32 s12, s28\n\t" \
    "s_addc_u32 s70, s70, 0\n\t" \
    "s_cmp_eq_u32 s14, s28\n\t" \
    "s_addc_u32 s70, s70, 0\n\t" \
    "s_cmp_eq_u32 s16, s28\n\t" \
    "s_addc_u32 s70, s70, 0\n\t" \
    "s_cmp_eq_u32 s18, s28\n\t" \
    "s_addc_u32 s70, s70, 0\n\t" \
    "s_cmp_eq_u32 s20, s28\n\t" \
    "s_addc_u32 s70, s70, 0\n\t" \
    "s_cmp_eq_u32 s22, s28\n\t" \
    "s_addc_u32 s70, s70, 0\n\t" \
    "s_cmp_eq_u32 s24, s28\n\t" \
    "s_addc_u32 s70, s70, 0\n\t" \
    "s_cmp_eq_u32 s26, s28\n\t" \
    "s_addc_u32 s70, s70, 0\n\t" \
    "s_mov_b32 s75, s73\n\t" \
    "s_mov_b32 s76, s74\n\t" \
    "s_lshl_b32 s70, s70, 3\n\t" \
    "s_sub_u32 s73, s56, s70\n\t" \
    "s_lshl_b32 s74, s28, 4\n\t" \
    "s_sub_u32 s69, s73, s75\n\t" \
    "s_sub_u32 s70, s74, s76\n\t" \
    "s_sub_u32 s71, s70, 1600\n\t" \
    "s_cmp_lt_u32 s71, 400000\n\t" \
    "s_cbranch_scc0 vv_K27_%=\n\t" \
    "v_cvt_f32_u32 v141, s69\n\t" \
    "v_cvt_f32_u32 v142, s70\n\t" \
    "v_rcp_f32 v143, v141\n\t" \
    "v_rcp_f32 v144, v142\n\t" \
    "v_mul_f32 v143, v143, v142\n\t" \
    "v_mul_f32 v144, v144, v141\n\t" \
    "v_mul_f32 v143, 0x4f800000, v143\n\t" \
    "v_mul_f32 v144, 0x4f000000, v144\n\t" \
    "v_cvt_u32_f32 v143, v143\n\t" \
    "v_cvt_u32_f32 v144, v144\n\t" \
    "v_readfirstlane_b32 s69, v143\n\t" \
    "v_readfirstlane_b32 s70, v144\n\t" \
    "s_nop 1\n\t" \
    "s_sub_u32 s69, s69, s78\n\t" \
    "s_ashr_i32 s69, s69, 2\n\t" \
    "s_add_u32 s78, s78, s69\n\t" \
    "s_sub_u32 s70, s70, s77\n\t" \
    "s_ashr_i32 s70, s70, 2\n\t" \
    "s_add_u32 s77, s77, s70\n\t" \
    "vv_K27_%=:\n\t" \
    "s_waitcnt vmcnt(6)\n\t" \
    "v_cmp_eq_u32 vcc, -1, v140\n\t" \
    "s_cbranch_vccz vv_Hok29_%=\n\t" \
    "vv_H28_%=:\n\t" \
    "global_load_dword v140, v20, s[46:47] sc1\n\t" \
    "s_waitcnt vmcnt(0)\n\t" \
    "v_cmp_eq_u32 vcc, -1, v140\n\t" \
    "s_cbranch_vccz vv_Hc30_%=\n\t" \
    "s_cmp_eq_u32 s68, 0\n\t" \
    "s_cbranch_scc1 vv_F31_%=\n\t" \
    "s_sub_u32 s68, s68, 1\n\t" \
    "s_sleep 5\n\t" \
    "s_branch vv_H28_%=\n\t" \
    "vv_F31_%=:\n\t" \
    "v_mov_b32 v141, 6095104\n\t" \
    "v_mov_b32 v142, 6\n\t" \
    "global_store_dword v141, v142, s[36:37] sc1\n\t" \
    "s_waitcnt vmcnt(0)\n\t" \
    "s_branch vv_Hc30_%=\n\t" \
    "vv_Hc30_%=:\n\t" \
    "vv_Hok29_%=:\n\t" \
    "v_subrev_u32 v140, s80, v140\n\t" \
    "v_and_b32 v20, -5, v20\n\t" \
    "s_nop 0\n\t" \
    "v_max_i32_dpp v140, v140, v140 row_ror:1 row_mask:0xf bank_mask:0xf\n\t" \
    "s_nop 1\n\t" \
    "v_max_i32_dpp v140, v140, v140 row_ror:2 row_mask:0xf bank_mask:0xf\n\t" \
    "s_nop 1\n\t" \
    "v_max_i32_dpp v140, v140, v140 row_ror:4 row_mask:0xf bank_mask:0xf\n\t" \
    "s_nop 1\n\t" \
    "v_max_i32_dpp v140, v140, v140 row_ror:8 row_mask:0xf bank_mask:0xf\n\t" \
    "s_nop 0\n\t" \
    "v_readlane_b32 s69, v140, 0\n\t" \
    "v_readlane_b32 s71, v140, 16\n\t" \
    "v_readlane_b32 s70, v140, 32\n\t" \
    "v_readlane_b32 s81, v140, 48\n\t" \
    "s_max_i32 s69, s69, s71\n\t" \
    "s_nop 0\n\t" \
    "s_max_i32 s71, s70, s81\n\t" \
    "s_max_i32 s69, s69, s71\n\t" \
    "s_add_u32 s81, s69, s80\n\t" \
    "s_cmp_lg_u32 s66, 0\n\t" \
    "s_cbranch_scc1 vv_SF32_%=\n\t" \
    "s_lshl_b32 s69, s67, 7\n\t" \
    "s_add_u32 s69, s69, 6131712\n\t" \
    "s_add_u32 s69, s69, s52\n\t" \
    "s_add_u32 s70, s65, -1\n\t" \
    "v_mov_b32 v140, s69\n\t" \
    "v_mov_b32 v141, s70\n\t" \
    "s_mov_b64 exec, 1\n\t" \
    "global_store_dword v140, v141, s[36:37] sc1\n\t" \
    "s_mov_b64 exec, -1\n\t" \
    "vv_SF32_%=:\n\t" \
    "v_mov_b32 v72, 0\n\t" \
    "v_mov_b32 v73, 0\n\t" \
    "v_mov_b32 v74, 0\n\t" \
    "v_mov_b32 v75, 0\n\t" \
    "v_mov_b32 v76, 0\n\t" \
    "v_mov_b32 v77, 0\n\t" \
    "v_mov_b32 v78, 0\n\t" \
    "v_mov_b32 v79, 0\n\t" \
    "v_mov_b32 v92, 0\n\t" \
    "v_mov_b32 v93, 0\n\t" \
    "v_mov_b32 v94, 0\n\t" \
    "v_mov_b32 v95, 0\n\t" \
    "v_mov_b32 v96, 0\n\t" \
    "v_mov_b32 v97, 0\n\t" \
    "v_mov_b32 v98, 0\n\t" \
    "v_mov_b32 v99, 0\n\t" \
    "v_mov_b32 v100, 0\n\t" \
    "v_mov_b32 v101, 0\n\t" \
    "v_mov_b32 v102, 0\n\t" \
    "v_mov_b32 v103, 0\n\t" \
    "v_mov_b32 v104, 0\n\t" \
    "v_mov_b32 v105, 0\n\t" \
    "v_mov_b32 v106, 0\n\t" \
    "v_mov_b32 v107, 0\n\t" \
    "v_mov_b32 v108, 0\n\t" \
    "v_mov_b32 v109, 0\n\t" \
    "v_mov_b32 v110, 0\n\t" \
    "v_mov_b32 v111, 0\n\t" \
    "v_mov_b32 v112, 0\n\t" \
    "v_mov_b32 v113, 0\n\t" \
    "v_mov_b32 v114, 0\n\t" \
    "v_mov_b32 v115, 0\n\t" \
    "v_mov_b32 v116, 0\n\t" \
    "v_mov_b32 v117, 0\n\t" \
    "v_mov_b32 v118, 0\n\t" \
    "v_mov_b32 v119, 0\n\t" \
    "v_mov_b32 v120, 0\n\t" \
    "v_mov_b32 v121, 0\n\t" \
    "v_mov_b32 v122, 0\n\t" \
    "v_mov_b32 v123, 0\n\t" \
    "v_mov_b32 v124, 0\n\t" \
    "v_mov_b32 v125, 0\n\t" \
    "v_mov_b32 v126, 0\n\t" \
    "v_mov_b32 v127, 0\n\t" \
    "s_cmp_lg_u32 s65, 0\n\t" \
    "s_cbranch_scc1 vv_Oc35_%=\n\t" \
    "s_add_u32 s79, s81, 240\n\t" \
    "s_sub_u32 s70, s79, s74\n\t" \
    "s_max_i32 s70, s70, 0\n\t" \
    "s_min_i32 s70, s70, 16000\n\t" \
    "s_lshl_b32 s70, s70, 1\n\t" \
    "s_mul_hi_u32 s70, s70, s77\n\t" \
    "s_add_u32 s70, s70, s73\n\t" \
    "vv_W36_%=:\n\t" \
    "s_memtime s[56:57]\n\t" \
    "s_waitcnt lgkmcnt(0)\n\t" \
    "s_sub_u32 s69, s70, s56\n\t" \
    "s_cmp_gt_i32 s69, 24\n\t" \
    "s_cbranch_scc1 vv_W36_%=\n\t" \
    "global_load_dword v140, v35, s[46:47] sc1\n\t" \
    "s_waitcnt vmcnt(0)\n\t" \
    "v_cmp_eq_u32 vcc, -1, v140\n\t" \
    "s_cbranch_vccz vv_Ook34_%=\n\t" \
    "vv_O33_%=:\n\t" \
    "global_load_dword v140, v35, s[46:47] offset:1536 sc1\n\t" \
    "s_waitcnt vmcnt(0)\n\t" \
    "v_cmp_eq_u32 vcc, -1, v140\n\t" \
    "s_cbranch_vccz vv_Ook34_%=\n\t" \
    "s_cmp_eq_u32 s68, 0\n\t" \
    "s_cbranch_scc1 vv_F37_%=\n\t" \
    "s_sub_u32 s68, s68, 1\n\t" \
    "s_sleep 3\n\t" \
    "s_branch vv_O33_%=\n\t" \
    "vv_F37_%=:\n\t" \
    "v_mov_b32 v141, 6095104\n\t" \
    "v_mov_b32 v142, 17\n\t" \
    "global_store_dword v141, v142, s[36:37] sc1\n\t" \
    "s_waitcnt vmcnt(0)\n\t" \
    "s_branch vv_Ook34_%=\n\t" \
    "vv_Ook34_%=:\n\t" \
    "v_subrev_u32 v140, s80, v140\n\t" \
    "s_nop 1\n\t" \
    "v_max_i32_dpp v140, v140, v140 row_ror:1 row_mask:0xf bank_mask:0xf\n\t" \
    "s_nop 1\n\t" \
    "v_max_i32_dpp v140, v140, v140 row_ror:2 row_mask:0xf bank_mask:0xf\n\t" \
    "s_nop 1\n\t" \
    "v_max_i32_dpp v140, v140, v140 row_ror:4 row_mask:0xf bank_mask:0xf\n\t" \
    "s_nop 1\n\t" \
    "v_max_i32_dpp v140, v140, v140 row_ror:8 row_mask:0xf bank_mask:0xf\n\t" \
    "s_nop 0\n\t" \
    "v_readlane_b32 s69, v140, 0\n\t" \
    "v_readlane_b32 s71, v140, 16\n\t" \
    "v_readlane_b32 s70, v140, 32\n\t" \
    "v_readlane_b32 s81, v140, 48\n\t" \
    "s_max_i32 s69, s69, s71\n\t" \
    "s_nop 0\n\t" \
    "s_max_i32 s71, s70, s81\n\t" \
    "s_max_i32 s69, s69, s71\n\t" \
    "s_add_u32 s81, s69, s80\n\t" \
    "vv_Oc35_%=:\n\t" \
    "s_mul_hi_u32 s69, s85, s78\n\t" \
    "s_add_u32 s79, s81, s69\n\t" \
    "s_add_u32 s79, s79, s87\n\t" \
    "s_cmp_ge_u32 s65, 36\n\t" \
    "s_cselect_b32 s69, 28, 0\n\t" \
    "s_sub_u32 s79, s79, s69\n\t" \
    "s_cmp_lt_u32 s65, 8\n\t" \
    "s_cselect_b32 s69, 96, 0\n\t" \
    "s_add_u32 s79, s79, s69\n\t" \
    "s_cmp_ge_u32 s65, 57\n\t" \
    "s_cselect_b32 s69, 96, 0\n\t" \
    "s_add_u32 s79, s79, s69\n\t" \
    "s_add_u32 s80, s79, s88\n\t" \
    "s_add_u32 s65, s65, 1\n\t" \
    "s_branch vv_go15_%=\n\t" \
    "vv_Gbad20_%=:\n\t" \
    "s_waitcnt vmcnt(0) lgkmcnt(0)\n\t" \
    "s_nop 7\n\t" \
    "v_mov_b32 v72, 0\n\t" \
    "v_mov_b32 v73, 0\n\t" \
    "v_mov_b32 v74, 0\n\t" \
    "v_mov_b32 v75, 0\n\t" \
    "v_mov_b32 v76, 0\n\t" \
    "v_mov_b32 v77, 0\n\t" \
    "v_mov_b32 v78, 0\n\t" \
    "v_mov_b32 v79, 0\n\t" \
    "s_cmp_eq_u32 s68, 0\n\t" \
    "s_cbranch_scc1 vv_F38_%=\n\t" \
    "s_sub_u32 s68, s68, 1\n\t" \
    "s_branch vv_G19_%=\n\t" \
    "vv_F38_%=:\n\t" \
    "v_mov_b32 v140, 6095104\n\t" \
    "v_mov_b32 v141, 1\n\t" \
    "global_store_dword v140, v141, s[36:37] sc1\n\t" \
    "s_waitcnt vmcnt(0)\n\t" \
    "s_branch vv_Gc22_%=\n\t" \
    "vv_Gbad221_%=:\n\t" \
    "global_load_dwordx4 v[44:47], v2, s[46:47] sc1\n\t" \
    "s_waitcnt vmcnt(0)\n\t" \
    "v_max3_u32 v140, v44, v45, v46\n\t" \
    "v_max_u32 v140, v140, v47\n\t" \
    "v_cmp_eq_u32 vcc, -1, v140\n\t" \
    "s_cbranch_vccz vv_Gre224_%=\n\t" \
    "s_cmp_eq_u32 s68, 0\n\t" \
    "s_cbranch_scc1 vv_F39_%=\n\t" \
    "s_sub_u32 s68, s68, 1\n\t" \
    "s_sleep 1\n\t" \
    "s_branch vv_Gbad221_%=\n\t" \
    "vv_F39_%=:\n\t" \
    "v_mov_b32 v140, 6095104\n\t" \
    "v_mov_b32 v141, 2\n\t" \
    "global_store_dword v140, v141, s[36:37] sc1\n\t" \
    "s_waitcnt vmcnt(0)\n\t" \
    "s_branch vv_Gre224_%=\n\t" \
    "vv_Gre224_%=:\n\t" \
    "v_mov_b32 v72, 0\n\t" \
    "v_mov_b32 v73, 0\n\t" \
    "v_mov_b32 v74, 0\n\t" \
    "v_mov_b32 v75, 0\n\t" \
    "v_mov_b32 v76, 0\n\t" \
    "v_mov_b32 v77, 0\n\t" \
    "v_mov_b32 v78, 0\n\t" \
    "v_mov_b32 v79, 0\n\t" \
    "v_smfmac_f32_16x16x64_bf16 v[72:75], v[36:39], a[0:7], v10\n\t" \
    "s_nop 0\n\t" \
    "v_smfmac_f32_16x16x64_bf16 v[76:79], v[36:39], a[72:79], v10\n\t" \
    "v_smfmac_f32_16x16x64_bf16 v[72:75], v[40:43], a[8:15], v10\n\t" \
    "v_smfmac_f32_16x16x64_bf16 v[76:79], v[40:43], a[80:87], v10\n\t" \
    "v_smfmac_f32_16x16x64_bf16 v[72:75], v[44:47], a[64:71], v10\n\t" \
    "v_smfmac_f32_16x16x64_bf16 v[76:79], v[44:47], a[136:143], v10\n\t" \
    "s_branch vv_Gc223_%=\n\t" \
    "vv_end14_%=:\n\t" \
    "s_endpgm\n\t" \
    "vv_loop11_%=:\n\t" \
    "v_mov_b32 v72, 0\n\t" \
    "v_mov_b32 v73, 0\n\t" \
    "v_mov_b32 v74, 0\n\t" \
    "v_mov_b32 v75, 0\n\t" \
    "v_mov_b32 v76, 0\n\t" \
    "v_mov_b32 v77, 0\n\t" \
    "v_mov_b32 v78, 0\n\t" \
    "v_mov_b32 v79, 0\n\t" \
    "v_mov_b32 v92, 0\n\t" \
    "v_mov_b32 v93, 0\n\t" \
    "v_mov_b32 v94, 0\n\t" \
    "v_mov_b32 v95, 0\n\t" \
    "v_mov_b32 v96, 0\n\t" \
    "v_mov_b32 v97, 0\n\t" \
    "v_mov_b32 v98, 0\n\t" \
    "v_mov_b32 v99, 0\n\t" \
    "v_mov_b32 v100, 0\n\t" \
    "v_mov_b32 v101, 0\n\t" \
    "v_mov_b32 v102, 0\n\t" \
    "v_mov_b32 v103, 0\n\t" \
    "v_mov_b32 v104, 0\n\t" \
    "v_mov_b32 v105, 0\n\t" \
    "v_mov_b32 v106, 0\n\t" \
    "v_mov_b32 v107, 0\n\t" \
    "v_mov_b32 v108, 0\n\t" \
    "v_mov_b32 v109, 0\n\t" \
    "v_mov_b32 v110, 0\n\t" \
    "v_mov_b32 v111, 0\n\t" \
    "v_mov_b32 v112, 0\n\t" \
    "v_mov_b32 v113, 0\n\t" \
    "v_mov_b32 v114, 0\n\t" \
    "v_mov_b32 v115, 0\n\t" \
    "v_mov_b32 v116, 0\n\t" \
    "v_mov_b32 v117, 0\n\t" \
    "v_mov_b32 v118, 0\n\t" \
    "v_mov_b32 v119, 0\n\t" \
    "v_mov_b32 v120, 0\n\t" \
    "v_mov_b32 v121, 0\n\t" \
    "v_mov_b32 v122, 0\n\t" \
    "v_mov_b32 v123, 0\n\t" \
    "v_mov_b32 v124, 0\n\t" \
    "v_mov_b32 v125, 0\n\t" \
    "v_mov_b32 v126, 0\n\t" \
    "v_mov_b32 v127, 0\n\t" \
    "vv_go41_%=:\n\t" \
    "s_add_u32 s69, s65, 0\n\t" \
    "s_bitcmp1_b32 s69, 0\n\t" \
    "s_cselect_b32 s70, s63, s62\n\t" \
    "s_bfe_u32 s69, s69, 0x20001\n\t" \
    "s_lshl_b32 s69, s69, 19\n\t" \
    "s_add_u32 s69, s69, s70\n\t" \
    "s_add_u32 s46, s36, s69\n\t" \
    "s_addc_u32 s47, s37, 0\n\t" \
    "s_add_u32 s69, s65, 0\n\t" \
    "s_lshr_b32 s69, s69, 1\n\t" \
    "s_xor_b32 s69, s69, s97\n\t" \
    "s_and_b32 s69, s69, 1\n\t" \
    "s_lshl_b32 s69, s69, 14\n\t" \
    "s_or_b32 s46, s46, s69\n\t" \
    "s_add_u32 s69, s65, 0\n\t" \
    "s_mul_i32 s70, s69, 171\n\t" \
    "s_lshr_b32 s70, s70, 9\n\t" \
    "s_mul_i32 s70, s70, 3\n\t" \
    "s_sub_u32 s69, s69, s70\n\t" \
    "s_mul_i32 s69, s69, 36\n\t" \
    "s_lshl_b32 s69, s69, 14\n\t" \
    "s_add_u32 s69, s69, s64\n\t" \
    "s_add_u32 s69, s69, 4194304\n\t" \
    "s_add_u32 s38, s36, s69\n\t" \
    "s_addc_u32 s39, s37, 0\n\t" \
    "s_mul_i32 s69, s65, 9216\n\t" \
    "s_add_u32 s69, s69, 123731968\n\t" \
    "s_add_u32 s40, s36, s69\n\t" \
    "s_addc_u32 s41, s37, 0\n\t" \
    "s_sub_u32 s70, s79, s74\n\t" \
    "s_max_i32 s70, s70, 0\n\t" \
    "s_min_i32 s70, s70, 16000\n\t" \
    "s_lshl_b32 s70, s70, 1\n\t" \
    "s_mul_hi_u32 s70, s70, s77\n\t" \
    "s_add_u32 s70, s70, s73\n\t" \
    "s_sub_u32 s70, s70, 100\n\t" \
    "s_getpc_b64 s[30:31]\n\t" \
    "vv_Wp43_%=:\n\t" \
    "s_add_u32 s30, s30, vv_We44_%=-vv_Wp43_%=\n\t" \
    "s_addc_u32 s31, s31, 0\n\t" \
    "vv_W42_%=:\n\t" \
    "s_memtime s[56:57]\n\t" \
    "s_waitcnt lgkmcnt(0)\n\t" \
    "s_sub_u32 s69, s70, s56\n\t" \
    "s_cmp_gt_i32 s69, 111\n\t" \
    "s_cbranch_scc1 vv_W42_%=\n\t" \
    "s_max_i32 s69, s69, 0\n\t" \
    "s_lshr_b32 s69, s69, 1\n\t" \
    "s_and_b32 s69, s69, 0x3c\n\t" \
    "s_sub_u32 s30, s30, s69\n\t" \
    "s_subb_u32 s31, s31, 0\n\t" \
    "s_setpc_b64 s[30:31]\n\t" \
    ".p2align 6\n\t" \
    "s_nop 0\n\t" \
    "s_nop 0\n\t" \
    "s_nop 0\n\t" \
    "s_nop 1\n\t" \
    "s_nop 1\n\t" \
    "s_nop 1\n\t" \
    "s_nop 1\n\t" \
    "s_nop 1\n\t" \
    "s_nop 1\n\t" \
    "s_nop 1\n\t" \
    "s_nop 1\n\t" \
    "s_nop 1\n\t" \
    "s_nop 1\n\t" \
    "s_nop 1\n\t" \
    "s_nop 1\n\t" \
    "s_nop 1\n\t" \
    "vv_We44_%=:\n\t" \
    "vv_G45_%=:\n\t" \
    "global_load_dwordx4 v[36:39], v1, s[46:47] sc1\n\t" \
    "s_nop 9\n\t" \
    "global_load_dwordx4 v[40:43], v1, s[46:47] offset:1024 sc1\n\t" \
    "s_nop 9\n\t" \
    "global_load_dwordx4 v[44:47], v2, s[46:47] sc1\n\t" \
    "s_waitcnt vmcnt(2)\n\t" \
    "v_smfmac_f32_16x16x64_bf16 v[72:75], v[36:39], a[16:23], v10\n\t" \
    "ds_write_b128 v0, v[36:39] offset:2048\n\t" \
    "v_smfmac_f32_16x16x64_bf16 v[76:79], v[36:39], a[88:95], v10\n\t" \
    "v_max3_u32 v140, v36, v37, v38\n\t" \
    "s_waitcnt vmcnt(1)\n\t" \
    "ds_write_b128 v0, v[40:43] offset:3072\n\t" \
    "v_smfmac_f32_16x16x64_bf16 v[72:75], v[40:43], a[24:31], v10\n\t" \
    "v_max3_u32 v140, v140, v39, v40\n\t" \
    "v_max3_u32 v140, v140, v41, v42\n\t" \
    "v_max_u32 v140, v140, v43\n\t" \
    "v_cmp_eq_u32 vcc, -1, v140\n\t" \
    "v_smfmac_f32_16x16x64_bf16 v[76:79], v[40:43], a[96:103], v10\n\t" \
    "s_cbranch_vccnz vv_Gbad46_%=\n\t" \
    "vv_Gc48_%=:\n\t" \
    "s_waitcnt lgkmcnt(0)\n\t" \
    "s_barrier\n\t" \
    "ds_read_b128 v[48:51], v0 offset:4096\n\t" \
    "ds_read_b128 v[52:55], v0 offset:5120\n\t" \
    "s_waitcnt vmcnt(0)\n\t" \
    "v_smfmac_f32_16x16x64_bf16 v[72:75], v[44:47], a[64:71], v10\n\t" \
    "ds_read_b128 v[56:59], v0 offset:6144\n\t" \
    "ds_read_b128 v[60:63], v0 offset:7168\n\t" \
    "v_max3_u32 v140, v44, v45, v46\n\t" \
    "v_max_u32 v140, v140, v47\n\t" \
    "v_smfmac_f32_16x16x64_bf16 v[76:79], v[44:47], a[136:143], v10\n\t" \
    "ds_read_b128 v[64:67], v0 offset:0\n\t" \
    "ds_read_b128 v[68:71], v0 offset:1024\n\t" \
    "v_cmp_eq_u32 vcc, -1, v140\n\t" \
    "s_cbranch_vccnz vv_Gbad247_%=\n\t" \
    "vv_Gc249_%=:\n\t" \
    "s_waitcnt lgkmcnt(5)\n\t" \
    "v_smfmac_f32_16x16x64_bf16 v[72:75], v[48:51], a[32:39], v10\n\t" \
    "s_waitcnt lgkmcnt(4)\n\t" \
    "v_smfmac_f32_16x16x64_bf16 v[72:75], v[52:55], a[40:47], v10\n\t" \
    "s_waitcnt lgkmcnt(3)\n\t" \
    "v_smfmac_f32_16x16x64_bf16 v[72:75], v[56:59], a[48:55], v10\n\t" \
    "s_waitcnt lgkmcnt(2)\n\t" \
    "v_smfmac_f32_16x16x64_bf16 v[72:75], v[60:63], a[56:63], v10\n\t" \
    "s_waitcnt lgkmcnt(1)\n\t" \
    "v_smfmac_f32_16x16x64_bf16 v[72:75], v[64:67], a[0:7], v10\n\t" \
    "s_waitcnt lgkmcnt(0)\n\t" \
    "v_smfmac_f32_16x16x64_bf16 v[72:75], v[68:71], a[8:15], v10\n\t" \
    "v_smfmac_f32_16x16x64_bf16 v[76:79], v[48:51], a[104:111], v10\n\t" \
    "s_nop 1\n\t" \
    "s_nop 0\n\t" \
    "v_smfmac_f32_16x16x64_bf16 v[76:79], v[52:55], a[112:119], v10\n\t" \
    "s_nop 1\n\t" \
    "s_nop 0\n\t" \
    "v_smfmac_f32_16x16x64_bf16 v[76:79], v[56:59], a[120:127], v10\n\t" \
    "v_add_f32 v72, v72, v74\n\t" \
    "v_smfmac_f32_16x16x64_bf16 v[76:79], v[60:63], a[128:135], v10\n\t" \
    "v_add_f32 v73, v73, v75\n\t" \
    "v_smfmac_f32_16x16x64_bf16 v[76:79], v[64:67], a[72:79], v10\n\t" \
    "v_cvt_pk_bf16_f32 v80, v72, v73\n\t" \
    "v_smfmac_f32_16x16x64_bf16 v[76:79], v[68:71], a[80:87], v10\n\t" \
    "ds_write_b32 v12, v80 offset:0\n\t" \
    "s_nop 6\n\t" \
    "v_add_f32 v76, v76, v78\n\t" \
    "v_add_f32 v77, v77, v79\n\t" \
    "v_cvt_pk_bf16_f32 v81, v76, v77\n\t" \
    "ds_write_b32 v12, v81 offset:64\n\t" \
    "s_waitcnt lgkmcnt(0)\n\t" \
    "s_barrier\n\t" \
    "ds_read_b64_tr_b16 v[84:85], v13 offset:0\n\t" \
    "ds_read_b64_tr_b16 v[86:87], v13 offset:128\n\t" \
    "ds_read_b64_tr_b16 v[88:89], v13 offset:1024\n\t" \
    "ds_read_b64_tr_b16 v[90:91], v13 offset:1152\n\t" \
    "s_waitcnt lgkmcnt(2)\n\t" \
    "v_smfmac_f32_16x16x64_bf16 v[92:95], v[84:87], a[144:151], v10\n\t" \
    "s_nop 2\n\t" \
    "s_waitcnt lgkmcnt(0)\n\t" \
    "v_smfmac_f32_16x16x64_bf16 v[92:95], v[88:91], a[152:159], v10\n\t" \
    "s_nop 2\n\t" \
    "v_smfmac_f32_16x16x64_bf16 v[96:99], v[84:87], a[160:167], v10\n\t" \
    "s_nop 2\n\t" \
    "v_smfmac_f32_16x16x64_bf16 v[96:99], v[88:91], a[168:175], v10\n\t" \
    "s_nop 2\n\t" \
    "v_smfmac_f32_16x16x64_bf16 v[100:103], v[84:87], a[176:183], v10\n\t" \
    "s_nop 0\n\t" \
    "v_add_f32 v92, v92, v94\n\t" \
    "v_add_f32 v93, v93, v95\n\t" \
    "v_smfmac_f32_16x16x64_bf16 v[100:103], v[88:91], a[184:191], v10\n\t" \
    "s_nop 1\n\t" \
    "v_cvt_pk_bf16_f32 v128, v92, v93\n\t" \
    "v_smfmac_f32_16x16x64_bf16 v[104:107], v[84:87], a[192:199], v10\n\t" \
    "s_nop 0\n\t" \
    "v_add_f32 v96, v96, v98\n\t" \
    "v_add_f32 v97, v97, v99\n\t" \
    "v_smfmac_f32_16x16x64_bf16 v[104:107], v[88:91], a[200:207], v10\n\t" \
    "s_nop 1\n\t" \
    "v_cvt_pk_bf16_f32 v129, v96, v97\n\t" \
    "v_smfmac_f32_16x16x64_bf16 v[124:127], v[84:87], v[240:247], v10\n\t" \
    "s_nop 0\n\t" \
    "v_add_f32 v100, v100, v102\n\t" \
    "v_add_f32 v101, v101, v103\n\t" \
    "v_smfmac_f32_16x16x64_bf16 v[124:127], v[88:91], v[248:255], v10\n\t" \
    "s_nop 1\n\t" \
    "global_load_dword v140, v20, s[46:47] sc1\n\t" \
    "v_cvt_pk_bf16_f32 v130, v100, v101\n\t" \
    "v_smfmac_f32_16x16x64_bf16 v[108:111], v[84:87], a[208:215], v10\n\t" \
    "s_nop 0\n\t" \
    "v_add_f32 v104, v104, v106\n\t" \
    "v_add_f32 v105, v105, v107\n\t" \
    "v_smfmac_f32_16x16x64_bf16 v[108:111], v[88:91], a[216:223], v10\n\t" \
    "s_nop 0\n\t" \
    "v_cvt_pk_bf16_f32 v131, v104, v105\n\t" \
    "v_permlane32_swap_b32 v128, v130\n\t" \
    "v_smfmac_f32_16x16x64_bf16 v[112:115], v[84:87], a[224:231], v10\n\t" \
    "s_nop 0\n\t" \
    "v_permlane32_swap_b32 v129, v131\n\t" \
    "s_nop 1\n\t" \
    "v_permlane16_swap_b32 v128, v129\n\t" \
    "v_smfmac_f32_16x16x64_bf16 v[112:115], v[88:91], a[232:239], v10\n\t" \
    "v_permlane16_swap_b32 v130, v131\n\t" \
    "global_store_dwordx4 v14, v[128:131], s[38:39] sc1\n\t" \
    "v_add_f32 v124, v124, v126\n\t" \
    "v_smfmac_f32_16x16x64_bf16 v[116:119], v[84:87], a[240:247], v10\n\t" \
    "s_nop 0\n\t" \
    "v_add_f32 v125, v125, v127\n\t" \
    "v_cvt_pk_bf16_f32 v136, v124, v125\n\t" \
    "v_smfmac_f32_16x16x64_bf16 v[116:119], v[88:91], a[248:255], v10\n\t" \
    "global_store_dword v16, v136, s[38:39] sc1\n\t" \
    "v_add_f32 v108, v108, v110\n\t" \
    "v_add_f32 v109, v109, v111\n\t" \
    "v_smfmac_f32_16x16x64_bf16 v[120:123], v[84:87], v[224:231], v10\n\t" \
    "s_nop 0\n\t" \
    "v_cvt_pk_bf16_f32 v132, v108, v109\n\t" \
    "v_add_f32 v112, v112, v114\n\t" \
    "v_smfmac_f32_16x16x64_bf16 v[120:123], v[88:91], v[232:239], v10\n\t" \
    "v_add_f32 v113, v113, v115\n\t" \
    "v_cvt_pk_bf16_f32 v133, v112, v113\n\t" \
    "v_add_f32 v116, v116, v118\n\t" \
    "v_add_f32 v117, v117, v119\n\t" \
    "v_cvt_pk_bf16_f32 v134, v116, v117\n\t" \
    "s_nop 2\n\t" \
    "v_pk_add_f32 v[120:121], v[120:121], v[122:123]\n\t" \
    "v_cvt_pk_bf16_f32 v135, v120, v121\n\t" \
    "v_permlane32_swap_b32 v132, v134\n\t" \
    "s_nop 0\n\t" \
    "v_permlane32_swap_b32 v133, v135\n\t" \
    "s_nop 1\n\t" \
    "v_permlane16_swap_b32 v132, v133\n\t" \
    "v_permlane16_swap_b32 v134, v135\n\t" \
    "global_store_dwordx4 v15, v[132:135], s[38:39] sc1\n\t" \
    "s_nop 1\n\t" \
    "global_store_dwordx4 v17, v[128:131], s[40:41] sc1\n\t" \
    "global_store_dwordx4 v17, v[132:135], s[40:41] offset:1024 sc1\n\t" \
    "global_store_dword v18, v136, s[40:41] sc1\n\t" \
    "s_cmp_eq_u32 s65, 63\n\t" \
    "s_cbranch_scc1 vv_end40_%=\n\t" \
    "s_mov_b32 s71, 3\n\t" \
    "vv_E51_%=:\n\t" \
    "s_memtime s[54:55]\n\t" \
    "s_nop 0\n\t" \
    "s_memrealtime s[4:5]\n\t" \
    "s_nop 0\n\t" \
    "s_memrealtime s[6:7]\n\t" \
    "s_nop 0\n\t" \
    "s_memrealtime s[8:9]\n\t" \
    "s_nop 0\n\t" \
    "s_memrealtime s[10:11]\n\t" \
    "s_nop 0\n\t" \
    "s_memrealtime s[12:13]\n\t" \
    "s_nop 0\n\t" \
    "s_memrealtime s[14:15]\n\t" \
    "s_nop 0\n\t" \
    "s_memrealtime s[16:17]\n\t" \
    "s_nop 0\n\t" \
    "s_memrealtime s[18:19]\n\t" \
    "s_nop 0\n\t" \
    "s_memrealtime s[20:21]\n\t" \
    "s_nop 0\n\t" \
    "s_memrealtime s[22:23]\n\t" \
    "s_nop 0\n\t" \
    "s_memrealtime s[24:25]\n\t" \
    "s_nop 0\n\t" \
    "s_memrealtime s[26:27]\n\t" \
    "s_nop 0\n\t" \
    "s_memrealtime s[28:29]\n\t" \
    "s_nop 0\n\t" \
    "s_memtime s[56:57]\n\t" \
    "s_mov_b32 s70, 1\n\t" \
    "s_sub_u32 s71, s71, 1\n\t" \
    "s_cselect_b32 s69, 0, -1\n\t" \
    "s_waitcnt lgkmcnt(0)\n\t" \
    "s_sub_u32 s54, s56, s54\n\t" \
    "s_and_b32 s54, s54, s69\n\t" \
    "s_cmp_gt_u32 s54, 124\n\t" \
    "s_cbranch_scc1 vv_E51_%=\n\t" \
    "s_cmp_eq_u32 s4, s28\n\t" \
    "s_addc_u32 s70, s70, 0\n\t" \
    "s_cmp_eq_u32 s6, s28\n\t" \
    "s_addc_u32 s70, s70, 0\n\t" \
    "s_cmp_eq_u32 s8, s28\n\t" \
    "s_addc_u32 s70, s70, 0\n\t" \
    "s_cmp_eq_u32 s10, s28\n\t" \
    "s_addc_u32 s70, s70, 0\n\t" \
    "s_cmp_eq_u32 s12, s28\n\t" \
    "s_addc_u32 s70, s70, 0\n\t" \
    "s_cmp_eq_u32 s14, s28\n\t" \
    "s_addc_u32 s70, s70, 0\n\t" \
    "s_cmp_eq_u32 s16, s28\n\t" \
    "s_addc_u32 s70, s70, 0\n\t" \
    "s_cmp_eq_u32 s18, s28\n\t" \
    "s_addc_u32 s70, s70, 0\n\t" \
    "s_cmp_eq_u32 s20, s28\n\t" \
    "s_addc_u32 s70, s70, 0\n\t" \
    "s_cmp_eq_u32 s22, s28\n\t" \
    "s_addc_u32 s70, s70, 0\n\t" \
    "s_cmp_eq_u32 s24, s28\n\t" \
    "s_addc_u32 s70, s70, 0\n\t" \
    "s_cmp_eq_u32 s26, s28\n\t" \
    "s_addc_u32 s70, s70, 0\n\t" \
    "s_mov_b32 s75, s73\n\t" \
    "s_mov_b32 s76, s74\n\t" \
    "s_lshl_b32 s70, s70, 3\n\t" \
    "s_sub_u32 s73, s56, s70\n\t" \
    "s_lshl_b32 s74, s28, 4\n\t" \
    "s_sub_u32 s69, s73, s75\n\t" \
    "s_sub_u32 s70, s74, s76\n\t" \
    "s_sub_u32 s71, s70, 1600\n\t" \
    "s_cmp_lt_u32 s71, 400000\n\t" \
    "s_cbranch_scc0 vv_K53_%=\n\t" \
    "v_cvt_f32_u32 v141, s69\n\t" \
    "v_cvt_f32_u32 v142, s70\n\t" \
    "v_rcp_f32 v143, v141\n\t" \
    "v_rcp_f32 v144, v142\n\t" \
    "v_mul_f32 v143, v143, v142\n\t" \
    "v_mul_f32 v144, v144, v141\n\t" \
    "v_mul_f32 v143, 0x4f800000, v143\n\t" \
    "v_mul_f32 v144, 0x4f000000, v144\n\t" \
    "v_cvt_u32_f32 v143, v143\n\t" \
    "v_cvt_u32_f32 v144, v144\n\t" \
    "v_readfirstlane_b32 s69, v143\n\t" \
    "v_readfirstlane_b32 s70, v144\n\t" \
    "s_nop 1\n\t" \
    "s_sub_u32 s69, s69, s78\n\t" \
    "s_ashr_i32 s69, s69, 2\n\t" \
    "s_add_u32 s78, s78, s69\n\t" \
    "s_sub_u32 s70, s70, s77\n\t" \
    "s_ashr_i32 s70, s70, 2\n\t" \
    "s_add_u32 s77, s77, s70\n\t" \
    "vv_K53_%=:\n\t" \
    "s_waitcnt vmcnt(6)\n\t" \
    "v_cmp_eq_u32 vcc, -1, v140\n\t" \
    "s_cbranch_vccz vv_Hok55_%=\n\t" \
    "vv_H54_%=:\n\t" \
    "global_load_dword v140, v20, s[46:47] sc1\n\t" \
    "s_waitcnt vmcnt(0)\n\t" \
    "v_cmp_eq_u32 vcc, -1, v140\n\t" \
    "s_cbranch_vccz vv_Hc56_%=\n\t" \
    "s_cmp_eq_u32 s68, 0\n\t" \
    "s_cbranch_scc1 vv_F57_%=\n\t" \
    "s_sub_u32 s68, s68, 1\n\t" \
    "s_sleep 5\n\t" \
    "s_branch vv_H54_%=\n\t" \
    "vv_F57_%=:\n\t" \
    "v_mov_b32 v141, 6095104\n\t" \
    "v_mov_b32 v142, 6\n\t" \
    "global_store_dword v141, v142, s[36:37] sc1\n\t" \
    "s_waitcnt vmcnt(0)\n\t" \
    "s_branch vv_Hc56_%=\n\t" \
    "vv_Hc56_%=:\n\t" \
    "vv_Hok55_%=:\n\t" \
    "v_subrev_u32 v140, s80, v140\n\t" \
    "v_and_b32 v20, -5, v20\n\t" \
    "s_nop 0\n\t" \
    "v_max_i32_dpp v140, v140, v140 row_ror:1 row_mask:0xf bank_mask:0xf\n\t" \
    "s_nop 1\n\t" \
    "v_max_i32_dpp v140, v140, v140 row_ror:2 row_mask:0xf bank_mask:0xf\n\t" \
    "s_nop 1\n\t" \
    "v_max_i32_dpp v140, v140, v140 row_ror:4 row_mask:0xf bank_mask:0xf\n\t" \
    "s_nop 1\n\t" \
    "v_max_i32_dpp v140, v140, v140 row_ror:8 row_mask:0xf bank_mask:0xf\n\t" \
    "s_nop 0\n\t" \
    "v_readlane_b32 s69, v140, 0\n\t" \
    "v_readlane_b32 s71, v140, 16\n\t" \
    "v_readlane_b32 s70, v140, 32\n\t" \
    "v_readlane_b32 s81, v140, 48\n\t" \
    "s_max_i32 s69, s69, s71\n\t" \
    "s_nop 0\n\t" \
    "s_max_i32 s71, s70, s81\n\t" \
    "s_max_i32 s69, s69, s71\n\t" \
    "s_add_u32 s81, s69, s80\n\t" \
    "v_mov_b32 v72, 0\n\t" \
    "v_mov_b32 v73, 0\n\t" \
    "v_mov_b32 v74, 0\n\t" \
    "v_mov_b32 v75, 0\n\t" \
    "v_mov_b32 v76, 0\n\t" \
    "v_mov_b32 v77, 0\n\t" \
    "v_mov_b32 v78, 0\n\t" \
    "v_mov_b32 v79, 0\n\t" \
    "v_mov_b32 v92, 0\n\t" \
    "v_mov_b32 v93, 0\n\t" \
    "v_mov_b32 v94, 0\n\t" \
    "v_mov_b32 v95, 0\n\t" \
    "v_mov_b32 v96, 0\n\t" \
    "v_mov_b32 v97, 0\n\t" \
    "v_mov_b32 v98, 0\n\t" \
    "v_mov_b32 v99, 0\n\t" \
    "v_mov_b32 v100, 0\n\t" \
    "v_mov_b32 v101, 0\n\t" \
    "v_mov_b32 v102, 0\n\t" \
    "v_mov_b32 v103, 0\n\t" \
    "v_mov_b32 v104, 0\n\t" \
    "v_mov_b32 v105, 0\n\t" \
    "v_mov_b32 v106, 0\n\t" \
    "v_mov_b32 v107, 0\n\t" \
    "v_mov_b32 v108, 0\n\t" \
    "v_mov_b32 v109, 0\n\t" \
    "v_mov_b32 v110, 0\n\t" \
    "v_mov_b32 v111, 0\n\t" \
    "v_mov_b32 v112, 0\n\t" \
    "v_mov_b32 v113, 0\n\t" \
    "v_mov_b32 v114, 0\n\t" \
    "v_mov_b32 v115, 0\n\t" \
    "v_mov_b32 v116, 0\n\t" \
    "v_mov_b32 v117, 0\n\t" \
    "v_mov_b32 v118, 0\n\t" \
    "v_mov_b32 v119, 0\n\t" \
    "v_mov_b32 v120, 0\n\t" \
    "v_mov_b32 v121, 0\n\t" \
    "v_mov_b32 v122, 0\n\t" \
    "v_mov_b32 v123, 0\n\t" \
    "v_mov_b32 v124, 0\n\t" \
    "v_mov_b32 v125, 0\n\t" \
    "v_mov_b32 v126, 0\n\t" \
    "v_mov_b32 v127, 0\n\t" \
    "s_cmp_lg_u32 s65, 0\n\t" \
    "s_cbranch_scc1 vv_Oc60_%=\n\t" \
    "s_add_u32 s79, s81, 240\n\t" \
    "s_sub_u32 s70, s79, s74\n\t" \
    "s_max_i32 s70, s70, 0\n\t" \
    "s_min_i32 s70, s70, 16000\n\t" \
    "s_lshl_b32 s70, s70, 1\n\t" \
    "s_mul_hi_u32 s70, s70, s77\n\t" \
    "s_add_u32 s70, s70, s73\n\t" \
    "vv_W61_%=:\n\t" \
    "s_memtime s[56:57]\n\t" \
    "s_waitcnt lgkmcnt(0)\n\t" \
    "s_sub_u32 s69, s70, s56\n\t" \
    "s_cmp_gt_i32 s69, 24\n\t" \
    "s_cbranch_scc1 vv_W61_%=\n\t" \
    "global_load_dword v140, v35, s[46:47] sc1\n\t" \
    "s_waitcnt vmcnt(0)\n\t" \
    "v_cmp_eq_u32 vcc, -1, v140\n\t" \
    "s_cbranch_vccz vv_Ook59_%=\n\t" \
    "vv_O58_%=:\n\t" \
    "global_load_dword v140, v35, s[46:47] offset:1536 sc1\n\t" \
    "s_waitcnt vmcnt(0)\n\t" \
    "v_cmp_eq_u32 vcc, -1, v140\n\t" \
    "s_cbranch_vccz vv_Ook59_%=\n\t" \
    "s_cmp_eq_u32 s68, 0\n\t" \
    "s_cbranch_scc1 vv_F62_%=\n\t" \
    "s_sub_u32 s68, s68, 1\n\t" \
    "s_sleep 3\n\t" \
    "s_branch vv_O58_%=\n\t" \
    "vv_F62_%=:\n\t" \
    "v_mov_b32 v141, 6095104\n\t" \
    "v_mov_b32 v142, 17\n\t" \
    "global_store_dword v141, v142, s[36:37] sc1\n\t" \
    "s_waitcnt vmcnt(0)\n\t" \
    "s_branch vv_Ook59_%=\n\t" \
    "vv_Ook59_%=:\n\t" \
    "v_subrev_u32 v140, s80, v140\n\t" \
    "s_nop 1\n\t" \
    "v_max_i32_dpp v140, v140, v140 row_ror:1 row_mask:0xf bank_mask:0xf\n\t" \
    "s_nop 1\n\t" \
    "v_max_i32_dpp v140, v140, v140 row_ror:2 row_mask:0xf bank_mask:0xf\n\t" \
    "s_nop 1\n\t" \
    "v_max_i32_dpp v140, v140, v140 row_ror:4 row_mask:0xf bank_mask:0xf\n\t" \
    "s_nop 1\n\t" \
    "v_max_i32_dpp v140, v140, v140 row_ror:8 row_mask:0xf bank_mask:0xf\n\t" \
    "s_nop 0\n\t" \
    "v_readlane_b32 s69, v140, 0\n\t" \
    "v_readlane_b32 s71, v140, 16\n\t" \
    "v_readlane_b32 s70, v140, 32\n\t" \
    "v_readlane_b32 s81, v140, 48\n\t" \
    "s_max_i32 s69, s69, s71\n\t" \
    "s_nop 0\n\t" \
    "s_max_i32 s71, s70, s81\n\t" \
    "s_max_i32 s69, s69, s71\n\t" \
    "s_add_u32 s81, s69, s80\n\t" \
    "vv_Oc60_%=:\n\t" \
    "s_mul_hi_u32 s69, s85, s78\n\t" \
    "s_add_u32 s79, s81, s69\n\t" \
    "s_add_u32 s79, s79, s87\n\t" \
    "s_cmp_ge_u32 s65, 36\n\t" \
    "s_cselect_b32 s69, 28, 0\n\t" \
    "s_sub_u32 s79, s79, s69\n\t" \
    "s_cmp_lt_u32 s65, 8\n\t" \
    "s_cselect_b32 s69, 96, 0\n\t" \
    "s_add_u32 s79, s79, s69\n\t" \
    "s_cmp_ge_u32 s65, 57\n\t" \
    "s_cselect_b32 s69, 96, 0\n\t" \
    "s_add_u32 s79, s79, s69\n\t" \
    "s_add_u32 s80, s79, s88\n\t" \
    "s_add_u32 s65, s65, 1\n\t" \
    "s_branch vv_go41_%=\n\t" \
    "vv_Gbad46_%=:\n\t" \
    "s_waitcnt vmcnt(0) lgkmcnt(0)\n\t" \
    "s_nop 7\n\t" \
    "v_mov_b32 v72, 0\n\t" \
    "v_mov_b32 v73, 0\n\t" \
    "v_mov_b32 v74, 0\n\t" \
    "v_mov_b32 v75, 0\n\t" \
    "v_mov_b32 v76, 0\n\t" \
    "v_mov_b32 v77, 0\n\t" \
    "v_mov_b32 v78, 0\n\t" \
    "v_mov_b32 v79, 0\n\t" \
    "s_cmp_eq_u32 s68, 0\n\t" \
    "s_cbranch_scc1 vv_F63_%=\n\t" \
    "s_sub_u32 s68, s68, 1\n\t" \
    "s_branch vv_G45_%=\n\t" \
    "vv_F63_%=:\n\t" \
    "v_mov_b32 v140, 6095104\n\t" \
    "v_mov_b32 v141, 1\n\t" \
    "global_store_dword v140, v141, s[36:37] sc1\n\t" \
    "s_waitcnt vmcnt(0)\n\t" \
    "s_branch vv_Gc48_%=\n\t" \
    "vv_Gbad247_%=:\n\t" \
    "global_load_dwordx4 v[44:47], v2, s[46:47] sc1\n\t" \
    "s_waitcnt vmcnt(0)\n\t" \
    "v_max3_u32 v140, v44, v45, v46\n\t" \
    "v_max_u32 v140, v140, v47\n\t" \
    "v_cmp_eq_u32 vcc, -1, v140\n\t" \
    "s_cbranch_vccz vv_Gre250_%=\n\t" \
    "s_cmp_eq_u32 s68, 0\n\t" \
    "s_cbranch_scc1 vv_F64_%=\n\t" \
    "s_sub_u32 s68, s68, 1\n\t" \
    "s_sleep 1\n\t" \
    "s_branch vv_Gbad247_%=\n\t" \
    "vv_F64_%=:\n\t" \
    "v_mov_b32 v140, 6095104\n\t" \
    "v_mov_b32 v141, 2\n\t" \
    "global_store_dword v140, v141, s[36:37] sc1\n\t" \
    "s_waitcnt vmcnt(0)\n\t" \
    "s_branch vv_Gre250_%=\n\t" \
    "vv_Gre250_%=:\n\t" \
    "v_mov_b32 v72, 0\n\t" \
    "v_mov_b32 v73, 0\n\t" \
    "v_mov_b32 v74, 0\n\t" \
    "v_mov_b32 v75, 0\n\t" \
    "v_mov_b32 v76, 0\n\t" \
    "v_mov_b32 v77, 0\n\t" \
    "v_mov_b32 v78, 0\n\t" \
    "v_mov_b32 v79, 0\n\t" \
    "v_smfmac_f32_16x16x64_bf16 v[72:75], v[36:39], a[16:23], v10\n\t" \
    "s_nop 0\n\t" \
    "v_smfmac_f32_16x16x64_bf16 v[76:79], v[36:39], a[88:95], v10\n\t" \
    "v_smfmac_f32_16x16x64_bf16 v[72:75], v[40:43], a[24:31], v10\n\t" \
    "v_smfmac_f32_16x16x64_bf16 v[76:79], v[40:43], a[96:103], v10\n\t" \
    "v_smfmac_f32_16x16x64_bf16 v[72:75], v[44:47], a[64:71], v10\n\t" \
    "v_smfmac_f32_16x16x64_bf16 v[76:79], v[44:47], a[136:143], v10\n\t" \
    "s_branch vv_Gc249_%=\n\t" \
    "vv_end40_%=:\n\t" \
    "s_endpgm\n\t" \
    "vv_loop12_%=:\n\t" \
    "v_mov_b32 v72, 0\n\t" \
    "v_mov_b32 v73, 0\n\t" \
    "v_mov_b32 v74, 0\n\t" \
    "v_mov_b32 v75, 0\n\t" \
    "v_mov_b32 v76, 0\n\t" \
    "v_mov_b32 v77, 0\n\t" \
    "v_mov_b32 v78, 0\n\t" \
    "v_mov_b32 v79, 0\n\t" \
    "v_mov_b32 v92, 0\n\t" \
    "v_mov_b32 v93, 0\n\t" \
    "v_mov_b32 v94, 0\n\t" \
    "v_mov_b32 v95, 0\n\t" \
    "v_mov_b32 v96, 0\n\t" \
    "v_mov_b32 v97, 0\n\t" \
    "v_mov_b32 v98, 0\n\t" \
    "v_mov_b32 v99, 0\n\t" \
    "v_mov_b32 v100, 0\n\t" \
    "v_mov_b32 v101, 0\n\t" \
    "v_mov_b32 v102, 0\n\t" \
    "v_mov_b32 v103, 0\n\t" \
    "v_mov_b32 v104, 0\n\t" \
    "v_mov_b32 v105, 0\n\t" \
    "v_mov_b32 v106, 0\n\t" \
    "v_mov_b32 v107, 0\n\t" \
    "v_mov_b32 v108, 0\n\t" \
    "v_mov_b32 v109, 0\n\t" \
    "v_mov_b32 v110, 0\n\t" \
    "v_mov_b32 v111, 0\n\t" \
    "v_mov_b32 v112, 0\n\t" \
    "v_mov_b32 v113, 0\n\t" \
    "v_mov_b32 v114, 0\n\t" \
    "v_mov_b32 v115, 0\n\t" \
    "v_mov_b32 v116, 0\n\t" \
    "v_mov_b32 v117, 0\n\t" \
    "v_mov_b32 v118, 0\n\t" \
    "v_mov_b32 v119, 0\n\t" \
    "v_mov_b32 v120, 0\n\t" \
    "v_mov_b32 v121, 0\n\t" \
    "v_mov_b32 v122, 0\n\t" \
    "v_mov_b32 v123, 0\n\t" \
    "v_mov_b32 v124, 0\n\t" \
    "v_mov_b32 v125, 0\n\t" \
    "v_mov_b32 v126, 0\n\t" \
    "v_mov_b32 v127, 0\n\t" \
    "vv_go66_%=:\n\t" \
    "s_add_u32 s69, s65, 0\n\t" \
    "s_bitcmp1_b32 s69, 0\n\t" \
    "s_cselect_b32 s70, s63, s62\n\t" \
    "s_bfe_u32 s69, s69, 0x20001\n\t" \
    "s_lshl_b32 s69, s69, 19\n\t" \
    "s_add_u32 s69, s69, s70\n\t" \
    "s_add_u32 s46, s36, s69\n\t" \
    "s_addc_u32 s47, s37, 0\n\t" \
    "s_add_u32 s69, s65, 0\n\t" \
    "s_lshr_b32 s69, s69, 1\n\t" \
    "s_xor_b32 s69, s69, s97\n\t" \
    "s_and_b32 s69, s69, 1\n\t" \
    "s_lshl_b32 s69, s69, 14\n\t" \
    "s_or_b32 s46, s46, s69\n\t" \
    "s_add_u32 s69, s65, 0\n\t" \
    "s_mul_i32 s70, s69, 171\n\t" \
    "s_lshr_b32 s70, s70, 9\n\t" \
    "s_mul_i32 s70, s70, 3\n\t" \
    "s_sub_u32 s69, s69, s70\n\t" \
    "s_mul_i32 s69, s69, 36\n\t" \
    "s_lshl_b32 s69, s69, 14\n\t" \
    "s_add_u32 s69, s69, s64\n\t" \
    "s_add_u32 s69, s69, 4194304\n\t" \
    "s_add_u32 s38, s36, s69\n\t" \
    "s_addc_u32 s39, s37, 0\n\t" \
    "s_mul_i32 s69, s65, 9216\n\t" \
    "s_add_u32 s69, s69, 123731968\n\t" \
    "s_add_u32 s40, s36, s69\n\t" \
    "s_addc_u32 s41, s37, 0\n\t" \
    "s_sub_u32 s70, s79, s74\n\t" \
    "s_max_i32 s70, s70, 0\n\t" \
    "s_min_i32 s70, s70, 16000\n\t" \
    "s_lshl_b32 s70, s70, 1\n\t" \
    "s_mul_hi_u32 s70, s70, s77\n\t" \
    "s_add_u32 s70, s70, s73\n\t" \
    "s_sub_u32 s70, s70, 100\n\t" \
    "s_getpc_b64 s[30:31]\n\t" \
    "vv_Wp68_%=:\n\t" \
    "s_add_u32 s30, s30, vv_We69_%=-vv_Wp68_%=\n\t" \
    "s_addc_u32 s31, s31, 0\n\t" \
    "vv_W67_%=:\n\t" \
    "s_memtime s[56:57]\n\t" \
    "s_waitcnt lgkmcnt(0)\n\t" \
    "s_sub_u32 s69, s70, s56\n\t" \
    "s_cmp_gt_i32 s69, 111\n\t" \
    "s_cbranch_scc1 vv_W67_%=\n\t" \
    "s_max_i32 s69, s69, 0\n\t" \
    "s_lshr_b32 s69, s69, 1\n\t" \
    "s_and_b32 s69, s69, 0x3c\n\t" \
    "s_sub_u32 s30, s30, s69\n\t" \
    "s_subb_u32 s31, s31, 0\n\t" \
    "s_setpc_b64 s[30:31]\n\t" \
    ".p2align 6\n\t" \
    "s_nop 0\n\t" \
    "s_nop 0\n\t" \
    "s_nop 0\n\t" \
    "s_nop 1\n\t" \
    "s_nop 1\n\t" \
    "s_nop 1\n\t" \
    "s_nop 1\n\t" \
    "s_nop 1\n\t" \
    "s_nop 1\n\t" \
    "s_nop 1\n\t" \
    "s_nop 1\n\t" \
    "s_nop 1\n\t" \
    "s_nop 1\n\t" \
    "s_nop 1\n\t" \
    "s_nop 1\n\t" \
    "s_nop 1\n\t" \
    "vv_We69_%=:\n\t" \
    "vv_G70_%=:\n\t" \
    "global_load_dwordx4 v[36:39], v1, s[46:47] sc1\n\t" \
    "s_nop 9\n\t" \
    "global_load_dwordx4 v[40:43], v1, s[46:47] offset:1024 sc1\n\t" \
    "s_nop 9\n\t" \
    "global_load_dwordx4 v[44:47], v2, s[46:47] sc1\n\t" \
    "s_waitcnt vmcnt(2)\n\t" \
    "v_smfmac_f32_16x16x64_bf16 v[72:75], v[36:39], a[32:39], v10\n\t" \
    "ds_write_b128 v0, v[36:39] offset:4096\n\t" \
    "v_smfmac_f32_16x16x64_bf16 v[76:79], v[36:39], a[104:111], v10\n\t" \
    "v_max3_u32 v140, v36, v37, v38\n\t" \
    "s_waitcnt vmcnt(1)\n\t" \
    "ds_write_b128 v0, v[40:43] offset:5120\n\t" \
    "v_smfmac_f32_16x16x64_bf16 v[72:75], v[40:43], a[40:47], v10\n\t" \
    "v_max3_u32 v140, v140, v39, v40\n\t" \
    "v_max3_u32 v140, v140, v41, v42\n\t" \
    "v_max_u32 v140, v140, v43\n\t" \
    "v_cmp_eq_u32 vcc, -1, v140\n\t" \
    "v_smfmac_f32_16x16x64_bf16 v[76:79], v[40:43], a[112:119], v10\n\t" \
    "s_cbranch_vccnz vv_Gbad71_%=\n\t" \
    "vv_Gc73_%=:\n\t" \
    "s_waitcnt lgkmcnt(0)\n\t" \
    "s_barrier\n\t" \
    "ds_read_b128 v[48:51], v0 offset:6144\n\t" \
    "ds_read_b128 v[52:55], v0 offset:7168\n\t" \
    "s_waitcnt vmcnt(0)\n\t" \
    "v_smfmac_f32_16x16x64_bf16 v[72:75], v[44:47], a[64:71], v10\n\t" \
    "ds_read_b128 v[56:59], v0 offset:0\n\t" \
    "ds_read_b128 v[60:63], v0 offset:1024\n\t" \
    "v_max3_u32 v140, v44, v45, v46\n\t" \
    "v_max_u32 v140, v140, v47\n\t" \
    "v_smfmac_f32_16x16x64_bf16 v[76:79], v[44:47], a[136:143], v10\n\t" \
    "ds_read_b128 v[64:67], v0 offset:2048\n\t" \
    "ds_read_b128 v[68:71], v0 offset:3072\n\t" \
    "v_cmp_eq_u32 vcc, -1, v140\n\t" \
    "s_cbranch_vccnz vv_Gbad272_%=\n\t" \
    "vv_Gc274_%=:\n\t" \
    "s_waitcnt lgkmcnt(5)\n\t" \
    "v_smfmac_f32_16x16x64_bf16 v[72:75], v[48:51], a[48:55], v10\n\t" \
    "s_waitcnt lgkmcnt(4)\n\t" \
    "v_smfmac_f32_16x16x64_bf16 v[72:75], v[52:55], a[56:63], v10\n\t" \
    "s_waitcnt lgkmcnt(3)\n\t" \
    "v_smfmac_f32_16x16x64_bf16 v[72:75], v[56:59], a[0:7], v10\n\t" \
    "s_waitcnt lgkmcnt(2)\n\t" \
    "v_smfmac_f32_16x16x64_bf16 v[72:75], v[60:63], a[8:15], v10\n\t" \
    "s_waitcnt lgkmcnt(1)\n\t" \
    "v_smfmac_f32_16x16x64_bf16 v[72:75], v[64:67], a[16:23], v10\n\t" \
    "s_waitcnt lgkmcnt(0)\n\t" \
    "v_smfmac_f32_16x16x64_bf16 v[72:75], v[68:71], a[24:31], v10\n\t" \
    "v_smfmac_f32_16x16x64_bf16 v[76:79], v[48:51], a[120:127], v10\n\t" \
    "s_nop 1\n\t" \
    "s_nop 0\n\t" \
    "v_smfmac_f32_16x16x64_bf16 v[76:79], v[52:55], a[128:135], v10\n\t" \
    "s_nop 1\n\t" \
    "s_nop 0\n\t" \
    "v_smfmac_f32_16x16x64_bf16 v[76:79], v[56:59], a[72:79], v10\n\t" \
    "v_add_f32 v72, v72, v74\n\t" \
    "v_smfmac_f32_16x16x64_bf16 v[76:79], v[60:63], a[80:87], v10\n\t" \
    "v_add_f32 v73, v73, v75\n\t" \
    "v_smfmac_f32_16x16x64_bf16 v[76:79], v[64:67], a[88:95], v10\n\t" \
    "v_cvt_pk_bf16_f32 v80, v72, v73\n\t" \
    "v_smfmac_f32_16x16x64_bf16 v[76:79], v[68:71], a[96:103], v10\n\t" \
    "ds_write_b32 v12, v80 offset:0\n\t" \
    "s_nop 6\n\t" \
    "v_add_f32 v76, v76, v78\n\t" \
    "v_add_f32 v77, v77, v79\n\t" \
    "v_cvt_pk_bf16_f32 v81, v76, v77\n\t" \
    "ds_write_b32 v12, v81 offset:64\n\t" \
    "s_waitcnt lgkmcnt(0)\n\t" \
    "s_barrier\n\t" \
    "ds_read_b64_tr_b16 v[84:85], v13 offset:0\n\t" \
    "ds_read_b64_tr_b16 v[86:87], v13 offset:128\n\t" \
    "ds_read_b64_tr_b16 v[88:89], v13 offset:1024\n\t" \
    "ds_read_b64_tr_b16 v[90:91], v13 offset:1152\n\t" \
    "s_waitcnt lgkmcnt(2)\n\t" \
    "v_smfmac_f32_16x16x64_bf16 v[92:95], v[84:87], a[144:151], v10\n\t" \
    "s_nop 2\n\t" \
    "s_waitcnt lgkmcnt(0)\n\t" \
    "v_smfmac_f32_16x16x64_bf16 v[92:95], v[88:91], a[152:159], v10\n\t" \
    "s_nop 2\n\t" \
    "v_smfmac_f32_16x16x64_bf16 v[96:99], v[84:87], a[160:167], v10\n\t" \
    "s_nop 2\n\t" \
    "v_smfmac_f32_16x16x64_bf16 v[96:99], v[88:91], a[168:175], v10\n\t" \
    "s_nop 2\n\t" \
    "v_smfmac_f32_16x16x64_bf16 v[100:103], v[84:87], a[176:183], v10\n\t" \
    "s_nop 0\n\t" \
    "v_add_f32 v92, v92, v94\n\t" \
    "v_add_f32 v93, v93, v95\n\t" \
    "v_smfmac_f32_16x16x64_bf16 v[100:103], v[88:91], a[184:191], v10\n\t" \
    "s_nop 1\n\t" \
    "v_cvt_pk_bf16_f32 v128, v92, v93\n\t" \
    "v_smfmac_f32_16x16x64_bf16 v[104:107], v[84:87], a[192:199], v10\n\t" \
    "s_nop 0\n\t" \
    "v_add_f32 v96, v96, v98\n\t" \
    "v_add_f32 v97, v97, v99\n\t" \
    "v_smfmac_f32_16x16x64_bf16 v[104:107], v[88:91], a[200:207], v10\n\t" \
    "s_nop 1\n\t" \
    "v_cvt_pk_bf16_f32 v129, v96, v97\n\t" \
    "v_smfmac_f32_16x16x64_bf16 v[124:127], v[84:87], v[240:247], v10\n\t" \
    "s_nop 0\n\t" \
    "v_add_f32 v100, v100, v102\n\t" \
    "v_add_f32 v101, v101, v103\n\t" \
    "v_smfmac_f32_16x16x64_bf16 v[124:127], v[88:91], v[248:255], v10\n\t" \
    "s_nop 1\n\t" \
    "global_load_dword v140, v20, s[46:47] sc1\n\t" \
    "v_cvt_pk_bf16_f32 v130, v100, v101\n\t" \
    "v_smfmac_f32_16x16x64_bf16 v[108:111], v[84:87], a[208:215], v10\n\t" \
    "s_nop 0\n\t" \
    "v_add_f32 v104, v104, v106\n\t" \
    "v_add_f32 v105, v105, v107\n\t" \
    "v_smfmac_f32_16x16x64_bf16 v[108:111], v[88:91], a[216:223], v10\n\t" \
    "s_nop 0\n\t" \
    "v_cvt_pk_bf16_f32 v131, v104, v105\n\t" \
    "v_permlane32_swap_b32 v128, v130\n\t" \
    "v_smfmac_f32_16x16x64_bf16 v[112:115], v[84:87], a[224:231], v10\n\t" \
    "s_nop 0\n\t" \
    "v_permlane32_swap_b32 v129, v131\n\t" \
    "s_nop 1\n\t" \
    "v_permlane16_swap_b32 v128, v129\n\t" \
    "v_smfmac_f32_16x16x64_bf16 v[112:115], v[88:91], a[232:239], v10\n\t" \
    "v_permlane16_swap_b32 v130, v131\n\t" \
    "global_store_dwordx4 v14, v[128:131], s[38:39] sc1\n\t" \
    "v_add_f32 v124, v124, v126\n\t" \
    "v_smfmac_f32_16x16x64_bf16 v[116:119], v[84:87], a[240:247], v10\n\t" \
    "s_nop 0\n\t" \
    "v_add_f32 v125, v125, v127\n\t" \
    "v_cvt_pk_bf16_f32 v136, v124, v125\n\t" \
    "v_smfmac_f32_16x16x64_bf16 v[116:119], v[88:91], a[248:255], v10\n\t" \
    "global_store_dword v16, v136, s[38:39] sc1\n\t" \
    "v_add_f32 v108, v108, v110\n\t" \
    "v_add_f32 v109, v109, v111\n\t" \
    "v_smfmac_f32_16x16x64_bf16 v[120:123], v[84:87], v[224:231], v10\n\t" \
    "s_nop 0\n\t" \
    "v_cvt_pk_bf16_f32 v132, v108, v109\n\t" \
    "v_add_f32 v112, v112, v114\n\t" \
    "v_smfmac_f32_16x16x64_bf16 v[120:123], v[88:91], v[232:239], v10\n\t" \
    "v_add_f32 v113, v113, v115\n\t" \
    "v_cvt_pk_bf16_f32 v133, v112, v113\n\t" \
    "v_add_f32 v116, v116, v118\n\t" \
    "v_add_f32 v117, v117, v119\n\t" \
    "v_cvt_pk_bf16_f32 v134, v116, v117\n\t" \
    "s_nop 2\n\t" \
    "v_pk_add_f32 v[120:121], v[120:121], v[122:123]\n\t" \
    "v_cvt_pk_bf16_f32 v135, v120, v121\n\t" \
    "v_permlane32_swap_b32 v132, v134\n\t" \
    "s_nop 0\n\t" \
    "v_permlane32_swap_b32 v133, v135\n\t" \
    "s_nop 1\n\t" \
    "v_permlane16_swap_b32 v132, v133\n\t" \
    "v_permlane16_swap_b32 v134, v135\n\t" \
    "global_store_dwordx4 v15, v[132:135], s[38:39] sc1\n\t" \
    "s_nop 1\n\t" \
    "global_store_dwordx4 v17, v[128:131], s[40:41] sc1\n\t" \
    "global_store_dwordx4 v17, v[132:135], s[40:41] offset:1024 sc1\n\t" \
    "global_store_dword v18, v136, s[40:41] sc1\n\t" \
    "s_cmp_eq_u32 s65, 63\n\t" \
    "s_cbranch_scc1 vv_end65_%=\n\t" \
    "s_mov_b32 s71, 3\n\t" \
    "vv_E76_%=:\n\t" \
    "s_memtime s[54:55]\n\t" \
    "s_nop 0\n\t" \
    "s_memrealtime s[4:5]\n\t" \
    "s_nop 0\n\t" \
    "s_memrealtime s[6:7]\n\t" \
    "s_nop 0\n\t" \
    "s_memrealtime s[8:9]\n\t" \
    "s_nop 0\n\t" \
    "s_memrealtime s[10:11]\n\t" \
    "s_nop 0\n\t" \
    "s_memrealtime s[12:13]\n\t" \
    "s_nop 0\n\t" \
    "s_memrealtime s[14:15]\n\t" \
    "s_nop 0\n\t" \
    "s_memrealtime s[16:17]\n\t" \
    "s_nop 0\n\t" \
    "s_memrealtime s[18:19]\n\t" \
    "s_nop 0\n\t" \
    "s_memrealtime s[20:21]\n\t" \
    "s_nop 0\n\t" \
    "s_memrealtime s[22:23]\n\t" \
    "s_nop 0\n\t" \
    "s_memrealtime s[24:25]\n\t" \
    "s_nop 0\n\t" \
    "s_memrealtime s[26:27]\n\t" \
    "s_nop 0\n\t" \
    "s_memrealtime s[28:29]\n\t" \
    "s_nop 0\n\t" \
    "s_memtime s[56:57]\n\t" \
    "s_mov_b32 s70, 1\n\t" \
    "s_sub_u32 s71, s71, 1\n\t" \
    "s_cselect_b32 s69, 0, -1\n\t" \
    "s_waitcnt lgkmcnt(0)\n\t" \
    "s_sub_u32 s54, s56, s54\n\t" \
    "s_and_b32 s54, s54, s69\n\t" \
    "s_cmp_gt_u32 s54, 124\n\t" \
    "s_cbranch_scc1 vv_E76_%=\n\t" \
    "s_cmp_eq_u32 s4, s28\n\t" \
    "s_addc_u32 s70, s70, 0\n\t" \
    "s_cmp_eq_u32 s6, s28\n\t" \
    "s_addc_u32 s70, s70, 0\n\t" \
    "s_cmp_eq_u32 s8, s28\n\t" \
    "s_addc_u32 s70, s70, 0\n\t" \
    "s_cmp_eq_u32 s10, s28\n\t" \
    "s_addc_u32 s70, s70, 0\n\t" \
    "s_cmp_eq_u32 s12, s28\n\t" \
    "s_addc_u32 s70, s70, 0\n\t" \
    "s_cmp_eq_u32 s14, s28\n\t" \
    "s_addc_u32 s70, s70, 0\n\t" \
    "s_cmp_eq_u32 s16, s28\n\t" \
    "s_addc_u32 s70, s70, 0\n\t" \
    "s_cmp_eq_u32 s18, s28\n\t" \
    "s_addc_u32 s70, s70, 0\n\t" \
    "s_cmp_eq_u32 s20, s28\n\t" \
    "s_addc_u32 s70, s70, 0\n\t" \
    "s_cmp_eq_u32 s22, s28\n\t" \
    "s_addc_u32 s70, s70, 0\n\t" \
    "s_cmp_eq_u32 s24, s28\n\t" \
    "s_addc_u32 s70, s70, 0\n\t" \
    "s_cmp_eq_u32 s26, s28\n\t" \
    "s_addc_u32 s70, s70, 0\n\t" \
    "s_mov_b32 s75, s73\n\t" \
    "s_mov_b32 s76, s74\n\t" \
    "s_lshl_b32 s70, s70, 3\n\t" \
    "s_sub_u32 s73, s56, s70\n\t" \
    "s_lshl_b32 s74, s28, 4\n\t" \
    "s_sub_u32 s69, s73, s75\n\t" \
    "s_sub_u32 s70, s74, s76\n\t" \
    "s_sub_u32 s71, s70, 1600\n\t" \
    "s_cmp_lt_u32 s71, 400000\n\t" \
    "s_cbranch_scc0 vv_K78_%=\n\t" \
    "v_cvt_f32_u32 v141, s69\n\t" \
    "v_cvt_f32_u32 v142, s70\n\t" \
    "v_rcp_f32 v143, v141\n\t" \
    "v_rcp_f32 v144, v142\n\t" \
    "v_mul_f32 v143, v143, v142\n\t" \
    "v_mul_f32 v144, v144, v141\n\t" \
    "v_mul_f32 v143, 0x4f800000, v143\n\t" \
    "v_mul_f32 v144, 0x4f000000, v144\n\t" \
    "v_cvt_u32_f32 v143, v143\n\t" \
    "v_cvt_u32_f32 v144, v144\n\t" \
    "v_readfirstlane_b32 s69, v143\n\t" \
    "v_readfirstlane_b32 s70, v144\n\t" \
    "s_nop 1\n\t" \
    "s_sub_u32 s69, s69, s78\n\t" \
    "s_ashr_i32 s69, s69, 2\n\t" \
    "s_add_u32 s78, s78, s69\n\t" \
    "s_sub_u32 s70, s70, s77\n\t" \
    "s_ashr_i32 s70, s70, 2\n\t" \
    "s_add_u32 s77, s77, s70\n\t" \
    "vv_K78_%=:\n\t" \
    "s_waitcnt vmcnt(6)\n\t" \
    "v_cmp_eq_u32 vcc, -1, v140\n\t" \
    "s_cbranch_vccz vv_Hok80_%=\n\t" \
    "vv_H79_%=:\n\t" \
    "global_load_dword v140, v20, s[46:47] sc1\n\t" \
    "s_waitcnt vmcnt(0)\n\t" \
    "v_cmp_eq_u32 vcc, -1, v140\n\t" \
    "s_cbranch_vccz vv_Hc81_%=\n\t" \
    "s_cmp_eq_u32 s68, 0\n\t" \
    "s_cbranch_scc1 vv_F82_%=\n\t" \
    "s_sub_u32 s68, s68, 1\n\t" \
    "s_sleep 5\n\t" \
    "s_branch vv_H79_%=\n\t" \
    "vv_F82_%=:\n\t" \
    "v_mov_b32 v141, 6095104\n\t" \
    "v_mov_b32 v142, 6\n\t" \
    "global_store_dword v141, v142, s[36:37] sc1\n\t" \
    "s_waitcnt vmcnt(0)\n\t" \
    "s_branch vv_Hc81_%=\n\t" \
    "vv_Hc81_%=:\n\t" \
    "vv_Hok80_%=:\n\t" \
    "v_subrev_u32 v140, s80, v140\n\t" \
    "v_and_b32 v20, -5, v20\n\t" \
    "s_nop 0\n\t" \
    "v_max_i32_dpp v140, v140, v140 row_ror:1 row_mask:0xf bank_mask:0xf\n\t" \
    "s_nop 1\n\t" \
    "v_max_i32_dpp v140, v140, v140 row_ror:2 row_mask:0xf bank_mask:0xf\n\t" \
    "s_nop 1\n\t" \
    "v_max_i32_dpp v140, v140, v140 row_ror:4 row_mask:0xf bank_mask:0xf\n\t" \
    "s_nop 1\n\t" \
    "v_max_i32_dpp v140, v140, v140 row_ror:8 row_mask:0xf bank_mask:0xf\n\t" \
    "s_nop 0\n\t" \
    "v_readlane_b32 s69, v140, 0\n\t" \
    "v_readlane_b32 s71, v140, 16\n\t" \
    "v_readlane_b32 s70, v140, 32\n\t" \
    "v_readlane_b32 s81, v140, 48\n\t" \
    "s_max_i32 s69, s69, s71\n\t" \
    "s_nop 0\n\t" \
    "s_max_i32 s71, s70, s81\n\t" \
    "s_max_i32 s69, s69, s71\n\t" \
    "s_add_u32 s81, s69, s80\n\t" \
    "v_mov_b32 v72, 0\n\t" \
    "v_mov_b32 v73, 0\n\t" \
    "v_mov_b32 v74, 0\n\t" \
    "v_mov_b32 v75, 0\n\t" \
    "v_mov_b32 v76, 0\n\t" \
    "v_mov_b32 v77, 0\n\t" \
    "v_mov_b32 v78, 0\n\t" \
    "v_mov_b32 v79, 0\n\t" \
    "v_mov_b32 v92, 0\n\t" \
    "v_mov_b32 v93, 0\n\t" \
    "v_mov_b32 v94, 0\n\t" \
    "v_mov_b32 v95, 0\n\t" \
    "v_mov_b32 v96, 0\n\t" \
    "v_mov_b32 v97, 0\n\t" \
    "v_mov_b32 v98, 0\n\t" \
    "v_mov_b32 v99, 0\n\t" \
    "v_mov_b32 v100, 0\n\t" \
    "v_mov_b32 v101, 0\n\t" \
    "v_mov_b32 v102, 0\n\t" \
    "v_mov_b32 v103, 0\n\t" \
    "v_mov_b32 v104, 0\n\t" \
    "v_mov_b32 v105, 0\n\t" \
    "v_mov_b32 v106, 0\n\t" \
    "v_mov_b32 v107, 0\n\t" \
    "v_mov_b32 v108, 0\n\t" \
    "v_mov_b32 v109, 0\n\t" \
    "v_mov_b32 v110, 0\n\t" \
    "v_mov_b32 v111, 0\n\t" \
    "v_mov_b32 v112, 0\n\t" \
    "v_mov_b32 v113, 0\n\t" \
    "v_mov_b32 v114, 0\n\t" \
    "v_mov_b32 v115, 0\n\t" \
    "v_mov_b32 v116, 0\n\t" \
    "v_mov_b32 v117, 0\n\t" \
    "v_mov_b32 v118, 0\n\t" \
    "v_mov_b32 v119, 0\n\t" \
    "v_mov_b32 v120, 0\n\t" \
    "v_mov_b32 v121, 0\n\t" \
    "v_mov_b32 v122, 0\n\t" \
    "v_mov_b32 v123, 0\n\t" \
    "v_mov_b32 v124, 0\n\t" \
    "v_mov_b32 v125, 0\n\t" \
    "v_mov_b32 v126, 0\n\t" \
    "v_mov_b32 v127, 0\n\t" \
    "s_cmp_lg_u32 s65, 0\n\t" \
    "s_cbranch_scc1 vv_Oc85_%=\n\t" \
    "s_add_u32 s79, s81, 240\n\t" \
    "s_sub_u32 s70, s79, s74\n\t" \
    "s_max_i32 s70, s70, 0\n\t" \
    "s_min_i32 s70, s70, 16000\n\t" \
    "s_lshl_b32 s70, s70, 1\n\t" \
    "s_mul_hi_u32 s70, s70, s77\n\t" \
    "s_add_u32 s70, s70, s73\n\t" \
    "vv_W86_%=:\n\t" \
    "s_memtime s[56:57]\n\t" \
    "s_waitcnt lgkmcnt(0)\n\t" \
    "s_sub_u32 s69, s70, s56\n\t" \
    "s_cmp_gt_i32 s69, 24\n\t" \
    "s_cbranch_scc1 vv_W86_%=\n\t" \
    "global_load_dword v140, v35, s[46:47] sc1\n\t" \
    "s_waitcnt vmcnt(0)\n\t" \
    "v_cmp_eq_u32 vcc, -1, v140\n\t" \
    "s_cbranch_vccz vv_Ook84_%=\n\t" \
    "vv_O83_%=:\n\t" \
    "global_load_dword v140, v35, s[46:47] offset:1536 sc1\n\t" \
    "s_waitcnt vmcnt(0)\n\t" \
    "v_cmp_eq_u32 vcc, -1, v140\n\t" \
    "s_cbranch_vccz vv_Ook84_%=\n\t" \
    "s_cmp_eq_u32 s68, 0\n\t" \
    "s_cbranch_scc1 vv_F87_%=\n\t" \
    "s_sub_u32 s68, s68, 1\n\t" \
    "s_sleep 3\n\t" \
    "s_branch vv_O83_%=\n\t" \
    "vv_F87_%=:\n\t" \
    "v_mov_b32 v141, 6095104\n\t" \
    "v_mov_b32 v142, 17\n\t" \
    "global_store_dword v141, v142, s[36:37] sc1\n\t" \
    "s_waitcnt vmcnt(0)\n\t" \
    "s_branch vv_Ook84_%=\n\t" \
    "vv_Ook84_%=:\n\t" \
    "v_subrev_u32 v140, s80, v140\n\t" \
    "s_nop 1\n\t" \
    "v_max_i32_dpp v140, v140, v140 row_ror:1 row_mask:0xf bank_mask:0xf\n\t" \
    "s_nop 1\n\t" \
    "v_max_i32_dpp v140, v140, v140 row_ror:2 row_mask:0xf bank_mask:0xf\n\t" \
    "s_nop 1\n\t" \
    "v_max_i32_dpp v140, v140, v140 row_ror:4 row_mask:0xf bank_mask:0xf\n\t" \
    "s_nop 1\n\t" \
    "v_max_i32_dpp v140, v140, v140 row_ror:8 row_mask:0xf bank_mask:0xf\n\t" \
    "s_nop 0\n\t" \
    "v_readlane_b32 s69, v140, 0\n\t" \
    "v_readlane_b32 s71, v140, 16\n\t" \
    "v_readlane_b32 s70, v140, 32\n\t" \
    "v_readlane_b32 s81, v140, 48\n\t" \
    "s_max_i32 s69, s69, s71\n\t" \
    "s_nop 0\n\t" \
    "s_max_i32 s71, s70, s81\n\t" \
    "s_max_i32 s69, s69, s71\n\t" \
    "s_add_u32 s81, s69, s80\n\t" \
    "vv_Oc85_%=:\n\t" \
    "s_mul_hi_u32 s69, s85, s78\n\t" \
    "s_add_u32 s79, s81, s69\n\t" \
    "s_add_u32 s79, s79, s87\n\t" \
    "s_cmp_ge_u32 s65, 36\n\t" \
    "s_cselect_b32 s69, 28, 0\n\t" \
    "s_sub_u32 s79, s79, s69\n\t" \
    "s_cmp_lt_u32 s65, 8\n\t" \
    "s_cselect_b32 s69, 96, 0\n\t" \
    "s_add_u32 s79, s79, s69\n\t" \
    "s_cmp_ge_u32 s65, 57\n\t" \
    "s_cselect_b32 s69, 96, 0\n\t" \
    "s_add_u32 s79, s79, s69\n\t" \
    "s_add_u32 s80, s79, s88\n\t" \
    "s_add_u32 s65, s65, 1\n\t" \
    "s_branch vv_go66_%=\n\t" \
    "vv_Gbad71_%=:\n\t" \
    "s_waitcnt vmcnt(0) lgkmcnt(0)\n\t" \
    "s_nop 7\n\t" \
    "v_mov_b32 v72, 0\n\t" \
    "v_mov_b32 v73, 0\n\t" \
    "v_mov_b32 v74, 0\n\t" \
    "v_mov_b32 v75, 0\n\t" \
    "v_mov_b32 v76, 0\n\t" \
    "v_mov_b32 v77, 0\n\t" \
    "v_mov_b32 v78, 0\n\t" \
    "v_mov_b32 v79, 0\n\t" \
    "s_cmp_eq_u32 s68, 0\n\t" \
    "s_cbranch_scc1 vv_F88_%=\n\t" \
    "s_sub_u32 s68, s68, 1\n\t" \
    "s_branch vv_G70_%=\n\t" \
    "vv_F88_%=:\n\t" \
    "v_mov_b32 v140, 6095104\n\t" \
    "v_mov_b32 v141, 1\n\t" \
    "global_store_dword v140, v141, s[36:37] sc1\n\t" \
    "s_waitcnt vmcnt(0)\n\t" \
    "s_branch vv_Gc73_%=\n\t" \
    "vv_Gbad272_%=:\n\t" \
    "global_load_dwordx4 v[44:47], v2, s[46:47] sc1\n\t" \
    "s_waitcnt vmcnt(0)\n\t" \
    "v_max3_u32 v140, v44, v45, v46\n\t" \
    "v_max_u32 v140, v140, v47\n\t" \
    "v_cmp_eq_u32 vcc, -1, v140\n\t" \
    "s_cbranch_vccz vv_Gre275_%=\n\t" \
    "s_cmp_eq_u32 s68, 0\n\t" \
    "s_cbranch_scc1 vv_F89_%=\n\t" \
    "s_sub_u32 s68, s68, 1\n\t" \
    "s_sleep 1\n\t" \
    "s_branch vv_Gbad272_%=\n\t" \
    "vv_F89_%=:\n\t" \
    "v_mov_b32 v140, 6095104\n\t" \
    "v_mov_b32 v141, 2\n\t" \
    "global_store_dword v140, v141, s[36:37] sc1\n\t" \
    "s_waitcnt vmcnt(0)\n\t" \
    "s_branch vv_Gre275_%=\n\t" \
    "vv_Gre275_%=:\n\t" \
    "v_mov_b32 v72, 0\n\t" \
    "v_mov_b32 v73, 0\n\t" \
    "v_mov_b32 v74, 0\n\t" \
    "v_mov_b32 v75, 0\n\t" \
    "v_mov_b32 v76, 0\n\t" \
    "v_mov_b32 v77, 0\n\t" \
    "v_mov_b32 v78, 0\n\t" \
    "v_mov_b32 v79, 0\n\t" \
    "v_smfmac_f32_16x16x64_bf16 v[72:75], v[36:39], a[32:39], v10\n\t" \
    "s_nop 0\n\t" \
    "v_smfmac_f32_16x16x64_bf16 v[76:79], v[36:39], a[104:111], v10\n\t" \
    "v_smfmac_f32_16x16x64_bf16 v[72:75], v[40:43], a[40:47], v10\n\t" \
    "v_smfmac_f32_16x16x64_bf16 v[76:79], v[40:43], a[112:119], v10\n\t" \
    "v_smfmac_f32_16x16x64_bf16 v[72:75], v[44:47], a[64:71], v10\n\t" \
    "v_smfmac_f32_16x16x64_bf16 v[76:79], v[44:47], a[136:143], v10\n\t" \
    "s_branch vv_Gc274_%=\n\t" \
    "vv_end65_%=:\n\t" \
    "s_endpgm\n\t" \
    "vv_loop13_%=:\n\t" \
    "v_mov_b32 v72, 0\n\t" \
    "v_mov_b32 v73, 0\n\t" \
    "v_mov_b32 v74, 0\n\t" \
    "v_mov_b32 v75, 0\n\t" \
    "v_mov_b32 v76, 0\n\t" \
    "v_mov_b32 v77, 0\n\t" \
    "v_mov_b32 v78, 0\n\t" \
    "v_mov_b32 v79, 0\n\t" \
    "v_mov_b32 v92, 0\n\t" \
    "v_mov_b32 v93, 0\n\t" \
    "v_mov_b32 v94, 0\n\t" \
    "v_mov_b32 v95, 0\n\t" \
    "v_mov_b32 v96, 0\n\t" \
    "v_mov_b32 v97, 0\n\t" \
    "v_mov_b32 v98, 0\n\t" \
    "v_mov_b32 v99, 0\n\t" \
    "v_mov_b32 v100, 0\n\t" \
    "v_mov_b32 v101, 0\n\t" \
    "v_mov_b32 v102, 0\n\t" \
    "v_mov_b32 v103, 0\n\t" \
    "v_mov_b32 v104, 0\n\t" \
    "v_mov_b32 v105, 0\n\t" \
    "v_mov_b32 v106, 0\n\t" \
    "v_mov_b32 v107, 0\n\t" \
    "v_mov_b32 v108, 0\n\t" \
    "v_mov_b32 v109, 0\n\t" \
    "v_mov_b32 v110, 0\n\t" \
    "v_mov_b32 v111, 0\n\t" \
    "v_mov_b32 v112, 0\n\t" \
    "v_mov_b32 v113, 0\n\t" \
    "v_mov_b32 v114, 0\n\t" \
    "v_mov_b32 v115, 0\n\t" \
    "v_mov_b32 v116, 0\n\t" \
    "v_mov_b32 v117, 0\n\t" \
    "v_mov_b32 v118, 0\n\t" \
    "v_mov_b32 v119, 0\n\t" \
    "v_mov_b32 v120, 0\n\t" \
    "v_mov_b32 v121, 0\n\t" \
    "v_mov_b32 v122, 0\n\t" \
    "v_mov_b32 v123, 0\n\t" \
    "v_mov_b32 v124, 0\n\t" \
    "v_mov_b32 v125, 0\n\t" \
    "v_mov_b32 v126, 0\n\t" \
    "v_mov_b32 v127, 0\n\t" \
    "vv_go91_%=:\n\t" \
    "s_add_u32 s69, s65, 0\n\t" \
    "s_bitcmp1_b32 s69, 0\n\t" \
    "s_cselect_b32 s70, s63, s62\n\t" \
    "s_bfe_u32 s69, s69, 0x20001\n\t" \
    "s_lshl_b32 s69, s69, 19\n\t" \
    "s_add_u32 s69, s69, s70\n\t" \
    "s_add_u32 s46, s36, s69\n\t" \
    "s_addc_u32 s47, s37, 0\n\t" \
    "s_add_u32 s69, s65, 0\n\t" \
    "s_lshr_b32 s69, s69, 1\n\t" \
    "s_xor_b32 s69, s69, s97\n\t" \
    "s_and_b32 s69, s69, 1\n\t" \
    "s_lshl_b32 s69, s69, 14\n\t" \
    "s_or_b32 s46, s46, s69\n\t" \
    "s_add_u32 s69, s65, 0\n\t" \
    "s_mul_i32 s70, s69, 171\n\t" \
    "s_lshr_b32 s70, s70, 9\n\t" \
    "s_mul_i32 s70, s70, 3\n\t" \
    "s_sub_u32 s69, s69, s70\n\t" \
    "s_mul_i32 s69, s69, 36\n\t" \
    "s_lshl_b32 s69, s69, 14\n\t" \
    "s_add_u32 s69, s69, s64\n\t" \
    "s_add_u32 s69, s69, 4194304\n\t" \
    "s_add_u32 s38, s36, s69\n\t" \
    "s_addc_u32 s39, s37, 0\n\t" \
    "s_mul_i32 s69, s65, 9216\n\t" \
    "s_add_u32 s69, s69, 123731968\n\t" \
    "s_add_u32 s40, s36, s69\n\t" \
    "s_addc_u32 s41, s37, 0\n\t" \
    "s_sub_u32 s70, s79, s74\n\t" \
    "s_max_i32 s70, s70, 0\n\t" \
    "s_min_i32 s70, s70, 16000\n\t" \
    "s_lshl_b32 s70, s70, 1\n\t" \
    "s_mul_hi_u32 s70, s70, s77\n\t" \
    "s_add_u32 s70, s70, s73\n\t" \
    "s_sub_u32 s70, s70, 100\n\t" \
    "s_getpc_b64 s[30:31]\n\t" \
    "vv_Wp93_%=:\n\t" \
    "s_add_u32 s30, s30, vv_We94_%=-vv_Wp93_%=\n\t" \
    "s_addc_u32 s31, s31, 0\n\t" \
    "vv_W92_%=:\n\t" \
    "s_memtime s[56:57]\n\t" \
    "s_waitcnt lgkmcnt(0)\n\t" \
    "s_sub_u32 s69, s70, s56\n\t" \
    "s_cmp_gt_i32 s69, 111\n\t" \
    "s_cbranch_scc1 vv_W92_%=\n\t" \
    "s_max_i32 s69, s69, 0\n\t" \
    "s_lshr_b32 s69, s69, 1\n\t" \
    "s_and_b32 s69, s69, 0x3c\n\t" \
    "s_sub_u32 s30, s30, s69\n\t" \
    "s_subb_u32 s31, s31, 0\n\t" \
    "s_setpc_b64 s[30:31]\n\t" \
    ".p2align 6\n\t" \
    "s_nop 0\n\t" \
    "s_nop 0\n\t" \
    "s_nop 0\n\t" \
    "s_nop 1\n\t" \
    "s_nop 1\n\t" \
    "s_nop 1\n\t" \
    "s_nop 1\n\t" \
    "s_nop 1\n\t" \
    "s_nop 1\n\t" \
    "s_nop 1\n\t" \
    "s_nop 1\n\t" \
    "s_nop 1\n\t" \
    "s_nop 1\n\t" \
    "s_nop 1\n\t" \
    "s_nop 1\n\t" \
    "s_nop 1\n\t" \
    "vv_We94_%=:\n\t" \
    "vv_G95_%=:\n\t" \
    "global_load_dwordx4 v[36:39], v1, s[46:47] sc1\n\t" \
    "s_nop 9\n\t" \
    "global_load_dwordx4 v[40:43], v1, s[46:47] offset:1024 sc1\n\t" \
    "s_nop 9\n\t" \
    "global_load_dwordx4 v[44:47], v2, s[46:47] sc1\n\t" \
    "s_waitcnt vmcnt(2)\n\t" \
    "v_smfmac_f32_16x16x64_bf16 v[72:75], v[36:39], a[48:55], v10\n\t" \
    "ds_write_b128 v0, v[36:39] offset:6144\n\t" \
    "v_smfmac_f32_16x16x64_bf16 v[76:79], v[36:39], a[120:127], v10\n\t" \
    "v_max3_u32 v140, v36, v37, v38\n\t" \
    "s_waitcnt vmcnt(1)\n\t" \
    "ds_write_b128 v0, v[40:43] offset:7168\n\t" \
    "v_smfmac_f32_16x16x64_bf16 v[72:75], v[40:43], a[56:63], v10\n\t" \
    "v_max3_u32 v140, v140, v39, v40\n\t" \
    "v_max3_u32 v140, v140, v41, v42\n\t" \
    "v_max_u32 v140, v140, v43\n\t" \
    "v_cmp_eq_u32 vcc, -1, v140\n\t" \
    "v_smfmac_f32_16x16x64_bf16 v[76:79], v[40:43], a[128:135], v10\n\t" \
    "s_cbranch_vccnz vv_Gbad96_%=\n\t" \
    "vv_Gc98_%=:\n\t" \
    "s_waitcnt lgkmcnt(0)\n\t" \
    "s_barrier\n\t" \
    "ds_read_b128 v[48:51], v0 offset:0\n\t" \
    "ds_read_b128 v[52:55], v0 offset:1024\n\t" \
    "s_waitcnt vmcnt(0)\n\t" \
    "v_smfmac_f32_16x16x64_bf16 v[72:75], v[44:47], a[64:71], v10\n\t" \
    "ds_read_b128 v[56:59], v0 offset:2048\n\t" \
    "ds_read_b128 v[60:63], v0 offset:3072\n\t" \
    "v_max3_u32 v140, v44, v45, v46\n\t" \
    "v_max_u32 v140, v140, v47\n\t" \
    "v_smfmac_f32_16x16x64_bf16 v[76:79], v[44:47], a[136:143], v10\n\t" \
    "ds_read_b128 v[64:67], v0 offset:4096\n\t" \
    "ds_read_b128 v[68:71], v0 offset:5120\n\t" \
    "v_cmp_eq_u32 vcc, -1, v140\n\t" \
    "s_cbranch_vccnz vv_Gbad297_%=\n\t" \
    "vv_Gc299_%=:\n\t" \
    "s_waitcnt lgkmcnt(5)\n\t" \
    "v_smfmac_f32_16x16x64_bf16 v[72:75], v[48:51], a[0:7], v10\n\t" \
    "s_waitcnt lgkmcnt(4)\n\t" \
    "v_smfmac_f32_16x16x64_bf16 v[72:75], v[52:55], a[8:15], v10\n\t" \
    "s_waitcnt lgkmcnt(3)\n\t" \
    "v_smfmac_f32_16x16x64_bf16 v[72:75], v[56:59], a[16:23], v10\n\t" \
    "s_waitcnt lgkmcnt(2)\n\t" \
    "v_smfmac_f32_16x16x64_bf16 v[72:75], v[60:63], a[24:31], v10\n\t" \
    "s_waitcnt lgkmcnt(1)\n\t" \
    "v_smfmac_f32_16x16x64_bf16 v[72:75], v[64:67], a[32:39], v10\n\t" \
    "s_waitcnt lgkmcnt(0)\n\t" \
    "v_smfmac_f32_16x16x64_bf16 v[72:75], v[68:71], a[40:47], v10\n\t" \
    "v_smfmac_f32_16x16x64_bf16 v[76:79], v[48:51], a[72:79], v10\n\t" \
    "s_nop 1\n\t" \
    "s_nop 0\n\t" \
    "v_smfmac_f32_16x16x64_bf16 v[76:79], v[52:55], a[80:87], v10\n\t" \
    "s_nop 1\n\t" \
    "s_nop 0\n\t" \
    "v_smfmac_f32_16x16x64_bf16 v[76:79], v[56:59], a[88:95], v10\n\t" \
    "v_add_f32 v72, v72, v74\n\t" \
    "v_smfmac_f32_16x16x64_bf16 v[76:79], v[60:63], a[96:103], v10\n\t" \
    "v_add_f32 v73, v73, v75\n\t" \
    "v_smfmac_f32_16x16x64_bf16 v[76:79], v[64:67], a[104:111], v10\n\t" \
    "v_cvt_pk_bf16_f32 v80, v72, v73\n\t" \
    "v_smfmac_f32_16x16x64_bf16 v[76:79], v[68:71], a[112:119], v10\n\t" \
    "ds_write_b32 v12, v80 offset:0\n\t" \
    "s_nop 6\n\t" \
    "v_add_f32 v76, v76, v78\n\t" \
    "v_add_f32 v77, v77, v79\n\t" \
    "v_cvt_pk_bf16_f32 v81, v76, v77\n\t" \
    "ds_write_b32 v12, v81 offset:64\n\t" \
    "s_waitcnt lgkmcnt(0)\n\t" \
    "s_barrier\n\t" \
    "ds_read_b64_tr_b16 v[84:85], v13 offset:0\n\t" \
    "ds_read_b64_tr_b16 v[86:87], v13 offset:128\n\t" \
    "ds_read_b64_tr_b16 v[88:89], v13 offset:1024\n\t" \
    "ds_read_b64_tr_b16 v[90:91], v13 offset:1152\n\t" \
    "s_waitcnt lgkmcnt(2)\n\t" \
    "v_smfmac_f32_16x16x64_bf16 v[92:95], v[84:87], a[144:151], v10\n\t" \
    "s_nop 2\n\t" \
    "s_waitcnt lgkmcnt(0)\n\t" \
    "v_smfmac_f32_16x16x64_bf16 v[92:95], v[88:91], a[152:159], v10\n\t" \
    "s_nop 2\n\t" \
    "v_smfmac_f32_16x16x64_bf16 v[96:99], v[84:87], a[160:167], v10\n\t" \
    "s_nop 2\n\t" \
    "v_smfmac_f32_16x16x64_bf16 v[96:99], v[88:91], a[168:175], v10\n\t" \
    "s_nop 2\n\t" \
    "v_smfmac_f32_16x16x64_bf16 v[100:103], v[84:87], a[176:183], v10\n\t" \
    "s_nop 0\n\t" \
    "v_add_f32 v92, v92, v94\n\t" \
    "v_add_f32 v93, v93, v95\n\t" \
    "v_smfmac_f32_16x16x64_bf16 v[100:103], v[88:91], a[184:191], v10\n\t" \
    "s_nop 1\n\t" \
    "v_cvt_pk_bf16_f32 v128, v92, v93\n\t" \
    "v_smfmac_f32_16x16x64_bf16 v[104:107], v[84:87], a[192:199], v10\n\t" \
    "s_nop 0\n\t" \
    "v_add_f32 v96, v96, v98\n\t" \
    "v_add_f32 v97, v97, v99\n\t" \
    "v_smfmac_f32_16x16x64_bf16 v[104:107], v[88:91], a[200:207], v10\n\t" \
    "s_nop 1\n\t" \
    "v_cvt_pk_bf16_f32 v129, v96, v97\n\t" \
    "v_smfmac_f32_16x16x64_bf16 v[124:127], v[84:87], v[240:247], v10\n\t" \
    "s_nop 0\n\t" \
    "v_add_f32 v100, v100, v102\n\t" \
    "v_add_f32 v101, v101, v103\n\t" \
    "v_smfmac_f32_16x16x64_bf16 v[124:127], v[88:91], v[248:255], v10\n\t" \
    "s_nop 1\n\t" \
    "global_load_dword v140, v20, s[46:47] sc1\n\t" \
    "v_cvt_pk_bf16_f32 v130, v100, v101\n\t" \
    "v_smfmac_f32_16x16x64_bf16 v[108:111], v[84:87], a[208:215], v10\n\t" \
    "s_nop 0\n\t" \
    "v_add_f32 v104, v104, v106\n\t" \
    "v_add_f32 v105, v105, v107\n\t" \
    "v_smfmac_f32_16x16x64_bf16 v[108:111], v[88:91], a[216:223], v10\n\t" \
    "s_nop 0\n\t" \
    "v_cvt_pk_bf16_f32 v131, v104, v105\n\t" \
    "v_permlane32_swap_b32 v128, v130\n\t" \
    "v_smfmac_f32_16x16x64_bf16 v[112:115], v[84:87], a[224:231], v10\n\t" \
    "s_nop 0\n\t" \
    "v_permlane32_swap_b32 v129, v131\n\t" \
    "s_nop 1\n\t" \
    "v_permlane16_swap_b32 v128, v129\n\t" \
    "v_smfmac_f32_16x16x64_bf16 v[112:115], v[88:91], a[232:239], v10\n\t" \
    "v_permlane16_swap_b32 v130, v131\n\t" \
    "global_store_dwordx4 v14, v[128:131], s[38:39] sc1\n\t" \
    "v_add_f32 v124, v124, v126\n\t" \
    "v_smfmac_f32_16x16x64_bf16 v[116:119], v[84:87], a[240:247], v10\n\t" \
    "s_nop 0\n\t" \
    "v_add_f32 v125, v125, v127\n\t" \
    "v_cvt_pk_bf16_f32 v136, v124, v125\n\t" \
    "v_smfmac_f32_16x16x64_bf16 v[116:119], v[88:91], a[248:255], v10\n\t" \
    "global_store_dword v16, v136, s[38:39] sc1\n\t" \
    "v_add_f32 v108, v108, v110\n\t" \
    "v_add_f32 v109, v109, v111\n\t" \
    "v_smfmac_f32_16x16x64_bf16 v[120:123], v[84:87], v[224:231], v10\n\t" \
    "s_nop 0\n\t" \
    "v_cvt_pk_bf16_f32 v132, v108, v109\n\t" \
    "v_add_f32 v112, v112, v114\n\t" \
    "v_smfmac_f32_16x16x64_bf16 v[120:123], v[88:91], v[232:239], v10\n\t" \
    "v_add_f32 v113, v113, v115\n\t" \
    "v_cvt_pk_bf16_f32 v133, v112, v113\n\t" \
    "v_add_f32 v116, v116, v118\n\t" \
    "v_add_f32 v117, v117, v119\n\t" \
    "v_cvt_pk_bf16_f32 v134, v116, v117\n\t" \
    "s_nop 2\n\t" \
    "v_pk_add_f32 v[120:121], v[120:121], v[122:123]\n\t" \
    "v_cvt_pk_bf16_f32 v135, v120, v121\n\t" \
    "v_permlane32_swap_b32 v132, v134\n\t" \
    "s_nop 0\n\t" \
    "v_permlane32_swap_b32 v133, v135\n\t" \
    "s_nop 1\n\t" \
    "v_permlane16_swap_b32 v132, v133\n\t" \
    "v_permlane16_swap_b32 v134, v135\n\t" \
    "global_store_dwordx4 v15, v[132:135], s[38:39] sc1\n\t" \
    "s_nop 1\n\t" \
    "global_store_dwordx4 v17, v[128:131], s[40:41] sc1\n\t" \
    "global_store_dwordx4 v17, v[132:135], s[40:41] offset:1024 sc1\n\t" \
    "global_store_dword v18, v136, s[40:41] sc1\n\t" \
    "s_cmp_eq_u32 s65, 63\n\t" \
    "s_cbranch_scc1 vv_end90_%=\n\t" \
    "s_mov_b32 s71, 3\n\t" \
    "vv_E101_%=:\n\t" \
    "s_memtime s[54:55]\n\t" \
    "s_nop 0\n\t" \
    "s_memrealtime s[4:5]\n\t" \
    "s_nop 0\n\t" \
    "s_memrealtime s[6:7]\n\t" \
    "s_nop 0\n\t" \
    "s_memrealtime s[8:9]\n\t" \
    "s_nop 0\n\t" \
    "s_memrealtime s[10:11]\n\t" \
    "s_nop 0\n\t" \
    "s_memrealtime s[12:13]\n\t" \
    "s_nop 0\n\t" \
    "s_memrealtime s[14:15]\n\t" \
    "s_nop 0\n\t" \
    "s_memrealtime s[16:17]\n\t" \
    "s_nop 0\n\t" \
    "s_memrealtime s[18:19]\n\t" \
    "s_nop 0\n\t" \
    "s_memrealtime s[20:21]\n\t" \
    "s_nop 0\n\t" \
    "s_memrealtime s[22:23]\n\t" \
    "s_nop 0\n\t" \
    "s_memrealtime s[24:25]\n\t" \
    "s_nop 0\n\t" \
    "s_memrealtime s[26:27]\n\t" \
    "s_nop 0\n\t" \
    "s_memrealtime s[28:29]\n\t" \
    "s_nop 0\n\t" \
    "s_memtime s[56:57]\n\t" \
    "s_mov_b32 s70, 1\n\t" \
    "s_sub_u32 s71, s71, 1\n\t" \
    "s_cselect_b32 s69, 0, -1\n\t" \
    "s_waitcnt lgkmcnt(0)\n\t" \
    "s_sub_u32 s54, s56, s54\n\t" \
    "s_and_b32 s54, s54, s69\n\t" \
    "s_cmp_gt_u32 s54, 124\n\t" \
    "s_cbranch_scc1 vv_E101_%=\n\t" \
    "s_cmp_eq_u32 s4, s28\n\t" \
    "s_addc_u32 s70, s70, 0\n\t" \
    "s_cmp_eq_u32 s6, s28\n\t" \
    "s_addc_u32 s70, s70, 0\n\t" \
    "s_cmp_eq_u32 s8, s28\n\t" \
    "s_addc_u32 s70, s70, 0\n\t" \
    "s_cmp_eq_u32 s10, s28\n\t" \
    "s_addc_u32 s70, s70, 0\n\t" \
    "s_cmp_eq_u32 s12, s28\n\t" \
    "s_addc_u32 s70, s70, 0\n\t" \
    "s_cmp_eq_u32 s14, s28\n\t" \
    "s_addc_u32 s70, s70, 0\n\t" \
    "s_cmp_eq_u32 s16, s28\n\t" \
    "s_addc_u32 s70, s70, 0\n\t" \
    "s_cmp_eq_u32 s18, s28\n\t" \
    "s_addc_u32 s70, s70, 0\n\t" \
    "s_cmp_eq_u32 s20, s28\n\t" \
    "s_addc_u32 s70, s70, 0\n\t" \
    "s_cmp_eq_u32 s22, s28\n\t" \
    "s_addc_u32 s70, s70, 0\n\t" \
    "s_cmp_eq_u32 s24, s28\n\t" \
    "s_addc_u32 s70, s70, 0\n\t" \
    "s_cmp_eq_u32 s26, s28\n\t" \
    "s_addc_u32 s70, s70, 0\n\t" \
    "s_mov_b32 s75, s73\n\t" \
    "s_mov_b32 s76, s74\n\t" \
    "s_lshl_b32 s70, s70, 3\n\t" \
    "s_sub_u32 s73, s56, s70\n\t" \
    "s_lshl_b32 s74, s28, 4\n\t" \
    "s_sub_u32 s69, s73, s75\n\t" \
    "s_sub_u32 s70, s74, s76\n\t" \
    "s_sub_u32 s71, s70, 1600\n\t" \
    "s_cmp_lt_u32 s71, 400000\n\t" \
    "s_cbranch_scc0 vv_K103_%=\n\t" \
    "v_cvt_f32_u32 v141, s69\n\t" \
    "v_cvt_f32_u32 v142, s70\n\t" \
    "v_rcp_f32 v143, v141\n\t" \
    "v_rcp_f32 v144, v142\n\t" \
    "v_mul_f32 v143, v143, v142\n\t" \
    "v_mul_f32 v144, v144, v141\n\t" \
    "v_mul_f32 v143, 0x4f800000, v143\n\t" \
    "v_mul_f32 v144, 0x4f000000, v144\n\t" \
    "v_cvt_u32_f32 v143, v143\n\t" \
    "v_cvt_u32_f32 v144, v144\n\t" \
    "v_readfirstlane_b32 s69, v143\n\t" \
    "v_readfirstlane_b32 s70, v144\n\t" \
    "s_nop 1\n\t" \
    "s_sub_u32 s69, s69, s78\n\t" \
    "s_ashr_i32 s69, s69, 2\n\t" \
    "s_add_u32 s78, s78, s69\n\t" \
    "s_sub_u32 s70, s70, s77\n\t" \
    "s_ashr_i32 s70, s70, 2\n\t" \
    "s_add_u32 s77, s77, s70\n\t" \
    "vv_K103_%=:\n\t" \
    "s_waitcnt vmcnt(6)\n\t" \
    "v_cmp_eq_u32 vcc, -1, v140\n\t" \
    "s_cbranch_vccz vv_Hok105_%=\n\t" \
    "vv_H104_%=:\n\t" \
    "global_load_dword v140, v20, s[46:47] sc1\n\t" \
    "s_waitcnt vmcnt(0)\n\t" \
    "v_cmp_eq_u32 vcc, -1, v140\n\t" \
    "s_cbranch_vccz vv_Hc106_%=\n\t" \
    "s_cmp_eq_u32 s68, 0\n\t" \
    "s_cbranch_scc1 vv_F107_%=\n\t" \
    "s_sub_u32 s68, s68, 1\n\t" \
    "s_sleep 5\n\t" \
    "s_branch vv_H104_%=\n\t" \
    "vv_F107_%=:\n\t" \
    "v_mov_b32 v141, 6095104\n\t" \
    "v_mov_b32 v142, 6\n\t" \
    "global_store_dword v141, v142, s[36:37] sc1\n\t" \
    "s_waitcnt vmcnt(0)\n\t" \
    "s_branch vv_Hc106_%=\n\t" \
    "vv_Hc106_%=:\n\t" \
    "vv_Hok105_%=:\n\t" \
    "v_subrev_u32 v140, s80, v140\n\t" \
    "v_and_b32 v20, -5, v20\n\t" \
    "s_nop 0\n\t" \
    "v_max_i32_dpp v140, v140, v140 row_ror:1 row_mask:0xf bank_mask:0xf\n\t" \
    "s_nop 1\n\t" \
    "v_max_i32_dpp v140, v140, v140 row_ror:2 row_mask:0xf bank_mask:0xf\n\t" \
    "s_nop 1\n\t" \
    "v_max_i32_dpp v140, v140, v140 row_ror:4 row_mask:0xf bank_mask:0xf\n\t" \
    "s_nop 1\n\t" \
    "v_max_i32_dpp v140, v140, v140 row_ror:8 row_mask:0xf bank_mask:0xf\n\t" \
    "s_nop 0\n\t" \
    "v_readlane_b32 s69, v140, 0\n\t" \
    "v_readlane_b32 s71, v140, 16\n\t" \
    "v_readlane_b32 s70, v140, 32\n\t" \
    "v_readlane_b32 s81, v140, 48\n\t" \
    "s_max_i32 s69, s69, s71\n\t" \
    "s_nop 0\n\t" \
    "s_max_i32 s71, s70, s81\n\t" \
    "s_max_i32 s69, s69, s71\n\t" \
    "s_add_u32 s81, s69, s80\n\t" \
    "v_mov_b32 v72, 0\n\t" \
    "v_mov_b32 v73, 0\n\t" \
    "v_mov_b32 v74, 0\n\t" \
    "v_mov_b32 v75, 0\n\t" \
    "v_mov_b32 v76, 0\n\t" \
    "v_mov_b32 v77, 0\n\t" \
    "v_mov_b32 v78, 0\n\t" \
    "v_mov_b32 v79, 0\n\t" \
    "v_mov_b32 v92, 0\n\t" \
    "v_mov_b32 v93, 0\n\t" \
    "v_mov_b32 v94, 0\n\t" \
    "v_mov_b32 v95, 0\n\t" \
    "v_mov_b32 v96, 0\n\t" \
    "v_mov_b32 v97, 0\n\t" \
    "v_mov_b32 v98, 0\n\t" \
    "v_mov_b32 v99, 0\n\t" \
    "v_mov_b32 v100, 0\n\t" \
    "v_mov_b32 v101, 0\n\t" \
    "v_mov_b32 v102, 0\n\t" \
    "v_mov_b32 v103, 0\n\t" \
    "v_mov_b32 v104, 0\n\t" \
    "v_mov_b32 v105, 0\n\t" \
    "v_mov_b32 v106, 0\n\t" \
    "v_mov_b32 v107, 0\n\t" \
    "v_mov_b32 v108, 0\n\t" \
    "v_mov_b32 v109, 0\n\t" \
    "v_mov_b32 v110, 0\n\t" \
    "v_mov_b32 v111, 0\n\t" \
    "v_mov_b32 v112, 0\n\t" \
    "v_mov_b32 v113, 0\n\t" \
    "v_mov_b32 v114, 0\n\t" \
    "v_mov_b32 v115, 0\n\t" \
    "v_mov_b32 v116, 0\n\t" \
    "v_mov_b32 v117, 0\n\t" \
    "v_mov_b32 v118, 0\n\t" \
    "v_mov_b32 v119, 0\n\t" \
    "v_mov_b32 v120, 0\n\t" \
    "v_mov_b32 v121, 0\n\t" \
    "v_mov_b32 v122, 0\n\t" \
    "v_mov_b32 v123, 0\n\t" \
    "v_mov_b32 v124, 0\n\t" \
    "v_mov_b32 v125, 0\n\t" \
    "v_mov_b32 v126, 0\n\t" \
    "v_mov_b32 v127, 0\n\t" \
    "s_cmp_lg_u32 s65, 0\n\t" \
    "s_cbranch_scc1 vv_Oc110_%=\n\t" \
    "s_add_u32 s79, s81, 240\n\t" \
    "s_sub_u32 s70, s79, s74\n\t" \
    "s_max_i32 s70, s70, 0\n\t" \
    "s_min_i32 s70, s70, 16000\n\t" \
    "s_lshl_b32 s70, s70, 1\n\t" \
    "s_mul_hi_u32 s70, s70, s77\n\t" \
    "s_add_u32 s70, s70, s73\n\t" \
    "vv_W111_%=:\n\t" \
    "s_memtime s[56:57]\n\t" \
    "s_waitcnt lgkmcnt(0)\n\t" \
    "s_sub_u32 s69, s70, s56\n\t" \
    "s_cmp_gt_i32 s69, 24\n\t" \
    "s_cbranch_scc1 vv_W111_%=\n\t" \
    "global_load_dword v140, v35, s[46:47] sc1\n\t" \
    "s_waitcnt vmcnt(0)\n\t" \
    "v_cmp_eq_u32 vcc, -1, v140\n\t" \
    "s_cbranch_vccz vv_Ook109_%=\n\t" \
    "vv_O108_%=:\n\t" \
    "global_load_dword v140, v35, s[46:47] offset:1536 sc1\n\t" \
    "s_waitcnt vmcnt(0)\n\t" \
    "v_cmp_eq_u32 vcc, -1, v140\n\t" \
    "s_cbranch_vccz vv_Ook109_%=\n\t" \
    "s_cmp_eq_u32 s68, 0\n\t" \
    "s_cbranch_scc1 vv_F112_%=\n\t" \
    "s_sub_u32 s68, s68, 1\n\t" \
    "s_sleep 3\n\t" \
    "s_branch vv_O108_%=\n\t" \
    "vv_F112_%=:\n\t" \
    "v_mov_b32 v141, 6095104\n\t" \
    "v_mov_b32 v142, 17\n\t" \
    "global_store_dword v141, v142, s[36:37] sc1\n\t" \
    "s_waitcnt vmcnt(0)\n\t" \
    "s_branch vv_Ook109_%=\n\t" \
    "vv_Ook109_%=:\n\t" \
    "v_subrev_u32 v140, s80, v140\n\t" \
    "s_nop 1\n\t" \
    "v_max_i32_dpp v140, v140, v140 row_ror:1 row_mask:0xf bank_mask:0xf\n\t" \
    "s_nop 1\n\t" \
    "v_max_i32_dpp v140, v140, v140 row_ror:2 row_mask:0xf bank_mask:0xf\n\t" \
    "s_nop 1\n\t" \
    "v_max_i32_dpp v140, v140, v140 row_ror:4 row_mask:0xf bank_mask:0xf\n\t" \
    "s_nop 1\n\t" \
    "v_max_i32_dpp v140, v140, v140 row_ror:8 row_mask:0xf bank_mask:0xf\n\t" \
    "s_nop 0\n\t" \
    "v_readlane_b32 s69, v140, 0\n\t" \
    "v_readlane_b32 s71, v140, 16\n\t" \
    "v_readlane_b32 s70, v140, 32\n\t" \
    "v_readlane_b32 s81, v140, 48\n\t" \
    "s_max_i32 s69, s69, s71\n\t" \
    "s_nop 0\n\t" \
    "s_max_i32 s71, s70, s81\n\t" \
    "s_max_i32 s69, s69, s71\n\t" \
    "s_add_u32 s81, s69, s80\n\t" \
    "vv_Oc110_%=:\n\t" \
    "s_mul_hi_u32 s69, s85, s78\n\t" \
    "s_add_u32 s79, s81, s69\n\t" \
    "s_add_u32 s79, s79, s87\n\t" \
    "s_cmp_ge_u32 s65, 36\n\t" \
    "s_cselect_b32 s69, 28, 0\n\t" \
    "s_sub_u32 s79, s79, s69\n\t" \
    "s_cmp_lt_u32 s65, 8\n\t" \
    "s_cselect_b32 s69, 96, 0\n\t" \
    "s_add_u32 s79, s79, s69\n\t" \
    "s_cmp_ge_u32 s65, 57\n\t" \
    "s_cselect_b32 s69, 96, 0\n\t" \
    "s_add_u32 s79, s79, s69\n\t" \
    "s_add_u32 s80, s79, s88\n\t" \
    "s_add_u32 s65, s65, 1\n\t" \
    "s_branch vv_go91_%=\n\t" \
    "vv_Gbad96_%=:\n\t" \
    "s_waitcnt vmcnt(0) lgkmcnt(0)\n\t" \
    "s_nop 7\n\t" \
    "v_mov_b32 v72, 0\n\t" \
    "v_mov_b32 v73, 0\n\t" \
    "v_mov_b32 v74, 0\n\t" \
    "v_mov_b32 v75, 0\n\t" \
    "v_mov_b32 v76, 0\n\t" \
    "v_mov_b32 v77, 0\n\t" \
    "v_mov_b32 v78, 0\n\t" \
    "v_mov_b32 v79, 0\n\t" \
    "s_cmp_eq_u32 s68, 0\n\t" \
    "s_cbranch_scc1 vv_F113_%=\n\t" \
    "s_sub_u32 s68, s68, 1\n\t" \
    "s_branch vv_G95_%=\n\t" \
    "vv_F113_%=:\n\t" \
    "v_mov_b32 v140, 6095104\n\t" \
    "v_mov_b32 v141, 1\n\t" \
    "global_store_dword v140, v141, s[36:37] sc1\n\t" \
    "s_waitcnt vmcnt(0)\n\t" \
    "s_branch vv_Gc98_%=\n\t" \
    "vv_Gbad297_%=:\n\t" \
    "global_load_dwordx4 v[44:47], v2, s[46:47] sc1\n\t" \
    "s_waitcnt vmcnt(0)\n\t" \
    "v_max3_u32 v140, v44, v45, v46\n\t" \
    "v_max_u32 v140, v140, v47\n\t" \
    "v_cmp_eq_u32 vcc, -1, v140\n\t" \
    "s_cbranch_vccz vv_Gre2100_%=\n\t" \
    "s_cmp_eq_u32 s68, 0\n\t" \
    "s_cbranch_scc1 vv_F114_%=\n\t" \
    "s_sub_u32 s68, s68, 1\n\t" \
    "s_sleep 1\n\t" \
    "s_branch vv_Gbad297_%=\n\t" \
    "vv_F114_%=:\n\t" \
    "v_mov_b32 v140, 6095104\n\t" \
    "v_mov_b32 v141, 2\n\t" \
    "global_store_dword v140, v141, s[36:37] sc1\n\t" \
    "s_waitcnt vmcnt(0)\n\t" \
    "s_branch vv_Gre2100_%=\n\t" \
    "vv_Gre2100_%=:\n\t" \
    "v_mov_b32 v72, 0\n\t" \
    "v_mov_b32 v73, 0\n\t" \
    "v_mov_b32 v74, 0\n\t" \
    "v_mov_b32 v75, 0\n\t" \
    "v_mov_b32 v76, 0\n\t" \
    "v_mov_b32 v77, 0\n\t" \
    "v_mov_b32 v78, 0\n\t" \
    "v_mov_b32 v79, 0\n\t" \
    "v_smfmac_f32_16x16x64_bf16 v[72:75], v[36:39], a[48:55], v10\n\t" \
    "s_nop 0\n\t" \
    "v_smfmac_f32_16x16x64_bf16 v[76:79], v[36:39], a[120:127], v10\n\t" \
    "v_smfmac_f32_16x16x64_bf16 v[72:75], v[40:43], a[56:63], v10\n\t" \
    "v_smfmac_f32_16x16x64_bf16 v[76:79], v[40:43], a[128:135], v10\n\t" \
    "v_smfmac_f32_16x16x64_bf16 v[72:75], v[44:47], a[64:71], v10\n\t" \
    "v_smfmac_f32_16x16x64_bf16 v[76:79], v[44:47], a[136:143], v10\n\t" \
    "s_branch vv_Gc299_%=\n\t" \
    "vv_end90_%=:\n\t" \
    "s_endpgm\n\t" \
    "gx_begin_%=:\n\t" \
    "v_mbcnt_lo_u32_b32 v223, -1, 0\n\t" \
    "v_mbcnt_hi_u32_b32 v223, -1, v223\n\t" \
    "s_mov_b32 s68, 262144\n\t" \
    "s_mov_b32 s72, 0\n\t" \
    "s_mov_b32 s83, -1\n\t" \
    "s_mov_b32 s65, 0\n\t" \
    "v_and_b32 v180, 3, v223\n\t" \
    "v_lshlrev_b32 v202, 3, v180\n\t" \
    "v_lshrrev_b32 v181, 5, v223\n\t" \
    "v_lshl_add_u32 v202, v181, 12, v202\n\t" \
    "v_bfe_u32 v182, v223, 2, 2\n\t" \
    "s_mov_b32 s70, 288\n\t" \
    "v_mad_u32_u24 v202, v182, s70, v202\n\t" \
    "v_bfe_u32 v183, v223, 4, 1\n\t" \
    "s_mov_b32 s70, 2176\n\t" \
    "v_mad_u32_u24 v202, v183, s70, v202\n\t" \
    "s_lshl_b32 s69, s66, 6\n\t" \
    "v_xor_b32 v202, s69, v202\n\t" \
    "v_xor_b32 v203, 32, v202\n\t" \
    "v_lshlrev_b32 v3, 4, v180\n\t" \
    "v_lshlrev_b32 v2, 5, v181\n\t" \
    "v_xor_b32 v3, v3, v2\n\t" \
    "s_bfe_u32 s71, s66, 0x10000\n\t" \
    "s_mul_i32 s69, s71, 576\n\t" \
    "s_bfe_u32 s71, s66, 0x10001\n\t" \
    "s_mul_i32 s71, s71, 18432\n\t" \
    "s_add_u32 s69, s69, s71\n\t" \
    "v_lshrrev_b32 v184, 2, v223\n\t" \
    "v_mov_b32 v1, s69\n\t" \
    "s_mov_b32 s70, 1152\n\t" \
    "v_mad_u32_u24 v1, v184, s70, v1\n\t" \
    "v_add_u32 v1, v1, v3\n\t" \
    "v_and_b32 v185, 15, v223\n\t" \
    "v_lshlrev_b32 v3, 6, v185\n\t" \
    "v_lshrrev_b32 v186, 4, v223\n\t" \
    "v_lshl_add_u32 v3, v186, 4, v3\n\t" \
    "v_bfe_u32 v187, v223, 3, 1\n\t" \
    "v_lshlrev_b32 v2, 5, v187\n\t" \
    "v_xor_b32 v3, v3, v2\n\t" \
    "v_mov_b32 v2, v3\n\t" \
    "s_mul_i32 s89, s66, 9216\n\t" \
    "s_mov_b32 s90, 0\n\t" \
    "s_waitcnt vmcnt(12) lgkmcnt(0)\n\t" \
    "s_barrier\n\t" \
    "s_add_u32 m0, s82, 65536\n\t" \
    "s_nop 0\n\t" \
    "global_load_lds_dwordx4 v200, s[42:43]\n\t" \
    "v_add_u32 v200, 131072, v200\n\t" \
    "s_add_u32 m0, s82, 69632\n\t" \
    "s_nop 0\n\t" \
    "global_load_lds_dwordx4 v200, s[42:43]\n\t" \
    "v_add_u32 v200, 131072, v200\n\t" \
    "s_add_u32 m0, s82, 73728\n\t" \
    "s_nop 0\n\t" \
    "global_load_lds_dwordx4 v200, s[42:43]\n\t" \
    "v_add_u32 v200, 131072, v200\n\t" \
    "s_add_u32 m0, s82, 77824\n\t" \
    "s_nop 0\n\t" \
    "global_load_lds_dwordx4 v200, s[42:43]\n\t" \
    "v_add_u32 v200, 131072, v200\n\t" \
    "v_add_u32 v202, 73728, v202\n\t" \
    "v_add_u32 v203, 73728, v203\n\t" \
    "ds_read_b64_tr_b16 a[144:145], v202 offset:0\n\t" \
    "ds_read_b64_tr_b16 a[146:147], v202 offset:1024\n\t" \
    "ds_read_b64_tr_b16 a[148:149], v202 offset:8192\n\t" \
    "ds_read_b64_tr_b16 a[150:151], v202 offset:9216\n\t" \
    "ds_read_b64_tr_b16 a[216:217], v203 offset:0\n\t" \
    "ds_read_b64_tr_b16 a[218:219], v203 offset:1024\n\t" \
    "ds_read_b64_tr_b16 a[220:221], v203 offset:8192\n\t" \
    "ds_read_b64_tr_b16 a[222:223], v203 offset:9216\n\t" \
    "v_lshlrev_b32 v0, 4, v223\n\t" \
    "s_mul_i32 s69, s66, 2048\n\t" \
    "s_add_u32 s69, s69, 147456\n\t" \
    "v_lshl_add_u32 v4, v223, 4, s69\n\t" \
    "s_mov_b32 s69, 147456\n\t" \
    "v_lshl_add_u32 v5, v223, 4, s69\n\t" \
    "v_lshlrev_b32 v6, 2, v223\n\t" \
    "v_mov_b32 v7, 0\n\t" \
    "s_mul_i32 s97, s67, 7\n\t" \
    "s_and_b32 s97, s97, 63\n\t" \
    "s_lshl_b32 s97, s97, 13\n\t" \
    "s_mul_i32 s71, s66, 2048\n\t" \
    "s_add_u32 s69, s97, s71\n\t" \
    "s_mul_i32 s71, s67, 524288\n\t" \
    "s_add_u32 s69, s69, s71\n\t" \
    "v_lshl_add_u32 v8, v223, 4, s69\n\t" \
    "s_add_u32 s38, s36, 6291456\n\t" \
    "s_addc_u32 s39, s37, 0\n\t" \
    "s_add_u32 s40, s36, 23068672\n\t" \
    "s_addc_u32 s41, s37, 0\n\t" \
    "s_mul_i32 s69, s67, 128\n\t" \
    "s_add_u32 s69, s69, 6098944\n\t" \
    "v_and_b32 v188, 1, v223\n\t" \
    "v_mov_b32 v10, s69\n\t" \
    "s_mov_b32 s70, 61440\n\t" \
    "v_mad_u32_u24 v10, v188, s70, v10\n\t" \
    "v_lshlrev_b32 v11, 0, v188\n\t" \
    "s_cmp_eq_u32 s66, 0\n\t" \
    "s_cselect_b32 s58, 3, 0\n\t" \
    "s_mov_b32 s59, 0\n\t" \
    "s_waitcnt vmcnt(12) lgkmcnt(0)\n\t" \
    "s_barrier\n\t" \
    "s_add_u32 m0, s82, 0\n\t" \
    "s_nop 0\n\t" \
    "global_load_lds_dwordx4 v200, s[42:43]\n\t" \
    "v_add_u32 v200, 131072, v200\n\t" \
    "s_add_u32 m0, s82, 4096\n\t" \
    "s_nop 0\n\t" \
    "global_load_lds_dwordx4 v200, s[42:43]\n\t" \
    "v_add_u32 v200, 131072, v200\n\t" \
    "s_add_u32 m0, s82, 8192\n\t" \
    "s_nop 0\n\t" \
    "global_load_lds_dwordx4 v200, s[42:43]\n\t" \
    "v_add_u32 v200, 131072, v200\n\t" \
    "s_add_u32 m0, s82, 12288\n\t" \
    "s_nop 0\n\t" \
    "global_load_lds_dwordx4 v200, s[42:43]\n\t" \
    "v_add_u32 v200, 131072, v200\n\t" \
    "ds_read_b64_tr_b16 a[152:153], v202 offset:16384\n\t" \
    "ds_read_b64_tr_b16 a[154:155], v202 offset:17408\n\t" \
    "ds_read_b64_tr_b16 a[156:157], v202 offset:24576\n\t" \
    "ds_read_b64_tr_b16 a[158:159], v202 offset:25600\n\t" \
    "ds_read_b64_tr_b16 a[224:225], v203 offset:16384\n\t" \
    "ds_read_b64_tr_b16 a[226:227], v203 offset:17408\n\t" \
    "ds_read_b64_tr_b16 a[228:229], v203 offset:24576\n\t" \
    "ds_read_b64_tr_b16 a[230:231], v203 offset:25600\n\t" \
    "s_waitcnt vmcnt(12) lgkmcnt(0)\n\t" \
    "s_barrier\n\t" \
    "s_add_u32 m0, s82, 16384\n\t" \
    "s_nop 0\n\t" \
    "global_load_lds_dwordx4 v200, s[42:43]\n\t" \
    "v_add_u32 v200, 131072, v200\n\t" \
    "s_add_u32 m0, s82, 20480\n\t" \
    "s_nop 0\n\t" \
    "global_load_lds_dwordx4 v200, s[42:43]\n\t" \
    "v_add_u32 v200, 131072, v200\n\t" \
    "s_add_u32 m0, s82, 24576\n\t" \
    "s_nop 0\n\t" \
    "global_load_lds_dwordx4 v200, s[42:43]\n\t" \
    "v_add_u32 v200, 131072, v200\n\t" \
    "s_add_u32 m0, s82, 28672\n\t" \
    "s_nop 0\n\t" \
    "global_load_lds_dwordx4 v200, s[42:43]\n\t" \
    "v_add_u32 v200, 131072, v200\n\t" \
    "s_add_u32 m0, s89, 0\n\t" \
    "s_nop 0\n\t" \
    "global_load_lds_dwordx4 v1, s[50:51] offset:0\n\t" \
    "s_add_u32 m0, s89, 960\n\t" \
    "s_nop 0\n\t" \
    "global_load_lds_dwordx4 v1, s[50:51] offset:64\n\t" \
    "s_add_u32 m0, s89, 1920\n\t" \
    "s_nop 0\n\t" \
    "global_load_lds_dwordx4 v1, s[50:51] offset:128\n\t" \
    "s_add_u32 m0, s89, 2880\n\t" \
    "s_nop 0\n\t" \
    "global_load_lds_dwordx4 v1, s[50:51] offset:192\n\t" \
    "s_add_u32 m0, s89, 3840\n\t" \
    "s_nop 0\n\t" \
    "global_load_lds_dwordx4 v1, s[50:51] offset:256\n\t" \
    "s_add_u32 m0, s89, 4800\n\t" \
    "s_nop 0\n\t" \
    "global_load_lds_dwordx4 v1, s[50:51] offset:320\n\t" \
    "s_add_u32 m0, s89, 5760\n\t" \
    "s_nop 0\n\t" \
    "global_load_lds_dwordx4 v1, s[50:51] offset:384\n\t" \
    "s_add_u32 m0, s89, 6720\n\t" \
    "s_nop 0\n\t" \
    "global_load_lds_dwordx4 v1, s[50:51] offset:448\n\t" \
    "s_add_u32 m0, s89, 7680\n\t" \
    "s_nop 0\n\t" \
    "global_load_lds_dwordx4 v1, s[50:51] offset:512\n\t" \
    "ds_read_b64_tr_b16 a[160:161], v202 offset:32768\n\t" \
    "ds_read_b64_tr_b16 a[162:163], v202 offset:33792\n\t" \
    "ds_read_b64_tr_b16 a[164:165], v202 offset:40960\n\t" \
    "ds_read_b64_tr_b16 a[166:167], v202 offset:41984\n\t" \
    "ds_read_b64_tr_b16 a[232:233], v203 offset:32768\n\t" \
    "ds_read_b64_tr_b16 a[234:235], v203 offset:33792\n\t" \
    "ds_read_b64_tr_b16 a[236:237], v203 offset:40960\n\t" \
    "ds_read_b64_tr_b16 a[238:239], v203 offset:41984\n\t" \
    "s_waitcnt vmcnt(21) lgkmcnt(0)\n\t" \
    "s_barrier\n\t" \
    "s_add_u32 m0, s82, 32768\n\t" \
    "s_nop 0\n\t" \
    "global_load_lds_dwordx4 v200, s[42:43]\n\t" \
    "v_add_u32 v200, 131072, v200\n\t" \
    "s_add_u32 m0, s82, 36864\n\t" \
    "s_nop 0\n\t" \
    "global_load_lds_dwordx4 v200, s[42:43]\n\t" \
    "v_add_u32 v200, 131072, v200\n\t" \
    "s_add_u32 m0, s82, 40960\n\t" \
    "s_nop 0\n\t" \
    "global_load_lds_dwordx4 v200, s[42:43]\n\t" \
    "v_add_u32 v200, 131072, v200\n\t" \
    "s_add_u32 m0, s82, 45056\n\t" \
    "s_nop 0\n\t" \
    "global_load_lds_dwordx4 v200, s[42:43]\n\t" \
    "v_add_u32 v200, 131072, v200\n\t" \
    "s_add_u32 s50, s50, 36864\n\t" \
    "s_addc_u32 s51, s51, 0\n\t" \
    "s_add_u32 m0, s89, 36864\n\t" \
    "s_nop 0\n\t" \
    "global_load_lds_dwordx4 v1, s[50:51] offset:0\n\t" \
    "s_add_u32 m0, s89, 37824\n\t" \
    "s_nop 0\n\t" \
    "global_load_lds_dwordx4 v1, s[50:51] offset:64\n\t" \
    "s_add_u32 m0, s89, 38784\n\t" \
    "s_nop 0\n\t" \
    "global_load_lds_dwordx4 v1, s[50:51] offset:128\n\t" \
    "s_add_u32 m0, s89, 39744\n\t" \
    "s_nop 0\n\t" \
    "global_load_lds_dwordx4 v1, s[50:51] offset:192\n\t" \
    "s_add_u32 m0, s89, 40704\n\t" \
    "s_nop 0\n\t" \
    "global_load_lds_dwordx4 v1, s[50:51] offset:256\n\t" \
    "s_add_u32 m0, s89, 41664\n\t" \
    "s_nop 0\n\t" \
    "global_load_lds_dwordx4 v1, s[50:51] offset:320\n\t" \
    "s_add_u32 m0, s89, 42624\n\t" \
    "s_nop 0\n\t" \
    "global_load_lds_dwordx4 v1, s[50:51] offset:384\n\t" \
    "s_add_u32 m0, s89, 43584\n\t" \
    "s_nop 0\n\t" \
    "global_load_lds_dwordx4 v1, s[50:51] offset:448\n\t" \
    "s_add_u32 m0, s89, 44544\n\t" \
    "s_nop 0\n\t" \
    "global_load_lds_dwordx4 v1, s[50:51] offset:512\n\t" \
    "ds_read_b64_tr_b16 a[168:169], v202 offset:49152\n\t" \
    "ds_read_b64_tr_b16 a[170:171], v202 offset:50176\n\t" \
    "ds_read_b64_tr_b16 a[172:173], v202 offset:57344\n\t" \
    "ds_read_b64_tr_b16 a[174:175], v202 offset:58368\n\t" \
    "ds_read_b64_tr_b16 a[240:241], v203 offset:49152\n\t" \
    "ds_read_b64_tr_b16 a[242:243], v203 offset:50176\n\t" \
    "ds_read_b64_tr_b16 a[244:245], v203 offset:57344\n\t" \
    "ds_read_b64_tr_b16 a[246:247], v203 offset:58368\n\t" \
    "s_waitcnt vmcnt(30) lgkmcnt(0)\n\t" \
    "s_barrier\n\t" \
    "s_add_u32 m0, s82, 49152\n\t" \
    "s_nop 0\n\t" \
    "global_load_lds_dwordx4 v200, s[42:43]\n\t" \
    "v_add_u32 v200, 131072, v200\n\t" \
    "s_add_u32 m0, s82, 53248\n\t" \
    "s_nop 0\n\t" \
    "global_load_lds_dwordx4 v200, s[42:43]\n\t" \
    "v_add_u32 v200, 131072, v200\n\t" \
    "s_add_u32 m0, s82, 57344\n\t" \
    "s_nop 0\n\t" \
    "global_load_lds_dwordx4 v200, s[42:43]\n\t" \
    "v_add_u32 v200, 131072, v200\n\t" \
    "s_add_u32 m0, s82, 61440\n\t" \
    "s_nop 0\n\t" \
    "global_load_lds_dwordx4 v200, s[42:43]\n\t" \
    "v_add_u32 v200, 131072, v200\n\t" \
    "v_add_u32 v202, 65536, v202\n\t" \
    "v_add_u32 v203, 65536, v203\n\t" \
    "ds_read_b64_tr_b16 a[176:177], v202 offset:0\n\t" \
    "ds_read_b64_tr_b16 a[178:179], v202 offset:1024\n\t" \
    "ds_read_b64_tr_b16 a[180:181], v202 offset:8192\n\t" \
    "ds_read_b64_tr_b16 a[182:183], v202 offset:9216\n\t" \
    "ds_read_b64_tr_b16 a[248:249], v203 offset:0\n\t" \
    "ds_read_b64_tr_b16 a[250:251], v203 offset:1024\n\t" \
    "ds_read_b64_tr_b16 a[252:253], v203 offset:8192\n\t" \
    "ds_read_b64_tr_b16 a[254:255], v203 offset:9216\n\t" \
    "s_waitcnt vmcnt(30) lgkmcnt(0)\n\t" \
    "s_barrier\n\t" \
    "v_subrev_u32 v200, 4718592, v200\n\t" \
    "s_add_u32 m0, s82, 65536\n\t" \
    "s_nop 0\n\t" \
    "global_load_lds_dwordx4 v200, s[44:45]\n\t" \
    "v_add_u32 v200, 131072, v200\n\t" \
    "s_add_u32 m0, s82, 69632\n\t" \
    "s_nop 0\n\t" \
    "global_load_lds_dwordx4 v200, s[44:45]\n\t" \
    "v_add_u32 v200, 131072, v200\n\t" \
    "s_add_u32 m0, s82, 73728\n\t" \
    "s_nop 0\n\t" \
    "global_load_lds_dwordx4 v200, s[44:45]\n\t" \
    "v_add_u32 v200, 131072, v200\n\t" \
    "s_add_u32 m0, s82, 77824\n\t" \
    "s_nop 0\n\t" \
    "global_load_lds_dwordx4 v200, s[44:45]\n\t" \
    "v_add_u32 v200, 131072, v200\n\t" \
    "v_add_u32 v202, -65536, v202\n\t" \
    "v_add_u32 v203, -65536, v203\n\t" \
    "ds_read_b64_tr_b16 a[184:185], v202 offset:0\n\t" \
    "ds_read_b64_tr_b16 a[186:187], v202 offset:1024\n\t" \
    "ds_read_b64_tr_b16 a[188:189], v202 offset:8192\n\t" \
    "ds_read_b64_tr_b16 a[190:191], v202 offset:9216\n\t" \
    "ds_read_b64_tr_b16 v[224:225], v203 offset:0\n\t" \
    "ds_read_b64_tr_b16 v[226:227], v203 offset:1024\n\t" \
    "ds_read_b64_tr_b16 v[228:229], v203 offset:8192\n\t" \
    "ds_read_b64_tr_b16 v[230:231], v203 offset:9216\n\t" \
    "s_waitcnt vmcnt(30) lgkmcnt(0)\n\t" \
    "s_barrier\n\t" \
    "s_add_u32 m0, s82, 0\n\t" \
    "s_nop 0\n\t" \
    "global_load_lds_dwordx4 v200, s[44:45]\n\t" \
    "v_add_u32 v200, 131072, v200\n\t" \
    "s_add_u32 m0, s82, 4096\n\t" \
    "s_nop 0\n\t" \
    "global_load_lds_dwordx4 v200, s[44:45]\n\t" \
    "v_add_u32 v200, 131072, v200\n\t" \
    "s_add_u32 m0, s82, 8192\n\t" \
    "s_nop 0\n\t" \
    "global_load_lds_dwordx4 v200, s[44:45]\n\t" \
    "v_add_u32 v200, 131072, v200\n\t" \
    "s_add_u32 m0, s82, 12288\n\t" \
    "s_nop 0\n\t" \
    "global_load_lds_dwordx4 v200, s[44:45]\n\t" \
    "v_add_u32 v200, 131072, v200\n\t" \
    "ds_read_b64_tr_b16 a[192:193], v202 offset:16384\n\t" \
    "ds_read_b64_tr_b16 a[194:195], v202 offset:17408\n\t" \
    "ds_read_b64_tr_b16 a[196:197], v202 offset:24576\n\t" \
    "ds_read_b64_tr_b16 a[198:199], v202 offset:25600\n\t" \
    "ds_read_b64_tr_b16 v[232:233], v203 offset:16384\n\t" \
    "ds_read_b64_tr_b16 v[234:235], v203 offset:17408\n\t" \
    "ds_read_b64_tr_b16 v[236:237], v203 offset:24576\n\t" \
    "ds_read_b64_tr_b16 v[238:239], v203 offset:25600\n\t" \
    "s_waitcnt vmcnt(21) lgkmcnt(0)\n\t" \
    "s_barrier\n\t" \
    "s_add_u32 m0, s82, 16384\n\t" \
    "s_nop 0\n\t" \
    "global_load_lds_dwordx4 v200, s[44:45]\n\t" \
    "v_add_u32 v200, 131072, v200\n\t" \
    "s_add_u32 m0, s82, 20480\n\t" \
    "s_nop 0\n\t" \
    "global_load_lds_dwordx4 v200, s[44:45]\n\t" \
    "v_add_u32 v200, 131072, v200\n\t" \
    "s_add_u32 m0, s82, 24576\n\t" \
    "s_nop 0\n\t" \
    "global_load_lds_dwordx4 v200, s[44:45]\n\t" \
    "v_add_u32 v200, 131072, v200\n\t" \
    "s_add_u32 m0, s82, 28672\n\t" \
    "s_nop 0\n\t" \
    "global_load_lds_dwordx4 v200, s[44:45]\n\t" \
    "v_add_u32 v200, 131072, v200\n\t" \
    "ds_read_b64_tr_b16 a[200:201], v202 offset:32768\n\t" \
    "ds_read_b64_tr_b16 a[202:203], v202 offset:33792\n\t" \
    "ds_read_b64_tr_b16 a[204:205], v202 offset:40960\n\t" \
    "ds_read_b64_tr_b16 a[206:207], v202 offset:41984\n\t" \
    "ds_read_b64_tr_b16 v[240:241], v203 offset:32768\n\t" \
    "ds_read_b64_tr_b16 v[242:243], v203 offset:33792\n\t" \
    "ds_read_b64_tr_b16 v[244:245], v203 offset:40960\n\t" \
    "ds_read_b64_tr_b16 v[246:247], v203 offset:41984\n\t" \
    "s_waitcnt vmcnt(12) lgkmcnt(0)\n\t" \
    "s_barrier\n\t" \
    "s_add_u32 m0, s82, 32768\n\t" \
    "s_nop 0\n\t" \
    "global_load_lds_dwordx4 v200, s[44:45]\n\t" \
    "v_add_u32 v200, 131072, v200\n\t" \
    "s_add_u32 m0, s82, 36864\n\t" \
    "s_nop 0\n\t" \
    "global_load_lds_dwordx4 v200, s[44:45]\n\t" \
    "v_add_u32 v200, 131072, v200\n\t" \
    "s_add_u32 m0, s82, 40960\n\t" \
    "s_nop 0\n\t" \
    "global_load_lds_dwordx4 v200, s[44:45]\n\t" \
    "v_add_u32 v200, 131072, v200\n\t" \
    "s_add_u32 m0, s82, 45056\n\t" \
    "s_nop 0\n\t" \
    "global_load_lds_dwordx4 v200, s[44:45]\n\t" \
    "v_add_u32 v200, 131072, v200\n\t" \
    "v_mbcnt_lo_u32_b32 v164, -1, 0\n\t" \
    "v_mbcnt_hi_u32_b32 v164, -1, v164\n\t" \
    "s_cmp_eq_u32 s66, 0\n\t" \
    "s_cselect_b32 s69, 59, 11\n\t" \
    "v_min_u32 v164, s69, v164\n\t" \
    "v_lshlrev_b32 v164, 6, v164\n\t" \
    "v_add_u32 v164, 6258688, v164\n\t" \
    "v_mov_b32 v165, 6266880\n\t" \
    "global_load_dword v165, v165, s[36:37] sc1\n\t" \
    "global_load_dwordx2 v[166:167], v164, s[36:37] sc1\n\t" \
    "ds_read_b64_tr_b16 a[208:209], v202 offset:49152\n\t" \
    "ds_read_b64_tr_b16 a[210:211], v202 offset:50176\n\t" \
    "ds_read_b64_tr_b16 a[212:213], v202 offset:57344\n\t" \
    "ds_read_b64_tr_b16 a[214:215], v202 offset:58368\n\t" \
    "ds_read_b64_tr_b16 v[248:249], v203 offset:49152\n\t" \
    "ds_read_b64_tr_b16 v[250:251], v203 offset:50176\n\t" \
    "ds_read_b64_tr_b16 v[252:253], v203 offset:57344\n\t" \
    "ds_read_b64_tr_b16 v[254:255], v203 offset:58368\n\t" \
    "s_waitcnt lgkmcnt(0)\n\t" \
    "s_waitcnt lgkmcnt(6)\n\t" \
    "v_add_u32 v2, 36864, v3\n\t" \
    "ds_read_b128 v[24:27], v2 offset:0\n\t" \
    "ds_read_b128 v[28:31], v2 offset:18432\n\t" \
    "ds_read_b128 v[32:35], v2 offset:1024\n\t" \
    "ds_read_b128 v[36:39], v2 offset:19456\n\t" \
    "s_waitcnt lgkmcnt(3)\n\t" \
    "v_mfma_f32_16x16x32_bf16 v[88:91], a[144:147], v[24:27], 0\n\t" \
    "ds_read_b128 v[40:43], v2 offset:2048\n\t" \
    "s_waitcnt lgkmcnt(3)\n\t" \
    "v_mfma_f32_16x16x32_bf16 v[92:95], a[144:147], v[28:31], 0\n\t" \
    "ds_read_b128 v[44:47], v2 offset:20480\n\t" \
    "v_mfma_f32_16x16x32_bf16 v[96:99], a[216:219], v[24:27], 0\n\t" \
    "v_mfma_f32_16x16x32_bf16 v[100:103], a[216:219], v[28:31], 0\n\t" \
    "s_waitcnt lgkmcnt(3)\n\t" \
    "v_mfma_f32_16x16x32_bf16 v[88:91], a[148:151], v[32:35], v[88:91]\n\t" \
    "ds_read_b128 v[48:51], v2 offset:3072\n\t" \
    "s_waitcnt lgkmcnt(3)\n\t" \
    "v_mfma_f32_16x16x32_bf16 v[92:95], a[148:151], v[36:39], v[92:95]\n\t" \
    "ds_read_b128 v[52:55], v2 offset:21504\n\t" \
    "v_mfma_f32_16x16x32_bf16 v[96:99], a[220:223], v[32:35], v[96:99]\n\t" \
    "v_mfma_f32_16x16x32_bf16 v[100:103], a[220:223], v[36:39], v[100:103]\n\t" \
    "s_waitcnt lgkmcnt(3)\n\t" \
    "v_mfma_f32_16x16x32_bf16 v[88:91], a[152:155], v[40:43], v[88:91]\n\t" \
    "ds_read_b128 v[56:59], v2 offset:4096\n\t" \
    "s_waitcnt lgkmcnt(3)\n\t" \
    "v_mfma_f32_16x16x32_bf16 v[92:95], a[152:155], v[44:47], v[92:95]\n\t" \
    "ds_read_b128 v[60:63], v2 offset:22528\n\t" \
    "v_mfma_f32_16x16x32_bf16 v[96:99], a[224:227], v[40:43], v[96:99]\n\t" \
    "v_mfma_f32_16x16x32_bf16 v[100:103], a[224:227], v[44:47], v[100:103]\n\t" \
    "s_waitcnt lgkmcnt(3)\n\t" \
    "v_mfma_f32_16x16x32_bf16 v[88:91], a[156:159], v[48:51], v[88:91]\n\t" \
    "ds_read_b128 v[64:67], v2 offset:5120\n\t" \
    "s_waitcnt lgkmcnt(3)\n\t" \
    "v_mfma_f32_16x16x32_bf16 v[92:95], a[156:159], v[52:55], v[92:95]\n\t" \
    "ds_read_b128 v[68:71], v2 offset:23552\n\t" \
    "v_mfma_f32_16x16x32_bf16 v[96:99], a[228:231], v[48:51], v[96:99]\n\t" \
    "v_mfma_f32_16x16x32_bf16 v[100:103], a[228:231], v[52:55], v[100:103]\n\t" \
    "s_waitcnt lgkmcnt(3)\n\t" \
    "v_mfma_f32_16x16x32_bf16 v[88:91], a[160:163], v[56:59], v[88:91]\n\t" \
    "ds_read_b128 v[24:27], v2 offset:6144\n\t" \
    "s_waitcnt lgkmcnt(3)\n\t" \
    "v_mfma_f32_16x16x32_bf16 v[92:95], a[160:163], v[60:63], v[92:95]\n\t" \
    "ds_read_b128 v[28:31], v2 offset:24576\n\t" \
    "v_mfma_f32_16x16x32_bf16 v[96:99], a[232:235], v[56:59], v[96:99]\n\t" \
    "v_mfma_f32_16x16x32_bf16 v[100:103], a[232:235], v[60:63], v[100:103]\n\t" \
    "s_waitcnt lgkmcnt(3)\n\t" \
    "v_mfma_f32_16x16x32_bf16 v[88:91], a[164:167], v[64:67], v[88:91]\n\t" \
    "ds_read_b128 v[32:35], v2 offset:7168\n\t" \
    "s_waitcnt lgkmcnt(3)\n\t" \
    "v_mfma_f32_16x16x32_bf16 v[92:95], a[164:167], v[68:71], v[92:95]\n\t" \
    "ds_read_b128 v[36:39], v2 offset:25600\n\t" \
    "v_mfma_f32_16x16x32_bf16 v[96:99], a[236:239], v[64:67], v[96:99]\n\t" \
    "v_mfma_f32_16x16x32_bf16 v[100:103], a[236:239], v[68:71], v[100:103]\n\t" \
    "s_waitcnt lgkmcnt(3)\n\t" \
    "v_mfma_f32_16x16x32_bf16 v[88:91], a[168:171], v[24:27], v[88:91]\n\t" \
    "ds_read_b128 v[40:43], v2 offset:8192\n\t" \
    "s_waitcnt lgkmcnt(3)\n\t" \
    "v_mfma_f32_16x16x32_bf16 v[92:95], a[168:171], v[28:31], v[92:95]\n\t" \
    "ds_read_b128 v[44:47], v2 offset:26624\n\t" \
    "v_mfma_f32_16x16x32_bf16 v[96:99], a[240:243], v[24:27], v[96:99]\n\t" \
    "v_mfma_f32_16x16x32_bf16 v[100:103], a[240:243], v[28:31], v[100:103]\n\t" \
    "s_waitcnt lgkmcnt(3)\n\t" \
    "v_mfma_f32_16x16x32_bf16 v[88:91], a[172:175], v[32:35], v[88:91]\n\t" \
    "ds_read_b128 v[48:51], v2 offset:9216\n\t" \
    "s_waitcnt lgkmcnt(3)\n\t" \
    "v_mfma_f32_16x16x32_bf16 v[92:95], a[172:175], v[36:39], v[92:95]\n\t" \
    "ds_read_b128 v[52:55], v2 offset:27648\n\t" \
    "v_mfma_f32_16x16x32_bf16 v[96:99], a[244:247], v[32:35], v[96:99]\n\t" \
    "v_mfma_f32_16x16x32_bf16 v[100:103], a[244:247], v[36:39], v[100:103]\n\t" \
    "s_waitcnt lgkmcnt(3)\n\t" \
    "v_mfma_f32_16x16x32_bf16 v[88:91], a[176:179], v[40:43], v[88:91]\n\t" \
    "ds_read_b128 v[56:59], v2 offset:10240\n\t" \
    "s_waitcnt lgkmcnt(3)\n\t" \
    "v_mfma_f32_16x16x32_bf16 v[92:95], a[176:179], v[44:47], v[92:95]\n\t" \
    "ds_read_b128 v[60:63], v2 offset:28672\n\t" \
    "v_mfma_f32_16x16x32_bf16 v[96:99], a[248:251], v[40:43], v[96:99]\n\t" \
    "v_mfma_f32_16x16x32_bf16 v[100:103], a[248:251], v[44:47], v[100:103]\n\t" \
    "s_waitcnt lgkmcnt(3)\n\t" \
    "v_mfma_f32_16x16x32_bf16 v[88:91], a[180:183], v[48:51], v[88:91]\n\t" \
    "ds_read_b128 v[64:67], v2 offset:11264\n\t" \
    "s_waitcnt lgkmcnt(3)\n\t" \
    "v_mfma_f32_16x16x32_bf16 v[92:95], a[180:183], v[52:55], v[92:95]\n\t" \
    "ds_read_b128 v[68:71], v2 offset:29696\n\t" \
    "v_mfma_f32_16x16x32_bf16 v[96:99], a[252:255], v[48:51], v[96:99]\n\t" \
    "v_mfma_f32_16x16x32_bf16 v[100:103], a[252:255], v[52:55], v[100:103]\n\t" \
    "s_waitcnt lgkmcnt(3)\n\t" \
    "v_mfma_f32_16x16x32_bf16 v[88:91], a[184:187], v[56:59], v[88:91]\n\t" \
    "ds_read_b128 v[24:27], v2 offset:12288\n\t" \
    "s_waitcnt lgkmcnt(3)\n\t" \
    "v_mfma_f32_16x16x32_bf16 v[92:95], a[184:187], v[60:63], v[92:95]\n\t" \
    "ds_read_b128 v[28:31], v2 offset:30720\n\t" \
    "v_mfma_f32_16x16x32_bf16 v[96:99], v[224:227], v[56:59], v[96:99]\n\t" \
    "v_mfma_f32_16x16x32_bf16 v[100:103], v[224:227], v[60:63], v[100:103]\n\t" \
    "s_waitcnt lgkmcnt(3)\n\t" \
    "v_mfma_f32_16x16x32_bf16 v[88:91], a[188:191], v[64:67], v[88:91]\n\t" \
    "ds_read_b128 v[32:35], v2 offset:13312\n\t" \
    "s_waitcnt lgkmcnt(3)\n\t" \
    "v_mfma_f32_16x16x32_bf16 v[92:95], a[188:191], v[68:71], v[92:95]\n\t" \
    "ds_read_b128 v[36:39], v2 offset:31744\n\t" \
    "v_mfma_f32_16x16x32_bf16 v[96:99], v[228:231], v[64:67], v[96:99]\n\t" \
    "v_mfma_f32_16x16x32_bf16 v[100:103], v[228:231], v[68:71], v[100:103]\n\t" \
    "s_waitcnt lgkmcnt(3)\n\t" \
    "v_mfma_f32_16x16x32_bf16 v[88:91], a[192:195], v[24:27], v[88:91]\n\t" \
    "ds_read_b128 v[40:43], v2 offset:14336\n\t" \
    "s_waitcnt lgkmcnt(3)\n\t" \
    "v_mfma_f32_16x16x32_bf16 v[92:95], a[192:195], v[28:31], v[92:95]\n\t" \
    "ds_read_b128 v[44:47], v2 offset:32768\n\t" \
    "v_mfma_f32_16x16x32_bf16 v[96:99], v[232:235], v[24:27], v[96:99]\n\t" \
    "v_mfma_f32_16x16x32_bf16 v[100:103], v[232:235], v[28:31], v[100:103]\n\t" \
    "s_waitcnt lgkmcnt(3)\n\t" \
    "v_mfma_f32_16x16x32_bf16 v[88:91], a[196:199], v[32:35], v[88:91]\n\t" \
    "ds_read_b128 v[48:51], v2 offset:15360\n\t" \
    "s_waitcnt lgkmcnt(3)\n\t" \
    "v_mfma_f32_16x16x32_bf16 v[92:95], a[196:199], v[36:39], v[92:95]\n\t" \
    "ds_read_b128 v[52:55], v2 offset:33792\n\t" \
    "v_mfma_f32_16x16x32_bf16 v[96:99], v[236:239], v[32:35], v[96:99]\n\t" \
    "v_mfma_f32_16x16x32_bf16 v[100:103], v[236:239], v[36:39], v[100:103]\n\t" \
    "s_waitcnt lgkmcnt(3)\n\t" \
    "v_mfma_f32_16x16x32_bf16 v[88:91], a[200:203], v[40:43], v[88:91]\n\t" \
    "ds_read_b128 v[56:59], v2 offset:16384\n\t" \
    "s_waitcnt lgkmcnt(3)\n\t" \
    "v_mfma_f32_16x16x32_bf16 v[92:95], a[200:203], v[44:47], v[92:95]\n\t" \
    "ds_read_b128 v[60:63], v2 offset:34816\n\t" \
    "v_mfma_f32_16x16x32_bf16 v[96:99], v[240:243], v[40:43], v[96:99]\n\t" \
    "v_mfma_f32_16x16x32_bf16 v[100:103], v[240:243], v[44:47], v[100:103]\n\t" \
    "s_waitcnt lgkmcnt(3)\n\t" \
    "v_mfma_f32_16x16x32_bf16 v[88:91], a[204:207], v[48:51], v[88:91]\n\t" \
    "ds_read_b128 v[64:67], v2 offset:17408\n\t" \
    "s_waitcnt lgkmcnt(3)\n\t" \
    "v_mfma_f32_16x16x32_bf16 v[92:95], a[204:207], v[52:55], v[92:95]\n\t" \
    "ds_read_b128 v[68:71], v2 offset:35840\n\t" \
    "v_mfma_f32_16x16x32_bf16 v[96:99], v[244:247], v[48:51], v[96:99]\n\t" \
    "v_mfma_f32_16x16x32_bf16 v[100:103], v[244:247], v[52:55], v[100:103]\n\t" \
    "s_waitcnt lgkmcnt(3)\n\t" \
    "v_mfma_f32_16x16x32_bf16 v[88:91], a[208:211], v[56:59], v[88:91]\n\t" \
    "s_waitcnt lgkmcnt(2)\n\t" \
    "v_mfma_f32_16x16x32_bf16 v[92:95], a[208:211], v[60:63], v[92:95]\n\t" \
    "v_mfma_f32_16x16x32_bf16 v[96:99], v[248:251], v[56:59], v[96:99]\n\t" \
    "v_mfma_f32_16x16x32_bf16 v[100:103], v[248:251], v[60:63], v[100:103]\n\t" \
    "s_waitcnt lgkmcnt(1)\n\t" \
    "v_mfma_f32_16x16x32_bf16 v[88:91], a[212:215], v[64:67], v[88:91]\n\t" \
    "s_waitcnt lgkmcnt(0)\n\t" \
    "v_mfma_f32_16x16x32_bf16 v[92:95], a[212:215], v[68:71], v[92:95]\n\t" \
    "v_mfma_f32_16x16x32_bf16 v[96:99], v[252:255], v[64:67], v[96:99]\n\t" \
    "v_mfma_f32_16x16x32_bf16 v[100:103], v[252:255], v[68:71], v[100:103]\n\t" \
    "v_xor_b32 v9, 8192, v8\n\t" \
    "s_nop 2\n\t" \
    "v_cvt_pk_bf16_f32 v112, v88, v89\n\t" \
    "v_cvt_pk_bf16_f32 v113, v90, v91\n\t" \
    "s_nop 0\n\t" \
    "v_cvt_pk_bf16_f32 v114, v96, v97\n\t" \
    "v_cvt_pk_bf16_f32 v115, v98, v99\n\t" \
    "global_store_dwordx4 v9, v[112:115], s[40:41] offset:0 sc1\n\t" \
    "v_cvt_pk_bf16_f32 v116, v92, v93\n\t" \
    "v_cvt_pk_bf16_f32 v117, v94, v95\n\t" \
    "v_cvt_pk_bf16_f32 v118, v100, v101\n\t" \
    "v_cvt_pk_bf16_f32 v119, v102, v103\n\t" \
    "global_store_dwordx4 v9, v[116:119], s[40:41] offset:1024 sc1\n\t" \
    "s_waitcnt lgkmcnt(6)\n\t" \
    "v_add_u32 v2, 0, v3\n\t" \
    "ds_read_b128 v[24:27], v2 offset:0\n\t" \
    "ds_read_b128 v[28:31], v2 offset:18432\n\t" \
    "ds_read_b128 v[32:35], v2 offset:1024\n\t" \
    "ds_read_b128 v[36:39], v2 offset:19456\n\t" \
    "s_waitcnt lgkmcnt(3)\n\t" \
    "v_mfma_f32_16x16x32_bf16 v[88:91], a[144:147], v[24:27], 0\n\t" \
    "ds_read_b128 v[40:43], v2 offset:2048\n\t" \
    "s_waitcnt lgkmcnt(3)\n\t" \
    "v_mfma_f32_16x16x32_bf16 v[92:95], a[144:147], v[28:31], 0\n\t" \
    "ds_read_b128 v[44:47], v2 offset:20480\n\t" \
    "v_mfma_f32_16x16x32_bf16 v[96:99], a[216:219], v[24:27], 0\n\t" \
    "v_mfma_f32_16x16x32_bf16 v[100:103], a[216:219], v[28:31], 0\n\t" \
    "s_waitcnt lgkmcnt(3)\n\t" \
    "v_mfma_f32_16x16x32_bf16 v[88:91], a[148:151], v[32:35], v[88:91]\n\t" \
    "ds_read_b128 v[48:51], v2 offset:3072\n\t" \
    "s_waitcnt lgkmcnt(3)\n\t" \
    "v_mfma_f32_16x16x32_bf16 v[92:95], a[148:151], v[36:39], v[92:95]\n\t" \
    "ds_read_b128 v[52:55], v2 offset:21504\n\t" \
    "v_mfma_f32_16x16x32_bf16 v[96:99], a[220:223], v[32:35], v[96:99]\n\t" \
    "v_mfma_f32_16x16x32_bf16 v[100:103], a[220:223], v[36:39], v[100:103]\n\t" \
    "s_waitcnt lgkmcnt(3)\n\t" \
    "v_mfma_f32_16x16x32_bf16 v[88:91], a[152:155], v[40:43], v[88:91]\n\t" \
    "ds_read_b128 v[56:59], v2 offset:4096\n\t" \
    "s_waitcnt lgkmcnt(3)\n\t" \
    "v_mfma_f32_16x16x32_bf16 v[92:95], a[152:155], v[44:47], v[92:95]\n\t" \
    "ds_read_b128 v[60:63], v2 offset:22528\n\t" \
    "v_mfma_f32_16x16x32_bf16 v[96:99], a[224:227], v[40:43], v[96:99]\n\t" \
    "v_mfma_f32_16x16x32_bf16 v[100:103], a[224:227], v[44:47], v[100:103]\n\t" \
    "s_waitcnt lgkmcnt(3)\n\t" \
    "v_mfma_f32_16x16x32_bf16 v[88:91], a[156:159], v[48:51], v[88:91]\n\t" \
    "ds_read_b128 v[64:67], v2 offset:5120\n\t" \
    "s_waitcnt lgkmcnt(3)\n\t" \
    "v_mfma_f32_16x16x32_bf16 v[92:95], a[156:159], v[52:55], v[92:95]\n\t" \
    "ds_read_b128 v[68:71], v2 offset:23552\n\t" \
    "v_mfma_f32_16x16x32_bf16 v[96:99], a[228:231], v[48:51], v[96:99]\n\t" \
    "v_mfma_f32_16x16x32_bf16 v[100:103], a[228:231], v[52:55], v[100:103]\n\t" \
    "s_waitcnt lgkmcnt(3)\n\t" \
    "v_mfma_f32_16x16x32_bf16 v[88:91], a[160:163], v[56:59], v[88:91]\n\t" \
    "ds_read_b128 v[24:27], v2 offset:6144\n\t" \
    "s_waitcnt lgkmcnt(3)\n\t" \
    "v_mfma_f32_16x16x32_bf16 v[92:95], a[160:163], v[60:63], v[92:95]\n\t" \
    "ds_read_b128 v[28:31], v2 offset:24576\n\t" \
    "v_mfma_f32_16x16x32_bf16 v[96:99], a[232:235], v[56:59], v[96:99]\n\t" \
    "v_mfma_f32_16x16x32_bf16 v[100:103], a[232:235], v[60:63], v[100:103]\n\t" \
    "s_waitcnt lgkmcnt(3)\n\t" \
    "v_mfma_f32_16x16x32_bf16 v[88:91], a[164:167], v[64:67], v[88:91]\n\t" \
    "ds_read_b128 v[32:35], v2 offset:7168\n\t" \
    "s_waitcnt lgkmcnt(3)\n\t" \
    "v_mfma_f32_16x16x32_bf16 v[92:95], a[164:167], v[68:71], v[92:95]\n\t" \
    "ds_read_b128 v[36:39], v2 offset:25600\n\t" \
    "v_mfma_f32_16x16x32_bf16 v[96:99], a[236:239], v[64:67], v[96:99]\n\t" \
    "v_mfma_f32_16x16x32_bf16 v[100:103], a[236:239], v[68:71], v[100:103]\n\t" \
    "s_waitcnt lgkmcnt(3)\n\t" \
    "v_mfma_f32_16x16x32_bf16 v[88:91], a[168:171], v[24:27], v[88:91]\n\t" \
    "ds_read_b128 v[40:43], v2 offset:8192\n\t" \
    "s_waitcnt lgkmcnt(3)\n\t" \
    "v_mfma_f32_16x16x32_bf16 v[92:95], a[168:171], v[28:31], v[92:95]\n\t" \
    "ds_read_b128 v[44:47], v2 offset:26624\n\t" \
    "v_mfma_f32_16x16x32_bf16 v[96:99], a[240:243], v[24:27], v[96:99]\n\t" \
    "v_mfma_f32_16x16x32_bf16 v[100:103], a[240:243], v[28:31], v[100:103]\n\t" \
    "s_waitcnt lgkmcnt(3)\n\t" \
    "v_mfma_f32_16x16x32_bf16 v[88:91], a[172:175], v[32:35], v[88:91]\n\t" \
    "ds_read_b128 v[48:51], v2 offset:9216\n\t" \
    "s_waitcnt lgkmcnt(3)\n\t" \
    "v_mfma_f32_16x16x32_bf16 v[92:95], a[172:175], v[36:39], v[92:95]\n\t" \
    "ds_read_b128 v[52:55], v2 offset:27648\n\t" \
    "v_mfma_f32_16x16x32_bf16 v[96:99], a[244:247], v[32:35], v[96:99]\n\t" \
    "v_mfma_f32_16x16x32_bf16 v[100:103], a[244:247], v[36:39], v[100:103]\n\t" \
    "s_waitcnt lgkmcnt(3)\n\t" \
    "v_mfma_f32_16x16x32_bf16 v[88:91], a[176:179], v[40:43], v[88:91]\n\t" \
    "ds_read_b128 v[56:59], v2 offset:10240\n\t" \
    "s_waitcnt lgkmcnt(3)\n\t" \
    "v_mfma_f32_16x16x32_bf16 v[92:95], a[176:179], v[44:47], v[92:95]\n\t" \
    "ds_read_b128 v[60:63], v2 offset:28672\n\t" \
    "v_mfma_f32_16x16x32_bf16 v[96:99], a[248:251], v[40:43], v[96:99]\n\t" \
    "v_mfma_f32_16x16x32_bf16 v[100:103], a[248:251], v[44:47], v[100:103]\n\t" \
    "s_waitcnt lgkmcnt(3)\n\t" \
    "v_mfma_f32_16x16x32_bf16 v[88:91], a[180:183], v[48:51], v[88:91]\n\t" \
    "ds_read_b128 v[64:67], v2 offset:11264\n\t" \
    "s_waitcnt lgkmcnt(3)\n\t" \
    "v_mfma_f32_16x16x32_bf16 v[92:95], a[180:183], v[52:55], v[92:95]\n\t" \
    "ds_read_b128 v[68:71], v2 offset:29696\n\t" \
    "v_mfma_f32_16x16x32_bf16 v[96:99], a[252:255], v[48:51], v[96:99]\n\t" \
    "v_mfma_f32_16x16x32_bf16 v[100:103], a[252:255], v[52:55], v[100:103]\n\t" \
    "s_waitcnt lgkmcnt(3)\n\t" \
    "v_mfma_f32_16x16x32_bf16 v[88:91], a[184:187], v[56:59], v[88:91]\n\t" \
    "ds_read_b128 v[24:27], v2 offset:12288\n\t" \
    "s_waitcnt lgkmcnt(3)\n\t" \
    "v_mfma_f32_16x16x32_bf16 v[92:95], a[184:187], v[60:63], v[92:95]\n\t" \
    "ds_read_b128 v[28:31], v2 offset:30720\n\t" \
    "v_mfma_f32_16x16x32_bf16 v[96:99], v[224:227], v[56:59], v[96:99]\n\t" \
    "v_mfma_f32_16x16x32_bf16 v[100:103], v[224:227], v[60:63], v[100:103]\n\t" \
    "s_waitcnt lgkmcnt(3)\n\t" \
    "v_mfma_f32_16x16x32_bf16 v[88:91], a[188:191], v[64:67], v[88:91]\n\t" \
    "ds_read_b128 v[32:35], v2 offset:13312\n\t" \
    "s_waitcnt lgkmcnt(3)\n\t" \
    "v_mfma_f32_16x16x32_bf16 v[92:95], a[188:191], v[68:71], v[92:95]\n\t" \
    "ds_read_b128 v[36:39], v2 offset:31744\n\t" \
    "v_mfma_f32_16x16x32_bf16 v[96:99], v[228:231], v[64:67], v[96:99]\n\t" \
    "v_mfma_f32_16x16x32_bf16 v[100:103], v[228:231], v[68:71], v[100:103]\n\t" \
    "s_waitcnt lgkmcnt(3)\n\t" \
    "v_mfma_f32_16x16x32_bf16 v[88:91], a[192:195], v[24:27], v[88:91]\n\t" \
    "ds_read_b128 v[40:43], v2 offset:14336\n\t" \
    "s_waitcnt lgkmcnt(3)\n\t" \
    "v_mfma_f32_16x16x32_bf16 v[92:95], a[192:195], v[28:31], v[92:95]\n\t" \
    "ds_read_b128 v[44:47], v2 offset:32768\n\t" \
    "v_mfma_f32_16x16x32_bf16 v[96:99], v[232:235], v[24:27], v[96:99]\n\t" \
    "v_mfma_f32_16x16x32_bf16 v[100:103], v[232:235], v[28:31], v[100:103]\n\t" \
    "s_waitcnt lgkmcnt(3)\n\t" \
    "v_mfma_f32_16x16x32_bf16 v[88:91], a[196:199], v[32:35], v[88:91]\n\t" \
    "ds_read_b128 v[48:51], v2 offset:15360\n\t" \
    "s_waitcnt lgkmcnt(3)\n\t" \
    "v_mfma_f32_16x16x32_bf16 v[92:95], a[196:199], v[36:39], v[92:95]\n\t" \
    "ds_read_b128 v[52:55], v2 offset:33792\n\t" \
    "v_mfma_f32_16x16x32_bf16 v[96:99], v[236:239], v[32:35], v[96:99]\n\t" \
    "v_mfma_f32_16x16x32_bf16 v[100:103], v[236:239], v[36:39], v[100:103]\n\t" \
    "s_waitcnt lgkmcnt(3)\n\t" \
    "v_mfma_f32_16x16x32_bf16 v[88:91], a[200:203], v[40:43], v[88:91]\n\t" \
    "ds_read_b128 v[56:59], v2 offset:16384\n\t" \
    "s_waitcnt lgkmcnt(3)\n\t" \
    "v_mfma_f32_16x16x32_bf16 v[92:95], a[200:203], v[44:47], v[92:95]\n\t" \
    "ds_read_b128 v[60:63], v2 offset:34816\n\t" \
    "v_mfma_f32_16x16x32_bf16 v[96:99], v[240:243], v[40:43], v[96:99]\n\t" \
    "v_mfma_f32_16x16x32_bf16 v[100:103], v[240:243], v[44:47], v[100:103]\n\t" \
    "s_waitcnt lgkmcnt(3)\n\t" \
    "v_mfma_f32_16x16x32_bf16 v[88:91], a[204:207], v[48:51], v[88:91]\n\t" \
    "ds_read_b128 v[64:67], v2 offset:17408\n\t" \
    "s_waitcnt lgkmcnt(3)\n\t" \
    "v_mfma_f32_16x16x32_bf16 v[92:95], a[204:207], v[52:55], v[92:95]\n\t" \
    "ds_read_b128 v[68:71], v2 offset:35840\n\t" \
    "v_mfma_f32_16x16x32_bf16 v[96:99], v[244:247], v[48:51], v[96:99]\n\t" \
    "v_mfma_f32_16x16x32_bf16 v[100:103], v[244:247], v[52:55], v[100:103]\n\t" \
    "s_waitcnt lgkmcnt(3)\n\t" \
    "v_mfma_f32_16x16x32_bf16 v[88:91], a[208:211], v[56:59], v[88:91]\n\t" \
    "s_waitcnt lgkmcnt(2)\n\t" \
    "v_mfma_f32_16x16x32_bf16 v[92:95], a[208:211], v[60:63], v[92:95]\n\t" \
    "v_mfma_f32_16x16x32_bf16 v[96:99], v[248:251], v[56:59], v[96:99]\n\t" \
    "v_mfma_f32_16x16x32_bf16 v[100:103], v[248:251], v[60:63], v[100:103]\n\t" \
    "s_waitcnt lgkmcnt(1)\n\t" \
    "v_mfma_f32_16x16x32_bf16 v[88:91], a[212:215], v[64:67], v[88:91]\n\t" \
    "s_waitcnt lgkmcnt(0)\n\t" \
    "v_mfma_f32_16x16x32_bf16 v[92:95], a[212:215], v[68:71], v[92:95]\n\t" \
    "v_mfma_f32_16x16x32_bf16 v[96:99], v[252:255], v[64:67], v[96:99]\n\t" \
    "v_mfma_f32_16x16x32_bf16 v[100:103], v[252:255], v[68:71], v[100:103]\n\t" \
    "v_xor_b32 v9, 0, v8\n\t" \
    "s_nop 2\n\t" \
    "v_cvt_pk_bf16_f32 v112, v88, v89\n\t" \
    "v_cvt_pk_bf16_f32 v113, v90, v91\n\t" \
    "s_nop 0\n\t" \
    "v_cvt_pk_bf16_f32 v114, v96, v97\n\t" \
    "v_cvt_pk_bf16_f32 v115, v98, v99\n\t" \
    "global_store_dwordx4 v9, v[112:115], s[40:41] offset:0 sc1\n\t" \
    "v_cvt_pk_bf16_f32 v116, v92, v93\n\t" \
    "v_cvt_pk_bf16_f32 v117, v94, v95\n\t" \
    "v_cvt_pk_bf16_f32 v118, v100, v101\n\t" \
    "v_cvt_pk_bf16_f32 v119, v102, v103\n\t" \
    "global_store_dwordx4 v9, v[116:119], s[40:41] offset:1024 sc1\n\t" \
    "s_waitcnt vmcnt(18) lgkmcnt(0)\n\t" \
    "s_barrier\n\t" \
    "s_add_u32 m0, s82, 49152\n\t" \
    "s_nop 0\n\t" \
    "global_load_lds_dwordx4 v200, s[44:45]\n\t" \
    "v_add_u32 v200, 131072, v200\n\t" \
    "s_add_u32 m0, s82, 53248\n\t" \
    "s_nop 0\n\t" \
    "global_load_lds_dwordx4 v200, s[44:45]\n\t" \
    "v_add_u32 v200, 131072, v200\n\t" \
    "s_add_u32 m0, s82, 57344\n\t" \
    "s_nop 0\n\t" \
    "global_load_lds_dwordx4 v200, s[44:45]\n\t" \
    "v_add_u32 v200, 131072, v200\n\t" \
    "s_add_u32 m0, s82, 61440\n\t" \
    "s_nop 0\n\t" \
    "global_load_lds_dwordx4 v200, s[44:45]\n\t" \
    "v_add_u32 v200, 131072, v200\n\t" \
    "v_add_u32 v202, 65536, v202\n\t" \
    "v_add_u32 v203, 65536, v203\n\t" \
    "ds_read_b64_tr_b16 a[0:1], v202 offset:0\n\t" \
    "ds_read_b64_tr_b16 a[2:3], v202 offset:1024\n\t" \
    "ds_read_b64_tr_b16 a[4:5], v202 offset:8192\n\t" \
    "ds_read_b64_tr_b16 a[6:7], v202 offset:9216\n\t" \
    "ds_read_b64_tr_b16 a[72:73], v203 offset:0\n\t" \
    "ds_read_b64_tr_b16 a[74:75], v203 offset:1024\n\t" \
    "ds_read_b64_tr_b16 a[76:77], v203 offset:8192\n\t" \
    "ds_read_b64_tr_b16 a[78:79], v203 offset:9216\n\t" \
    "s_waitcnt vmcnt(18) lgkmcnt(0)\n\t" \
    "s_barrier\n\t" \
    "s_add_u32 m0, s82, 65536\n\t" \
    "s_nop 0\n\t" \
    "global_load_lds_dwordx4 v200, s[44:45]\n\t" \
    "v_add_u32 v200, 131072, v200\n\t" \
    "s_add_u32 m0, s82, 69632\n\t" \
    "s_nop 0\n\t" \
    "global_load_lds_dwordx4 v200, s[44:45]\n\t" \
    "v_add_u32 v200, 131072, v200\n\t" \
    "s_add_u32 m0, s82, 73728\n\t" \
    "s_nop 0\n\t" \
    "global_load_lds_dwordx4 v200, s[44:45]\n\t" \
    "v_add_u32 v200, 131072, v200\n\t" \
    "s_add_u32 m0, s82, 77824\n\t" \
    "s_nop 0\n\t" \
    "global_load_lds_dwordx4 v200, s[44:45]\n\t" \
    "v_add_u32 v200, 131072, v200\n\t" \
    "v_add_u32 v202, -65536, v202\n\t" \
    "v_add_u32 v203, -65536, v203\n\t" \
    "ds_read_b64_tr_b16 a[8:9], v202 offset:0\n\t" \
    "ds_read_b64_tr_b16 a[10:11], v202 offset:1024\n\t" \
    "ds_read_b64_tr_b16 a[12:13], v202 offset:8192\n\t" \
    "ds_read_b64_tr_b16 a[14:15], v202 offset:9216\n\t" \
    "ds_read_b64_tr_b16 a[80:81], v203 offset:0\n\t" \
    "ds_read_b64_tr_b16 a[82:83], v203 offset:1024\n\t" \
    "ds_read_b64_tr_b16 a[84:85], v203 offset:8192\n\t" \
    "ds_read_b64_tr_b16 a[86:87], v203 offset:9216\n\t" \
    "s_waitcnt vmcnt(18) lgkmcnt(0)\n\t" \
    "s_barrier\n\t" \
    "s_add_u32 m0, s82, 0\n\t" \
    "s_nop 0\n\t" \
    "global_load_lds_dwordx4 v200, s[44:45]\n\t" \
    "v_add_u32 v200, 131072, v200\n\t" \
    "s_add_u32 m0, s82, 4096\n\t" \
    "s_nop 0\n\t" \
    "global_load_lds_dwordx4 v200, s[44:45]\n\t" \
    "v_add_u32 v200, 131072, v200\n\t" \
    "s_add_u32 m0, s82, 8192\n\t" \
    "s_nop 0\n\t" \
    "global_load_lds_dwordx4 v200, s[44:45]\n\t" \
    "v_add_u32 v200, 131072, v200\n\t" \
    "s_add_u32 m0, s82, 12288\n\t" \
    "s_nop 0\n\t" \
    "global_load_lds_dwordx4 v200, s[44:45]\n\t" \
    "v_add_u32 v200, 131072, v200\n\t" \
    "ds_read_b64_tr_b16 a[16:17], v202 offset:16384\n\t" \
    "ds_read_b64_tr_b16 a[18:19], v202 offset:17408\n\t" \
    "ds_read_b64_tr_b16 a[20:21], v202 offset:24576\n\t" \
    "ds_read_b64_tr_b16 a[22:23], v202 offset:25600\n\t" \
    "ds_read_b64_tr_b16 a[88:89], v203 offset:16384\n\t" \
    "ds_read_b64_tr_b16 a[90:91], v203 offset:17408\n\t" \
    "ds_read_b64_tr_b16 a[92:93], v203 offset:24576\n\t" \
    "ds_read_b64_tr_b16 a[94:95], v203 offset:25600\n\t" \
    "s_waitcnt vmcnt(18) lgkmcnt(0)\n\t" \
    "s_barrier\n\t" \
    "s_add_u32 m0, s82, 16384\n\t" \
    "s_nop 0\n\t" \
    "global_load_lds_dwordx4 v200, s[44:45]\n\t" \
    "v_add_u32 v200, 131072, v200\n\t" \
    "s_add_u32 m0, s82, 20480\n\t" \
    "s_nop 0\n\t" \
    "global_load_lds_dwordx4 v200, s[44:45]\n\t" \
    "v_add_u32 v200, 131072, v200\n\t" \
    "s_add_u32 m0, s82, 24576\n\t" \
    "s_nop 0\n\t" \
    "global_load_lds_dwordx4 v200, s[44:45]\n\t" \
    "v_add_u32 v200, 131072, v200\n\t" \
    "s_add_u32 m0, s82, 28672\n\t" \
    "s_nop 0\n\t" \
    "global_load_lds_dwordx4 v200, s[44:45]\n\t" \
    "v_add_u32 v200, 131072, v200\n\t" \
    "ds_read_b64_tr_b16 a[24:25], v202 offset:32768\n\t" \
    "ds_read_b64_tr_b16 a[26:27], v202 offset:33792\n\t" \
    "ds_read_b64_tr_b16 a[28:29], v202 offset:40960\n\t" \
    "ds_read_b64_tr_b16 a[30:31], v202 offset:41984\n\t" \
    "ds_read_b64_tr_b16 a[96:97], v203 offset:32768\n\t" \
    "ds_read_b64_tr_b16 a[98:99], v203 offset:33792\n\t" \
    "ds_read_b64_tr_b16 a[100:101], v203 offset:40960\n\t" \
    "ds_read_b64_tr_b16 a[102:103], v203 offset:41984\n\t" \
    "s_waitcnt vmcnt(12) lgkmcnt(0)\n\t" \
    "s_barrier\n\t" \
    "s_add_u32 m0, s82, 32768\n\t" \
    "s_nop 0\n\t" \
    "global_load_lds_dwordx4 v200, s[44:45]\n\t" \
    "v_add_u32 v200, 131072, v200\n\t" \
    "s_add_u32 m0, s82, 36864\n\t" \
    "s_nop 0\n\t" \
    "global_load_lds_dwordx4 v200, s[44:45]\n\t" \
    "v_add_u32 v200, 131072, v200\n\t" \
    "s_add_u32 m0, s82, 40960\n\t" \
    "s_nop 0\n\t" \
    "global_load_lds_dwordx4 v200, s[44:45]\n\t" \
    "v_add_u32 v200, 131072, v200\n\t" \
    "s_add_u32 m0, s82, 45056\n\t" \
    "s_nop 0\n\t" \
    "global_load_lds_dwordx4 v200, s[44:45]\n\t" \
    "v_add_u32 v200, 131072, v200\n\t" \
    "v_sub_u32 v166, v166, v165\n\t" \
    "v_cmp_ne_u32 vcc, 1, v166\n\t" \
    "s_cbranch_vccz gx_Cok2_%=\n\t" \
    "gx_C1_%=:\n\t" \
    "global_load_dwordx2 v[166:167], v164, s[36:37] sc1\n\t" \
    "s_waitcnt vmcnt(0)\n\t" \
    "v_sub_u32 v166, v166, v165\n\t" \
    "v_cmp_ne_u32 vcc, 1, v166\n\t" \
    "s_cbranch_vccz gx_Cok2_%=\n\t" \
    "s_cmp_eq_u32 s68, 0\n\t" \
    "s_cbranch_scc1 gx_F3_%=\n\t" \
    "s_sub_u32 s68, s68, 1\n\t" \
    "s_sleep 4\n\t" \
    "s_branch gx_C1_%=\n\t" \
    "gx_F3_%=:\n\t" \
    "v_mov_b32 v168, 6095104\n\t" \
    "v_mov_b32 v169, 4\n\t" \
    "global_store_dword v168, v169, s[36:37] sc1\n\t" \
    "s_waitcnt vmcnt(0)\n\t" \
    "s_branch gx_Cok2_%=\n\t" \
    "gx_Cok2_%=:\n\t" \
    "s_cmp_lg_u32 s66, 0\n\t" \
    "s_cbranch_scc1 gx_Ca4_%=\n\t" \
    "v_mov_b32 v164, 6176768\n\t" \
    "s_mov_b64 exec, 1\n\t" \
    "global_atomic_add v164, v166, s[36:37] sc1\n\t" \
    "s_mov_b64 exec, -1\n\t" \
    "gx_Ca4_%=:\n\t" \
    "v_min_u32_dpp v167, v167, v167 row_shl:6 row_mask:0xf bank_mask:0xf\n\t" \
    "s_nop 0\n\t" \
    "v_readlane_b32 s69, v167, 4\n\t" \
    "v_readlane_b32 s70, v167, 5\n\t" \
    "s_nop 1\n\t" \
    "s_cmp_lt_u32 s70, s69\n\t" \
    "s_cselect_b32 s52, 8192, 0\n\t" \
    "s_xor_b32 s52, s52, 8192\n\t" \
    "s_cmp_lg_u32 s66, 0\n\t" \
    "s_cbranch_scc1 gx_SF5_%=\n\t" \
    "s_lshl_b32 s69, s67, 7\n\t" \
    "s_add_u32 s69, s69, 6160384\n\t" \
    "s_add_u32 s69, s69, s52\n\t" \
    "s_add_u32 s70, s65, 1\n\t" \
    "v_mov_b32 v156, s69\n\t" \
    "v_mov_b32 v157, s70\n\t" \
    "s_mov_b64 exec, 1\n\t" \
    "global_store_dword v156, v157, s[36:37] sc1\n\t" \
    "s_mov_b64 exec, -1\n\t" \
    "gx_SF5_%=:\n\t" \
    "v_add_u32 v10, s52, v10\n\t" \
    "ds_read_b64_tr_b16 a[32:33], v202 offset:49152\n\t" \
    "ds_read_b64_tr_b16 a[34:35], v202 offset:50176\n\t" \
    "ds_read_b64_tr_b16 a[36:37], v202 offset:57344\n\t" \
    "ds_read_b64_tr_b16 a[38:39], v202 offset:58368\n\t" \
    "ds_read_b64_tr_b16 a[104:105], v203 offset:49152\n\t" \
    "ds_read_b64_tr_b16 a[106:107], v203 offset:50176\n\t" \
    "ds_read_b64_tr_b16 a[108:109], v203 offset:57344\n\t" \
    "ds_read_b64_tr_b16 a[110:111], v203 offset:58368\n\t" \
    "s_waitcnt vmcnt(12) lgkmcnt(0)\n\t" \
    "s_barrier\n\t" \
    "v_add_u32 v202, 65536, v202\n\t" \
    "v_add_u32 v203, 65536, v203\n\t" \
    "ds_read_b64_tr_b16 a[40:41], v202 offset:0\n\t" \
    "ds_read_b64_tr_b16 a[42:43], v202 offset:1024\n\t" \
    "ds_read_b64_tr_b16 a[44:45], v202 offset:8192\n\t" \
    "ds_read_b64_tr_b16 a[46:47], v202 offset:9216\n\t" \
    "ds_read_b64_tr_b16 a[112:113], v203 offset:0\n\t" \
    "ds_read_b64_tr_b16 a[114:115], v203 offset:1024\n\t" \
    "ds_read_b64_tr_b16 a[116:117], v203 offset:8192\n\t" \
    "ds_read_b64_tr_b16 a[118:119], v203 offset:9216\n\t" \
    "s_waitcnt vmcnt(8) lgkmcnt(0)\n\t" \
    "s_barrier\n\t" \
    "v_add_u32 v202, -65536, v202\n\t" \
    "v_add_u32 v203, -65536, v203\n\t" \
    "ds_read_b64_tr_b16 a[48:49], v202 offset:0\n\t" \
    "ds_read_b64_tr_b16 a[50:51], v202 offset:1024\n\t" \
    "ds_read_b64_tr_b16 a[52:53], v202 offset:8192\n\t" \
    "ds_read_b64_tr_b16 a[54:55], v202 offset:9216\n\t" \
    "ds_read_b64_tr_b16 a[120:121], v203 offset:0\n\t" \
    "ds_read_b64_tr_b16 a[122:123], v203 offset:1024\n\t" \
    "ds_read_b64_tr_b16 a[124:125], v203 offset:8192\n\t" \
    "ds_read_b64_tr_b16 a[126:127], v203 offset:9216\n\t" \
    "s_waitcnt vmcnt(4) lgkmcnt(0)\n\t" \
    "s_barrier\n\t" \
    "ds_read_b64_tr_b16 a[56:57], v202 offset:16384\n\t" \
    "ds_read_b64_tr_b16 a[58:59], v202 offset:17408\n\t" \
    "ds_read_b64_tr_b16 a[60:61], v202 offset:24576\n\t" \
    "ds_read_b64_tr_b16 a[62:63], v202 offset:25600\n\t" \
    "ds_read_b64_tr_b16 a[128:129], v203 offset:16384\n\t" \
    "ds_read_b64_tr_b16 a[130:131], v203 offset:17408\n\t" \
    "ds_read_b64_tr_b16 a[132:133], v203 offset:24576\n\t" \
    "ds_read_b64_tr_b16 a[134:135], v203 offset:25600\n\t" \
    "s_waitcnt vmcnt(0) lgkmcnt(0)\n\t" \
    "s_barrier\n\t" \
    "ds_read_b64_tr_b16 a[64:65], v202 offset:32768\n\t" \
    "ds_read_b64_tr_b16 a[66:67], v202 offset:33792\n\t" \
    "ds_read_b64_tr_b16 a[68:69], v202 offset:40960\n\t" \
    "ds_read_b64_tr_b16 a[70:71], v202 offset:41984\n\t" \
    "ds_read_b64_tr_b16 a[136:137], v203 offset:32768\n\t" \
    "ds_read_b64_tr_b16 a[138:139], v203 offset:33792\n\t" \
    "ds_read_b64_tr_b16 a[140:141], v203 offset:40960\n\t" \
    "ds_read_b64_tr_b16 a[142:143], v203 offset:41984\n\t" \
    "s_waitcnt lgkmcnt(0)\n\t" \
    "s_barrier\n\t" \
    "ds_read_b128 v[24:27], v2 offset:0\n\t" \
    "ds_read_b128 v[28:31], v2 offset:18432\n\t" \
    "ds_read_b128 v[32:35], v2 offset:1024\n\t" \
    "ds_read_b128 v[36:39], v2 offset:19456\n\t" \
    "ds_read_b128 v[40:43], v2 offset:2048\n\t" \
    "ds_read_b128 v[44:47], v2 offset:20480\n\t" \
    "s_cmp_lt_u32 s65, 62\n\t" \
    "s_cselect_b32 s69, 36864, 0\n\t" \
    "s_add_u32 s50, s50, s69\n\t" \
    "s_addc_u32 s51, s51, 0\n\t" \
    "s_add_u32 s90, s90, 36864\n\t" \
    "s_cmp_eq_u32 s90, 147456\n\t" \
    "s_cselect_b32 s90, 0, s90\n\t" \
    "s_add_u32 s91, s90, 36864\n\t" \
    "s_cmp_eq_u32 s91, 147456\n\t" \
    "s_cselect_b32 s91, 0, s91\n\t" \
    "s_add_u32 s91, s91, s89\n\t" \
    "v_add_u32 v17, s90, v3\n\t" \
    "s_branch gx_mid8_%=\n\t" \
    ".p2align 6\n\t" \
    "s_nop 0\n\t" \
    "s_nop 0\n\t" \
    "gx_loop6_%=:\n\t" \
    "s_waitcnt lgkmcnt(5)\n\t" \
    "v_mfma_f32_16x16x32_bf16 v[88:91], a[144:147], v[24:27], 0\n\t" \
    "ds_read_b128 v[48:51], v2 offset:3072\n\t" \
    "s_waitcnt lgkmcnt(5)\n\t" \
    "v_mfma_f32_16x16x32_bf16 v[92:95], a[144:147], v[28:31], 0\n\t" \
    "ds_read_b128 v[52:55], v2 offset:21504\n\t" \
    "v_mfma_f32_16x16x32_bf16 v[96:99], a[216:219], v[24:27], 0\n\t" \
    "s_sub_u32 s70, s65, 1\n\t" \
    "v_max_i32 v157, s70, v11\n\t" \
    "s_cmp_lt_u32 s65, 62\n\t" \
    "v_mfma_f32_16x16x32_bf16 v[100:103], a[216:219], v[28:31], 0\n\t" \
    "s_cselect_b32 s69, 36864, 0\n\t" \
    "s_add_u32 s50, s50, s69\n\t" \
    "s_addc_u32 s51, s51, 0\n\t" \
    "s_waitcnt lgkmcnt(5)\n\t" \
    "v_mfma_f32_16x16x32_bf16 v[88:91], a[148:151], v[32:35], v[88:91]\n\t" \
    "ds_read_b128 v[56:59], v2 offset:4096\n\t" \
    "s_waitcnt lgkmcnt(5)\n\t" \
    "v_mfma_f32_16x16x32_bf16 v[92:95], a[148:151], v[36:39], v[92:95]\n\t" \
    "ds_read_b128 v[60:63], v2 offset:22528\n\t" \
    "v_mfma_f32_16x16x32_bf16 v[96:99], a[220:223], v[32:35], v[96:99]\n\t" \
    "s_add_u32 s90, s90, 36864\n\t" \
    "s_cmp_eq_u32 s90, 147456\n\t" \
    "s_cselect_b32 s90, 0, s90\n\t" \
    "v_mfma_f32_16x16x32_bf16 v[100:103], a[220:223], v[36:39], v[100:103]\n\t" \
    "s_add_u32 s91, s90, 36864\n\t" \
    "s_cmp_eq_u32 s91, 147456\n\t" \
    "s_cselect_b32 s91, 0, s91\n\t" \
    "s_waitcnt lgkmcnt(5)\n\t" \
    "v_mfma_f32_16x16x32_bf16 v[88:91], a[152:155], v[40:43], v[88:91]\n\t" \
    "ds_read_b128 v[64:67], v2 offset:5120\n\t" \
    "s_waitcnt lgkmcnt(5)\n\t" \
    "v_mfma_f32_16x16x32_bf16 v[92:95], a[152:155], v[44:47], v[92:95]\n\t" \
    "ds_read_b128 v[68:71], v2 offset:23552\n\t" \
    "v_mfma_f32_16x16x32_bf16 v[96:99], a[224:227], v[40:43], v[96:99]\n\t" \
    "s_add_u32 s91, s91, s89\n\t" \
    "v_add_u32 v17, s90, v3\n\t" \
    "v_mfma_f32_16x16x32_bf16 v[100:103], a[224:227], v[44:47], v[100:103]\n\t" \
    "s_waitcnt lgkmcnt(5)\n\t" \
    "v_mfma_f32_16x16x32_bf16 v[88:91], a[156:159], v[48:51], v[88:91]\n\t" \
    "ds_read_b128 v[24:27], v2 offset:6144\n\t" \
    "s_waitcnt lgkmcnt(5)\n\t" \
    "v_mfma_f32_16x16x32_bf16 v[92:95], a[156:159], v[52:55], v[92:95]\n\t" \
    "ds_read_b128 v[28:31], v2 offset:24576\n\t" \
    "v_mfma_f32_16x16x32_bf16 v[96:99], a[228:231], v[48:51], v[96:99]\n\t" \
    "s_waitcnt vmcnt(0)\n\t" \
    "s_barrier\n\t" \
    "v_mfma_f32_16x16x32_bf16 v[100:103], a[228:231], v[52:55], v[100:103]\n\t" \
    "s_mov_b64 exec, s[58:59]\n\t" \
    "global_store_dword v10, v157, s[36:37] sc1\n\t" \
    "s_mov_b64 exec, -1\n\t" \
    "s_waitcnt lgkmcnt(5)\n\t" \
    "v_mfma_f32_16x16x32_bf16 v[88:91], a[160:163], v[56:59], v[88:91]\n\t" \
    "ds_read_b128 v[32:35], v2 offset:7168\n\t" \
    "s_waitcnt lgkmcnt(5)\n\t" \
    "v_mfma_f32_16x16x32_bf16 v[92:95], a[160:163], v[60:63], v[92:95]\n\t" \
    "ds_read_b128 v[36:39], v2 offset:25600\n\t" \
    "v_mfma_f32_16x16x32_bf16 v[96:99], a[232:235], v[56:59], v[96:99]\n\t" \
    "v_cvt_pk_bf16_f32 v104, v72, v73\n\t" \
    "v_mfma_f32_16x16x32_bf16 v[100:103], a[232:235], v[60:63], v[100:103]\n\t" \
    "v_cvt_pk_bf16_f32 v105, v74, v75\n\t" \
    "s_waitcnt lgkmcnt(5)\n\t" \
    "v_mfma_f32_16x16x32_bf16 v[88:91], a[164:167], v[64:67], v[88:91]\n\t" \
    "ds_read_b128 v[40:43], v2 offset:8192\n\t" \
    "s_waitcnt lgkmcnt(5)\n\t" \
    "v_mfma_f32_16x16x32_bf16 v[92:95], a[164:167], v[68:71], v[92:95]\n\t" \
    "ds_read_b128 v[44:47], v2 offset:26624\n\t" \
    "v_mfma_f32_16x16x32_bf16 v[96:99], a[236:239], v[64:67], v[96:99]\n\t" \
    "v_cvt_pk_bf16_f32 v106, v80, v81\n\t" \
    "v_mfma_f32_16x16x32_bf16 v[100:103], a[236:239], v[68:71], v[100:103]\n\t" \
    "v_cvt_pk_bf16_f32 v107, v82, v83\n\t" \
    "s_waitcnt lgkmcnt(5)\n\t" \
    "v_mfma_f32_16x16x32_bf16 v[88:91], a[168:171], v[24:27], v[88:91]\n\t" \
    "ds_read_b128 v[48:51], v2 offset:9216\n\t" \
    "s_waitcnt lgkmcnt(5)\n\t" \
    "v_mfma_f32_16x16x32_bf16 v[92:95], a[168:171], v[28:31], v[92:95]\n\t" \
    "ds_read_b128 v[52:55], v2 offset:27648\n\t" \
    "v_mfma_f32_16x16x32_bf16 v[96:99], a[240:243], v[24:27], v[96:99]\n\t" \
    "global_store_dwordx4 v9, v[104:107], s[38:39] offset:0 sc1\n\t" \
    "v_mfma_f32_16x16x32_bf16 v[100:103], a[240:243], v[28:31], v[100:103]\n\t" \
    "v_cvt_pk_bf16_f32 v108, v76, v77\n\t" \
    "s_waitcnt lgkmcnt(5)\n\t" \
    "v_mfma_f32_16x16x32_bf16 v[88:91], a[172:175], v[32:35], v[88:91]\n\t" \
    "ds_read_b128 v[56:59], v2 offset:10240\n\t" \
    "s_waitcnt lgkmcnt(5)\n\t" \
    "v_mfma_f32_16x16x32_bf16 v[92:95], a[172:175], v[36:39], v[92:95]\n\t" \
    "ds_read_b128 v[60:63], v2 offset:28672\n\t" \
    "v_mfma_f32_16x16x32_bf16 v[96:99], a[244:247], v[32:35], v[96:99]\n\t" \
    "v_cvt_pk_bf16_f32 v109, v78, v79\n\t" \
    "v_mfma_f32_16x16x32_bf16 v[100:103], a[244:247], v[36:39], v[100:103]\n\t" \
    "v_cvt_pk_bf16_f32 v110, v84, v85\n\t" \
    "s_waitcnt lgkmcnt(5)\n\t" \
    "v_mfma_f32_16x16x32_bf16 v[88:91], a[176:179], v[40:43], v[88:91]\n\t" \
    "ds_read_b128 v[64:67], v2 offset:11264\n\t" \
    "s_waitcnt lgkmcnt(5)\n\t" \
    "v_mfma_f32_16x16x32_bf16 v[92:95], a[176:179], v[44:47], v[92:95]\n\t" \
    "ds_read_b128 v[68:71], v2 offset:29696\n\t" \
    "v_mfma_f32_16x16x32_bf16 v[96:99], a[248:251], v[40:43], v[96:99]\n\t" \
    "v_cvt_pk_bf16_f32 v111, v86, v87\n\t" \
    "v_mfma_f32_16x16x32_bf16 v[100:103], a[248:251], v[44:47], v[100:103]\n\t" \
    "global_store_dwordx4 v9, v[108:111], s[38:39] offset:1024 sc1\n\t" \
    "s_waitcnt lgkmcnt(5)\n\t" \
    "v_mfma_f32_16x16x32_bf16 v[88:91], a[180:183], v[48:51], v[88:91]\n\t" \
    "ds_read_b128 v[24:27], v2 offset:12288\n\t" \
    "s_waitcnt lgkmcnt(5)\n\t" \
    "v_mfma_f32_16x16x32_bf16 v[92:95], a[180:183], v[52:55], v[92:95]\n\t" \
    "ds_read_b128 v[28:31], v2 offset:30720\n\t" \
    "v_mfma_f32_16x16x32_bf16 v[96:99], a[252:255], v[48:51], v[96:99]\n\t" \
    "s_add_u32 m0, s91, 0\n\t" \
    "v_mfma_f32_16x16x32_bf16 v[100:103], a[252:255], v[52:55], v[100:103]\n\t" \
    "global_load_lds_dwordx4 v1, s[50:51] offset:0\n\t" \
    "s_waitcnt lgkmcnt(5)\n\t" \
    "v_mfma_f32_16x16x32_bf16 v[88:91], a[184:187], v[56:59], v[88:91]\n\t" \
    "ds_read_b128 v[32:35], v2 offset:13312\n\t" \
    "s_waitcnt lgkmcnt(5)\n\t" \
    "v_mfma_f32_16x16x32_bf16 v[92:95], a[184:187], v[60:63], v[92:95]\n\t" \
    "ds_read_b128 v[36:39], v2 offset:31744\n\t" \
    "v_mfma_f32_16x16x32_bf16 v[96:99], v[224:227], v[56:59], v[96:99]\n\t" \
    "s_add_u32 m0, s91, 960\n\t" \
    "v_mfma_f32_16x16x32_bf16 v[100:103], v[224:227], v[60:63], v[100:103]\n\t" \
    "global_load_lds_dwordx4 v1, s[50:51] offset:64\n\t" \
    "s_waitcnt lgkmcnt(5)\n\t" \
    "v_mfma_f32_16x16x32_bf16 v[88:91], a[188:191], v[64:67], v[88:91]\n\t" \
    "ds_read_b128 v[40:43], v2 offset:14336\n\t" \
    "s_waitcnt lgkmcnt(5)\n\t" \
    "v_mfma_f32_16x16x32_bf16 v[92:95], a[188:191], v[68:71], v[92:95]\n\t" \
    "ds_read_b128 v[44:47], v2 offset:32768\n\t" \
    "v_mfma_f32_16x16x32_bf16 v[96:99], v[228:231], v[64:67], v[96:99]\n\t" \
    "s_add_u32 m0, s91, 1920\n\t" \
    "v_mfma_f32_16x16x32_bf16 v[100:103], v[228:231], v[68:71], v[100:103]\n\t" \
    "global_load_lds_dwordx4 v1, s[50:51] offset:128\n\t" \
    "s_waitcnt lgkmcnt(5)\n\t" \
    "v_mfma_f32_16x16x32_bf16 v[88:91], a[192:195], v[24:27], v[88:91]\n\t" \
    "ds_read_b128 v[48:51], v2 offset:15360\n\t" \
    "s_waitcnt lgkmcnt(5)\n\t" \
    "v_mfma_f32_16x16x32_bf16 v[92:95], a[192:195], v[28:31], v[92:95]\n\t" \
    "ds_read_b128 v[52:55], v2 offset:33792\n\t" \
    "v_mfma_f32_16x16x32_bf16 v[96:99], v[232:235], v[24:27], v[96:99]\n\t" \
    "s_add_u32 m0, s91, 2880\n\t" \
    "v_mfma_f32_16x16x32_bf16 v[100:103], v[232:235], v[28:31], v[100:103]\n\t" \
    "global_load_lds_dwordx4 v1, s[50:51] offset:192\n\t" \
    "s_waitcnt lgkmcnt(5)\n\t" \
    "v_mfma_f32_16x16x32_bf16 v[88:91], a[196:199], v[32:35], v[88:91]\n\t" \
    "ds_read_b128 v[56:59], v2 offset:16384\n\t" \
    "s_waitcnt lgkmcnt(5)\n\t" \
    "v_mfma_f32_16x16x32_bf16 v[92:95], a[196:199], v[36:39], v[92:95]\n\t" \
    "ds_read_b128 v[60:63], v2 offset:34816\n\t" \
    "v_mfma_f32_16x16x32_bf16 v[96:99], v[236:239], v[32:35], v[96:99]\n\t" \
    "s_add_u32 m0, s91, 3840\n\t" \
    "v_mfma_f32_16x16x32_bf16 v[100:103], v[236:239], v[36:39], v[100:103]\n\t" \
    "global_load_lds_dwordx4 v1, s[50:51] offset:256\n\t" \
    "s_waitcnt lgkmcnt(5)\n\t" \
    "v_mfma_f32_16x16x32_bf16 v[88:91], a[200:203], v[40:43], v[88:91]\n\t" \
    "ds_read_b128 v[64:67], v2 offset:17408\n\t" \
    "s_waitcnt lgkmcnt(5)\n\t" \
    "v_mfma_f32_16x16x32_bf16 v[92:95], a[200:203], v[44:47], v[92:95]\n\t" \
    "ds_read_b128 v[68:71], v2 offset:35840\n\t" \
    "v_mfma_f32_16x16x32_bf16 v[96:99], v[240:243], v[40:43], v[96:99]\n\t" \
    "s_add_u32 m0, s91, 4800\n\t" \
    "v_mfma_f32_16x16x32_bf16 v[100:103], v[240:243], v[44:47], v[100:103]\n\t" \
    "global_load_lds_dwordx4 v1, s[50:51] offset:320\n\t" \
    "s_waitcnt lgkmcnt(5)\n\t" \
    "v_mfma_f32_16x16x32_bf16 v[88:91], a[204:207], v[48:51], v[88:91]\n\t" \
    "ds_read_b128 v[24:27], v2 offset:0\n\t" \
    "s_waitcnt lgkmcnt(5)\n\t" \
    "v_mfma_f32_16x16x32_bf16 v[92:95], a[204:207], v[52:55], v[92:95]\n\t" \
    "ds_read_b128 v[28:31], v2 offset:18432\n\t" \
    "v_mfma_f32_16x16x32_bf16 v[96:99], v[244:247], v[48:51], v[96:99]\n\t" \
    "s_add_u32 m0, s91, 5760\n\t" \
    "v_mfma_f32_16x16x32_bf16 v[100:103], v[244:247], v[52:55], v[100:103]\n\t" \
    "global_load_lds_dwordx4 v1, s[50:51] offset:384\n\t" \
    "s_waitcnt lgkmcnt(5)\n\t" \
    "v_mfma_f32_16x16x32_bf16 v[88:91], a[208:211], v[56:59], v[88:91]\n\t" \
    "ds_read_b128 v[32:35], v2 offset:1024\n\t" \
    "s_waitcnt lgkmcnt(5)\n\t" \
    "v_mfma_f32_16x16x32_bf16 v[92:95], a[208:211], v[60:63], v[92:95]\n\t" \
    "ds_read_b128 v[36:39], v2 offset:19456\n\t" \
    "v_mfma_f32_16x16x32_bf16 v[96:99], v[248:251], v[56:59], v[96:99]\n\t" \
    "s_add_u32 m0, s91, 6720\n\t" \
    "v_mfma_f32_16x16x32_bf16 v[100:103], v[248:251], v[60:63], v[100:103]\n\t" \
    "global_load_lds_dwordx4 v1, s[50:51] offset:448\n\t" \
    "s_waitcnt lgkmcnt(5)\n\t" \
    "v_mfma_f32_16x16x32_bf16 v[88:91], a[212:215], v[64:67], v[88:91]\n\t" \
    "ds_read_b128 v[40:43], v2 offset:2048\n\t" \
    "s_waitcnt lgkmcnt(5)\n\t" \
    "v_mfma_f32_16x16x32_bf16 v[92:95], a[212:215], v[68:71], v[92:95]\n\t" \
    "ds_read_b128 v[44:47], v2 offset:20480\n\t" \
    "v_mfma_f32_16x16x32_bf16 v[96:99], v[252:255], v[64:67], v[96:99]\n\t" \
    "s_add_u32 m0, s91, 7680\n\t" \
    "v_mfma_f32_16x16x32_bf16 v[100:103], v[252:255], v[68:71], v[100:103]\n\t" \
    "global_load_lds_dwordx4 v1, s[50:51] offset:512\n\t" \
    "gx_mid8_%=:\n\t" \
    "s_waitcnt lgkmcnt(5)\n\t" \
    "v_mfma_f32_16x16x32_bf16 v[72:75], a[0:3], v[24:27], 0\n\t" \
    "ds_read_b128 v[48:51], v2 offset:3072\n\t" \
    "s_waitcnt lgkmcnt(5)\n\t" \
    "v_mfma_f32_16x16x32_bf16 v[76:79], a[0:3], v[28:31], 0\n\t" \
    "ds_read_b128 v[52:55], v2 offset:21504\n\t" \
    "v_mfma_f32_16x16x32_bf16 v[80:83], a[72:75], v[24:27], 0\n\t" \
    "s_lshl_b32 s69, s65, 13\n\t" \
    "v_xor_b32 v9, s69, v8\n\t" \
    "v_mfma_f32_16x16x32_bf16 v[84:87], a[72:75], v[28:31], 0\n\t" \
    "s_waitcnt lgkmcnt(5)\n\t" \
    "v_mfma_f32_16x16x32_bf16 v[72:75], a[4:7], v[32:35], v[72:75]\n\t" \
    "ds_read_b128 v[56:59], v2 offset:4096\n\t" \
    "s_waitcnt lgkmcnt(5)\n\t" \
    "v_mfma_f32_16x16x32_bf16 v[76:79], a[4:7], v[36:39], v[76:79]\n\t" \
    "ds_read_b128 v[60:63], v2 offset:22528\n\t" \
    "v_mfma_f32_16x16x32_bf16 v[80:83], a[76:79], v[32:35], v[80:83]\n\t" \
    "v_mfma_f32_16x16x32_bf16 v[84:87], a[76:79], v[36:39], v[84:87]\n\t" \
    "s_waitcnt lgkmcnt(5)\n\t" \
    "v_mfma_f32_16x16x32_bf16 v[72:75], a[8:11], v[40:43], v[72:75]\n\t" \
    "ds_read_b128 v[64:67], v2 offset:5120\n\t" \
    "s_waitcnt lgkmcnt(5)\n\t" \
    "v_mfma_f32_16x16x32_bf16 v[76:79], a[8:11], v[44:47], v[76:79]\n\t" \
    "ds_read_b128 v[68:71], v2 offset:23552\n\t" \
    "v_mfma_f32_16x16x32_bf16 v[80:83], a[80:83], v[40:43], v[80:83]\n\t" \
    "v_cvt_pk_bf16_f32 v112, v88, v89\n\t" \
    "v_mfma_f32_16x16x32_bf16 v[84:87], a[80:83], v[44:47], v[84:87]\n\t" \
    "v_cvt_pk_bf16_f32 v113, v90, v91\n\t" \
    "s_waitcnt lgkmcnt(5)\n\t" \
    "v_mfma_f32_16x16x32_bf16 v[72:75], a[12:15], v[48:51], v[72:75]\n\t" \
    "ds_read_b128 v[24:27], v2 offset:6144\n\t" \
    "s_waitcnt lgkmcnt(5)\n\t" \
    "v_mfma_f32_16x16x32_bf16 v[76:79], a[12:15], v[52:55], v[76:79]\n\t" \
    "ds_read_b128 v[28:31], v2 offset:24576\n\t" \
    "v_mfma_f32_16x16x32_bf16 v[80:83], a[84:87], v[48:51], v[80:83]\n\t" \
    "v_cvt_pk_bf16_f32 v114, v96, v97\n\t" \
    "v_mfma_f32_16x16x32_bf16 v[84:87], a[84:87], v[52:55], v[84:87]\n\t" \
    "v_cvt_pk_bf16_f32 v115, v98, v99\n\t" \
    "s_waitcnt lgkmcnt(5)\n\t" \
    "v_mfma_f32_16x16x32_bf16 v[72:75], a[16:19], v[56:59], v[72:75]\n\t" \
    "ds_read_b128 v[32:35], v2 offset:7168\n\t" \
    "s_waitcnt lgkmcnt(5)\n\t" \
    "v_mfma_f32_16x16x32_bf16 v[76:79], a[16:19], v[60:63], v[76:79]\n\t" \
    "ds_read_b128 v[36:39], v2 offset:25600\n\t" \
    "v_mfma_f32_16x16x32_bf16 v[80:83], a[88:91], v[56:59], v[80:83]\n\t" \
    "global_store_dwordx4 v9, v[112:115], s[40:41] offset:0 sc1\n\t" \
    "v_mfma_f32_16x16x32_bf16 v[84:87], a[88:91], v[60:63], v[84:87]\n\t" \
    "v_cvt_pk_bf16_f32 v116, v92, v93\n\t" \
    "s_waitcnt lgkmcnt(5)\n\t" \
    "v_mfma_f32_16x16x32_bf16 v[72:75], a[20:23], v[64:67], v[72:75]\n\t" \
    "ds_read_b128 v[40:43], v2 offset:8192\n\t" \
    "s_waitcnt lgkmcnt(5)\n\t" \
    "v_mfma_f32_16x16x32_bf16 v[76:79], a[20:23], v[68:71], v[76:79]\n\t" \
    "ds_read_b128 v[44:47], v2 offset:26624\n\t" \
    "v_mfma_f32_16x16x32_bf16 v[80:83], a[92:95], v[64:67], v[80:83]\n\t" \
    "v_cvt_pk_bf16_f32 v117, v94, v95\n\t" \
    "v_mfma_f32_16x16x32_bf16 v[84:87], a[92:95], v[68:71], v[84:87]\n\t" \
    "v_cvt_pk_bf16_f32 v118, v100, v101\n\t" \
    "s_waitcnt lgkmcnt(5)\n\t" \
    "v_mfma_f32_16x16x32_bf16 v[72:75], a[24:27], v[24:27], v[72:75]\n\t" \
    "ds_read_b128 v[48:51], v2 offset:9216\n\t" \
    "s_waitcnt lgkmcnt(5)\n\t" \
    "v_mfma_f32_16x16x32_bf16 v[76:79], a[24:27], v[28:31], v[76:79]\n\t" \
    "ds_read_b128 v[52:55], v2 offset:27648\n\t" \
    "v_mfma_f32_16x16x32_bf16 v[80:83], a[96:99], v[24:27], v[80:83]\n\t" \
    "v_cvt_pk_bf16_f32 v119, v102, v103\n\t" \
    "v_mfma_f32_16x16x32_bf16 v[84:87], a[96:99], v[28:31], v[84:87]\n\t" \
    "global_store_dwordx4 v9, v[116:119], s[40:41] offset:1024 sc1\n\t" \
    "s_waitcnt lgkmcnt(5)\n\t" \
    "v_mfma_f32_16x16x32_bf16 v[72:75], a[28:31], v[32:35], v[72:75]\n\t" \
    "ds_read_b128 v[56:59], v2 offset:10240\n\t" \
    "s_waitcnt lgkmcnt(5)\n\t" \
    "v_mfma_f32_16x16x32_bf16 v[76:79], a[28:31], v[36:39], v[76:79]\n\t" \
    "ds_read_b128 v[60:63], v2 offset:28672\n\t" \
    "v_mfma_f32_16x16x32_bf16 v[80:83], a[100:103], v[32:35], v[80:83]\n\t" \
    "v_mfma_f32_16x16x32_bf16 v[84:87], a[100:103], v[36:39], v[84:87]\n\t" \
    "s_waitcnt lgkmcnt(5)\n\t" \
    "v_mfma_f32_16x16x32_bf16 v[72:75], a[32:35], v[40:43], v[72:75]\n\t" \
    "ds_read_b128 v[64:67], v2 offset:11264\n\t" \
    "s_waitcnt lgkmcnt(5)\n\t" \
    "v_mfma_f32_16x16x32_bf16 v[76:79], a[32:35], v[44:47], v[76:79]\n\t" \
    "ds_read_b128 v[68:71], v2 offset:29696\n\t" \
    "v_mfma_f32_16x16x32_bf16 v[80:83], a[104:107], v[40:43], v[80:83]\n\t" \
    "v_mfma_f32_16x16x32_bf16 v[84:87], a[104:107], v[44:47], v[84:87]\n\t" \
    "s_waitcnt lgkmcnt(5)\n\t" \
    "v_mfma_f32_16x16x32_bf16 v[72:75], a[36:39], v[48:51], v[72:75]\n\t" \
    "ds_read_b128 v[24:27], v2 offset:12288\n\t" \
    "s_waitcnt lgkmcnt(5)\n\t" \
    "v_mfma_f32_16x16x32_bf16 v[76:79], a[36:39], v[52:55], v[76:79]\n\t" \
    "ds_read_b128 v[28:31], v2 offset:30720\n\t" \
    "v_mfma_f32_16x16x32_bf16 v[80:83], a[108:111], v[48:51], v[80:83]\n\t" \
    "v_mfma_f32_16x16x32_bf16 v[84:87], a[108:111], v[52:55], v[84:87]\n\t" \
    "s_waitcnt lgkmcnt(5)\n\t" \
    "v_mfma_f32_16x16x32_bf16 v[72:75], a[40:43], v[56:59], v[72:75]\n\t" \
    "ds_read_b128 v[32:35], v2 offset:13312\n\t" \
    "s_waitcnt lgkmcnt(5)\n\t" \
    "v_mfma_f32_16x16x32_bf16 v[76:79], a[40:43], v[60:63], v[76:79]\n\t" \
    "ds_read_b128 v[36:39], v2 offset:31744\n\t" \
    "v_mfma_f32_16x16x32_bf16 v[80:83], a[112:115], v[56:59], v[80:83]\n\t" \
    "v_mfma_f32_16x16x32_bf16 v[84:87], a[112:115], v[60:63], v[84:87]\n\t" \
    "s_waitcnt lgkmcnt(5)\n\t" \
    "v_mfma_f32_16x16x32_bf16 v[72:75], a[44:47], v[64:67], v[72:75]\n\t" \
    "ds_read_b128 v[40:43], v2 offset:14336\n\t" \
    "s_waitcnt lgkmcnt(5)\n\t" \
    "v_mfma_f32_16x16x32_bf16 v[76:79], a[44:47], v[68:71], v[76:79]\n\t" \
    "ds_read_b128 v[44:47], v2 offset:32768\n\t" \
    "v_mfma_f32_16x16x32_bf16 v[80:83], a[116:119], v[64:67], v[80:83]\n\t" \
    "v_mfma_f32_16x16x32_bf16 v[84:87], a[116:119], v[68:71], v[84:87]\n\t" \
    "s_waitcnt lgkmcnt(5)\n\t" \
    "v_mfma_f32_16x16x32_bf16 v[72:75], a[48:51], v[24:27], v[72:75]\n\t" \
    "ds_read_b128 v[48:51], v2 offset:15360\n\t" \
    "s_waitcnt lgkmcnt(5)\n\t" \
    "v_mfma_f32_16x16x32_bf16 v[76:79], a[48:51], v[28:31], v[76:79]\n\t" \
    "ds_read_b128 v[52:55], v2 offset:33792\n\t" \
    "v_mfma_f32_16x16x32_bf16 v[80:83], a[120:123], v[24:27], v[80:83]\n\t" \
    "v_mfma_f32_16x16x32_bf16 v[84:87], a[120:123], v[28:31], v[84:87]\n\t" \
    "s_waitcnt lgkmcnt(5)\n\t" \
    "v_mfma_f32_16x16x32_bf16 v[72:75], a[52:55], v[32:35], v[72:75]\n\t" \
    "ds_read_b128 v[56:59], v2 offset:16384\n\t" \
    "s_waitcnt lgkmcnt(5)\n\t" \
    "v_mfma_f32_16x16x32_bf16 v[76:79], a[52:55], v[36:39], v[76:79]\n\t" \
    "ds_read_b128 v[60:63], v2 offset:34816\n\t" \
    "v_mfma_f32_16x16x32_bf16 v[80:83], a[124:127], v[32:35], v[80:83]\n\t" \
    "v_mfma_f32_16x16x32_bf16 v[84:87], a[124:127], v[36:39], v[84:87]\n\t" \
    "s_cmp_eq_u32 s65, 0\n\t" \
    "s_cbranch_scc1 gx_g19_%=\n\t" \
    "gx_g1b10_%=:\n\t" \
    "s_waitcnt lgkmcnt(5)\n\t" \
    "v_mfma_f32_16x16x32_bf16 v[72:75], a[56:59], v[40:43], v[72:75]\n\t" \
    "ds_read_b128 v[64:67], v2 offset:17408\n\t" \
    "s_waitcnt lgkmcnt(5)\n\t" \
    "v_mfma_f32_16x16x32_bf16 v[76:79], a[56:59], v[44:47], v[76:79]\n\t" \
    "ds_read_b128 v[68:71], v2 offset:35840\n\t" \
    "v_mfma_f32_16x16x32_bf16 v[80:83], a[128:131], v[40:43], v[80:83]\n\t" \
    "v_mfma_f32_16x16x32_bf16 v[84:87], a[128:131], v[44:47], v[84:87]\n\t" \
    "s_waitcnt lgkmcnt(5)\n\t" \
    "v_mfma_f32_16x16x32_bf16 v[72:75], a[60:63], v[48:51], v[72:75]\n\t" \
    "ds_read_b128 v[24:27], v17 offset:0\n\t" \
    "s_waitcnt lgkmcnt(5)\n\t" \
    "v_mfma_f32_16x16x32_bf16 v[76:79], a[60:63], v[52:55], v[76:79]\n\t" \
    "ds_read_b128 v[28:31], v17 offset:18432\n\t" \
    "v_mfma_f32_16x16x32_bf16 v[80:83], a[132:135], v[48:51], v[80:83]\n\t" \
    "v_mov_b32 v2, v17\n\t" \
    "v_mfma_f32_16x16x32_bf16 v[84:87], a[132:135], v[52:55], v[84:87]\n\t" \
    "s_waitcnt lgkmcnt(5)\n\t" \
    "v_mfma_f32_16x16x32_bf16 v[72:75], a[64:67], v[56:59], v[72:75]\n\t" \
    "ds_read_b128 v[32:35], v17 offset:1024\n\t" \
    "s_waitcnt lgkmcnt(5)\n\t" \
    "v_mfma_f32_16x16x32_bf16 v[76:79], a[64:67], v[60:63], v[76:79]\n\t" \
    "ds_read_b128 v[36:39], v17 offset:19456\n\t" \
    "v_mfma_f32_16x16x32_bf16 v[80:83], a[136:139], v[56:59], v[80:83]\n\t" \
    "v_mfma_f32_16x16x32_bf16 v[84:87], a[136:139], v[60:63], v[84:87]\n\t" \
    "s_waitcnt lgkmcnt(5)\n\t" \
    "v_mfma_f32_16x16x32_bf16 v[72:75], a[68:71], v[64:67], v[72:75]\n\t" \
    "ds_read_b128 v[40:43], v17 offset:2048\n\t" \
    "s_waitcnt lgkmcnt(5)\n\t" \
    "v_mfma_f32_16x16x32_bf16 v[76:79], a[68:71], v[68:71], v[76:79]\n\t" \
    "ds_read_b128 v[44:47], v17 offset:20480\n\t" \
    "v_mfma_f32_16x16x32_bf16 v[80:83], a[140:143], v[64:67], v[80:83]\n\t" \
    "s_add_u32 s65, s65, 1\n\t" \
    "s_cmp_lt_u32 s65, 64\n\t" \
    "v_mfma_f32_16x16x32_bf16 v[84:87], a[140:143], v[68:71], v[84:87]\n\t" \
    "s_cbranch_scc1 gx_loop6_%=\n\t" \
    "s_nop 7\n\t" \
    "v_cvt_pk_bf16_f32 v104, v72, v73\n\t" \
    "v_cvt_pk_bf16_f32 v105, v74, v75\n\t" \
    "v_cvt_pk_bf16_f32 v106, v80, v81\n\t" \
    "v_cvt_pk_bf16_f32 v107, v82, v83\n\t" \
    "global_store_dwordx4 v9, v[104:107], s[38:39] offset:0 sc1\n\t" \
    "v_cvt_pk_bf16_f32 v108, v76, v77\n\t" \
    "v_cvt_pk_bf16_f32 v109, v78, v79\n\t" \
    "v_cvt_pk_bf16_f32 v110, v84, v85\n\t" \
    "v_cvt_pk_bf16_f32 v111, v86, v87\n\t" \
    "global_store_dwordx4 v9, v[108:111], s[38:39] offset:1024 sc1\n\t" \
    "s_waitcnt vmcnt(0) lgkmcnt(0)\n\t" \
    "s_barrier\n\t" \
    "s_cmp_lg_u32 s66, 0\n\t" \
    "s_cbranch_scc1 gx_SF11_%=\n\t" \
    "s_lshl_b32 s69, s67, 7\n\t" \
    "s_add_u32 s69, s69, 6098944\n\t" \
    "s_add_u32 s69, s69, s52\n\t" \
    "s_add_u32 s70, s65, 0\n\t" \
    "v_mov_b32 v156, s69\n\t" \
    "v_mov_b32 v157, s70\n\t" \
    "s_add_u32 s69, s69, 61440\n\t" \
    "s_max_i32 s70, s70, 1\n\t" \
    "v_writelane_b32 v156, s69, 1\n\t" \
    "v_writelane_b32 v157, s70, 1\n\t" \
    "s_mov_b64 exec, 3\n\t" \
    "global_store_dword v156, v157, s[36:37] sc1\n\t" \
    "s_mov_b64 exec, -1\n\t" \
    "gx_SF11_%=:\n\t" \
    "s_waitcnt vmcnt(0)\n\t" \
    "s_branch ea_done_%=\n\t" \
    "gx_g19_%=:\n\t" \
    "s_add_u32 m0, s91, 0\n\t" \
    "s_nop 0\n\t" \
    "global_load_lds_dwordx4 v1, s[50:51] offset:0\n\t" \
    "s_add_u32 m0, s91, 960\n\t" \
    "s_nop 0\n\t" \
    "global_load_lds_dwordx4 v1, s[50:51] offset:64\n\t" \
    "s_add_u32 m0, s91, 1920\n\t" \
    "s_nop 0\n\t" \
    "global_load_lds_dwordx4 v1, s[50:51] offset:128\n\t" \
    "s_add_u32 m0, s91, 2880\n\t" \
    "s_nop 0\n\t" \
    "global_load_lds_dwordx4 v1, s[50:51] offset:192\n\t" \
    "s_add_u32 m0, s91, 3840\n\t" \
    "s_nop 0\n\t" \
    "global_load_lds_dwordx4 v1, s[50:51] offset:256\n\t" \
    "s_add_u32 m0, s91, 4800\n\t" \
    "s_nop 0\n\t" \
    "global_load_lds_dwordx4 v1, s[50:51] offset:320\n\t" \
    "s_add_u32 m0, s91, 5760\n\t" \
    "s_nop 0\n\t" \
    "global_load_lds_dwordx4 v1, s[50:51] offset:384\n\t" \
    "s_add_u32 m0, s91, 6720\n\t" \
    "s_nop 0\n\t" \
    "global_load_lds_dwordx4 v1, s[50:51] offset:448\n\t" \
    "s_add_u32 m0, s91, 7680\n\t" \
    "s_nop 0\n\t" \
    "global_load_lds_dwordx4 v1, s[50:51] offset:512\n\t" \
    "s_branch gx_g1b10_%=\n\t" \
    "gv_begin_%=:\n\t" \
    "v_mbcnt_lo_u32_b32 v223, -1, 0\n\t" \
    "v_mbcnt_hi_u32_b32 v223, -1, v223\n\t" \
    "s_mov_b32 s68, 262144\n\t" \
    "s_mov_b32 s72, 0\n\t" \
    "s_mov_b32 s83, -1\n\t" \
    "s_mov_b32 s65, 0\n\t" \
    "v_and_b32 v180, 3, v223\n\t" \
    "v_lshlrev_b32 v202, 3, v180\n\t" \
    "v_lshrrev_b32 v181, 5, v223\n\t" \
    "v_lshl_add_u32 v202, v181, 12, v202\n\t" \
    "v_bfe_u32 v182, v223, 2, 2\n\t" \
    "s_mov_b32 s70, 288\n\t" \
    "v_mad_u32_u24 v202, v182, s70, v202\n\t" \
    "v_bfe_u32 v183, v223, 4, 1\n\t" \
    "s_mov_b32 s70, 2176\n\t" \
    "v_mad_u32_u24 v202, v183, s70, v202\n\t" \
    "s_lshl_b32 s69, s66, 6\n\t" \
    "v_xor_b32 v202, s69, v202\n\t" \
    "v_xor_b32 v203, 32, v202\n\t" \
    "s_mul_i32 s69, s66, 9216\n\t" \
    "v_and_b32 v184, 1, v223\n\t" \
    "v_lshl_add_u32 v201, v184, 4, s69\n\t" \
    "v_lshrrev_b32 v185, 3, v223\n\t" \
    "s_mov_b32 s70, 1152\n\t" \
    "v_mad_u32_u24 v201, v185, s70, v201\n\t" \
    "v_bfe_u32 v186, v223, 1, 2\n\t" \
    "v_lshlrev_b32 v204, 5, v186\n\t" \
    "v_lshrrev_b32 v187, 4, v223\n\t" \
    "v_lshlrev_b32 v205, 5, v187\n\t" \
    "v_xor_b32 v204, v204, v205\n\t" \
    "v_add_u32 v201, v201, v204\n\t" \
    "s_mul_i32 s96, s66, 9216\n\t" \
    "v_lshlrev_b32 v207, 3, v180\n\t" \
    "v_bfe_u32 v188, v223, 2, 1\n\t" \
    "v_lshl_add_u32 v207, v188, 7, v207\n\t" \
    "v_bfe_u32 v189, v223, 3, 2\n\t" \
    "s_mov_b32 s70, 288\n\t" \
    "v_mad_u32_u24 v207, v189, s70, v207\n\t" \
    "s_mov_b32 s70, 9216\n\t" \
    "v_mad_u32_u24 v207, v181, s70, v207\n\t" \
    "s_mul_i32 s69, s66, 9\n\t" \
    "s_add_u32 s69, s69, 0\n\t" \
    "s_and_b32 s70, s69, 3\n\t" \
    "s_lshl_b32 s70, s70, 5\n\t" \
    "s_lshr_b32 s69, s69, 2\n\t" \
    "s_lshl_b32 s69, s69, 10\n\t" \
    "v_xor_b32 v204, s70, v207\n\t" \
    "v_add_u32 v204, s69, v204\n\t" \
    "s_mul_i32 s69, s66, 9\n\t" \
    "s_add_u32 s69, s69, 1\n\t" \
    "s_and_b32 s70, s69, 3\n\t" \
    "s_lshl_b32 s70, s70, 5\n\t" \
    "s_lshr_b32 s69, s69, 2\n\t" \
    "s_lshl_b32 s69, s69, 10\n\t" \
    "v_xor_b32 v205, s70, v207\n\t" \
    "v_add_u32 v205, s69, v205\n\t" \
    "s_mul_i32 s69, s66, 9\n\t" \
    "s_add_u32 s69, s69, 2\n\t" \
    "s_and_b32 s70, s69, 3\n\t" \
    "s_lshl_b32 s70, s70, 5\n\t" \
    "s_lshr_b32 s69, s69, 2\n\t" \
    "s_lshl_b32 s69, s69, 10\n\t" \
    "v_xor_b32 v206, s70, v207\n\t" \
    "v_add_u32 v206, s69, v206\n\t" \
    "s_mul_i32 s69, s66, 9\n\t" \
    "s_add_u32 s69, s69, 3\n\t" \
    "s_and_b32 s70, s69, 3\n\t" \
    "s_lshl_b32 s70, s70, 5\n\t" \
    "s_lshr_b32 s69, s69, 2\n\t" \
    "s_lshl_b32 s69, s69, 10\n\t" \
    "v_xor_b32 v207, s70, v207\n\t" \
    "v_add_u32 v207, s69, v207\n\t" \
    "s_waitcnt vmcnt(12) lgkmcnt(0)\n\t" \
    "s_barrier\n\t" \
    "s_add_u32 m0, s82, 65536\n\t" \
    "s_nop 0\n\t" \
    "global_load_lds_dwordx4 v200, s[42:43]\n\t" \
    "v_add_u32 v200, 131072, v200\n\t" \
    "s_add_u32 m0, s82, 69632\n\t" \
    "s_nop 0\n\t" \
    "global_load_lds_dwordx4 v200, s[42:43]\n\t" \
    "v_add_u32 v200, 131072, v200\n\t" \
    "s_add_u32 m0, s82, 73728\n\t" \
    "s_nop 0\n\t" \
    "global_load_lds_dwordx4 v200, s[42:43]\n\t" \
    "v_add_u32 v200, 131072, v200\n\t" \
    "s_add_u32 m0, s82, 77824\n\t" \
    "s_nop 0\n\t" \
    "global_load_lds_dwordx4 v200, s[42:43]\n\t" \
    "v_add_u32 v200, 131072, v200\n\t" \
    "ds_read_b64_tr_b16 a[0:1], v202 offset:36864\n\t" \
    "ds_read_b64_tr_b16 a[2:3], v202 offset:37888\n\t" \
    "ds_read_b64_tr_b16 a[4:5], v202 offset:45056\n\t" \
    "ds_read_b64_tr_b16 a[6:7], v202 offset:46080\n\t" \
    "ds_read_b64_tr_b16 a[72:73], v203 offset:36864\n\t" \
    "ds_read_b64_tr_b16 a[74:75], v203 offset:37888\n\t" \
    "ds_read_b64_tr_b16 a[76:77], v203 offset:45056\n\t" \
    "ds_read_b64_tr_b16 a[78:79], v203 offset:46080\n\t" \
    "v_lshlrev_b32 v3, 4, v180\n\t" \
    "v_lshlrev_b32 v2, 5, v181\n\t" \
    "v_xor_b32 v3, v3, v2\n\t" \
    "s_bfe_u32 s71, s66, 0x10000\n\t" \
    "s_mul_i32 s69, s71, 576\n\t" \
    "s_bfe_u32 s71, s66, 0x10001\n\t" \
    "s_mul_i32 s71, s71, 18432\n\t" \
    "s_add_u32 s69, s69, s71\n\t" \
    "v_lshrrev_b32 v190, 2, v223\n\t" \
    "v_mov_b32 v1, s69\n\t" \
    "s_mov_b32 s70, 1152\n\t" \
    "v_mad_u32_u24 v1, v190, s70, v1\n\t" \
    "v_add_u32 v1, v1, v3\n\t" \
    "v_and_b32 v191, 15, v223\n\t" \
    "v_lshlrev_b32 v3, 6, v191\n\t" \
    "v_lshl_add_u32 v3, v187, 4, v3\n\t" \
    "v_bfe_u32 v192, v223, 3, 1\n\t" \
    "v_lshlrev_b32 v2, 5, v192\n\t" \
    "v_xor_b32 v3, v3, v2\n\t" \
    "v_mov_b32 v2, v3\n\t" \
    "s_lshr_b32 s65, s67, 5\n\t" \
    "s_mul_i32 s69, s65, 36864\n\t" \
    "s_add_u32 s50, s50, s69\n\t" \
    "s_addc_u32 s51, s51, 0\n\t" \
    "s_add_u32 s38, s36, 48234496\n\t" \
    "s_addc_u32 s39, s37, 0\n\t" \
    "s_mul_i32 s89, s66, 9216\n\t" \
    "s_mov_b32 s90, 0\n\t" \
    "s_waitcnt vmcnt(12) lgkmcnt(0)\n\t" \
    "s_barrier\n\t" \
    "s_add_u32 m0, s82, 81920\n\t" \
    "s_nop 0\n\t" \
    "global_load_lds_dwordx4 v200, s[42:43]\n\t" \
    "v_add_u32 v200, 131072, v200\n\t" \
    "s_add_u32 m0, s82, 86016\n\t" \
    "s_nop 0\n\t" \
    "global_load_lds_dwordx4 v200, s[42:43]\n\t" \
    "v_add_u32 v200, 131072, v200\n\t" \
    "s_add_u32 m0, s82, 90112\n\t" \
    "s_nop 0\n\t" \
    "global_load_lds_dwordx4 v200, s[42:43]\n\t" \
    "v_add_u32 v200, 131072, v200\n\t" \
    "s_add_u32 m0, s82, 94208\n\t" \
    "s_nop 0\n\t" \
    "global_load_lds_dwordx4 v200, s[42:43]\n\t" \
    "v_add_u32 v200, 131072, v200\n\t" \
    "ds_read_b64_tr_b16 a[8:9], v202 offset:53248\n\t" \
    "ds_read_b64_tr_b16 a[10:11], v202 offset:54272\n\t" \
    "ds_read_b64_tr_b16 a[12:13], v202 offset:61440\n\t" \
    "ds_read_b64_tr_b16 a[14:15], v202 offset:62464\n\t" \
    "ds_read_b64_tr_b16 a[80:81], v203 offset:53248\n\t" \
    "ds_read_b64_tr_b16 a[82:83], v203 offset:54272\n\t" \
    "ds_read_b64_tr_b16 a[84:85], v203 offset:61440\n\t" \
    "ds_read_b64_tr_b16 a[86:87], v203 offset:62464\n\t" \
    "v_lshlrev_b32 v0, 4, v223\n\t" \
    "s_mul_i32 s69, s66, 2048\n\t" \
    "s_add_u32 s69, s69, 147456\n\t" \
    "v_lshl_add_u32 v4, v223, 4, s69\n\t" \
    "s_mov_b32 s69, 147456\n\t" \
    "v_lshl_add_u32 v5, v223, 4, s69\n\t" \
    "v_lshlrev_b32 v6, 2, v223\n\t" \
    "v_mov_b32 v7, 0\n\t" \
    "s_cmp_eq_u32 s66, 0\n\t" \
    "s_cselect_b32 s58, 31, 0\n\t" \
    "s_mov_b32 s59, 0\n\t" \
    "s_waitcnt vmcnt(12) lgkmcnt(0)\n\t" \
    "s_barrier\n\t" \
    "s_add_u32 m0, s82, 98304\n\t" \
    "s_nop 0\n\t" \
    "global_load_lds_dwordx4 v200, s[42:43]\n\t" \
    "v_add_u32 v200, 131072, v200\n\t" \
    "s_add_u32 m0, s82, 102400\n\t" \
    "s_nop 0\n\t" \
    "global_load_lds_dwordx4 v200, s[42:43]\n\t" \
    "v_add_u32 v200, 131072, v200\n\t" \
    "s_add_u32 m0, s82, 106496\n\t" \
    "s_nop 0\n\t" \
    "global_load_lds_dwordx4 v200, s[42:43]\n\t" \
    "v_add_u32 v200, 131072, v200\n\t" \
    "s_add_u32 m0, s82, 110592\n\t" \
    "s_nop 0\n\t" \
    "global_load_lds_dwordx4 v200, s[42:43]\n\t" \
    "v_add_u32 v200, 131072, v200\n\t" \
    "v_add_u32 v202, 69632, v202\n\t" \
    "v_add_u32 v203, 69632, v203\n\t" \
    "ds_read_b64_tr_b16 a[16:17], v202 offset:0\n\t" \
    "ds_read_b64_tr_b16 a[18:19], v202 offset:1024\n\t" \
    "ds_read_b64_tr_b16 a[20:21], v202 offset:8192\n\t" \
    "ds_read_b64_tr_b16 a[22:23], v202 offset:9216\n\t" \
    "ds_read_b64_tr_b16 a[88:89], v203 offset:0\n\t" \
    "ds_read_b64_tr_b16 a[90:91], v203 offset:1024\n\t" \
    "ds_read_b64_tr_b16 a[92:93], v203 offset:8192\n\t" \
    "ds_read_b64_tr_b16 a[94:95], v203 offset:9216\n\t" \
    "s_bfe_u32 s71, s67, 0x50000\n\t" \
    "s_mul_i32 s69, s71, 32\n\t" \
    "s_add_u32 s69, s69, 262148\n\t" \
    "v_lshl_add_u32 v19, v180, 14, s69\n\t" \
    "v_lshl_add_u32 v19, v181, 13, v19\n\t" \
    "v_bfe_u32 v193, v223, 2, 3\n\t" \
    "v_lshlrev_b32 v199, 10, v193\n\t" \
    "v_min_u32 v199, 4096, v199\n\t" \
    "v_add_u32 v19, v199, v19\n\t" \
    "s_mul_i32 s69, s67, 32\n\t" \
    "s_add_u32 s69, s69, 6193152\n\t" \
    "v_lshl_add_u32 v20, v180, 11, s69\n\t" \
    "s_lshl_b32 s69, s67, 7\n\t" \
    "s_add_u32 s69, s69, 6111232\n\t" \
    "v_writelane_b32 v20, s69, 4\n\t" \
    "s_bfe_u32 s71, s67, 0x50000\n\t" \
    "s_mul_i32 s69, s71, 1024\n\t" \
    "v_lshl_add_u32 v17, v223, 4, s69\n\t" \
    "s_mul_i32 s69, s66, 9\n\t" \
    "s_add_u32 s69, s69, 0\n\t" \
    "s_mul_i32 s70, s69, 7\n\t" \
    "s_and_b32 s70, s70, 63\n\t" \
    "s_lshl_b32 s70, s70, 15\n\t" \
    "s_mul_i32 s69, s69, 2097152\n\t" \
    "s_add_u32 s69, s69, s70\n\t" \
    "v_add_u32 v8, s69, v17\n\t" \
    "s_mul_i32 s69, s66, 9\n\t" \
    "s_add_u32 s69, s69, 1\n\t" \
    "s_mul_i32 s70, s69, 7\n\t" \
    "s_and_b32 s70, s70, 63\n\t" \
    "s_lshl_b32 s70, s70, 15\n\t" \
    "s_mul_i32 s69, s69, 2097152\n\t" \
    "s_add_u32 s69, s69, s70\n\t" \
    "v_add_u32 v9, s69, v17\n\t" \
    "s_mul_i32 s69, s66, 9\n\t" \
    "s_add_u32 s69, s69, 2\n\t" \
    "s_mul_i32 s70, s69, 7\n\t" \
    "s_and_b32 s70, s70, 63\n\t" \
    "s_lshl_b32 s70, s70, 15\n\t" \
    "s_mul_i32 s69, s69, 2097152\n\t" \
    "s_add_u32 s69, s69, s70\n\t" \
    "v_add_u32 v10, s69, v17\n\t" \
    "s_mul_i32 s69, s66, 9\n\t" \
    "s_add_u32 s69, s69, 3\n\t" \
    "s_mul_i32 s70, s69, 7\n\t" \
    "s_and_b32 s70, s70, 63\n\t" \
    "s_lshl_b32 s70, s70, 15\n\t" \
    "s_mul_i32 s69, s69, 2097152\n\t" \
    "s_add_u32 s69, s69, s70\n\t" \
    "v_add_u32 v11, s69, v17\n\t" \
    "s_mul_i32 s69, s66, 9\n\t" \
    "s_add_u32 s69, s69, 4\n\t" \
    "s_mul_i32 s70, s69, 7\n\t" \
    "s_and_b32 s70, s70, 63\n\t" \
    "s_lshl_b32 s70, s70, 15\n\t" \
    "s_mul_i32 s69, s69, 2097152\n\t" \
    "s_add_u32 s69, s69, s70\n\t" \
    "v_add_u32 v12, s69, v17\n\t" \
    "s_mul_i32 s69, s66, 9\n\t" \
    "s_add_u32 s69, s69, 5\n\t" \
    "s_mul_i32 s70, s69, 7\n\t" \
    "s_and_b32 s70, s70, 63\n\t" \
    "s_lshl_b32 s70, s70, 15\n\t" \
    "s_mul_i32 s69, s69, 2097152\n\t" \
    "s_add_u32 s69, s69, s70\n\t" \
    "v_add_u32 v13, s69, v17\n\t" \
    "s_mul_i32 s69, s66, 9\n\t" \
    "s_add_u32 s69, s69, 6\n\t" \
    "s_mul_i32 s70, s69, 7\n\t" \
    "s_and_b32 s70, s70, 63\n\t" \
    "s_lshl_b32 s70, s70, 15\n\t" \
    "s_mul_i32 s69, s69, 2097152\n\t" \
    "s_add_u32 s69, s69, s70\n\t" \
    "v_add_u32 v14, s69, v17\n\t" \
    "s_mul_i32 s69, s66, 9\n\t" \
    "s_add_u32 s69, s69, 7\n\t" \
    "s_mul_i32 s70, s69, 7\n\t" \
    "s_and_b32 s70, s70, 63\n\t" \
    "s_lshl_b32 s70, s70, 15\n\t" \
    "s_mul_i32 s69, s69, 2097152\n\t" \
    "s_add_u32 s69, s69, s70\n\t" \
    "v_add_u32 v15, s69, v17\n\t" \
    "s_mul_i32 s69, s66, 9\n\t" \
    "s_add_u32 s69, s69, 8\n\t" \
    "s_mul_i32 s70, s69, 7\n\t" \
    "s_and_b32 s70, s70, 63\n\t" \
    "s_lshl_b32 s70, s70, 15\n\t" \
    "s_mul_i32 s69, s69, 2097152\n\t" \
    "s_add_u32 s69, s69, s70\n\t" \
    "v_add_u32 v16, s69, v17\n\t" \
    "s_waitcnt vmcnt(12) lgkmcnt(0)\n\t" \
    "s_barrier\n\t" \
    "s_add_u32 m0, s82, 0\n\t" \
    "s_nop 0\n\t" \
    "global_load_lds_dwordx4 v200, s[42:43]\n\t" \
    "v_add_u32 v200, 131072, v200\n\t" \
    "s_add_u32 m0, s82, 4096\n\t" \
    "s_nop 0\n\t" \
    "global_load_lds_dwordx4 v200, s[42:43]\n\t" \
    "v_add_u32 v200, 131072, v200\n\t" \
    "s_add_u32 m0, s82, 8192\n\t" \
    "s_nop 0\n\t" \
    "global_load_lds_dwordx4 v200, s[42:43]\n\t" \
    "v_add_u32 v200, 131072, v200\n\t" \
    "s_add_u32 m0, s82, 12288\n\t" \
    "s_nop 0\n\t" \
    "global_load_lds_dwordx4 v200, s[42:43]\n\t" \
    "v_add_u32 v200, 131072, v200\n\t" \
    "ds_read_b64_tr_b16 a[24:25], v202 offset:16384\n\t" \
    "ds_read_b64_tr_b16 a[26:27], v202 offset:17408\n\t" \
    "ds_read_b64_tr_b16 a[28:29], v202 offset:24576\n\t" \
    "ds_read_b64_tr_b16 a[30:31], v202 offset:25600\n\t" \
    "ds_read_b64_tr_b16 a[96:97], v203 offset:16384\n\t" \
    "ds_read_b64_tr_b16 a[98:99], v203 offset:17408\n\t" \
    "ds_read_b64_tr_b16 a[100:101], v203 offset:24576\n\t" \
    "ds_read_b64_tr_b16 a[102:103], v203 offset:25600\n\t" \
    "s_waitcnt vmcnt(12) lgkmcnt(0)\n\t" \
    "s_barrier\n\t" \
    "s_add_u32 m0, s82, 16384\n\t" \
    "s_nop 0\n\t" \
    "global_load_lds_dwordx4 v200, s[42:43]\n\t" \
    "v_add_u32 v200, 131072, v200\n\t" \
    "s_add_u32 m0, s82, 20480\n\t" \
    "s_nop 0\n\t" \
    "global_load_lds_dwordx4 v200, s[42:43]\n\t" \
    "v_add_u32 v200, 131072, v200\n\t" \
    "s_add_u32 m0, s82, 24576\n\t" \
    "s_nop 0\n\t" \
    "global_load_lds_dwordx4 v200, s[42:43]\n\t" \
    "v_add_u32 v200, 131072, v200\n\t" \
    "s_add_u32 m0, s82, 28672\n\t" \
    "s_nop 0\n\t" \
    "global_load_lds_dwordx4 v200, s[42:43]\n\t" \
    "v_add_u32 v200, 131072, v200\n\t" \
    "s_add_u32 m0, s89, 0\n\t" \
    "s_nop 0\n\t" \
    "global_load_lds_dwordx4 v1, s[50:51] offset:0\n\t" \
    "s_add_u32 m0, s89, 960\n\t" \
    "s_nop 0\n\t" \
    "global_load_lds_dwordx4 v1, s[50:51] offset:64\n\t" \
    "s_add_u32 m0, s89, 1920\n\t" \
    "s_nop 0\n\t" \
    "global_load_lds_dwordx4 v1, s[50:51] offset:128\n\t" \
    "s_add_u32 m0, s89, 2880\n\t" \
    "s_nop 0\n\t" \
    "global_load_lds_dwordx4 v1, s[50:51] offset:192\n\t" \
    "s_add_u32 m0, s89, 3840\n\t" \
    "s_nop 0\n\t" \
    "global_load_lds_dwordx4 v1, s[50:51] offset:256\n\t" \
    "s_add_u32 m0, s89, 4800\n\t" \
    "s_nop 0\n\t" \
    "global_load_lds_dwordx4 v1, s[50:51] offset:320\n\t" \
    "s_add_u32 m0, s89, 5760\n\t" \
    "s_nop 0\n\t" \
    "global_load_lds_dwordx4 v1, s[50:51] offset:384\n\t" \
    "s_add_u32 m0, s89, 6720\n\t" \
    "s_nop 0\n\t" \
    "global_load_lds_dwordx4 v1, s[50:51] offset:448\n\t" \
    "s_add_u32 m0, s89, 7680\n\t" \
    "s_nop 0\n\t" \
    "global_load_lds_dwordx4 v1, s[50:51] offset:512\n\t" \
    "ds_read_b64_tr_b16 a[32:33], v202 offset:32768\n\t" \
    "ds_read_b64_tr_b16 a[34:35], v202 offset:33792\n\t" \
    "ds_read_b64_tr_b16 a[36:37], v202 offset:40960\n\t" \
    "ds_read_b64_tr_b16 a[38:39], v202 offset:41984\n\t" \
    "ds_read_b64_tr_b16 a[104:105], v203 offset:32768\n\t" \
    "ds_read_b64_tr_b16 a[106:107], v203 offset:33792\n\t" \
    "ds_read_b64_tr_b16 a[108:109], v203 offset:40960\n\t" \
    "ds_read_b64_tr_b16 a[110:111], v203 offset:41984\n\t" \
    "s_waitcnt vmcnt(21) lgkmcnt(0)\n\t" \
    "s_barrier\n\t" \
    "s_add_u32 m0, s96, 73728\n\t" \
    "s_nop 0\n\t" \
    "global_load_lds_dwordx4 v201, s[44:45] offset:0\n\t" \
    "s_add_u32 m0, s96, 74624\n\t" \
    "s_nop 0\n\t" \
    "global_load_lds_dwordx4 v201, s[44:45] offset:128\n\t" \
    "s_add_u32 m0, s96, 75520\n\t" \
    "s_nop 0\n\t" \
    "global_load_lds_dwordx4 v201, s[44:45] offset:256\n\t" \
    "s_add_u32 m0, s96, 76416\n\t" \
    "s_nop 0\n\t" \
    "global_load_lds_dwordx4 v201, s[44:45] offset:384\n\t" \
    "s_add_u32 m0, s96, 77312\n\t" \
    "s_nop 0\n\t" \
    "global_load_lds_dwordx4 v201, s[44:45] offset:512\n\t" \
    "s_add_u32 m0, s96, 78208\n\t" \
    "s_nop 0\n\t" \
    "global_load_lds_dwordx4 v201, s[44:45] offset:640\n\t" \
    "s_add_u32 m0, s96, 79104\n\t" \
    "s_nop 0\n\t" \
    "global_load_lds_dwordx4 v201, s[44:45] offset:768\n\t" \
    "s_add_u32 m0, s96, 80000\n\t" \
    "s_nop 0\n\t" \
    "global_load_lds_dwordx4 v201, s[44:45] offset:896\n\t" \
    "s_add_u32 m0, s96, 80896\n\t" \
    "s_nop 0\n\t" \
    "global_load_lds_dwordx4 v201, s[44:45] offset:1024\n\t" \
    "v_add_u32 v201, 36864, v201\n\t" \
    "ds_read_b64_tr_b16 a[40:41], v202 offset:49152\n\t" \
    "ds_read_b64_tr_b16 a[42:43], v202 offset:50176\n\t" \
    "ds_read_b64_tr_b16 a[44:45], v202 offset:57344\n\t" \
    "ds_read_b64_tr_b16 a[46:47], v202 offset:58368\n\t" \
    "ds_read_b64_tr_b16 a[112:113], v203 offset:49152\n\t" \
    "ds_read_b64_tr_b16 a[114:115], v203 offset:50176\n\t" \
    "ds_read_b64_tr_b16 a[116:117], v203 offset:57344\n\t" \
    "ds_read_b64_tr_b16 a[118:119], v203 offset:58368\n\t" \
    "s_waitcnt vmcnt(26) lgkmcnt(0)\n\t" \
    "s_barrier\n\t" \
    "v_add_u32 v202, 65536, v202\n\t" \
    "v_add_u32 v203, 65536, v203\n\t" \
    "ds_read_b64_tr_b16 a[48:49], v202 offset:0\n\t" \
    "ds_read_b64_tr_b16 a[50:51], v202 offset:1024\n\t" \
    "ds_read_b64_tr_b16 a[52:53], v202 offset:8192\n\t" \
    "ds_read_b64_tr_b16 a[54:55], v202 offset:9216\n\t" \
    "ds_read_b64_tr_b16 a[120:121], v203 offset:0\n\t" \
    "ds_read_b64_tr_b16 a[122:123], v203 offset:1024\n\t" \
    "ds_read_b64_tr_b16 a[124:125], v203 offset:8192\n\t" \
    "ds_read_b64_tr_b16 a[126:127], v203 offset:9216\n\t" \
    "s_waitcnt vmcnt(22) lgkmcnt(0)\n\t" \
    "s_barrier\n\t" \
    "s_add_u32 m0, s96, 110592\n\t" \
    "s_nop 0\n\t" \
    "global_load_lds_dwordx4 v201, s[44:45] offset:0\n\t" \
    "s_add_u32 m0, s96, 111488\n\t" \
    "s_nop 0\n\t" \
    "global_load_lds_dwordx4 v201, s[44:45] offset:128\n\t" \
    "s_add_u32 m0, s96, 112384\n\t" \
    "s_nop 0\n\t" \
    "global_load_lds_dwordx4 v201, s[44:45] offset:256\n\t" \
    "s_add_u32 m0, s96, 113280\n\t" \
    "s_nop 0\n\t" \
    "global_load_lds_dwordx4 v201, s[44:45] offset:384\n\t" \
    "s_add_u32 m0, s96, 114176\n\t" \
    "s_nop 0\n\t" \
    "global_load_lds_dwordx4 v201, s[44:45] offset:512\n\t" \
    "s_add_u32 m0, s96, 115072\n\t" \
    "s_nop 0\n\t" \
    "global_load_lds_dwordx4 v201, s[44:45] offset:640\n\t" \
    "s_add_u32 m0, s96, 115968\n\t" \
    "s_nop 0\n\t" \
    "global_load_lds_dwordx4 v201, s[44:45] offset:768\n\t" \
    "s_add_u32 m0, s96, 116864\n\t" \
    "s_nop 0\n\t" \
    "global_load_lds_dwordx4 v201, s[44:45] offset:896\n\t" \
    "s_add_u32 m0, s96, 117760\n\t" \
    "s_nop 0\n\t" \
    "global_load_lds_dwordx4 v201, s[44:45] offset:1024\n\t" \
    "v_add_u32 v201, 36864, v201\n\t" \
    "v_add_u32 v202, -98304, v202\n\t" \
    "v_add_u32 v203, -98304, v203\n\t" \
    "ds_read_b64_tr_b16 a[56:57], v202 offset:0\n\t" \
    "ds_read_b64_tr_b16 a[58:59], v202 offset:1024\n\t" \
    "ds_read_b64_tr_b16 a[60:61], v202 offset:8192\n\t" \
    "ds_read_b64_tr_b16 a[62:63], v202 offset:9216\n\t" \
    "ds_read_b64_tr_b16 a[128:129], v203 offset:0\n\t" \
    "ds_read_b64_tr_b16 a[130:131], v203 offset:1024\n\t" \
    "ds_read_b64_tr_b16 a[132:133], v203 offset:8192\n\t" \
    "ds_read_b64_tr_b16 a[134:135], v203 offset:9216\n\t" \
    "s_waitcnt vmcnt(27) lgkmcnt(0)\n\t" \
    "s_barrier\n\t" \
    "ds_read_b64_tr_b16 a[64:65], v202 offset:16384\n\t" \
    "ds_read_b64_tr_b16 a[66:67], v202 offset:17408\n\t" \
    "ds_read_b64_tr_b16 a[68:69], v202 offset:24576\n\t" \
    "ds_read_b64_tr_b16 a[70:71], v202 offset:25600\n\t" \
    "ds_read_b64_tr_b16 a[136:137], v203 offset:16384\n\t" \
    "ds_read_b64_tr_b16 a[138:139], v203 offset:17408\n\t" \
    "ds_read_b64_tr_b16 a[140:141], v203 offset:24576\n\t" \
    "ds_read_b64_tr_b16 a[142:143], v203 offset:25600\n\t" \
    "s_waitcnt vmcnt(9) lgkmcnt(0)\n\t" \
    "s_barrier\n\t" \
    "s_add_u32 m0, s96, 36864\n\t" \
    "s_nop 0\n\t" \
    "global_load_lds_dwordx4 v201, s[44:45] offset:0\n\t" \
    "s_add_u32 m0, s96, 37760\n\t" \
    "s_nop 0\n\t" \
    "global_load_lds_dwordx4 v201, s[44:45] offset:128\n\t" \
    "s_add_u32 m0, s96, 38656\n\t" \
    "s_nop 0\n\t" \
    "global_load_lds_dwordx4 v201, s[44:45] offset:256\n\t" \
    "s_add_u32 m0, s96, 39552\n\t" \
    "s_nop 0\n\t" \
    "global_load_lds_dwordx4 v201, s[44:45] offset:384\n\t" \
    "s_add_u32 m0, s96, 40448\n\t" \
    "s_nop 0\n\t" \
    "global_load_lds_dwordx4 v201, s[44:45] offset:512\n\t" \
    "s_add_u32 m0, s96, 41344\n\t" \
    "s_nop 0\n\t" \
    "global_load_lds_dwordx4 v201, s[44:45] offset:640\n\t" \
    "s_add_u32 m0, s96, 42240\n\t" \
    "s_nop 0\n\t" \
    "global_load_lds_dwordx4 v201, s[44:45] offset:768\n\t" \
    "s_add_u32 m0, s96, 43136\n\t" \
    "s_nop 0\n\t" \
    "global_load_lds_dwordx4 v201, s[44:45] offset:896\n\t" \
    "s_add_u32 m0, s96, 44032\n\t" \
    "s_nop 0\n\t" \
    "global_load_lds_dwordx4 v201, s[44:45] offset:1024\n\t" \
    "v_add_u32 v201, 36864, v201\n\t" \
    "v_add_u32 v204, 73728, v204\n\t" \
    "v_add_u32 v205, 73728, v205\n\t" \
    "v_add_u32 v206, 73728, v206\n\t" \
    "v_add_u32 v207, 73728, v207\n\t" \
    "ds_read_b64_tr_b16 a[144:145], v204 offset:0\n\t" \
    "ds_read_b64_tr_b16 a[146:147], v204 offset:18432\n\t" \
    "ds_read_b64_tr_b16 a[160:161], v205 offset:0\n\t" \
    "ds_read_b64_tr_b16 a[162:163], v205 offset:18432\n\t" \
    "ds_read_b64_tr_b16 a[176:177], v206 offset:0\n\t" \
    "ds_read_b64_tr_b16 a[178:179], v206 offset:18432\n\t" \
    "s_waitcnt lgkmcnt(6)\n\t" \
    "ds_read_b64_tr_b16 a[192:193], v207 offset:0\n\t" \
    "ds_read_b64_tr_b16 a[194:195], v207 offset:18432\n\t" \
    "ds_read_b64_tr_b16 a[208:209], v204 offset:1024\n\t" \
    "ds_read_b64_tr_b16 a[210:211], v204 offset:19456\n\t" \
    "ds_read_b64_tr_b16 a[224:225], v205 offset:1024\n\t" \
    "ds_read_b64_tr_b16 a[226:227], v205 offset:19456\n\t" \
    "s_waitcnt lgkmcnt(6)\n\t" \
    "ds_read_b64_tr_b16 a[240:241], v206 offset:1024\n\t" \
    "ds_read_b64_tr_b16 a[242:243], v206 offset:19456\n\t" \
    "ds_read_b64_tr_b16 v[224:225], v207 offset:1024\n\t" \
    "ds_read_b64_tr_b16 v[226:227], v207 offset:19456\n\t" \
    "ds_read_b64_tr_b16 v[240:241], v204 offset:2048\n\t" \
    "ds_read_b64_tr_b16 v[242:243], v204 offset:20480\n\t" \
    "s_waitcnt lgkmcnt(6)\n\t" \
    "s_waitcnt lgkmcnt(6)\n\t" \
    "ds_read_b128 v[24:27], v2 offset:0\n\t" \
    "ds_read_b128 v[28:31], v2 offset:18432\n\t" \
    "ds_read_b128 v[32:35], v2 offset:1024\n\t" \
    "ds_read_b128 v[36:39], v2 offset:19456\n\t" \
    "s_waitcnt lgkmcnt(3)\n\t" \
    "v_mfma_f32_16x16x32_bf16 v[72:75], a[0:3], v[24:27], 0\n\t" \
    "ds_read_b128 v[40:43], v2 offset:2048\n\t" \
    "s_waitcnt lgkmcnt(3)\n\t" \
    "v_mfma_f32_16x16x32_bf16 v[76:79], a[0:3], v[28:31], 0\n\t" \
    "ds_read_b128 v[44:47], v2 offset:20480\n\t" \
    "v_mfma_f32_16x16x32_bf16 v[80:83], a[72:75], v[24:27], 0\n\t" \
    "v_mfma_f32_16x16x32_bf16 v[84:87], a[72:75], v[28:31], 0\n\t" \
    "s_waitcnt lgkmcnt(3)\n\t" \
    "v_mfma_f32_16x16x32_bf16 v[72:75], a[4:7], v[32:35], v[72:75]\n\t" \
    "ds_read_b128 v[48:51], v2 offset:3072\n\t" \
    "s_waitcnt lgkmcnt(3)\n\t" \
    "v_mfma_f32_16x16x32_bf16 v[76:79], a[4:7], v[36:39], v[76:79]\n\t" \
    "ds_read_b128 v[52:55], v2 offset:21504\n\t" \
    "v_mfma_f32_16x16x32_bf16 v[80:83], a[76:79], v[32:35], v[80:83]\n\t" \
    "v_mfma_f32_16x16x32_bf16 v[84:87], a[76:79], v[36:39], v[84:87]\n\t" \
    "s_waitcnt lgkmcnt(3)\n\t" \
    "v_mfma_f32_16x16x32_bf16 v[72:75], a[8:11], v[40:43], v[72:75]\n\t" \
    "ds_read_b128 v[56:59], v2 offset:4096\n\t" \
    "s_waitcnt lgkmcnt(3)\n\t" \
    "v_mfma_f32_16x16x32_bf16 v[76:79], a[8:11], v[44:47], v[76:79]\n\t" \
    "ds_read_b128 v[60:63], v2 offset:22528\n\t" \
    "v_mfma_f32_16x16x32_bf16 v[80:83], a[80:83], v[40:43], v[80:83]\n\t" \
    "v_mfma_f32_16x16x32_bf16 v[84:87], a[80:83], v[44:47], v[84:87]\n\t" \
    "s_waitcnt lgkmcnt(3)\n\t" \
    "v_mfma_f32_16x16x32_bf16 v[72:75], a[12:15], v[48:51], v[72:75]\n\t" \
    "ds_read_b128 v[64:67], v2 offset:5120\n\t" \
    "s_waitcnt lgkmcnt(3)\n\t" \
    "v_mfma_f32_16x16x32_bf16 v[76:79], a[12:15], v[52:55], v[76:79]\n\t" \
    "ds_read_b128 v[68:71], v2 offset:23552\n\t" \
    "v_mfma_f32_16x16x32_bf16 v[80:83], a[84:87], v[48:51], v[80:83]\n\t" \
    "v_mfma_f32_16x16x32_bf16 v[84:87], a[84:87], v[52:55], v[84:87]\n\t" \
    "s_waitcnt lgkmcnt(3)\n\t" \
    "v_mfma_f32_16x16x32_bf16 v[72:75], a[16:19], v[56:59], v[72:75]\n\t" \
    "s_waitcnt lgkmcnt(2)\n\t" \
    "v_mfma_f32_16x16x32_bf16 v[76:79], a[16:19], v[60:63], v[76:79]\n\t" \
    "v_mfma_f32_16x16x32_bf16 v[80:83], a[88:91], v[56:59], v[80:83]\n\t" \
    "v_mfma_f32_16x16x32_bf16 v[84:87], a[88:91], v[60:63], v[84:87]\n\t" \
    "s_waitcnt lgkmcnt(1)\n\t" \
    "v_mfma_f32_16x16x32_bf16 v[72:75], a[20:23], v[64:67], v[72:75]\n\t" \
    "s_waitcnt lgkmcnt(0)\n\t" \
    "v_mfma_f32_16x16x32_bf16 v[76:79], a[20:23], v[68:71], v[76:79]\n\t" \
    "v_mfma_f32_16x16x32_bf16 v[80:83], a[92:95], v[64:67], v[80:83]\n\t" \
    "v_mfma_f32_16x16x32_bf16 v[84:87], a[92:95], v[68:71], v[84:87]\n\t" \
    "s_waitcnt vmcnt(9) lgkmcnt(0)\n\t" \
    "s_barrier\n\t" \
    "s_add_u32 m0, s96, 73728\n\t" \
    "s_nop 0\n\t" \
    "global_load_lds_dwordx4 v201, s[44:45] offset:0\n\t" \
    "s_add_u32 m0, s96, 74624\n\t" \
    "s_nop 0\n\t" \
    "global_load_lds_dwordx4 v201, s[44:45] offset:128\n\t" \
    "s_add_u32 m0, s96, 75520\n\t" \
    "s_nop 0\n\t" \
    "global_load_lds_dwordx4 v201, s[44:45] offset:256\n\t" \
    "s_add_u32 m0, s96, 76416\n\t" \
    "s_nop 0\n\t" \
    "global_load_lds_dwordx4 v201, s[44:45] offset:384\n\t" \
    "s_add_u32 m0, s96, 77312\n\t" \
    "s_nop 0\n\t" \
    "global_load_lds_dwordx4 v201, s[44:45] offset:512\n\t" \
    "s_add_u32 m0, s96, 78208\n\t" \
    "s_nop 0\n\t" \
    "global_load_lds_dwordx4 v201, s[44:45] offset:640\n\t" \
    "s_add_u32 m0, s96, 79104\n\t" \
    "s_nop 0\n\t" \
    "global_load_lds_dwordx4 v201, s[44:45] offset:768\n\t" \
    "s_add_u32 m0, s96, 80000\n\t" \
    "s_nop 0\n\t" \
    "global_load_lds_dwordx4 v201, s[44:45] offset:896\n\t" \
    "s_add_u32 m0, s96, 80896\n\t" \
    "s_nop 0\n\t" \
    "global_load_lds_dwordx4 v201, s[44:45] offset:1024\n\t" \
    "v_add_u32 v201, 36864, v201\n\t" \
    "v_mbcnt_lo_u32_b32 v164, -1, 0\n\t" \
    "v_mbcnt_hi_u32_b32 v164, -1, v164\n\t" \
    "s_cmp_eq_u32 s66, 0\n\t" \
    "s_cselect_b32 s69, 59, 11\n\t" \
    "v_min_u32 v164, s69, v164\n\t" \
    "v_lshlrev_b32 v164, 6, v164\n\t" \
    "v_add_u32 v164, 6258688, v164\n\t" \
    "v_mov_b32 v165, 6266880\n\t" \
    "global_load_dword v165, v165, s[36:37] sc1\n\t" \
    "global_load_dwordx2 v[166:167], v164, s[36:37] sc1\n\t" \
    "ds_read_b64_tr_b16 a[148:149], v204 offset:36864\n\t" \
    "ds_read_b64_tr_b16 a[150:151], v204 offset:55296\n\t" \
    "ds_read_b64_tr_b16 a[164:165], v205 offset:36864\n\t" \
    "ds_read_b64_tr_b16 a[166:167], v205 offset:55296\n\t" \
    "ds_read_b64_tr_b16 a[180:181], v206 offset:36864\n\t" \
    "ds_read_b64_tr_b16 a[182:183], v206 offset:55296\n\t" \
    "s_waitcnt lgkmcnt(6)\n\t" \
    "ds_read_b64_tr_b16 a[196:197], v207 offset:36864\n\t" \
    "ds_read_b64_tr_b16 a[198:199], v207 offset:55296\n\t" \
    "ds_read_b64_tr_b16 a[212:213], v204 offset:37888\n\t" \
    "ds_read_b64_tr_b16 a[214:215], v204 offset:56320\n\t" \
    "ds_read_b64_tr_b16 a[228:229], v205 offset:37888\n\t" \
    "ds_read_b64_tr_b16 a[230:231], v205 offset:56320\n\t" \
    "s_waitcnt lgkmcnt(6)\n\t" \
    "ds_read_b64_tr_b16 a[244:245], v206 offset:37888\n\t" \
    "ds_read_b64_tr_b16 a[246:247], v206 offset:56320\n\t" \
    "ds_read_b64_tr_b16 v[228:229], v207 offset:37888\n\t" \
    "ds_read_b64_tr_b16 v[230:231], v207 offset:56320\n\t" \
    "ds_read_b64_tr_b16 v[244:245], v204 offset:38912\n\t" \
    "ds_read_b64_tr_b16 v[246:247], v204 offset:57344\n\t" \
    "s_waitcnt lgkmcnt(6)\n\t" \
    "s_waitcnt lgkmcnt(6)\n\t" \
    "ds_read_b128 v[24:27], v2 offset:6144\n\t" \
    "ds_read_b128 v[28:31], v2 offset:24576\n\t" \
    "ds_read_b128 v[32:35], v2 offset:7168\n\t" \
    "ds_read_b128 v[36:39], v2 offset:25600\n\t" \
    "s_waitcnt lgkmcnt(3)\n\t" \
    "v_mfma_f32_16x16x32_bf16 v[72:75], a[24:27], v[24:27], v[72:75]\n\t" \
    "ds_read_b128 v[40:43], v2 offset:8192\n\t" \
    "s_waitcnt lgkmcnt(3)\n\t" \
    "v_mfma_f32_16x16x32_bf16 v[76:79], a[24:27], v[28:31], v[76:79]\n\t" \
    "ds_read_b128 v[44:47], v2 offset:26624\n\t" \
    "v_mfma_f32_16x16x32_bf16 v[80:83], a[96:99], v[24:27], v[80:83]\n\t" \
    "v_mfma_f32_16x16x32_bf16 v[84:87], a[96:99], v[28:31], v[84:87]\n\t" \
    "s_waitcnt lgkmcnt(3)\n\t" \
    "v_mfma_f32_16x16x32_bf16 v[72:75], a[28:31], v[32:35], v[72:75]\n\t" \
    "ds_read_b128 v[48:51], v2 offset:9216\n\t" \
    "s_waitcnt lgkmcnt(3)\n\t" \
    "v_mfma_f32_16x16x32_bf16 v[76:79], a[28:31], v[36:39], v[76:79]\n\t" \
    "ds_read_b128 v[52:55], v2 offset:27648\n\t" \
    "v_mfma_f32_16x16x32_bf16 v[80:83], a[100:103], v[32:35], v[80:83]\n\t" \
    "v_mfma_f32_16x16x32_bf16 v[84:87], a[100:103], v[36:39], v[84:87]\n\t" \
    "s_waitcnt lgkmcnt(3)\n\t" \
    "v_mfma_f32_16x16x32_bf16 v[72:75], a[32:35], v[40:43], v[72:75]\n\t" \
    "ds_read_b128 v[56:59], v2 offset:10240\n\t" \
    "s_waitcnt lgkmcnt(3)\n\t" \
    "v_mfma_f32_16x16x32_bf16 v[76:79], a[32:35], v[44:47], v[76:79]\n\t" \
    "ds_read_b128 v[60:63], v2 offset:28672\n\t" \
    "v_mfma_f32_16x16x32_bf16 v[80:83], a[104:107], v[40:43], v[80:83]\n\t" \
    "v_mfma_f32_16x16x32_bf16 v[84:87], a[104:107], v[44:47], v[84:87]\n\t" \
    "s_waitcnt lgkmcnt(3)\n\t" \
    "v_mfma_f32_16x16x32_bf16 v[72:75], a[36:39], v[48:51], v[72:75]\n\t" \
    "ds_read_b128 v[64:67], v2 offset:11264\n\t" \
    "s_waitcnt lgkmcnt(3)\n\t" \
    "v_mfma_f32_16x16x32_bf16 v[76:79], a[36:39], v[52:55], v[76:79]\n\t" \
    "ds_read_b128 v[68:71], v2 offset:29696\n\t" \
    "v_mfma_f32_16x16x32_bf16 v[80:83], a[108:111], v[48:51], v[80:83]\n\t" \
    "v_mfma_f32_16x16x32_bf16 v[84:87], a[108:111], v[52:55], v[84:87]\n\t" \
    "s_waitcnt lgkmcnt(3)\n\t" \
    "v_mfma_f32_16x16x32_bf16 v[72:75], a[40:43], v[56:59], v[72:75]\n\t" \
    "s_waitcnt lgkmcnt(2)\n\t" \
    "v_mfma_f32_16x16x32_bf16 v[76:79], a[40:43], v[60:63], v[76:79]\n\t" \
    "v_mfma_f32_16x16x32_bf16 v[80:83], a[112:115], v[56:59], v[80:83]\n\t" \
    "v_mfma_f32_16x16x32_bf16 v[84:87], a[112:115], v[60:63], v[84:87]\n\t" \
    "s_waitcnt lgkmcnt(1)\n\t" \
    "v_mfma_f32_16x16x32_bf16 v[72:75], a[44:47], v[64:67], v[72:75]\n\t" \
    "s_waitcnt lgkmcnt(0)\n\t" \
    "v_mfma_f32_16x16x32_bf16 v[76:79], a[44:47], v[68:71], v[76:79]\n\t" \
    "v_mfma_f32_16x16x32_bf16 v[80:83], a[116:119], v[64:67], v[80:83]\n\t" \
    "v_mfma_f32_16x16x32_bf16 v[84:87], a[116:119], v[68:71], v[84:87]\n\t" \
    "s_waitcnt vmcnt(11) lgkmcnt(0)\n\t" \
    "s_barrier\n\t" \
    "s_add_u32 s50, s50, 73728\n\t" \
    "s_addc_u32 s51, s51, 0\n\t" \
    "s_add_u32 m0, s89, 110592\n\t" \
    "s_nop 0\n\t" \
    "global_load_lds_dwordx4 v1, s[50:51] offset:0\n\t" \
    "s_add_u32 m0, s89, 111552\n\t" \
    "s_nop 0\n\t" \
    "global_load_lds_dwordx4 v1, s[50:51] offset:64\n\t" \
    "s_add_u32 m0, s89, 112512\n\t" \
    "s_nop 0\n\t" \
    "global_load_lds_dwordx4 v1, s[50:51] offset:128\n\t" \
    "s_add_u32 m0, s89, 113472\n\t" \
    "s_nop 0\n\t" \
    "global_load_lds_dwordx4 v1, s[50:51] offset:192\n\t" \
    "s_add_u32 m0, s89, 114432\n\t" \
    "s_nop 0\n\t" \
    "global_load_lds_dwordx4 v1, s[50:51] offset:256\n\t" \
    "s_add_u32 m0, s89, 115392\n\t" \
    "s_nop 0\n\t" \
    "global_load_lds_dwordx4 v1, s[50:51] offset:320\n\t" \
    "s_add_u32 m0, s89, 116352\n\t" \
    "s_nop 0\n\t" \
    "global_load_lds_dwordx4 v1, s[50:51] offset:384\n\t" \
    "s_add_u32 m0, s89, 117312\n\t" \
    "s_nop 0\n\t" \
    "global_load_lds_dwordx4 v1, s[50:51] offset:448\n\t" \
    "s_add_u32 m0, s89, 118272\n\t" \
    "s_nop 0\n\t" \
    "global_load_lds_dwordx4 v1, s[50:51] offset:512\n\t" \
    "v_add_u32 v204, -36864, v204\n\t" \
    "v_add_u32 v205, -36864, v205\n\t" \
    "v_add_u32 v206, -36864, v206\n\t" \
    "v_add_u32 v207, -36864, v207\n\t" \
    "ds_read_b64_tr_b16 a[152:153], v204 offset:0\n\t" \
    "ds_read_b64_tr_b16 a[154:155], v204 offset:18432\n\t" \
    "ds_read_b64_tr_b16 a[168:169], v205 offset:0\n\t" \
    "ds_read_b64_tr_b16 a[170:171], v205 offset:18432\n\t" \
    "ds_read_b64_tr_b16 a[184:185], v206 offset:0\n\t" \
    "ds_read_b64_tr_b16 a[186:187], v206 offset:18432\n\t" \
    "s_waitcnt lgkmcnt(6)\n\t" \
    "ds_read_b64_tr_b16 a[200:201], v207 offset:0\n\t" \
    "ds_read_b64_tr_b16 a[202:203], v207 offset:18432\n\t" \
    "ds_read_b64_tr_b16 a[216:217], v204 offset:1024\n\t" \
    "ds_read_b64_tr_b16 a[218:219], v204 offset:19456\n\t" \
    "ds_read_b64_tr_b16 a[232:233], v205 offset:1024\n\t" \
    "ds_read_b64_tr_b16 a[234:235], v205 offset:19456\n\t" \
    "s_waitcnt lgkmcnt(6)\n\t" \
    "ds_read_b64_tr_b16 a[248:249], v206 offset:1024\n\t" \
    "ds_read_b64_tr_b16 a[250:251], v206 offset:19456\n\t" \
    "ds_read_b64_tr_b16 v[232:233], v207 offset:1024\n\t" \
    "ds_read_b64_tr_b16 v[234:235], v207 offset:19456\n\t" \
    "ds_read_b64_tr_b16 v[248:249], v204 offset:2048\n\t" \
    "ds_read_b64_tr_b16 v[250:251], v204 offset:20480\n\t" \
    "s_waitcnt lgkmcnt(6)\n\t" \
    "s_waitcnt lgkmcnt(6)\n\t" \
    "ds_read_b128 v[24:27], v2 offset:12288\n\t" \
    "ds_read_b128 v[28:31], v2 offset:30720\n\t" \
    "ds_read_b128 v[32:35], v2 offset:13312\n\t" \
    "ds_read_b128 v[36:39], v2 offset:31744\n\t" \
    "s_waitcnt lgkmcnt(3)\n\t" \
    "v_mfma_f32_16x16x32_bf16 v[72:75], a[48:51], v[24:27], v[72:75]\n\t" \
    "ds_read_b128 v[40:43], v2 offset:14336\n\t" \
    "s_waitcnt lgkmcnt(3)\n\t" \
    "v_mfma_f32_16x16x32_bf16 v[76:79], a[48:51], v[28:31], v[76:79]\n\t" \
    "ds_read_b128 v[44:47], v2 offset:32768\n\t" \
    "v_mfma_f32_16x16x32_bf16 v[80:83], a[120:123], v[24:27], v[80:83]\n\t" \
    "v_mfma_f32_16x16x32_bf16 v[84:87], a[120:123], v[28:31], v[84:87]\n\t" \
    "s_waitcnt lgkmcnt(3)\n\t" \
    "v_mfma_f32_16x16x32_bf16 v[72:75], a[52:55], v[32:35], v[72:75]\n\t" \
    "ds_read_b128 v[48:51], v2 offset:15360\n\t" \
    "s_waitcnt lgkmcnt(3)\n\t" \
    "v_mfma_f32_16x16x32_bf16 v[76:79], a[52:55], v[36:39], v[76:79]\n\t" \
    "ds_read_b128 v[52:55], v2 offset:33792\n\t" \
    "v_mfma_f32_16x16x32_bf16 v[80:83], a[124:127], v[32:35], v[80:83]\n\t" \
    "v_mfma_f32_16x16x32_bf16 v[84:87], a[124:127], v[36:39], v[84:87]\n\t" \
    "s_waitcnt lgkmcnt(3)\n\t" \
    "v_mfma_f32_16x16x32_bf16 v[72:75], a[56:59], v[40:43], v[72:75]\n\t" \
    "ds_read_b128 v[56:59], v2 offset:16384\n\t" \
    "s_waitcnt lgkmcnt(3)\n\t" \
    "v_mfma_f32_16x16x32_bf16 v[76:79], a[56:59], v[44:47], v[76:79]\n\t" \
    "ds_read_b128 v[60:63], v2 offset:34816\n\t" \
    "v_mfma_f32_16x16x32_bf16 v[80:83], a[128:131], v[40:43], v[80:83]\n\t" \
    "v_mfma_f32_16x16x32_bf16 v[84:87], a[128:131], v[44:47], v[84:87]\n\t" \
    "s_waitcnt lgkmcnt(3)\n\t" \
    "v_mfma_f32_16x16x32_bf16 v[72:75], a[60:63], v[48:51], v[72:75]\n\t" \
    "ds_read_b128 v[64:67], v2 offset:17408\n\t" \
    "s_waitcnt lgkmcnt(3)\n\t" \
    "v_mfma_f32_16x16x32_bf16 v[76:79], a[60:63], v[52:55], v[76:79]\n\t" \
    "ds_read_b128 v[68:71], v2 offset:35840\n\t" \
    "v_mfma_f32_16x16x32_bf16 v[80:83], a[132:135], v[48:51], v[80:83]\n\t" \
    "v_mfma_f32_16x16x32_bf16 v[84:87], a[132:135], v[52:55], v[84:87]\n\t" \
    "s_waitcnt lgkmcnt(3)\n\t" \
    "v_mfma_f32_16x16x32_bf16 v[72:75], a[64:67], v[56:59], v[72:75]\n\t" \
    "s_waitcnt lgkmcnt(2)\n\t" \
    "v_mfma_f32_16x16x32_bf16 v[76:79], a[64:67], v[60:63], v[76:79]\n\t" \
    "v_mfma_f32_16x16x32_bf16 v[80:83], a[136:139], v[56:59], v[80:83]\n\t" \
    "v_mfma_f32_16x16x32_bf16 v[84:87], a[136:139], v[60:63], v[84:87]\n\t" \
    "s_waitcnt lgkmcnt(1)\n\t" \
    "v_mfma_f32_16x16x32_bf16 v[72:75], a[68:71], v[64:67], v[72:75]\n\t" \
    "s_waitcnt lgkmcnt(0)\n\t" \
    "v_mfma_f32_16x16x32_bf16 v[76:79], a[68:71], v[68:71], v[76:79]\n\t" \
    "v_mfma_f32_16x16x32_bf16 v[80:83], a[140:143], v[64:67], v[80:83]\n\t" \
    "v_mfma_f32_16x16x32_bf16 v[84:87], a[140:143], v[68:71], v[84:87]\n\t" \
    "s_waitcnt vmcnt(11) lgkmcnt(0)\n\t" \
    "s_barrier\n\t" \
    "ds_read_b64_tr_b16 a[156:157], v204 offset:36864\n\t" \
    "ds_read_b64_tr_b16 a[158:159], v204 offset:55296\n\t" \
    "ds_read_b64_tr_b16 a[172:173], v205 offset:36864\n\t" \
    "ds_read_b64_tr_b16 a[174:175], v205 offset:55296\n\t" \
    "ds_read_b64_tr_b16 a[188:189], v206 offset:36864\n\t" \
    "ds_read_b64_tr_b16 a[190:191], v206 offset:55296\n\t" \
    "s_waitcnt lgkmcnt(6)\n\t" \
    "ds_read_b64_tr_b16 a[204:205], v207 offset:36864\n\t" \
    "ds_read_b64_tr_b16 a[206:207], v207 offset:55296\n\t" \
    "ds_read_b64_tr_b16 a[220:221], v204 offset:37888\n\t" \
    "ds_read_b64_tr_b16 a[222:223], v204 offset:56320\n\t" \
    "ds_read_b64_tr_b16 a[236:237], v205 offset:37888\n\t" \
    "ds_read_b64_tr_b16 a[238:239], v205 offset:56320\n\t" \
    "s_waitcnt lgkmcnt(6)\n\t" \
    "ds_read_b64_tr_b16 a[252:253], v206 offset:37888\n\t" \
    "ds_read_b64_tr_b16 a[254:255], v206 offset:56320\n\t" \
    "ds_read_b64_tr_b16 v[236:237], v207 offset:37888\n\t" \
    "ds_read_b64_tr_b16 v[238:239], v207 offset:56320\n\t" \
    "ds_read_b64_tr_b16 v[252:253], v204 offset:38912\n\t" \
    "ds_read_b64_tr_b16 v[254:255], v204 offset:57344\n\t" \
    "s_waitcnt lgkmcnt(6)\n\t" \
    "s_waitcnt vmcnt(9) lgkmcnt(0)\n\t" \
    "s_barrier\n\t" \
    "v_sub_u32 v166, v166, v165\n\t" \
    "v_cmp_ne_u32 vcc, 1, v166\n\t" \
    "s_cbranch_vccz gv_Cok2_%=\n\t" \
    "gv_C1_%=:\n\t" \
    "global_load_dwordx2 v[166:167], v164, s[36:37] sc1\n\t" \
    "s_waitcnt vmcnt(0)\n\t" \
    "v_sub_u32 v166, v166, v165\n\t" \
    "v_cmp_ne_u32 vcc, 1, v166\n\t" \
    "s_cbranch_vccz gv_Cok2_%=\n\t" \
    "s_cmp_eq_u32 s68, 0\n\t" \
    "s_cbranch_scc1 gv_F3_%=\n\t" \
    "s_sub_u32 s68, s68, 1\n\t" \
    "s_sleep 4\n\t" \
    "s_branch gv_C1_%=\n\t" \
    "gv_F3_%=:\n\t" \
    "v_mov_b32 v168, 6095104\n\t" \
    "v_mov_b32 v169, 4\n\t" \
    "global_store_dword v168, v169, s[36:37] sc1\n\t" \
    "s_waitcnt vmcnt(0)\n\t" \
    "s_branch gv_Cok2_%=\n\t" \
    "gv_Cok2_%=:\n\t" \
    "s_cmp_lg_u32 s66, 0\n\t" \
    "s_cbranch_scc1 gv_Ca4_%=\n\t" \
    "v_mov_b32 v164, 6176768\n\t" \
    "s_mov_b64 exec, 1\n\t" \
    "global_atomic_add v164, v166, s[36:37] sc1\n\t" \
    "s_mov_b64 exec, -1\n\t" \
    "gv_Ca4_%=:\n\t" \
    "v_min_u32_dpp v167, v167, v167 row_shl:6 row_mask:0xf bank_mask:0xf\n\t" \
    "s_nop 0\n\t" \
    "v_readlane_b32 s69, v167, 4\n\t" \
    "v_readlane_b32 s70, v167, 5\n\t" \
    "s_nop 1\n\t" \
    "s_cmp_lt_u32 s70, s69\n\t" \
    "s_cselect_b32 s52, 8192, 0\n\t" \
    "s_xor_b32 s52, s52, 8192\n\t" \
    "v_xor_b32 v20, s52, v20\n\t" \
    "s_lshr_b32 s69, s67, 5\n\t" \
    "s_or_b32 s69, s69, s66\n\t" \
    "s_cbranch_scc1 gv_npr5_%=\n\t" \
    "s_memrealtime s[54:55]\n\t" \
    "s_waitcnt lgkmcnt(0)\n\t" \
    "s_lshl_b32 s70, s54, 4\n\t" \
    "s_add_u32 s70, s70, 3840\n\t" \
    "v_mov_b32 v157, s70\n\t" \
    "s_mov_b32 exec_lo, 0xf00f0\n\t" \
    "s_mov_b32 exec_hi, 0xf00f0\n\t" \
    "global_store_dword v19, v157, s[36:37] sc1\n\t" \
    "s_mov_b64 exec, -1\n\t" \
    "gv_npr5_%=:\n\t" \
    "s_cmp_lt_u32 s65, 60\n\t" \
    "s_cselect_b32 s69, 73728, 0\n\t" \
    "s_add_u32 s50, s50, s69\n\t" \
    "s_addc_u32 s51, s51, 0\n\t" \
    "s_sub_u32 s90, s90, 36864\n\t" \
    "s_cselect_b32 s90, 110592, s90\n\t" \
    "s_sub_u32 s91, s90, 36864\n\t" \
    "s_cselect_b32 s91, 110592, s91\n\t" \
    "s_add_u32 s91, s91, s89\n\t" \
    "v_add_u32 v17, s90, v3\n\t" \
    "s_lshl_b32 s40, s65, 15\n\t" \
    "v_mov_b32 v2, v17\n\t" \
    "s_mov_b64 s[46:47], 0\n\t" \
    "s_branch gv_mid8_%=\n\t" \
    ".p2align 6\n\t" \
    "s_nop 0\n\t" \
    "s_nop 0\n\t" \
    "gv_loop6_%=:\n\t" \
    "s_waitcnt lgkmcnt(5)\n\t" \
    "v_mfma_f32_16x16x32_bf16 v[72:75], a[0:3], v[24:27], 0\n\t" \
    "ds_read_b128 v[48:51], v2 offset:3072\n\t" \
    "s_waitcnt lgkmcnt(5)\n\t" \
    "v_mfma_f32_16x16x32_bf16 v[76:79], a[0:3], v[28:31], 0\n\t" \
    "ds_read_b128 v[52:55], v2 offset:21504\n\t" \
    "v_mfma_f32_16x16x32_bf16 v[80:83], a[72:75], v[24:27], 0\n\t" \
    "s_cmp_lt_u32 s65, 60\n\t" \
    "s_cselect_b32 s69, 73728, 0\n\t" \
    "s_add_u32 s50, s50, s69\n\t" \
    "v_mfma_f32_16x16x32_bf16 v[84:87], a[72:75], v[28:31], 0\n\t" \
    "s_addc_u32 s51, s51, 0\n\t" \
    "s_sub_u32 s90, s90, 36864\n\t" \
    "s_cselect_b32 s90, 110592, s90\n\t" \
    "s_waitcnt lgkmcnt(5)\n\t" \
    "v_mfma_f32_16x16x32_bf16 v[72:75], a[4:7], v[32:35], v[72:75]\n\t" \
    "ds_read_b128 v[56:59], v2 offset:4096\n\t" \
    "s_waitcnt lgkmcnt(5)\n\t" \
    "v_mfma_f32_16x16x32_bf16 v[76:79], a[4:7], v[36:39], v[76:79]\n\t" \
    "ds_read_b128 v[60:63], v2 offset:22528\n\t" \
    "v_mfma_f32_16x16x32_bf16 v[80:83], a[76:79], v[32:35], v[80:83]\n\t" \
    "s_sub_u32 s91, s90, 36864\n\t" \
    "s_cselect_b32 s91, 110592, s91\n\t" \
    "s_add_u32 s91, s91, s89\n\t" \
    "v_mfma_f32_16x16x32_bf16 v[84:87], a[76:79], v[36:39], v[84:87]\n\t" \
    "v_add_u32 v17, s90, v3\n\t" \
    "s_lshl_b32 s40, s65, 15\n\t" \
    "s_sub_u32 s70, s65, 4\n\t" \
    "s_waitcnt lgkmcnt(5)\n\t" \
    "v_mfma_f32_16x16x32_bf16 v[72:75], a[8:11], v[40:43], v[72:75]\n\t" \
    "ds_read_b128 v[64:67], v2 offset:5120\n\t" \
    "s_waitcnt lgkmcnt(5)\n\t" \
    "v_mfma_f32_16x16x32_bf16 v[76:79], a[8:11], v[44:47], v[76:79]\n\t" \
    "ds_read_b128 v[68:71], v2 offset:23552\n\t" \
    "v_mfma_f32_16x16x32_bf16 v[80:83], a[80:83], v[40:43], v[80:83]\n\t" \
    "v_mov_b32 v157, s70\n\t" \
    "s_cmp_lt_u32 s65, 14\n\t" \
    "s_cselect_b64 s[46:47], 0, s[58:59]\n\t" \
    "v_mfma_f32_16x16x32_bf16 v[84:87], a[80:83], v[44:47], v[84:87]\n\t" \
    "s_waitcnt lgkmcnt(5)\n\t" \
    "v_mfma_f32_16x16x32_bf16 v[72:75], a[12:15], v[48:51], v[72:75]\n\t" \
    "ds_read_b128 v[24:27], v2 offset:6144\n\t" \
    "s_waitcnt lgkmcnt(5)\n\t" \
    "v_mfma_f32_16x16x32_bf16 v[76:79], a[12:15], v[52:55], v[76:79]\n\t" \
    "ds_read_b128 v[28:31], v2 offset:24576\n\t" \
    "v_mfma_f32_16x16x32_bf16 v[80:83], a[84:87], v[48:51], v[80:83]\n\t" \
    "s_add_u32 m0, s91, 0\n\t" \
    "v_mfma_f32_16x16x32_bf16 v[84:87], a[84:87], v[52:55], v[84:87]\n\t" \
    "global_load_lds_dwordx4 v1, s[50:51] offset:0\n\t" \
    "s_waitcnt lgkmcnt(5)\n\t" \
    "v_mfma_f32_16x16x32_bf16 v[72:75], a[16:19], v[56:59], v[72:75]\n\t" \
    "ds_read_b128 v[32:35], v2 offset:7168\n\t" \
    "s_waitcnt lgkmcnt(5)\n\t" \
    "v_mfma_f32_16x16x32_bf16 v[76:79], a[16:19], v[60:63], v[76:79]\n\t" \
    "ds_read_b128 v[36:39], v2 offset:25600\n\t" \
    "v_mfma_f32_16x16x32_bf16 v[80:83], a[88:91], v[56:59], v[80:83]\n\t" \
    "s_add_u32 m0, s91, 960\n\t" \
    "v_mfma_f32_16x16x32_bf16 v[84:87], a[88:91], v[60:63], v[84:87]\n\t" \
    "global_load_lds_dwordx4 v1, s[50:51] offset:64\n\t" \
    "s_waitcnt lgkmcnt(5)\n\t" \
    "v_mfma_f32_16x16x32_bf16 v[72:75], a[20:23], v[64:67], v[72:75]\n\t" \
    "ds_read_b128 v[40:43], v2 offset:8192\n\t" \
    "s_waitcnt lgkmcnt(5)\n\t" \
    "v_mfma_f32_16x16x32_bf16 v[76:79], a[20:23], v[68:71], v[76:79]\n\t" \
    "ds_read_b128 v[44:47], v2 offset:26624\n\t" \
    "v_mfma_f32_16x16x32_bf16 v[80:83], a[92:95], v[64:67], v[80:83]\n\t" \
    "s_add_u32 m0, s91, 1920\n\t" \
    "v_mfma_f32_16x16x32_bf16 v[84:87], a[92:95], v[68:71], v[84:87]\n\t" \
    "global_load_lds_dwordx4 v1, s[50:51] offset:128\n\t" \
    "s_waitcnt lgkmcnt(5)\n\t" \
    "v_mfma_f32_16x16x32_bf16 v[72:75], a[24:27], v[24:27], v[72:75]\n\t" \
    "ds_read_b128 v[48:51], v2 offset:9216\n\t" \
    "s_waitcnt lgkmcnt(5)\n\t" \
    "v_mfma_f32_16x16x32_bf16 v[76:79], a[24:27], v[28:31], v[76:79]\n\t" \
    "ds_read_b128 v[52:55], v2 offset:27648\n\t" \
    "v_mfma_f32_16x16x32_bf16 v[80:83], a[96:99], v[24:27], v[80:83]\n\t" \
    "s_add_u32 m0, s91, 2880\n\t" \
    "v_mfma_f32_16x16x32_bf16 v[84:87], a[96:99], v[28:31], v[84:87]\n\t" \
    "global_load_lds_dwordx4 v1, s[50:51] offset:192\n\t" \
    "s_waitcnt lgkmcnt(5)\n\t" \
    "v_mfma_f32_16x16x32_bf16 v[72:75], a[28:31], v[32:35], v[72:75]\n\t" \
    "ds_read_b128 v[56:59], v2 offset:10240\n\t" \
    "s_waitcnt lgkmcnt(5)\n\t" \
    "v_mfma_f32_16x16x32_bf16 v[76:79], a[28:31], v[36:39], v[76:79]\n\t" \
    "ds_read_b128 v[60:63], v2 offset:28672\n\t" \
    "v_mfma_f32_16x16x32_bf16 v[80:83], a[100:103], v[32:35], v[80:83]\n\t" \
    "s_add_u32 m0, s91, 3840\n\t" \
    "v_mfma_f32_16x16x32_bf16 v[84:87], a[100:103], v[36:39], v[84:87]\n\t" \
    "global_load_lds_dwordx4 v1, s[50:51] offset:256\n\t" \
    "s_waitcnt lgkmcnt(5)\n\t" \
    "v_mfma_f32_16x16x32_bf16 v[72:75], a[32:35], v[40:43], v[72:75]\n\t" \
    "ds_read_b128 v[64:67], v2 offset:11264\n\t" \
    "s_waitcnt lgkmcnt(5)\n\t" \
    "v_mfma_f32_16x16x32_bf16 v[76:79], a[32:35], v[44:47], v[76:79]\n\t" \
    "ds_read_b128 v[68:71], v2 offset:29696\n\t" \
    "v_mfma_f32_16x16x32_bf16 v[80:83], a[104:107], v[40:43], v[80:83]\n\t" \
    "s_add_u32 m0, s91, 4800\n\t" \
    "v_mfma_f32_16x16x32_bf16 v[84:87], a[104:107], v[44:47], v[84:87]\n\t" \
    "global_load_lds_dwordx4 v1, s[50:51] offset:320\n\t" \
    "s_waitcnt lgkmcnt(5)\n\t" \
    "v_mfma_f32_16x16x32_bf16 v[72:75], a[36:39], v[48:51], v[72:75]\n\t" \
    "ds_read_b128 v[24:27], v2 offset:12288\n\t" \
    "s_waitcnt lgkmcnt(5)\n\t" \
    "v_mfma_f32_16x16x32_bf16 v[76:79], a[36:39], v[52:55], v[76:79]\n\t" \
    "ds_read_b128 v[28:31], v2 offset:30720\n\t" \
    "v_mfma_f32_16x16x32_bf16 v[80:83], a[108:111], v[48:51], v[80:83]\n\t" \
    "s_add_u32 m0, s91, 5760\n\t" \
    "v_mfma_f32_16x16x32_bf16 v[84:87], a[108:111], v[52:55], v[84:87]\n\t" \
    "global_load_lds_dwordx4 v1, s[50:51] offset:384\n\t" \
    "s_waitcnt lgkmcnt(5)\n\t" \
    "v_mfma_f32_16x16x32_bf16 v[72:75], a[40:43], v[56:59], v[72:75]\n\t" \
    "ds_read_b128 v[32:35], v2 offset:13312\n\t" \
    "s_waitcnt lgkmcnt(5)\n\t" \
    "v_mfma_f32_16x16x32_bf16 v[76:79], a[40:43], v[60:63], v[76:79]\n\t" \
    "ds_read_b128 v[36:39], v2 offset:31744\n\t" \
    "v_mfma_f32_16x16x32_bf16 v[80:83], a[112:115], v[56:59], v[80:83]\n\t" \
    "s_add_u32 m0, s91, 6720\n\t" \
    "v_mfma_f32_16x16x32_bf16 v[84:87], a[112:115], v[60:63], v[84:87]\n\t" \
    "global_load_lds_dwordx4 v1, s[50:51] offset:448\n\t" \
    "s_waitcnt lgkmcnt(5)\n\t" \
    "v_mfma_f32_16x16x32_bf16 v[72:75], a[44:47], v[64:67], v[72:75]\n\t" \
    "ds_read_b128 v[40:43], v2 offset:14336\n\t" \
    "s_waitcnt lgkmcnt(5)\n\t" \
    "v_mfma_f32_16x16x32_bf16 v[76:79], a[44:47], v[68:71], v[76:79]\n\t" \
    "ds_read_b128 v[44:47], v2 offset:32768\n\t" \
    "v_mfma_f32_16x16x32_bf16 v[80:83], a[116:119], v[64:67], v[80:83]\n\t" \
    "s_add_u32 m0, s91, 7680\n\t" \
    "v_mfma_f32_16x16x32_bf16 v[84:87], a[116:119], v[68:71], v[84:87]\n\t" \
    "global_load_lds_dwordx4 v1, s[50:51] offset:512\n\t" \
    "s_waitcnt lgkmcnt(5)\n\t" \
    "v_mfma_f32_16x16x32_bf16 v[72:75], a[48:51], v[24:27], v[72:75]\n\t" \
    "ds_read_b128 v[48:51], v2 offset:15360\n\t" \
    "s_waitcnt lgkmcnt(5)\n\t" \
    "v_mfma_f32_16x16x32_bf16 v[76:79], a[48:51], v[28:31], v[76:79]\n\t" \
    "ds_read_b128 v[52:55], v2 offset:33792\n\t" \
    "v_mfma_f32_16x16x32_bf16 v[80:83], a[120:123], v[24:27], v[80:83]\n\t" \
    "v_mfma_f32_16x16x32_bf16 v[84:87], a[120:123], v[28:31], v[84:87]\n\t" \
    "s_waitcnt lgkmcnt(5)\n\t" \
    "v_mfma_f32_16x16x32_bf16 v[72:75], a[52:55], v[32:35], v[72:75]\n\t" \
    "ds_read_b128 v[56:59], v2 offset:16384\n\t" \
    "s_waitcnt lgkmcnt(5)\n\t" \
    "v_mfma_f32_16x16x32_bf16 v[76:79], a[52:55], v[36:39], v[76:79]\n\t" \
    "ds_read_b128 v[60:63], v2 offset:34816\n\t" \
    "v_mfma_f32_16x16x32_bf16 v[80:83], a[124:127], v[32:35], v[80:83]\n\t" \
    "v_mfma_f32_16x16x32_bf16 v[84:87], a[124:127], v[36:39], v[84:87]\n\t" \
    "s_waitcnt lgkmcnt(5)\n\t" \
    "v_mfma_f32_16x16x32_bf16 v[72:75], a[56:59], v[40:43], v[72:75]\n\t" \
    "ds_read_b128 v[64:67], v2 offset:17408\n\t" \
    "s_waitcnt lgkmcnt(5)\n\t" \
    "v_mfma_f32_16x16x32_bf16 v[76:79], a[56:59], v[44:47], v[76:79]\n\t" \
    "ds_read_b128 v[68:71], v2 offset:35840\n\t" \
    "v_mfma_f32_16x16x32_bf16 v[80:83], a[128:131], v[40:43], v[80:83]\n\t" \
    "v_mfma_f32_16x16x32_bf16 v[84:87], a[128:131], v[44:47], v[84:87]\n\t" \
    "s_waitcnt lgkmcnt(5)\n\t" \
    "v_mfma_f32_16x16x32_bf16 v[72:75], a[60:63], v[48:51], v[72:75]\n\t" \
    "s_waitcnt lgkmcnt(4)\n\t" \
    "v_mfma_f32_16x16x32_bf16 v[76:79], a[60:63], v[52:55], v[76:79]\n\t" \
    "v_mfma_f32_16x16x32_bf16 v[80:83], a[132:135], v[48:51], v[80:83]\n\t" \
    "v_mfma_f32_16x16x32_bf16 v[84:87], a[132:135], v[52:55], v[84:87]\n\t" \
    "s_waitcnt lgkmcnt(3)\n\t" \
    "v_mfma_f32_16x16x32_bf16 v[72:75], a[64:67], v[56:59], v[72:75]\n\t" \
    "s_waitcnt lgkmcnt(2)\n\t" \
    "v_mfma_f32_16x16x32_bf16 v[76:79], a[64:67], v[60:63], v[76:79]\n\t" \
    "v_mfma_f32_16x16x32_bf16 v[80:83], a[136:139], v[56:59], v[80:83]\n\t" \
    "v_mov_b32 v2, v17\n\t" \
    "v_mfma_f32_16x16x32_bf16 v[84:87], a[136:139], v[60:63], v[84:87]\n\t" \
    "s_waitcnt lgkmcnt(1)\n\t" \
    "v_mfma_f32_16x16x32_bf16 v[72:75], a[68:71], v[64:67], v[72:75]\n\t" \
    "s_waitcnt lgkmcnt(0)\n\t" \
    "v_mfma_f32_16x16x32_bf16 v[76:79], a[68:71], v[68:71], v[76:79]\n\t" \
    "v_mfma_f32_16x16x32_bf16 v[80:83], a[140:143], v[64:67], v[80:83]\n\t" \
    "v_mfma_f32_16x16x32_bf16 v[84:87], a[140:143], v[68:71], v[84:87]\n\t" \
    "gv_mid8_%=:\n\t" \
    "s_nop 3\n\t" \
    "v_cvt_pk_bf16_f32 v104, v72, v73\n\t" \
    "v_cvt_pk_bf16_f32 v105, v74, v75\n\t" \
    "s_nop 0\n\t" \
    "v_cvt_pk_bf16_f32 v106, v80, v81\n\t" \
    "v_cvt_pk_bf16_f32 v107, v82, v83\n\t" \
    "ds_write_b128 v4, v[104:107] offset:0\n\t" \
    "v_cvt_pk_bf16_f32 v108, v76, v77\n\t" \
    "v_cvt_pk_bf16_f32 v109, v78, v79\n\t" \
    "v_cvt_pk_bf16_f32 v110, v84, v85\n\t" \
    "v_cvt_pk_bf16_f32 v111, v86, v87\n\t" \
    "ds_write_b128 v4, v[108:111] offset:1024\n\t" \
    "s_waitcnt vmcnt(18) lgkmcnt(0)\n\t" \
    "s_barrier\n\t" \
    "ds_read_b128 v[164:167], v5 offset:0\n\t" \
    "ds_read_b128 v[168:171], v5 offset:1024\n\t" \
    "ds_read_b128 v[172:175], v5 offset:2048\n\t" \
    "ds_read_b128 v[176:179], v5 offset:3072\n\t" \
    "ds_read_b128 v[180:183], v5 offset:4096\n\t" \
    "ds_read_b128 v[184:187], v5 offset:5120\n\t" \
    "ds_read_b128 v[188:191], v5 offset:6144\n\t" \
    "ds_read_b128 v[192:195], v5 offset:7168\n\t" \
    "s_mov_b64 exec, s[46:47]\n\t" \
    "global_store_dword v20, v157, s[36:37] sc1\n\t" \
    "s_mov_b64 exec, -1\n\t" \
    "v_xor_b32 v4, 0x2000, v4\n\t" \
    "v_xor_b32 v5, 0x2000, v5\n\t" \
    "s_waitcnt lgkmcnt(7)\n\t" \
    "v_mfma_f32_16x16x32_bf16 v[72:75], v[164:167], a[144:147], 0\n\t" \
    "s_waitcnt lgkmcnt(6)\n\t" \
    "v_mfma_f32_16x16x32_bf16 v[76:79], v[168:171], a[144:147], 0\n\t" \
    "s_waitcnt lgkmcnt(5)\n\t" \
    "v_mfma_f32_16x16x32_bf16 v[72:75], v[172:175], a[148:151], v[72:75]\n\t" \
    "s_waitcnt lgkmcnt(4)\n\t" \
    "v_mfma_f32_16x16x32_bf16 v[76:79], v[176:179], a[148:151], v[76:79]\n\t" \
    "s_waitcnt lgkmcnt(3)\n\t" \
    "v_mfma_f32_16x16x32_bf16 v[72:75], v[180:183], a[152:155], v[72:75]\n\t" \
    "s_waitcnt lgkmcnt(2)\n\t" \
    "v_mfma_f32_16x16x32_bf16 v[76:79], v[184:187], a[152:155], v[76:79]\n\t" \
    "s_waitcnt lgkmcnt(1)\n\t" \
    "v_mfma_f32_16x16x32_bf16 v[72:75], v[188:191], a[156:159], v[72:75]\n\t" \
    "s_waitcnt lgkmcnt(0)\n\t" \
    "v_mfma_f32_16x16x32_bf16 v[76:79], v[192:195], a[156:159], v[76:79]\n\t" \
    "v_mfma_f32_16x16x32_bf16 v[80:83], v[164:167], a[160:163], 0\n\t" \
    "v_mfma_f32_16x16x32_bf16 v[84:87], v[168:171], a[160:163], 0\n\t" \
    "v_mfma_f32_16x16x32_bf16 v[80:83], v[172:175], a[164:167], v[80:83]\n\t" \
    "v_mfma_f32_16x16x32_bf16 v[84:87], v[176:179], a[164:167], v[84:87]\n\t" \
    "v_mfma_f32_16x16x32_bf16 v[80:83], v[180:183], a[168:171], v[80:83]\n\t" \
    "v_mfma_f32_16x16x32_bf16 v[84:87], v[184:187], a[168:171], v[84:87]\n\t" \
    "v_cvt_pk_bf16_f32 v104, v72, v73\n\t" \
    "v_mfma_f32_16x16x32_bf16 v[80:83], v[188:191], a[172:175], v[80:83]\n\t" \
    "v_cvt_pk_bf16_f32 v105, v74, v75\n\t" \
    "v_mfma_f32_16x16x32_bf16 v[84:87], v[192:195], a[172:175], v[84:87]\n\t" \
    "v_cvt_pk_bf16_f32 v106, v76, v77\n\t" \
    "v_mfma_f32_16x16x32_bf16 v[88:91], v[164:167], a[176:179], 0\n\t" \
    "v_cvt_pk_bf16_f32 v107, v78, v79\n\t" \
    "v_mfma_f32_16x16x32_bf16 v[92:95], v[168:171], a[176:179], 0\n\t" \
    "v_xor_b32 v18, s40, v8\n\t" \
    "v_mfma_f32_16x16x32_bf16 v[88:91], v[172:175], a[180:183], v[88:91]\n\t" \
    "global_store_dwordx4 v18, v[104:107], s[38:39] sc1\n\t" \
    "v_mfma_f32_16x16x32_bf16 v[92:95], v[176:179], a[180:183], v[92:95]\n\t" \
    "v_mfma_f32_16x16x32_bf16 v[88:91], v[180:183], a[184:187], v[88:91]\n\t" \
    "v_mfma_f32_16x16x32_bf16 v[92:95], v[184:187], a[184:187], v[92:95]\n\t" \
    "v_cvt_pk_bf16_f32 v108, v80, v81\n\t" \
    "v_mfma_f32_16x16x32_bf16 v[88:91], v[188:191], a[188:191], v[88:91]\n\t" \
    "v_cvt_pk_bf16_f32 v109, v82, v83\n\t" \
    "v_mfma_f32_16x16x32_bf16 v[92:95], v[192:195], a[188:191], v[92:95]\n\t" \
    "v_cvt_pk_bf16_f32 v110, v84, v85\n\t" \
    "v_mfma_f32_16x16x32_bf16 v[72:75], v[164:167], a[192:195], 0\n\t" \
    "v_cvt_pk_bf16_f32 v111, v86, v87\n\t" \
    "v_mfma_f32_16x16x32_bf16 v[76:79], v[168:171], a[192:195], 0\n\t" \
    "v_xor_b32 v18, s40, v9\n\t" \
    "v_mfma_f32_16x16x32_bf16 v[72:75], v[172:175], a[196:199], v[72:75]\n\t" \
    "global_store_dwordx4 v18, v[108:111], s[38:39] sc1\n\t" \
    "v_mfma_f32_16x16x32_bf16 v[76:79], v[176:179], a[196:199], v[76:79]\n\t" \
    "v_mfma_f32_16x16x32_bf16 v[72:75], v[180:183], a[200:203], v[72:75]\n\t" \
    "v_mfma_f32_16x16x32_bf16 v[76:79], v[184:187], a[200:203], v[76:79]\n\t" \
    "v_cvt_pk_bf16_f32 v104, v88, v89\n\t" \
    "v_mfma_f32_16x16x32_bf16 v[72:75], v[188:191], a[204:207], v[72:75]\n\t" \
    "v_cvt_pk_bf16_f32 v105, v90, v91\n\t" \
    "v_mfma_f32_16x16x32_bf16 v[76:79], v[192:195], a[204:207], v[76:79]\n\t" \
    "v_cvt_pk_bf16_f32 v106, v92, v93\n\t" \
    "v_mfma_f32_16x16x32_bf16 v[80:83], v[164:167], a[208:211], 0\n\t" \
    "v_cvt_pk_bf16_f32 v107, v94, v95\n\t" \
    "v_mfma_f32_16x16x32_bf16 v[84:87], v[168:171], a[208:211], 0\n\t" \
    "v_xor_b32 v18, s40, v10\n\t" \
    "v_mfma_f32_16x16x32_bf16 v[80:83], v[172:175], a[212:215], v[80:83]\n\t" \
    "global_store_dwordx4 v18, v[104:107], s[38:39] sc1\n\t" \
    "v_mfma_f32_16x16x32_bf16 v[84:87], v[176:179], a[212:215], v[84:87]\n\t" \
    "v_mfma_f32_16x16x32_bf16 v[80:83], v[180:183], a[216:219], v[80:83]\n\t" \
    "v_mfma_f32_16x16x32_bf16 v[84:87], v[184:187], a[216:219], v[84:87]\n\t" \
    "v_cvt_pk_bf16_f32 v108, v72, v73\n\t" \
    "v_mfma_f32_16x16x32_bf16 v[80:83], v[188:191], a[220:223], v[80:83]\n\t" \
    "v_cvt_pk_bf16_f32 v109, v74, v75\n\t" \
    "v_mfma_f32_16x16x32_bf16 v[84:87], v[192:195], a[220:223], v[84:87]\n\t" \
    "v_cvt_pk_bf16_f32 v110, v76, v77\n\t" \
    "v_mfma_f32_16x16x32_bf16 v[88:91], v[164:167], a[224:227], 0\n\t" \
    "v_cvt_pk_bf16_f32 v111, v78, v79\n\t" \
    "v_mfma_f32_16x16x32_bf16 v[92:95], v[168:171], a[224:227], 0\n\t" \
    "v_xor_b32 v18, s40, v11\n\t" \
    "v_mfma_f32_16x16x32_bf16 v[88:91], v[172:175], a[228:231], v[88:91]\n\t" \
    "global_store_dwordx4 v18, v[108:111], s[38:39] sc1\n\t" \
    "v_mfma_f32_16x16x32_bf16 v[92:95], v[176:179], a[228:231], v[92:95]\n\t" \
    "v_mfma_f32_16x16x32_bf16 v[88:91], v[180:183], a[232:235], v[88:91]\n\t" \
    "v_mfma_f32_16x16x32_bf16 v[92:95], v[184:187], a[232:235], v[92:95]\n\t" \
    "v_cvt_pk_bf16_f32 v104, v80, v81\n\t" \
    "v_mfma_f32_16x16x32_bf16 v[88:91], v[188:191], a[236:239], v[88:91]\n\t" \
    "v_cvt_pk_bf16_f32 v105, v82, v83\n\t" \
    "v_mfma_f32_16x16x32_bf16 v[92:95], v[192:195], a[236:239], v[92:95]\n\t" \
    "v_cvt_pk_bf16_f32 v106, v84, v85\n\t" \
    "v_mfma_f32_16x16x32_bf16 v[72:75], v[164:167], a[240:243], 0\n\t" \
    "v_cvt_pk_bf16_f32 v107, v86, v87\n\t" \
    "v_mfma_f32_16x16x32_bf16 v[76:79], v[168:171], a[240:243], 0\n\t" \
    "v_xor_b32 v18, s40, v12\n\t" \
    "v_mfma_f32_16x16x32_bf16 v[72:75], v[172:175], a[244:247], v[72:75]\n\t" \
    "global_store_dwordx4 v18, v[104:107], s[38:39] sc1\n\t" \
    "v_mfma_f32_16x16x32_bf16 v[76:79], v[176:179], a[244:247], v[76:79]\n\t" \
    "v_mfma_f32_16x16x32_bf16 v[72:75], v[180:183], a[248:251], v[72:75]\n\t" \
    "v_mfma_f32_16x16x32_bf16 v[76:79], v[184:187], a[248:251], v[76:79]\n\t" \
    "v_cvt_pk_bf16_f32 v108, v88, v89\n\t" \
    "v_mfma_f32_16x16x32_bf16 v[72:75], v[188:191], a[252:255], v[72:75]\n\t" \
    "v_cvt_pk_bf16_f32 v109, v90, v91\n\t" \
    "v_mfma_f32_16x16x32_bf16 v[76:79], v[192:195], a[252:255], v[76:79]\n\t" \
    "v_cvt_pk_bf16_f32 v110, v92, v93\n\t" \
    "v_mfma_f32_16x16x32_bf16 v[80:83], v[164:167], v[224:227], 0\n\t" \
    "v_cvt_pk_bf16_f32 v111, v94, v95\n\t" \
    "v_mfma_f32_16x16x32_bf16 v[84:87], v[168:171], v[224:227], 0\n\t" \
    "v_xor_b32 v18, s40, v13\n\t" \
    "v_mfma_f32_16x16x32_bf16 v[80:83], v[172:175], v[228:231], v[80:83]\n\t" \
    "global_store_dwordx4 v18, v[108:111], s[38:39] sc1\n\t" \
    "v_mfma_f32_16x16x32_bf16 v[84:87], v[176:179], v[228:231], v[84:87]\n\t" \
    "v_mfma_f32_16x16x32_bf16 v[80:83], v[180:183], v[232:235], v[80:83]\n\t" \
    "v_mfma_f32_16x16x32_bf16 v[84:87], v[184:187], v[232:235], v[84:87]\n\t" \
    "v_cvt_pk_bf16_f32 v104, v72, v73\n\t" \
    "v_mfma_f32_16x16x32_bf16 v[80:83], v[188:191], v[236:239], v[80:83]\n\t" \
    "v_cvt_pk_bf16_f32 v105, v74, v75\n\t" \
    "v_mfma_f32_16x16x32_bf16 v[84:87], v[192:195], v[236:239], v[84:87]\n\t" \
    "v_cvt_pk_bf16_f32 v106, v76, v77\n\t" \
    "s_cmp_lt_u32 s65, 4\n\t" \
    "s_cbranch_scc1 gv_f1st9_%=\n\t" \
    "gv_f1b10_%=:\n\t" \
    "v_mfma_f32_16x16x32_bf16 v[88:91], v[164:167], v[240:243], 0\n\t" \
    "v_cvt_pk_bf16_f32 v107, v78, v79\n\t" \
    "ds_read_b128 v[24:27], v17 offset:0\n\t" \
    "v_mfma_f32_16x16x32_bf16 v[92:95], v[168:171], v[240:243], 0\n\t" \
    "v_xor_b32 v18, s40, v14\n\t" \
    "ds_read_b128 v[28:31], v17 offset:18432\n\t" \
    "v_mfma_f32_16x16x32_bf16 v[88:91], v[172:175], v[244:247], v[88:91]\n\t" \
    "global_store_dwordx4 v18, v[104:107], s[38:39] sc1\n\t" \
    "ds_read_b128 v[32:35], v17 offset:1024\n\t" \
    "v_mfma_f32_16x16x32_bf16 v[92:95], v[176:179], v[244:247], v[92:95]\n\t" \
    "ds_read_b128 v[36:39], v17 offset:19456\n\t" \
    "v_mfma_f32_16x16x32_bf16 v[88:91], v[180:183], v[248:251], v[88:91]\n\t" \
    "ds_read_b128 v[40:43], v17 offset:2048\n\t" \
    "v_mfma_f32_16x16x32_bf16 v[92:95], v[184:187], v[248:251], v[92:95]\n\t" \
    "v_cvt_pk_bf16_f32 v108, v80, v81\n\t" \
    "ds_read_b128 v[44:47], v17 offset:20480\n\t" \
    "v_mfma_f32_16x16x32_bf16 v[88:91], v[188:191], v[252:255], v[88:91]\n\t" \
    "v_cvt_pk_bf16_f32 v109, v82, v83\n\t" \
    "v_mfma_f32_16x16x32_bf16 v[92:95], v[192:195], v[252:255], v[92:95]\n\t" \
    "v_cvt_pk_bf16_f32 v110, v84, v85\n\t" \
    "v_cvt_pk_bf16_f32 v111, v86, v87\n\t" \
    "v_xor_b32 v18, s40, v15\n\t" \
    "global_store_dwordx4 v18, v[108:111], s[38:39] sc1\n\t" \
    "s_nop 1\n\t" \
    "v_cvt_pk_bf16_f32 v104, v88, v89\n\t" \
    "v_cvt_pk_bf16_f32 v105, v90, v91\n\t" \
    "v_cvt_pk_bf16_f32 v106, v92, v93\n\t" \
    "v_cvt_pk_bf16_f32 v107, v94, v95\n\t" \
    "v_xor_b32 v18, s40, v16\n\t" \
    "global_store_dwordx4 v18, v[104:107], s[38:39] sc1\n\t" \
    "s_sub_u32 s69, s65, 10\n\t" \
    "s_cmp_ge_u32 s69, 52\n\t" \
    "s_cbranch_scc1 gv_fast11_%=\n\t" \
    "gv_nofast7_%=:\n\t" \
    "s_add_u32 s65, s65, 2\n\t" \
    "s_cmp_lt_u32 s65, 64\n\t" \
    "s_cbranch_scc1 gv_loop6_%=\n\t" \
    "s_waitcnt vmcnt(0) lgkmcnt(0)\n\t" \
    "s_barrier\n\t" \
    "s_branch ea_done_%=\n\t" \
    "gv_f1st9_%=:\n\t" \
    "s_waitcnt vmcnt(6)\n\t" \
    "s_barrier\n\t" \
    "s_branch gv_f1b10_%=\n\t" \
    "gv_fast11_%=:\n\t" \
    "s_waitcnt vmcnt(0)\n\t" \
    "s_barrier\n\t" \
    "s_cmp_lg_u32 s66, 0\n\t" \
    "s_cbranch_scc1 gv_SF12_%=\n\t" \
    "s_add_u32 s70, s65, 0\n\t" \
    "v_mov_b32 v157, s70\n\t" \
    "s_mov_b64 exec, 31\n\t" \
    "global_store_dword v20, v157, s[36:37] sc1\n\t" \
    "s_mov_b64 exec, -1\n\t" \
    "gv_SF12_%=:\n\t" \
    "s_cmp_ge_u32 s65, 2\n\t" \
    "s_cbranch_scc1 gv_nofast7_%=\n\t" \
    "s_add_u32 m0, s91, 0\n\t" \
    "s_nop 0\n\t" \
    "global_load_lds_dwordx4 v1, s[50:51] offset:0\n\t" \
    "s_add_u32 m0, s91, 960\n\t" \
    "s_nop 0\n\t" \
    "global_load_lds_dwordx4 v1, s[50:51] offset:64\n\t" \
    "s_add_u32 m0, s91, 1920\n\t" \
    "s_nop 0\n\t" \
    "global_load_lds_dwordx4 v1, s[50:51] offset:128\n\t" \
    "s_add_u32 m0, s91, 2880\n\t" \
    "s_nop 0\n\t" \
    "global_load_lds_dwordx4 v1, s[50:51] offset:192\n\t" \
    "s_add_u32 m0, s91, 3840\n\t" \
    "s_nop 0\n\t" \
    "global_load_lds_dwordx4 v1, s[50:51] offset:256\n\t" \
    "s_add_u32 m0, s91, 4800\n\t" \
    "s_nop 0\n\t" \
    "global_load_lds_dwordx4 v1, s[50:51] offset:320\n\t" \
    "s_add_u32 m0, s91, 5760\n\t" \
    "s_nop 0\n\t" \
    "global_load_lds_dwordx4 v1, s[50:51] offset:384\n\t" \
    "s_add_u32 m0, s91, 6720\n\t" \
    "s_nop 0\n\t" \
    "global_load_lds_dwordx4 v1, s[50:51] offset:448\n\t" \
    "s_add_u32 m0, s91, 7680\n\t" \
    "s_nop 0\n\t" \
    "global_load_lds_dwordx4 v1, s[50:51] offset:512\n\t" \
    "s_branch gv_nofast7_%=\n\t" \
    "ea_done_%=:\n\t" \
    "s_waitcnt lgkmcnt(0)\n\t" \
    "ea_last_%=:\n\t" \
    ""
#define ASM_R \
    "s_mov_b64 s[36:37], %0\n\t" \
    "s_mov_b64 s[42:43], %1\n\t" \
    "s_mov_b64 s[44:45], %2\n\t" \
    "s_mov_b32 s66, %3\n\t" \
    "s_mov_b32 s67, %4\n\t" \
    "s_mov_b64 s[50:51], %5\n\t" \
    "s_mov_b32 s82, %7\n\t" \
    "s_load_dwordx8 s[84:91], %6, 0\n\t" \
    "v_mbcnt_lo_u32_b32 v223, -1, 0\n\t" \
    "v_mbcnt_hi_u32_b32 v223, -1, v223\n\t" \
    "s_getpc_b64 s[46:47]\n\t" \
    "rr_pc_%=:\n\t" \
    "s_lshl_b32 s69, s66, 13\n\t" \
    "v_lshl_add_u32 v222, v223, 7, s69\n\t" \
    "v_min_u32 v222, rr_last_%=-rr_pc_%=-4, v222\n\t" \
    "global_load_dword v222, v222, s[46:47]\n\t" \
    "v_lshlrev_b32 v223, 2, v223\n\t" \
    "v_add_u32 v223, s82, v223\n\t" \
    "ds_read_b32 v0, v223 offset:0\n\t" \
    "ds_read_b32 v1, v223 offset:256\n\t" \
    "ds_read_b32 v2, v223 offset:512\n\t" \
    "ds_read_b32 v3, v223 offset:768\n\t" \
    "ds_read_b32 v4, v223 offset:1024\n\t" \
    "ds_read_b32 v5, v223 offset:1280\n\t" \
    "ds_read_b32 v6, v223 offset:1536\n\t" \
    "ds_read_b32 v7, v223 offset:1792\n\t" \
    "ds_read_b32 v8, v223 offset:2048\n\t" \
    "ds_read_b32 v9, v223 offset:2304\n\t" \
    "ds_read_b32 v10, v223 offset:2560\n\t" \
    "ds_read_b32 v11, v223 offset:2816\n\t" \
    "s_waitcnt lgkmcnt(0)\n\t" \
    "ds_read_b32 v12, v223 offset:3072\n\t" \
    "ds_read_b32 v13, v223 offset:3328\n\t" \
    "ds_read_b32 v14, v223 offset:3584\n\t" \
    "ds_read_b32 v15, v223 offset:3840\n\t" \
    "ds_read_b32 v16, v223 offset:4096\n\t" \
    "ds_read_b32 v17, v223 offset:4352\n\t" \
    "ds_read_b32 v18, v223 offset:4608\n\t" \
    "ds_read_b32 v19, v223 offset:4864\n\t" \
    "ds_read_b32 v20, v223 offset:5120\n\t" \
    "ds_read_b32 v21, v223 offset:5376\n\t" \
    "ds_read_b32 v22, v223 offset:5632\n\t" \
    "ds_read_b32 v23, v223 offset:5888\n\t" \
    "s_waitcnt lgkmcnt(0)\n\t" \
    "ds_read_b32 v24, v223 offset:6144\n\t" \
    "ds_read_b32 v25, v223 offset:6400\n\t" \
    "ds_read_b32 v26, v223 offset:6656\n\t" \
    "ds_read_b32 v27, v223 offset:6912\n\t" \
    "ds_read_b32 v28, v223 offset:7168\n\t" \
    "ds_read_b32 v29, v223 offset:7424\n\t" \
    "ds_read_b32 v30, v223 offset:7680\n\t" \
    "ds_read_b32 v31, v223 offset:7936\n\t" \
    "ds_read_b32 v32, v223 offset:8192\n\t" \
    "ds_read_b32 v33, v223 offset:8448\n\t" \
    "ds_read_b32 v34, v223 offset:8704\n\t" \
    "ds_read_b32 v35, v223 offset:8960\n\t" \
    "s_waitcnt lgkmcnt(0)\n\t" \
    "ds_read_b32 v36, v223 offset:9216\n\t" \
    "ds_read_b32 v37, v223 offset:9472\n\t" \
    "ds_read_b32 v38, v223 offset:9728\n\t" \
    "ds_read_b32 v39, v223 offset:9984\n\t" \
    "ds_read_b32 v40, v223 offset:10240\n\t" \
    "ds_read_b32 v41, v223 offset:10496\n\t" \
    "ds_read_b32 v42, v223 offset:10752\n\t" \
    "ds_read_b32 v43, v223 offset:11008\n\t" \
    "ds_read_b32 v44, v223 offset:11264\n\t" \
    "ds_read_b32 v45, v223 offset:11520\n\t" \
    "ds_read_b32 v46, v223 offset:11776\n\t" \
    "ds_read_b32 v164, v223 offset:12032\n\t" \
    "s_waitcnt lgkmcnt(0)\n\t" \
    "ds_read_b32 v165, v223 offset:12288\n\t" \
    "ds_read_b32 v166, v223 offset:12544\n\t" \
    "ds_read_b32 v167, v223 offset:12800\n\t" \
    "ds_read_b32 v196, v223 offset:13312\n\t" \
    "ds_read_b32 v197, v223 offset:13568\n\t" \
    "ds_read_b32 v198, v223 offset:13824\n\t" \
    "ds_read_b32 v199, v223 offset:14080\n\t" \
    "ds_read_b32 v200, v223 offset:14336\n\t" \
    "s_waitcnt lgkmcnt(0)\n\t" \
    "s_mov_b32 s68, 262144\n\t" \
    "s_mov_b32 s72, 0\n\t" \
    "s_mov_b32 s83, -1\n\t" \
    "s_mov_b32 s65, 0\n\t" \
    "v_add_u32 v181, 16384, v2\n\t" \
    "v_add_u32 v182, 131072, v2\n\t" \
    "v_add_u32 v183, 147456, v2\n\t" \
    "s_mov_b32 s77, 3019898880\n\t" \
    "s_mov_b32 s78, 3054198966\n\t" \
    "s_mov_b32 s71, 3\n\t" \
    "rr_E1_%=:\n\t" \
    "s_memtime s[54:55]\n\t" \
    "s_nop 0\n\t" \
    "s_memrealtime s[4:5]\n\t" \
    "s_nop 0\n\t" \
    "s_memrealtime s[6:7]\n\t" \
    "s_nop 0\n\t" \
    "s_memrealtime s[8:9]\n\t" \
    "s_nop 0\n\t" \
    "s_memrealtime s[10:11]\n\t" \
    "s_nop 0\n\t" \
    "s_memrealtime s[12:13]\n\t" \
    "s_nop 0\n\t" \
    "s_memrealtime s[14:15]\n\t" \
    "s_nop 0\n\t" \
    "s_memrealtime s[16:17]\n\t" \
    "s_nop 0\n\t" \
    "s_memrealtime s[18:19]\n\t" \
    "s_nop 0\n\t" \
    "s_memrealtime s[20:21]\n\t" \
    "s_nop 0\n\t" \
    "s_memrealtime s[22:23]\n\t" \
    "s_nop 0\n\t" \
    "s_memrealtime s[24:25]\n\t" \
    "s_nop 0\n\t" \
    "s_memrealtime s[26:27]\n\t" \
    "s_nop 0\n\t" \
    "s_memrealtime s[28:29]\n\t" \
    "s_nop 0\n\t" \
    "s_memtime s[56:57]\n\t" \
    "s_mov_b32 s70, 1\n\t" \
    "s_sub_u32 s71, s71, 1\n\t" \
    "s_cselect_b32 s69, 0, -1\n\t" \
    "s_waitcnt lgkmcnt(0)\n\t" \
    "s_sub_u32 s54, s56, s54\n\t" \
    "s_and_b32 s54, s54, s69\n\t" \
    "s_cmp_gt_u32 s54, 124\n\t" \
    "s_cbranch_scc1 rr_E1_%=\n\t" \
    "s_cmp_eq_u32 s4, s28\n\t" \
    "s_addc_u32 s70, s70, 0\n\t" \
    "s_cmp_eq_u32 s6, s28\n\t" \
    "s_addc_u32 s70, s70, 0\n\t" \
    "s_cmp_eq_u32 s8, s28\n\t" \
    "s_addc_u32 s70, s70, 0\n\t" \
    "s_cmp_eq_u32 s10, s28\n\t" \
    "s_addc_u32 s70, s70, 0\n\t" \
    "s_cmp_eq_u32 s12, s28\n\t" \
    "s_addc_u32 s70, s70, 0\n\t" \
    "s_cmp_eq_u32 s14, s28\n\t" \
    "s_addc_u32 s70, s70, 0\n\t" \
    "s_cmp_eq_u32 s16, s28\n\t" \
    "s_addc_u32 s70, s70, 0\n\t" \
    "s_cmp_eq_u32 s18, s28\n\t" \
    "s_addc_u32 s70, s70, 0\n\t" \
    "s_cmp_eq_u32 s20, s28\n\t" \
    "s_addc_u32 s70, s70, 0\n\t" \
    "s_cmp_eq_u32 s22, s28\n\t" \
    "s_addc_u32 s70, s70, 0\n\t" \
    "s_cmp_eq_u32 s24, s28\n\t" \
    "s_addc_u32 s70, s70, 0\n\t" \
    "s_cmp_eq_u32 s26, s28\n\t" \
    "s_addc_u32 s70, s70, 0\n\t" \
    "s_mov_b32 s75, s73\n\t" \
    "s_mov_b32 s76, s74\n\t" \
    "s_lshl_b32 s70, s70, 3\n\t" \
    "s_sub_u32 s73, s56, s70\n\t" \
    "s_lshl_b32 s74, s28, 4\n\t" \
    "v_mov_b32 v172, 0x1f0000\n\t" \
    "v_mov_b32 v173, 0x3f0000\n\t" \
    "global_load_dword v172, v172, s[36:37]\n\t" \
    "global_load_dword v173, v173, s[36:37]\n\t" \
    "s_mov_b32 s65, 63\n\t" \
    "s_mul_i32 s70, s67, 7\n\t" \
    "s_add_u32 s69, s65, 0\n\t" \
    "s_xor_b32 s69, s69, s70\n\t" \
    "s_and_b32 s69, s69, 63\n\t" \
    "s_lshl_b32 s69, s69, 15\n\t" \
    "s_mul_i32 s70, s67, 2097152\n\t" \
    "s_add_u32 s69, s69, s70\n\t" \
    "s_add_u32 s69, s69, 48234496\n\t" \
    "s_add_u32 s38, s36, s69\n\t" \
    "s_addc_u32 s39, s37, 0\n\t" \
    "v_mov_b32 v174, 0\n\t" \
    "global_load_dword v174, v174, s[38:39]\n\t" \
    "v_mbcnt_lo_u32_b32 v184, -1, 0\n\t" \
    "v_mbcnt_hi_u32_b32 v184, -1, v184\n\t" \
    "v_min_u32 v184, 59, v184\n\t" \
    "v_lshlrev_b32 v184, 6, v184\n\t" \
    "v_add_u32 v184, 6258688, v184\n\t" \
    "v_mov_b32 v185, 6266880\n\t" \
    "global_load_dword v185, v185, s[36:37] sc1\n\t" \
    "global_load_dwordx2 v[186:187], v184, s[36:37] sc1\n\t" \
    "s_waitcnt vmcnt(0)\n\t" \
    "v_sub_u32 v186, v186, v185\n\t" \
    "v_cmp_ne_u32 vcc, 1, v186\n\t" \
    "s_cbranch_vccz rr_Cok4_%=\n\t" \
    "rr_C3_%=:\n\t" \
    "global_load_dwordx2 v[186:187], v184, s[36:37] sc1\n\t" \
    "s_waitcnt vmcnt(0)\n\t" \
    "v_sub_u32 v186, v186, v185\n\t" \
    "v_cmp_ne_u32 vcc, 1, v186\n\t" \
    "s_cbranch_vccz rr_Cok4_%=\n\t" \
    "s_cmp_eq_u32 s68, 0\n\t" \
    "s_cbranch_scc1 rr_F5_%=\n\t" \
    "s_sub_u32 s68, s68, 1\n\t" \
    "s_sleep 4\n\t" \
    "s_branch rr_C3_%=\n\t" \
    "rr_F5_%=:\n\t" \
    "v_mov_b32 v188, 6095104\n\t" \
    "v_mov_b32 v189, 4\n\t" \
    "global_store_dword v188, v189, s[36:37] sc1\n\t" \
    "s_waitcnt vmcnt(0)\n\t" \
    "s_branch rr_Cok4_%=\n\t" \
    "rr_Cok4_%=:\n\t" \
    "v_sub_u32_dpp v186, v187, v187 row_shl:6 row_mask:0xf bank_mask:0xf\n\t" \
    "v_min_u32_dpp v187, v187, v187 row_shl:6 row_mask:0xf bank_mask:0xf\n\t" \
    "v_min_i32 v186, 64, v186\n\t" \
    "v_max_i32 v186, -64, v186\n\t" \
    "s_mov_b32 s33, 0\n\t" \
    "v_readlane_b32 s69, v187, 0\n\t" \
    "v_readlane_b32 s70, v187, 1\n\t" \
    "v_readlane_b32 s54, v186, 0\n\t" \
    "v_readlane_b32 s55, v186, 1\n\t" \
    "s_nop 1\n\t" \
    "s_cmp_lt_u32 s70, s69\n\t" \
    "s_cselect_b32 s62, 8192, 0\n\t" \
    "s_cselect_b32 s54, s55, s54\n\t" \
    "s_cselect_b32 s69, s70, s69\n\t" \
    "s_mov_b32 s71, s69\n\t" \
    "s_add_u32 s33, s33, s54\n\t" \
    "v_readlane_b32 s69, v187, 2\n\t" \
    "v_readlane_b32 s70, v187, 3\n\t" \
    "v_readlane_b32 s54, v186, 2\n\t" \
    "v_readlane_b32 s55, v186, 3\n\t" \
    "s_nop 1\n\t" \
    "s_sub_u32 s55, 0, s55\n\t" \
    "s_cmp_lt_u32 s70, s69\n\t" \
    "s_cselect_b32 s63, 8192, 0\n\t" \
    "s_cselect_b32 s54, s55, s54\n\t" \
    "s_cselect_b32 s69, s70, s69\n\t" \
    "s_max_u32 s71, s69, s71\n\t" \
    "s_add_u32 s33, s33, s54\n\t" \
    "v_readlane_b32 s69, v187, 4\n\t" \
    "v_readlane_b32 s70, v187, 5\n\t" \
    "v_readlane_b32 s54, v186, 4\n\t" \
    "v_readlane_b32 s55, v186, 5\n\t" \
    "s_nop 1\n\t" \
    "s_sub_u32 s54, 0, s54\n\t" \
    "s_sub_u32 s55, 0, s55\n\t" \
    "s_cmp_lt_u32 s70, s69\n\t" \
    "s_cselect_b32 s64, 8192, 0\n\t" \
    "s_cselect_b32 s54, s55, s54\n\t" \
    "s_cselect_b32 s69, s70, s69\n\t" \
    "s_max_u32 s71, s69, s71\n\t" \
    "s_add_u32 s33, s33, s54\n\t" \
    "s_add_u32 s63, s63, 0x200000\n\t" \
    "s_xor_b32 s52, s64, 8192\n\t" \
    "s_cmp_gt_u32 s71, 625\n\t" \
    "s_cselect_b32 s69, 1, 0\n\t" \
    "s_mul_i32 s70, s69, 160\n\t" \
    "s_add_u32 s86, s86, s70\n\t" \
    "s_mul_i32 s70, s69, 536\n\t" \
    "s_add_u32 s87, s87, s70\n\t" \
    "s_mul_i32 s70, s69, 256\n\t" \
    "s_add_u32 s88, s88, s70\n\t" \
    "v_xor_b32 v6, s52, v6\n\t" \
    "v_lshlrev_b32 v178, 3, v0\n\t" \
    "v_and_b32 v178, 0xf80, v178\n\t" \
    "s_add_u32 s69, s52, 6148096\n\t" \
    "v_add_u32 v178, s69, v178\n\t" \
    "v_mov_b32 v172, -1\n\t" \
    "v_mov_b32 v173, -1\n\t" \
    "v_mov_b32 v174, -1\n\t" \
    "v_mov_b32 v175, -1\n\t" \
    "v_mov_b32 v170, 0\n\t" \
    "s_mov_b32 s65, s66\n\t" \
    "s_sub_u32 s80, s74, 32000\n\t" \
    "v_mov_b32 v177, -1\n\t" \
    "s_cmp_lg_u32 s51, 0\n\t" \
    "s_cselect_b32 s42, 0, 0\n\t" \
    "s_cselect_b32 s43, 32, 0\n\t" \
    "s_cmp_le_u32 s51, s65\n\t" \
    "s_cbranch_scc1 rr_n316_%=\n\t" \
    "s_add_u32 s69, s65, 2\n\t" \
    "s_mul_i32 s70, s69, 171\n\t" \
    "s_lshr_b32 s70, s70, 9\n\t" \
    "s_mul_i32 s70, s70, 3\n\t" \
    "s_sub_u32 s69, s69, s70\n\t" \
    "s_mul_i32 s69, s69, 36\n\t" \
    "s_lshl_b32 s69, s69, 14\n\t" \
    "s_add_u32 s69, s69, s64\n\t" \
    "s_add_u32 s69, s69, 4194304\n\t" \
    "s_add_u32 s40, s36, s69\n\t" \
    "s_addc_u32 s41, s37, 0\n\t" \
    "global_store_dwordx4 v196, v[172:175], s[40:41] sc1\n\t" \
    "global_store_dwordx4 v197, v[172:175], s[40:41] sc1\n\t" \
    "global_store_dwordx4 v198, v[172:175], s[40:41] sc1\n\t" \
    "s_add_u32 s69, s65, 2\n\t" \
    "s_mul_i32 s70, s69, 171\n\t" \
    "s_lshr_b32 s70, s70, 9\n\t" \
    "s_mul_i32 s70, s70, 3\n\t" \
    "s_sub_u32 s69, s69, s70\n\t" \
    "s_mul_i32 s69, s69, 36\n\t" \
    "s_add_u32 s69, s69, s67\n\t" \
    "s_lshl_b32 s69, s69, 14\n\t" \
    "s_add_u32 s69, s69, s64\n\t" \
    "s_add_u32 s69, s69, 4194304\n\t" \
    "s_add_u32 s40, s36, s69\n\t" \
    "s_addc_u32 s41, s37, 0\n\t" \
    "global_store_dwordx4 v0, v[172:175], s[40:41] offset:0 sc1\n\t" \
    "global_store_dwordx4 v0, v[172:175], s[40:41] offset:1024 sc1\n\t" \
    "global_store_dwordx4 v0, v[172:175], s[40:41] offset:2048 sc1\n\t" \
    "global_store_dwordx4 v0, v[172:175], s[40:41] offset:3072 sc1\n\t" \
    "global_store_dwordx4 v1, v[172:175], s[40:41] offset:0 sc1\n\t" \
    "global_store_dwordx4 v1, v[172:175], s[40:41] offset:1024 sc1\n\t" \
    "global_store_dwordx4 v1, v[172:175], s[40:41] offset:2048 sc1\n\t" \
    "global_store_dwordx4 v1, v[172:175], s[40:41] offset:3072 sc1\n\t" \
    "rr_n316_%=:\n\t" \
    "s_cmp_eq_u32 s51, 0\n\t" \
    "s_cbranch_scc1 rr_arm17_%=\n\t" \
    "s_cmp_gt_u32 s65, 61\n\t" \
    "s_cbranch_scc1 rr_arm17_%=\n\t" \
    "s_add_u32 s69, s65, 2\n\t" \
    "s_bitcmp1_b32 s69, 0\n\t" \
    "s_cselect_b32 s70, s63, s62\n\t" \
    "s_bfe_u32 s69, s69, 0x20001\n\t" \
    "s_lshl_b32 s69, s69, 19\n\t" \
    "s_add_u32 s69, s69, s70\n\t" \
    "s_add_u32 s46, s36, s69\n\t" \
    "s_addc_u32 s47, s37, 0\n\t" \
    "global_store_dwordx4 v164, v[172:175], s[46:47] sc1\n\t" \
    "global_store_dwordx4 v165, v[172:175], s[46:47] sc1\n\t" \
    "global_store_dwordx4 v166, v[172:175], s[46:47] sc1\n\t" \
    "global_store_dwordx4 v167, v[172:175], s[46:47] sc1\n\t" \
    "rr_arm17_%=:\n\t" \
    "s_waitcnt vmcnt(0)\n\t" \
    "s_cmp_gt_i32 s33, 0\n\t" \
    "s_cselect_b32 s70, 1, 0\n\t" \
    "s_mul_i32 s69, s67, 57\n\t" \
    "s_lshr_b32 s69, s69, 9\n\t" \
    "s_xor_b32 s69, s69, s70\n\t" \
    "s_and_b32 s33, s69, 1\n\t" \
    "s_and_b32 s69, s33, 1\n\t" \
    "s_lshl_b32 s69, s69, 14\n\t" \
    "s_add_u32 s69, s69, s62\n\t" \
    "s_add_u32 s52, s36, s69\n\t" \
    "s_addc_u32 s53, s37, 0\n\t" \
    "v_lshrrev_b32 v187, 9, v0\n\t" \
    "v_mul_i32_i24 v187, -2044, v187\n\t" \
    "v_add_u32 v187, v187, v7\n\t" \
    "v_add_u32 v187, 3072, v187\n\t" \
    "v_mov_b32 v189, -1\n\t" \
    "s_sleep 40\n\t" \
    "s_mov_b32 s71, 3\n\t" \
    "rr_E18_%=:\n\t" \
    "s_memtime s[54:55]\n\t" \
    "s_nop 0\n\t" \
    "s_memrealtime s[4:5]\n\t" \
    "s_nop 0\n\t" \
    "s_memrealtime s[6:7]\n\t" \
    "s_nop 0\n\t" \
    "s_memrealtime s[8:9]\n\t" \
    "s_nop 0\n\t" \
    "s_memrealtime s[10:11]\n\t" \
    "s_nop 0\n\t" \
    "s_memrealtime s[12:13]\n\t" \
    "s_nop 0\n\t" \
    "s_memrealtime s[14:15]\n\t" \
    "s_nop 0\n\t" \
    "s_memrealtime s[16:17]\n\t" \
    "s_nop 0\n\t" \
    "s_memrealtime s[18:19]\n\t" \
    "s_nop 0\n\t" \
    "s_memrealtime s[20:21]\n\t" \
    "s_nop 0\n\t" \
    "s_memrealtime s[22:23]\n\t" \
    "s_nop 0\n\t" \
    "s_memrealtime s[24:25]\n\t" \
    "s_nop 0\n\t" \
    "s_memrealtime s[26:27]\n\t" \
    "s_nop 0\n\t" \
    "s_memrealtime s[28:29]\n\t" \
    "s_nop 0\n\t" \
    "s_memtime s[56:57]\n\t" \
    "s_mov_b32 s70, 1\n\t" \
    "s_sub_u32 s71, s71, 1\n\t" \
    "s_cselect_b32 s69, 0, -1\n\t" \
    "s_waitcnt lgkmcnt(0)\n\t" \
    "s_sub_u32 s54, s56, s54\n\t" \
    "s_and_b32 s54, s54, s69\n\t" \
    "s_cmp_gt_u32 s54, 124\n\t" \
    "s_cbranch_scc1 rr_E18_%=\n\t" \
    "s_cmp_eq_u32 s4, s28\n\t" \
    "s_addc_u32 s70, s70, 0\n\t" \
    "s_cmp_eq_u32 s6, s28\n\t" \
    "s_addc_u32 s70, s70, 0\n\t" \
    "s_cmp_eq_u32 s8, s28\n\t" \
    "s_addc_u32 s70, s70, 0\n\t" \
    "s_cmp_eq_u32 s10, s28\n\t" \
    "s_addc_u32 s70, s70, 0\n\t" \
    "s_cmp_eq_u32 s12, s28\n\t" \
    "s_addc_u32 s70, s70, 0\n\t" \
    "s_cmp_eq_u32 s14, s28\n\t" \
    "s_addc_u32 s70, s70, 0\n\t" \
    "s_cmp_eq_u32 s16, s28\n\t" \
    "s_addc_u32 s70, s70, 0\n\t" \
    "s_cmp_eq_u32 s18, s28\n\t" \
    "s_addc_u32 s70, s70, 0\n\t" \
    "s_cmp_eq_u32 s20, s28\n\t" \
    "s_addc_u32 s70, s70, 0\n\t" \
    "s_cmp_eq_u32 s22, s28\n\t" \
    "s_addc_u32 s70, s70, 0\n\t" \
    "s_cmp_eq_u32 s24, s28\n\t" \
    "s_addc_u32 s70, s70, 0\n\t" \
    "s_cmp_eq_u32 s26, s28\n\t" \
    "s_addc_u32 s70, s70, 0\n\t" \
    "s_mov_b32 s75, s73\n\t" \
    "s_mov_b32 s76, s74\n\t" \
    "s_lshl_b32 s70, s70, 3\n\t" \
    "s_sub_u32 s73, s56, s70\n\t" \
    "s_lshl_b32 s74, s28, 4\n\t" \
    "s_sub_u32 s69, s73, s75\n\t" \
    "s_sub_u32 s70, s74, s76\n\t" \
    "s_sub_u32 s71, s70, 1600\n\t" \
    "s_cmp_lt_u32 s71, 400000\n\t" \
    "s_cbranch_scc0 rr_K20_%=\n\t" \
    "v_cvt_f32_u32 v190, s69\n\t" \
    "v_cvt_f32_u32 v191, s70\n\t" \
    "v_rcp_f32 v192, v190\n\t" \
    "v_rcp_f32 v193, v191\n\t" \
    "v_mul_f32 v192, v192, v191\n\t" \
    "v_mul_f32 v193, v193, v190\n\t" \
    "v_mul_f32 v192, 0x4f800000, v192\n\t" \
    "v_mul_f32 v193, 0x4f000000, v193\n\t" \
    "v_cvt_u32_f32 v192, v192\n\t" \
    "v_cvt_u32_f32 v193, v193\n\t" \
    "v_readfirstlane_b32 s69, v192\n\t" \
    "v_readfirstlane_b32 s70, v193\n\t" \
    "s_nop 1\n\t" \
    "s_mov_b32 s78, s69\n\t" \
    "s_mov_b32 s77, s70\n\t" \
    "rr_K20_%=:\n\t" \
    "s_add_u32 s69, s65, 0\n\t" \
    "s_bitcmp1_b32 s69, 0\n\t" \
    "s_cselect_b32 s70, s63, s62\n\t" \
    "s_bfe_u32 s69, s69, 0x20001\n\t" \
    "s_lshl_b32 s69, s69, 19\n\t" \
    "s_add_u32 s69, s69, s70\n\t" \
    "s_add_u32 s46, s36, s69\n\t" \
    "s_addc_u32 s47, s37, 0\n\t" \
    "s_add_u32 s69, s65, 0\n\t" \
    "s_lshr_b32 s69, s69, 1\n\t" \
    "s_xor_b32 s69, s69, s33\n\t" \
    "s_and_b32 s69, s69, 1\n\t" \
    "s_lshl_b32 s69, s69, 14\n\t" \
    "s_or_b32 s46, s46, s69\n\t" \
    "s_add_u32 s69, s65, 1\n\t" \
    "s_bitcmp1_b32 s69, 0\n\t" \
    "s_cselect_b32 s70, s63, s62\n\t" \
    "s_bfe_u32 s69, s69, 0x20001\n\t" \
    "s_lshl_b32 s69, s69, 19\n\t" \
    "s_add_u32 s69, s69, s70\n\t" \
    "s_add_u32 s48, s36, s69\n\t" \
    "s_addc_u32 s49, s37, 0\n\t" \
    "s_mul_i32 s70, s67, 7\n\t" \
    "s_add_u32 s69, s65, 0\n\t" \
    "s_xor_b32 s69, s69, s70\n\t" \
    "s_and_b32 s69, s69, 63\n\t" \
    "s_lshl_b32 s69, s69, 15\n\t" \
    "s_mul_i32 s70, s67, 2097152\n\t" \
    "s_add_u32 s69, s69, s70\n\t" \
    "s_add_u32 s69, s69, 48234496\n\t" \
    "s_add_u32 s38, s36, s69\n\t" \
    "s_addc_u32 s39, s37, 0\n\t" \
    "s_add_u32 s69, s65, 0\n\t" \
    "s_mul_i32 s70, s69, 171\n\t" \
    "s_lshr_b32 s70, s70, 9\n\t" \
    "s_mul_i32 s70, s70, 3\n\t" \
    "s_sub_u32 s69, s69, s70\n\t" \
    "s_mul_i32 s69, s69, 36\n\t" \
    "s_add_u32 s69, s69, s67\n\t" \
    "s_lshl_b32 s69, s69, 14\n\t" \
    "s_add_u32 s69, s69, s64\n\t" \
    "s_add_u32 s69, s69, 4194304\n\t" \
    "s_add_u32 s40, s36, s69\n\t" \
    "s_addc_u32 s41, s37, 0\n\t" \
    "s_mov_b32 s91, 0\n\t" \
    "s_mov_b32 s96, 0x55555555\n\t" \
    "s_mov_b32 s58, 0\n\t" \
    "s_mov_b32 s59, -1\n\t" \
    "v_mov_b32 v160, 0\n\t" \
    "v_mov_b32 v161, 0\n\t" \
    "v_mov_b32 v162, 0\n\t" \
    "v_mov_b32 v163, 0\n\t" \
    "v_mov_b32 v185, 0\n\t" \
    "s_mov_b32 s91, 3\n\t" \
    "s_mov_b64 exec, 0\n\t" \
    "s_branch rr_h025_%=\n\t" \
    "rr_dry28_%=:\n\t" \
    "s_mov_b64 exec, -1\n\t" \
    "s_mov_b32 s91, 0\n\t" \
    "s_waitcnt vmcnt(0) lgkmcnt(0)\n\t" \
    "s_nop 7\n\t" \
    "v_mov_b32 v160, 0\n\t" \
    "v_mov_b32 v161, 0\n\t" \
    "v_mov_b32 v162, 0\n\t" \
    "v_mov_b32 v163, 0\n\t" \
    "rr_p129_%=:\n\t" \
    "s_mov_b32 exec_lo, 0\n\t" \
    "global_load_dword v189, v187, s[52:53] sc1\n\t" \
    "s_mov_b64 exec, -1\n\t" \
    "s_waitcnt vmcnt(0)\n\t" \
    "v_cmp_eq_u32 vcc, -1, v189\n\t" \
    "s_nop 1\n\t" \
    "s_cmp_eq_u32 vcc_hi, 0\n\t" \
    "s_cbranch_scc1 rr_p1x30_%=\n\t" \
    "s_cmp_eq_u32 s68, 0\n\t" \
    "s_cbranch_scc1 rr_F31_%=\n\t" \
    "s_sub_u32 s68, s68, 1\n\t" \
    "s_sleep 10\n\t" \
    "s_branch rr_p129_%=\n\t" \
    "rr_F31_%=:\n\t" \
    "v_mov_b32 v184, 6095104\n\t" \
    "v_mov_b32 v185, 13\n\t" \
    "global_store_dword v184, v185, s[36:37] sc1\n\t" \
    "s_waitcnt vmcnt(0)\n\t" \
    "s_branch rr_p1x30_%=\n\t" \
    "rr_p1x30_%=:\n\t" \
    "s_mov_b32 s58, 1\n\t" \
    "rr_pg21_%=:\n\t" \
    "global_load_dword v177, v6, s[36:37] sc1\n\t" \
    "s_waitcnt vmcnt(0)\n\t" \
    "v_cmp_le_i32 vcc, s65, v177\n\t" \
    "s_nop 1\n\t" \
    "s_lshr_b32 s69, vcc_hi, 1\n\t" \
    "s_and_b32 s69, s69, vcc_hi\n\t" \
    "s_and_b32 s54, s69, s96\n\t" \
    "s_andn2_b32 s96, s96, s54\n\t" \
    "s_mov_b32 s55, s54\n\t" \
    "s_lshr_b64 s[54:55], s[54:55], s50\n\t" \
    "s_and_b32 s69, s54, 85\n\t" \
    "s_cbranch_scc0 rr_pq32_%=\n\t" \
    "s_bitcmp1_b32 s54, 0\n\t" \
    "s_cbranch_scc0 rr_pk33_%=\n\t" \
    "global_load_dwordx4 a[0:3], v26, s[38:39] offset:0 sc1\n\t" \
    "global_load_dwordx4 a[4:7], v26, s[38:39] offset:1024 sc1\n\t" \
    "rr_pk33_%=:\n\t" \
    "s_bitcmp1_b32 s54, 2\n\t" \
    "s_cbranch_scc0 rr_pk34_%=\n\t" \
    "global_load_dwordx4 a[8:11], v27, s[38:39] offset:0 sc1\n\t" \
    "global_load_dwordx4 a[12:15], v27, s[38:39] offset:1024 sc1\n\t" \
    "rr_pk34_%=:\n\t" \
    "s_bitcmp1_b32 s54, 4\n\t" \
    "s_cbranch_scc0 rr_pk35_%=\n\t" \
    "global_load_dwordx4 a[16:19], v28, s[38:39] offset:0 sc1\n\t" \
    "global_load_dwordx4 a[20:23], v28, s[38:39] offset:1024 sc1\n\t" \
    "rr_pk35_%=:\n\t" \
    "s_bitcmp1_b32 s54, 6\n\t" \
    "s_cbranch_scc0 rr_pk36_%=\n\t" \
    "global_load_dwordx4 a[24:27], v29, s[38:39] offset:0 sc1\n\t" \
    "global_load_dwordx4 a[28:31], v29, s[38:39] offset:1024 sc1\n\t" \
    "rr_pk36_%=:\n\t" \
    "rr_pq32_%=:\n\t" \
    "s_and_b32 s69, s54, 21760\n\t" \
    "s_cbranch_scc0 rr_pq37_%=\n\t" \
    "s_bitcmp1_b32 s54, 8\n\t" \
    "s_cbranch_scc0 rr_pk38_%=\n\t" \
    "global_load_dwordx4 a[32:35], v30, s[38:39] offset:0 sc1\n\t" \
    "global_load_dwordx4 a[36:39], v30, s[38:39] offset:1024 sc1\n\t" \
    "rr_pk38_%=:\n\t" \
    "s_bitcmp1_b32 s54, 10\n\t" \
    "s_cbranch_scc0 rr_pk39_%=\n\t" \
    "global_load_dwordx4 a[40:43], v31, s[38:39] offset:0 sc1\n\t" \
    "global_load_dwordx4 a[44:47], v31, s[38:39] offset:1024 sc1\n\t" \
    "rr_pk39_%=:\n\t" \
    "s_bitcmp1_b32 s54, 12\n\t" \
    "s_cbranch_scc0 rr_pk40_%=\n\t" \
    "global_load_dwordx4 a[48:51], v32, s[38:39] offset:0 sc1\n\t" \
    "global_load_dwordx4 a[52:55], v32, s[38:39] offset:1024 sc1\n\t" \
    "rr_pk40_%=:\n\t" \
    "s_bitcmp1_b32 s54, 14\n\t" \
    "s_cbranch_scc0 rr_pk41_%=\n\t" \
    "global_load_dwordx4 a[56:59], v33, s[38:39] offset:0 sc1\n\t" \
    "global_load_dwordx4 a[60:63], v33, s[38:39] offset:1024 sc1\n\t" \
    "rr_pk41_%=:\n\t" \
    "rr_pq37_%=:\n\t" \
    "s_and_b32 s69, s54, 5570560\n\t" \
    "s_cbranch_scc0 rr_pq42_%=\n\t" \
    "s_bitcmp1_b32 s54, 16\n\t" \
    "s_cbranch_scc0 rr_pk43_%=\n\t" \
    "global_load_dwordx4 a[64:67], v34, s[38:39] offset:0 sc1\n\t" \
    "global_load_dwordx4 a[68:71], v34, s[38:39] offset:1024 sc1\n\t" \
    "rr_pk43_%=:\n\t" \
    "s_bitcmp1_b32 s54, 18\n\t" \
    "s_cbranch_scc0 rr_pk44_%=\n\t" \
    "global_load_dwordx4 a[72:75], v35, s[38:39] offset:0 sc1\n\t" \
    "global_load_dwordx4 a[76:79], v35, s[38:39] offset:1024 sc1\n\t" \
    "rr_pk44_%=:\n\t" \
    "s_bitcmp1_b32 s54, 20\n\t" \
    "s_cbranch_scc0 rr_pk45_%=\n\t" \
    "global_load_dwordx4 a[80:83], v36, s[38:39] offset:0 sc1\n\t" \
    "global_load_dwordx4 a[84:87], v36, s[38:39] offset:1024 sc1\n\t" \
    "rr_pk45_%=:\n\t" \
    "s_bitcmp1_b32 s54, 22\n\t" \
    "s_cbranch_scc0 rr_pk46_%=\n\t" \
    "global_load_dwordx4 a[88:91], v37, s[38:39] offset:0 sc1\n\t" \
    "global_load_dwordx4 a[92:95], v37, s[38:39] offset:1024 sc1\n\t" \
    "rr_pk46_%=:\n\t" \
    "rr_pq42_%=:\n\t" \
    "s_and_b32 s69, s54, 1426063360\n\t" \
    "s_cbranch_scc0 rr_pq47_%=\n\t" \
    "s_bitcmp1_b32 s54, 24\n\t" \
    "s_cbranch_scc0 rr_pk48_%=\n\t" \
    "global_load_dwordx4 a[96:99], v38, s[38:39] offset:0 sc1\n\t" \
    "global_load_dwordx4 a[100:103], v38, s[38:39] offset:1024 sc1\n\t" \
    "rr_pk48_%=:\n\t" \
    "s_bitcmp1_b32 s54, 26\n\t" \
    "s_cbranch_scc0 rr_pk49_%=\n\t" \
    "global_load_dwordx4 a[104:107], v39, s[38:39] offset:0 sc1\n\t" \
    "global_load_dwordx4 a[108:111], v39, s[38:39] offset:1024 sc1\n\t" \
    "rr_pk49_%=:\n\t" \
    "s_bitcmp1_b32 s54, 28\n\t" \
    "s_cbranch_scc0 rr_pk50_%=\n\t" \
    "global_load_dwordx4 a[112:115], v40, s[38:39] offset:0 sc1\n\t" \
    "global_load_dwordx4 a[116:119], v40, s[38:39] offset:1024 sc1\n\t" \
    "rr_pk50_%=:\n\t" \
    "s_bitcmp1_b32 s54, 30\n\t" \
    "s_cbranch_scc0 rr_pk51_%=\n\t" \
    "global_load_dwordx4 a[120:123], v41, s[38:39] offset:0 sc1\n\t" \
    "global_load_dwordx4 a[124:127], v41, s[38:39] offset:1024 sc1\n\t" \
    "rr_pk51_%=:\n\t" \
    "rr_pq47_%=:\n\t" \
    "s_cmp_eq_u32 s58, 0\n\t" \
    "s_cbranch_scc1 rr_pn22_%=\n\t" \
    "v_cmp_eq_u32 vcc, -1, v189\n\t" \
    "s_cbranch_vccz rr_p2x52_%=\n\t" \
    "rr_p253_%=:\n\t" \
    "s_mov_b32 exec_hi, 0\n\t" \
    "global_load_dword v189, v187, s[52:53] sc1\n\t" \
    "s_mov_b64 exec, -1\n\t" \
    "s_waitcnt vmcnt(0)\n\t" \
    "v_cmp_eq_u32 vcc, -1, v189\n\t" \
    "s_cbranch_vccz rr_p2x52_%=\n\t" \
    "s_cmp_eq_u32 s68, 0\n\t" \
    "s_cbranch_scc1 rr_F54_%=\n\t" \
    "s_sub_u32 s68, s68, 1\n\t" \
    "s_sleep 10\n\t" \
    "s_branch rr_p253_%=\n\t" \
    "rr_F54_%=:\n\t" \
    "v_mov_b32 v184, 6095104\n\t" \
    "v_mov_b32 v185, 13\n\t" \
    "global_store_dword v184, v185, s[36:37] sc1\n\t" \
    "s_waitcnt vmcnt(0)\n\t" \
    "s_branch rr_p2x52_%=\n\t" \
    "rr_p2x52_%=:\n\t" \
    "s_mov_b32 s71, 3\n\t" \
    "rr_E55_%=:\n\t" \
    "s_memtime s[54:55]\n\t" \
    "s_nop 0\n\t" \
    "s_memrealtime s[4:5]\n\t" \
    "s_nop 0\n\t" \
    "s_memrealtime s[6:7]\n\t" \
    "s_nop 0\n\t" \
    "s_memrealtime s[8:9]\n\t" \
    "s_nop 0\n\t" \
    "s_memrealtime s[10:11]\n\t" \
    "s_nop 0\n\t" \
    "s_memrealtime s[12:13]\n\t" \
    "s_nop 0\n\t" \
    "s_memrealtime s[14:15]\n\t" \
    "s_nop 0\n\t" \
    "s_memrealtime s[16:17]\n\t" \
    "s_nop 0\n\t" \
    "s_memrealtime s[18:19]\n\t" \
    "s_nop 0\n\t" \
    "s_memrealtime s[20:21]\n\t" \
    "s_nop 0\n\t" \
    "s_memrealtime s[22:23]\n\t" \
    "s_nop 0\n\t" \
    "s_memrealtime s[24:25]\n\t" \
    "s_nop 0\n\t" \
    "s_memrealtime s[26:27]\n\t" \
    "s_nop 0\n\t" \
    "s_memrealtime s[28:29]\n\t" \
    "s_nop 0\n\t" \
    "s_memtime s[56:57]\n\t" \
    "s_mov_b32 s70, 1\n\t" \
    "s_sub_u32 s71, s71, 1\n\t" \
    "s_cselect_b32 s69, 0, -1\n\t" \
    "s_waitcnt lgkmcnt(0)\n\t" \
    "s_sub_u32 s54, s56, s54\n\t" \
    "s_and_b32 s54, s54, s69\n\t" \
    "s_cmp_gt_u32 s54, 124\n\t" \
    "s_cbranch_scc1 rr_E55_%=\n\t" \
    "s_cmp_eq_u32 s4, s28\n\t" \
    "s_addc_u32 s70, s70, 0\n\t" \
    "s_cmp_eq_u32 s6, s28\n\t" \
    "s_addc_u32 s70, s70, 0\n\t" \
    "s_cmp_eq_u32 s8, s28\n\t" \
    "s_addc_u32 s70, s70, 0\n\t" \
    "s_cmp_eq_u32 s10, s28\n\t" \
    "s_addc_u32 s70, s70, 0\n\t" \
    "s_cmp_eq_u32 s12, s28\n\t" \
    "s_addc_u32 s70, s70, 0\n\t" \
    "s_cmp_eq_u32 s14, s28\n\t" \
    "s_addc_u32 s70, s70, 0\n\t" \
    "s_cmp_eq_u32 s16, s28\n\t" \
    "s_addc_u32 s70, s70, 0\n\t" \
    "s_cmp_eq_u32 s18, s28\n\t" \
    "s_addc_u32 s70, s70, 0\n\t" \
    "s_cmp_eq_u32 s20, s28\n\t" \
    "s_addc_u32 s70, s70, 0\n\t" \
    "s_cmp_eq_u32 s22, s28\n\t" \
    "s_addc_u32 s70, s70, 0\n\t" \
    "s_cmp_eq_u32 s24, s28\n\t" \
    "s_addc_u32 s70, s70, 0\n\t" \
    "s_cmp_eq_u32 s26, s28\n\t" \
    "s_addc_u32 s70, s70, 0\n\t" \
    "s_mov_b32 s75, s73\n\t" \
    "s_mov_b32 s76, s74\n\t" \
    "s_lshl_b32 s70, s70, 3\n\t" \
    "s_sub_u32 s73, s56, s70\n\t" \
    "s_lshl_b32 s74, s28, 4\n\t" \
    "s_sub_u32 s69, s73, s75\n\t" \
    "s_sub_u32 s70, s74, s76\n\t" \
    "s_sub_u32 s71, s70, 1600\n\t" \
    "s_cmp_lt_u32 s71, 400000\n\t" \
    "s_cbranch_scc0 rr_K57_%=\n\t" \
    "v_cvt_f32_u32 v190, s69\n\t" \
    "v_cvt_f32_u32 v191, s70\n\t" \
    "v_rcp_f32 v192, v190\n\t" \
    "v_rcp_f32 v193, v191\n\t" \
    "v_mul_f32 v192, v192, v191\n\t" \
    "v_mul_f32 v193, v193, v190\n\t" \
    "v_mul_f32 v192, 0x4f800000, v192\n\t" \
    "v_mul_f32 v193, 0x4f000000, v193\n\t" \
    "v_cvt_u32_f32 v192, v192\n\t" \
    "v_cvt_u32_f32 v193, v193\n\t" \
    "v_readfirstlane_b32 s69, v192\n\t" \
    "v_readfirstlane_b32 s70, v193\n\t" \
    "s_nop 1\n\t" \
    "s_sub_u32 s69, s69, s78\n\t" \
    "s_ashr_i32 s69, s69, 2\n\t" \
    "s_add_u32 s78, s78, s69\n\t" \
    "s_sub_u32 s70, s70, s77\n\t" \
    "s_ashr_i32 s70, s70, 2\n\t" \
    "s_add_u32 s77, s77, s70\n\t" \
    "rr_K57_%=:\n\t" \
    "v_mov_b32 v184, v189\n\t" \
    "v_subrev_u32 v184, s80, v184\n\t" \
    "s_nop 1\n\t" \
    "v_max_i32_dpp v184, v184, v184 row_ror:1 row_mask:0xf bank_mask:0xf\n\t" \
    "s_nop 1\n\t" \
    "v_max_i32_dpp v184, v184, v184 row_ror:2 row_mask:0xf bank_mask:0xf\n\t" \
    "s_nop 1\n\t" \
    "v_max_i32_dpp v184, v184, v184 row_ror:4 row_mask:0xf bank_mask:0xf\n\t" \
    "s_nop 1\n\t" \
    "v_max_i32_dpp v184, v184, v184 row_ror:8 row_mask:0xf bank_mask:0xf\n\t" \
    "s_nop 0\n\t" \
    "v_readlane_b32 s69, v184, 0\n\t" \
    "v_readlane_b32 s71, v184, 16\n\t" \
    "v_readlane_b32 s70, v184, 32\n\t" \
    "v_readlane_b32 s81, v184, 48\n\t" \
    "s_max_i32 s69, s69, s71\n\t" \
    "s_nop 0\n\t" \
    "s_max_i32 s71, s70, s81\n\t" \
    "s_max_i32 s69, s69, s71\n\t" \
    "s_add_u32 s81, s69, s80\n\t" \
    "s_mov_b32 s58, 0\n\t" \
    "s_cmp_eq_u32 s96, 0\n\t" \
    "s_cbranch_scc1 rr_pe23_%=\n\t" \
    "s_add_u32 s79, s71, s80\n\t" \
    "s_sub_u32 s79, s79, 880\n\t" \
    "s_sub_u32 s70, s79, s74\n\t" \
    "s_max_i32 s70, s70, 0\n\t" \
    "s_min_i32 s70, s70, 16000\n\t" \
    "s_lshl_b32 s70, s70, 1\n\t" \
    "s_mul_hi_u32 s70, s70, s77\n\t" \
    "s_add_u32 s70, s70, s73\n\t" \
    "rr_W58_%=:\n\t" \
    "s_memtime s[56:57]\n\t" \
    "s_waitcnt lgkmcnt(0)\n\t" \
    "s_sub_u32 s69, s70, s56\n\t" \
    "s_cmp_gt_i32 s69, 24\n\t" \
    "s_cbranch_scc1 rr_W58_%=\n\t" \
    "s_branch rr_pg21_%=\n\t" \
    "rr_pn22_%=:\n\t" \
    "s_cmp_eq_u32 s96, 0\n\t" \
    "s_cbranch_scc1 rr_pe23_%=\n\t" \
    "s_cmp_eq_u32 s68, 0\n\t" \
    "s_cbranch_scc1 rr_F59_%=\n\t" \
    "s_sub_u32 s68, s68, 1\n\t" \
    "s_sleep 2\n\t" \
    "s_branch rr_pg21_%=\n\t" \
    "rr_F59_%=:\n\t" \
    "v_mov_b32 v184, 6095104\n\t" \
    "v_mov_b32 v185, 10\n\t" \
    "global_store_dword v184, v185, s[36:37] sc1\n\t" \
    "s_waitcnt vmcnt(0)\n\t" \
    "s_branch rr_pe23_%=\n\t" \
    "rr_pe23_%=:\n\t" \
    "s_mov_b32 s83, s65\n\t" \
    "s_cmp_lg_u32 s65, 0\n\t" \
    "s_cbranch_scc1 rr_u227_%=\n\t" \
    "s_memtime s[56:57]\n\t" \
    "v_mov_b32 v184, v189\n\t" \
    "v_subrev_u32 v184, s80, v184\n\t" \
    "s_nop 1\n\t" \
    "v_max_i32_dpp v184, v184, v184 row_ror:1 row_mask:0xf bank_mask:0xf\n\t" \
    "s_nop 1\n\t" \
    "v_max_i32_dpp v184, v184, v184 row_ror:2 row_mask:0xf bank_mask:0xf\n\t" \
    "s_nop 1\n\t" \
    "v_max_i32_dpp v184, v184, v184 row_ror:4 row_mask:0xf bank_mask:0xf\n\t" \
    "s_nop 1\n\t" \
    "v_max_i32_dpp v184, v184, v184 row_ror:8 row_mask:0xf bank_mask:0xf\n\t" \
    "s_nop 0\n\t" \
    "v_readlane_b32 s69, v184, 0\n\t" \
    "v_readlane_b32 s71, v184, 16\n\t" \
    "s_nop 1\n\t" \
    "s_max_i32 s69, s69, s71\n\t" \
    "s_add_u32 s81, s69, s80\n\t" \
    "s_waitcnt lgkmcnt(0)\n\t" \
    "s_sub_u32 s69, s56, s73\n\t" \
    "s_mul_hi_u32 s69, s69, s78\n\t" \
    "s_add_u32 s69, s69, s74\n\t" \
    "s_add_u32 s69, s69, 208\n\t" \
    "s_sub_u32 s69, s69, s81\n\t" \
    "s_max_i32 s69, s69, 0\n\t" \
    "s_add_u32 s69, s69, s81\n\t" \
    "v_and_b32_e64 v184, -2, s69\n\t" \
    "s_add_u32 s69, s69, 160\n\t" \
    "v_and_b32_e64 v185, -2, s69\n\t" \
    "s_add_u32 s69, s65, 0\n\t" \
    "s_bitcmp1_b32 s69, 0\n\t" \
    "s_cselect_b32 s70, s63, s62\n\t" \
    "s_bfe_u32 s69, s69, 0x20001\n\t" \
    "s_lshl_b32 s69, s69, 19\n\t" \
    "s_add_u32 s69, s69, s70\n\t" \
    "s_add_u32 s52, s36, s69\n\t" \
    "s_addc_u32 s53, s37, 0\n\t" \
    "global_store_dword v3, v185, s[52:53] sc1\n\t" \
    "s_branch rr_h126_%=\n\t" \
    "rr_pf13_%=:\n\t" \
    "s_cmp_gt_u32 s65, 62\n\t" \
    "s_cbranch_scc1 rr_end8_%=\n\t" \
    "v_min_i32_dpp v177, v177, v177 row_ror:1 row_mask:0xf bank_mask:0xf\n\t" \
    "s_nop 1\n\t" \
    "v_min_i32_dpp v177, v177, v177 row_ror:2 row_mask:0xf bank_mask:0xf\n\t" \
    "s_nop 1\n\t" \
    "v_min_i32_dpp v177, v177, v177 row_ror:4 row_mask:0xf bank_mask:0xf\n\t" \
    "s_nop 1\n\t" \
    "v_min_i32_dpp v177, v177, v177 row_ror:8 row_mask:0xf bank_mask:0xf\n\t" \
    "s_nop 0\n\t" \
    "v_readlane_b32 s69, v177, 0\n\t" \
    "v_readlane_b32 s70, v177, 16\n\t" \
    "s_nop 1\n\t" \
    "s_min_i32 s69, s69, s70\n\t" \
    "s_max_i32 s83, s83, s69\n\t" \
    "rr_WF60_%=:\n\t" \
    "s_cmp_ge_i32 s83, s65\n\t" \
    "s_cbranch_scc1 rr_WFok61_%=\n\t" \
    "global_load_dword v184, v6, s[36:37] sc1\n\t" \
    "s_waitcnt vmcnt(0)\n\t" \
    "v_min_i32_dpp v184, v184, v184 row_ror:1 row_mask:0xf bank_mask:0xf\n\t" \
    "s_nop 1\n\t" \
    "v_min_i32_dpp v184, v184, v184 row_ror:2 row_mask:0xf bank_mask:0xf\n\t" \
    "s_nop 1\n\t" \
    "v_min_i32_dpp v184, v184, v184 row_ror:4 row_mask:0xf bank_mask:0xf\n\t" \
    "s_nop 1\n\t" \
    "v_min_i32_dpp v184, v184, v184 row_ror:8 row_mask:0xf bank_mask:0xf\n\t" \
    "s_nop 0\n\t" \
    "v_readlane_b32 s69, v184, 0\n\t" \
    "v_readlane_b32 s70, v184, 16\n\t" \
    "s_nop 1\n\t" \
    "s_min_i32 s83, s69, s70\n\t" \
    "s_cmp_ge_i32 s83, s65\n\t" \
    "s_cbranch_scc1 rr_WFok61_%=\n\t" \
    "s_cmp_eq_u32 s68, 0\n\t" \
    "s_cbranch_scc1 rr_F62_%=\n\t" \
    "s_sub_u32 s68, s68, 1\n\t" \
    "s_sleep 2\n\t" \
    "s_branch rr_WF60_%=\n\t" \
    "rr_F62_%=:\n\t" \
    "v_mov_b32 v184, 6095104\n\t" \
    "v_mov_b32 v185, 10\n\t" \
    "global_store_dword v184, v185, s[36:37] sc1\n\t" \
    "s_waitcnt vmcnt(0)\n\t" \
    "s_branch rr_WFok61_%=\n\t" \
    "rr_WFok61_%=:\n\t" \
    "s_mul_i32 s70, s67, 7\n\t" \
    "s_add_u32 s69, s65, 0\n\t" \
    "s_xor_b32 s69, s69, s70\n\t" \
    "s_and_b32 s69, s69, 63\n\t" \
    "s_lshl_b32 s69, s69, 15\n\t" \
    "s_mul_i32 s70, s67, 2097152\n\t" \
    "s_add_u32 s69, s69, s70\n\t" \
    "s_add_u32 s69, s69, 48234496\n\t" \
    "s_add_u32 s38, s36, s69\n\t" \
    "s_addc_u32 s39, s37, 0\n\t" \
    "s_mul_hi_u32 s69, s85, s78\n\t" \
    "s_add_u32 s79, s81, s69\n\t" \
    "s_add_u32 s79, s79, s87\n\t" \
    "s_cmp_ge_u32 s65, 38\n\t" \
    "s_cselect_b32 s69, 28, 0\n\t" \
    "s_sub_u32 s79, s79, s69\n\t" \
    "s_cmp_lt_u32 s65, 10\n\t" \
    "s_cselect_b32 s69, 96, 0\n\t" \
    "s_add_u32 s79, s79, s69\n\t" \
    "s_cmp_ge_u32 s65, 59\n\t" \
    "s_cselect_b32 s69, 96, 0\n\t" \
    "s_add_u32 s79, s79, s69\n\t" \
    "s_add_u32 s80, s79, s88\n\t" \
    "s_add_u32 s79, s79, -96\n\t" \
    "s_add_u32 s79, s79, s42\n\t" \
    "s_sub_u32 s70, s79, s74\n\t" \
    "s_max_i32 s70, s70, 0\n\t" \
    "s_min_i32 s70, s70, 16000\n\t" \
    "s_lshl_b32 s70, s70, 1\n\t" \
    "s_mul_hi_u32 s70, s70, s77\n\t" \
    "s_add_u32 s70, s70, s73\n\t" \
    "rr_W63_%=:\n\t" \
    "s_memtime s[56:57]\n\t" \
    "s_waitcnt lgkmcnt(0)\n\t" \
    "s_sub_u32 s69, s70, s56\n\t" \
    "s_cmp_gt_i32 s69, 24\n\t" \
    "s_cbranch_scc1 rr_W63_%=\n\t" \
    "s_add_u32 s69, s65, 4\n\t" \
    "s_min_i32 s69, s69, 62\n\t" \
    "s_cmp_ge_i32 s83, s69\n\t" \
    "s_cbranch_scc1 rr_nfl64_%=\n\t" \
    "global_load_dword v177, v6, s[36:37] sc1\n\t" \
    "rr_nfl64_%=:\n\t" \
    "global_load_dwordx4 a[0:3], v26, s[38:39] offset:0 sc1\n\t" \
    "global_load_dwordx4 a[4:7], v26, s[38:39] offset:1024 sc1\n\t" \
    "global_load_dwordx4 a[8:11], v27, s[38:39] offset:0 sc1\n\t" \
    "global_load_dwordx4 a[12:15], v27, s[38:39] offset:1024 sc1\n\t" \
    "global_load_dwordx4 a[16:19], v28, s[38:39] offset:0 sc1\n\t" \
    "global_load_dwordx4 a[20:23], v28, s[38:39] offset:1024 sc1\n\t" \
    "global_load_dwordx4 a[24:27], v29, s[38:39] offset:0 sc1\n\t" \
    "global_load_dwordx4 a[28:31], v29, s[38:39] offset:1024 sc1\n\t" \
    "global_load_dwordx4 a[32:35], v30, s[38:39] offset:0 sc1\n\t" \
    "global_load_dwordx4 a[36:39], v30, s[38:39] offset:1024 sc1\n\t" \
    "global_load_dwordx4 a[40:43], v31, s[38:39] offset:0 sc1\n\t" \
    "global_load_dwordx4 a[44:47], v31, s[38:39] offset:1024 sc1\n\t" \
    "global_load_dwordx4 a[48:51], v32, s[38:39] offset:0 sc1\n\t" \
    "global_load_dwordx4 a[52:55], v32, s[38:39] offset:1024 sc1\n\t" \
    "global_load_dwordx4 a[56:59], v33, s[38:39] offset:0 sc1\n\t" \
    "global_load_dwordx4 a[60:63], v33, s[38:39] offset:1024 sc1\n\t" \
    "global_load_dwordx4 a[64:67], v34, s[38:39] offset:0 sc1\n\t" \
    "global_load_dwordx4 a[68:71], v34, s[38:39] offset:1024 sc1\n\t" \
    "global_load_dwordx4 a[72:75], v35, s[38:39] offset:0 sc1\n\t" \
    "global_load_dwordx4 a[76:79], v35, s[38:39] offset:1024 sc1\n\t" \
    "global_load_dwordx4 a[80:83], v36, s[38:39] offset:0 sc1\n\t" \
    "global_load_dwordx4 a[84:87], v36, s[38:39] offset:1024 sc1\n\t" \
    "global_load_dwordx4 a[88:91], v37, s[38:39] offset:0 sc1\n\t" \
    "global_load_dwordx4 a[92:95], v37, s[38:39] offset:1024 sc1\n\t" \
    "global_load_dwordx4 a[96:99], v38, s[38:39] offset:0 sc1\n\t" \
    "global_load_dwordx4 a[100:103], v38, s[38:39] offset:1024 sc1\n\t" \
    "global_load_dwordx4 a[104:107], v39, s[38:39] offset:0 sc1\n\t" \
    "global_load_dwordx4 a[108:111], v39, s[38:39] offset:1024 sc1\n\t" \
    "global_load_dwordx4 a[112:115], v40, s[38:39] offset:0 sc1\n\t" \
    "global_load_dwordx4 a[116:119], v40, s[38:39] offset:1024 sc1\n\t" \
    "global_load_dwordx4 a[120:123], v41, s[38:39] offset:0 sc1\n\t" \
    "global_load_dwordx4 a[124:127], v41, s[38:39] offset:1024 sc1\n\t" \
    "s_cmp_eq_u32 s51, 0\n\t" \
    "s_cbranch_scc1 rr_nb65_%=\n\t" \
    "s_add_u32 s69, s65, 2\n\t" \
    "s_cmp_lt_i32 s83, s69\n\t" \
    "s_cbranch_scc1 rr_nb65_%=\n\t" \
    "s_add_u32 s69, s65, 1\n\t" \
    "s_lshl_b32 s69, s69, 15\n\t" \
    "v_xor_b32 v201, s69, v199\n\t" \
    "v_xor_b32 v202, s69, v200\n\t" \
    "global_load_dword v201, v201, s[36:37] sc1\n\t" \
    "global_load_dword v202, v202, s[36:37] sc1\n\t" \
    "rr_nb65_%=:\n\t" \
    "s_mul_hi_u32 s69, s85, s78\n\t" \
    "s_add_u32 s79, s81, s69\n\t" \
    "s_add_u32 s79, s79, s87\n\t" \
    "s_cmp_ge_u32 s65, 38\n\t" \
    "s_cselect_b32 s69, 28, 0\n\t" \
    "s_sub_u32 s79, s79, s69\n\t" \
    "s_cmp_lt_u32 s65, 10\n\t" \
    "s_cselect_b32 s69, 96, 0\n\t" \
    "s_add_u32 s79, s79, s69\n\t" \
    "s_cmp_ge_u32 s65, 59\n\t" \
    "s_cselect_b32 s69, 96, 0\n\t" \
    "s_add_u32 s79, s79, s69\n\t" \
    "s_add_u32 s80, s79, s88\n\t" \
    "s_add_u32 s79, s79, 320\n\t" \
    "s_add_u32 s79, s79, s43\n\t" \
    "s_sub_u32 s70, s79, s74\n\t" \
    "s_max_i32 s70, s70, 0\n\t" \
    "s_min_i32 s70, s70, 16000\n\t" \
    "s_lshl_b32 s70, s70, 1\n\t" \
    "s_mul_hi_u32 s70, s70, s77\n\t" \
    "s_add_u32 s70, s70, s73\n\t" \
    "rr_W66_%=:\n\t" \
    "s_memtime s[56:57]\n\t" \
    "s_waitcnt lgkmcnt(0)\n\t" \
    "s_sub_u32 s69, s70, s56\n\t" \
    "s_cmp_gt_i32 s69, 24\n\t" \
    "s_cbranch_scc1 rr_W66_%=\n\t" \
    "s_cmp_eq_u32 s51, 0\n\t" \
    "s_cbranch_scc1 rr_arm67_%=\n\t" \
    "global_store_dwordx4 v0, v[172:175], s[40:41] offset:0 sc1\n\t" \
    "global_store_dwordx4 v0, v[172:175], s[40:41] offset:1024 sc1\n\t" \
    "global_store_dwordx4 v0, v[172:175], s[40:41] offset:2048 sc1\n\t" \
    "global_store_dwordx4 v0, v[172:175], s[40:41] offset:3072 sc1\n\t" \
    "global_store_dwordx4 v1, v[172:175], s[40:41] offset:0 sc1\n\t" \
    "global_store_dwordx4 v1, v[172:175], s[40:41] offset:1024 sc1\n\t" \
    "global_store_dwordx4 v1, v[172:175], s[40:41] offset:2048 sc1\n\t" \
    "global_store_dwordx4 v1, v[172:175], s[40:41] offset:3072 sc1\n\t" \
    "s_cmp_gt_u32 s65, 61\n\t" \
    "s_cbranch_scc1 rr_arm67_%=\n\t" \
    "s_add_u32 s69, s65, 2\n\t" \
    "s_bitcmp1_b32 s69, 0\n\t" \
    "s_cselect_b32 s70, s63, s62\n\t" \
    "s_bfe_u32 s69, s69, 0x20001\n\t" \
    "s_lshl_b32 s69, s69, 19\n\t" \
    "s_add_u32 s69, s69, s70\n\t" \
    "s_add_u32 s46, s36, s69\n\t" \
    "s_addc_u32 s47, s37, 0\n\t" \
    "global_store_dwordx4 v164, v[172:175], s[46:47] sc1\n\t" \
    "global_store_dwordx4 v165, v[172:175], s[46:47] sc1\n\t" \
    "global_store_dwordx4 v166, v[172:175], s[46:47] sc1\n\t" \
    "global_store_dwordx4 v167, v[172:175], s[46:47] sc1\n\t" \
    "rr_arm67_%=:\n\t" \
    "s_waitcnt vmcnt(0)\n\t" \
    "v_min_i32_dpp v177, v177, v177 row_ror:1 row_mask:0xf bank_mask:0xf\n\t" \
    "s_nop 1\n\t" \
    "v_min_i32_dpp v177, v177, v177 row_ror:2 row_mask:0xf bank_mask:0xf\n\t" \
    "s_nop 1\n\t" \
    "v_min_i32_dpp v177, v177, v177 row_ror:4 row_mask:0xf bank_mask:0xf\n\t" \
    "s_nop 1\n\t" \
    "v_min_i32_dpp v177, v177, v177 row_ror:8 row_mask:0xf bank_mask:0xf\n\t" \
    "s_nop 0\n\t" \
    "v_readlane_b32 s69, v177, 0\n\t" \
    "v_readlane_b32 s70, v177, 16\n\t" \
    "s_nop 1\n\t" \
    "s_min_i32 s69, s69, s70\n\t" \
    "s_max_i32 s83, s83, s69\n\t" \
    "s_cmp_lg_u32 s65, 40\n\t" \
    "s_cbranch_scc1 rr_f268_%=\n\t" \
    "s_cmp_lg_u32 s67, 35\n\t" \
    "s_cbranch_scc1 rr_f268_%=\n\t" \
    "rr_f2l69_%=:\n\t" \
    "global_load_dword v186, v178, s[36:37] sc1\n\t" \
    "s_waitcnt vmcnt(0)\n\t" \
    "v_cmp_ne_u32 vcc, 1, v186\n\t" \
    "s_cbranch_vccz rr_f268_%=\n\t" \
    "s_cmp_eq_u32 s68, 0\n\t" \
    "s_cbranch_scc1 rr_F70_%=\n\t" \
    "s_sub_u32 s68, s68, 1\n\t" \
    "s_sleep 2\n\t" \
    "s_branch rr_f2l69_%=\n\t" \
    "rr_F70_%=:\n\t" \
    "v_mov_b32 v187, 6095104\n\t" \
    "v_mov_b32 v188, 11\n\t" \
    "global_store_dword v187, v188, s[36:37] sc1\n\t" \
    "s_waitcnt vmcnt(0)\n\t" \
    "s_branch rr_f268_%=\n\t" \
    "rr_f268_%=:\n\t" \
    "s_cmp_eq_u32 s51, 0\n\t" \
    "s_cbranch_scc1 rr_sl24_%=\n\t" \
    "s_add_u32 s69, s65, -1\n\t" \
    "s_bitcmp1_b32 s69, 0\n\t" \
    "s_cselect_b32 s70, s63, s62\n\t" \
    "s_bfe_u32 s69, s69, 0x20001\n\t" \
    "s_lshl_b32 s69, s69, 19\n\t" \
    "s_add_u32 s69, s69, s70\n\t" \
    "s_add_u32 s46, s36, s69\n\t" \
    "s_addc_u32 s47, s37, 0\n\t" \
    "s_add_u32 s69, s65, -1\n\t" \
    "s_lshr_b32 s69, s69, 1\n\t" \
    "s_xor_b32 s69, s69, s33\n\t" \
    "s_and_b32 s69, s69, 1\n\t" \
    "s_lshl_b32 s69, s69, 14\n\t" \
    "s_or_b32 s46, s46, s69\n\t" \
    "s_add_u32 s79, s80, 480\n\t" \
    "s_sub_u32 s70, s79, s74\n\t" \
    "s_max_i32 s70, s70, 0\n\t" \
    "s_min_i32 s70, s70, 16000\n\t" \
    "s_lshl_b32 s70, s70, 1\n\t" \
    "s_mul_hi_u32 s70, s70, s77\n\t" \
    "s_add_u32 s70, s70, s73\n\t" \
    "rr_W71_%=:\n\t" \
    "s_memtime s[56:57]\n\t" \
    "s_waitcnt lgkmcnt(0)\n\t" \
    "s_sub_u32 s69, s70, s56\n\t" \
    "s_cmp_gt_i32 s69, 24\n\t" \
    "s_cbranch_scc1 rr_W71_%=\n\t" \
    "v_add_u32 v187, 4096, v7\n\t" \
    "global_load_dword v184, v187, s[46:47] sc1\n\t" \
    "s_waitcnt vmcnt(0)\n\t" \
    "v_cmp_eq_u32 vcc, -1, v184\n\t" \
    "s_cbranch_vccz rr_Hok73_%=\n\t" \
    "rr_H72_%=:\n\t" \
    "global_load_dword v184, v187, s[46:47] sc1\n\t" \
    "s_waitcnt vmcnt(0)\n\t" \
    "v_cmp_eq_u32 vcc, -1, v184\n\t" \
    "s_cbranch_vccz rr_Hc74_%=\n\t" \
    "s_cmp_eq_u32 s68, 0\n\t" \
    "s_cbranch_scc1 rr_F75_%=\n\t" \
    "s_sub_u32 s68, s68, 1\n\t" \
    "s_sleep 4\n\t" \
    "s_branch rr_H72_%=\n\t" \
    "rr_F75_%=:\n\t" \
    "v_mov_b32 v185, 6095104\n\t" \
    "v_mov_b32 v186, 19\n\t" \
    "global_store_dword v185, v186, s[36:37] sc1\n\t" \
    "s_waitcnt vmcnt(0)\n\t" \
    "s_branch rr_Hc74_%=\n\t" \
    "rr_Hc74_%=:\n\t" \
    "rr_Hok73_%=:\n\t" \
    "v_subrev_u32 v184, s80, v184\n\t" \
    "s_nop 1\n\t" \
    "v_max_i32_dpp v184, v184, v184 row_ror:1 row_mask:0xf bank_mask:0xf\n\t" \
    "s_nop 1\n\t" \
    "v_max_i32_dpp v184, v184, v184 row_ror:2 row_mask:0xf bank_mask:0xf\n\t" \
    "s_nop 1\n\t" \
    "v_max_i32_dpp v184, v184, v184 row_ror:4 row_mask:0xf bank_mask:0xf\n\t" \
    "s_nop 1\n\t" \
    "v_max_i32_dpp v184, v184, v184 row_ror:8 row_mask:0xf bank_mask:0xf\n\t" \
    "s_nop 0\n\t" \
    "v_readlane_b32 s69, v184, 0\n\t" \
    "v_readlane_b32 s71, v184, 16\n\t" \
    "s_nop 1\n\t" \
    "s_max_i32 s69, s69, s71\n\t" \
    "s_add_u32 s81, s69, s80\n\t" \
    "s_mul_hi_u32 s69, s85, s78\n\t" \
    "s_add_u32 s79, s81, s69\n\t" \
    "s_add_u32 s79, s79, s87\n\t" \
    "s_cmp_ge_u32 s65, 37\n\t" \
    "s_cselect_b32 s69, 28, 0\n\t" \
    "s_sub_u32 s79, s79, s69\n\t" \
    "s_cmp_lt_u32 s65, 9\n\t" \
    "s_cselect_b32 s69, 96, 0\n\t" \
    "s_add_u32 s79, s79, s69\n\t" \
    "s_cmp_ge_u32 s65, 58\n\t" \
    "s_cselect_b32 s69, 96, 0\n\t" \
    "s_add_u32 s79, s79, s69\n\t" \
    "s_add_u32 s80, s79, s88\n\t" \
    "s_add_u32 s79, s79, 96\n\t" \
    "s_add_u32 s69, s65, 1\n\t" \
    "s_mul_i32 s70, s69, 171\n\t" \
    "s_lshr_b32 s70, s70, 9\n\t" \
    "s_mul_i32 s70, s70, 3\n\t" \
    "s_sub_u32 s69, s69, s70\n\t" \
    "s_mul_i32 s69, s69, 36\n\t" \
    "s_lshl_b32 s69, s69, 14\n\t" \
    "s_add_u32 s69, s69, s64\n\t" \
    "s_add_u32 s69, s69, 4194304\n\t" \
    "s_add_u32 s40, s36, s69\n\t" \
    "s_addc_u32 s41, s37, 0\n\t" \
    "s_sub_u32 s70, s79, s74\n\t" \
    "s_max_i32 s70, s70, 0\n\t" \
    "s_min_i32 s70, s70, 16000\n\t" \
    "s_lshl_b32 s70, s70, 1\n\t" \
    "s_mul_hi_u32 s70, s70, s77\n\t" \
    "s_add_u32 s70, s70, s73\n\t" \
    "rr_W76_%=:\n\t" \
    "s_memtime s[56:57]\n\t" \
    "s_waitcnt lgkmcnt(0)\n\t" \
    "s_sub_u32 s69, s70, s56\n\t" \
    "s_cmp_gt_i32 s69, 24\n\t" \
    "s_cbranch_scc1 rr_W76_%=\n\t" \
    "global_store_dwordx4 v196, v[172:175], s[40:41] sc1\n\t" \
    "global_store_dwordx4 v197, v[172:175], s[40:41] sc1\n\t" \
    "global_store_dwordx4 v198, v[172:175], s[40:41] sc1\n\t" \
    "s_branch rr_u227_%=\n\t" \
    "rr_sl24_%=:\n\t" \
    "ds_read_b64 v[184:185], v9\n\t" \
    "s_sub_u32 s70, s65, 1\n\t" \
    "s_waitcnt lgkmcnt(0)\n\t" \
    "v_readfirstlane_b32 s69, v185\n\t" \
    "v_readfirstlane_b32 s81, v184\n\t" \
    "s_nop 0\n\t" \
    "s_cmp_eq_u32 s69, s70\n\t" \
    "s_cbranch_scc1 rr_u227_%=\n\t" \
    "s_cmp_eq_u32 s68, 0\n\t" \
    "s_cbranch_scc1 rr_F77_%=\n\t" \
    "s_sub_u32 s68, s68, 1\n\t" \
    "s_sleep 2\n\t" \
    "s_branch rr_sl24_%=\n\t" \
    "rr_F77_%=:\n\t" \
    "v_mov_b32 v184, 6095104\n\t" \
    "v_mov_b32 v185, 14\n\t" \
    "global_store_dword v184, v185, s[36:37] sc1\n\t" \
    "s_waitcnt vmcnt(0)\n\t" \
    "s_branch rr_u227_%=\n\t" \
    "rr_u227_%=:\n\t" \
    "s_add_u32 s69, s65, -1\n\t" \
    "s_bitcmp1_b32 s69, 0\n\t" \
    "s_cselect_b32 s70, s63, s62\n\t" \
    "s_bfe_u32 s69, s69, 0x20001\n\t" \
    "s_lshl_b32 s69, s69, 19\n\t" \
    "s_add_u32 s69, s69, s70\n\t" \
    "s_add_u32 s46, s36, s69\n\t" \
    "s_addc_u32 s47, s37, 0\n\t" \
    "s_add_u32 s69, s65, -1\n\t" \
    "s_lshr_b32 s69, s69, 1\n\t" \
    "s_xor_b32 s69, s69, s33\n\t" \
    "s_and_b32 s69, s69, 1\n\t" \
    "s_lshl_b32 s69, s69, 14\n\t" \
    "s_or_b32 s46, s46, s69\n\t" \
    "s_cmp_lg_u32 s65, 1\n\t" \
    "s_cbranch_scc1 rr_Oc80_%=\n\t" \
    "s_add_u32 s79, s81, 480\n\t" \
    "s_sub_u32 s70, s79, s74\n\t" \
    "s_max_i32 s70, s70, 0\n\t" \
    "s_min_i32 s70, s70, 16000\n\t" \
    "s_lshl_b32 s70, s70, 1\n\t" \
    "s_mul_hi_u32 s70, s70, s77\n\t" \
    "s_add_u32 s70, s70, s73\n\t" \
    "rr_W81_%=:\n\t" \
    "s_memtime s[56:57]\n\t" \
    "s_waitcnt lgkmcnt(0)\n\t" \
    "s_sub_u32 s69, s70, s56\n\t" \
    "s_cmp_gt_i32 s69, 24\n\t" \
    "s_cbranch_scc1 rr_W81_%=\n\t" \
    "global_load_dword v184, v46, s[46:47] sc1\n\t" \
    "s_waitcnt vmcnt(0)\n\t" \
    "v_cmp_eq_u32 vcc, -1, v184\n\t" \
    "s_cbranch_vccz rr_Ook79_%=\n\t" \
    "rr_O78_%=:\n\t" \
    "global_load_dword v184, v46, s[46:47] offset:1536 sc1\n\t" \
    "s_waitcnt vmcnt(0)\n\t" \
    "v_cmp_eq_u32 vcc, -1, v184\n\t" \
    "s_cbranch_vccz rr_Ook79_%=\n\t" \
    "s_cmp_eq_u32 s68, 0\n\t" \
    "s_cbranch_scc1 rr_F82_%=\n\t" \
    "s_sub_u32 s68, s68, 1\n\t" \
    "s_sleep 3\n\t" \
    "s_branch rr_O78_%=\n\t" \
    "rr_F82_%=:\n\t" \
    "v_mov_b32 v185, 6095104\n\t" \
    "v_mov_b32 v186, 18\n\t" \
    "global_store_dword v185, v186, s[36:37] sc1\n\t" \
    "s_waitcnt vmcnt(0)\n\t" \
    "s_branch rr_Ook79_%=\n\t" \
    "rr_Ook79_%=:\n\t" \
    "v_subrev_u32 v184, s80, v184\n\t" \
    "s_nop 1\n\t" \
    "v_max_i32_dpp v184, v184, v184 row_ror:1 row_mask:0xf bank_mask:0xf\n\t" \
    "s_nop 1\n\t" \
    "v_max_i32_dpp v184, v184, v184 row_ror:2 row_mask:0xf bank_mask:0xf\n\t" \
    "s_nop 1\n\t" \
    "v_max_i32_dpp v184, v184, v184 row_ror:4 row_mask:0xf bank_mask:0xf\n\t" \
    "s_nop 1\n\t" \
    "v_max_i32_dpp v184, v184, v184 row_ror:8 row_mask:0xf bank_mask:0xf\n\t" \
    "s_nop 0\n\t" \
    "v_readlane_b32 s69, v184, 0\n\t" \
    "v_readlane_b32 s71, v184, 16\n\t" \
    "v_readlane_b32 s70, v184, 32\n\t" \
    "v_readlane_b32 s81, v184, 48\n\t" \
    "s_max_i32 s69, s69, s71\n\t" \
    "s_nop 0\n\t" \
    "s_max_i32 s71, s70, s81\n\t" \
    "s_max_i32 s69, s69, s71\n\t" \
    "s_add_u32 s81, s69, s80\n\t" \
    "rr_Oc80_%=:\n\t" \
    "s_mul_hi_u32 s69, s85, s78\n\t" \
    "s_add_u32 s79, s81, s69\n\t" \
    "s_add_u32 s79, s79, s87\n\t" \
    "s_cmp_ge_u32 s65, 37\n\t" \
    "s_cselect_b32 s69, 28, 0\n\t" \
    "s_sub_u32 s79, s79, s69\n\t" \
    "s_cmp_lt_u32 s65, 9\n\t" \
    "s_cselect_b32 s69, 96, 0\n\t" \
    "s_add_u32 s79, s79, s69\n\t" \
    "s_cmp_ge_u32 s65, 58\n\t" \
    "s_cselect_b32 s69, 96, 0\n\t" \
    "s_add_u32 s79, s79, s69\n\t" \
    "s_add_u32 s80, s79, s88\n\t" \
    "s_sub_u32 s70, s79, s74\n\t" \
    "s_max_i32 s70, s70, 0\n\t" \
    "s_min_i32 s70, s70, 16000\n\t" \
    "s_lshl_b32 s70, s70, 1\n\t" \
    "s_mul_hi_u32 s70, s70, s77\n\t" \
    "s_add_u32 s70, s70, s73\n\t" \
    "rr_W83_%=:\n\t" \
    "s_memtime s[56:57]\n\t" \
    "s_waitcnt lgkmcnt(0)\n\t" \
    "s_sub_u32 s69, s70, s56\n\t" \
    "s_cmp_gt_i32 s69, 24\n\t" \
    "s_cbranch_scc1 rr_W83_%=\n\t" \
    "rr_act7_%=:\n\t" \
    "s_add_u32 s69, s65, 0\n\t" \
    "s_bitcmp1_b32 s69, 0\n\t" \
    "s_cselect_b32 s70, s63, s62\n\t" \
    "s_bfe_u32 s69, s69, 0x20001\n\t" \
    "s_lshl_b32 s69, s69, 19\n\t" \
    "s_add_u32 s69, s69, s70\n\t" \
    "s_add_u32 s46, s36, s69\n\t" \
    "s_addc_u32 s47, s37, 0\n\t" \
    "s_add_u32 s69, s65, 0\n\t" \
    "s_lshr_b32 s69, s69, 1\n\t" \
    "s_xor_b32 s69, s69, s33\n\t" \
    "s_and_b32 s69, s69, 1\n\t" \
    "s_lshl_b32 s69, s69, 14\n\t" \
    "s_or_b32 s46, s46, s69\n\t" \
    "s_add_u32 s69, s65, 1\n\t" \
    "s_bitcmp1_b32 s69, 0\n\t" \
    "s_cselect_b32 s70, s63, s62\n\t" \
    "s_bfe_u32 s69, s69, 0x20001\n\t" \
    "s_lshl_b32 s69, s69, 19\n\t" \
    "s_add_u32 s69, s69, s70\n\t" \
    "s_add_u32 s48, s36, s69\n\t" \
    "s_addc_u32 s49, s37, 0\n\t" \
    "s_add_u32 s69, s65, 0\n\t" \
    "s_mul_i32 s70, s69, 171\n\t" \
    "s_lshr_b32 s70, s70, 9\n\t" \
    "s_mul_i32 s70, s70, 3\n\t" \
    "s_sub_u32 s69, s69, s70\n\t" \
    "s_mul_i32 s69, s69, 36\n\t" \
    "s_add_u32 s69, s69, s67\n\t" \
    "s_lshl_b32 s69, s69, 14\n\t" \
    "s_add_u32 s69, s69, s64\n\t" \
    "s_add_u32 s69, s69, 4194304\n\t" \
    "s_add_u32 s40, s36, s69\n\t" \
    "s_addc_u32 s41, s37, 0\n\t" \
    "s_add_u32 s69, s65, 2\n\t" \
    "s_cmp_ge_i32 s83, s69\n\t" \
    "s_cbranch_scc1 rr_nf84_%=\n\t" \
    "global_load_dword v177, v6, s[36:37] sc1\n\t" \
    "rr_nf84_%=:\n\t" \
    "s_mov_b32 s71, 3\n\t" \
    "rr_E85_%=:\n\t" \
    "s_memtime s[54:55]\n\t" \
    "s_nop 0\n\t" \
    "s_memrealtime s[4:5]\n\t" \
    "s_nop 0\n\t" \
    "s_memrealtime s[6:7]\n\t" \
    "s_nop 0\n\t" \
    "s_memrealtime s[8:9]\n\t" \
    "s_nop 0\n\t" \
    "s_memrealtime s[10:11]\n\t" \
    "s_nop 0\n\t" \
    "s_memrealtime s[12:13]\n\t" \
    "s_nop 0\n\t" \
    "s_memrealtime s[14:15]\n\t" \
    "s_nop 0\n\t" \
    "s_memrealtime s[16:17]\n\t" \
    "s_nop 0\n\t" \
    "s_memrealtime s[18:19]\n\t" \
    "s_nop 0\n\t" \
    "s_memrealtime s[20:21]\n\t" \
    "s_nop 0\n\t" \
    "s_memrealtime s[22:23]\n\t" \
    "s_nop 0\n\t" \
    "s_memrealtime s[24:25]\n\t" \
    "s_nop 0\n\t" \
    "s_memrealtime s[26:27]\n\t" \
    "s_nop 0\n\t" \
    "s_memrealtime s[28:29]\n\t" \
    "s_nop 0\n\t" \
    "s_memtime s[56:57]\n\t" \
    "s_mov_b32 s70, 1\n\t" \
    "s_sub_u32 s71, s71, 1\n\t" \
    "s_cselect_b32 s69, 0, -1\n\t" \
    "s_waitcnt lgkmcnt(0)\n\t" \
    "s_sub_u32 s54, s56, s54\n\t" \
    "s_and_b32 s54, s54, s69\n\t" \
    "s_cmp_gt_u32 s54, 124\n\t" \
    "s_cbranch_scc1 rr_E85_%=\n\t" \
    "s_cmp_eq_u32 s4, s28\n\t" \
    "s_addc_u32 s70, s70, 0\n\t" \
    "s_cmp_eq_u32 s6, s28\n\t" \
    "s_addc_u32 s70, s70, 0\n\t" \
    "s_cmp_eq_u32 s8, s28\n\t" \
    "s_addc_u32 s70, s70, 0\n\t" \
    "s_cmp_eq_u32 s10, s28\n\t" \
    "s_addc_u32 s70, s70, 0\n\t" \
    "s_cmp_eq_u32 s12, s28\n\t" \
    "s_addc_u32 s70, s70, 0\n\t" \
    "s_cmp_eq_u32 s14, s28\n\t" \
    "s_addc_u32 s70, s70, 0\n\t" \
    "s_cmp_eq_u32 s16, s28\n\t" \
    "s_addc_u32 s70, s70, 0\n\t" \
    "s_cmp_eq_u32 s18, s28\n\t" \
    "s_addc_u32 s70, s70, 0\n\t" \
    "s_cmp_eq_u32 s20, s28\n\t" \
    "s_addc_u32 s70, s70, 0\n\t" \
    "s_cmp_eq_u32 s22, s28\n\t" \
    "s_addc_u32 s70, s70, 0\n\t" \
    "s_cmp_eq_u32 s24, s28\n\t" \
    "s_addc_u32 s70, s70, 0\n\t" \
    "s_cmp_eq_u32 s26, s28\n\t" \
    "s_addc_u32 s70, s70, 0\n\t" \
    "s_mov_b32 s75, s73\n\t" \
    "s_mov_b32 s76, s74\n\t" \
    "s_lshl_b32 s70, s70, 3\n\t" \
    "s_sub_u32 s73, s56, s70\n\t" \
    "s_lshl_b32 s74, s28, 4\n\t" \
    "s_sub_u32 s69, s73, s75\n\t" \
    "s_sub_u32 s70, s74, s76\n\t" \
    "s_sub_u32 s71, s70, 1600\n\t" \
    "s_cmp_lt_u32 s71, 400000\n\t" \
    "s_cbranch_scc0 rr_K87_%=\n\t" \
    "v_cvt_f32_u32 v184, s69\n\t" \
    "v_cvt_f32_u32 v185, s70\n\t" \
    "v_rcp_f32 v186, v184\n\t" \
    "v_rcp_f32 v187, v185\n\t" \
    "v_mul_f32 v186, v186, v185\n\t" \
    "v_mul_f32 v187, v187, v184\n\t" \
    "v_mul_f32 v186, 0x4f800000, v186\n\t" \
    "v_mul_f32 v187, 0x4f000000, v187\n\t" \
    "v_cvt_u32_f32 v186, v186\n\t" \
    "v_cvt_u32_f32 v187, v187\n\t" \
    "v_readfirstlane_b32 s69, v186\n\t" \
    "v_readfirstlane_b32 s70, v187\n\t" \
    "s_nop 1\n\t" \
    "s_sub_u32 s69, s69, s78\n\t" \
    "s_ashr_i32 s69, s69, 2\n\t" \
    "s_add_u32 s78, s78, s69\n\t" \
    "s_sub_u32 s70, s70, s77\n\t" \
    "s_ashr_i32 s70, s70, 2\n\t" \
    "s_add_u32 s77, s77, s70\n\t" \
    "rr_K87_%=:\n\t" \
    "s_mov_b32 s91, 0\n\t" \
    "v_mov_b32 v160, 0\n\t" \
    "v_mov_b32 v161, 0\n\t" \
    "v_mov_b32 v162, 0\n\t" \
    "v_mov_b32 v163, 0\n\t" \
    "s_sub_u32 s70, s80, s74\n\t" \
    "s_max_i32 s70, s70, 0\n\t" \
    "s_min_i32 s70, s70, 16000\n\t" \
    "s_lshl_b32 s70, s70, 1\n\t" \
    "s_mul_hi_u32 s70, s70, s77\n\t" \
    "s_add_u32 s70, s70, s73\n\t" \
    "s_sub_u32 s70, s70, 100\n\t" \
    "s_getpc_b64 s[30:31]\n\t" \
    "rr_Wp94_%=:\n\t" \
    "s_add_u32 s30, s30, rr_We95_%=-rr_Wp94_%=\n\t" \
    "s_addc_u32 s31, s31, 0\n\t" \
    "rr_W93_%=:\n\t" \
    "s_memtime s[56:57]\n\t" \
    "s_waitcnt lgkmcnt(0)\n\t" \
    "s_sub_u32 s69, s70, s56\n\t" \
    "s_cmp_gt_i32 s69, 111\n\t" \
    "s_cbranch_scc1 rr_W93_%=\n\t" \
    "s_max_i32 s69, s69, 0\n\t" \
    "s_lshr_b32 s69, s69, 1\n\t" \
    "s_and_b32 s69, s69, 0x3c\n\t" \
    "s_sub_u32 s30, s30, s69\n\t" \
    "s_subb_u32 s31, s31, 0\n\t" \
    "s_setpc_b64 s[30:31]\n\t" \
    ".p2align 6\n\t" \
    "s_nop 0\n\t" \
    "s_nop 0\n\t" \
    "s_nop 0\n\t" \
    "s_nop 1\n\t" \
    "s_nop 1\n\t" \
    "s_nop 1\n\t" \
    "s_nop 1\n\t" \
    "s_nop 1\n\t" \
    "s_nop 1\n\t" \
    "s_nop 1\n\t" \
    "s_nop 1\n\t" \
    "s_nop 1\n\t" \
    "s_nop 1\n\t" \
    "s_nop 1\n\t" \
    "s_nop 1\n\t" \
    "s_nop 1\n\t" \
    "rr_We95_%=:\n\t" \
    "global_load_dword v184, v7, s[46:47] sc1\n\t" \
    "s_sub_u32 s97, s74, s86\n\t" \
    "s_sub_u32 s71, s73, 190\n\t" \
    "s_getpc_b64 s[30:31]\n\t" \
    "rr_wp96_%=:\n\t" \
    "s_add_u32 s30, s30, rr_again9_%=-rr_wp96_%=\n\t" \
    "s_addc_u32 s31, s31, (rr_again9_%=-rr_wp96_%=)>>32\n\t" \
    "s_waitcnt vmcnt(0) lgkmcnt(0)\n\t" \
    "rr_hok88_%=:\n\t" \
    "s_memtime s[56:57]\n\t" \
    "v_cmp_eq_u32 vcc, -1, v184\n\t" \
    "v_subrev_u32 v185, s97, v184\n\t" \
    "s_nop 1\n\t" \
    "v_max_i32_dpp v185, v185, v185 row_ror:1 row_mask:0xf bank_mask:0xf\n\t" \
    "s_cbranch_vccnz rr_hlate89_%=\n\t" \
    "s_nop 0\n\t" \
    "v_max_i32_dpp v185, v185, v185 row_ror:2 row_mask:0xf bank_mask:0xf\n\t" \
    "s_nop 1\n\t" \
    "v_max_i32_dpp v185, v185, v185 row_ror:4 row_mask:0xf bank_mask:0xf\n\t" \
    "s_nop 1\n\t" \
    "v_max_i32_dpp v185, v185, v185 row_ror:8 row_mask:0xf bank_mask:0xf\n\t" \
    "s_nop 0\n\t" \
    "v_readlane_b32 s69, v185, 0\n\t" \
    "v_readlane_b32 s70, v185, 16\n\t" \
    "s_waitcnt lgkmcnt(0)\n\t" \
    "s_sub_u32 s71, s71, s56\n\t" \
    "s_max_i32 s81, s69, s70\n\t" \
    "s_max_i32 s69, s81, 0\n\t" \
    "s_lshl_b32 s69, s69, 1\n\t" \
    "s_mul_hi_u32 s69, s69, s77\n\t" \
    "s_add_u32 s69, s69, s71\n\t" \
    "s_cmp_gt_i32 s69, 111\n\t" \
    "s_cbranch_scc1 rr_wl90_%=\n\t" \
    "rr_wf92_%=:\n\t" \
    "s_max_i32 s69, s69, 0\n\t" \
    "s_lshr_b32 s69, s69, 1\n\t" \
    "s_and_b32 s69, s69, 0x3c\n\t" \
    "s_sub_u32 s30, s30, s69\n\t" \
    "s_subb_u32 s31, s31, 0\n\t" \
    "s_setpc_b64 s[30:31]\n\t" \
    "rr_wl90_%=:\n\t" \
    "s_min_i32 s69, s69, 24000\n\t" \
    "s_add_u32 s70, s69, s56\n\t" \
    "s_add_u32 s70, s70, 62\n\t" \
    "rr_wp91_%=:\n\t" \
    "s_memtime s[56:57]\n\t" \
    "s_waitcnt lgkmcnt(0)\n\t" \
    "s_sub_u32 s69, s70, s56\n\t" \
    "s_cmp_gt_i32 s69, 111\n\t" \
    "s_cbranch_scc1 rr_wp91_%=\n\t" \
    "s_branch rr_wf92_%=\n\t" \
    ".p2align 6\n\t" \
    "s_nop 0\n\t" \
    "s_nop 0\n\t" \
    "s_nop 0\n\t" \
    "s_nop 1\n\t" \
    "s_nop 1\n\t" \
    "s_nop 1\n\t" \
    "s_nop 1\n\t" \
    "s_nop 1\n\t" \
    "s_nop 1\n\t" \
    "s_nop 1\n\t" \
    "s_nop 1\n\t" \
    "s_nop 1\n\t" \
    "s_nop 1\n\t" \
    "s_nop 1\n\t" \
    "s_nop 1\n\t" \
    "s_nop 1\n\t" \
    "rr_again9_%=:\n\t" \
    "global_load_dwordx4 v[48:51], v10, s[46:47] sc1\n\t" \
    "global_load_dwordx4 v[52:55], v11, s[46:47] sc1\n\t" \
    "global_load_dwordx4 v[56:59], v12, s[46:47] sc1\n\t" \
    "global_load_dwordx4 v[60:63], v13, s[46:47] sc1\n\t" \
    "global_load_dwordx4 v[64:67], v14, s[46:47] sc1\n\t" \
    "global_load_dwordx4 v[68:71], v15, s[46:47] sc1\n\t" \
    "global_load_dwordx4 v[72:75], v16, s[46:47] sc1\n\t" \
    "global_load_dwordx4 v[76:79], v17, s[46:47] sc1\n\t" \
    "global_load_dwordx4 v[80:83], v18, s[46:47] sc1\n\t" \
    "global_load_dwordx4 v[84:87], v19, s[46:47] sc1\n\t" \
    "global_load_dwordx4 v[88:91], v20, s[46:47] sc1\n\t" \
    "global_load_dwordx4 v[92:95], v21, s[46:47] sc1\n\t" \
    "global_load_dwordx4 v[96:99], v22, s[46:47] sc1\n\t" \
    "global_load_dwordx4 v[100:103], v23, s[46:47] sc1\n\t" \
    "global_load_dwordx4 v[104:107], v24, s[46:47] sc1\n\t" \
    "global_load_dwordx4 v[108:111], v25, s[46:47] sc1\n\t" \
    "global_load_dwordx4 v[112:115], v42, s[46:47] sc1\n\t" \
    "global_load_dwordx4 v[116:119], v43, s[46:47] sc1\n\t" \
    "global_load_dwordx4 v[120:123], v44, s[46:47] sc1\n\t" \
    "global_load_dwordx4 v[124:127], v45, s[46:47] sc1\n\t" \
    "global_load_dwordx4 v[128:131], v0, s[40:41] offset:0 sc1\n\t" \
    "global_load_dwordx4 v[132:135], v0, s[40:41] offset:1024 sc1\n\t" \
    "global_load_dwordx4 v[136:139], v0, s[40:41] offset:2048 sc1\n\t" \
    "global_load_dwordx4 v[140:143], v0, s[40:41] offset:3072 sc1\n\t" \
    "global_load_dwordx4 v[144:147], v1, s[40:41] offset:0 sc1\n\t" \
    "global_load_dwordx4 v[148:151], v1, s[40:41] offset:1024 sc1\n\t" \
    "global_load_dwordx4 v[152:155], v1, s[40:41] offset:2048 sc1\n\t" \
    "global_load_dwordx4 v[156:159], v1, s[40:41] offset:3072 sc1\n\t" \
    "s_cmp_lg_u32 s91, 0\n\t" \
    "s_cbranch_scc1 rr_nob12_%=\n\t" \
    "s_add_u32 s81, s81, s97\n\t" \
    "v_mov_b32 v184, s81\n\t" \
    "v_mov_b32 v185, s65\n\t" \
    "ds_write_b64 v9, v[184:185]\n\t" \
    "rr_nob12_%=:\n\t" \
    "s_waitcnt vmcnt(27)\n\t" \
    "v_smfmac_f32_16x16x64_bf16 v[160:163], v[48:51], a[0:7], v4\n\t" \
    "s_waitcnt vmcnt(26)\n\t" \
    "v_smfmac_f32_16x16x64_bf16 v[160:163], v[52:55], a[8:15], v4\n\t" \
    "s_waitcnt vmcnt(25)\n\t" \
    "v_smfmac_f32_16x16x64_bf16 v[160:163], v[56:59], a[16:23], v4\n\t" \
    "s_waitcnt vmcnt(24)\n\t" \
    "v_smfmac_f32_16x16x64_bf16 v[160:163], v[60:63], a[24:31], v4\n\t" \
    "s_waitcnt vmcnt(23)\n\t" \
    "v_smfmac_f32_16x16x64_bf16 v[160:163], v[64:67], a[32:39], v4\n\t" \
    "s_waitcnt vmcnt(22)\n\t" \
    "v_smfmac_f32_16x16x64_bf16 v[160:163], v[68:71], a[40:47], v4\n\t" \
    "s_waitcnt vmcnt(21)\n\t" \
    "v_smfmac_f32_16x16x64_bf16 v[160:163], v[72:75], a[48:55], v4\n\t" \
    "s_waitcnt vmcnt(20)\n\t" \
    "v_smfmac_f32_16x16x64_bf16 v[160:163], v[76:79], a[56:63], v4\n\t" \
    "s_waitcnt vmcnt(19)\n\t" \
    "v_smfmac_f32_16x16x64_bf16 v[160:163], v[80:83], a[64:71], v4\n\t" \
    "s_waitcnt vmcnt(18)\n\t" \
    "v_smfmac_f32_16x16x64_bf16 v[160:163], v[84:87], a[72:79], v4\n\t" \
    "s_waitcnt vmcnt(17)\n\t" \
    "v_smfmac_f32_16x16x64_bf16 v[160:163], v[88:91], a[80:87], v4\n\t" \
    "s_waitcnt vmcnt(16)\n\t" \
    "v_smfmac_f32_16x16x64_bf16 v[160:163], v[92:95], a[88:95], v4\n\t" \
    "s_waitcnt vmcnt(15)\n\t" \
    "v_smfmac_f32_16x16x64_bf16 v[160:163], v[96:99], a[96:103], v4\n\t" \
    "s_waitcnt vmcnt(14)\n\t" \
    "v_smfmac_f32_16x16x64_bf16 v[160:163], v[100:103], a[104:111], v4\n\t" \
    "s_waitcnt vmcnt(13)\n\t" \
    "v_smfmac_f32_16x16x64_bf16 v[160:163], v[104:107], a[112:119], v4\n\t" \
    "s_waitcnt vmcnt(12)\n\t" \
    "v_smfmac_f32_16x16x64_bf16 v[160:163], v[108:111], a[120:127], v4\n\t" \
    "s_waitcnt vmcnt(6)\n\t" \
    "v_smfmac_f32_16x16x64_bf16 v[160:163], v[112:115], v[128:135], v4\n\t" \
    "s_waitcnt vmcnt(4)\n\t" \
    "v_smfmac_f32_16x16x64_bf16 v[160:163], v[116:119], v[136:143], v4\n\t" \
    "s_waitcnt vmcnt(2)\n\t" \
    "v_smfmac_f32_16x16x64_bf16 v[160:163], v[120:123], v[144:151], v4\n\t" \
    "s_waitcnt vmcnt(0)\n\t" \
    "v_smfmac_f32_16x16x64_bf16 v[160:163], v[124:127], v[152:159], v4\n\t" \
    "s_nop 7\n\t" \
    "v_pk_add_f32 v[160:161], v[160:161], v[162:163]\n\t" \
    "v_cvt_pk_bf16_f32 v184, v160, v161\n\t" \
    "v_cmp_u_f32 vcc, v160, v161\n\t" \
    "s_cbranch_vccnz rr_bad10_%=\n\t" \
    "rr_cont11_%=:\n\t" \
    "v_mov_b32_dpp v185, v184 quad_perm:[1,0,3,2] row_mask:0xf bank_mask:0xf\n\t" \
    "v_perm_b32 v184, v185, v184, v5\n\t" \
    "global_store_dword v2, v184, s[48:49] sc1\n\t" \
    "global_store_dword v181, v184, s[48:49] sc1\n\t" \
    "global_store_dword v182, v184, s[48:49] sc1\n\t" \
    "global_store_dword v183, v184, s[48:49] sc1\n\t" \
    "s_cmp_eq_u32 s91, 3\n\t" \
    "s_cbranch_scc1 rr_dry28_%=\n\t" \
    "s_cmp_eq_u32 s65, 62\n\t" \
    "s_cbranch_scc1 rr_end8_%=\n\t" \
    "s_add_u32 s65, s65, 2\n\t" \
    "s_branch rr_pf13_%=\n\t" \
    "rr_bad10_%=:\n\t" \
    "s_waitcnt lgkmcnt(0)\n\t" \
    "v_max_u32 v185, v48, v49\n\t" \
    "v_max3_u32 v185, v185, v50, v51\n\t" \
    "v_max3_u32 v185, v185, v52, v53\n\t" \
    "v_max3_u32 v185, v185, v54, v55\n\t" \
    "v_max3_u32 v185, v185, v56, v57\n\t" \
    "v_max3_u32 v185, v185, v58, v59\n\t" \
    "v_max3_u32 v185, v185, v60, v61\n\t" \
    "v_max3_u32 v185, v185, v62, v63\n\t" \
    "v_max3_u32 v185, v185, v64, v65\n\t" \
    "v_max3_u32 v185, v185, v66, v67\n\t" \
    "v_max3_u32 v185, v185, v68, v69\n\t" \
    "v_max3_u32 v185, v185, v70, v71\n\t" \
    "v_max3_u32 v185, v185, v72, v73\n\t" \
    "v_max3_u32 v185, v185, v74, v75\n\t" \
    "v_max3_u32 v185, v185, v76, v77\n\t" \
    "v_max3_u32 v185, v185, v78, v79\n\t" \
    "v_max3_u32 v185, v185, v80, v81\n\t" \
    "v_max3_u32 v185, v185, v82, v83\n\t" \
    "v_max3_u32 v185, v185, v84, v85\n\t" \
    "v_max3_u32 v185, v185, v86, v87\n\t" \
    "v_max3_u32 v185, v185, v88, v89\n\t" \
    "v_max3_u32 v185, v185, v90, v91\n\t" \
    "v_max3_u32 v185, v185, v92, v93\n\t" \
    "v_max3_u32 v185, v185, v94, v95\n\t" \
    "v_max3_u32 v185, v185, v96, v97\n\t" \
    "v_max3_u32 v185, v185, v98, v99\n\t" \
    "v_max3_u32 v185, v185, v100, v101\n\t" \
    "v_max3_u32 v185, v185, v102, v103\n\t" \
    "v_max3_u32 v185, v185, v104, v105\n\t" \
    "v_max3_u32 v185, v185, v106, v107\n\t" \
    "v_max3_u32 v185, v185, v108, v109\n\t" \
    "v_max3_u32 v185, v185, v110, v111\n\t" \
    "v_max3_u32 v185, v185, v112, v113\n\t" \
    "v_max3_u32 v185, v185, v114, v115\n\t" \
    "v_max3_u32 v185, v185, v116, v117\n\t" \
    "v_max3_u32 v185, v185, v118, v119\n\t" \
    "v_max3_u32 v185, v185, v120, v121\n\t" \
    "v_max3_u32 v185, v185, v122, v123\n\t" \
    "v_max3_u32 v185, v185, v124, v125\n\t" \
    "v_max3_u32 v185, v185, v126, v127\n\t" \
    "v_max_u32 v186, v128, v129\n\t" \
    "v_max3_u32 v186, v186, v130, v131\n\t" \
    "v_max3_u32 v186, v186, v132, v133\n\t" \
    "v_max3_u32 v186, v186, v134, v135\n\t" \
    "v_max3_u32 v186, v186, v136, v137\n\t" \
    "v_max3_u32 v186, v186, v138, v139\n\t" \
    "v_max3_u32 v186, v186, v140, v141\n\t" \
    "v_max3_u32 v186, v186, v142, v143\n\t" \
    "v_max3_u32 v186, v186, v144, v145\n\t" \
    "v_max3_u32 v186, v186, v146, v147\n\t" \
    "v_max3_u32 v186, v186, v148, v149\n\t" \
    "v_max3_u32 v186, v186, v150, v151\n\t" \
    "v_max3_u32 v186, v186, v152, v153\n\t" \
    "v_max3_u32 v186, v186, v154, v155\n\t" \
    "v_max3_u32 v186, v186, v156, v157\n\t" \
    "v_max3_u32 v186, v186, v158, v159\n\t" \
    "v_max_u32 v185, v185, v186\n\t" \
    "v_cmp_eq_u32 vcc, -1, v185\n\t" \
    "s_cbranch_vccz rr_cont11_%=\n\t" \
    "s_mov_b32 s91, 1\n\t" \
    "v_mov_b32 v160, 0\n\t" \
    "v_mov_b32 v161, 0\n\t" \
    "v_mov_b32 v162, 0\n\t" \
    "v_mov_b32 v163, 0\n\t" \
    "s_cmp_eq_u32 s68, 0\n\t" \
    "s_cbranch_scc1 rr_F97_%=\n\t" \
    "s_sub_u32 s68, s68, 1\n\t" \
    "s_branch rr_again9_%=\n\t" \
    "rr_F97_%=:\n\t" \
    "v_mov_b32 v186, 6095104\n\t" \
    "v_mov_b32 v187, 3\n\t" \
    "global_store_dword v186, v187, s[36:37] sc1\n\t" \
    "s_waitcnt vmcnt(0)\n\t" \
    "s_branch rr_cont11_%=\n\t" \
    "rr_hlate89_%=:\n\t" \
    "s_waitcnt lgkmcnt(0)\n\t" \
    "v_add_u32 v189, 1024, v7\n\t" \
    "global_load_dword v186, v189, s[46:47] sc1\n\t" \
    "s_sleep 4\n\t" \
    "v_add_u32 v189, 2048, v7\n\t" \
    "global_load_dword v187, v189, s[46:47] sc1\n\t" \
    "s_waitcnt vmcnt(1)\n\t" \
    "v_cmp_eq_u32 vcc, -1, v186\n\t" \
    "s_cbranch_vccz rr_hg98_%=\n\t" \
    "v_add_u32 v189, 3072, v7\n\t" \
    "global_load_dword v186, v189, s[46:47] sc1\n\t" \
    "s_waitcnt vmcnt(1)\n\t" \
    "v_cmp_eq_u32 vcc, -1, v187\n\t" \
    "s_cbranch_vccz rr_hg99_%=\n\t" \
    "s_waitcnt vmcnt(0)\n\t" \
    "v_cmp_eq_u32 vcc, -1, v186\n\t" \
    "s_cbranch_vccz rr_hg98_%=\n\t" \
    "rr_hp100_%=:\n\t" \
    "global_load_dword v184, v189, s[46:47] sc1\n\t" \
    "s_waitcnt vmcnt(0)\n\t" \
    "v_cmp_eq_u32 vcc, -1, v184\n\t" \
    "s_cbranch_vccz rr_hpd101_%=\n\t" \
    "s_cmp_eq_u32 s68, 0\n\t" \
    "s_cbranch_scc1 rr_F102_%=\n\t" \
    "s_sub_u32 s68, s68, 1\n\t" \
    "s_sleep 1\n\t" \
    "s_branch rr_hp100_%=\n\t" \
    "rr_F102_%=:\n\t" \
    "v_mov_b32 v186, 6095104\n\t" \
    "v_mov_b32 v187, 7\n\t" \
    "global_store_dword v186, v187, s[36:37] sc1\n\t" \
    "s_waitcnt vmcnt(0)\n\t" \
    "s_branch rr_hpd101_%=\n\t" \
    "rr_hpd101_%=:\n\t" \
    "rr_h025_%=:\n\t" \
    "v_and_b32 v184, -2, v184\n\t" \
    "s_mov_b32 s71, 3\n\t" \
    "rr_E103_%=:\n\t" \
    "s_memtime s[54:55]\n\t" \
    "s_nop 0\n\t" \
    "s_memrealtime s[4:5]\n\t" \
    "s_nop 0\n\t" \
    "s_memrealtime s[6:7]\n\t" \
    "s_nop 0\n\t" \
    "s_memrealtime s[8:9]\n\t" \
    "s_nop 0\n\t" \
    "s_memrealtime s[10:11]\n\t" \
    "s_nop 0\n\t" \
    "s_memrealtime s[12:13]\n\t" \
    "s_nop 0\n\t" \
    "s_memrealtime s[14:15]\n\t" \
    "s_nop 0\n\t" \
    "s_memrealtime s[16:17]\n\t" \
    "s_nop 0\n\t" \
    "s_memrealtime s[18:19]\n\t" \
    "s_nop 0\n\t" \
    "s_memrealtime s[20:21]\n\t" \
    "s_nop 0\n\t" \
    "s_memrealtime s[22:23]\n\t" \
    "s_nop 0\n\t" \
    "s_memrealtime s[24:25]\n\t" \
    "s_nop 0\n\t" \
    "s_memrealtime s[26:27]\n\t" \
    "s_nop 0\n\t" \
    "s_memrealtime s[28:29]\n\t" \
    "s_nop 0\n\t" \
    "s_memtime s[56:57]\n\t" \
    "s_mov_b32 s70, 1\n\t" \
    "s_sub_u32 s71, s71, 1\n\t" \
    "s_cselect_b32 s69, 0, -1\n\t" \
    "s_waitcnt lgkmcnt(0)\n\t" \
    "s_sub_u32 s54, s56, s54\n\t" \
    "s_and_b32 s54, s54, s69\n\t" \
    "s_cmp_gt_u32 s54, 124\n\t" \
    "s_cbranch_scc1 rr_E103_%=\n\t" \
    "s_cmp_eq_u32 s4, s28\n\t" \
    "s_addc_u32 s70, s70, 0\n\t" \
    "s_cmp_eq_u32 s6, s28\n\t" \
    "s_addc_u32 s70, s70, 0\n\t" \
    "s_cmp_eq_u32 s8, s28\n\t" \
    "s_addc_u32 s70, s70, 0\n\t" \
    "s_cmp_eq_u32 s10, s28\n\t" \
    "s_addc_u32 s70, s70, 0\n\t" \
    "s_cmp_eq_u32 s12, s28\n\t" \
    "s_addc_u32 s70, s70, 0\n\t" \
    "s_cmp_eq_u32 s14, s28\n\t" \
    "s_addc_u32 s70, s70, 0\n\t" \
    "s_cmp_eq_u32 s16, s28\n\t" \
    "s_addc_u32 s70, s70, 0\n\t" \
    "s_cmp_eq_u32 s18, s28\n\t" \
    "s_addc_u32 s70, s70, 0\n\t" \
    "s_cmp_eq_u32 s20, s28\n\t" \
    "s_addc_u32 s70, s70, 0\n\t" \
    "s_cmp_eq_u32 s22, s28\n\t" \
    "s_addc_u32 s70, s70, 0\n\t" \
    "s_cmp_eq_u32 s24, s28\n\t" \
    "s_addc_u32 s70, s70, 0\n\t" \
    "s_cmp_eq_u32 s26, s28\n\t" \
    "s_addc_u32 s70, s70, 0\n\t" \
    "s_mov_b32 s75, s73\n\t" \
    "s_mov_b32 s76, s74\n\t" \
    "s_lshl_b32 s70, s70, 3\n\t" \
    "s_sub_u32 s73, s56, s70\n\t" \
    "s_lshl_b32 s74, s28, 4\n\t" \
    "rr_h126_%=:\n\t" \
    "s_sub_u32 s97, s74, s86\n\t" \
    "s_sub_u32 s71, s73, 190\n\t" \
    "s_getpc_b64 s[30:31]\n\t" \
    "rr_wp105_%=:\n\t" \
    "s_add_u32 s30, s30, rr_again9_%=-rr_wp105_%=\n\t" \
    "s_addc_u32 s31, s31, (rr_again9_%=-rr_wp105_%=)>>32\n\t" \
    "s_branch rr_hok88_%=\n\t" \
    "rr_hg98_%=:\n\t" \
    "v_mov_b32 v184, v186\n\t" \
    "s_branch rr_hok88_%=\n\t" \
    "rr_hg99_%=:\n\t" \
    "v_mov_b32 v184, v187\n\t" \
    "s_branch rr_hok88_%=\n\t" \
    "rr_end8_%=:\n\t" \
    "s_waitcnt vmcnt(0) lgkmcnt(0)\n\t" \
    "s_endpgm\n\t" \
    "rr_last_%=:\n\t" \
    ""
#define ASM_O \
    "s_mov_b64 s[36:37], %0\n\t" \
    "s_mov_b64 s[42:43], %1\n\t" \
    "s_mov_b64 s[44:45], %2\n\t" \
    "s_mov_b32 s66, %3\n\t" \
    "s_mov_b32 s67, %4\n\t" \
    "s_mov_b64 s[50:51], %5\n\t" \
    "s_mov_b32 s82, %7\n\t" \
    "v_mbcnt_lo_u32_b32 v223, -1, 0\n\t" \
    "v_mbcnt_hi_u32_b32 v223, -1, v223\n\t" \
    "s_getpc_b64 s[46:47]\n\t" \
    "oo_pc_%=:\n\t" \
    "s_lshl_b32 s69, s66, 13\n\t" \
    "v_lshl_add_u32 v222, v223, 7, s69\n\t" \
    "v_min_u32 v222, oo_last_%=-oo_pc_%=-4, v222\n\t" \
    "global_load_dword v222, v222, s[46:47]\n\t" \
    "v_lshlrev_b32 v223, 2, v223\n\t" \
    "v_add_u32 v223, s82, v223\n\t" \
    "ds_read_b32 v0, v223 offset:0\n\t" \
    "ds_read_b32 v1, v223 offset:256\n\t" \
    "ds_read_b32 v2, v223 offset:512\n\t" \
    "ds_read_b32 v3, v223 offset:768\n\t" \
    "ds_read_b32 v4, v223 offset:1024\n\t" \
    "ds_read_b32 v5, v223 offset:1280\n\t" \
    "ds_read_b32 v6, v223 offset:1536\n\t" \
    "ds_read_b32 v7, v223 offset:1792\n\t" \
    "ds_read_b32 v8, v223 offset:2048\n\t" \
    "ds_read_b32 v9, v223 offset:2304\n\t" \
    "ds_read_b32 v10, v223 offset:2560\n\t" \
    "ds_read_b32 v11, v223 offset:2816\n\t" \
    "s_waitcnt lgkmcnt(0)\n\t" \
    "ds_read_b32 v12, v223 offset:3072\n\t" \
    "ds_read_b32 v13, v223 offset:3328\n\t" \
    "ds_read_b32 v14, v223 offset:3584\n\t" \
    "ds_read_b32 v15, v223 offset:3840\n\t" \
    "ds_read_b32 v16, v223 offset:4096\n\t" \
    "ds_read_b32 v17, v223 offset:4352\n\t" \
    "s_waitcnt lgkmcnt(0)\n\t" \
    "s_mov_b32 s68, 262144\n\t" \
    "s_mov_b32 s72, 0\n\t" \
    "s_mov_b32 s83, -1\n\t" \
    "s_mov_b32 s65, 0\n\t" \
    "s_barrier\n\t" \
    "s_mov_b32 s65, s67\n\t" \
    "s_mov_b32 s96, s67\n\t" \
    "s_add_u32 s38, s36, 48234496\n\t" \
    "s_addc_u32 s39, s37, 0\n\t" \
    "oo_poll1_%=:\n\t" \
    "s_cmp_gt_u32 s96, 34\n\t" \
    "s_cselect_b64 vcc, -1, 0\n\t" \
    "v_cndmask_b32 v25, v12, v13, vcc\n\t" \
    "global_load_dword v24, v25, s[36:37] sc1\n\t" \
    "s_waitcnt vmcnt(0)\n\t" \
    "v_cmp_gt_i32 vcc, s96, v24\n\t" \
    "s_cbranch_vccz oo_go2_%=\n\t" \
    "s_cmp_eq_u32 s68, 0\n\t" \
    "s_cbranch_scc1 oo_F7_%=\n\t" \
    "s_sub_u32 s68, s68, 1\n\t" \
    "s_sleep 10\n\t" \
    "s_branch oo_poll1_%=\n\t" \
    "oo_F7_%=:\n\t" \
    "v_mov_b32 v25, 6095104\n\t" \
    "v_mov_b32 v26, 40\n\t" \
    "global_store_dword v25, v26, s[36:37] sc1\n\t" \
    "s_waitcnt vmcnt(0)\n\t" \
    "s_branch oo_go2_%=\n\t" \
    "oo_go2_%=:\n\t" \
    "s_mul_i32 s69, s96, 65536\n\t" \
    "s_add_u32 s69, s69, 146800640\n\t" \
    "s_add_u32 s40, s36, s69\n\t" \
    "s_addc_u32 s41, s37, 0\n\t" \
    "s_lshl_b32 s69, s96, 15\n\t" \
    "v_xor_b32 v18, s69, v0\n\t" \
    "v_xor_b32 v19, s69, v1\n\t" \
    "v_xor_b32 v20, s69, v2\n\t" \
    "v_xor_b32 v21, s69, v3\n\t" \
    "v_xor_b32 v22, s69, v4\n\t" \
    "v_xor_b32 v23, s69, v5\n\t" \
    "global_load_dwordx4 v[32:35], v6, s[40:41] offset:-4096 sc1\n\t" \
    "global_load_dwordx4 v[36:39], v6, s[40:41] offset:-3072 sc1\n\t" \
    "global_load_dwordx4 a[0:3], v18, s[38:39] offset:-4096 sc1\n\t" \
    "global_load_dwordx4 a[4:7], v19, s[38:39] offset:-4096 sc1\n\t" \
    "global_load_dwordx4 a[8:11], v20, s[38:39] offset:-4096 sc1\n\t" \
    "global_load_dwordx4 a[12:15], v21, s[38:39] offset:-4096 sc1\n\t" \
    "global_load_dwordx4 a[16:19], v22, s[38:39] offset:-4096 sc1\n\t" \
    "global_load_dwordx4 a[20:23], v23, s[38:39] offset:-4096 sc1\n\t" \
    "global_load_dwordx4 v[40:43], v6, s[40:41] offset:-2048 sc1\n\t" \
    "global_load_dwordx4 v[44:47], v6, s[40:41] offset:-1024 sc1\n\t" \
    "global_load_dwordx4 a[24:27], v18, s[38:39] offset:-3072 sc1\n\t" \
    "global_load_dwordx4 a[28:31], v19, s[38:39] offset:-3072 sc1\n\t" \
    "global_load_dwordx4 a[32:35], v20, s[38:39] offset:-3072 sc1\n\t" \
    "global_load_dwordx4 a[36:39], v21, s[38:39] offset:-3072 sc1\n\t" \
    "global_load_dwordx4 a[40:43], v22, s[38:39] offset:-3072 sc1\n\t" \
    "global_load_dwordx4 a[44:47], v23, s[38:39] offset:-3072 sc1\n\t" \
    "global_load_dwordx4 v[48:51], v6, s[40:41] offset:0 sc1\n\t" \
    "global_load_dwordx4 v[52:55], v6, s[40:41] offset:1024 sc1\n\t" \
    "global_load_dwordx4 a[48:51], v18, s[38:39] offset:-2048 sc1\n\t" \
    "global_load_dwordx4 a[52:55], v19, s[38:39] offset:-2048 sc1\n\t" \
    "global_load_dwordx4 a[56:59], v20, s[38:39] offset:-2048 sc1\n\t" \
    "global_load_dwordx4 a[60:63], v21, s[38:39] offset:-2048 sc1\n\t" \
    "global_load_dwordx4 a[64:67], v22, s[38:39] offset:-2048 sc1\n\t" \
    "global_load_dwordx4 a[68:71], v23, s[38:39] offset:-2048 sc1\n\t" \
    "global_load_dwordx4 v[56:59], v6, s[40:41] offset:2048 sc1\n\t" \
    "global_load_dwordx4 v[60:63], v6, s[40:41] offset:3072 sc1\n\t" \
    "global_load_dwordx4 a[72:75], v18, s[38:39] offset:-1024 sc1\n\t" \
    "global_load_dwordx4 a[76:79], v19, s[38:39] offset:-1024 sc1\n\t" \
    "global_load_dwordx4 a[80:83], v20, s[38:39] offset:-1024 sc1\n\t" \
    "global_load_dwordx4 a[84:87], v21, s[38:39] offset:-1024 sc1\n\t" \
    "global_load_dwordx4 a[88:91], v22, s[38:39] offset:-1024 sc1\n\t" \
    "global_load_dwordx4 a[92:95], v23, s[38:39] offset:-1024 sc1\n\t" \
    "oo_loop3_%=:\n\t" \
    "s_mul_i32 s69, s96, 16384\n\t" \
    "s_add_u32 s69, s69, 150994944\n\t" \
    "s_add_u32 s46, s36, s69\n\t" \
    "s_addc_u32 s47, s37, 0\n\t" \
    "s_mul_i32 s69, s96, 9216\n\t" \
    "s_add_u32 s69, s69, 0\n\t" \
    "s_add_u32 s48, s36, s69\n\t" \
    "s_addc_u32 s49, s37, 0\n\t" \
    "s_waitcnt vmcnt(29)\n\t" \
    "v_mfma_f32_16x16x32_bf16 v[72:75], a[0:3], v[32:35], 0\n\t" \
    "global_load_dwordx4 v[64:67], v7, s[40:41] offset:-4096 sc1\n\t" \
    "v_mfma_f32_16x16x32_bf16 v[76:79], a[0:3], v[36:39], 0\n\t" \
    "global_load_dwordx4 v[68:71], v7, s[40:41] offset:-3072 sc1\n\t" \
    "s_waitcnt vmcnt(30)\n\t" \
    "v_mfma_f32_16x16x32_bf16 v[80:83], a[4:7], v[32:35], 0\n\t" \
    "global_load_dwordx4 a[96:99], v18, s[38:39] offset:0 sc1\n\t" \
    "v_mfma_f32_16x16x32_bf16 v[84:87], a[4:7], v[36:39], 0\n\t" \
    "global_load_dwordx4 a[100:103], v19, s[38:39] offset:0 sc1\n\t" \
    "s_waitcnt vmcnt(31)\n\t" \
    "v_mfma_f32_16x16x32_bf16 v[88:91], a[8:11], v[32:35], 0\n\t" \
    "global_load_dwordx4 a[104:107], v20, s[38:39] offset:0 sc1\n\t" \
    "v_mfma_f32_16x16x32_bf16 v[92:95], a[8:11], v[36:39], 0\n\t" \
    "global_load_dwordx4 a[108:111], v21, s[38:39] offset:0 sc1\n\t" \
    "s_waitcnt vmcnt(32)\n\t" \
    "v_mfma_f32_16x16x32_bf16 v[96:99], a[12:15], v[32:35], 0\n\t" \
    "global_load_dwordx4 a[112:115], v22, s[38:39] offset:0 sc1\n\t" \
    "v_mfma_f32_16x16x32_bf16 v[100:103], a[12:15], v[36:39], 0\n\t" \
    "global_load_dwordx4 a[116:119], v23, s[38:39] offset:0 sc1\n\t" \
    "s_waitcnt vmcnt(33)\n\t" \
    "v_mfma_f32_16x16x32_bf16 v[104:107], a[16:19], v[32:35], 0\n\t" \
    "v_mfma_f32_16x16x32_bf16 v[108:111], a[16:19], v[36:39], 0\n\t" \
    "s_waitcnt vmcnt(32)\n\t" \
    "v_mfma_f32_16x16x32_bf16 v[112:115], a[20:23], v[32:35], 0\n\t" \
    "v_mfma_f32_16x16x32_bf16 v[116:119], a[20:23], v[36:39], 0\n\t" \
    "s_add_u32 s96, s96, 4\n\t" \
    "s_cmp_gt_u32 s96, 34\n\t" \
    "s_cselect_b64 vcc, -1, 0\n\t" \
    "v_cndmask_b32 v25, v12, v13, vcc\n\t" \
    "global_load_dword v24, v25, s[36:37] sc1\n\t" \
    "s_waitcnt vmcnt(30)\n\t" \
    "v_mfma_f32_16x16x32_bf16 v[72:75], a[24:27], v[40:43], v[72:75]\n\t" \
    "global_load_dwordx4 v[32:35], v7, s[40:41] offset:-2048 sc1\n\t" \
    "v_mfma_f32_16x16x32_bf16 v[76:79], a[24:27], v[44:47], v[76:79]\n\t" \
    "global_load_dwordx4 v[36:39], v7, s[40:41] offset:-1024 sc1\n\t" \
    "s_waitcnt vmcnt(31)\n\t" \
    "v_mfma_f32_16x16x32_bf16 v[80:83], a[28:31], v[40:43], v[80:83]\n\t" \
    "global_load_dwordx4 a[0:3], v18, s[38:39] offset:1024 sc1\n\t" \
    "v_mfma_f32_16x16x32_bf16 v[84:87], a[28:31], v[44:47], v[84:87]\n\t" \
    "global_load_dwordx4 a[4:7], v19, s[38:39] offset:1024 sc1\n\t" \
    "s_waitcnt vmcnt(32)\n\t" \
    "v_mfma_f32_16x16x32_bf16 v[88:91], a[32:35], v[40:43], v[88:91]\n\t" \
    "global_load_dwordx4 a[8:11], v20, s[38:39] offset:1024 sc1\n\t" \
    "v_mfma_f32_16x16x32_bf16 v[92:95], a[32:35], v[44:47], v[92:95]\n\t" \
    "global_load_dwordx4 a[12:15], v21, s[38:39] offset:1024 sc1\n\t" \
    "s_waitcnt vmcnt(33)\n\t" \
    "v_mfma_f32_16x16x32_bf16 v[96:99], a[36:39], v[40:43], v[96:99]\n\t" \
    "global_load_dwordx4 a[16:19], v22, s[38:39] offset:1024 sc1\n\t" \
    "v_mfma_f32_16x16x32_bf16 v[100:103], a[36:39], v[44:47], v[100:103]\n\t" \
    "global_load_dwordx4 a[20:23], v23, s[38:39] offset:1024 sc1\n\t" \
    "s_waitcnt vmcnt(34)\n\t" \
    "v_mfma_f32_16x16x32_bf16 v[104:107], a[40:43], v[40:43], v[104:107]\n\t" \
    "v_mfma_f32_16x16x32_bf16 v[108:111], a[40:43], v[44:47], v[108:111]\n\t" \
    "s_waitcnt vmcnt(33)\n\t" \
    "v_mfma_f32_16x16x32_bf16 v[112:115], a[44:47], v[40:43], v[112:115]\n\t" \
    "v_mfma_f32_16x16x32_bf16 v[116:119], a[44:47], v[44:47], v[116:119]\n\t" \
    "s_waitcnt vmcnt(30)\n\t" \
    "v_mfma_f32_16x16x32_bf16 v[72:75], a[48:51], v[48:51], v[72:75]\n\t" \
    "global_load_dwordx4 v[40:43], v7, s[40:41] offset:0 sc1\n\t" \
    "v_mfma_f32_16x16x32_bf16 v[76:79], a[48:51], v[52:55], v[76:79]\n\t" \
    "global_load_dwordx4 v[44:47], v7, s[40:41] offset:1024 sc1\n\t" \
    "s_waitcnt vmcnt(31)\n\t" \
    "v_mfma_f32_16x16x32_bf16 v[80:83], a[52:55], v[48:51], v[80:83]\n\t" \
    "global_load_dwordx4 a[24:27], v18, s[38:39] offset:2048 sc1\n\t" \
    "v_mfma_f32_16x16x32_bf16 v[84:87], a[52:55], v[52:55], v[84:87]\n\t" \
    "global_load_dwordx4 a[28:31], v19, s[38:39] offset:2048 sc1\n\t" \
    "s_waitcnt vmcnt(32)\n\t" \
    "v_mfma_f32_16x16x32_bf16 v[88:91], a[56:59], v[48:51], v[88:91]\n\t" \
    "global_load_dwordx4 a[32:35], v20, s[38:39] offset:2048 sc1\n\t" \
    "v_mfma_f32_16x16x32_bf16 v[92:95], a[56:59], v[52:55], v[92:95]\n\t" \
    "global_load_dwordx4 a[36:39], v21, s[38:39] offset:2048 sc1\n\t" \
    "s_waitcnt vmcnt(33)\n\t" \
    "v_mfma_f32_16x16x32_bf16 v[96:99], a[60:63], v[48:51], v[96:99]\n\t" \
    "global_load_dwordx4 a[40:43], v22, s[38:39] offset:2048 sc1\n\t" \
    "v_mfma_f32_16x16x32_bf16 v[100:103], a[60:63], v[52:55], v[100:103]\n\t" \
    "global_load_dwordx4 a[44:47], v23, s[38:39] offset:2048 sc1\n\t" \
    "s_waitcnt vmcnt(34)\n\t" \
    "v_mfma_f32_16x16x32_bf16 v[104:107], a[64:67], v[48:51], v[104:107]\n\t" \
    "v_mfma_f32_16x16x32_bf16 v[108:111], a[64:67], v[52:55], v[108:111]\n\t" \
    "s_waitcnt vmcnt(33)\n\t" \
    "v_mfma_f32_16x16x32_bf16 v[112:115], a[68:71], v[48:51], v[112:115]\n\t" \
    "v_mfma_f32_16x16x32_bf16 v[116:119], a[68:71], v[52:55], v[116:119]\n\t" \
    "s_waitcnt vmcnt(30)\n\t" \
    "v_mfma_f32_16x16x32_bf16 v[72:75], a[72:75], v[56:59], v[72:75]\n\t" \
    "global_load_dwordx4 v[48:51], v7, s[40:41] offset:2048 sc1\n\t" \
    "v_mfma_f32_16x16x32_bf16 v[76:79], a[72:75], v[60:63], v[76:79]\n\t" \
    "global_load_dwordx4 v[52:55], v7, s[40:41] offset:3072 sc1\n\t" \
    "s_waitcnt vmcnt(31)\n\t" \
    "v_mfma_f32_16x16x32_bf16 v[80:83], a[76:79], v[56:59], v[80:83]\n\t" \
    "global_load_dwordx4 a[48:51], v18, s[38:39] offset:3072 sc1\n\t" \
    "v_mfma_f32_16x16x32_bf16 v[84:87], a[76:79], v[60:63], v[84:87]\n\t" \
    "global_load_dwordx4 a[52:55], v19, s[38:39] offset:3072 sc1\n\t" \
    "s_waitcnt vmcnt(32)\n\t" \
    "v_mfma_f32_16x16x32_bf16 v[88:91], a[80:83], v[56:59], v[88:91]\n\t" \
    "global_load_dwordx4 a[56:59], v20, s[38:39] offset:3072 sc1\n\t" \
    "v_mfma_f32_16x16x32_bf16 v[92:95], a[80:83], v[60:63], v[92:95]\n\t" \
    "global_load_dwordx4 a[60:63], v21, s[38:39] offset:3072 sc1\n\t" \
    "s_waitcnt vmcnt(33)\n\t" \
    "v_mfma_f32_16x16x32_bf16 v[96:99], a[84:87], v[56:59], v[96:99]\n\t" \
    "global_load_dwordx4 a[64:67], v22, s[38:39] offset:3072 sc1\n\t" \
    "v_mfma_f32_16x16x32_bf16 v[100:103], a[84:87], v[60:63], v[100:103]\n\t" \
    "global_load_dwordx4 a[68:71], v23, s[38:39] offset:3072 sc1\n\t" \
    "s_waitcnt vmcnt(34)\n\t" \
    "v_mfma_f32_16x16x32_bf16 v[104:107], a[88:91], v[56:59], v[104:107]\n\t" \
    "v_mfma_f32_16x16x32_bf16 v[108:111], a[88:91], v[60:63], v[108:111]\n\t" \
    "s_waitcnt vmcnt(33)\n\t" \
    "v_mfma_f32_16x16x32_bf16 v[112:115], a[92:95], v[56:59], v[112:115]\n\t" \
    "v_mfma_f32_16x16x32_bf16 v[116:119], a[92:95], v[60:63], v[116:119]\n\t" \
    "s_waitcnt vmcnt(30)\n\t" \
    "v_mfma_f32_16x16x32_bf16 v[72:75], a[96:99], v[64:67], v[72:75]\n\t" \
    "global_load_dwordx4 v[56:59], v8, s[46:47] offset:0 sc1\n\t" \
    "v_mfma_f32_16x16x32_bf16 v[76:79], a[96:99], v[68:71], v[76:79]\n\t" \
    "global_load_dwordx4 v[60:63], v9, s[46:47] offset:0 sc1\n\t" \
    "s_waitcnt vmcnt(31)\n\t" \
    "v_mfma_f32_16x16x32_bf16 v[80:83], a[100:103], v[64:67], v[80:83]\n\t" \
    "global_load_dwordx4 a[72:75], v10, s[48:49] offset:0 sc1\n\t" \
    "v_mfma_f32_16x16x32_bf16 v[84:87], a[100:103], v[68:71], v[84:87]\n\t" \
    "global_load_dwordx4 a[76:79], v10, s[48:49] offset:256 sc1\n\t" \
    "s_waitcnt vmcnt(32)\n\t" \
    "v_mfma_f32_16x16x32_bf16 v[88:91], a[104:107], v[64:67], v[88:91]\n\t" \
    "global_load_dwordx4 a[80:83], v10, s[48:49] offset:512 sc1\n\t" \
    "v_mfma_f32_16x16x32_bf16 v[92:95], a[104:107], v[68:71], v[92:95]\n\t" \
    "global_load_dwordx4 a[84:87], v10, s[48:49] offset:768 sc1\n\t" \
    "s_waitcnt vmcnt(33)\n\t" \
    "v_mfma_f32_16x16x32_bf16 v[96:99], a[108:111], v[64:67], v[96:99]\n\t" \
    "global_load_dwordx4 a[88:91], v10, s[48:49] offset:1024 sc1\n\t" \
    "v_mfma_f32_16x16x32_bf16 v[100:103], a[108:111], v[68:71], v[100:103]\n\t" \
    "global_load_dwordx4 a[92:95], v10, s[48:49] offset:1280 sc1\n\t" \
    "s_waitcnt vmcnt(34)\n\t" \
    "v_mfma_f32_16x16x32_bf16 v[104:107], a[112:115], v[64:67], v[104:107]\n\t" \
    "v_mfma_f32_16x16x32_bf16 v[108:111], a[112:115], v[68:71], v[108:111]\n\t" \
    "s_waitcnt vmcnt(33)\n\t" \
    "v_mfma_f32_16x16x32_bf16 v[112:115], a[116:119], v[64:67], v[112:115]\n\t" \
    "v_mfma_f32_16x16x32_bf16 v[116:119], a[116:119], v[68:71], v[116:119]\n\t" \
    "s_waitcnt vmcnt(29)\n\t" \
    "v_mfma_f32_16x16x32_bf16 v[72:75], a[0:3], v[32:35], v[72:75]\n\t" \
    "global_load_dwordx4 v[64:67], v8, s[46:47] offset:1024 sc1\n\t" \
    "v_mfma_f32_16x16x32_bf16 v[76:79], a[0:3], v[36:39], v[76:79]\n\t" \
    "global_load_dwordx4 v[68:71], v9, s[46:47] offset:1024 sc1\n\t" \
    "s_waitcnt vmcnt(30)\n\t" \
    "v_mfma_f32_16x16x32_bf16 v[80:83], a[4:7], v[32:35], v[80:83]\n\t" \
    "global_load_dwordx4 a[96:99], v11, s[48:49] offset:0 sc1\n\t" \
    "v_mfma_f32_16x16x32_bf16 v[84:87], a[4:7], v[36:39], v[84:87]\n\t" \
    "global_load_dwordx4 a[100:103], v11, s[48:49] offset:256 sc1\n\t" \
    "s_waitcnt vmcnt(31)\n\t" \
    "v_mfma_f32_16x16x32_bf16 v[88:91], a[8:11], v[32:35], v[88:91]\n\t" \
    "global_load_dwordx4 a[104:107], v11, s[48:49] offset:512 sc1\n\t" \
    "v_mfma_f32_16x16x32_bf16 v[92:95], a[8:11], v[36:39], v[92:95]\n\t" \
    "global_load_dwordx4 a[108:111], v11, s[48:49] offset:768 sc1\n\t" \
    "s_waitcnt vmcnt(32)\n\t" \
    "v_mfma_f32_16x16x32_bf16 v[96:99], a[12:15], v[32:35], v[96:99]\n\t" \
    "global_load_dwordx4 a[112:115], v11, s[48:49] offset:1024 sc1\n\t" \
    "v_mfma_f32_16x16x32_bf16 v[100:103], a[12:15], v[36:39], v[100:103]\n\t" \
    "global_load_dwordx4 a[116:119], v11, s[48:49] offset:1280 sc1\n\t" \
    "s_waitcnt vmcnt(33)\n\t" \
    "v_mfma_f32_16x16x32_bf16 v[104:107], a[16:19], v[32:35], v[104:107]\n\t" \
    "v_mfma_f32_16x16x32_bf16 v[108:111], a[16:19], v[36:39], v[108:111]\n\t" \
    "s_waitcnt vmcnt(32)\n\t" \
    "v_mfma_f32_16x16x32_bf16 v[112:115], a[20:23], v[32:35], v[112:115]\n\t" \
    "v_mfma_f32_16x16x32_bf16 v[116:119], a[20:23], v[36:39], v[116:119]\n\t" \
    "v_cmp_gt_i32 vcc, s96, v24\n\t" \
    "s_cmp_ge_u32 s96, 58\n\t" \
    "s_cbranch_scc1 oo_stop4_%=\n\t" \
    "s_cbranch_vccnz oo_stop4_%=\n\t" \
    "s_mul_i32 s69, s96, 65536\n\t" \
    "s_add_u32 s69, s69, 146800640\n\t" \
    "s_add_u32 s40, s36, s69\n\t" \
    "s_addc_u32 s41, s37, 0\n\t" \
    "s_lshl_b32 s69, s96, 15\n\t" \
    "v_xor_b32 v18, s69, v0\n\t" \
    "v_xor_b32 v19, s69, v1\n\t" \
    "v_xor_b32 v20, s69, v2\n\t" \
    "v_xor_b32 v21, s69, v3\n\t" \
    "v_xor_b32 v22, s69, v4\n\t" \
    "v_xor_b32 v23, s69, v5\n\t" \
    "s_waitcnt vmcnt(29)\n\t" \
    "v_mfma_f32_16x16x32_bf16 v[72:75], a[24:27], v[40:43], v[72:75]\n\t" \
    "global_load_dwordx4 v[32:35], v6, s[40:41] offset:-4096 sc1\n\t" \
    "v_mfma_f32_16x16x32_bf16 v[76:79], a[24:27], v[44:47], v[76:79]\n\t" \
    "global_load_dwordx4 v[36:39], v6, s[40:41] offset:-3072 sc1\n\t" \
    "s_waitcnt vmcnt(30)\n\t" \
    "v_mfma_f32_16x16x32_bf16 v[80:83], a[28:31], v[40:43], v[80:83]\n\t" \
    "global_load_dwordx4 a[0:3], v18, s[38:39] offset:-4096 sc1\n\t" \
    "v_mfma_f32_16x16x32_bf16 v[84:87], a[28:31], v[44:47], v[84:87]\n\t" \
    "global_load_dwordx4 a[4:7], v19, s[38:39] offset:-4096 sc1\n\t" \
    "s_waitcnt vmcnt(31)\n\t" \
    "v_mfma_f32_16x16x32_bf16 v[88:91], a[32:35], v[40:43], v[88:91]\n\t" \
    "global_load_dwordx4 a[8:11], v20, s[38:39] offset:-4096 sc1\n\t" \
    "v_mfma_f32_16x16x32_bf16 v[92:95], a[32:35], v[44:47], v[92:95]\n\t" \
    "global_load_dwordx4 a[12:15], v21, s[38:39] offset:-4096 sc1\n\t" \
    "s_waitcnt vmcnt(32)\n\t" \
    "v_mfma_f32_16x16x32_bf16 v[96:99], a[36:39], v[40:43], v[96:99]\n\t" \
    "global_load_dwordx4 a[16:19], v22, s[38:39] offset:-4096 sc1\n\t" \
    "v_mfma_f32_16x16x32_bf16 v[100:103], a[36:39], v[44:47], v[100:103]\n\t" \
    "global_load_dwordx4 a[20:23], v23, s[38:39] offset:-4096 sc1\n\t" \
    "s_waitcnt vmcnt(33)\n\t" \
    "v_mfma_f32_16x16x32_bf16 v[104:107], a[40:43], v[40:43], v[104:107]\n\t" \
    "v_mfma_f32_16x16x32_bf16 v[108:111], a[40:43], v[44:47], v[108:111]\n\t" \
    "s_waitcnt vmcnt(32)\n\t" \
    "v_mfma_f32_16x16x32_bf16 v[112:115], a[44:47], v[40:43], v[112:115]\n\t" \
    "v_mfma_f32_16x16x32_bf16 v[116:119], a[44:47], v[44:47], v[116:119]\n\t" \
    "s_waitcnt vmcnt(29)\n\t" \
    "v_mfma_f32_16x16x32_bf16 v[72:75], a[48:51], v[48:51], v[72:75]\n\t" \
    "global_load_dwordx4 v[40:43], v6, s[40:41] offset:-2048 sc1\n\t" \
    "v_mfma_f32_16x16x32_bf16 v[76:79], a[48:51], v[52:55], v[76:79]\n\t" \
    "global_load_dwordx4 v[44:47], v6, s[40:41] offset:-1024 sc1\n\t" \
    "s_waitcnt vmcnt(30)\n\t" \
    "v_mfma_f32_16x16x32_bf16 v[80:83], a[52:55], v[48:51], v[80:83]\n\t" \
    "global_load_dwordx4 a[24:27], v18, s[38:39] offset:-3072 sc1\n\t" \
    "v_mfma_f32_16x16x32_bf16 v[84:87], a[52:55], v[52:55], v[84:87]\n\t" \
    "global_load_dwordx4 a[28:31], v19, s[38:39] offset:-3072 sc1\n\t" \
    "s_waitcnt vmcnt(31)\n\t" \
    "v_mfma_f32_16x16x32_bf16 v[88:91], a[56:59], v[48:51], v[88:91]\n\t" \
    "global_load_dwordx4 a[32:35], v20, s[38:39] offset:-3072 sc1\n\t" \
    "v_mfma_f32_16x16x32_bf16 v[92:95], a[56:59], v[52:55], v[92:95]\n\t" \
    "global_load_dwordx4 a[36:39], v21, s[38:39] offset:-3072 sc1\n\t" \
    "s_waitcnt vmcnt(32)\n\t" \
    "v_mfma_f32_16x16x32_bf16 v[96:99], a[60:63], v[48:51], v[96:99]\n\t" \
    "global_load_dwordx4 a[40:43], v22, s[38:39] offset:-3072 sc1\n\t" \
    "v_mfma_f32_16x16x32_bf16 v[100:103], a[60:63], v[52:55], v[100:103]\n\t" \
    "global_load_dwordx4 a[44:47], v23, s[38:39] offset:-3072 sc1\n\t" \
    "s_waitcnt vmcnt(33)\n\t" \
    "v_mfma_f32_16x16x32_bf16 v[104:107], a[64:67], v[48:51], v[104:107]\n\t" \
    "v_mfma_f32_16x16x32_bf16 v[108:111], a[64:67], v[52:55], v[108:111]\n\t" \
    "s_waitcnt vmcnt(32)\n\t" \
    "v_mfma_f32_16x16x32_bf16 v[112:115], a[68:71], v[48:51], v[112:115]\n\t" \
    "v_mfma_f32_16x16x32_bf16 v[116:119], a[68:71], v[52:55], v[116:119]\n\t" \
    "s_waitcnt vmcnt(29)\n\t" \
    "v_mfma_f32_16x16x32_bf16 v[72:75], a[72:75], v[56:59], v[72:75]\n\t" \
    "global_load_dwordx4 v[48:51], v6, s[40:41] offset:0 sc1\n\t" \
    "v_mfma_f32_16x16x32_bf16 v[76:79], a[72:75], v[60:63], v[76:79]\n\t" \
    "global_load_dwordx4 v[52:55], v6, s[40:41] offset:1024 sc1\n\t" \
    "s_waitcnt vmcnt(30)\n\t" \
    "v_mfma_f32_16x16x32_bf16 v[80:83], a[76:79], v[56:59], v[80:83]\n\t" \
    "global_load_dwordx4 a[48:51], v18, s[38:39] offset:-2048 sc1\n\t" \
    "v_mfma_f32_16x16x32_bf16 v[84:87], a[76:79], v[60:63], v[84:87]\n\t" \
    "global_load_dwordx4 a[52:55], v19, s[38:39] offset:-2048 sc1\n\t" \
    "s_waitcnt vmcnt(31)\n\t" \
    "v_mfma_f32_16x16x32_bf16 v[88:91], a[80:83], v[56:59], v[88:91]\n\t" \
    "global_load_dwordx4 a[56:59], v20, s[38:39] offset:-2048 sc1\n\t" \
    "v_mfma_f32_16x16x32_bf16 v[92:95], a[80:83], v[60:63], v[92:95]\n\t" \
    "global_load_dwordx4 a[60:63], v21, s[38:39] offset:-2048 sc1\n\t" \
    "s_waitcnt vmcnt(32)\n\t" \
    "v_mfma_f32_16x16x32_bf16 v[96:99], a[84:87], v[56:59], v[96:99]\n\t" \
    "global_load_dwordx4 a[64:67], v22, s[38:39] offset:-2048 sc1\n\t" \
    "v_mfma_f32_16x16x32_bf16 v[100:103], a[84:87], v[60:63], v[100:103]\n\t" \
    "global_load_dwordx4 a[68:71], v23, s[38:39] offset:-2048 sc1\n\t" \
    "s_waitcnt vmcnt(33)\n\t" \
    "v_mfma_f32_16x16x32_bf16 v[104:107], a[88:91], v[56:59], v[104:107]\n\t" \
    "v_mfma_f32_16x16x32_bf16 v[108:111], a[88:91], v[60:63], v[108:111]\n\t" \
    "s_waitcnt vmcnt(32)\n\t" \
    "v_mfma_f32_16x16x32_bf16 v[112:115], a[92:95], v[56:59], v[112:115]\n\t" \
    "v_mfma_f32_16x16x32_bf16 v[116:119], a[92:95], v[60:63], v[116:119]\n\t" \
    "s_waitcnt vmcnt(29)\n\t" \
    "v_mfma_f32_16x16x32_bf16 v[72:75], a[96:99], v[64:67], v[72:75]\n\t" \
    "global_load_dwordx4 v[56:59], v6, s[40:41] offset:2048 sc1\n\t" \
    "v_mfma_f32_16x16x32_bf16 v[76:79], a[96:99], v[68:71], v[76:79]\n\t" \
    "global_load_dwordx4 v[60:63], v6, s[40:41] offset:3072 sc1\n\t" \
    "s_waitcnt vmcnt(30)\n\t" \
    "v_mfma_f32_16x16x32_bf16 v[80:83], a[100:103], v[64:67], v[80:83]\n\t" \
    "global_load_dwordx4 a[72:75], v18, s[38:39] offset:-1024 sc1\n\t" \
    "v_mfma_f32_16x16x32_bf16 v[84:87], a[100:103], v[68:71], v[84:87]\n\t" \
    "global_load_dwordx4 a[76:79], v19, s[38:39] offset:-1024 sc1\n\t" \
    "s_waitcnt vmcnt(31)\n\t" \
    "v_mfma_f32_16x16x32_bf16 v[88:91], a[104:107], v[64:67], v[88:91]\n\t" \
    "global_load_dwordx4 a[80:83], v20, s[38:39] offset:-1024 sc1\n\t" \
    "v_mfma_f32_16x16x32_bf16 v[92:95], a[104:107], v[68:71], v[92:95]\n\t" \
    "global_load_dwordx4 a[84:87], v21, s[38:39] offset:-1024 sc1\n\t" \
    "s_waitcnt vmcnt(32)\n\t" \
    "v_mfma_f32_16x16x32_bf16 v[96:99], a[108:111], v[64:67], v[96:99]\n\t" \
    "global_load_dwordx4 a[88:91], v22, s[38:39] offset:-1024 sc1\n\t" \
    "v_mfma_f32_16x16x32_bf16 v[100:103], a[108:111], v[68:71], v[100:103]\n\t" \
    "global_load_dwordx4 a[92:95], v23, s[38:39] offset:-1024 sc1\n\t" \
    "s_waitcnt vmcnt(33)\n\t" \
    "v_mfma_f32_16x16x32_bf16 v[104:107], a[112:115], v[64:67], v[104:107]\n\t" \
    "v_mfma_f32_16x16x32_bf16 v[108:111], a[112:115], v[68:71], v[108:111]\n\t" \
    "s_waitcnt vmcnt(32)\n\t" \
    "v_mfma_f32_16x16x32_bf16 v[112:115], a[116:119], v[64:67], v[112:115]\n\t" \
    "v_mfma_f32_16x16x32_bf16 v[116:119], a[116:119], v[68:71], v[116:119]\n\t" \
    "s_mov_b32 s97, 1\n\t" \
    "s_branch oo_epi5_%=\n\t" \
    "oo_stop4_%=:\n\t" \
    "s_waitcnt vmcnt(29)\n\t" \
    "v_mfma_f32_16x16x32_bf16 v[72:75], a[24:27], v[40:43], v[72:75]\n\t" \
    "v_mfma_f32_16x16x32_bf16 v[76:79], a[24:27], v[44:47], v[76:79]\n\t" \
    "s_waitcnt vmcnt(28)\n\t" \
    "v_mfma_f32_16x16x32_bf16 v[80:83], a[28:31], v[40:43], v[80:83]\n\t" \
    "v_mfma_f32_16x16x32_bf16 v[84:87], a[28:31], v[44:47], v[84:87]\n\t" \
    "s_waitcnt vmcnt(27)\n\t" \
    "v_mfma_f32_16x16x32_bf16 v[88:91], a[32:35], v[40:43], v[88:91]\n\t" \
    "v_mfma_f32_16x16x32_bf16 v[92:95], a[32:35], v[44:47], v[92:95]\n\t" \
    "s_waitcnt vmcnt(26)\n\t" \
    "v_mfma_f32_16x16x32_bf16 v[96:99], a[36:39], v[40:43], v[96:99]\n\t" \
    "v_mfma_f32_16x16x32_bf16 v[100:103], a[36:39], v[44:47], v[100:103]\n\t" \
    "s_waitcnt vmcnt(25)\n\t" \
    "v_mfma_f32_16x16x32_bf16 v[104:107], a[40:43], v[40:43], v[104:107]\n\t" \
    "v_mfma_f32_16x16x32_bf16 v[108:111], a[40:43], v[44:47], v[108:111]\n\t" \
    "s_waitcnt vmcnt(24)\n\t" \
    "v_mfma_f32_16x16x32_bf16 v[112:115], a[44:47], v[40:43], v[112:115]\n\t" \
    "v_mfma_f32_16x16x32_bf16 v[116:119], a[44:47], v[44:47], v[116:119]\n\t" \
    "s_waitcnt vmcnt(21)\n\t" \
    "v_mfma_f32_16x16x32_bf16 v[72:75], a[48:51], v[48:51], v[72:75]\n\t" \
    "v_mfma_f32_16x16x32_bf16 v[76:79], a[48:51], v[52:55], v[76:79]\n\t" \
    "s_waitcnt vmcnt(20)\n\t" \
    "v_mfma_f32_16x16x32_bf16 v[80:83], a[52:55], v[48:51], v[80:83]\n\t" \
    "v_mfma_f32_16x16x32_bf16 v[84:87], a[52:55], v[52:55], v[84:87]\n\t" \
    "s_waitcnt vmcnt(19)\n\t" \
    "v_mfma_f32_16x16x32_bf16 v[88:91], a[56:59], v[48:51], v[88:91]\n\t" \
    "v_mfma_f32_16x16x32_bf16 v[92:95], a[56:59], v[52:55], v[92:95]\n\t" \
    "s_waitcnt vmcnt(18)\n\t" \
    "v_mfma_f32_16x16x32_bf16 v[96:99], a[60:63], v[48:51], v[96:99]\n\t" \
    "v_mfma_f32_16x16x32_bf16 v[100:103], a[60:63], v[52:55], v[100:103]\n\t" \
    "s_waitcnt vmcnt(17)\n\t" \
    "v_mfma_f32_16x16x32_bf16 v[104:107], a[64:67], v[48:51], v[104:107]\n\t" \
    "v_mfma_f32_16x16x32_bf16 v[108:111], a[64:67], v[52:55], v[108:111]\n\t" \
    "s_waitcnt vmcnt(16)\n\t" \
    "v_mfma_f32_16x16x32_bf16 v[112:115], a[68:71], v[48:51], v[112:115]\n\t" \
    "v_mfma_f32_16x16x32_bf16 v[116:119], a[68:71], v[52:55], v[116:119]\n\t" \
    "s_waitcnt vmcnt(13)\n\t" \
    "v_mfma_f32_16x16x32_bf16 v[72:75], a[72:75], v[56:59], v[72:75]\n\t" \
    "v_mfma_f32_16x16x32_bf16 v[76:79], a[72:75], v[60:63], v[76:79]\n\t" \
    "s_waitcnt vmcnt(12)\n\t" \
    "v_mfma_f32_16x16x32_bf16 v[80:83], a[76:79], v[56:59], v[80:83]\n\t" \
    "v_mfma_f32_16x16x32_bf16 v[84:87], a[76:79], v[60:63], v[84:87]\n\t" \
    "s_waitcnt vmcnt(11)\n\t" \
    "v_mfma_f32_16x16x32_bf16 v[88:91], a[80:83], v[56:59], v[88:91]\n\t" \
    "v_mfma_f32_16x16x32_bf16 v[92:95], a[80:83], v[60:63], v[92:95]\n\t" \
    "s_waitcnt vmcnt(10)\n\t" \
    "v_mfma_f32_16x16x32_bf16 v[96:99], a[84:87], v[56:59], v[96:99]\n\t" \
    "v_mfma_f32_16x16x32_bf16 v[100:103], a[84:87], v[60:63], v[100:103]\n\t" \
    "s_waitcnt vmcnt(9)\n\t" \
    "v_mfma_f32_16x16x32_bf16 v[104:107], a[88:91], v[56:59], v[104:107]\n\t" \
    "v_mfma_f32_16x16x32_bf16 v[108:111], a[88:91], v[60:63], v[108:111]\n\t" \
    "s_waitcnt vmcnt(8)\n\t" \
    "v_mfma_f32_16x16x32_bf16 v[112:115], a[92:95], v[56:59], v[112:115]\n\t" \
    "v_mfma_f32_16x16x32_bf16 v[116:119], a[92:95], v[60:63], v[116:119]\n\t" \
    "s_waitcnt vmcnt(5)\n\t" \
    "v_mfma_f32_16x16x32_bf16 v[72:75], a[96:99], v[64:67], v[72:75]\n\t" \
    "v_mfma_f32_16x16x32_bf16 v[76:79], a[96:99], v[68:71], v[76:79]\n\t" \
    "s_waitcnt vmcnt(4)\n\t" \
    "v_mfma_f32_16x16x32_bf16 v[80:83], a[100:103], v[64:67], v[80:83]\n\t" \
    "v_mfma_f32_16x16x32_bf16 v[84:87], a[100:103], v[68:71], v[84:87]\n\t" \
    "s_waitcnt vmcnt(3)\n\t" \
    "v_mfma_f32_16x16x32_bf16 v[88:91], a[104:107], v[64:67], v[88:91]\n\t" \
    "v_mfma_f32_16x16x32_bf16 v[92:95], a[104:107], v[68:71], v[92:95]\n\t" \
    "s_waitcnt vmcnt(2)\n\t" \
    "v_mfma_f32_16x16x32_bf16 v[96:99], a[108:111], v[64:67], v[96:99]\n\t" \
    "v_mfma_f32_16x16x32_bf16 v[100:103], a[108:111], v[68:71], v[100:103]\n\t" \
    "s_waitcnt vmcnt(1)\n\t" \
    "v_mfma_f32_16x16x32_bf16 v[104:107], a[112:115], v[64:67], v[104:107]\n\t" \
    "v_mfma_f32_16x16x32_bf16 v[108:111], a[112:115], v[68:71], v[108:111]\n\t" \
    "s_waitcnt vmcnt(0)\n\t" \
    "v_mfma_f32_16x16x32_bf16 v[112:115], a[116:119], v[64:67], v[112:115]\n\t" \
    "v_mfma_f32_16x16x32_bf16 v[116:119], a[116:119], v[68:71], v[116:119]\n\t" \
    "s_mov_b32 s97, 0\n\t" \
    "oo_epi5_%=:\n\t" \
    "s_mul_i32 s69, s65, 73728\n\t" \
    "s_add_u32 s44, s42, s69\n\t" \
    "s_addc_u32 s45, s43, 0\n\t" \
    "s_nop 7\n\t" \
    "ds_write_b128 v14, v[72:75] offset:0\n\t" \
    "ds_write_b128 v14, v[76:79] offset:1024\n\t" \
    "ds_write_b128 v14, v[80:83] offset:2048\n\t" \
    "ds_write_b128 v14, v[84:87] offset:3072\n\t" \
    "ds_write_b128 v14, v[88:91] offset:4096\n\t" \
    "ds_write_b128 v14, v[92:95] offset:5120\n\t" \
    "ds_write_b128 v14, v[96:99] offset:6144\n\t" \
    "ds_write_b128 v14, v[100:103] offset:7168\n\t" \
    "ds_write_b128 v14, v[104:107] offset:8192\n\t" \
    "ds_write_b128 v14, v[108:111] offset:9216\n\t" \
    "ds_write_b128 v14, v[112:115] offset:10240\n\t" \
    "ds_write_b128 v14, v[116:119] offset:11264\n\t" \
    "s_waitcnt lgkmcnt(0)\n\t" \
    "s_barrier\n\t" \
    "ds_read_b128 v[120:123], v15 offset:0\n\t" \
    "ds_read_b128 v[124:127], v15 offset:12288\n\t" \
    "ds_read_b128 v[128:131], v15 offset:24576\n\t" \
    "ds_read_b128 v[132:135], v15 offset:36864\n\t" \
    "ds_read_b128 v[136:139], v15 offset:4096\n\t" \
    "ds_read_b128 v[140:143], v15 offset:16384\n\t" \
    "ds_read_b128 v[144:147], v15 offset:28672\n\t" \
    "ds_read_b128 v[148:151], v15 offset:40960\n\t" \
    "ds_read_b128 v[152:155], v15 offset:8192\n\t" \
    "ds_read_b128 v[156:159], v15 offset:20480\n\t" \
    "ds_read_b128 v[160:163], v15 offset:32768\n\t" \
    "ds_read_b128 v[164:167], v15 offset:45056\n\t" \
    "v_xor_b32 v14, 0x10000, v14\n\t" \
    "v_xor_b32 v15, 0x10000, v15\n\t" \
    "s_waitcnt lgkmcnt(10)\n\t" \
    "v_pk_add_f32 v[120:121], v[120:121], v[124:125]\n\t" \
    "v_pk_add_f32 v[122:123], v[122:123], v[126:127]\n\t" \
    "s_waitcnt lgkmcnt(8)\n\t" \
    "v_pk_add_f32 v[128:129], v[128:129], v[132:133]\n\t" \
    "v_pk_add_f32 v[130:131], v[130:131], v[134:135]\n\t" \
    "v_pk_add_f32 v[120:121], v[120:121], v[128:129]\n\t" \
    "v_pk_add_f32 v[122:123], v[122:123], v[130:131]\n\t" \
    "v_cvt_pk_bf16_f32 v120, v120, v121\n\t" \
    "v_cvt_pk_bf16_f32 v121, v122, v123\n\t" \
    "global_store_dwordx2 v16, v[120:121], s[44:45] offset:0\n\t" \
    "s_waitcnt lgkmcnt(6)\n\t" \
    "v_pk_add_f32 v[136:137], v[136:137], v[140:141]\n\t" \
    "v_pk_add_f32 v[138:139], v[138:139], v[142:143]\n\t" \
    "s_waitcnt lgkmcnt(4)\n\t" \
    "v_pk_add_f32 v[144:145], v[144:145], v[148:149]\n\t" \
    "v_pk_add_f32 v[146:147], v[146:147], v[150:151]\n\t" \
    "v_pk_add_f32 v[136:137], v[136:137], v[144:145]\n\t" \
    "v_pk_add_f32 v[138:139], v[138:139], v[146:147]\n\t" \
    "v_cvt_pk_bf16_f32 v136, v136, v137\n\t" \
    "v_cvt_pk_bf16_f32 v137, v138, v139\n\t" \
    "global_store_dwordx2 v16, v[136:137], s[44:45] offset:64\n\t" \
    "s_waitcnt lgkmcnt(2)\n\t" \
    "v_pk_add_f32 v[152:153], v[152:153], v[156:157]\n\t" \
    "v_pk_add_f32 v[154:155], v[154:155], v[158:159]\n\t" \
    "s_waitcnt lgkmcnt(0)\n\t" \
    "v_pk_add_f32 v[160:161], v[160:161], v[164:165]\n\t" \
    "v_pk_add_f32 v[162:163], v[162:163], v[166:167]\n\t" \
    "v_pk_add_f32 v[152:153], v[152:153], v[160:161]\n\t" \
    "v_pk_add_f32 v[154:155], v[154:155], v[162:163]\n\t" \
    "v_cvt_pk_bf16_f32 v152, v152, v153\n\t" \
    "v_cvt_pk_bf16_f32 v153, v154, v155\n\t" \
    "global_store_dwordx2 v16, v[152:153], s[44:45] offset:128\n\t" \
    "s_mov_b32 s65, s96\n\t" \
    "s_cmp_lg_u32 s97, 0\n\t" \
    "s_cbranch_scc1 oo_loop3_%=\n\t" \
    "s_cmp_lt_u32 s65, 58\n\t" \
    "s_cbranch_scc1 oo_poll1_%=\n\t" \
    "s_waitcnt vmcnt(0) lgkmcnt(0)\n\t" \
    "s_barrier\n\t" \
    "oo_last_%=:\n\t" \
    ""
#define ASM_CV \
    "v_mov_b32 v0, %0\n\t" \
    "v_mov_b32 v2, %2\n\t" \
    "v_subrev_u32 v1, 768, %1\n\t" \
    "v_mov_b32 v4, 0\n\t" \
    "v_mov_b32 v5, 0\n\t" \
    "v_mov_b32 v6, 0\n\t" \
    "v_mov_b32 v7, 0\n\t" \
    "v_mov_b32 v8, 0\n\t" \
    "v_mov_b32 v9, 0\n\t" \
    "v_mov_b32 v10, 0\n\t" \
    "v_mov_b32 v11, 0\n\t" \
    "v_mov_b32 v12, 0\n\t" \
    "v_mov_b32 v13, 0\n\t" \
    "v_mov_b32 v14, 0\n\t" \
    "v_mov_b32 v15, 0\n\t" \
    "v_mov_b32 v16, 0\n\t" \
    "v_mov_b32 v17, 0\n\t" \
    "v_mov_b32 v18, 0\n\t" \
    "v_mov_b32 v19, 0\n\t" \
    "v_mov_b32 v20, 0\n\t" \
    "v_mov_b32 v21, 0\n\t" \
    "v_mov_b32 v22, 0\n\t" \
    "v_mov_b32 v23, 0\n\t" \
    "v_mov_b32 v24, 0\n\t" \
    "v_mov_b32 v25, 0\n\t" \
    "v_mov_b32 v26, 0\n\t" \
    "v_mov_b32 v27, 0\n\t" \
    "v_mov_b32 v28, 0\n\t" \
    "v_mov_b32 v29, 0\n\t" \
    "v_mov_b32 v30, 0\n\t" \
    "v_mov_b32 v31, 0\n\t" \
    "v_mov_b32 v32, 0\n\t" \
    "v_mov_b32 v33, 0\n\t" \
    "v_mov_b32 v34, 0\n\t" \
    "v_mov_b32 v35, 0\n\t" \
    "s_waitcnt lgkmcnt(0)\n\t" \
    "ds_read_b64 v[36:37], v0 offset:0\n\t" \
    "ds_read_b64 v[38:39], v0 offset:8\n\t" \
    "ds_read_b128 v[44:47], v1 offset:768\n\t" \
    "ds_read_b64 v[40:41], v0 offset:32\n\t" \
    "ds_read_b64 v[42:43], v0 offset:40\n\t" \
    "ds_read_b128 v[48:51], v1 offset:2304\n\t" \
    "ds_read_b128 v[52:55], v1 offset:3840\n\t" \
    "ds_read_b128 v[56:59], v1 offset:5376\n\t" \
    "s_mov_b32 s69, 4\n\t" \
    "cv_c1_%=:\n\t" \
    "s_waitcnt lgkmcnt(5)\n\t" \
    "v_mfma_f32_16x16x32_f16 v[4:7], v[36:39], v[44:47], v[4:7]\n\t" \
    "ds_read_b64 v[60:61], v0 offset:64\n\t" \
    "s_waitcnt lgkmcnt(4)\n\t" \
    "v_mfma_f32_16x16x32_f16 v[20:23], v[40:43], v[44:47], v[20:23]\n\t" \
    "ds_read_b64 v[62:63], v0 offset:72\n\t" \
    "s_waitcnt lgkmcnt(4)\n\t" \
    "v_mfma_f32_16x16x32_f16 v[8:11], v[36:39], v[48:51], v[8:11]\n\t" \
    "ds_read_b128 v[68:71], v1 offset:672\n\t" \
    "v_mfma_f32_16x16x32_f16 v[24:27], v[40:43], v[48:51], v[24:27]\n\t" \
    "ds_read_b64 v[64:65], v0 offset:96\n\t" \
    "s_waitcnt lgkmcnt(5)\n\t" \
    "v_mfma_f32_16x16x32_f16 v[12:15], v[36:39], v[52:55], v[12:15]\n\t" \
    "ds_read_b64 v[66:67], v0 offset:104\n\t" \
    "v_mfma_f32_16x16x32_f16 v[28:31], v[40:43], v[52:55], v[28:31]\n\t" \
    "ds_read_b128 v[72:75], v1 offset:2208\n\t" \
    "s_waitcnt lgkmcnt(6)\n\t" \
    "v_mfma_f32_16x16x32_f16 v[16:19], v[36:39], v[56:59], v[16:19]\n\t" \
    "ds_read_b128 v[76:79], v1 offset:3744\n\t" \
    "v_mfma_f32_16x16x32_f16 v[32:35], v[40:43], v[56:59], v[32:35]\n\t" \
    "ds_read_b128 v[80:83], v1 offset:5280\n\t" \
    "s_waitcnt lgkmcnt(5)\n\t" \
    "v_mfma_f32_16x16x32_f16 v[4:7], v[60:63], v[68:71], v[4:7]\n\t" \
    "ds_read_b64 v[84:85], v0 offset:128\n\t" \
    "s_waitcnt lgkmcnt(4)\n\t" \
    "v_mfma_f32_16x16x32_f16 v[20:23], v[64:67], v[68:71], v[20:23]\n\t" \
    "ds_read_b64 v[86:87], v0 offset:136\n\t" \
    "s_waitcnt lgkmcnt(4)\n\t" \
    "v_mfma_f32_16x16x32_f16 v[8:11], v[60:63], v[72:75], v[8:11]\n\t" \
    "ds_read_b128 v[92:95], v1 offset:576\n\t" \
    "v_mfma_f32_16x16x32_f16 v[24:27], v[64:67], v[72:75], v[24:27]\n\t" \
    "ds_read_b64 v[88:89], v0 offset:160\n\t" \
    "s_waitcnt lgkmcnt(5)\n\t" \
    "v_mfma_f32_16x16x32_f16 v[12:15], v[60:63], v[76:79], v[12:15]\n\t" \
    "ds_read_b64 v[90:91], v0 offset:168\n\t" \
    "v_mfma_f32_16x16x32_f16 v[28:31], v[64:67], v[76:79], v[28:31]\n\t" \
    "ds_read_b128 v[96:99], v1 offset:2112\n\t" \
    "s_waitcnt lgkmcnt(6)\n\t" \
    "v_mfma_f32_16x16x32_f16 v[16:19], v[60:63], v[80:83], v[16:19]\n\t" \
    "ds_read_b128 v[100:103], v1 offset:3648\n\t" \
    "v_mfma_f32_16x16x32_f16 v[32:35], v[64:67], v[80:83], v[32:35]\n\t" \
    "ds_read_b128 v[104:107], v1 offset:5184\n\t" \
    "s_waitcnt lgkmcnt(5)\n\t" \
    "v_mfma_f32_16x16x32_f16 v[4:7], v[84:87], v[92:95], v[4:7]\n\t" \
    "ds_read_b64 v[108:109], v0 offset:192\n\t" \
    "s_waitcnt lgkmcnt(4)\n\t" \
    "v_mfma_f32_16x16x32_f16 v[20:23], v[88:91], v[92:95], v[20:23]\n\t" \
    "ds_read_b64 v[110:111], v0 offset:200\n\t" \
    "s_waitcnt lgkmcnt(4)\n\t" \
    "v_mfma_f32_16x16x32_f16 v[8:11], v[84:87], v[96:99], v[8:11]\n\t" \
    "ds_read_b128 v[116:119], v1 offset:480\n\t" \
    "v_mfma_f32_16x16x32_f16 v[24:27], v[88:91], v[96:99], v[24:27]\n\t" \
    "ds_read_b64 v[112:113], v0 offset:224\n\t" \
    "s_waitcnt lgkmcnt(5)\n\t" \
    "v_mfma_f32_16x16x32_f16 v[12:15], v[84:87], v[100:103], v[12:15]\n\t" \
    "ds_read_b64 v[114:115], v0 offset:232\n\t" \
    "v_mfma_f32_16x16x32_f16 v[28:31], v[88:91], v[100:103], v[28:31]\n\t" \
    "ds_read_b128 v[120:123], v1 offset:2016\n\t" \
    "s_waitcnt lgkmcnt(6)\n\t" \
    "v_mfma_f32_16x16x32_f16 v[16:19], v[84:87], v[104:107], v[16:19]\n\t" \
    "ds_read_b128 v[124:127], v1 offset:3552\n\t" \
    "v_mfma_f32_16x16x32_f16 v[32:35], v[88:91], v[104:107], v[32:35]\n\t" \
    "ds_read_b128 v[128:131], v1 offset:5088\n\t" \
    "s_waitcnt lgkmcnt(5)\n\t" \
    "v_mfma_f32_16x16x32_f16 v[4:7], v[108:111], v[116:119], v[4:7]\n\t" \
    "ds_read_b64 v[36:37], v0 offset:256\n\t" \
    "s_waitcnt lgkmcnt(4)\n\t" \
    "v_mfma_f32_16x16x32_f16 v[20:23], v[112:115], v[116:119], v[20:23]\n\t" \
    "ds_read_b64 v[38:39], v0 offset:264\n\t" \
    "s_waitcnt lgkmcnt(4)\n\t" \
    "v_mfma_f32_16x16x32_f16 v[8:11], v[108:111], v[120:123], v[8:11]\n\t" \
    "ds_read_b128 v[44:47], v1 offset:384\n\t" \
    "v_mfma_f32_16x16x32_f16 v[24:27], v[112:115], v[120:123], v[24:27]\n\t" \
    "ds_read_b64 v[40:41], v0 offset:288\n\t" \
    "s_waitcnt lgkmcnt(5)\n\t" \
    "v_mfma_f32_16x16x32_f16 v[12:15], v[108:111], v[124:127], v[12:15]\n\t" \
    "ds_read_b64 v[42:43], v0 offset:296\n\t" \
    "v_mfma_f32_16x16x32_f16 v[28:31], v[112:115], v[124:127], v[28:31]\n\t" \
    "ds_read_b128 v[48:51], v1 offset:1920\n\t" \
    "s_waitcnt lgkmcnt(6)\n\t" \
    "v_mfma_f32_16x16x32_f16 v[16:19], v[108:111], v[128:131], v[16:19]\n\t" \
    "ds_read_b128 v[52:55], v1 offset:3456\n\t" \
    "v_mfma_f32_16x16x32_f16 v[32:35], v[112:115], v[128:131], v[32:35]\n\t" \
    "ds_read_b128 v[56:59], v1 offset:4992\n\t" \
    "v_add_u32 v0, 256, v0\n\t" \
    "v_subrev_u32 v1, 384, v1\n\t" \
    "s_sub_u32 s69, s69, 1\n\t" \
    "s_cmp_lg_u32 s69, 0\n\t" \
    "s_cbranch_scc1 cv_c1_%=\n\t" \
    "s_waitcnt lgkmcnt(0)\n\t" \
    "ds_read_b64 v[36:37], v0 offset:0\n\t" \
    "ds_read_b64 v[38:39], v0 offset:8\n\t" \
    "ds_read_b128 v[48:51], v1 offset:2304\n\t" \
    "ds_read_b64 v[40:41], v0 offset:32\n\t" \
    "ds_read_b64 v[42:43], v0 offset:40\n\t" \
    "ds_read_b128 v[52:55], v1 offset:3840\n\t" \
    "ds_read_b128 v[56:59], v1 offset:5376\n\t" \
    "ds_read_b64 v[60:61], v0 offset:64\n\t" \
    "ds_read_b64 v[62:63], v0 offset:72\n\t" \
    "ds_read_b128 v[72:75], v1 offset:2208\n\t" \
    "ds_read_b64 v[64:65], v0 offset:96\n\t" \
    "ds_read_b64 v[66:67], v0 offset:104\n\t" \
    "ds_read_b128 v[76:79], v1 offset:3744\n\t" \
    "ds_read_b128 v[80:83], v1 offset:5280\n\t" \
    "s_mov_b32 s69, 4\n\t" \
    "cv_c2_%=:\n\t" \
    "s_waitcnt lgkmcnt(11)\n\t" \
    "v_mfma_f32_16x16x32_f16 v[8:11], v[36:39], v[48:51], v[8:11]\n\t" \
    "ds_read_b64 v[84:85], v0 offset:128\n\t" \
    "ds_read_b64 v[86:87], v0 offset:136\n\t" \
    "s_waitcnt lgkmcnt(11)\n\t" \
    "v_mfma_f32_16x16x32_f16 v[24:27], v[40:43], v[48:51], v[24:27]\n\t" \
    "ds_read_b128 v[96:99], v1 offset:2112\n\t" \
    "ds_read_b64 v[88:89], v0 offset:160\n\t" \
    "s_waitcnt lgkmcnt(12)\n\t" \
    "v_mfma_f32_16x16x32_f16 v[12:15], v[36:39], v[52:55], v[12:15]\n\t" \
    "ds_read_b64 v[90:91], v0 offset:168\n\t" \
    "ds_read_b128 v[100:103], v1 offset:3648\n\t" \
    "v_mfma_f32_16x16x32_f16 v[28:31], v[40:43], v[52:55], v[28:31]\n\t" \
    "ds_read_b128 v[104:107], v1 offset:5184\n\t" \
    "s_waitcnt lgkmcnt(14)\n\t" \
    "v_mfma_f32_16x16x32_f16 v[16:19], v[36:39], v[56:59], v[16:19]\n\t" \
    "v_mfma_f32_16x16x32_f16 v[32:35], v[40:43], v[56:59], v[32:35]\n\t" \
    "s_waitcnt lgkmcnt(11)\n\t" \
    "v_mfma_f32_16x16x32_f16 v[8:11], v[60:63], v[72:75], v[8:11]\n\t" \
    "ds_read_b64 v[108:109], v0 offset:192\n\t" \
    "ds_read_b64 v[110:111], v0 offset:200\n\t" \
    "s_waitcnt lgkmcnt(11)\n\t" \
    "v_mfma_f32_16x16x32_f16 v[24:27], v[64:67], v[72:75], v[24:27]\n\t" \
    "ds_read_b128 v[120:123], v1 offset:2016\n\t" \
    "ds_read_b64 v[112:113], v0 offset:224\n\t" \
    "s_waitcnt lgkmcnt(12)\n\t" \
    "v_mfma_f32_16x16x32_f16 v[12:15], v[60:63], v[76:79], v[12:15]\n\t" \
    "ds_read_b64 v[114:115], v0 offset:232\n\t" \
    "ds_read_b128 v[124:127], v1 offset:3552\n\t" \
    "v_mfma_f32_16x16x32_f16 v[28:31], v[64:67], v[76:79], v[28:31]\n\t" \
    "ds_read_b128 v[128:131], v1 offset:5088\n\t" \
    "s_waitcnt lgkmcnt(14)\n\t" \
    "v_mfma_f32_16x16x32_f16 v[16:19], v[60:63], v[80:83], v[16:19]\n\t" \
    "v_mfma_f32_16x16x32_f16 v[32:35], v[64:67], v[80:83], v[32:35]\n\t" \
    "s_waitcnt lgkmcnt(11)\n\t" \
    "v_mfma_f32_16x16x32_f16 v[8:11], v[84:87], v[96:99], v[8:11]\n\t" \
    "ds_read_b64 v[36:37], v0 offset:256\n\t" \
    "ds_read_b64 v[38:39], v0 offset:264\n\t" \
    "s_waitcnt lgkmcnt(11)\n\t" \
    "v_mfma_f32_16x16x32_f16 v[24:27], v[88:91], v[96:99], v[24:27]\n\t" \
    "ds_read_b128 v[48:51], v1 offset:1920\n\t" \
    "ds_read_b64 v[40:41], v0 offset:288\n\t" \
    "s_waitcnt lgkmcnt(12)\n\t" \
    "v_mfma_f32_16x16x32_f16 v[12:15], v[84:87], v[100:103], v[12:15]\n\t" \
    "ds_read_b64 v[42:43], v0 offset:296\n\t" \
    "ds_read_b128 v[52:55], v1 offset:3456\n\t" \
    "v_mfma_f32_16x16x32_f16 v[28:31], v[88:91], v[100:103], v[28:31]\n\t" \
    "ds_read_b128 v[56:59], v1 offset:4992\n\t" \
    "s_waitcnt lgkmcnt(14)\n\t" \
    "v_mfma_f32_16x16x32_f16 v[16:19], v[84:87], v[104:107], v[16:19]\n\t" \
    "v_mfma_f32_16x16x32_f16 v[32:35], v[88:91], v[104:107], v[32:35]\n\t" \
    "s_waitcnt lgkmcnt(11)\n\t" \
    "v_mfma_f32_16x16x32_f16 v[8:11], v[108:111], v[120:123], v[8:11]\n\t" \
    "ds_read_b64 v[60:61], v0 offset:320\n\t" \
    "ds_read_b64 v[62:63], v0 offset:328\n\t" \
    "s_waitcnt lgkmcnt(11)\n\t" \
    "v_mfma_f32_16x16x32_f16 v[24:27], v[112:115], v[120:123], v[24:27]\n\t" \
    "ds_read_b128 v[72:75], v1 offset:1824\n\t" \
    "ds_read_b64 v[64:65], v0 offset:352\n\t" \
    "s_waitcnt lgkmcnt(12)\n\t" \
    "v_mfma_f32_16x16x32_f16 v[12:15], v[108:111], v[124:127], v[12:15]\n\t" \
    "ds_read_b64 v[66:67], v0 offset:360\n\t" \
    "ds_read_b128 v[76:79], v1 offset:3360\n\t" \
    "v_mfma_f32_16x16x32_f16 v[28:31], v[112:115], v[124:127], v[28:31]\n\t" \
    "ds_read_b128 v[80:83], v1 offset:4896\n\t" \
    "s_waitcnt lgkmcnt(14)\n\t" \
    "v_mfma_f32_16x16x32_f16 v[16:19], v[108:111], v[128:131], v[16:19]\n\t" \
    "v_mfma_f32_16x16x32_f16 v[32:35], v[112:115], v[128:131], v[32:35]\n\t" \
    "v_add_u32 v0, 256, v0\n\t" \
    "v_subrev_u32 v1, 384, v1\n\t" \
    "s_sub_u32 s69, s69, 1\n\t" \
    "s_cmp_lg_u32 s69, 0\n\t" \
    "s_cbranch_scc1 cv_c2_%=\n\t" \
    "s_waitcnt lgkmcnt(0)\n\t" \
    "ds_read_b64 v[36:37], v0 offset:0\n\t" \
    "ds_read_b64 v[38:39], v0 offset:8\n\t" \
    "ds_read_b128 v[52:55], v1 offset:3840\n\t" \
    "ds_read_b64 v[40:41], v0 offset:32\n\t" \
    "ds_read_b64 v[42:43], v0 offset:40\n\t" \
    "ds_read_b128 v[56:59], v1 offset:5376\n\t" \
    "ds_read_b64 v[60:61], v0 offset:64\n\t" \
    "ds_read_b64 v[62:63], v0 offset:72\n\t" \
    "ds_read_b128 v[76:79], v1 offset:3744\n\t" \
    "ds_read_b64 v[64:65], v0 offset:96\n\t" \
    "ds_read_b64 v[66:67], v0 offset:104\n\t" \
    "ds_read_b128 v[80:83], v1 offset:5280\n\t" \
    "s_mov_b32 s69, 4\n\t" \
    "cv_c3_%=:\n\t" \
    "s_waitcnt lgkmcnt(9)\n\t" \
    "v_mfma_f32_16x16x32_f16 v[12:15], v[36:39], v[52:55], v[12:15]\n\t" \
    "ds_read_b64 v[84:85], v0 offset:128\n\t" \
    "ds_read_b64 v[86:87], v0 offset:136\n\t" \
    "s_waitcnt lgkmcnt(9)\n\t" \
    "v_mfma_f32_16x16x32_f16 v[28:31], v[40:43], v[52:55], v[28:31]\n\t" \
    "ds_read_b128 v[100:103], v1 offset:3648\n\t" \
    "ds_read_b64 v[88:89], v0 offset:160\n\t" \
    "s_waitcnt lgkmcnt(10)\n\t" \
    "v_mfma_f32_16x16x32_f16 v[16:19], v[36:39], v[56:59], v[16:19]\n\t" \
    "ds_read_b64 v[90:91], v0 offset:168\n\t" \
    "ds_read_b128 v[104:107], v1 offset:5184\n\t" \
    "v_mfma_f32_16x16x32_f16 v[32:35], v[40:43], v[56:59], v[32:35]\n\t" \
    "s_waitcnt lgkmcnt(9)\n\t" \
    "v_mfma_f32_16x16x32_f16 v[12:15], v[60:63], v[76:79], v[12:15]\n\t" \
    "ds_read_b64 v[108:109], v0 offset:192\n\t" \
    "ds_read_b64 v[110:111], v0 offset:200\n\t" \
    "s_waitcnt lgkmcnt(9)\n\t" \
    "v_mfma_f32_16x16x32_f16 v[28:31], v[64:67], v[76:79], v[28:31]\n\t" \
    "ds_read_b128 v[124:127], v1 offset:3552\n\t" \
    "ds_read_b64 v[112:113], v0 offset:224\n\t" \
    "s_waitcnt lgkmcnt(10)\n\t" \
    "v_mfma_f32_16x16x32_f16 v[16:19], v[60:63], v[80:83], v[16:19]\n\t" \
    "ds_read_b64 v[114:115], v0 offset:232\n\t" \
    "ds_read_b128 v[128:131], v1 offset:5088\n\t" \
    "v_mfma_f32_16x16x32_f16 v[32:35], v[64:67], v[80:83], v[32:35]\n\t" \
    "s_waitcnt lgkmcnt(9)\n\t" \
    "v_mfma_f32_16x16x32_f16 v[12:15], v[84:87], v[100:103], v[12:15]\n\t" \
    "ds_read_b64 v[36:37], v0 offset:256\n\t" \
    "ds_read_b64 v[38:39], v0 offset:264\n\t" \
    "s_waitcnt lgkmcnt(9)\n\t" \
    "v_mfma_f32_16x16x32_f16 v[28:31], v[88:91], v[100:103], v[28:31]\n\t" \
    "ds_read_b128 v[52:55], v1 offset:3456\n\t" \
    "ds_read_b64 v[40:41], v0 offset:288\n\t" \
    "s_waitcnt lgkmcnt(10)\n\t" \
    "v_mfma_f32_16x16x32_f16 v[16:19], v[84:87], v[104:107], v[16:19]\n\t" \
    "ds_read_b64 v[42:43], v0 offset:296\n\t" \
    "ds_read_b128 v[56:59], v1 offset:4992\n\t" \
    "v_mfma_f32_16x16x32_f16 v[32:35], v[88:91], v[104:107], v[32:35]\n\t" \
    "s_waitcnt lgkmcnt(9)\n\t" \
    "v_mfma_f32_16x16x32_f16 v[12:15], v[108:111], v[124:127], v[12:15]\n\t" \
    "ds_read_b64 v[60:61], v0 offset:320\n\t" \
    "ds_read_b64 v[62:63], v0 offset:328\n\t" \
    "s_waitcnt lgkmcnt(9)\n\t" \
    "v_mfma_f32_16x16x32_f16 v[28:31], v[112:115], v[124:127], v[28:31]\n\t" \
    "ds_read_b128 v[76:79], v1 offset:3360\n\t" \
    "ds_read_b64 v[64:65], v0 offset:352\n\t" \
    "s_waitcnt lgkmcnt(10)\n\t" \
    "v_mfma_f32_16x16x32_f16 v[16:19], v[108:111], v[128:131], v[16:19]\n\t" \
    "ds_read_b64 v[66:67], v0 offset:360\n\t" \
    "ds_read_b128 v[80:83], v1 offset:4896\n\t" \
    "v_mfma_f32_16x16x32_f16 v[32:35], v[112:115], v[128:131], v[32:35]\n\t" \
    "v_add_u32 v0, 256, v0\n\t" \
    "v_subrev_u32 v1, 384, v1\n\t" \
    "s_sub_u32 s69, s69, 1\n\t" \
    "s_cmp_lg_u32 s69, 0\n\t" \
    "s_cbranch_scc1 cv_c3_%=\n\t" \
    "s_waitcnt lgkmcnt(0)\n\t" \
    "ds_read_b64 v[36:37], v0 offset:0\n\t" \
    "ds_read_b64 v[38:39], v0 offset:8\n\t" \
    "ds_read_b128 v[56:59], v1 offset:5376\n\t" \
    "ds_read_b64 v[40:41], v0 offset:32\n\t" \
    "ds_read_b64 v[42:43], v0 offset:40\n\t" \
    "ds_read_b64 v[60:61], v0 offset:64\n\t" \
    "ds_read_b64 v[62:63], v0 offset:72\n\t" \
    "ds_read_b128 v[80:83], v1 offset:5280\n\t" \
    "ds_read_b64 v[64:65], v0 offset:96\n\t" \
    "ds_read_b64 v[66:67], v0 offset:104\n\t" \
    "ds_read_b64 v[84:85], v0 offset:128\n\t" \
    "ds_read_b64 v[86:87], v0 offset:136\n\t" \
    "ds_read_b128 v[104:107], v1 offset:5184\n\t" \
    "ds_read_b64 v[88:89], v0 offset:160\n\t" \
    "ds_read_b64 v[90:91], v0 offset:168\n\t" \
    "s_mov_b32 s69, 4\n\t" \
    "cv_c4_%=:\n\t" \
    "s_waitcnt lgkmcnt(12)\n\t" \
    "v_mfma_f32_16x16x32_f16 v[16:19], v[36:39], v[56:59], v[16:19]\n\t" \
    "ds_read_b64 v[108:109], v0 offset:192\n\t" \
    "ds_read_b64 v[110:111], v0 offset:200\n\t" \
    "ds_read_b128 v[128:131], v1 offset:5088\n\t" \
    "s_waitcnt lgkmcnt(13)\n\t" \
    "v_mfma_f32_16x16x32_f16 v[32:35], v[40:43], v[56:59], v[32:35]\n\t" \
    "ds_read_b64 v[112:113], v0 offset:224\n\t" \
    "ds_read_b64 v[114:115], v0 offset:232\n\t" \
    "s_waitcnt lgkmcnt(12)\n\t" \
    "v_mfma_f32_16x16x32_f16 v[16:19], v[60:63], v[80:83], v[16:19]\n\t" \
    "ds_read_b64 v[36:37], v0 offset:256\n\t" \
    "ds_read_b64 v[38:39], v0 offset:264\n\t" \
    "ds_read_b128 v[56:59], v1 offset:4992\n\t" \
    "s_waitcnt lgkmcnt(13)\n\t" \
    "v_mfma_f32_16x16x32_f16 v[32:35], v[64:67], v[80:83], v[32:35]\n\t" \
    "ds_read_b64 v[40:41], v0 offset:288\n\t" \
    "ds_read_b64 v[42:43], v0 offset:296\n\t" \
    "s_waitcnt lgkmcnt(12)\n\t" \
    "v_mfma_f32_16x16x32_f16 v[16:19], v[84:87], v[104:107], v[16:19]\n\t" \
    "ds_read_b64 v[60:61], v0 offset:320\n\t" \
    "ds_read_b64 v[62:63], v0 offset:328\n\t" \
    "ds_read_b128 v[80:83], v1 offset:4896\n\t" \
    "s_waitcnt lgkmcnt(13)\n\t" \
    "v_mfma_f32_16x16x32_f16 v[32:35], v[88:91], v[104:107], v[32:35]\n\t" \
    "ds_read_b64 v[64:65], v0 offset:352\n\t" \
    "ds_read_b64 v[66:67], v0 offset:360\n\t" \
    "s_waitcnt lgkmcnt(12)\n\t" \
    "v_mfma_f32_16x16x32_f16 v[16:19], v[108:111], v[128:131], v[16:19]\n\t" \
    "ds_read_b64 v[84:85], v0 offset:384\n\t" \
    "ds_read_b64 v[86:87], v0 offset:392\n\t" \
    "ds_read_b128 v[104:107], v1 offset:4800\n\t" \
    "s_waitcnt lgkmcnt(13)\n\t" \
    "v_mfma_f32_16x16x32_f16 v[32:35], v[112:115], v[128:131], v[32:35]\n\t" \
    "ds_read_b64 v[88:89], v0 offset:416\n\t" \
    "ds_read_b64 v[90:91], v0 offset:424\n\t" \
    "v_add_u32 v0, 256, v0\n\t" \
    "v_subrev_u32 v1, 384, v1\n\t" \
    "s_sub_u32 s69, s69, 1\n\t" \
    "s_cmp_lg_u32 s69, 0\n\t" \
    "s_cbranch_scc1 cv_c4_%=\n\t" \
    "s_waitcnt lgkmcnt(0)\n\t" \
    "s_nop 7\n\t" \
    "ds_write_b128 v2, v[4:7] offset:0\n\t" \
    "ds_write_b128 v2, v[8:11] offset:1024\n\t" \
    "ds_write_b128 v2, v[12:15] offset:2048\n\t" \
    "ds_write_b128 v2, v[16:19] offset:3072\n\t" \
    "ds_write_b128 v2, v[20:23] offset:4096\n\t" \
    "ds_write_b128 v2, v[24:27] offset:5120\n\t" \
    "ds_write_b128 v2, v[28:31] offset:6144\n\t" \
    "ds_write_b128 v2, v[32:35] offset:7168\n\t" \
    "s_waitcnt lgkmcnt(0)\n\t" \
    "cv_last_%=:\n\t" \
    ""
#define ASM_CLOB "v0", "v1", "v2", "v3", "v4", "v5", "v6", "v7", "v8", "v9", "v10", "v11", "v12", "v13", "v14", "v15", "v16", "v17", "v18", "v19", "v20", "v21", "v22", "v23", "v24", "v25", "v26", "v27", "v28", "v29", "v30", "v31", "v32", "v33", "v34", "v35", "v36", "v37", "v38", "v39", "v40", "v41", "v42", "v43", "v44", "v45", "v46", "v47", "v48", "v49", "v50", "v51", "v52", "v53", "v54", "v55", "v56", "v57", "v58", "v59", "v60", "v61", "v62", "v63", "v64", "v65", "v66", "v67", "v68", "v69", "v70", "v71", "v72", "v73", "v74", "v75", "v76", "v77", "v78", "v79", "v80", "v81", "v82", "v83", "v84", "v85", "v86", "v87", "v88", "v89", "v90", "v91", "v92", "v93", "v94", "v95", "v96", "v97", "v98", "v99", "v100", "v101", "v102", "v103", "v104", "v105", "v106", "v107", "v108", "v109", "v110", "v111", "v112", "v113", "v114", "v115", "v116", "v117", "v118", "v119", "v120", "v121", "v122", "v123", "v124", "v125", "v126", "v127", "v128", "v129", "v130", "v131", "v132", "v133", "v134", "v135", "v136", "v137", "v138", "v139", "v140", "v141", "v142", "v143", "v144", "v145", "v146", "v147", "v148", "v149", "v150", "v151", "v152", "v153", "v154", "v155", "v156", "v157", "v158", "v159", "v160", "v161", "v162", "v163", "v164", "v165", "v166", "v167", "v168", "v169", "v170", "v171", "v172", "v173", "v174", "v175", "v176", "v177", "v178", "v179", "v180", "v181", "v182", "v183", "v184", "v185", "v186", "v187", "v188", "v189", "v190", "v191", "v192", "v193", "v194", "v195", "v196", "v197", "v198", "v199", "v200", "v201", "v202", "v203", "v204", "v205", "v206", "v207", "v208", "v209", "v210", "v211", "v212", "v213", "v214", "v215", "v222", "v223", "v224", "v225", "v226", "v227", "v228", "v229", "v230", "v231", "v232", "v233", "v234", "v235", "v236", "v237", "v238", "v239", "v240", "v241", "v242", "v243", "v244", "v245", "v246", "v247", "v248", "v249", "v250", "v251", "v252", "v253", "v254", "v255", "a0", "a1", "a2", "a3", "a4", "a5", "a6", "a7", "a8", "a9", "a10", "a11", "a12", "a13", "a14", "a15", "a16", "a17", "a18", "a19", "a20", "a21", "a22", "a23", "a24", "a25", "a26", "a27", "a28", "a29", "a30", "a31", "a32", "a33", "a34", "a35", "a36", "a37", "a38", "a39", "a40", "a41", "a42", "a43", "a44", "a45", "a46", "a47", "a48", "a49", "a50", "a51", "a52", "a53", "a54", "a55", "a56", "a57", "a58", "a59", "a60", "a61", "a62", "a63", "a64", "a65", "a66", "a67", "a68", "a69", "a70", "a71", "a72", "a73", "a74", "a75", "a76", "a77", "a78", "a79", "a80", "a81", "a82", "a83", "a84", "a85", "a86", "a87", "a88", "a89", "a90", "a91", "a92", "a93", "a94", "a95", "a96", "a97", "a98", "a99", "a100", "a101", "a102", "a103", "a104", "a105", "a106", "a107", "a108", "a109", "a110", "a111", "a112", "a113", "a114", "a115", "a116", "a117", "a118", "a119", "a120", "a121", "a122", "a123", "a124", "a125", "a126", "a127", "a128", "a129", "a130", "a131", "a132", "a133", "a134", "a135", "a136", "a137", "a138", "a139", "a140", "a141", "a142", "a143", "a144", "a145", "a146", "a147", "a148", "a149", "a150", "a151", "a152", "a153", "a154", "a155", "a156", "a157", "a158", "a159", "a160", "a161", "a162", "a163", "a164", "a165", "a166", "a167", "a168", "a169", "a170", "a171", "a172", "a173", "a174", "a175", "a176", "a177", "a178", "a179", "a180", "a181", "a182", "a183", "a184", "a185", "a186", "a187", "a188", "a189", "a190", "a191", "a192", "a193", "a194", "a195", "a196", "a197", "a198", "a199", "a200", "a201", "a202", "a203", "a204", "a205", "a206", "a207", "a208", "a209", "a210", "a211", "a212", "a213", "a214", "a215", "a216", "a217", "a218", "a219", "a220", "a221", "a222", "a223", "a224", "a225", "a226", "a227", "a228", "a229", "a230", "a231", "a232", "a233", "a234", "a235", "a236", "a237", "a238", "a239", "a240", "a241", "a242", "a243", "a244", "a245", "a246", "a247", "a248", "a249", "a250", "a251", "a252", "a253", "a254", "a255", "s33", "s34", "s35", "s36", "s37", "s38", "s39", "s40", "s41", "s42", "s43", "s44", "s45", "s46", "s47", "s48", "s49", "s50", "s51", "s52", "s53", "s54", "s55", "s56", "s57", "s58", "s59", "s60", "s61", "s62", "s63", "s64", "s65", "s66", "s67", "s68", "s69", "s70", "s71", "s72", "s73", "s74", "s75", "s76", "s77", "s78", "s79", "s80", "s81", "s82", "s83", "s84", "s85", "s86", "s87", "s88", "s89", "s90", "s91", "s92", "s93", "s94", "s95", "s96", "s97", "s98", "s99", "vcc", "scc", "memory"
#define CV_CLOB "v0", "v1", "v2", "v3", "v4", "v5", "v6", "v7", "v8", "v9", "v10", "v11", "v12", "v13", "v14", "v15", "v16", "v17", "v18", "v19", "v20", "v21", "v22", "v23", "v24", "v25", "v26", "v27", "v28", "v29", "v30", "v31", "v32", "v33", "v34", "v35", "v36", "v37", "v38", "v39", "v40", "v41", "v42", "v43", "v44", "v45", "v46", "v47", "v48", "v49", "v50", "v51", "v52", "v53", "v54", "v55", "v56", "v57", "v58", "v59", "v60", "v61", "v62", "v63", "v64", "v65", "v66", "v67", "v68", "v69", "v70", "v71", "v72", "v73", "v74", "v75", "v76", "v77", "v78", "v79", "v80", "v81", "v82", "v83", "v84", "v85", "v86", "v87", "v88", "v89", "v90", "v91", "v92", "v93", "v94", "v95", "v96", "v97", "v98", "v99", "v100", "v101", "v102", "v103", "v104", "v105", "v106", "v107", "v108", "v109", "v110", "v111", "v112", "v113", "v114", "v115", "v116", "v117", "v118", "v119", "v120", "v121", "v122", "v123", "v124", "v125", "v126", "v127", "v128", "v129", "v130", "v131", "s69", "scc", "memory"
static constexpr unsigned G_STEP = 0x80000;
static constexpr unsigned G_HOFF = 0x40000;
static constexpr unsigned G_RM = 0x4;
static constexpr unsigned G_RP = 0x2;
static constexpr unsigned G_HP = 0x20;
static constexpr unsigned G_HKB = 0x8;
static constexpr unsigned G_RHO = 0x1400;
static constexpr unsigned G_RHS = 0x600;
static constexpr unsigned G_RARM = 0x1;
static constexpr unsigned G_NARM = 0x4;
static constexpr unsigned G_WS_UM = 0x400000;
static constexpr unsigned G_WS_CTL = 0x5d0000;
static constexpr unsigned G_WS_DONE = 0x5f8000;
static constexpr unsigned G_WS_EP = 0x5fa000;
static constexpr unsigned G_WS_STAMPS = 0x10000000;
static constexpr unsigned G_C_TASK = 0x200;
static constexpr unsigned G_C_GX = 0x1000;
static constexpr unsigned G_C_GV = 0x4000;
static constexpr unsigned G_C_KM = 0x8000;
static constexpr unsigned G_C_UM = 0x9000;
static constexpr unsigned G_C_PC = 0xc000;
static constexpr unsigned G_C_UT = 0xd000;
static constexpr unsigned G_C_GK = 0x10000;
static constexpr unsigned G_C_YC = 0x11000;
static constexpr unsigned G_C_ERR = 0x100;
static constexpr unsigned G_C_ARR = 0x14000;
static constexpr unsigned G_C_KG = 0x1c000;
static constexpr unsigned G_C_END = 0x22000;
static constexpr unsigned G_CTF = 0x2000;
static constexpr unsigned G_GVC = 0x2;
static constexpr unsigned G_GVH = 0x14000;
static constexpr unsigned G_HS8 = 0x80000;
static constexpr unsigned G_HS4 = 0x40000;
static constexpr unsigned G_UXS = 0x200000;
static constexpr unsigned G_UMH = 0xa0000;
static constexpr unsigned G_D_QX = 0x600000;
static constexpr unsigned G_D_KX = 0x1600000;
static constexpr unsigned G_D_KM = 0x2600000;
static constexpr unsigned G_D_UX = 0x2e00000;
static constexpr unsigned G_D_UMX = 0x7600000;
static constexpr unsigned G_D_PX = 0x8c00000;
static constexpr unsigned G_D_PM = 0x9000000;
static constexpr unsigned G_D_KT = 0x9100000;
static constexpr unsigned G_D_UT = 0x9600000;
static constexpr unsigned G_D_YB = 0x9900000;
static constexpr unsigned G_D_END = 0x9c00000;
static constexpr unsigned G_SKM = 0x7;
static constexpr unsigned G_GVN = 0x2;
static constexpr unsigned G_STG = 0x9000;
static constexpr unsigned G_NA_ = 0x22;
static constexpr unsigned G_KROW = 0x30;
#define FLG(x_) (kDefault[5])
#define FLG2(x_) (kDefault[7])
static constexpr int kDefault[8] = {PRM_DEFAULT};
#define MFMA32(a_, b_, c_) __builtin_amdgcn_mfma_f32_16x16x32_bf16(__builtin_bit_cast(bf16x8, a_), __builtin_bit_cast(bf16x8, b_), c_, 0, 0, 0)
#define MFMA16(a_, b_, c_) __builtin_amdgcn_mfma_f32_16x16x16bf16_1k(__builtin_bit_cast(s16x4, a_), __builtin_bit_cast(s16x4, b_), c_, 0, 0, 0)
#define WLD8(d_, voff_, base_, off_) asm volatile("s_nop 4\n\tglobal_load_dwordx2 %0, %1, %2 offset:%3 sc1" : "=v"(d_) : "v"(voff_), "s"(base_), "i"(off_) : "memory")
#define WLD16(d_, voff_, base_, off_) asm volatile("s_nop 4\n\tglobal_load_dwordx4 %0, %1, %2 offset:%3 sc1" : "=v"(d_) : "v"(voff_), "s"(base_), "i"(off_) : "memory")
#define WST16(voff_, d_, base_, off_) asm volatile("s_nop 4\n\tglobal_store_dwordx4 %0, %1, %2 offset:%3 sc1\n\ts_nop 1" :: "v"(voff_), "v"(d_), "s"(base_), "i"(off_) : "memory")
#define WLD16X4(d0, d1, d2, d3, voff_, base_) asm volatile("s_nop 4\n\tglobal_load_dwordx4 %0, %4, %5 sc1\n\tglobal_load_dwordx4 %1, %4, %5 offset:1024 sc1\n\t" \
    "global_load_dwordx4 %2, %4, %5 offset:2048 sc1\n\tglobal_load_dwordx4 %3, %4, %5 offset:3072 sc1" \
    : "=&v"(d0), "=&v"(d1), "=&v"(d2), "=&v"(d3) : "v"(voff_), "s"(base_) : "memory")

__device__ __forceinline__ u32x4 pack8(const f32x4& a, const f32x4& b) {
    return u32x4{pk2bf(a[0], a[1]), pk2bf(a[2], a[3]), pk2bf(b[0], b[1]), pk2bf(b[2], b[3])};
}
template <class T> __device__ __forceinline__ T* uni(T* q) {
    const u64 v = (u64)q;
    unsigned lo = __builtin_amdgcn_readfirstlane((unsigned)v), hi = __builtin_amdgcn_readfirstlane((unsigned)(v >> 32));
    asm volatile("s_nop 4" : "+s"(lo), "+s"(hi));
    return (T*)(((u64)hi << 32) | lo);
}
#define CTLO (G_WS_CTL + (unsigned)p.prm[0])
template <bool FAST = false>
__device__ __forceinline__ void wait_word(const MainP& p, const char* f, int want, unsigned code) {
    unsigned tries = 0;
    while (__any((int)__hip_atomic_load((const gu32*)f, RLX_AGENT) < want)) {
        if (++tries > kWaitCap) { __hip_atomic_store((gu32*)(p.ws + G_WS_CTL + G_C_ERR), code, RLX_AGENT); break; }
        if ((tries & 1023u) == 0u && __hip_atomic_load((const gu32*)(p.ws + G_WS_CTL + G_C_ERR), RLX_AGENT) != 0xffffffffu) break;
        if (FAST) __builtin_amdgcn_s_sleep(1); else __builtin_amdgcn_s_sleep(10);
    }
}

__device__ __forceinline__ int skew(int s, int id) { return (s ^ (G_SKM * id)) & 63; }
__device__ __forceinline__ const char* qx_ptr(const MainP& p, int s, int h) {
    return p.ws + G_D_QX + (size_t)h * G_HS8 + skew(s, h) * 8192;
}
__device__ __forceinline__ unsigned umx_base(int h) { return G_D_UMX + (h / 3) * 0x200000 + (h % 3) * G_UMH; }
typedef GAS unsigned long long gu64;
__device__ __forceinline__ void init_done(char* ws, int slot, unsigned val) {
    const unsigned e = __hip_atomic_load((const gu32*)(ws + G_WS_EP), RLX_AGENT);
    __hip_atomic_store((gu64*)(ws + G_WS_DONE + slot * 64), ((u64)val << 32) | (e + 1u), RLX_AGENT);
}
__device__ __forceinline__ void init_seen(char* ws, int lane, unsigned e) {
    for (unsigned tries = 0; tries < kWaitCap; ++tries) {
        if (!__any(__hip_atomic_load((const gu32*)(ws + G_WS_DONE + min(lane, 59) * 64), RLX_AGENT) != e + 1u)) break;
        __builtin_amdgcn_s_sleep(8);
    }
}
__device__ __forceinline__ void init_count(char* ws, int lane) { if (lane == 0) __hip_atomic_fetch_add((gu32*)(ws + G_WS_CTL + G_C_ARR), 1u, RLX_AGENT); }
__device__ __forceinline__ void fill_block(char* ws, int b, int tid, const u32x4& v0, const u32x4& v1) {
    const unsigned o = b * 8192 + tid * 16, o2 = o + 4096;
    asm volatile("global_store_dwordx4 %0, %1, %2 sc1\n\ts_nop 1" :: "v"(o), "v"(v0), "s"(ws) : "memory");
    asm volatile("global_store_dwordx4 %0, %1, %2 sc1\n\ts_nop 1" :: "v"(o2), "v"(v1), "s"(ws) : "memory");
}
__device__ __forceinline__ void init_fill(char* ws, const u16* mem0, int f, int tid) {
    const u32x4 sent = {0xffffffffu, 0xffffffffu, 0xffffffffu, 0xffffffffu};
    u32x4 v0 = sent, v1 = sent;
    if (f < 16) {
#pragma unroll
        for (int k = 0; k < 2; ++k) {
            const int q = tid + 256 * k + ((f & 2) ? 256 : 0), m = q & 15;
            if (!(f & 2) || (q >= 512 && q < 576)) {
                const unsigned* src = (const unsigned*)(mem0 + (2 * (m >> 2) + (m & 1)) * DM + 16 * (q >> 4) + (m & 2));
                const u32x4 o = {min(src[0], 0xfffffffeu), min(src[2], 0xfffffffeu), min(src[4], 0xfffffffeu), min(src[6], 0xfffffffeu)};
                if (k) v1 = o; else v0 = o;
            }
        }
    }
    static_assert(G_HKB == 8 && G_RP == 2 && G_RM == 4, "layout");
    constexpr int NH = 48, NP = NH + 16, NU = 144;
#pragma unroll 1
    for (int q = f; q < NP + NU + (int)(G_C_END >> 13); q += 48) {
        const int u = q - 32, y = q - NH;
        fill_block(ws, q < 32 ? (q >> 4) * 256 + ((q >> 3) & 1) * 16 + ((q >> 2) & 1) * 2 + 8 + (q & 2) * 2 + (q & 1) : q < NH ? (u >> 3) * 256 + (u & 7) : q < NP ? (y >> 3) * 256 + 32 + (y & 7) : q < NP + NU ? q - NP + 512 : q - NP - NU + (int)(G_WS_CTL >> 13), tid, v0, v1);
        v0 = sent; v1 = sent;
    }
    const unsigned e = __hip_atomic_load((const gu32*)(ws + G_WS_EP), RLX_AGENT);
    asm volatile("s_waitcnt vmcnt(0)" :: "v"(e) : "memory");
    __syncthreads();
    if (tid == 0) __hip_atomic_store((gu64*)(ws + G_WS_DONE + (12 + f) * 64), (u64)(e + 1u), RLX_AGENT);
}
__device__ __forceinline__ void init_cal(char* ws, char* smem, int id, int tid, int n) {
    const int sl = id % 6, w = tid >> 6;
    char* a = ws + (size_t)(sl >> 1) * 0x200000 + 0x1B0000 + (id / 6) * 16384 + (sl & 1) * 8192 + w * 2048;
    volatile unsigned* lds = (volatile unsigned*)smem;
    const u32x4 z = {0u, 0u, 0u, 0u};
    asm volatile("global_store_dwordx4 %0, %1, off sc1\n\tglobal_store_dwordx4 %0, %1, off offset:1024 sc1\n\ts_waitcnt vmcnt(0)" :: "v"(a + (tid & 63) * 16), "v"(z) : "memory");
    __syncthreads();
    if ((tid & 63) == 0) {
        unsigned best = 0xfffffffeu;
#pragma unroll 1
        for (int it = 0; it < 3 * n; ++it) {
            const unsigned c0 = (unsigned)__builtin_amdgcn_s_memtime();
            const unsigned v = __hip_atomic_fetch_or((gu32*)(a + it * 128), 0u, RLX_AGENT);
            asm volatile("s_waitcnt vmcnt(0)" :: "v"(v) : "memory");
            best = min(best, (unsigned)__builtin_amdgcn_s_memtime() - c0);
            if (it + 1 >= 2 * n && best > 560u) break;
        }
        lds[w] = best;
    }
    __syncthreads();
    if (tid == 0) init_done(ws, id, min(min(lds[0], lds[1]), min(lds[2], lds[3])));
    __syncthreads();
}

#define TST(slot_, i_)
#define PIN8(a) asm volatile("" : "+v"(a[0]), "+v"(a[1]), "+v"(a[2]), "+v"(a[3]), "+v"(a[4]), "+v"(a[5]), "+v"(a[6]), "+v"(a[7]))
struct AOps { u32x4 q[4], k[8]; int fl; };
struct APtr { const char* q; const char* k; const char* m; unsigned voff, vkm, voffm, vfl; };
__device__ __forceinline__ void attn_batch(const MainP& p, AOps& o, const APtr& a) {
    const char* q2 = a.q + 4096; const char* k2 = a.k + 4096;
    WLD16(o.q[0], a.voff, a.q, 0); WLD16(o.q[1], a.voff, a.q, 2048); WLD16(o.q[2], a.voff, q2, 0); WLD16(o.q[3], a.voff, q2, 2048);
    WLD16X4(o.k[0], o.k[1], o.k[2], o.k[3], a.voff, a.k); WLD16X4(o.k[4], o.k[5], o.k[6], o.k[7], a.voff, k2);
    asm volatile("global_load_dword %0, %1, %2 sc1" : "=v"(o.fl) : "v"(a.vfl), "s"(p.ws) : "memory");
}
__device__ __forceinline__ void attn_pin(AOps& o) {
    asm volatile("" : "+v"(o.fl), "+v"(o.q[0]), "+v"(o.q[1]), "+v"(o.q[2]), "+v"(o.q[3])); PIN8(o.k);
}
static constexpr int NFINE = 6;
static constexpr int NG1 = 64 - NFINE;
__device__ __forceinline__ void score_round4(float* r, const f32x4& x) {
    asm volatile("v_cvt_pk_bf16_f32 %0, 0, %4\n\tv_cvt_pk_bf16_f32 %1, 0, %5\n\tv_cvt_pk_bf16_f32 %2, 0, %6\n\tv_cvt_pk_bf16_f32 %3, 0, %7\n\t"
                 "v_mul_f32 %0, 0x3db504f3, %0\n\tv_mul_f32 %1, 0x3db504f3, %1\n\tv_mul_f32 %2, 0x3db504f3, %2\n\tv_mul_f32 %3, 0x3db504f3, %3\n\t"
                 "v_cvt_pk_bf16_f32 %0, 0, %0\n\tv_cvt_pk_bf16_f32 %1, 0, %1\n\tv_cvt_pk_bf16_f32 %2, 0, %2\n\tv_cvt_pk_bf16_f32 %3, 0, %3"
                 : "=&v"(r[0]), "=&v"(r[1]), "=&v"(r[2]), "=&v"(r[3]) : "v"(x[0]), "v"(x[1]), "v"(x[2]), "v"(x[3]));
}
__device__ __forceinline__ float vmax3(float a, float b, float c) { float r; asm("v_max3_f32 %0, %1, %2, %3" : "=v"(r) : "v"(a), "v"(b), "v"(c)); return r; }
template <int N>
__device__ __forceinline__ void attn_soft(float (&q)[12], u32x2& Pm, u32x4& Px) {
    float m = vmax3(q[0], q[1], q[2]);
#pragma unroll
    for (int i = 3; i < N; i += 2) m = vmax3(m, q[i], q[i + 1 < N ? i + 1 : i]);
    { auto a = __builtin_amdgcn_permlane16_swap(__float_as_uint(m), __float_as_uint(m), false, false); m = vmax3(m, __uint_as_float(a[0]), __uint_as_float(a[1])); }
    { auto a = __builtin_amdgcn_permlane32_swap(__float_as_uint(m), __float_as_uint(m), false, false); m = vmax3(m, __uint_as_float(a[0]), __uint_as_float(a[1])); }
    const float ml = -m * 1.4426950408889634f;
#pragma unroll
    for (int i = 0; i < N; ++i) q[i] = __builtin_amdgcn_exp2f(fmaf(q[i], 1.4426950408889634f, ml));
    float sum = 0.5f * ((q[0] + q[1]) + (q[2] + q[3])) + ((q[4] + q[5]) + (q[6] + q[7]));
    if (N > 8) sum += (q[8] + q[9]) + (q[10] + q[11]);
    sum = rowsum4(sum);
    const float inv = __builtin_amdgcn_rcpf(sum);
    Pm = u32x2{pk2bf(q[0] * inv, q[1] * inv), pk2bf(q[2] * inv, q[3] * inv)};
    Px = u32x4{pk2bf(q[4] * inv, q[5] * inv), pk2bf(q[6] * inv, q[7] * inv), N > 8 ? pk2bf(q[8] * inv, q[9] * inv) : 0u, N > 8 ? pk2bf(q[10] * inv, q[11] * inv) : 0u};
}
static constexpr int NA = G_NA_;
#define NPRE_ 38
static constexpr int NPRE = NPRE_;
__device__ __forceinline__ int attn_next(int s) { return s < NA ? s + 1 : s + 2; }
__device__ __forceinline__ unsigned attn_flag(const MainP& p, int s, int hq) { return CTLO + G_C_PC + ((s > NA && (s & 1)) ? 1024 : 0) + hq * 128; }
struct BOps { u32x4 q[8], k[8], m[4]; int fl; };
#define WLD16X4A(d0, d1, d2, d3, voff_, base_) asm volatile("s_nop 4\n\tglobal_load_dwordx4 %0, %4, %5 sc1\n\tglobal_load_dwordx4 %1, %4, %5 offset:1024 sc1\n\t" \
    "global_load_dwordx4 %2, %4, %5 offset:2048 sc1\n\tglobal_load_dwordx4 %3, %4, %5 offset:3072 sc1" \
    : "=&a"(d0), "=&a"(d1), "=&a"(d2), "=&a"(d3) : "v"(voff_), "s"(base_) : "memory")
#define PIN8A(a) asm volatile("" : "+a"(a[0]), "+a"(a[1]), "+a"(a[2]), "+a"(a[3]), "+a"(a[4]), "+a"(a[5]), "+a"(a[6]), "+a"(a[7]))
#define MFMA_AV0(acc, a_, b_) asm volatile("v_mfma_f32_16x16x32_bf16 %0, %1, %2, 0" : "=&v"(acc) : "a"(a_), "v"(b_))
#define MFMA_AV(acc, a_, b_) asm volatile("v_mfma_f32_16x16x32_bf16 %0, %1, %2, %0" : "+v"(acc) : "a"(a_), "v"(b_))
__device__ __forceinline__ void attn_batch8(const MainP& p, BOps& o, const APtr& a) {
    const char* q2 = a.q + 4096; const char* k2 = a.k + 4096;
    WLD16X4(o.q[0], o.q[1], o.q[2], o.q[3], a.voff, a.q); WLD16X4(o.q[4], o.q[5], o.q[6], o.q[7], a.voff, q2);
    WLD16X4A(o.k[0], o.k[1], o.k[2], o.k[3], a.voff, a.k); WLD16X4A(o.k[4], o.k[5], o.k[6], o.k[7], a.voff, k2);
}
template <bool PRE>
__device__ __forceinline__ void attn_step(const MainP& p, BOps& o, BOps& n1, BOps& n2, APtr& a, int& kg, int& kk, int s, int prev, int h, int w, int lane) {
    const int g = lane >> 4, c = lane & 15;
    const float scale = 0.08838834764831845f;
    TST(s * 64 + h * 2, 0);
    const int s2 = attn_next(attn_next(s));
    const int sm = PRE ? attn_next(s) : s;
    if (kg <= s2 || kk < sm) {
        VMC0();
        wait_word<true>(p, p.ws + a.vfl, (lane & 1) ? sm : s2 + 1, 13u);
    }
    TST(s * 64 + h * 2, 1);
    a.q = qx_ptr(p, s2, h); a.k = a.q + (G_D_KX - G_D_QX); a.m = p.ws + G_D_KM + (size_t)h * G_HS4 + skew(sm, h) * 4096;
    if (PRE) {
        asm volatile("s_waitcnt vmcnt(20)\n\ts_barrier" : "+a"(o.m[0]), "+a"(o.m[1]), "+a"(o.m[2]), "+a"(o.m[3]), "+v"(o.fl) :: "memory");
        PIN8(o.q); PIN8A(o.k);
    }
    __builtin_amdgcn_sched_barrier(0);
    BOps& d = PRE ? n1 : o;
    u32x4 (&km)[4] = o.m;
    int& fl = o.fl;
    WLD16X4A(d.m[0], d.m[1], d.m[2], d.m[3], a.vkm, a.m);
    asm volatile("global_load_dword %0, %1, %2 sc1" : "=v"(d.fl) : "v"(a.vfl), "s"(p.ws) : "memory");
    const char* q2 = a.q + 4096; const char* k2 = a.k + 4096;
#define SPREAD(x_) do { if (PRE) { __builtin_amdgcn_sched_barrier(0); x_; __builtin_amdgcn_sched_barrier(0); } } while (0)
    if (!PRE) attn_batch8(p, n2, a);
    f32x4 y[5];
#pragma unroll
    for (int wc = 0; wc < 4; ++wc)
#pragma unroll
        for (int i = 0; i < 3; ++i) { if (wc == 0) MFMA_AV0(y[i], o.k[i >> 1], o.q[(i + 1) >> 1]); else MFMA_AV(y[i], o.k[wc * 2 + (i >> 1)], o.q[wc * 2 + ((i + 1) >> 1)]); }
    asm volatile("s_nop 7" : "+v"(y[0]), "+v"(y[1]), "+v"(y[2]));
    SPREAD(WLD16X4(n2.q[0], n2.q[1], n2.q[2], n2.q[3], a.voff, a.q));
    float q0[12], q1[12];
    score_round4(q0 + 4, y[0]); score_round4(q1 + 4, y[1]); score_round4(q1 + 8, y[2]);
#pragma unroll
    for (int i = 0; i < 4; ++i) {
        const bool vis = 4 * g + i <= c;
        q0[4 + i] = vis ? q0[4 + i] : -INFINITY; q1[8 + i] = vis ? q1[8 + i] : -INFINITY;
    }
    __builtin_amdgcn_sched_barrier(0);
    if (!PRE) {
        asm volatile("s_waitcnt vmcnt(16)\n\ts_barrier" : "+a"(km[0]), "+a"(km[1]), "+a"(km[2]), "+a"(km[3]), "+v"(fl) :: "memory");
        PIN8(n1.q); PIN8A(n1.k);
    }
    { const unsigned fo = attn_flag(p, s, h >> 2); const int fv = PRE ? prev - 1 : s - 1;
      asm volatile("s_cmp_lg_u32 %3, 0\n\ts_cbranch_scc1 1f\n\tglobal_store_dword %0, %1, %2 sc1\n1:" :: "v"(fo), "v"(fv), "s"(p.ws), "s"(w) : "scc", "memory"); }
#pragma unroll
    for (int wc = 0; wc < 4; ++wc)
#pragma unroll
        for (int i = 0; i < 2; ++i) { if (wc == 0) MFMA_AV0(y[3 + i], km[0], o.q[i]); else MFMA_AV(y[3 + i], km[wc], o.q[wc * 2 + i]); }
    asm volatile("s_nop 7" : "+v"(y[3]), "+v"(y[4]));
    kg = __builtin_amdgcn_readlane(fl, 0); kk = __builtin_amdgcn_readlane(fl, 1);
    SPREAD(WLD16X4(n2.q[4], n2.q[5], n2.q[6], n2.q[7], a.voff, q2));
    score_round4(q0, y[3]); score_round4(q1, y[4]);
    u32x2 Pm0, Pm1; u32x4 Px0, Px1;
    attn_soft<8>(q0, Pm0, Px0);
    SPREAD(WLD16X4A(n2.k[0], n2.k[1], n2.k[2], n2.k[3], a.voff, a.k));
    attn_soft<12>(q1, Pm1, Px1);
    SPREAD(WLD16X4A(n2.k[4], n2.k[5], n2.k[6], n2.k[7], a.voff, k2));
    char* pxb = p.ws + G_D_PX + (size_t)(s * 32 + h) * 2048;
    char* pmb = p.ws + G_D_PM + (size_t)s * 16384 + h * 256; char* pm2 = pmb + 8192;
    WST16(a.voff, Px0, pxb, 0); WST16(a.voff, Px1, pxb, 1024);
    asm volatile("global_store_dwordx2 %0, %1, %3 sc1\n\tglobal_store_dwordx2 %0, %2, %4 sc1" :: "v"(a.voffm), "v"(Pm0), "v"(Pm1), "s"(pmb), "s"(pm2) : "memory");
    if (!PRE) { const unsigned fo = attn_flag(p, s, h >> 2);
      asm volatile("s_cmp_lt_u32 %4, 46\n\ts_cbranch_scc1 1f\n\ts_waitcnt vmcnt(0)\n\ts_barrier\n\ts_cmp_lg_u32 %3, 0\n\ts_cbranch_scc1 1f\n\tglobal_store_dword %0, %1, %2 sc1\n1:"
                   :: "v"(fo), "v"(s), "s"(p.ws), "s"(w), "s"(s) : "scc", "memory"); }
    TST(s * 64 + h * 2, 2);
}
__device__ __forceinline__ void attn_role(const MainP& p, int s, int h, int w, int lane) {
    APtr a;
    a.q = qx_ptr(p, s, h); a.k = a.q + (G_D_KX - G_D_QX);
    a.voff = lane * 16; a.vkm = (lane & 55) * 16; a.voffm = (lane & 15) * 16 + ((lane >> 4) & 1) * 8;
    a.vfl = CTLO + ((lane & 1) ? G_C_KM : G_C_GX) + h * 128;
    BOps o0, o1, o2;
    wait_word(p, p.ws + a.vfl, (lane & 1) ? s : attn_next(s) + 1, 10u);
    attn_batch8(p, o0, a);
    a.q = qx_ptr(p, attn_next(s), h); a.k = a.q + (G_D_KX - G_D_QX); a.m = p.ws + G_D_KM + (size_t)h * G_HS4 + skew(s, h) * 4096;
    attn_batch8(p, o1, a);
    WLD16X4A(o0.m[0], o0.m[1], o0.m[2], o0.m[3], a.vkm, a.m);
    VMC0();
    o0.fl = -1;
    PIN8(o0.q); PIN8A(o0.k); PIN8(o1.q); PIN8A(o1.k);
    int kg = -1, kk = -1, pv = s;
    static_assert(NA % 2 == 0 && NG1 + 3 < NSEG && NPRE + 6 < 46, "ranges");
#pragma unroll 1
    while (s < NPRE) {
        attn_step<true>(p, o0, o1, o2, a, kg, kk, s, pv, h, w, lane); pv = s; s = attn_next(s);
        attn_step<true>(p, o1, o2, o0, a, kg, kk, s, pv, h, w, lane); pv = s; s = attn_next(s);
        attn_step<true>(p, o2, o0, o1, a, kg, kk, s, pv, h, w, lane); pv = s; s = attn_next(s);
    }
    asm volatile("s_waitcnt vmcnt(0)" : "+a"(o0.m[0]), "+a"(o0.m[1]), "+a"(o0.m[2]), "+a"(o0.m[3]), "+v"(o0.fl) :: "memory");
    PIN8(o0.q); PIN8A(o0.k); PIN8(o1.q); PIN8A(o1.k);
#pragma unroll 1
    for (;;) {
        attn_step<false>(p, o0, o1, o2, a, kg, kk, s, 0, h, w, lane);
        if ((s = attn_next(s)) >= NG1) break;
        attn_step<false>(p, o1, o2, o0, a, kg, kk, s, 0, h, w, lane);
        if ((s = attn_next(s)) >= NG1) break;
        attn_step<false>(p, o2, o0, o1, a, kg, kk, s, 0, h, w, lane);
        if ((s = attn_next(s)) >= NG1) break;
    }
    VMC0();
}
__device__ __forceinline__ unsigned umax4(const u32x4& v) { return max(max(v[0], v[1]), max(v[2], v[3])); }
__device__ __forceinline__ bool give_up(const MainP& p, unsigned& tries, unsigned code) {
    if (++tries <= kWaitCap / 4) return false;
    __hip_atomic_store((gu32*)(p.ws + G_WS_CTL + G_C_ERR), code, RLX_AGENT);
    return true;
}
__device__ __forceinline__ void task_attn_tail(const MainP& p, int s, int h, int w, int lane) {
    const int g = lane >> 4, c = lane & 15, qt = w & 1;
    const float scale = 0.08838834764831845f;
    APtr a;
    a.q = qx_ptr(p, s, h) + qt * 1024; a.k = qx_ptr(p, s, h) + (G_D_KX - G_D_QX); a.m = p.ws + G_D_KM + (size_t)h * G_HS4 + skew(s, h) * 4096;
    a.voff = lane * 16; a.vkm = (lane & 55) * 16; a.voffm = c * 16 + (g & 1) * 8; a.vfl = G_WS_CTL;
    AOps o;
    wait_word(p, p.ws + CTLO + G_C_GX + h * 128, s + 1, 14u);
    attn_batch(p, o, a);
    VMC0();
    attn_pin(o);
    float q[12];
    u32x4 km[4];
#pragma unroll
    for (int kt = 1; kt >= -1; --kt) {
        if (kt < 0) {
            TST(s * 64 + h * 2 + qt, 0);
            wait_word(p, p.ws + CTLO + G_C_KM + h * 128, s - 2, 15u);
            if (FLG(p.prm[5]) & 2) wait_word<true>(p, p.ws + CTLO + G_C_KM + h * 128, s - 1, 15u);
            for (unsigned tries = 0;;) {
                WLD16X4(km[0], km[1], km[2], km[3], a.vkm, a.m);
                asm volatile("s_waitcnt vmcnt(0)" : "+v"(km[0]), "+v"(km[1]), "+v"(km[2]), "+v"(km[3]) :: "memory");
                if (!__any(max(max(umax4(km[0]), umax4(km[1])), max(umax4(km[2]), umax4(km[3]))) == 0xffffffffu) || give_up(p, tries, 16u)) break;
            }
            TST(s * 64 + h * 2 + qt, 1);
        }
        f32x4 y = {0.f, 0.f, 0.f, 0.f};
#pragma unroll
        for (int wc = 0; wc < 4; ++wc) y = MFMA32(kt < 0 ? km[wc] : o.k[wc * 2 + (kt < 0 ? 0 : kt)], o.q[wc], y);
#pragma unroll
        for (int i = 0; i < 4; ++i) q[4 + 4 * kt + i] = (16 * kt + 4 * g + i <= 16 * qt + c) ? bfr(bfr(y[i]) * scale) : -INFINITY;
    }
    u32x2 Pm; u32x4 Px;
    attn_soft<12>(q, Pm, Px);
    char* pxb = p.ws + G_D_PX + (size_t)(s * 32 + h) * 2048 + qt * 1024;
    char* pmb = p.ws + G_D_PM + (size_t)s * 16384 + h * 256 + qt * 8192;
    WST16(a.voff, Px, pxb, 0);
    asm volatile("global_store_dwordx2 %0, %1, %2 sc1" :: "v"(a.voffm), "v"(Pm), "s"(pmb) : "memory");
    TST(s * 64 + h * 2 + qt, 2);
}

#define RLD(d_, voff_, base_, off_) asm volatile("global_load_dwordx4 %0, %1, %2 offset:%3 sc1" : "=v"(d_) : "v"(voff_), "s"(base_), "i"(off_) : "memory")
#define RLDA(d_, voff_, base_, off_) asm volatile("global_load_dwordx4 %0, %1, %2 offset:%3 sc1" : "=a"(d_) : "v"(voff_), "s"(base_), "i"(off_) : "memory")
#define MFMA_ACC_A(acc, a_, b_) asm volatile("v_mfma_f32_16x16x32_bf16 %0, %1, %2, %0" : "+v"(acc) : "a"(a_), "v"(b_))
#define VMW(n_) asm volatile("s_waitcnt vmcnt(%0)" :: "i"(n_) : "memory")
template <int NT, int DEPTH, bool TAIL>
__device__ __forceinline__ void task_out(const MainP& p, char* smem, int s, int ct0, int w, int lane) {
    const int g = lane >> 4, c = lane & 15;
    const unsigned voff = lane * 16;
    f32x4 acc[NT][2];
#pragma unroll
    for (int t = 0; t < NT; ++t) { acc[t][0] = f32x4{0.f, 0.f, 0.f, 0.f}; acc[t][1] = acc[t][0]; asm volatile("" : "+v"(acc[t][0]), "+v"(acc[t][1])); }
    const char* ub[NT];
#pragma unroll
    for (int t = 0; t < NT; ++t) ub[t] = uni(p.ws + G_D_UX + (size_t)(ct0 + t) * G_UXS + skew(s, ct0 + t) * 32768 + w * 8192);
    const char* pb = uni(p.ws + G_D_PX + (size_t)(s * 32 + w * 8) * 2048);
    const char* um = uni(p.ws + s * 9216 + ct0 * 256);
    const unsigned vu0 = umx_base(w * 8 + g) + c * 16, vu1 = umx_base(w * 8 + 4 + g) + c * 16;
    const char* pm = uni(p.ws + G_D_PM + (size_t)s * 16384 + w * 2048);
    constexpr int NR = DEPTH + 1 > 10 ? 10 : DEPTH + 1;
    u32x4 a[NR][NT], b[10][2];
    unsigned vb = voff, va = voff;
    TST(4096 + (s * 36 + ct0) * 4 + w, 0);
    const int f0 = (int)__hip_atomic_load((const gu32*)(p.ws + CTLO + (lane < 8 ? G_C_UM + (w * 8 + lane) * 128 : lane < 16 ? G_C_GV + ((s & (G_GVN - 1)) * 32 + w * 8 + lane - 8) * 128
                                                                                                                                : attn_flag(p, s, w * 2 + (lane & 1)) - CTLO)), RLX_AGENT);
    const bool early = !TAIL && !__any(f0 < s);
    if (!early) wait_word(p, p.ws + CTLO + (lane < 8 ? G_C_GV + ((s & (G_GVN - 1)) * 32 + w * 8 + lane) * 128 : G_C_UM + w * 8 * 128), lane < 8 ? s : s - 3, 21u);
#define ALOAD(q) do { if ((q) < 8) { _Pragma("unroll") for (int t = 0; t < NT; ++t) RLDA(a[(q) % NR][t], va, ub[t], 0);  \
                                     asm volatile("v_add_u32 %0, 0x400, %0" : "+v"(va)); }                                            \
                      else { _Pragma("unroll") for (int t = 0; t < NT; ++t) RLDA(a[(q) % NR][t], ((q) & 1) ? vu1 : vu0, um, t * 256); } } while (0)
    constexpr int D0 = DEPTH < 8 ? DEPTH : 8;
    asm volatile("s_nop 4");
#pragma unroll
    for (int q = 0; q < D0; ++q) ALOAD(q);
    asm volatile("s_cmp_lg_u32 %0, 0\n\ts_cbranch_scc1 1f\n\ts_waitcnt vmcnt(0)\n1:\n\ts_waitcnt vmcnt(30)" :: "s"(__builtin_amdgcn_readfirstlane((int)early)) : "scc", "memory");
    unsigned tries = 0;
    if (TAIL) {
        const char* sp = pm + (lane & 7) * 256 + ((lane >> 3) & 1) * 8192;
        const int dl = (FLG(p.prm[5]) >> 26) & 31;
        if (dl) {
            wait_word<true>(p, p.ws + CTLO + G_C_KM + ((ct0 >> 1) + w) * 128, s - 1, 25u);
            for (int i = 0; i < dl + (s < 63 ? 5 : 0); ++i) __builtin_amdgcn_s_sleep(2);
        } else
        while (__any(__hip_atomic_load((const gu32*)sp, RLX_AGENT) == 0xffffffffu) && !give_up(p, tries, 23u)) __builtin_amdgcn_s_sleep(1);
    } else if (!early) wait_word(p, p.ws + CTLO + (lane < 8 ? G_C_UM + (w * 8 + lane) * 128 : attn_flag(p, s, w * 2 + (lane & 1)) - CTLO), s, 22u);
    if (!early) TST(4096 + (s * 36 + ct0) * 4 + w, 1);
    for (;;) {
    asm volatile("s_nop 4");
#pragma unroll
    for (int q = 0; q < 8; ++q) { RLD(b[q][0], vb, pb, 0); RLD(b[q][1], vb, pb, 1024); asm volatile("v_add_u32 %0, 0x800, %0" : "+v"(vb)); }
#pragma unroll
    for (int q = 8; q < 10; ++q) { RLD(b[q][0], voff, pm, (q & 1) * 1024); RLD(b[q][1], voff, pm + 8192, (q & 1) * 1024); }
#pragma unroll
    for (int q = D0; q < NR; ++q) ALOAD(q);
#pragma unroll
    for (int q = 0; q < 10; ++q) {
        const int issuedA = (NR + q < 10 ? NR + q : 10) - D0;
        if (q < D0) VMW(2 * (9 - q) + issuedA * NT); else VMW((issuedA - (q - D0 + 1)) * NT);
#pragma unroll
        for (int t = 0; t < NT; ++t) { MFMA_ACC_A(acc[t][0], a[q % NR][t], b[q][0]); MFMA_ACC_A(acc[t][1], a[q % NR][t], b[q][1]); }
        if (NR + q < 10) ALOAD(NR + q);
    }
    asm volatile("s_nop 7" ::: "memory");
#pragma unroll
    for (int t = 0; t < NT; ++t) asm volatile("" : "+v"(acc[t][0]), "+v"(acc[t][1]));
    if (!TAIL) break;
    f32x4 chk = acc[0][0];
#pragma unroll
    for (int t = 0; t < NT; ++t) chk += acc[t][1] + acc[t][0];
    const float ck = (chk[0] + chk[1]) + (chk[2] + chk[3]);
    if (!__any(ck != ck)) break;
    unsigned mx = 0;
#pragma unroll
    for (int q = 0; q < 10; ++q) { asm volatile("" : "+v"(b[q][0]), "+v"(b[q][1])); mx = max(mx, max(umax4(b[q][0]), umax4(b[q][1]))); }
#pragma unroll
    for (int t = 0; t < NT; ++t) { asm volatile("" : "+a"(a[8 % NR][t]), "+a"(a[9 % NR][t])); mx = max(mx, max(umax4(a[8 % NR][t]), umax4(a[9 % NR][t]))); }
    if (!__any(mx == 0xffffffffu) || give_up(p, tries, 24u)) break;
    vb = voff;
#pragma unroll
    for (int t = 0; t < NT; ++t) { acc[t][0] = f32x4{0.f, 0.f, 0.f, 0.f}; acc[t][1] = acc[t][0]; asm volatile("" : "+v"(acc[t][0]), "+v"(acc[t][1])); }
    }
    TST(4096 + (s * 36 + ct0) * 4 + w, 2);
    f32x4* red = (f32x4*)smem;
#pragma unroll
    for (int t = 0; t < NT; ++t) { red[((w * NT + t) * 2 + 0) * 64 + lane] = acc[t][0]; red[((w * NT + t) * 2 + 1) * 64 + lane] = acc[t][1]; }
    __syncthreads();
    u16* orow = p.out + (size_t)(32 * s + 16 * (w & 1) + c) * OUTW + 16 * ct0 + 4 * g;
#pragma unroll
    for (int t = 0; t < NT; ++t)
        if ((t & 1) == (w >> 1)) {
            const int m = w & 1;
            const f32x4 v = (red[((0 * NT + t) * 2 + m) * 64 + lane] + red[((1 * NT + t) * 2 + m) * 64 + lane]) +
                            (red[((2 * NT + t) * 2 + m) * 64 + lane] + red[((3 * NT + t) * 2 + m) * 64 + lane]);
            *(u32x2*)(orow + 16 * t) = u32x2{pk2bf(v[0], v[1]), pk2bf(v[2], v[3])};
        }
}

static constexpr int KROW = G_KROW;
static constexpr int KARR = 80 * KROW;
static constexpr int UCS = 2208;
struct BCh {
    u16 uc[4][UCS];
    f32x4 cst[NST];
    u16 Kh[KARR + 256];
    u16 ub[SL];
};
static_assert(4 * sizeof(BCh) <= LDS_TOTAL - 32, "LDS");
__device__ __forceinline__ unsigned wave_umax(unsigned m) {
#pragma unroll
    for (int o = 32; o; o >>= 1) m = max(m, (unsigned)__shfl_xor((int)m, o));
    return m;
}
__device__ __forceinline__ unsigned scale_exp(unsigned absbits) { return min(max(absbits >> 23, 15u), 254u); }
__device__ __forceinline__ unsigned pk2h(float a, float b) { unsigned r; asm("v_cvt_pk_f16_f32 %0, %1, %2" : "=v"(r) : "v"(a), "v"(b)); return r; }
__device__ __forceinline__ unsigned prm(unsigned hi, unsigned lo, unsigned sel) { return __builtin_amdgcn_perm(hi, lo, sel); }
#define BST(i)
#define BST2(i)
__device__ __forceinline__ void task_ut(const MainP& p, char* smem, int rb, int w, int lane) {
    const int tid = w * 64 + lane, g = lane >> 4, c = lane & 15;
    const u32x4* src = (const u32x4*)(p.U + (size_t)rb * 64 * DM);
    u32x4 v[18];
#pragma unroll
    for (int i = 0; i < 18; ++i) {
        v[i] = src[tid + 256 * i];
    }
    BST2(0);
#pragma unroll
    for (int i = 0; i < 18; ++i) { const int ch = tid + 256 * i, row = ch / 72; *(u32x4*)(smem + row * 1184 + (ch - 72 * row) * 16) = v[i]; }
    __syncthreads();
    const unsigned ad = (unsigned)(uintptr_t)smem + (16 * g + (c >> 2)) * 1184 + (w * 144 + 4 * (c & 3)) * 2;
#pragma unroll
    for (int i = 0; i < 9; ++i) {
        u32x2 x0, x1, x2, x3;
        asm volatile("ds_read_b64_tr_b16 %0, %4 offset:%5\n\tds_read_b64_tr_b16 %1, %4 offset:%6\n\tds_read_b64_tr_b16 %2, %4 offset:%7\n\tds_read_b64_tr_b16 %3, %4 offset:%8\n\ts_waitcnt lgkmcnt(0)"
                     : "=&v"(x0), "=&v"(x1), "=&v"(x2), "=&v"(x3) : "v"(ad), "i"(i * 32), "i"(4 * 1184 + i * 32), "i"(8 * 1184 + i * 32), "i"(12 * 1184 + i * 32) : "memory");
        v[2 * i] = u32x4{x0[0], x0[1], x1[0], x1[1]}; v[2 * i + 1] = u32x4{x2[0], x2[1], x3[0], x3[1]};
    }
    __syncthreads();
    BST2(1);
    char* tw = smem + w * (144 * 144);
#pragma unroll
    for (int i = 0; i < 9; ++i) { *(u32x4*)(tw + (16 * i + c) * 144 + g * 32) = v[2 * i]; *(u32x4*)(tw + (16 * i + c) * 144 + g * 32 + 16) = v[2 * i + 1]; }
    const char* dst = uni(p.ws + G_D_UT + (size_t)rb * 73728 + w * 18432);
#pragma unroll
    for (int i = 0; i < 18; ++i) {
        const u32x4 o = *(const u32x4*)(tw + (8 * i + (lane >> 3)) * 144 + (lane & 7) * 16);
        const unsigned vi = lane * 16 + i * 1024;
        WST16(vi, o, dst, 0);
    }
    BST2(2);
    const u32x4 sent = {0xffffffffu, 0xffffffffu, 0xffffffffu, 0xffffffffu};
#pragma unroll 1
    for (int sg = NG1; sg < 64; ++sg) {
        const unsigned ov = umx_base(rb) + sg * 9216 + tid * 16, ok = G_D_KM + rb * G_HS4 + skew(sg, rb) * 4096 + tid * 16;
        const unsigned op = tid < 128 ? G_D_PX + (sg * 32 + rb) * 2048 + tid * 16
                          : tid < 192 ? ov + 6144 : G_D_PM + sg * 16384 + ((tid >> 4) & 1) * 8192 + rb * 256 + (tid & 15) * 16;
        asm volatile("global_store_dwordx4 %0, %4, %5 sc1\n\tglobal_store_dwordx4 %1, %4, %5 sc1\n\tglobal_store_dwordx4 %2, %4, %5 sc1\n\t"
                     "global_store_dwordx4 %3, %4, %5 sc1\n\ts_nop 1" :: "v"(ov), "v"(ov + 4096), "v"(ok), "v"(op), "v"(sent), "s"(p.ws) : "memory");
    }
    BST2(3);
    VMC0();
    BST2(4);
    __syncthreads();
    if (tid == 0) __hip_atomic_store((gu32*)(p.ws + CTLO + G_C_UT + rb * 128), 1u, RLX_AGENT);
}
struct KPar { u16 ldt, pa[2][4]; };
__device__ __forceinline__ KPar kgen_par(const MainP& p, int d, int lane) {
    KPar k;
    k.ldt = p.logdt[d];
#pragma unroll
    for (int q = 0; q < 2; ++q) { const int i = d * NST + lane + 64 * q; k.pa[q][0] = p.logA[i]; k.pa[q][1] = p.Aim[i]; k.pa[q][2] = p.Cre[i]; k.pa[q][3] = p.Cim[i]; }
    return k;
}
struct Pull { const char* a; const char* b; const char* c; unsigned at; };
__device__ __forceinline__ void pull_inputs(Pull& q, int w, bool now) {
    if (q.a && (now || (int)((unsigned)__builtin_amdgcn_s_memrealtime() - q.at) >= 0)) {
#pragma unroll
        for (int i = 0; i < 3; ++i)
            if (i < 2 || w == 0) asm volatile("s_mov_b32 m0, %3\n\ts_nop 0\n\tglobal_load_lds_dword %0, %1\n\tglobal_load_lds_dword %0, %2" :: "v"(threadIdx.x * 128 + i * 32768), "s"(q.a), "s"(q.b), "s"(155648) : "memory");
        asm volatile("global_load_lds_dword %0, %1" :: "v"(min(threadIdx.x, 143u) * 128), "s"(q.c) : "memory");
        q.a = nullptr;
    }
}
__device__ __forceinline__ void task_kgen(const MainP& p, char* smem, int d, int w, int lane, const KPar& kp, bool first, unsigned ep, Pull& pl) {
    BCh& L = reinterpret_cast<BCh*>(smem)[w];
    const int g = lane >> 4, c = lane & 15, d0 = d;
    const int tid = (d & 3) * 64 + lane;
    unsigned* Kd = (unsigned*)L.uc;
    unsigned ek;
    BST(10);
    const auto& pa = kp.pa;
    const float dt = bfr(expf(bf2f(kp.ldt)));
#pragma unroll
    for (int q = 0; q < 2; ++q) {
        const float ar = -bfr(expf(bf2f(pa[q][0])));
        const float cr = bf2f(pa[q][2]) * dt, ci = bf2f(pa[q][3]) * dt;
        L.cst[lane + 64 * q] = f32x4{ar * dt * 1.4426950408889634f, bf2f(pa[q][1]) * dt * 0.15915494309189535f,
                                     __log2f(hypotf(cr, ci)), atan2f(ci, cr) * 0.15915494309189535f};
    }
    BST(11);
    {
        f32x4 acc[3];
#pragma unroll
        for (int v = 0; v < 3; ++v) acc[v] = f32x4{0.f, 0.f, 0.f, 0.f};
        auto base_of = [](int v, int r) { return v == 0 ? (r & 7) + 128 * (r >> 3) : v == 1 ? (r < 8 ? 256 + 2 * (r & 3) + 128 * (r >> 2) : 512 + 4 * (r & 1) + 128 * ((r - 8) >> 1))
                                                                                           : 1024 + 128 * min(r, 8); };
        const float b0 = (float)base_of(0, c), b1 = (float)base_of(1, c), b2 = (float)base_of(2, c), off = (float)(8 * c);
#define KG_POLAR(h_, l_, lg_, f_, a_, b_) do { const float m_ = __builtin_amdgcn_exp2f(fmaf(cc[0], b_, lg_)), p_ = __builtin_amdgcn_fractf(fmaf(a_, b_, f_)); \
            const float re_ = m_ * __builtin_amdgcn_cosf(p_), im_ = m_ * __builtin_amdgcn_sinf(p_);                                                            \
            h_ = pk2bf(re_, im_); l_ = pk2bf(re_ - lo2f(h_), im_ - hi2f(h_)); } while (0)
#pragma unroll 1
        for (int t = 0; t < NST / 16; ++t) {
            u32x4 fh, fl, ah[3], al[3];
            pull_inputs(pl, w, false);
#pragma unroll
            for (int j = 0; j < 4; ++j) {
                const f32x4 cc = L.cst[16 * t + 4 * g + j];
                KG_POLAR(fh[j], fl[j], 0.f, 0.f, -cc[1], off);
                KG_POLAR(ah[0][j], al[0][j], cc[2], cc[3], cc[1], b0); KG_POLAR(ah[1][j], al[1][j], cc[2], cc[3], cc[1], b1); KG_POLAR(ah[2][j], al[2][j], cc[2], cc[3], cc[1], b2);
            }
#pragma unroll
            for (int v = 0; v < 3; ++v) { acc[v] = MFMA32(al[v], fh, acc[v]); acc[v] = MFMA32(ah[v], fl, acc[v]); acc[v] = MFMA32(ah[v], fh, acc[v]); }
        }
        BST(12);
        unsigned mx = 0;
#pragma unroll
        for (int v = 0; v < 3; ++v)
#pragma unroll
            for (int i = 0; i < 4; ++i)
                if (base_of(v, 4 * g + i) + 8 * c <= 2048) mx = max(mx, __float_as_uint(acc[v][i]) & 0x7fffffffu);
        ek = scale_exp(wave_umax(mx));
        const float sk = __uint_as_float((268u - ek) << 23);
#pragma unroll
        for (int v = 0; v < 3; ++v)
#pragma unroll
            for (int i = 0; i < 4; ++i) {
                const int pos = base_of(v, 4 * g + i) + 8 * c;
                if (pos <= 2048) Kd[pos] = pk2h(acc[v][i] * sk, 0.f);
            }
    }
    char* kt = p.ws + G_D_KT + (size_t)d * 8192;
    char* kt2 = kt + 4096;
    const unsigned voff = lane * 16;
#pragma unroll
    for (int q = 0; q < 4; ++q) {
        const int e = lane + 64 * q;
        unsigned t[8], k[8];
        const unsigned k0 = Kd[8 * e], k8 = Kd[8 * e + 8];
        if (q == 0) {
            const u32x4 a = *(const u32x4*)&Kd[8 * e], b = *(const u32x4*)&Kd[8 * e + 4];
            const bool two = lane >= 32;
            t[0] = a[0]; t[1] = two ? a[0] : a[1]; t[2] = a[2]; t[3] = two ? b[0] : a[3]; t[4] = b[0]; t[5] = two ? b[0] : b[1]; t[6] = b[2]; t[7] = two ? k8 : b[3];
        } else if (q == 1) {
            const unsigned k4 = Kd[8 * e + 4];
            t[0] = t[1] = t[2] = k0; t[3] = t[4] = t[5] = k4; t[6] = t[7] = k8;
        } else {
            t[0] = t[1] = t[2] = t[3] = k0; t[4] = (lane & 1) ? k8 : k0; t[5] = t[6] = t[7] = k8;
        }
#pragma unroll
        for (int j = 0; j < 8; ++j) k[j] = t[7 - j];
        u32x4 kh;
#pragma unroll
        for (int j = 0; j < 4; ++j) kh[j] = prm(k[2 * j + 1], k[2 * j], 0x05040100u);
        WST16(voff, kh, kt, q * 1024);
    }
    if (lane < 8) { const u32x4 e4 = {ek, ek, ek, ek}; WST16(voff, e4, kt2, 0); }
    BST(13);
    VMC0();
    BST(14);
    if (first) init_seen(p.ws, lane, ep);
    BST(15);
    if (lane == 0) __hip_atomic_fetch_add((gu32*)(p.ws + G_WS_CTL + G_C_KG + (d >> 2) * 128), 1u, RLX_AGENT);
}
__device__ __forceinline__ void task_s4d(const MainP& p, char* smem, int d0, int w, int lane, int& nb, bool more) {
    BCh* LL = reinterpret_cast<BCh*>(smem);
    BCh& L = LL[w];
    const int tid = w * 64 + lane, g = lane >> 4, c = lane & 15, d = d0 + w;
    BST(0);
    wait_word(p, p.ws + (lane < 32 ? CTLO + G_C_UT + lane * 128 : G_WS_CTL + G_C_KG + (d0 >> 2) * 128), lane < 32 ? 1 : 3, 30u);
    const float dsk = bf2f(p.Dskip[d]);
    const char* kt = p.ws + G_D_KT + (size_t)d * 8192;
    const char* kt2 = kt + 4096;
    const char* ut = p.ws + G_D_UT + d * 128;
    const unsigned voff = lane * 16;
    u32x4 kh[4], uu[4];
    unsigned ek;
#pragma unroll
    for (int q = 0; q < 4; ++q) { const unsigned vu = (8 * q + (lane >> 3)) * 73728 + (lane & 7) * 16; WLD16(uu[q], vu, ut, 0); }
    WLD16X4(kh[0], kh[1], kh[2], kh[3], voff, kt);
    asm volatile("global_load_dword %0, %1, %2 sc1" : "=v"(ek) : "v"(0u), "s"(kt2) : "memory");
    const u32x4 z4 = {0u, 0u, 0u, 0u};
    *(u32x4*)&L.Kh[8 * lane] = z4;
    *(u32x4*)&L.Kh[512 + 8 * (lane & 31)] = z4;
    *(u32x4*)&L.uc[0][64 + SL] = z4;
    *(u32x4*)&L.uc[(lane >> 3) & 3][8 * (lane & 7)] = z4;
    asm volatile("s_waitcnt vmcnt(0)" : "+v"(kh[0]), "+v"(kh[1]), "+v"(kh[2]), "+v"(kh[3]), "+v"(ek),
                 "+v"(uu[0]), "+v"(uu[1]), "+v"(uu[2]), "+v"(uu[3]) :: "memory");
    BST(1);
    unsigned mu = 0;
#pragma unroll
    for (int q = 0; q < 4; ++q)
#pragma unroll
        for (int j = 0; j < 4; ++j) mu = max(mu, max(uu[q][j] & 0x7fff0000u, (uu[q][j] << 16) & 0x7fff0000u));
    const unsigned eu = scale_exp(wave_umax(mu));
    const float su = __uint_as_float((268u - eu) << 23), iu = __uint_as_float((eu - 14u) << 23), ik = __uint_as_float((ek - 13u) << 23);
#pragma unroll
    for (int q = 0; q < 4; ++q) {
        const int e = lane + 64 * q, at = (16 + (e >> 2)) * KROW + 24 - 8 * (e & 3);
        u32x4 h;
#pragma unroll
        for (int j = 0; j < 4; ++j) h[j] = pk2h(lo2f(uu[q][j]) * su, hi2f(uu[q][j]) * su);
        *(u32x4*)&L.uc[0][64 + 8 * e] = h;
        *(u32x4*)&L.ub[8 * e] = uu[q];
        *(u32x4*)&L.Kh[at] = kh[q];
    }
    BST(2);
    BST(3);
#pragma unroll
    for (int q = 0; q < 4; ++q) {
        const int e = lane + 64 * q;
        const int ue = 56 + 8 * e;
        const u32x4 a = *(const u32x4*)&L.uc[0][ue];
        const u32x2 b = *(const u32x2*)&L.uc[0][ue + 8];
        const unsigned s0 = __builtin_amdgcn_alignbit(a[1], a[0], 16), s1 = __builtin_amdgcn_alignbit(a[2], a[1], 16), s2 = __builtin_amdgcn_alignbit(a[3], a[2], 16),
                       s3 = __builtin_amdgcn_alignbit(b[0], a[3], 16), s4 = __builtin_amdgcn_alignbit(b[1], b[0], 16);
        *(u32x4*)&L.uc[1][ue] = u32x4{s0, s1, s2, s3};
        *(u32x4*)&L.uc[2][ue] = u32x4{a[1], a[2], a[3], b[0]};
        *(u32x4*)&L.uc[3][ue] = u32x4{s1, s2, s3, s4};
    }
    if (lane == 0) {
        const u32x4 a = *(const u32x4*)&L.uc[0][56 + SL];
        const unsigned s0 = __builtin_amdgcn_alignbit(a[1], a[0], 16), s1 = __builtin_amdgcn_alignbit(a[2], a[1], 16), s2 = __builtin_amdgcn_alignbit(a[3], a[2], 16), s3 = a[3] >> 16;
        *(u32x4*)&L.uc[1][56 + SL] = u32x4{s0, s1, s2, s3};
        *(u32x4*)&L.uc[2][56 + SL] = u32x4{a[1], a[2], a[3], 0u};
        *(u32x4*)&L.uc[3][56 + SL] = u32x4{s1, s2, s3, 0u};
    }
    BST(4);
    {
        const int o = 33 + c + 8 * g;
        const unsigned ua = (unsigned)(uintptr_t)&L.uc[o & 3][o & ~3], ka = (unsigned)(uintptr_t)&L.Kh[(16 + c) * KROW + 8 * g];
        const f32x4* res = (const f32x4*)L.Kh + lane;
        asm volatile(ASM_CV :: "v"(ua), "v"(ka), "v"((unsigned)(uintptr_t)res) : CV_CLOB);
        BST(5);
#pragma unroll
        for (int par = 0; par < 2; ++par)
#pragma unroll
            for (int t = 0; t < 4; ++t) {
                const int n = 16 * (par + 2 * (16 * t + c)) + 4 * g;
                const u32x2 uu = *(const u32x2*)&L.ub[n];
                const f32x4 y4 = res[(par * 4 + t) * 64] * fminf(ik, iu) * fmaxf(ik, iu);
                const float r0 = bfr(y4[0]) + bfr(lo2f(uu[0]) * dsk), r1 = bfr(y4[1]) + bfr(hi2f(uu[0]) * dsk);
                const float r2 = bfr(y4[2]) + bfr(lo2f(uu[1]) * dsk), r3 = bfr(y4[3]) + bfr(hi2f(uu[1]) * dsk);
                *(u32x2*)&L.uc[1][n] = u32x2{pk2bf(r0, r1), pk2bf(r2, r3)};
            }
    }
    __syncthreads();
    BST(6);
    u32x4 y[4];
#pragma unroll
    for (int ch = 0; ch < 4; ++ch) y[ch] = *(const u32x4*)&LL[ch].uc[1][8 * tid];
    const unsigned vt = tid * 16;
    char* yb = p.ws + G_D_YB + (size_t)(d0 >> 2) * 16384;
#pragma unroll
    for (int i = 0; i < 4; ++i) {
        const u32x4 o = {prm(y[1][i], y[0][i], 0x05040100u), prm(y[3][i], y[2][i], 0x05040100u), prm(y[1][i], y[0][i], 0x07060302u), prm(y[3][i], y[2][i], 0x07060302u)};
        char* yi = yb + i * 4096;
        WST16(vt, o, yi, 0);
    }
    BST(7);
    if (tid == 0 && more) nb = (int)(__hip_atomic_fetch_add((gu32*)(p.ws + CTLO + G_C_TASK + 128), 1u, RLX_AGENT) + 1u);
    VMC0();
    __syncthreads();
    if (tid == 0) __hip_atomic_fetch_add((gu32*)(p.ws + CTLO + G_C_YC + (d0 >> 6) * 128), 1u, RLX_AGENT);
    BST(8);
}
__device__ __forceinline__ void task_merge(const MainP& p, char* smem, int d0, int i, int w, int lane) {
    const unsigned vt = (w * 64 + lane) * 16;
    wait_word(p, p.ws + CTLO + G_C_YC + (d0 >> 6) * 128, 15, 31u);
    {
        const char* sb = p.ws + G_D_YB + (size_t)(d0 >> 6) * 262144 + i * 4096;
        u32x4 z[16];
#pragma unroll
        for (int j = 0; j < 16; ++j) { const char* sj = sb + j * 16384; WLD16(z[j], vt, sj, 0); }
        asm volatile("s_waitcnt vmcnt(0)" : "+v"(z[0]), "+v"(z[1]), "+v"(z[2]), "+v"(z[3]), "+v"(z[4]), "+v"(z[5]), "+v"(z[6]), "+v"(z[7]),
                     "+v"(z[8]), "+v"(z[9]), "+v"(z[10]), "+v"(z[11]), "+v"(z[12]), "+v"(z[13]), "+v"(z[14]), "+v"(z[15]) :: "memory");
        char* tw = smem + w * 18432;
#pragma unroll
        for (int j = 0; j < 8; ++j) {
            *(u32x4*)(tw + lane * 144 + j * 16) = u32x4{z[2 * j][0], z[2 * j][1], z[2 * j + 1][0], z[2 * j + 1][1]};
            *(u32x4*)(tw + (64 + lane) * 144 + j * 16) = u32x4{z[2 * j][2], z[2 * j][3], z[2 * j + 1][2], z[2 * j + 1][3]};
        }
#pragma unroll
        for (int k = 0; k < 16; ++k) {
            const int q = 8 * k + (lane >> 3);
            const int row = 8 * (64 * w + (q & 63)) + 2 * i + (q >> 6);
            *(u32x4*)(p.out + (size_t)row * OUTW + DM + (d0 & ~63) + 8 * (lane & 7)) = *(const u32x4*)(tw + q * 144 + (lane & 7) * 16);
        }
    }
}

static constexpr int NKG = 144, E_B = NKG + 36;
static constexpr int Q1 = 0, Q2 = Q1 + (NFINE - 2) * 25;
static constexpr int NTASK = E_B + Q2 + 68;

__device__ __forceinline__ void worker_body(const MainP& p, char* smem, const int w, const int lane, int quota) {
    const int tid = w * 64 + lane;
    int* slot = (int*)(smem + LDS_TOTAL - 16);
    int nb = -1;
    int nst = 0;
    gu32* qa = (gu32*)(p.ws + CTLO + G_C_TASK);
    for (;;) {
        if (tid == 0) {
            int t = E_B;
            if (quota > 0) {
                const int k = nb >= 0 ? nb : (int)(__hip_atomic_fetch_add(qa + 32, 1u, RLX_AGENT) + 1u);
                if (k < E_B) t = k;
                nb = -1;
            }
            if (t >= E_B) t = E_B + (int)(__hip_atomic_fetch_add(qa, 1u, RLX_AGENT) + 1u);
            *slot = t;
        }
        __syncthreads();
        const int t = __builtin_amdgcn_readfirstlane(*slot);
        if (t >= NTASK) break;
        quota = t < E_B ? quota - 1 : 0;
        if (t < E_B) {
            if (t < 128 || (t >= 144 && t < 160)) task_s4d(p, smem, 4 * (t < 128 ? t : t - 16), w, lane, nb, quota > 0);
            else task_merge(p, smem, 64 * ((t < 144 ? t - 128 : t - 144) >> 2), t & 3, w, lane);
        } else {
            const int u = t - E_B;
            const bool last2 = u >= Q2;
            const int sg = last2 ? 62 + (u - Q2) / 34 : NG1 + (u - Q1) / 25, r = last2 ? (u - Q2) % 34 : (u - Q1) % 25;
            if (r < 16) task_attn_tail(p, sg, 2 * r + (w >> 1), w, lane);
            else if (last2) task_out<2, 9, true>(p, smem, sg, 2 * (r - 16), w, lane);
            else task_out<4, 9, true>(p, smem, sg, 4 * (r - 16), w, lane);
            if (last2) break;
        }
        __syncthreads();
    }
}

__host__ __device__ constexpr unsigned phys(unsigned x) { return (x >> 13) * 32768 + (x & 8191); }
__host__ __device__ constexpr unsigned cbase(int c) { return (c >> 1) * 131072 + (c & 1) * 16384; }
__host__ __device__ constexpr unsigned hbase(int c) { return G_HOFF + (c >> 1) * 32768 + (c & 1) * 16384; }
__host__ __device__ constexpr unsigned aMB(int f, int b) { return phys(f < 8 ? 16384 + f * 1024 + b : 28672 + b); }
__host__ __device__ constexpr unsigned aPX(int pr, int b) { return phys(pr * 1024 + b); }
__host__ __device__ constexpr unsigned aPM(int u, int b) { return phys(24576 + u * 1024 + b); }
#define IST(k)

static constexpr int L_RW = 160000;
__device__ __forceinline__ int lane_id() { int l; asm volatile("v_mbcnt_lo_u32_b32 %0, -1, 0\n\tv_mbcnt_hi_u32_b32 %0, -1, %0" : "=v"(l)); return l; }
typedef const __attribute__((address_space(4))) MainP* KArg;

__global__ __launch_bounds__(256, 1) void main_kernel(MainP p_) {
    __shared__ __attribute__((aligned(16))) char smem[LDS_TOTAL];
    KArg ka = (KArg)__builtin_amdgcn_kernarg_segment_ptr();
    static_assert(offsetof(MainP, W) == 0 && offsetof(MainP, ws) == 0x20 && offsetof(MainP, X) == 0x28 && offsetof(MainP, prm) == 0x40, "argument layout");
    asm volatile(ASM_EA :: "s"(ka), "s"(blockIdx.x), "v"(threadIdx.x) : ASM_CLOB);
    asm volatile("" : "+s"(ka));
    const unsigned t00 = (unsigned)__builtin_amdgcn_s_memrealtime();
    const int b = blockIdx.x, x = b & 7, ix = b >> 3;
    const int w = __builtin_amdgcn_readfirstlane(threadIdx.x >> 6);
    const bool isr = x < 4 && ix >= 16;
    if (ix == (x < 4 ? 24 : 31) && (FLG(ka->prm[5]) & 8)) {
        const unsigned t = threadIdx.x;
        const char* pg = t < (G_D_END >> 21) ? ka->ws + (size_t)t * 0x200000 + 0x1FFF80 : t < 128 ? ka->ws : (const char*)(t < 192 ? ka->out : t < 224 ? ka->X : ka->U) + min((t & 31) * 0x100000u, t < 192 ? 4718588u : 2359292u);
        asm volatile("s_mov_b32 m0, %1\n\ts_nop 0\n\tglobal_load_lds_dword %0, off" :: "v"(pg), "s"(155648) : "memory");
    }
    if (x < 4 && ix >= 29) init_cal(ka->ws, smem, x * 3 + ix - 29, threadIdx.x, min((FLG(ka->prm[5]) >> 16) & 7, 5));
    if (x >= 4 ? ix >= 24 : (ix >= 25 && ix < 29)) init_fill(ka->ws, ka->mem0, x >= 4 ? (ix - 24) * 4 + (x & 3) : 32 + (ix - 25) * 4 + x, threadIdx.x);
    IST(0);
    const int NS = (FLG(ka->prm[5]) >> 8) & 3, NF = (FLG(ka->prm[5]) >> 21) & 7, RK = (FLG(ka->prm[5]) >> 10) & 3;
    const int NSP = G_RARM ? 7 : FLG2(ka->prm[7]) & 7;
    const int NA1 = 18 + 4 * NSP, NB1 = NA1 - 3 * max(NSP - 4, 0), N1 = RK ? NA1 + (RK - 1) * NB1 : 0, N2 = N1 + (28 - 4 * NSP) * NS, NKE = min(N2 + max(24 * NF - 8, 0), NKG);
    if (ix >= (x < 4 ? 16 : 24)) {
        MainP p;
        p.ws = ka->ws; p.logA = ka->logA; p.Aim = ka->Aim; p.Cre = ka->Cre; p.Cim = ka->Cim; p.logdt = ka->logdt;
        Pull pl = {nullptr, nullptr, nullptr, t00 + 400 + 50 * ((FLG(ka->prm[5]) >> 4) & 15)};
        const bool own = x < 4 && ix < 25 + NSP;
        int d0, nr, dd;
        if (own) {
            const int jt = ix < 25 ? ix - 16 : ix - 25, solo = jt < NSP;
            const int k = ix >= 25 ? 18 + NSP + 3 * jt + w - 1 : solo ? 3 * jt + w - 1 : NSP + 2 * jt + w - 2;
            d0 = k < NB1 ? x * NB1 + k : 4 * NB1 * RK + x * (NA1 - NB1) + k - NB1; nr = w < 2 - solo ? 0 : k < NB1 ? RK : min(RK, 1); dd = 4 * NB1;
            if (threadIdx.x < 8) ((unsigned*)(smem + L_RW))[threadIdx.x] = 0xffffffffu;
        } else if (x < 4) { d0 = 4 * (N1 + (ix - 25 - NSP) * 4 + x) + w; nr = NS; dd = 4 * (28 - 4 * NSP); }
        else {
            d0 = 4 * (N2 + (ix - 24) * 4 + (x & 3)) + w; nr = ix < 28 ? NF : ix < 30 ? max(NF - 1, 0) : 0; dd = 96;
            const size_t o = (size_t)((ix - 24) * 4 + (x & 3)) * 73728; pl.a = (const char*)ka->X + o; pl.b = (const char*)ka->U + o;
            pl.c = (const char*)((x & 2) ? ((x & 1) ? ka->Cim : ka->Cre) : ((x & 1) ? ka->Aim : ka->logA)) + (ix - 24) * 18432;
        }
        while (nr > 0 && d0 + dd * (nr - 1) >= 4 * NKG) --nr;
        KPar kn;
        if (nr > 0) kn = kgen_par(p, d0, threadIdx.x & 63);
        __builtin_amdgcn_sched_barrier(0);
        const unsigned ep = __hip_atomic_load((const gu32*)(p.ws + G_WS_EP), RLX_AGENT);
        asm volatile("s_waitcnt vmcnt(0)" :: "v"(ep) : "memory");
        __syncthreads();
        if (x >= 4 && ix == 24) {
            const char* xs = (const char*)ka->X + threadIdx.x * 128;
#pragma unroll
            for (int i = 0; i < 7; ++i) asm volatile("s_mov_b32 m0, %1\n\ts_nop 0\n\tglobal_load_lds_dword %0, off" :: "v"(xs + i * 32768), "s"(155648) : "memory");
        }
        {
            const int q = x < 4 ? ix - 25 : ix - 26;
            if (q >= 0 && q < 4 && (FLG(ka->prm[5]) & 1)) {
                const int hs = (x & 3) * 8 + 4 * (q & 1) + ((1 - (int)((u64)ka->W[q & 2] >> 8)) & 3);
                const char* src = (q & 2) ? (const char*)ka->W[3] + (size_t)hs * 147456 + threadIdx.x * 128 : (const char*)ka->W[x < 4] + hs * 256 + (threadIdx.x >> 1) * 8192 + (threadIdx.x & 1) * 128;
                const unsigned st = (q & 2) ? 32768u : 1048576u;
#pragma unroll
                for (int i = 0; i < 5; ++i)
                    if (i < 4 || w < 2) {
                        if (FLG(ka->prm[5]) & 4) asm volatile("s_mov_b32 m0, %1\n\ts_nop 0\n\tglobal_load_lds_dword %0, off nt" :: "v"(src + (size_t)i * st), "s"(155648) : "memory");
                        else asm volatile("s_mov_b32 m0, %1\n\ts_nop 0\n\tglobal_load_lds_dword %0, off" :: "v"(src + (size_t)i * st), "s"(155648) : "memory");
                    }
            }
        }
        if (nr == 0) init_seen(p.ws, threadIdx.x & 63, ep);
#pragma unroll 1
        for (int r = 0; r < nr; ++r) {
            const KPar kp = kn;
            kn = kgen_par(p, d0 + dd * min(r + 1, nr - 1), threadIdx.x & 63);
            __builtin_amdgcn_sched_barrier(0);
            task_kgen(p, smem, d0 + dd * r, w, threadIdx.x & 63, kp, r == 0, ep, pl);
        }
        if (w == 0) init_count(p.ws, threadIdx.x & 63);
        while (pl.a) { pull_inputs(pl, w, false); __builtin_amdgcn_s_sleep(8); }
        if (x < 4 && !own) return;
        asm volatile("" : "+s"(ka));
    }
    if (isr) {
        const int lane = threadIdx.x & 63, g = lane >> 4, c = lane & 15;
        const int jt = ix < 25 ? ix - 16 : ix - 25, solo = jt < NSP, par = solo ? ix >= 25 : w;
        const unsigned lb = w * G_STG;
        unsigned* ct = (unsigned*)(smem + lb) + lane;
        asm volatile("" :: "v"(ct) : "memory");
#define CTW(k, val) ct[(k) * 64] = (unsigned)(val)
        const u64 ws = (u64)ka->ws, prm = (u64)ka + 0x40;
        const u64 stb = 0;
        IST(1);
        CTW(0, lane * 16);
        const int j = x * 9 + jt;
        const int rot = (jt * 16) / 9;
        if (w >= 2 - solo) return;
        CTW(1, lane * 16 + 4096);
        CTW(2, aMB(j >> 2, (j & 3) * 256 + (4 * g + 2 * ((c >> 1) & 1) + (c & 1)) * 16 + (c >> 2) * 4));
        for (int k = 0; k < (int)G_NARM; ++k) {
            constexpr int N0 = G_HKB, N1 = N0 + (9 * G_RM + 3) / 4, N2 = N1 + 5 * G_RP;
            constexpr int NO = G_RARM ? 7 : 9;
            static_assert(N2 <= NO * G_NARM && G_NARM <= 5, "shares");
            const int q = min(jt + NO * k, N2 - 1), qm = min(x * (N1 - N0) + q - N0, (int)(9 * G_RM) - 1), qp = x * 5 * G_RP + q - N1;
            const int cp = q < N0 ? 4 + x : q < N1 ? qm / 9 : qp / 20, kb = q < N0 ? q : q < N1 ? (qm % 9 == 8 ? 28 : 16 + qm % 9) : (qp % 20 < 16 ? qp % 20 : 8 + qp % 20);
            CTW(47 + k, (cp >= 4 ? hbase(x) : cbase(cp)) + phys(kb * 1024 + lane * 16));
        }
        for (int k = 0; k < 3; ++k) { const int q = min(jt + 7 * k, 15); CTW(52 + k, (x * 9 + 7 + (q >> 3)) * 16384 + (q & 7) * 1024 + lane * 16); }
        for (int k = 0; k < 2; ++k) { const int L = min(jt * 74 + min(k * 64 + lane, 73), 511), tl = x * 9 + 7 + (L >> 8); CTW(55 + k, G_D_UX + tl * G_UXS + skew(0, tl) * 32768 + (L & 255) * 128); }
        const unsigned pc = 0;
        CTW(3, hbase(lane & 3) + G_RHO + ((lane >> 2) & 1) * G_RHS + j * 32); CTW(46, hbase(x & 2) + G_RHO + min(lane, 35) * 32);
        CTW(4, (c & 2) ? 0xEEEE : 0x4444); CTW(5, (c & 1) ? 0x03020706u : 0x05040100u);
        CTW(6, G_GVC ? G_WS_CTL + G_C_GV + G_GVH + x * 2048 + (par * 32 + (lane & 31)) * 32 : G_WS_CTL + G_C_GV + (par * 32 + (lane & 31)) * 128); CTW(7, hbase(x & 2) + (lane & 31) * G_HP); CTW(8, lane * 4); CTW(9, L_RW);
        for (int k = 0; k < 16; ++k) { const int i = (k + rot) & 15; CTW(10 + k, pc + aPX(i, lane * 16)); CTW(26 + k, i * 2048 + lane * 16); }
        for (int k = 0; k < 4; ++k) CTW(42 + k, pc + aPM(k, lane * 16));
        IST(2);
        asm volatile(ASM_R :: "s"(ws), "s"(ws), "s"((u64)(t00 << 4)), "s"(__builtin_amdgcn_readfirstlane(par)), "s"(__builtin_amdgcn_readfirstlane(j)), "s"((u64)__builtin_amdgcn_readfirstlane(2 * rot) + ((u64)__builtin_amdgcn_readfirstlane(solo) << 32)), "s"(prm), "s"(lb), "s"(stb) : ASM_CLOB);
    }
    asm volatile("" : "+s"(ka));
    MainP p;
    p.prm[5] = ka->prm[5];
    p.U = ka->U; p.ws = ka->ws; p.logA = ka->logA; p.Aim = ka->Aim; p.Cre = ka->Cre; p.Cim = ka->Cim; p.logdt = ka->logdt; p.Dskip = ka->Dskip; p.out = ka->out;
    {
        auto cal = [&](int k) { return __hip_atomic_load((const gu32*)(p.ws + G_WS_DONE + k * 64 + 4), RLX_AGENT); };
        p.prm[0] = __builtin_amdgcn_readfirstlane((min(cal(5), cal(11)) < min(cal(4), cal(10)) ? 8192 : 0) ^ G_CTF);
    }
    const bool s4 = ix < 22 - ((FLG(p.prm[5]) >> 12) & 15);
    if (s4 && ix >= 8 && ix < 24) for (int i = 0; i < (int)(((ix & 7) * 4 + (x & 3)) * ((FLG(p.prm[5]) >> 24) & 3)); ++i) __builtin_amdgcn_s_sleep(3);
    if (x >= 4 && ix >= 8 && ix < 22) {
#pragma unroll 1
        for (int q = NKE + (ix - 8) * 4 + (x & 3); q < NKG; q += 56) {
            Pull pl = {nullptr, nullptr, nullptr, 0u};
            task_kgen(p, smem, 4 * q + w, w, lane_id(), kgen_par(p, 4 * q + w, lane_id()), false, 0u, pl);
        }
    }
    if (ix >= 24 && ix < 30) {
#pragma unroll 1
        for (int rb = (ix - 24) * 4 + (x & 3); rb < 32; rb += 8) {
            __syncthreads();
            task_ut(p, smem, rb, w, lane_id());
            if (ix < 28) break;
        }
        __syncthreads();
        const int lane = lane_id(), g = lane >> 4, c = lane & 15, ct0 = 6 * (ix - 24), s0 = x & 3;
        const unsigned lb = w * G_STG;
        unsigned* ct = (unsigned*)(smem + lb) + lane;
        asm volatile("" :: "v"(ct) : "memory");
        for (int t = 0; t < 6; ++t) CTW(t, (ct0 + t) * G_UXS + skew(0, ct0 + t) * 32768 + w * 8192 + 4096 + lane * 16);
        CTW(6, w * 16384 + 4096 + lane * 16); CTW(7, w * 16384 + 12288 + lane * 16); CTW(8, w * 2048 + lane * 16); CTW(9, w * 2048 + 8192 + lane * 16);
        CTW(10, umx_base(w * 8 + g) + ct0 * 256 + c * 16); CTW(11, umx_base(w * 8 + 4 + g) + ct0 * 256 + c * 16);
        const unsigned fa = lane < 8 ? CTLO + G_C_UM + (w * 8 + lane) * 128 : CTLO + G_C_GV + ((s0 & 1) * 32 + w * 8 + (lane & 7)) * 128;
        CTW(12, lane < 16 ? fa : attn_flag(p, s0, w * 2 + (lane & 1))); CTW(13, lane < 16 ? fa : attn_flag(p, s0 + 60, w * 2 + (lane & 1)));
        CTW(14, w * 12288 + lane * 16); CTW(15, w * 1024 + lane * 16);
        CTW(16, (16 * (w & 1) + c) * (OUTW * 2) + (16 * ct0 + 16 * (w >> 1) + 4 * g) * 2); CTW(17, lane * 4);
        static_assert(NG1 == 58, "as in the generated block");
        const u64 ws = (u64)p.ws;
        const u64 stb = 0;
        asm volatile(ASM_O :: "s"(ws), "s"((u64)p.out), "s"(ws), "s"(w), "s"(s0), "s"(ws), "s"(ws), "s"(lb), "s"(stb) : ASM_CLOB);
        asm volatile("" : "+s"(ka));
        p.prm[5] = ka->prm[5];
        p.U = ka->U; p.ws = ka->ws; p.logA = ka->logA; p.Aim = ka->Aim; p.Cre = ka->Cre; p.Cim = ka->Cim; p.logdt = ka->logdt; p.Dskip = ka->Dskip; p.out = ka->out;
    }
    if ((ix & 22) == 22 && x >= 4) { attn_role(p, ix >= 30 ? 0 : NA + 1, (x & 3) * 8 + (ix & 1) * 4 + w, w, lane_id());
        if (b == 255) {
            wait_word(p, p.ws + G_WS_CTL + G_C_ARR, 255, 50u);
            if (w == 0 && lane_id() == 0) __hip_atomic_fetch_add((gu32*)(p.ws + G_WS_EP), 1u, RLX_AGENT);
        }
        __syncthreads();
    }
    worker_body(p, smem, w, lane_id(), s4 ? 1000 : 0);
}

extern "C" __global__ void code_end_marker() {}

static constexpr int g_prm[8] = {PRM_DEFAULT};

extern "C" void kernel_launch(void* const* d_in, const int* in_sizes, int n_in, void* d_out, int out_size,
                              void* d_ws, size_t ws_size, hipStream_t stream) {
    char* ws = (char*)(((uintptr_t)d_ws + 0x1FFFFF) & ~(uintptr_t)0x1FFFFF);
    MainP p;
    for (int i = 0; i < 8; ++i) p.prm[i] = g_prm[i];
    p.mem0 = (const u16*)d_in[1];
    for (int i = 0; i < 4; ++i) p.W[i] = (const u16*)d_in[2 + i];
    p.X = (const u16*)d_in[0]; p.U = (const u16*)d_in[7]; p.ws = ws;
    p.logA = (const u16*)d_in[8]; p.Aim = (const u16*)d_in[9]; p.Cre = (const u16*)d_in[10]; p.Cim = (const u16*)d_in[11];
    p.logdt = (const u16*)d_in[12]; p.Dskip = (const u16*)d_in[13]; p.out = (u16*)d_out;
    main_kernel<<<256, 256, 0, stream>>>(p);
}
```

```cpp
#include <hip/hip_runtime.h>
#include <hip/hip_bf16.h>
#include <stdint.h>
#include <stddef.h>

typedef __attribute__((ext_vector_type(8))) __bf16 bf16x8;
typedef __attribute__((ext_vector_type(4))) short s16x4;
typedef __attribute__((ext_vector_type(4))) float f32x4;
typedef __attribute__((ext_vector_type(4))) unsigned u32x4;
typedef __attribute__((ext_vector_type(2))) unsigned u32x2;
typedef __attribute__((ext_vector_type(2))) float f32x2;
typedef unsigned short u16;
typedef unsigned long long u64;

#define GAS __attribute__((address_space(1)))
typedef GAS unsigned gu32;
#define RLX_AGENT __ATOMIC_RELAXED, __HIP_MEMORY_SCOPE_AGENT

static constexpr int DM = 576, NH = 32, DH = 128, MM = 8, SEG = 32, NSEG = 64, SL = 2048;
static constexpr int HD = NH * DH;
static constexpr int OUTW = 2 * DM;
static constexpr int NST = 128;

__device__ __forceinline__ float bf2f(u16 h) { return __uint_as_float(((unsigned)h) << 16); }
typedef __attribute__((ext_vector_type(2))) __bf16 bf16x2_t;
__device__ __forceinline__ unsigned pk2bf(float a, float b) {
    bf16x2_t v; v[0] = (__bf16)a; v[1] = (__bf16)b;
    return __builtin_bit_cast(unsigned, v);
}
__device__ __forceinline__ float bfr(float f) { return __uint_as_float(pk2bf(f, f) << 16); }
__device__ __forceinline__ float lo2f(unsigned u) { return __uint_as_float(u << 16); }
__device__ __forceinline__ float hi2f(unsigned u) { return __uint_as_float(u & 0xffff0000u); }
__device__ __forceinline__ float rowmax4(float x) {
    auto a = __builtin_amdgcn_permlane16_swap(__float_as_uint(x), __float_as_uint(x), false, false);
    x = fmaxf(__uint_as_float(a[0]), __uint_as_float(a[1]));
    auto b = __builtin_amdgcn_permlane32_swap(__float_as_uint(x), __float_as_uint(x), false, false);
    return fmaxf(__uint_as_float(b[0]), __uint_as_float(b[1]));
}
__device__ __forceinline__ float rowsum4(float x) {
    auto a = __builtin_amdgcn_permlane16_swap(__float_as_uint(x), __float_as_uint(x), false, false);
    x = __uint_as_float(a[0]) + __uint_as_float(a[1]);
    auto b = __builtin_amdgcn_permlane32_swap(__float_as_uint(x), __float_as_uint(x), false, false);
    return __uint_as_float(b[0]) + __uint_as_float(b[1]);
}

struct MainP {
    const u16* W[4];
    char* ws; const u16* X; const u16* mem0; const u16* U;
    int prm[8];
    const u16* logA; const u16* Aim; const u16* Cre; const u16* Cim; const u16* logdt; const u16* Dskip;
    u16* out;
};
static constexpr int LDS_TOTAL = 163840;
static constexpr unsigned kWaitCap = 1u << 18;
#define VMC0() asm volatile("s_waitcnt vmcnt(0)" ::: "memory")
#define PRM_DEFAULT 1050, 600, 118, 665, 760, 1615006499, 15728816, 3143
#define ASM_EA \
    "s_load_dwordx8 s[40:47], %0, 0\n\t" \
    "s_load_dwordx4 s[36:39], %0, 0x20\n\t" \
    "s_load_dwordx8 s[84:91], %0, 0x40\n\t" \
    "s_getpc_b64 s[54:55]\n\t" \
    "ea_pc_%=:\n\t" \
    "s_bfe_u32 s69, %1, 0x20003\n\t" \
    "s_lshl_b32 s69, s69, 15\n\t" \
    "v_lshl_add_u32 v1, %2, 7, s69\n\t" \
    "v_min_u32 v1, code_end_marker-ea_pc_%=, v1\n\t" \
    "s_mov_b32 m0, 155648\n\t" \
    "v_readfirstlane_b32 s66, %2\n\t" \
    "global_load_lds_dword v1, s[54:55]\n\t" \
    "s_bitcmp1_b32 %1, 2\n\t" \
    "s_cselect_b32 s69, 24, 16\n\t" \
    "s_lshr_b32 s70, %1, 3\n\t" \
    "s_cmp_ge_u32 s70, s69\n\t" \
    "s_cbranch_scc1 ea_done_%=\n\t" \
    "s_lshr_b32 s66, s66, 6\n\t" \
    "s_and_b32 s69, %1, 0xc4\n\t" \
    "s_cmp_eq_u32 s69, 4\n\t" \
    "s_cselect_b32 s69, 72, 36\n\t" \
    "s_add_u32 s82, s66, s69\n\t" \
    "s_lshl_b32 s82, s82, 10\n\t" \
    "s_mov_b32 m0, s82\n\t" \
    "s_and_b32 s67, %1, 3\n\t" \
    "s_lshl_b32 s67, s67, 3\n\t" \
    "s_and_b32 s69, s70, 7\n\t" \
    "s_add_u32 s67, s67, s69\n\t" \
    "s_lshl_b32 s69, s67, 8\n\t" \
    "v_lshrrev_b32 v1, 4, %2\n\t" \
    "v_lshrrev_b32 v2, 3, %2\n\t" \
    "v_and_b32 v2, 6, v2\n\t" \
    "v_and_or_b32 v2, v1, 8, v2\n\t" \
    "v_xor_b32 v2, v2, %2\n\t" \
    "v_and_b32 v2, 15, v2\n\t" \
    "v_lshl_add_u32 v2, v2, 4, s69\n\t" \
    "v_lshl_add_u32 v200, v1, 13, v2\n\t" \
    "s_mov_b32 s71, 16\n\t" \
    "s_bitcmp1_b32 %1, 2\n\t" \
    "s_waitcnt lgkmcnt(0)\n\t" \
    "s_cselect_b64 s[48:49], s[42:43], s[40:41]\n\t" \
    "s_cselect_b64 s[40:41], s[40:41], s[42:43]\n\t" \
    "s_cmp_ge_u32 s70, 8\n\t" \
    "s_cselect_b64 s[42:43], s[44:45], s[48:49]\n\t" \
    "s_cselect_b64 s[44:45], s[46:47], s[40:41]\n\t" \
    "ea_lp1_%=:\n\t" \
    "global_load_lds_dwordx4 v200, s[42:43]\n\t" \
    "v_add_u32 v200, 131072, v200\n\t" \
    "s_add_u32 m0, m0, 4096\n\t" \
    "s_sub_u32 s71, s71, 1\n\t" \
    "s_cmp_lg_u32 s71, 0\n\t" \
    "s_cbranch_scc1 ea_lp1_%=\n\t" \
    "s_mov_b64 s[50:51], s[38:39]\n\t" \
    "s_cmp_lt_u32 s70, 8\n\t" \
    "s_cbranch_scc1 ea_w_%=\n\t" \
    "s_mul_i32 s69, s67, 147456\n\t" \
    "s_add_u32 s44, s44, s69\n\t" \
    "s_addc_u32 s45, s45, 0\n\t" \
    "s_and_b32 s69, s70, 16\n\t" \
    "s_lshl_b32 s69, s69, 1\n\t" \
    "s_add_u32 s67, s67, s69\n\t" \
    "s_bitcmp1_b32 %1, 2\n\t" \
    "s_cbranch_scc1 gv_begin_%=\n\t" \
    "s_branch vv_begin_%=\n\t" \
    "ea_w_%=:\n\t" \
    "s_bitcmp1_b32 %1, 2\n\t" \
    "s_cbranch_scc1 gx_begin_%=\n\t" \
    "qk_begin_%=:\n\t" \
    "v_mbcnt_lo_u32_b32 v223, -1, 0\n\t" \
    "v_mbcnt_hi_u32_b32 v223, -1, v223\n\t" \
    "s_mov_b32 s68, 262144\n\t" \
    "s_mov_b32 s72, 0\n\t" \
    "s_mov_b32 s83, -1\n\t" \
    "s_mov_b32 s65, 0\n\t" \
    "v_and_b32 v180, 3, v223\n\t" \
    "v_lshlrev_b32 v202, 3, v180\n\t" \
    "v_lshrrev_b32 v181, 5, v223\n\t" \
    "v_lshl_add_u32 v202, v181, 12, v202\n\t" \
    "v_bfe_u32 v182, v223, 2, 2\n\t" \
    "s_mov_b32 s70, 288\n\t" \
    "v_mad_u32_u24 v202, v182, s70, v202\n\t" \
    "v_bfe_u32 v183, v223, 4, 1\n\t" \
    "s_mov_b32 s70, 2176\n\t" \
    "v_mad_u32_u24 v202, v183, s70, v202\n\t" \
    "s_lshl_b32 s69, s66, 6\n\t" \
    "v_xor_b32 v202, s69, v202\n\t" \
    "v_xor_b32 v203, 32, v202\n\t" \
    "v_lshlrev_b32 v0, 4, v223\n\t" \
    "s_mul_i32 s69, s66, 2048\n\t" \
    "s_bfe_u32 s71, s67, 0x10004\n\t" \
    "s_mul_i32 s71, s71, 131072\n\t" \
    "s_add_u32 s69, s69, s71\n\t" \
    "s_add_u32 s69, s69, 65536\n\t" \
    "v_lshl_add_u32 v1, v223, 4, s69\n\t" \
    "s_bfe_u32 s71, s67, 0x10004\n\t" \
    "s_mul_i32 s69, s71, 131072\n\t" \
    "s_add_u32 s69, s69, 102400\n\t" \
    "v_lshl_add_u32 v2, v223, 4, s69\n\t" \
    "v_xor_b32 v34, 131072, v1\n\t" \
    "s_mov_b32 s69, 17476\n\t" \
    "v_bfe_u32 v184, v223, 1, 1\n\t" \
    "v_mov_b32 v10, s69\n\t" \
    "s_mov_b32 s70, 43690\n\t" \
    "v_mad_u32_u24 v10, v184, s70, v10\n\t" \
    "v_mov_b32 v11, 0\n\t" \
    "s_waitcnt vmcnt(12) lgkmcnt(0)\n\t" \
    "s_barrier\n\t" \
    "s_add_u32 m0, s82, 65536\n\t" \
    "s_nop 0\n\t" \
    "global_load_lds_dwordx4 v200, s[42:43]\n\t" \
    "v_add_u32 v200, 131072, v200\n\t" \
    "s_add_u32 m0, s82, 69632\n\t" \
    "s_nop 0\n\t" \
    "global_load_lds_dwordx4 v200, s[42:43]\n\t" \
    "v_add_u32 v200, 131072, v200\n\t" \
    "s_add_u32 m0, s82, 73728\n\t" \
    "s_nop 0\n\t" \
    "global_load_lds_dwordx4 v200, s[42:43]\n\t" \
    "v_add_u32 v200, 131072, v200\n\t" \
    "s_add_u32 m0, s82, 77824\n\t" \
    "s_nop 0\n\t" \
    "global_load_lds_dwordx4 v200, s[42:43]\n\t" \
    "v_add_u32 v200, 131072, v200\n\t" \
    "ds_read_b64_tr_b16 a[0:1], v202 offset:36864\n\t" \
    "ds_read_b64_tr_b16 a[2:3], v202 offset:37888\n\t" \
    "ds_read_b64_tr_b16 a[4:5], v202 offset:45056\n\t" \
    "ds_read_b64_tr_b16 a[6:7], v202 offset:46080\n\t" \
    "ds_read_b64_tr_b16 a[72:73], v203 offset:36864\n\t" \
    "ds_read_b64_tr_b16 a[74:75], v203 offset:37888\n\t" \
    "ds_read_b64_tr_b16 a[76:77], v203 offset:45056\n\t" \
    "ds_read_b64_tr_b16 a[78:79], v203 offset:46080\n\t" \
    "s_mul_i32 s69, s66, 512\n\t" \
    "s_add_u32 s69, s69, 16384\n\t" \
    "v_and_b32 v185, 13, v223\n\t" \
    "v_lshl_add_u32 v12, v185, 5, s69\n\t" \
    "v_lshl_add_u32 v12, v184, 2, v12\n\t" \
    "v_lshrrev_b32 v186, 4, v223\n\t" \
    "v_lshl_add_u32 v12, v186, 3, v12\n\t" \
    "s_mov_b32 s69, 16384\n\t" \
    "v_and_b32 v187, 15, v223\n\t" \
    "v_lshl_add_u32 v13, v187, 3, s69\n\t" \
    "v_lshl_add_u32 v13, v186, 8, v13\n\t" \
    "s_bfe_u32 s71, s67, 0x10004\n\t" \
    "s_mul_i32 s69, s71, 32768\n\t" \
    "s_add_u32 s69, s69, 266240\n\t" \
    "v_and_b32 v188, 31, v223\n\t" \
    "v_lshl_add_u32 v20, v188, 5, s69\n\t" \
    "v_lshl_add_u32 v20, v181, 2, v20\n\t" \
    "s_mul_i32 s69, s67, 32\n\t" \
    "s_add_u32 s69, s69, 262144\n\t" \
    "v_lshl_add_u32 v21, v180, 14, s69\n\t" \
    "v_bfe_u32 v189, v223, 2, 3\n\t" \
    "v_lshlrev_b32 v199, 10, v189\n\t" \
    "v_min_u32 v199, 4096, v199\n\t" \
    "v_add_u32 v21, v199, v21\n\t" \
    "s_bfe_u32 s71, s67, 0x10004\n\t" \
    "s_mul_i32 s69, s71, 32768\n\t" \
    "s_add_u32 s69, s69, 267264\n\t" \
    "v_min_u32 v35, 35, v223\n\t" \
    "v_lshl_add_u32 v35, v35, 5, s69\n\t" \
    "v_lshlrev_b32 v22, 2, v223\n\t" \
    "s_waitcnt vmcnt(12) lgkmcnt(0)\n\t" \
    "s_barrier\n\t" \
    "s_add_u32 m0, s82, 81920\n\t" \
    "s_nop 0\n\t" \
    "global_load_lds_dwordx4 v200, s[42:43]\n\t" \
    "v_add_u32 v200, 131072, v200\n\t" \
    "s_add_u32 m0, s82, 86016\n\t" \
    "s_nop 0\n\t" \
    "global_load_lds_dwordx4 v200, s[42:43]\n\t" \
    "v_add_u32 v200, 131072, v200\n\t" \
    "s_add_u32 m0, s82, 90112\n\t" \
    "s_nop 0\n\t" \
    "global_load_lds_dwordx4 v200, s[42:43]\n\t" \
    "v_add_u32 v200, 131072, v200\n\t" \
    "s_add_u32 m0, s82, 94208\n\t" \
    "s_nop 0\n\t" \
    "global_load_lds_dwordx4 v200, s[42:43]\n\t" \
    "v_add_u32 v200, 131072, v200\n\t" \
    "ds_read_b64_tr_b16 a[8:9], v202 offset:53248\n\t" \
    "ds_read_b64_tr_b16 a[10:11], v202 offset:54272\n\t" \
    "ds_read_b64_tr_b16 a[12:13], v202 offset:61440\n\t" \
    "ds_read_b64_tr_b16 a[14:15], v202 offset:62464\n\t" \
    "ds_read_b64_tr_b16 a[80:81], v203 offset:53248\n\t" \
    "ds_read_b64_tr_b16 a[82:83], v203 offset:54272\n\t" \
    "ds_read_b64_tr_b16 a[84:85], v203 offset:61440\n\t" \
    "ds_read_b64_tr_b16 a[86:87], v203 offset:62464\n\t" \
    "s_mul_i32 s69, s67, 128\n\t" \
    "s_add_u32 s69, s69, 6127616\n\t" \
    "v_mov_b32 v14, s69\n\t" \
    "v_and_b32 v190, 57, v223\n\t" \
    "v_lshlrev_b32 v23, 4, v190\n\t" \
    "v_lshl_add_u32 v23, v184, 6, v23\n\t" \
    "v_bfe_u32 v191, v223, 2, 1\n\t" \
    "v_lshl_add_u32 v23, v191, 5, v23\n\t" \
    "v_add_u32 v15, 4096, v23\n\t" \
    "s_mul_i32 s69, s66, 512\n\t" \
    "s_add_u32 s69, s69, 8192\n\t" \
    "v_lshl_add_u32 v16, v187, 4, s69\n\t" \
    "v_lshl_add_u32 v16, v186, 2, v16\n\t" \
    "s_mov_b32 s69, 8192\n\t" \
    "v_and_b32 v192, 1, v223\n\t" \
    "v_lshl_add_u32 v17, v192, 3, s69\n\t" \
    "v_lshrrev_b32 v193, 2, v223\n\t" \
    "v_lshl_add_u32 v17, v193, 4, v17\n\t" \
    "s_mul_i32 s69, s67, 128\n\t" \
    "s_add_u32 s69, s69, 6160384\n\t" \
    "v_mov_b32 v24, s69\n\t" \
    "s_waitcnt vmcnt(12) lgkmcnt(0)\n\t" \
    "s_barrier\n\t" \
    "s_add_u32 m0, s82, 98304\n\t" \
    "s_nop 0\n\t" \
    "global_load_lds_dwordx4 v200, s[42:43]\n\t" \
    "v_add_u32 v200, 131072, v200\n\t" \
    "s_add_u32 m0, s82, 102400\n\t" \
    "s_nop 0\n\t" \
    "global_load_lds_dwordx4 v200, s[42:43]\n\t" \
    "v_add_u32 v200, 131072, v200\n\t" \
    "s_add_u32 m0, s82, 106496\n\t" \
    "s_nop 0\n\t" \
    "global_load_lds_dwordx4 v200, s[42:43]\n\t" \
    "v_add_u32 v200, 131072, v200\n\t" \
    "s_add_u32 m0, s82, 110592\n\t" \
    "s_nop 0\n\t" \
    "global_load_lds_dwordx4 v200, s[42:43]\n\t" \
    "v_add_u32 v200, 131072, v200\n\t" \
    "v_add_u32 v202, 69632, v202\n\t" \
    "v_add_u32 v203, 69632, v203\n\t" \
    "ds_read_b64_tr_b16 a[16:17], v202 offset:0\n\t" \
    "ds_read_b64_tr_b16 a[18:19], v202 offset:1024\n\t" \
    "ds_read_b64_tr_b16 a[20:21], v202 offset:8192\n\t" \
    "ds_read_b64_tr_b16 a[22:23], v202 offset:9216\n\t" \
    "ds_read_b64_tr_b16 a[88:89], v203 offset:0\n\t" \
    "ds_read_b64_tr_b16 a[90:91], v203 offset:1024\n\t" \
    "ds_read_b64_tr_b16 a[92:93], v203 offset:8192\n\t" \
    "ds_read_b64_tr_b16 a[94:95], v203 offset:9216\n\t" \
    "s_bfe_u32 s71, s66, 0x10000\n\t" \
    "s_mul_i32 s69, s71, 128\n\t" \
    "s_bfe_u32 s71, s67, 0x40000\n\t" \
    "s_mul_i32 s71, s71, 512\n\t" \
    "s_add_u32 s69, s69, s71\n\t" \
    "s_bfe_u32 s71, s67, 0x10004\n\t" \
    "s_mul_i32 s71, s71, 32768\n\t" \
    "s_add_u32 s69, s69, s71\n\t" \
    "v_lshl_add_u32 v18, v180, 2, s69\n\t" \
    "v_lshl_add_u32 v18, v191, 5, v18\n\t" \
    "v_bfe_u32 v194, v223, 3, 1\n\t" \
    "v_lshl_add_u32 v18, v194, 8, v18\n\t" \
    "v_lshl_add_u32 v18, v183, 4, v18\n\t" \
    "v_lshl_add_u32 v18, v181, 6, v18\n\t" \
    "s_bfe_u32 s71, s66, 0x10000\n\t" \
    "s_mul_i32 s69, s71, 128\n\t" \
    "s_bfe_u32 s71, s67, 0x10000\n\t" \
    "s_mul_i32 s71, s71, 8\n\t" \
    "s_add_u32 s69, s69, s71\n\t" \
    "s_bfe_u32 s71, s67, 0x40001\n\t" \
    "s_mul_i32 s71, s71, 256\n\t" \
    "s_add_u32 s69, s69, s71\n\t" \
    "s_add_u32 s69, s69, 98304\n\t" \
    "v_lshl_add_u32 v19, v184, 5, s69\n\t" \
    "v_and_b32 v195, 5, v193\n\t" \
    "v_lshl_add_u32 v19, v195, 2, v19\n\t" \
    "v_lshl_add_u32 v19, v181, 6, v19\n\t" \
    "v_add_u32 v3, 16384, v18\n\t" \
    "v_add_u32 v4, 16384, v19\n\t" \
    "s_waitcnt vmcnt(12) lgkmcnt(0)\n\t" \
    "s_barrier\n\t" \
    "s_add_u32 m0, s82, 0\n\t" \
    "s_nop 0\n\t" \
    "global_load_lds_dwordx4 v200, s[42:43]\n\t" \
    "v_add_u32 v200, 131072, v200\n\t" \
    "s_add_u32 m0, s82, 4096\n\t" \
    "s_nop 0\n\t" \
    "global_load_lds_dwordx4 v200, s[42:43]\n\t" \
    "v_add_u32 v200, 131072, v200\n\t" \
    "s_add_u32 m0, s82, 8192\n\t" \
    "s_nop 0\n\t" \
    "global_load_lds_dwordx4 v200, s[42:43]\n\t" \
    "v_add_u32 v200, 131072, v200\n\t" \
    "s_add_u32 m0, s82, 12288\n\t" \
    "s_nop 0\n\t" \
    "global_load_lds_dwordx4 v200, s[42:43]\n\t" \
    "v_add_u32 v200, 131072, v200\n\t" \
    "ds_read_b64_tr_b16 a[24:25], v202 offset:16384\n\t" \
    "ds_read_b64_tr_b16 a[26:27], v202 offset:17408\n\t" \
    "ds_read_b64_tr_b16 a[28:29], v202 offset:24576\n\t" \
    "ds_read_b64_tr_b16 a[30:31], v202 offset:25600\n\t" \
    "ds_read_b64_tr_b16 a[96:97], v203 offset:16384\n\t" \
    "ds_read_b64_tr_b16 a[98:99], v203 offset:17408\n\t" \
    "ds_read_b64_tr_b16 a[100:101], v203 offset:24576\n\t" \
    "ds_read_b64_tr_b16 a[102:103], v203 offset:25600\n\t" \
    "s_mov_b32 s77, 3019898880\n\t" \
    "s_mov_b32 s78, 3054198966\n\t" \
    "s_mov_b32 s71, 3\n\t" \
    "qk_E1_%=:\n\t" \
    "s_memtime s[54:55]\n\t" \
    "s_nop 0\n\t" \
    "s_memrealtime s[4:5]\n\t" \
    "s_nop 0\n\t" \
    "s_memrealtime s[6:7]\n\t" \
    "s_nop 0\n\t" \
    "s_memrealtime s[8:9]\n\t" \
    "s_nop 0\n\t" \
    "s_memrealtime s[10:11]\n\t" \
    "s_nop 0\n\t" \
    "s_memrealtime s[12:13]\n\t" \
    "s_nop 0\n\t" \
    "s_memrealtime s[14:15]\n\t" \
    "s_nop 0\n\t" \
    "s_memrealtime s[16:17]\n\t" \
    "s_nop 0\n\t" \
    "s_memrealtime s[18:19]\n\t" \
    "s_nop 0\n\t" \
    "s_memrealtime s[20:21]\n\t" \
    "s_nop 0\n\t" \
    "s_memrealtime s[22:23]\n\t" \
    "s_nop 0\n\t" \
    "s_memrealtime s[24:25]\n\t" \
    "s_nop 0\n\t" \
    "s_memrealtime s[26:27]\n\t" \
    "s_nop 0\n\t" \
    "s_memrealtime s[28:29]\n\t" \
    "s_nop 0\n\t" \
    "s_memtime s[56:57]\n\t" \
    "s_mov_b32 s70, 1\n\t" \
    "s_sub_u32 s71, s71, 1\n\t" \
    "s_cselect_b32 s69, 0, -1\n\t" \
    "s_waitcnt lgkmcnt(0)\n\t" \
    "s_sub_u32 s54, s56, s54\n\t" \
    "s_and_b32 s54, s54, s69\n\t" \
    "s_cmp_gt_u32 s54, 124\n\t" \
    "s_cbranch_scc1 qk_E1_%=\n\t" \
    "s_cmp_eq_u32 s4, s28\n\t" \
    "s_addc_u32 s70, s70, 0\n\t" \
    "s_cmp_eq_u32 s6, s28\n\t" \
    "s_addc_u32 s70, s70, 0\n\t" \
    "s_cmp_eq_u32 s8, s28\n\t" \
    "s_addc_u32 s70, s70, 0\n\t" \
    "s_cmp_eq_u32 s10, s28\n\t" \
    "s_addc_u32 s70, s70, 0\n\t" \
    "s_cmp_eq_u32 s12, s28\n\t" \
    "s_addc_u32 s70, s70, 0\n\t" \
    "s_cmp_eq_u32 s14, s28\n\t" \
    "s_addc_u32 s70, s70, 0\n\t" \
    "s_cmp_eq_u32 s16, s28\n\t" \
    "s_addc_u32 s70, s70, 0\n\t" \
    "s_cmp_eq_u32 s18, s28\n\t" \
    "s_addc_u32 s70, s70, 0\n\t" \
    "s_cmp_eq_u32 s20, s28\n\t" \
    "s_addc_u32 s70, s70, 0\n\t" \
    "s_cmp_eq_u32 s22, s28\n\t" \
    "s_addc_u32 s70, s70, 0\n\t" \
    "s_cmp_eq_u32 s24, s28\n\t" \
    "s_addc_u32 s70, s70, 0\n\t" \
    "s_cmp_eq_u32 s26, s28\n\t" \
    "s_addc_u32 s70, s70, 0\n\t" \
    "s_mov_b32 s75, s73\n\t" \
    "s_mov_b32 s76, s74\n\t" \
    "s_lshl_b32 s70, s70, 3\n\t" \
    "s_sub_u32 s73, s56, s70\n\t" \
    "s_lshl_b32 s74, s28, 4\n\t" \
    "s_waitcnt vmcnt(12) lgkmcnt(0)\n\t" \
    "s_barrier\n\t" \
    "s_add_u32 m0, s82, 16384\n\t" \
    "s_nop 0\n\t" \
    "global_load_lds_dwordx4 v200, s[42:43]\n\t" \
    "v_add_u32 v200, 131072, v200\n\t" \
    "s_add_u32 m0, s82, 20480\n\t" \
    "s_nop 0\n\t" \
    "global_load_lds_dwordx4 v200, s[42:43]\n\t" \
    "v_add_u32 v200, 131072, v200\n\t" \
    "s_add_u32 m0, s82, 24576\n\t" \
    "s_nop 0\n\t" \
    "global_load_lds_dwordx4 v200, s[42:43]\n\t" \
    "v_add_u32 v200, 131072, v200\n\t" \
    "s_add_u32 m0, s82, 28672\n\t" \
    "s_nop 0\n\t" \
    "global_load_lds_dwordx4 v200, s[42:43]\n\t" \
    "v_add_u32 v200, 131072, v200\n\t" \
    "ds_read_b64_tr_b16 a[32:33], v202 offset:32768\n\t" \
    "ds_read_b64_tr_b16 a[34:35], v202 offset:33792\n\t" \
    "ds_read_b64_tr_b16 a[36:37], v202 offset:40960\n\t" \
    "ds_read_b64_tr_b16 a[38:39], v202 offset:41984\n\t" \
    "ds_read_b64_tr_b16 a[104:105], v203 offset:32768\n\t" \
    "ds_read_b64_tr_b16 a[106:107], v203 offset:33792\n\t" \
    "ds_read_b64_tr_b16 a[108:109], v203 offset:40960\n\t" \
    "ds_read_b64_tr_b16 a[110:111], v203 offset:41984\n\t" \
    "s_waitcnt vmcnt(12) lgkmcnt(0)\n\t" \
    "s_barrier\n\t" \
    "v_subrev_u32 v200, 4718592, v200\n\t" \
    "s_add_u32 m0, s82, 32768\n\t" \
    "s_nop 0\n\t" \
    "global_load_lds_dwordx4 v200, s[44:45]\n\t" \
    "v_add_u32 v200, 131072, v200\n\t" \
    "s_add_u32 m0, s82, 36864\n\t" \
    "s_nop 0\n\t" \
    "global_load_lds_dwordx4 v200, s[44:45]\n\t" \
    "v_add_u32 v200, 131072, v200\n\t" \
    "s_add_u32 m0, s82, 40960\n\t" \
    "s_nop 0\n\t" \
    "global_load_lds_dwordx4 v200, s[44:45]\n\t" \
    "v_add_u32 v200, 131072, v200\n\t" \
    "s_add_u32 m0, s82, 45056\n\t" \
    "s_nop 0\n\t" \
    "global_load_lds_dwordx4 v200, s[44:45]\n\t" \
    "v_add_u32 v200, 131072, v200\n\t" \
    "ds_read_b64_tr_b16 a[40:41], v202 offset:49152\n\t" \
    "ds_read_b64_tr_b16 a[42:43], v202 offset:50176\n\t" \
    "ds_read_b64_tr_b16 a[44:45], v202 offset:57344\n\t" \
    "ds_read_b64_tr_b16 a[46:47], v202 offset:58368\n\t" \
    "ds_read_b64_tr_b16 a[112:113], v203 offset:49152\n\t" \
    "ds_read_b64_tr_b16 a[114:115], v203 offset:50176\n\t" \
    "ds_read_b64_tr_b16 a[116:117], v203 offset:57344\n\t" \
    "ds_read_b64_tr_b16 a[118:119], v203 offset:58368\n\t" \
    "s_waitcnt vmcnt(12) lgkmcnt(0)\n\t" \
    "s_barrier\n\t" \
    "s_add_u32 m0, s82, 49152\n\t" \
    "s_nop 0\n\t" \
    "global_load_lds_dwordx4 v200, s[44:45]\n\t" \
    "v_add_u32 v200, 131072, v200\n\t" \
    "s_add_u32 m0, s82, 53248\n\t" \
    "s_nop 0\n\t" \
    "global_load_lds_dwordx4 v200, s[44:45]\n\t" \
    "v_add_u32 v200, 131072, v200\n\t" \
    "s_add_u32 m0, s82, 57344\n\t" \
    "s_nop 0\n\t" \
    "global_load_lds_dwordx4 v200, s[44:45]\n\t" \
    "v_add_u32 v200, 131072, v200\n\t" \
    "s_add_u32 m0, s82, 61440\n\t" \
    "s_nop 0\n\t" \
    "global_load_lds_dwordx4 v200, s[44:45]\n\t" \
    "v_add_u32 v200, 131072, v200\n\t" \
    "v_add_u32 v202, 65536, v202\n\t" \
    "v_add_u32 v203, 65536, v203\n\t" \
    "ds_read_b64_tr_b16 a[48:49], v202 offset:0\n\t" \
    "ds_read_b64_tr_b16 a[50:51], v202 offset:1024\n\t" \
    "ds_read_b64_tr_b16 a[52:53], v202 offset:8192\n\t" \
    "ds_read_b64_tr_b16 a[54:55], v202 offset:9216\n\t" \
    "ds_read_b64_tr_b16 a[120:121], v203 offset:0\n\t" \
    "ds_read_b64_tr_b16 a[122:123], v203 offset:1024\n\t" \
    "ds_read_b64_tr_b16 a[124:125], v203 offset:8192\n\t" \
    "ds_read_b64_tr_b16 a[126:127], v203 offset:9216\n\t" \
    "s_waitcnt vmcnt(12) lgkmcnt(0)\n\t" \
    "s_barrier\n\t" \
    "s_add_u32 m0, s82, 65536\n\t" \
    "s_nop 0\n\t" \
    "global_load_lds_dwordx4 v200, s[44:45]\n\t" \
    "v_add_u32 v200, 131072, v200\n\t" \
    "s_add_u32 m0, s82, 69632\n\t" \
    "s_nop 0\n\t" \
    "global_load_lds_dwordx4 v200, s[44:45]\n\t" \
    "v_add_u32 v200, 131072, v200\n\t" \
    "s_add_u32 m0, s82, 73728\n\t" \
    "s_nop 0\n\t" \
    "global_load_lds_dwordx4 v200, s[44:45]\n\t" \
    "v_add_u32 v200, 131072, v200\n\t" \
    "s_add_u32 m0, s82, 77824\n\t" \
    "s_nop 0\n\t" \
    "global_load_lds_dwordx4 v200, s[44:45]\n\t" \
    "v_add_u32 v200, 131072, v200\n\t" \
    "v_add_u32 v202, -98304, v202\n\t" \
    "v_add_u32 v203, -98304, v203\n\t" \
    "ds_read_b64_tr_b16 a[56:57], v202 offset:0\n\t" \
    "ds_read_b64_tr_b16 a[58:59], v202 offset:1024\n\t" \
    "ds_read_b64_tr_b16 a[60:61], v202 offset:8192\n\t" \
    "ds_read_b64_tr_b16 a[62:63], v202 offset:9216\n\t" \
    "ds_read_b64_tr_b16 a[128:129], v203 offset:0\n\t" \
    "ds_read_b64_tr_b16 a[130:131], v203 offset:1024\n\t" \
    "ds_read_b64_tr_b16 a[132:133], v203 offset:8192\n\t" \
    "ds_read_b64_tr_b16 a[134:135], v203 offset:9216\n\t" \
    "s_waitcnt vmcnt(12) lgkmcnt(0)\n\t" \
    "s_barrier\n\t" \
    "s_add_u32 m0, s82, 81920\n\t" \
    "s_nop 0\n\t" \
    "global_load_lds_dwordx4 v200, s[44:45]\n\t" \
    "v_add_u32 v200, 131072, v200\n\t" \
    "s_add_u32 m0, s82, 86016\n\t" \
    "s_nop 0\n\t" \
    "global_load_lds_dwordx4 v200, s[44:45]\n\t" \
    "v_add_u32 v200, 131072, v200\n\t" \
    "s_add_u32 m0, s82, 90112\n\t" \
    "s_nop 0\n\t" \
    "global_load_lds_dwordx4 v200, s[44:45]\n\t" \
    "v_add_u32 v200, 131072, v200\n\t" \
    "s_add_u32 m0, s82, 94208\n\t" \
    "s_nop 0\n\t" \
    "global_load_lds_dwordx4 v200, s[44:45]\n\t" \
    "v_add_u32 v200, 131072, v200\n\t" \
    "ds_read_b64_tr_b16 a[64:65], v202 offset:16384\n\t" \
    "ds_read_b64_tr_b16 a[66:67], v202 offset:17408\n\t" \
    "ds_read_b64_tr_b16 a[68:69], v202 offset:24576\n\t" \
    "ds_read_b64_tr_b16 a[70:71], v202 offset:25600\n\t" \
    "ds_read_b64_tr_b16 a[136:137], v203 offset:16384\n\t" \
    "ds_read_b64_tr_b16 a[138:139], v203 offset:17408\n\t" \
    "ds_read_b64_tr_b16 a[140:141], v203 offset:24576\n\t" \
    "ds_read_b64_tr_b16 a[142:143], v203 offset:25600\n\t" \
    "s_waitcnt vmcnt(12) lgkmcnt(0)\n\t" \
    "s_barrier\n\t" \
    "s_add_u32 m0, s82, 98304\n\t" \
    "s_nop 0\n\t" \
    "global_load_lds_dwordx4 v200, s[44:45]\n\t" \
    "v_add_u32 v200, 131072, v200\n\t" \
    "s_add_u32 m0, s82, 102400\n\t" \
    "s_nop 0\n\t" \
    "global_load_lds_dwordx4 v200, s[44:45]\n\t" \
    "v_add_u32 v200, 131072, v200\n\t" \
    "s_add_u32 m0, s82, 106496\n\t" \
    "s_nop 0\n\t" \
    "global_load_lds_dwordx4 v200, s[44:45]\n\t" \
    "v_add_u32 v200, 131072, v200\n\t" \
    "s_add_u32 m0, s82, 110592\n\t" \
    "s_nop 0\n\t" \
    "global_load_lds_dwordx4 v200, s[44:45]\n\t" \
    "v_add_u32 v200, 131072, v200\n\t" \
    "ds_read_b64_tr_b16 a[144:145], v202 offset:32768\n\t" \
    "ds_read_b64_tr_b16 a[146:147], v202 offset:33792\n\t" \
    "ds_read_b64_tr_b16 a[148:149], v202 offset:40960\n\t" \
    "ds_read_b64_tr_b16 a[150:151], v202 offset:41984\n\t" \
    "ds_read_b64_tr_b16 a[216:217], v203 offset:32768\n\t" \
    "ds_read_b64_tr_b16 a[218:219], v203 offset:33792\n\t" \
    "ds_read_b64_tr_b16 a[220:221], v203 offset:40960\n\t" \
    "ds_read_b64_tr_b16 a[222:223], v203 offset:41984\n\t" \
    "s_waitcnt vmcnt(12) lgkmcnt(0)\n\t" \
    "s_barrier\n\t" \
    "s_add_u32 m0, s82, 0\n\t" \
    "s_nop 0\n\t" \
    "global_load_lds_dwordx4 v200, s[44:45]\n\t" \
    "v_add_u32 v200, 131072, v200\n\t" \
    "s_add_u32 m0, s82, 4096\n\t" \
    "s_nop 0\n\t" \
    "global_load_lds_dwordx4 v200, s[44:45]\n\t" \
    "v_add_u32 v200, 131072, v200\n\t" \
    "s_add_u32 m0, s82, 8192\n\t" \
    "s_nop 0\n\t" \
    "global_load_lds_dwordx4 v200, s[44:45]\n\t" \
    "v_add_u32 v200, 131072, v200\n\t" \
    "s_add_u32 m0, s82, 12288\n\t" \
    "s_nop 0\n\t" \
    "global_load_lds_dwordx4 v200, s[44:45]\n\t" \
    "v_add_u32 v200, 131072, v200\n\t" \
    "ds_read_b64_tr_b16 a[152:153], v202 offset:49152\n\t" \
    "ds_read_b64_tr_b16 a[154:155], v202 offset:50176\n\t" \
    "ds_read_b64_tr_b16 a[156:157], v202 offset:57344\n\t" \
    "ds_read_b64_tr_b16 a[158:159], v202 offset:58368\n\t" \
    "ds_read_b64_tr_b16 a[224:225], v203 offset:49152\n\t" \
    "ds_read_b64_tr_b16 a[226:227], v203 offset:50176\n\t" \
    "ds_read_b64_tr_b16 a[228:229], v203 offset:57344\n\t" \
    "ds_read_b64_tr_b16 a[230:231], v203 offset:58368\n\t" \
    "s_waitcnt vmcnt(12) lgkmcnt(0)\n\t" \
    "s_barrier\n\t" \
    "s_add_u32 m0, s82, 16384\n\t" \
    "s_nop 0\n\t" \
    "global_load_lds_dwordx4 v200, s[44:45]\n\t" \
    "v_add_u32 v200, 131072, v200\n\t" \
    "s_add_u32 m0, s82, 20480\n\t" \
    "s_nop 0\n\t" \
    "global_load_lds_dwordx4 v200, s[44:45]\n\t" \
    "v_add_u32 v200, 131072, v200\n\t" \
    "s_add_u32 m0, s82, 24576\n\t" \
    "s_nop 0\n\t" \
    "global_load_lds_dwordx4 v200, s[44:45]\n\t" \
    "v_add_u32 v200, 131072, v200\n\t" \
    "s_add_u32 m0, s82, 28672\n\t" \
    "s_nop 0\n\t" \
    "global_load_lds_dwordx4 v200, s[44:45]\n\t" \
    "v_add_u32 v200, 131072, v200\n\t" \
    "v_add_u32 v202, 65536, v202\n\t" \
    "v_add_u32 v203, 65536, v203\n\t" \
    "ds_read_b64_tr_b16 a[160:161], v202 offset:0\n\t" \
    "ds_read_b64_tr_b16 a[162:163], v202 offset:1024\n\t" \
    "ds_read_b64_tr_b16 a[164:165], v202 offset:8192\n\t" \
    "ds_read_b64_tr_b16 a[166:167], v202 offset:9216\n\t" \
    "ds_read_b64_tr_b16 a[232:233], v203 offset:0\n\t" \
    "ds_read_b64_tr_b16 a[234:235], v203 offset:1024\n\t" \
    "ds_read_b64_tr_b16 a[236:237], v203 offset:8192\n\t" \
    "ds_read_b64_tr_b16 a[238:239], v203 offset:9216\n\t" \
    "s_waitcnt vmcnt(12) lgkmcnt(0)\n\t" \
    "s_barrier\n\t" \
    "s_add_u32 m0, s82, 32768\n\t" \
    "s_nop 0\n\t" \
    "global_load_lds_dwordx4 v200, s[44:45]\n\t" \
    "v_add_u32 v200, 131072, v200\n\t" \
    "s_add_u32 m0, s82, 36864\n\t" \
    "s_nop 0\n\t" \
    "global_load_lds_dwordx4 v200, s[44:45]\n\t" \
    "v_add_u32 v200, 131072, v200\n\t" \
    "s_add_u32 m0, s82, 40960\n\t" \
    "s_nop 0\n\t" \
    "global_load_lds_dwordx4 v200, s[44:45]\n\t" \
    "v_add_u32 v200, 131072, v200\n\t" \
    "s_add_u32 m0, s82, 45056\n\t" \
    "s_nop 0\n\t" \
    "global_load_lds_dwordx4 v200, s[44:45]\n\t" \
    "v_add_u32 v200, 131072, v200\n\t" \
    "ds_read_b64_tr_b16 a[168:169], v202 offset:16384\n\t" \
    "ds_read_b64_tr_b16 a[170:171], v202 offset:17408\n\t" \
    "ds_read_b64_tr_b16 a[172:173], v202 offset:24576\n\t" \
    "ds_read_b64_tr_b16 a[174:175], v202 offset:25600\n\t" \
    "ds_read_b64_tr_b16 a[240:241], v203 offset:16384\n\t" \
    "ds_read_b64_tr_b16 a[242:243], v203 offset:17408\n\t" \
    "ds_read_b64_tr_b16 a[244:245], v203 offset:24576\n\t" \
    "ds_read_b64_tr_b16 a[246:247], v203 offset:25600\n\t" \
    "s_waitcnt vmcnt(12) lgkmcnt(0)\n\t" \
    "s_barrier\n\t" \
    "s_add_u32 m0, s82, 49152\n\t" \
    "s_nop 0\n\t" \
    "global_load_lds_dwordx4 v200, s[44:45]\n\t" \
    "v_add_u32 v200, 131072, v200\n\t" \
    "s_add_u32 m0, s82, 53248\n\t" \
    "s_nop 0\n\t" \
    "global_load_lds_dwordx4 v200, s[44:45]\n\t" \
    "v_add_u32 v200, 131072, v200\n\t" \
    "s_add_u32 m0, s82, 57344\n\t" \
    "s_nop 0\n\t" \
    "global_load_lds_dwordx4 v200, s[44:45]\n\t" \
    "v_add_u32 v200, 131072, v200\n\t" \
    "s_add_u32 m0, s82, 61440\n\t" \
    "s_nop 0\n\t" \
    "global_load_lds_dwordx4 v200, s[44:45]\n\t" \
    "v_add_u32 v200, 131072, v200\n\t" \
    "ds_read_b64_tr_b16 a[176:177], v202 offset:32768\n\t" \
    "ds_read_b64_tr_b16 a[178:179], v202 offset:33792\n\t" \
    "ds_read_b64_tr_b16 a[180:181], v202 offset:40960\n\t" \
    "ds_read_b64_tr_b16 a[182:183], v202 offset:41984\n\t" \
    "ds_read_b64_tr_b16 a[248:249], v203 offset:32768\n\t" \
    "ds_read_b64_tr_b16 a[250:251], v203 offset:33792\n\t" \
    "ds_read_b64_tr_b16 a[252:253], v203 offset:40960\n\t" \
    "ds_read_b64_tr_b16 a[254:255], v203 offset:41984\n\t" \
    "s_waitcnt vmcnt(12) lgkmcnt(0)\n\t" \
    "s_barrier\n\t" \
    "v_mbcnt_lo_u32_b32 v160, -1, 0\n\t" \
    "v_mbcnt_hi_u32_b32 v160, -1, v160\n\t" \
    "v_min_u32 v160, 59, v160\n\t" \
    "v_lshlrev_b32 v160, 6, v160\n\t" \
    "v_add_u32 v160, 6258688, v160\n\t" \
    "v_mov_b32 v161, 6266880\n\t" \
    "global_load_dword v161, v161, s[36:37] sc1\n\t" \
    "global_load_dwordx2 v[162:163], v160, s[36:37] sc1\n\t" \
    "v_add_u32 v202, -65536, v202\n\t" \
    "v_add_u32 v203, -65536, v203\n\t" \
    "ds_read_b64_tr_b16 a[184:185], v202 offset:0\n\t" \
    "ds_read_b64_tr_b16 a[186:187], v202 offset:1024\n\t" \
    "ds_read_b64_tr_b16 a[188:189], v202 offset:8192\n\t" \
    "ds_read_b64_tr_b16 a[190:191], v202 offset:9216\n\t" \
    "ds_read_b64_tr_b16 v[224:225], v203 offset:0\n\t" \
    "ds_read_b64_tr_b16 v[226:227], v203 offset:1024\n\t" \
    "ds_read_b64_tr_b16 v[228:229], v203 offset:8192\n\t" \
    "ds_read_b64_tr_b16 v[230:231], v203 offset:9216\n\t" \
    "s_waitcnt vmcnt(10) lgkmcnt(0)\n\t" \
    "s_barrier\n\t" \
    "ds_read_b64_tr_b16 a[192:193], v202 offset:16384\n\t" \
    "ds_read_b64_tr_b16 a[194:195], v202 offset:17408\n\t" \
    "ds_read_b64_tr_b16 a[196:197], v202 offset:24576\n\t" \
    "ds_read_b64_tr_b16 a[198:199], v202 offset:25600\n\t" \
    "ds_read_b64_tr_b16 v[232:233], v203 offset:16384\n\t" \
    "ds_read_b64_tr_b16 v[234:235], v203 offset:17408\n\t" \
    "ds_read_b64_tr_b16 v[236:237], v203 offset:24576\n\t" \
    "ds_read_b64_tr_b16 v[238:239], v203 offset:25600\n\t" \
    "s_waitcnt vmcnt(6) lgkmcnt(0)\n\t" \
    "s_barrier\n\t" \
    "ds_read_b64_tr_b16 a[200:201], v202 offset:32768\n\t" \
    "ds_read_b64_tr_b16 a[202:203], v202 offset:33792\n\t" \
    "ds_read_b64_tr_b16 a[204:205], v202 offset:40960\n\t" \
    "ds_read_b64_tr_b16 a[206:207], v202 offset:41984\n\t" \
    "ds_read_b64_tr_b16 v[240:241], v203 offset:32768\n\t" \
    "ds_read_b64_tr_b16 v[242:243], v203 offset:33792\n\t" \
    "ds_read_b64_tr_b16 v[244:245], v203 offset:40960\n\t" \
    "ds_read_b64_tr_b16 v[246:247], v203 offset:41984\n\t" \
    "s_waitcnt vmcnt(2) lgkmcnt(0)\n\t" \
    "s_barrier\n\t" \
    "ds_read_b64_tr_b16 a[208:209], v202 offset:49152\n\t" \
    "ds_read_b64_tr_b16 a[210:211], v202 offset:50176\n\t" \
    "ds_read_b64_tr_b16 a[212:213], v202 offset:57344\n\t" \
    "ds_read_b64_tr_b16 a[214:215], v202 offset:58368\n\t" \
    "ds_read_b64_tr_b16 v[248:249], v203 offset:49152\n\t" \
    "ds_read_b64_tr_b16 v[250:251], v203 offset:50176\n\t" \
    "ds_read_b64_tr_b16 v[252:253], v203 offset:57344\n\t" \
    "ds_read_b64_tr_b16 v[254:255], v203 offset:58368\n\t" \
    "s_waitcnt vmcnt(0) lgkmcnt(0)\n\t" \
    "s_barrier\n\t" \
    "v_sub_u32 v162, v162, v161\n\t" \
    "v_cmp_ne_u32 vcc, 1, v162\n\t" \
    "s_cbranch_vccz qk_Cok4_%=\n\t" \
    "qk_C3_%=:\n\t" \
    "global_load_dwordx2 v[162:163], v160, s[36:37] sc1\n\t" \
    "s_waitcnt vmcnt(0)\n\t" \
    "v_sub_u32 v162, v162, v161\n\t" \
    "v_cmp_ne_u32 vcc, 1, v162\n\t" \
    "s_cbranch_vccz qk_Cok4_%=\n\t" \
    "s_cmp_eq_u32 s68, 0\n\t" \
    "s_cbranch_scc1 qk_F5_%=\n\t" \
    "s_sub_u32 s68, s68, 1\n\t" \
    "s_sleep 4\n\t" \
    "s_branch qk_C3_%=\n\t" \
    "qk_F5_%=:\n\t" \
    "v_mov_b32 v164, 6095104\n\t" \
    "v_mov_b32 v165, 4\n\t" \
    "global_store_dword v164, v165, s[36:37] sc1\n\t" \
    "s_waitcnt vmcnt(0)\n\t" \
    "s_branch qk_Cok4_%=\n\t" \
    "qk_Cok4_%=:\n\t" \
    "s_cmp_lg_u32 s66, 0\n\t" \
    "s_cbranch_scc1 qk_Ca6_%=\n\t" \
    "v_mov_b32 v160, 6176768\n\t" \
    "s_mov_b64 exec, 1\n\t" \
    "global_atomic_add v160, v162, s[36:37] sc1\n\t" \
    "s_mov_b64 exec, -1\n\t" \
    "qk_Ca6_%=:\n\t" \
    "v_sub_u32_dpp v162, v163, v163 row_shl:6 row_mask:0xf bank_mask:0xf\n\t" \
    "v_min_u32_dpp v163, v163, v163 row_shl:6 row_mask:0xf bank_mask:0xf\n\t" \
    "v_min_i32 v162, 64, v162\n\t" \
    "v_max_i32 v162, -64, v162\n\t" \
    "s_mov_b32 s97, 0\n\t" \
    "v_readlane_b32 s69, v163, 0\n\t" \
    "v_readlane_b32 s70, v163, 1\n\t" \
    "v_readlane_b32 s54, v162, 0\n\t" \
    "v_readlane_b32 s55, v162, 1\n\t" \
    "s_nop 1\n\t" \
    "s_cmp_lt_u32 s70, s69\n\t" \
    "s_cselect_b32 s62, 8192, 0\n\t" \
    "s_cselect_b32 s54, s55, s54\n\t" \
    "s_cselect_b32 s69, s70, s69\n\t" \
    "s_mov_b32 s71, s69\n\t" \
    "s_add_u32 s97, s97, s54\n\t" \
    "v_readlane_b32 s69, v163, 2\n\t" \
    "v_readlane_b32 s70, v163, 3\n\t" \
    "v_readlane_b32 s54, v162, 2\n\t" \
    "v_readlane_b32 s55, v162, 3\n\t" \
    "s_nop 1\n\t" \
    "s_sub_u32 s55, 0, s55\n\t" \
    "s_cmp_lt_u32 s70, s69\n\t" \
    "s_cselect_b32 s63, 8192, 0\n\t" \
    "s_cselect_b32 s54, s55, s54\n\t" \
    "s_cselect_b32 s69, s70, s69\n\t" \
    "s_min_u32 s71, s69, s71\n\t" \
    "s_add_u32 s97, s97, s54\n\t" \
    "v_readlane_b32 s69, v163, 4\n\t" \
    "v_readlane_b32 s70, v163, 5\n\t" \
    "v_readlane_b32 s54, v162, 4\n\t" \
    "v_readlane_b32 s55, v162, 5\n\t" \
    "s_nop 1\n\t" \
    "s_sub_u32 s54, 0, s54\n\t" \
    "s_sub_u32 s55, 0, s55\n\t" \
    "s_cmp_lt_u32 s70, s69\n\t" \
    "s_cselect_b32 s64, 8192, 0\n\t" \
    "s_cselect_b32 s54, s55, s54\n\t" \
    "s_cselect_b32 s69, s70, s69\n\t" \
    "s_min_u32 s71, s69, s71\n\t" \
    "s_add_u32 s97, s97, s54\n\t" \
    "s_add_u32 s63, s63, 0x200000\n\t" \
    "s_xor_b32 s52, s64, 8192\n\t" \
    "s_cmp_gt_u32 s71, 625\n\t" \
    "s_cselect_b32 s69, 1, 0\n\t" \
    "s_mul_i32 s70, s69, 160\n\t" \
    "s_add_u32 s86, s86, s70\n\t" \
    "s_mul_i32 s70, s69, 536\n\t" \
    "s_add_u32 s87, s87, s70\n\t" \
    "s_mul_i32 s70, s69, 256\n\t" \
    "s_add_u32 s88, s88, s70\n\t" \
    "s_mul_i32 s70, s67, 7\n\t" \
    "s_add_u32 s69, s65, 0\n\t" \
    "s_xor_b32 s69, s69, s70\n\t" \
    "s_and_b32 s69, s69, 63\n\t" \
    "s_lshl_b32 s69, s69, 13\n\t" \
    "s_mul_i32 s70, s67, 524288\n\t" \
    "s_add_u32 s69, s69, s70\n\t" \
    "s_add_u32 s69, s69, 23068672\n\t" \
    "s_add_u32 s50, s36, s69\n\t" \
    "s_addc_u32 s51, s37, 0\n\t" \
    "v_add_u32 v14, s52, v14\n\t" \
    "v_add_u32 v24, s52, v24\n\t" \
    "v_mov_b32 v26, 0x3db504f3\n\t" \
    "v_mov_b32 v28, 0x3fb8aa3b\n\t" \
    "v_mov_b32 v27, 0x3db504f3\n\t" \
    "v_mov_b32 v29, 0x3fb8aa3b\n\t" \
    "v_mov_b32 v29, 0x3e0293ee\n\t" \
    "s_mov_b32 s48, 0xaaaaaaaa\n\t" \
    "s_mov_b32 s49, 0xaaaaaaaa\n\t" \
    "s_mov_b32 s58, 0x00ff00ff\n\t" \
    "s_mov_b32 s59, 0x00ff00ff\n\t" \
    "s_lshl_b32 s69, s66, 2\n\t" \
    "v_mov_b32 v30, s69\n\t" \
    "v_mov_b32 v31, 0\n\t" \
    "v_and_b32 v33, 12, v22\n\t" \
    "ds_write_b32 v30, v31 offset:20496\n\t" \
    "s_waitcnt lgkmcnt(0)\n\t" \
    "s_mov_b64 s[40:41], s[36:37]\n\t" \
    "s_cmp_gt_i32 s97, 0\n\t" \
    "s_cselect_b32 s70, 1, 0\n\t" \
    "s_lshr_b32 s69, s67, 3\n\t" \
    "s_xor_b32 s69, s69, s70\n\t" \
    "s_and_b32 s97, s69, 1\n\t" \
    "s_mov_b64 s[44:45], 0\n\t" \
    "s_barrier\n\t" \
    "s_mov_b32 s71, 3\n\t" \
    "qk_E7_%=:\n\t" \
    "s_memtime s[54:55]\n\t" \
    "s_nop 0\n\t" \
    "s_memrealtime s[4:5]\n\t" \
    "s_nop 0\n\t" \
    "s_memrealtime s[6:7]\n\t" \
    "s_nop 0\n\t" \
    "s_memrealtime s[8:9]\n\t" \
    "s_nop 0\n\t" \
    "s_memrealtime s[10:11]\n\t" \
    "s_nop 0\n\t" \
    "s_memrealtime s[12:13]\n\t" \
    "s_nop 0\n\t" \
    "s_memrealtime s[14:15]\n\t" \
    "s_nop 0\n\t" \
    "s_memrealtime s[16:17]\n\t" \
    "s_nop 0\n\t" \
    "s_memrealtime s[18:19]\n\t" \
    "s_nop 0\n\t" \
    "s_memrealtime s[20:21]\n\t" \
    "s_nop 0\n\t" \
    "s_memrealtime s[22:23]\n\t" \
    "s_nop 0\n\t" \
    "s_memrealtime s[24:25]\n\t" \
    "s_nop 0\n\t" \
    "s_memrealtime s[26:27]\n\t" \
    "s_nop 0\n\t" \
    "s_memrealtime s[28:29]\n\t" \
    "s_nop 0\n\t" \
    "s_memtime s[56:57]\n\t" \
    "s_mov_b32 s70, 1\n\t" \
    "s_sub_u32 s71, s71, 1\n\t" \
    "s_cselect_b32 s69, 0, -1\n\t" \
    "s_waitcnt lgkmcnt(0)\n\t" \
    "s_sub_u32 s54, s56, s54\n\t" \
    "s_and_b32 s54, s54, s69\n\t" \
    "s_cmp_gt_u32 s54, 124\n\t" \
    "s_cbranch_scc1 qk_E7_%=\n\t" \
    "s_cmp_eq_u32 s4, s28\n\t" \
    "s_addc_u32 s70, s70, 0\n\t" \
    "s_cmp_eq_u32 s6, s28\n\t" \
    "s_addc_u32 s70, s70, 0\n\t" \
    "s_cmp_eq_u32 s8, s28\n\t" \
    "s_addc_u32 s70, s70, 0\n\t" \
    "s_cmp_eq_u32 s10, s28\n\t" \
    "s_addc_u32 s70, s70, 0\n\t" \
    "s_cmp_eq_u32 s12, s28\n\t" \
    "s_addc_u32 s70, s70, 0\n\t" \
    "s_cmp_eq_u32 s14, s28\n\t" \
    "s_addc_u32 s70, s70, 0\n\t" \
    "s_cmp_eq_u32 s16, s28\n\t" \
    "s_addc_u32 s70, s70, 0\n\t" \
    "s_cmp_eq_u32 s18, s28\n\t" \
    "s_addc_u32 s70, s70, 0\n\t" \
    "s_cmp_eq_u32 s20, s28\n\t" \
    "s_addc_u32 s70, s70, 0\n\t" \
    "s_cmp_eq_u32 s22, s28\n\t" \
    "s_addc_u32 s70, s70, 0\n\t" \
    "s_cmp_eq_u32 s24, s28\n\t" \
    "s_addc_u32 s70, s70, 0\n\t" \
    "s_cmp_eq_u32 s26, s28\n\t" \
    "s_addc_u32 s70, s70, 0\n\t" \
    "s_mov_b32 s75, s73\n\t" \
    "s_mov_b32 s76, s74\n\t" \
    "s_lshl_b32 s70, s70, 3\n\t" \
    "s_sub_u32 s73, s56, s70\n\t" \
    "s_lshl_b32 s74, s28, 4\n\t" \
    "s_sub_u32 s69, s73, s75\n\t" \
    "s_sub_u32 s70, s74, s76\n\t" \
    "s_sub_u32 s71, s70, 1600\n\t" \
    "s_cmp_lt_u32 s71, 400000\n\t" \
    "s_cbranch_scc0 qk_K9_%=\n\t" \
    "v_cvt_f32_u32 v160, s69\n\t" \
    "v_cvt_f32_u32 v161, s70\n\t" \
    "v_rcp_f32 v162, v160\n\t" \
    "v_rcp_f32 v163, v161\n\t" \
    "v_mul_f32 v162, v162, v161\n\t" \
    "v_mul_f32 v163, v163, v160\n\t" \
    "v_mul_f32 v162, 0x4f800000, v162\n\t" \
    "v_mul_f32 v163, 0x4f000000, v163\n\t" \
    "v_cvt_u32_f32 v162, v162\n\t" \
    "v_cvt_u32_f32 v163, v163\n\t" \
    "v_readfirstlane_b32 s69, v162\n\t" \
    "v_readfirstlane_b32 s70, v163\n\t" \
    "s_nop 1\n\t" \
    "s_mov_b32 s78, s69\n\t" \
    "s_mov_b32 s77, s70\n\t" \
    "qk_K9_%=:\n\t" \
    "s_mov_b32 s79, s73\n\t" \
    "s_add_u32 s80, s74, 2400\n\t" \
    "s_cmp_eq_u32 s66, 1\n\t" \
    "s_cbranch_scc1 qk_loop11_%=\n\t" \
    "s_cmp_eq_u32 s66, 2\n\t" \
    "s_cbranch_scc1 qk_loop12_%=\n\t" \
    "s_cmp_eq_u32 s66, 3\n\t" \
    "s_cbranch_scc1 qk_loop13_%=\n\t" \
    "qk_loop10_%=:\n\t" \
    "v_mov_b32 v72, 0\n\t" \
    "v_mov_b32 v73, 0\n\t" \
    "v_mov_b32 v74, 0\n\t" \
    "v_mov_b32 v75, 0\n\t" \
    "v_mov_b32 v76, 0\n\t" \
    "v_mov_b32 v77, 0\n\t" \
    "v_mov_b32 v78, 0\n\t" \
    "v_mov_b32 v79, 0\n\t" \
    "v_mov_b32 v80, 0\n\t" \
    "v_mov_b32 v81, 0\n\t" \
    "v_mov_b32 v82, 0\n\t" \
    "v_mov_b32 v83, 0\n\t" \
    "v_mov_b32 v84, 0\n\t" \
    "v_mov_b32 v85, 0\n\t" \
    "v_mov_b32 v86, 0\n\t" \
    "v_mov_b32 v87, 0\n\t" \
    "v_mov_b32 v88, 0\n\t" \
    "v_mov_b32 v89, 0\n\t" \
    "v_mov_b32 v90, 0\n\t" \
    "v_mov_b32 v91, 0\n\t" \
    "v_mov_b32 v92, 0\n\t" \
    "v_mov_b32 v93, 0\n\t" \
    "v_mov_b32 v94, 0\n\t" \
    "v_mov_b32 v95, 0\n\t" \
    "v_mov_b32 v96, 0\n\t" \
    "v_mov_b32 v97, 0\n\t" \
    "v_mov_b32 v98, 0\n\t" \
    "v_mov_b32 v99, 0\n\t" \
    "v_add_u32 v31, 1, v31\n\t" \
    "s_add_u32 s69, s65, 0\n\t" \
    "s_bitcmp1_b32 s69, 0\n\t" \
    "s_cselect_b32 s70, s63, s62\n\t" \
    "s_bfe_u32 s69, s69, 0x20001\n\t" \
    "s_lshl_b32 s69, s69, 19\n\t" \
    "s_add_u32 s69, s69, s70\n\t" \
    "s_add_u32 s46, s36, s69\n\t" \
    "s_addc_u32 s47, s37, 0\n\t" \
    "s_mov_b64 s[42:43], s[46:47]\n\t" \
    "s_add_u32 s69, s65, 0\n\t" \
    "s_lshr_b32 s69, s69, 1\n\t" \
    "s_xor_b32 s69, s69, s97\n\t" \
    "s_and_b32 s69, s69, 1\n\t" \
    "s_lshl_b32 s69, s69, 14\n\t" \
    "s_or_b32 s46, s46, s69\n\t" \
    "s_add_u32 s69, s65, 3\n\t" \
    "s_cmp_ge_i32 s83, s69\n\t" \
    "s_cselect_b64 vcc, -1, 0\n\t" \
    "v_mov_b32 v9, 6233984\n\t" \
    "v_cndmask_b32 v9, v24, v9, vcc\n\t" \
    "s_mul_i32 s70, s67, 7\n\t" \
    "s_add_u32 s69, s65, 1\n\t" \
    "s_xor_b32 s69, s69, s70\n\t" \
    "s_and_b32 s69, s69, 63\n\t" \
    "s_lshl_b32 s69, s69, 13\n\t" \
    "s_mul_i32 s70, s67, 524288\n\t" \
    "s_add_u32 s69, s69, s70\n\t" \
    "s_add_u32 s69, s69, 23068672\n\t" \
    "s_add_u32 s38, s36, s69\n\t" \
    "s_addc_u32 s39, s37, 0\n\t" \
    "qk_go19_%=:\n\t" \
    "s_mov_b32 s70, s79\n\t" \
    "s_sub_u32 s70, s70, 100\n\t" \
    "s_getpc_b64 s[30:31]\n\t" \
    "qk_Wp21_%=:\n\t" \
    "s_add_u32 s30, s30, qk_We22_%=-qk_Wp21_%=\n\t" \
    "s_addc_u32 s31, s31, 0\n\t" \
    "qk_W20_%=:\n\t" \
    "s_memtime s[56:57]\n\t" \
    "s_waitcnt lgkmcnt(0)\n\t" \
    "s_sub_u32 s69, s70, s56\n\t" \
    "s_cmp_gt_i32 s69, 111\n\t" \
    "s_cbranch_scc1 qk_W20_%=\n\t" \
    "s_max_i32 s69, s69, 0\n\t" \
    "s_lshr_b32 s69, s69, 1\n\t" \
    "s_and_b32 s69, s69, 0x3c\n\t" \
    "s_sub_u32 s30, s30, s69\n\t" \
    "s_subb_u32 s31, s31, 0\n\t" \
    "s_setpc_b64 s[30:31]\n\t" \
    ".p2align 6\n\t" \
    "qk_al23_%=:\n\t" \
    "s_nop 0\n\t" \
    "s_nop 1\n\t" \
    "s_nop 1\n\t" \
    "s_nop 1\n\t" \
    "s_nop 1\n\t" \
    "s_nop 1\n\t" \
    "s_nop 1\n\t" \
    "s_nop 1\n\t" \
    "s_nop 1\n\t" \
    "s_nop 1\n\t" \
    "s_nop 1\n\t" \
    "s_nop 1\n\t" \
    "s_nop 1\n\t" \
    "s_nop 1\n\t" \
    "qk_We22_%=:\n\t" \
    "qk_G24_%=:\n\t" \
    "global_load_dwordx4 v[36:39], v1, s[46:47] sc1\n\t" \
    "s_nop 9\n\t" \
    "global_load_dwordx4 v[40:43], v1, s[46:47] offset:1024 sc1\n\t" \
    "s_nop 9\n\t" \
    "global_load_dwordx4 v[44:47], v2, s[46:47] sc1\n\t" \
    "s_waitcnt vmcnt(2)\n\t" \
    "v_smfmac_f32_16x16x64_bf16 v[72:75], v[36:39], a[0:7], v10\n\t" \
    "ds_write_b128 v0, v[36:39] offset:0\n\t" \
    "v_smfmac_f32_16x16x64_bf16 v[76:79], v[36:39], a[72:79], v10\n\t" \
    "v_max3_u32 v160, v36, v37, v38\n\t" \
    "v_smfmac_f32_16x16x64_bf16 v[80:83], v[36:39], a[144:151], v10\n\t" \
    "v_smfmac_f32_16x16x64_bf16 v[84:87], v[36:39], a[216:223], v10\n\t" \
    "s_waitcnt vmcnt(1)\n\t" \
    "ds_write_b128 v0, v[40:43] offset:1024\n\t" \
    "v_smfmac_f32_16x16x64_bf16 v[72:75], v[40:43], a[8:15], v10\n\t" \
    "v_max3_u32 v160, v160, v39, v40\n\t" \
    "v_max3_u32 v160, v160, v41, v42\n\t" \
    "v_smfmac_f32_16x16x64_bf16 v[76:79], v[40:43], a[80:87], v10\n\t" \
    "v_max_u32 v160, v160, v43\n\t" \
    "v_cmp_eq_u32 vcc, -1, v160\n\t" \
    "v_smfmac_f32_16x16x64_bf16 v[80:83], v[40:43], a[152:159], v10\n\t" \
    "v_smfmac_f32_16x16x64_bf16 v[84:87], v[40:43], a[224:231], v10\n\t" \
    "s_cbranch_vccnz qk_Gbad25_%=\n\t" \
    "qk_Gc27_%=:\n\t" \
    "s_waitcnt lgkmcnt(0)\n\t" \
    "s_barrier\n\t" \
    "ds_read_b128 v[48:51], v0 offset:2048\n\t" \
    "s_waitcnt vmcnt(0)\n\t" \
    "v_smfmac_f32_16x16x64_bf16 v[72:75], v[44:47], a[64:71], v10\n\t" \
    "v_max3_u32 v160, v44, v45, v46\n\t" \
    "v_smfmac_f32_16x16x64_bf16 v[76:79], v[44:47], a[136:143], v10\n\t" \
    "ds_read_b128 v[52:55], v0 offset:3072\n\t" \
    "v_max_u32 v160, v160, v47\n\t" \
    "v_smfmac_f32_16x16x64_bf16 v[80:83], v[44:47], a[208:215], v10\n\t" \
    "v_cmp_eq_u32 vcc, -1, v160\n\t" \
    "v_smfmac_f32_16x16x64_bf16 v[84:87], v[44:47], v[248:255], v10\n\t" \
    "ds_read_b128 v[56:59], v0 offset:4096\n\t" \
    "s_cbranch_vccnz qk_Gbad226_%=\n\t" \
    "qk_Gc228_%=:\n\t" \
    "s_waitcnt lgkmcnt(2)\n\t" \
    "v_smfmac_f32_16x16x64_bf16 v[72:75], v[48:51], a[16:23], v10\n\t" \
    "ds_read_b128 v[60:63], v0 offset:5120\n\t" \
    "v_smfmac_f32_16x16x64_bf16 v[76:79], v[48:51], a[88:95], v10\n\t" \
    "ds_read_b128 v[64:67], v0 offset:6144\n\t" \
    "v_smfmac_f32_16x16x64_bf16 v[80:83], v[48:51], a[160:167], v10\n\t" \
    "ds_read_b128 v[68:71], v0 offset:7168\n\t" \
    "global_load_dword v25, v9, s[36:37] sc1\n\t" \
    "v_smfmac_f32_16x16x64_bf16 v[84:87], v[48:51], a[232:239], v10\n\t" \
    "s_waitcnt lgkmcnt(4)\n\t" \
    "v_smfmac_f32_16x16x64_bf16 v[72:75], v[52:55], a[24:31], v10\n\t" \
    "s_waitcnt lgkmcnt(3)\n\t" \
    "v_smfmac_f32_16x16x64_bf16 v[72:75], v[56:59], a[32:39], v10\n\t" \
    "s_waitcnt lgkmcnt(2)\n\t" \
    "v_smfmac_f32_16x16x64_bf16 v[72:75], v[60:63], a[40:47], v10\n\t" \
    "s_waitcnt lgkmcnt(1)\n\t" \
    "v_smfmac_f32_16x16x64_bf16 v[72:75], v[64:67], a[48:55], v10\n\t" \
    "s_waitcnt lgkmcnt(0)\n\t" \
    "v_smfmac_f32_16x16x64_bf16 v[72:75], v[68:71], a[56:63], v10\n\t" \
    "v_smfmac_f32_16x16x64_bf16 v[76:79], v[52:55], a[96:103], v10\n\t" \
    "s_nop 1\n\t" \
    "s_nop 0\n\t" \
    "v_smfmac_f32_16x16x64_bf16 v[76:79], v[56:59], a[104:111], v10\n\t" \
    "s_nop 1\n\t" \
    "s_nop 0\n\t" \
    "v_smfmac_f32_16x16x64_bf16 v[76:79], v[60:63], a[112:119], v10\n\t" \
    "v_add_f32 v72, v72, v74\n\t" \
    "v_smfmac_f32_16x16x64_bf16 v[76:79], v[64:67], a[120:127], v10\n\t" \
    "v_add_f32 v73, v73, v75\n\t" \
    "v_smfmac_f32_16x16x64_bf16 v[76:79], v[68:71], a[128:135], v10\n\t" \
    "v_cvt_pk_bf16_f32 v156, v72, v73\n\t" \
    "v_smfmac_f32_16x16x64_bf16 v[80:83], v[52:55], a[168:175], v10\n\t" \
    "s_nop 1\n\t" \
    "ds_write_b32 v12, v156 offset:0\n\t" \
    "v_smfmac_f32_16x16x64_bf16 v[80:83], v[56:59], a[176:183], v10\n\t" \
    "s_nop 1\n\t" \
    "s_nop 0\n\t" \
    "v_smfmac_f32_16x16x64_bf16 v[80:83], v[60:63], a[184:191], v10\n\t" \
    "v_add_f32 v76, v76, v78\n\t" \
    "v_smfmac_f32_16x16x64_bf16 v[80:83], v[64:67], a[192:199], v10\n\t" \
    "v_add_f32 v77, v77, v79\n\t" \
    "v_smfmac_f32_16x16x64_bf16 v[80:83], v[68:71], a[200:207], v10\n\t" \
    "v_cvt_pk_bf16_f32 v157, v76, v77\n\t" \
    "v_smfmac_f32_16x16x64_bf16 v[84:87], v[52:55], a[240:247], v10\n\t" \
    "s_nop 1\n\t" \
    "ds_write_b32 v12, v157 offset:64\n\t" \
    "v_smfmac_f32_16x16x64_bf16 v[84:87], v[56:59], a[248:255], v10\n\t" \
    "s_nop 1\n\t" \
    "s_nop 0\n\t" \
    "v_smfmac_f32_16x16x64_bf16 v[84:87], v[60:63], v[224:231], v10\n\t" \
    "v_add_f32 v80, v80, v82\n\t" \
    "v_smfmac_f32_16x16x64_bf16 v[84:87], v[64:67], v[232:239], v10\n\t" \
    "v_add_f32 v81, v81, v83\n\t" \
    "v_smfmac_f32_16x16x64_bf16 v[84:87], v[68:71], v[240:247], v10\n\t" \
    "v_cvt_pk_bf16_f32 v158, v80, v81\n\t" \
    "ds_write_b32 v16, v158 offset:0\n\t" \
    "s_nop 5\n\t" \
    "v_pk_add_f32 v[84:85], v[84:85], v[86:87]\n\t" \
    "v_cvt_pk_bf16_f32 v159, v84, v85\n\t" \
    "ds_write_b32 v16, v159 offset:256\n\t" \
    "s_cmp_eq_u32 s65, 0\n\t" \
    "s_cbranch_scc1 qk_k017_%=\n\t" \
    "qk_k0b18_%=:\n\t" \
    "s_waitcnt lgkmcnt(0)\n\t" \
    "s_barrier\n\t" \
    "ds_read_b64_tr_b16 v[132:133], v13 offset:0\n\t" \
    "ds_read_b64_tr_b16 v[134:135], v13 offset:128\n\t" \
    "ds_read_b64_tr_b16 v[136:137], v13 offset:1024\n\t" \
    "ds_read_b64_tr_b16 v[138:139], v13 offset:1152\n\t" \
    "ds_read_b64_tr_b16 v[140:141], v17 offset:0\n\t" \
    "ds_read_b64_tr_b16 v[142:143], v17 offset:256\n\t" \
    "ds_read_b64_tr_b16 v[144:145], v17 offset:512\n\t" \
    "ds_read_b64_tr_b16 v[146:147], v17 offset:768\n\t" \
    "ds_read_b64_tr_b16 v[148:149], v17 offset:1024\n\t" \
    "ds_read_b64_tr_b16 v[150:151], v17 offset:1280\n\t" \
    "ds_read_b64_tr_b16 v[152:153], v17 offset:1536\n\t" \
    "ds_read_b64_tr_b16 v[154:155], v17 offset:1792\n\t" \
    "s_waitcnt lgkmcnt(10)\n\t" \
    "v_smfmac_f32_16x16x64_bf16 v[88:91], v[132:135], v[100:107], v10\n\t" \
    "v_smfmac_f32_16x16x64_bf16 v[92:95], v[132:135], v[116:123], v10\n\t" \
    "s_waitcnt lgkmcnt(8)\n\t" \
    "v_smfmac_f32_16x16x64_bf16 v[88:91], v[136:139], v[108:115], v10\n\t" \
    "v_smfmac_f32_16x16x64_bf16 v[92:95], v[136:139], v[124:131], v10\n\t" \
    "s_waitcnt lgkmcnt(4)\n\t" \
    "v_smfmac_f32_16x16x64_bf16 v[96:99], v[132:135], v[140:147], v10\n\t" \
    "s_waitcnt lgkmcnt(0)\n\t" \
    "s_nop 1\n\t" \
    "v_smfmac_f32_16x16x64_bf16 v[96:99], v[136:139], v[148:155], v10\n\t" \
    "s_nop 0\n\t" \
    "v_add_f32 v168, v88, v90\n\t" \
    "v_add_f32 v169, v92, v94\n\t" \
    "v_max_f32 v178, v168, v169\n\t" \
    "v_mul_f32 v168, v168, v29\n\t" \
    "v_mul_f32 v169, v169, v29\n\t" \
    "v_max_f32_dpp v178, v178, v178 row_mirror row_mask:0xf bank_mask:0xf\n\t" \
    "s_nop 0\n\t" \
    "v_add_f32 v172, v96, v98\n\t" \
    "v_max_f32 v173, v178, v172\n\t" \
    "v_mul_f32 v172, v172, v29\n\t" \
    "s_nop 0\n\t" \
    "v_max_f32_dpp v173, v173, v173 quad_perm:[1,0,3,2] row_mask:0xf bank_mask:0xf\n\t" \
    "s_nop 1\n\t" \
    "v_max_f32_dpp v173, v173, v173 quad_perm:[2,3,0,1] row_mask:0xf bank_mask:0xf\n\t" \
    "s_nop 1\n\t" \
    "v_max_f32_dpp v173, v173, v173 row_half_mirror row_mask:0xf bank_mask:0xf\n\t" \
    "v_fma_f32 v172, -v173, v29, v172\n\t" \
    "v_fma_f32 v168, -v173, v29, v168\n\t" \
    "v_fma_f32 v169, -v173, v29, v169\n\t" \
    "v_exp_f32 v172, v172\n\t" \
    "v_exp_f32 v168, v168\n\t" \
    "v_exp_f32 v169, v169\n\t" \
    "v_fma_f32 v173, v172, 0.5, v168\n\t" \
    "v_add_f32 v173, v173, v169\n\t" \
    "v_mov_b32_dpp v174, v172 quad_perm:[0,0,2,2] row_mask:0xf bank_mask:0xf\n\t" \
    "v_mov_b32_dpp v175, v172 quad_perm:[1,1,3,3] row_mask:0xf bank_mask:0xf\n\t" \
    "v_add_f32_dpp v173, v173, v173 quad_perm:[1,0,3,2] row_mask:0xf bank_mask:0xf\n\t" \
    "v_mov_b32_dpp v176, v168 quad_perm:[1,0,3,2] row_mask:0xf bank_mask:0xf\n\t" \
    "v_mov_b32_dpp v177, v169 quad_perm:[1,0,3,2] row_mask:0xf bank_mask:0xf\n\t" \
    "v_add_f32_dpp v173, v173, v173 quad_perm:[2,3,0,1] row_mask:0xf bank_mask:0xf\n\t" \
    "v_cndmask_b32 v168, v168, v177, s[48:49]\n\t" \
    "v_cndmask_b32 v169, v176, v169, s[48:49]\n\t" \
    "v_add_f32_dpp v173, v173, v173 row_half_mirror row_mask:0xf bank_mask:0xf\n\t" \
    "s_nop 1\n\t" \
    "v_add_f32_dpp v173, v173, v173 row_mirror row_mask:0xf bank_mask:0xf\n\t" \
    "v_rcp_f32 v173, v173\n\t" \
    "s_nop 0\n\t" \
    "v_pk_mul_f32 v[168:169], v[168:169], v[172:173] op_sel:[0,1] op_sel_hi:[1,1]\n\t" \
    "v_pk_mul_f32 v[174:175], v[174:175], v[172:173] op_sel:[0,1] op_sel_hi:[1,1]\n\t" \
    "v_cvt_pk_bf16_f32 v176, v168, v169\n\t" \
    "global_store_dword v18, v176, s[42:43] sc1\n\t" \
    "global_store_dword v3, v176, s[42:43] sc1\n\t" \
    "v_cvt_pk_bf16_f32 v177, v174, v175\n\t" \
    "global_store_dword v19, v177, s[42:43] sc1\n\t" \
    "global_store_dword v4, v177, s[42:43] sc1\n\t" \
    "s_cmp_eq_u32 s65, 63\n\t" \
    "s_cbranch_scc1 qk_last14_%=\n\t" \
    "s_memtime s[54:55]\n\t" \
    "s_waitcnt vmcnt(4) lgkmcnt(0)\n\t" \
    "s_add_u32 s79, s54, 728\n\t" \
    "v_readfirstlane_b32 s69, v25\n\t" \
    "s_add_u32 s71, s65, 1\n\t" \
    "s_max_i32 s83, s83, s69\n\t" \
    "qk_WF30_%=:\n\t" \
    "s_cmp_ge_i32 s83, s71\n\t" \
    "s_cbranch_scc1 qk_WFok31_%=\n\t" \
    "global_load_dword v160, v24, s[36:37] sc1\n\t" \
    "s_waitcnt vmcnt(0)\n\t" \
    "v_readfirstlane_b32 s83, v160\n\t" \
    "s_nop 1\n\t" \
    "s_cmp_ge_i32 s83, s71\n\t" \
    "s_cbranch_scc1 qk_WFok31_%=\n\t" \
    "s_cmp_eq_u32 s68, 0\n\t" \
    "s_cbranch_scc1 qk_F32_%=\n\t" \
    "s_sub_u32 s68, s68, 1\n\t" \
    "s_sleep 2\n\t" \
    "s_branch qk_WF30_%=\n\t" \
    "qk_F32_%=:\n\t" \
    "v_mov_b32 v160, 6095104\n\t" \
    "v_mov_b32 v161, 9\n\t" \
    "global_store_dword v160, v161, s[36:37] sc1\n\t" \
    "s_waitcnt vmcnt(0)\n\t" \
    "s_branch qk_WFok31_%=\n\t" \
    "qk_WFok31_%=:\n\t" \
    "v_mov_b32 v72, 0\n\t" \
    "v_mov_b32 v73, 0\n\t" \
    "v_mov_b32 v74, 0\n\t" \
    "v_mov_b32 v75, 0\n\t" \
    "v_mov_b32 v76, 0\n\t" \
    "v_mov_b32 v77, 0\n\t" \
    "v_mov_b32 v78, 0\n\t" \
    "v_mov_b32 v79, 0\n\t" \
    "v_mov_b32 v80, 0\n\t" \
    "v_mov_b32 v81, 0\n\t" \
    "v_mov_b32 v82, 0\n\t" \
    "v_mov_b32 v83, 0\n\t" \
    "v_mov_b32 v84, 0\n\t" \
    "v_mov_b32 v85, 0\n\t" \
    "v_mov_b32 v86, 0\n\t" \
    "v_mov_b32 v87, 0\n\t" \
    "v_mov_b32 v88, 0\n\t" \
    "v_mov_b32 v89, 0\n\t" \
    "v_mov_b32 v90, 0\n\t" \
    "v_mov_b32 v91, 0\n\t" \
    "v_mov_b32 v92, 0\n\t" \
    "v_mov_b32 v93, 0\n\t" \
    "v_mov_b32 v94, 0\n\t" \
    "v_mov_b32 v95, 0\n\t" \
    "v_mov_b32 v96, 0\n\t" \
    "v_mov_b32 v97, 0\n\t" \
    "v_mov_b32 v98, 0\n\t" \
    "v_mov_b32 v99, 0\n\t" \
    "s_mov_b32 s70, s79\n\t" \
    "qk_W33_%=:\n\t" \
    "s_memtime s[56:57]\n\t" \
    "s_waitcnt lgkmcnt(0)\n\t" \
    "s_sub_u32 s69, s70, s56\n\t" \
    "s_cmp_gt_i32 s69, 24\n\t" \
    "s_cbranch_scc1 qk_W33_%=\n\t" \
    "global_load_dwordx4 v[100:103], v23, s[38:39] offset:0 sc1\n\t" \
    "global_load_dwordx4 v[104:107], v23, s[38:39] offset:2048 sc1\n\t" \
    "global_load_dwordx4 v[108:111], v15, s[38:39] offset:0 sc1\n\t" \
    "global_load_dwordx4 v[112:115], v15, s[38:39] offset:2048 sc1\n\t" \
    "global_load_dwordx4 v[116:119], v23, s[38:39] offset:1024 sc1\n\t" \
    "global_load_dwordx4 v[120:123], v23, s[38:39] offset:3072 sc1\n\t" \
    "global_load_dwordx4 v[124:127], v15, s[38:39] offset:1024 sc1\n\t" \
    "global_load_dwordx4 v[128:131], v15, s[38:39] offset:3072 sc1\n\t" \
    "s_add_u32 s65, s65, 1\n\t" \
    "v_add_u32 v31, 1, v31\n\t" \
    "s_add_u32 s69, s65, 0\n\t" \
    "s_bitcmp1_b32 s69, 0\n\t" \
    "s_cselect_b32 s70, s63, s62\n\t" \
    "s_bfe_u32 s69, s69, 0x20001\n\t" \
    "s_lshl_b32 s69, s69, 19\n\t" \
    "s_add_u32 s69, s69, s70\n\t" \
    "s_add_u32 s46, s36, s69\n\t" \
    "s_addc_u32 s47, s37, 0\n\t" \
    "s_mov_b64 s[42:43], s[46:47]\n\t" \
    "s_add_u32 s69, s65, 0\n\t" \
    "s_lshr_b32 s69, s69, 1\n\t" \
    "s_xor_b32 s69, s69, s97\n\t" \
    "s_and_b32 s69, s69, 1\n\t" \
    "s_lshl_b32 s69, s69, 14\n\t" \
    "s_or_b32 s46, s46, s69\n\t" \
    "s_add_u32 s69, s65, 3\n\t" \
    "s_cmp_ge_i32 s83, s69\n\t" \
    "s_cselect_b64 vcc, -1, 0\n\t" \
    "v_mov_b32 v9, 6233984\n\t" \
    "v_cndmask_b32 v9, v24, v9, vcc\n\t" \
    "s_mul_i32 s70, s67, 7\n\t" \
    "s_add_u32 s69, s65, 1\n\t" \
    "s_xor_b32 s69, s69, s70\n\t" \
    "s_and_b32 s69, s69, 63\n\t" \
    "s_lshl_b32 s69, s69, 13\n\t" \
    "s_mul_i32 s70, s67, 524288\n\t" \
    "s_add_u32 s69, s69, s70\n\t" \
    "s_add_u32 s69, s69, 23068672\n\t" \
    "s_add_u32 s38, s36, s69\n\t" \
    "s_addc_u32 s39, s37, 0\n\t" \
    "s_waitcnt lgkmcnt(0)\n\t" \
    "s_barrier\n\t" \
    "ds_read_b32 v160, v11 offset:20480\n\t" \
    "s_waitcnt lgkmcnt(0)\n\t" \
    "v_readfirstlane_b32 s79, v160\n\t" \
    "s_nop 1\n\t" \
    "s_branch qk_go19_%=\n\t" \
    "qk_Gbad25_%=:\n\t" \
    "s_waitcnt vmcnt(0) lgkmcnt(0)\n\t" \
    "s_nop 7\n\t" \
    "v_mov_b32 v72, 0\n\t" \
    "v_mov_b32 v73, 0\n\t" \
    "v_mov_b32 v74, 0\n\t" \
    "v_mov_b32 v75, 0\n\t" \
    "v_mov_b32 v76, 0\n\t" \
    "v_mov_b32 v77, 0\n\t" \
    "v_mov_b32 v78, 0\n\t" \
    "v_mov_b32 v79, 0\n\t" \
    "v_mov_b32 v80, 0\n\t" \
    "v_mov_b32 v81, 0\n\t" \
    "v_mov_b32 v82, 0\n\t" \
    "v_mov_b32 v83, 0\n\t" \
    "v_mov_b32 v84, 0\n\t" \
    "v_mov_b32 v85, 0\n\t" \
    "v_mov_b32 v86, 0\n\t" \
    "v_mov_b32 v87, 0\n\t" \
    "s_cmp_eq_u32 s68, 0\n\t" \
    "s_cbranch_scc1 qk_F34_%=\n\t" \
    "s_sub_u32 s68, s68, 1\n\t" \
    "s_branch qk_G24_%=\n\t" \
    "qk_F34_%=:\n\t" \
    "v_mov_b32 v160, 6095104\n\t" \
    "v_mov_b32 v161, 1\n\t" \
    "global_store_dword v160, v161, s[36:37] sc1\n\t" \
    "s_waitcnt vmcnt(0)\n\t" \
    "s_branch qk_Gc27_%=\n\t" \
    "qk_Gbad226_%=:\n\t" \
    "global_load_dwordx4 v[44:47], v2, s[46:47] sc1\n\t" \
    "s_waitcnt vmcnt(0)\n\t" \
    "v_max3_u32 v160, v44, v45, v46\n\t" \
    "v_max_u32 v160, v160, v47\n\t" \
    "v_cmp_eq_u32 vcc, -1, v160\n\t" \
    "s_cbranch_vccz qk_Gre229_%=\n\t" \
    "s_cmp_eq_u32 s68, 0\n\t" \
    "s_cbranch_scc1 qk_F35_%=\n\t" \
    "s_sub_u32 s68, s68, 1\n\t" \
    "s_sleep 1\n\t" \
    "s_branch qk_Gbad226_%=\n\t" \
    "qk_F35_%=:\n\t" \
    "v_mov_b32 v160, 6095104\n\t" \
    "v_mov_b32 v161, 2\n\t" \
    "global_store_dword v160, v161, s[36:37] sc1\n\t" \
    "s_waitcnt vmcnt(0)\n\t" \
    "s_branch qk_Gre229_%=\n\t" \
    "qk_Gre229_%=:\n\t" \
    "v_mov_b32 v72, 0\n\t" \
    "v_mov_b32 v73, 0\n\t" \
    "v_mov_b32 v74, 0\n\t" \
    "v_mov_b32 v75, 0\n\t" \
    "v_mov_b32 v76, 0\n\t" \
    "v_mov_b32 v77, 0\n\t" \
    "v_mov_b32 v78, 0\n\t" \
    "v_mov_b32 v79, 0\n\t" \
    "v_mov_b32 v80, 0\n\t" \
    "v_mov_b32 v81, 0\n\t" \
    "v_mov_b32 v82, 0\n\t" \
    "v_mov_b32 v83, 0\n\t" \
    "v_mov_b32 v84, 0\n\t" \
    "v_mov_b32 v85, 0\n\t" \
    "v_mov_b32 v86, 0\n\t" \
    "v_mov_b32 v87, 0\n\t" \
    "v_smfmac_f32_16x16x64_bf16 v[72:75], v[36:39], a[0:7], v10\n\t" \
    "v_smfmac_f32_16x16x64_bf16 v[76:79], v[36:39], a[72:79], v10\n\t" \
    "v_smfmac_f32_16x16x64_bf16 v[80:83], v[36:39], a[144:151], v10\n\t" \
    "v_smfmac_f32_16x16x64_bf16 v[84:87], v[36:39], a[216:223], v10\n\t" \
    "v_smfmac_f32_16x16x64_bf16 v[72:75], v[40:43], a[8:15], v10\n\t" \
    "v_smfmac_f32_16x16x64_bf16 v[76:79], v[40:43], a[80:87], v10\n\t" \
    "v_smfmac_f32_16x16x64_bf16 v[80:83], v[40:43], a[152:159], v10\n\t" \
    "v_smfmac_f32_16x16x64_bf16 v[84:87], v[40:43], a[224:231], v10\n\t" \
    "v_smfmac_f32_16x16x64_bf16 v[72:75], v[44:47], a[64:71], v10\n\t" \
    "v_smfmac_f32_16x16x64_bf16 v[76:79], v[44:47], a[136:143], v10\n\t" \
    "v_smfmac_f32_16x16x64_bf16 v[80:83], v[44:47], a[208:215], v10\n\t" \
    "v_smfmac_f32_16x16x64_bf16 v[84:87], v[44:47], v[248:255], v10\n\t" \
    "s_branch qk_Gc228_%=\n\t" \
    "qk_k017_%=:\n\t" \
    "qk_WF36_%=:\n\t" \
    "s_cmp_ge_i32 s83, s65\n\t" \
    "s_cbranch_scc1 qk_WFok37_%=\n\t" \
    "global_load_dword v160, v24, s[36:37] sc1\n\t" \
    "s_waitcnt vmcnt(0)\n\t" \
    "v_readfirstlane_b32 s83, v160\n\t" \
    "s_nop 1\n\t" \
    "s_cmp_ge_i32 s83, s65\n\t" \
    "s_cbranch_scc1 qk_WFok37_%=\n\t" \
    "s_cmp_eq_u32 s68, 0\n\t" \
    "s_cbranch_scc1 qk_F38_%=\n\t" \
    "s_sub_u32 s68, s68, 1\n\t" \
    "s_sleep 2\n\t" \
    "s_branch qk_WF36_%=\n\t" \
    "qk_F38_%=:\n\t" \
    "v_mov_b32 v160, 6095104\n\t" \
    "v_mov_b32 v161, 8\n\t" \
    "global_store_dword v160, v161, s[36:37] sc1\n\t" \
    "s_waitcnt vmcnt(0)\n\t" \
    "s_branch qk_WFok37_%=\n\t" \
    "qk_WFok37_%=:\n\t" \
    "s_memrealtime s[54:55]\n\t" \
    "s_waitcnt lgkmcnt(0)\n\t" \
    "s_lshl_b32 s69, s54, 4\n\t" \
    "s_add_u32 s69, s69, 1280\n\t" \
    "v_mov_b32 v160, s69\n\t" \
    "global_load_dwordx4 v[100:103], v23, s[50:51] offset:0 sc1\n\t" \
    "global_load_dwordx4 v[104:107], v23, s[50:51] offset:2048 sc1\n\t" \
    "global_load_dwordx4 v[108:111], v15, s[50:51] offset:0 sc1\n\t" \
    "global_load_dwordx4 v[112:115], v15, s[50:51] offset:2048 sc1\n\t" \
    "global_load_dwordx4 v[116:119], v23, s[50:51] offset:1024 sc1\n\t" \
    "global_load_dwordx4 v[120:123], v23, s[50:51] offset:3072 sc1\n\t" \
    "global_load_dwordx4 v[124:127], v15, s[50:51] offset:1024 sc1\n\t" \
    "global_load_dwordx4 v[128:131], v15, s[50:51] offset:3072 sc1\n\t" \
    "s_mov_b64 exec, 0xff000\n\t" \
    "global_store_dword v21, v160, s[42:43] sc1\n\t" \
    "s_mov_b64 exec, -1\n\t" \
    "s_waitcnt vmcnt(1)\n\t" \
    "s_branch qk_k0b18_%=\n\t" \
    "qk_last14_%=:\n\t" \
    "s_endpgm\n\t" \
    "qk_loop11_%=:\n\t" \
    "v_mov_b32 v72, 0\n\t" \
    "v_mov_b32 v73, 0\n\t" \
    "v_mov_b32 v74, 0\n\t" \
    "v_mov_b32 v75, 0\n\t" \
    "v_mov_b32 v76, 0\n\t" \
    "v_mov_b32 v77, 0\n\t" \
    "v_mov_b32 v78, 0\n\t" \
    "v_mov_b32 v79, 0\n\t" \
    "v_mov_b32 v80, 0\n\t" \
    "v_mov_b32 v81, 0\n\t" \
    "v_mov_b32 v82, 0\n\t" \
    "v_mov_b32 v83, 0\n\t" \
    "v_mov_b32 v84, 0\n\t" \
    "v_mov_b32 v85, 0\n\t" \
    "v_mov_b32 v86, 0\n\t" \
    "v_mov_b32 v87, 0\n\t" \
    "v_mov_b32 v88, 0\n\t" \
    "v_mov_b32 v89, 0\n\t" \
    "v_mov_b32 v90, 0\n\t" \
    "v_mov_b32 v91, 0\n\t" \
    "v_mov_b32 v92, 0\n\t" \
    "v_mov_b32 v93, 0\n\t" \
    "v_mov_b32 v94, 0\n\t" \
    "v_mov_b32 v95, 0\n\t" \
    "v_mov_b32 v96, 0\n\t" \
    "v_mov_b32 v97, 0\n\t" \
    "v_mov_b32 v98, 0\n\t" \
    "v_mov_b32 v99, 0\n\t" \
    "v_add_u32 v31, 1, v31\n\t" \
    "s_add_u32 s69, s65, 0\n\t" \
    "s_bitcmp1_b32 s69, 0\n\t" \
    "s_cselect_b32 s70, s63, s62\n\t" \
    "s_bfe_u32 s69, s69, 0x20001\n\t" \
    "s_lshl_b32 s69, s69, 19\n\t" \
    "s_add_u32 s69, s69, s70\n\t" \
    "s_add_u32 s46, s36, s69\n\t" \
    "s_addc_u32 s47, s37, 0\n\t" \
    "s_mov_b64 s[42:43], s[46:47]\n\t" \
    "s_add_u32 s69, s65, 0\n\t" \
    "s_lshr_b32 s69, s69, 1\n\t" \
    "s_xor_b32 s69, s69, s97\n\t" \
    "s_and_b32 s69, s69, 1\n\t" \
    "s_lshl_b32 s69, s69, 14\n\t" \
    "s_or_b32 s46, s46, s69\n\t" \
    "s_add_u32 s69, s65, 3\n\t" \
    "s_cmp_ge_i32 s83, s69\n\t" \
    "s_cselect_b64 vcc, -1, 0\n\t" \
    "v_mov_b32 v9, 6233984\n\t" \
    "v_cndmask_b32 v9, v24, v9, vcc\n\t" \
    "s_mul_i32 s70, s67, 7\n\t" \
    "s_add_u32 s69, s65, 1\n\t" \
    "s_xor_b32 s69, s69, s70\n\t" \
    "s_and_b32 s69, s69, 63\n\t" \
    "s_lshl_b32 s69, s69, 13\n\t" \
    "s_mul_i32 s70, s67, 524288\n\t" \
    "s_add_u32 s69, s69, s70\n\t" \
    "s_add_u32 s69, s69, 23068672\n\t" \
    "s_add_u32 s38, s36, s69\n\t" \
    "s_addc_u32 s39, s37, 0\n\t" \
    "qk_go44_%=:\n\t" \
    "s_mov_b32 s70, s79\n\t" \
    "s_sub_u32 s70, s70, 100\n\t" \
    "s_getpc_b64 s[30:31]\n\t" \
    "qk_Wp46_%=:\n\t" \
    "s_add_u32 s30, s30, qk_We47_%=-qk_Wp46_%=\n\t" \
    "s_addc_u32 s31, s31, 0\n\t" \
    "qk_W45_%=:\n\t" \
    "s_memtime s[56:57]\n\t" \
    "s_waitcnt lgkmcnt(0)\n\t" \
    "s_sub_u32 s69, s70, s56\n\t" \
    "s_cmp_gt_i32 s69, 111\n\t" \
    "s_cbranch_scc1 qk_W45_%=\n\t" \
    "s_max_i32 s69, s69, 0\n\t" \
    "s_lshr_b32 s69, s69, 1\n\t" \
    "s_and_b32 s69, s69, 0x3c\n\t" \
    "s_sub_u32 s30, s30, s69\n\t" \
    "s_subb_u32 s31, s31, 0\n\t" \
    "s_setpc_b64 s[30:31]\n\t" \
    ".p2align 6\n\t" \
    "qk_al48_%=:\n\t" \
    "s_nop 0\n\t" \
    "s_nop 1\n\t" \
    "s_nop 1\n\t" \
    "s_nop 1\n\t" \
    "s_nop 1\n\t" \
    "s_nop 1\n\t" \
    "s_nop 1\n\t" \
    "s_nop 1\n\t" \
    "s_nop 1\n\t" \
    "s_nop 1\n\t" \
    "s_nop 1\n\t" \
    "s_nop 1\n\t" \
    "s_nop 1\n\t" \
    "s_nop 1\n\t" \
    "qk_We47_%=:\n\t" \
    "qk_G49_%=:\n\t" \
    "global_load_dwordx4 v[36:39], v1, s[46:47] sc1\n\t" \
    "s_nop 9\n\t" \
    "global_load_dwordx4 v[40:43], v1, s[46:47] offset:1024 sc1\n\t" \
    "s_nop 9\n\t" \
    "global_load_dwordx4 v[44:47], v2, s[46:47] sc1\n\t" \
    "s_waitcnt vmcnt(2)\n\t" \
    "v_smfmac_f32_16x16x64_bf16 v[72:75], v[36:39], a[16:23], v10\n\t" \
    "ds_write_b128 v0, v[36:39] offset:2048\n\t" \
    "v_smfmac_f32_16x16x64_bf16 v[76:79], v[36:39], a[88:95], v10\n\t" \
    "v_max3_u32 v160, v36, v37, v38\n\t" \
    "v_smfmac_f32_16x16x64_bf16 v[80:83], v[36:39], a[160:167], v10\n\t" \
    "v_smfmac_f32_16x16x64_bf16 v[84:87], v[36:39], a[232:239], v10\n\t" \
    "s_waitcnt vmcnt(1)\n\t" \
    "ds_write_b128 v0, v[40:43] offset:3072\n\t" \
    "v_smfmac_f32_16x16x64_bf16 v[72:75], v[40:43], a[24:31], v10\n\t" \
    "v_max3_u32 v160, v160, v39, v40\n\t" \
    "v_max3_u32 v160, v160, v41, v42\n\t" \
    "v_smfmac_f32_16x16x64_bf16 v[76:79], v[40:43], a[96:103], v10\n\t" \
    "v_max_u32 v160, v160, v43\n\t" \
    "v_cmp_eq_u32 vcc, -1, v160\n\t" \
    "v_smfmac_f32_16x16x64_bf16 v[80:83], v[40:43], a[168:175], v10\n\t" \
    "v_smfmac_f32_16x16x64_bf16 v[84:87], v[40:43], a[240:247], v10\n\t" \
    "s_cbranch_vccnz qk_Gbad50_%=\n\t" \
    "qk_Gc52_%=:\n\t" \
    "s_waitcnt lgkmcnt(0)\n\t" \
    "s_barrier\n\t" \
    "ds_read_b128 v[48:51], v0 offset:4096\n\t" \
    "s_waitcnt vmcnt(0)\n\t" \
    "v_smfmac_f32_16x16x64_bf16 v[72:75], v[44:47], a[64:71], v10\n\t" \
    "v_max3_u32 v160, v44, v45, v46\n\t" \
    "v_smfmac_f32_16x16x64_bf16 v[76:79], v[44:47], a[136:143], v10\n\t" \
    "ds_read_b128 v[52:55], v0 offset:5120\n\t" \
    "v_max_u32 v160, v160, v47\n\t" \
    "v_smfmac_f32_16x16x64_bf16 v[80:83], v[44:47], a[208:215], v10\n\t" \
    "v_cmp_eq_u32 vcc, -1, v160\n\t" \
    "v_smfmac_f32_16x16x64_bf16 v[84:87], v[44:47], v[248:255], v10\n\t" \
    "ds_read_b128 v[56:59], v0 offset:6144\n\t" \
    "s_cbranch_vccnz qk_Gbad251_%=\n\t" \
    "qk_Gc253_%=:\n\t" \
    "s_waitcnt lgkmcnt(2)\n\t" \
    "v_smfmac_f32_16x16x64_bf16 v[72:75], v[48:51], a[32:39], v10\n\t" \
    "ds_read_b128 v[60:63], v0 offset:7168\n\t" \
    "v_smfmac_f32_16x16x64_bf16 v[76:79], v[48:51], a[104:111], v10\n\t" \
    "ds_read_b128 v[64:67], v0 offset:0\n\t" \
    "v_smfmac_f32_16x16x64_bf16 v[80:83], v[48:51], a[176:183], v10\n\t" \
    "ds_read_b128 v[68:71], v0 offset:1024\n\t" \
    "global_load_dword v25, v9, s[36:37] sc1\n\t" \
    "v_smfmac_f32_16x16x64_bf16 v[84:87], v[48:51], a[248:255], v10\n\t" \
    "s_waitcnt lgkmcnt(4)\n\t" \
    "v_smfmac_f32_16x16x64_bf16 v[72:75], v[52:55], a[40:47], v10\n\t" \
    "s_waitcnt lgkmcnt(3)\n\t" \
    "v_smfmac_f32_16x16x64_bf16 v[72:75], v[56:59], a[48:55], v10\n\t" \
    "s_waitcnt lgkmcnt(2)\n\t" \
    "v_smfmac_f32_16x16x64_bf16 v[72:75], v[60:63], a[56:63], v10\n\t" \
    "s_waitcnt lgkmcnt(1)\n\t" \
    "v_smfmac_f32_16x16x64_bf16 v[72:75], v[64:67], a[0:7], v10\n\t" \
    "s_waitcnt lgkmcnt(0)\n\t" \
    "v_smfmac_f32_16x16x64_bf16 v[72:75], v[68:71], a[8:15], v10\n\t" \
    "v_smfmac_f32_16x16x64_bf16 v[76:79], v[52:55], a[112:119], v10\n\t" \
    "s_nop 1\n\t" \
    "s_nop 0\n\t" \
    "v_smfmac_f32_16x16x64_bf16 v[76:79], v[56:59], a[120:127], v10\n\t" \
    "s_nop 1\n\t" \
    "s_nop 0\n\t" \
    "v_smfmac_f32_16x16x64_bf16 v[76:79], v[60:63], a[128:135], v10\n\t" \
    "v_add_f32 v72, v72, v74\n\t" \
    "v_smfmac_f32_16x16x64_bf16 v[76:79], v[64:67], a[72:79], v10\n\t" \
    "v_add_f32 v73, v73, v75\n\t" \
    "v_smfmac_f32_16x16x64_bf16 v[76:79], v[68:71], a[80:87], v10\n\t" \
    "v_cvt_pk_bf16_f32 v156, v72, v73\n\t" \
    "v_smfmac_f32_16x16x64_bf16 v[80:83], v[52:55], a[184:191], v10\n\t" \
    "s_nop 1\n\t" \
    "ds_write_b32 v12, v156 offset:0\n\t" \
    "v_smfmac_f32_16x16x64_bf16 v[80:83], v[56:59], a[192:199], v10\n\t" \
    "s_nop 1\n\t" \
    "s_nop 0\n\t" \
    "v_smfmac_f32_16x16x64_bf16 v[80:83], v[60:63], a[200:207], v10\n\t" \
    "v_add_f32 v76, v76, v78\n\t" \
    "v_smfmac_f32_16x16x64_bf16 v[80:83], v[64:67], a[144:151], v10\n\t" \
    "v_add_f32 v77, v77, v79\n\t" \
    "v_smfmac_f32_16x16x64_bf16 v[80:83], v[68:71], a[152:159], v10\n\t" \
    "v_cvt_pk_bf16_f32 v157, v76, v77\n\t" \
    "v_smfmac_f32_16x16x64_bf16 v[84:87], v[52:55], v[224:231], v10\n\t" \
    "s_nop 1\n\t" \
    "ds_write_b32 v12, v157 offset:64\n\t" \
    "v_smfmac_f32_16x16x64_bf16 v[84:87], v[56:59], v[232:239], v10\n\t" \
    "s_nop 1\n\t" \
    "s_nop 0\n\t" \
    "v_smfmac_f32_16x16x64_bf16 v[84:87], v[60:63], v[240:247], v10\n\t" \
    "v_add_f32 v80, v80, v82\n\t" \
    "v_smfmac_f32_16x16x64_bf16 v[84:87], v[64:67], a[216:223], v10\n\t" \
    "v_add_f32 v81, v81, v83\n\t" \
    "v_smfmac_f32_16x16x64_bf16 v[84:87], v[68:71], a[224:231], v10\n\t" \
    "v_cvt_pk_bf16_f32 v158, v80, v81\n\t" \
    "ds_write_b32 v16, v158 offset:0\n\t" \
    "s_nop 5\n\t" \
    "v_pk_add_f32 v[84:85], v[84:85], v[86:87]\n\t" \
    "v_cvt_pk_bf16_f32 v159, v84, v85\n\t" \
    "ds_write_b32 v16, v159 offset:256\n\t" \
    "s_cmp_eq_u32 s65, 0\n\t" \
    "s_cbranch_scc1 qk_k042_%=\n\t" \
    "qk_k0b43_%=:\n\t" \
    "s_waitcnt lgkmcnt(0)\n\t" \
    "s_barrier\n\t" \
    "ds_read_b64_tr_b16 v[132:133], v13 offset:0\n\t" \
    "ds_read_b64_tr_b16 v[134:135], v13 offset:128\n\t" \
    "ds_read_b64_tr_b16 v[136:137], v13 offset:1024\n\t" \
    "ds_read_b64_tr_b16 v[138:139], v13 offset:1152\n\t" \
    "ds_read_b64_tr_b16 v[140:141], v17 offset:0\n\t" \
    "ds_read_b64_tr_b16 v[142:143], v17 offset:256\n\t" \
    "ds_read_b64_tr_b16 v[144:145], v17 offset:512\n\t" \
    "ds_read_b64_tr_b16 v[146:147], v17 offset:768\n\t" \
    "ds_read_b64_tr_b16 v[148:149], v17 offset:1024\n\t" \
    "ds_read_b64_tr_b16 v[150:151], v17 offset:1280\n\t" \
    "ds_read_b64_tr_b16 v[152:153], v17 offset:1536\n\t" \
    "ds_read_b64_tr_b16 v[154:155], v17 offset:1792\n\t" \
    "s_waitcnt lgkmcnt(10)\n\t" \
    "v_smfmac_f32_16x16x64_bf16 v[88:91], v[132:135], v[100:107], v10\n\t" \
    "v_smfmac_f32_16x16x64_bf16 v[92:95], v[132:135], v[116:123], v10\n\t" \
    "s_waitcnt lgkmcnt(8)\n\t" \
    "v_smfmac_f32_16x16x64_bf16 v[88:91], v[136:139], v[108:115], v10\n\t" \
    "v_smfmac_f32_16x16x64_bf16 v[92:95], v[136:139], v[124:131], v10\n\t" \
    "s_waitcnt lgkmcnt(4)\n\t" \
    "v_smfmac_f32_16x16x64_bf16 v[96:99], v[132:135], v[140:147], v10\n\t" \
    "s_waitcnt lgkmcnt(0)\n\t" \
    "s_nop 1\n\t" \
    "v_smfmac_f32_16x16x64_bf16 v[96:99], v[136:139], v[148:155], v10\n\t" \
    "s_nop 0\n\t" \
    "v_add_f32 v168, v89, v91\n\t" \
    "v_add_f32 v169, v93, v95\n\t" \
    "v_max_f32 v178, v168, v169\n\t" \
    "v_mul_f32 v168, v168, v29\n\t" \
    "v_mul_f32 v169, v169, v29\n\t" \
    "v_max_f32_dpp v178, v178, v178 row_mirror row_mask:0xf bank_mask:0xf\n\t" \
    "s_nop 0\n\t" \
    "v_add_f32 v172, v97, v99\n\t" \
    "v_max_f32 v173, v178, v172\n\t" \
    "v_mul_f32 v172, v172, v29\n\t" \
    "s_nop 0\n\t" \
    "v_max_f32_dpp v173, v173, v173 quad_perm:[1,0,3,2] row_mask:0xf bank_mask:0xf\n\t" \
    "s_nop 1\n\t" \
    "v_max_f32_dpp v173, v173, v173 quad_perm:[2,3,0,1] row_mask:0xf bank_mask:0xf\n\t" \
    "s_nop 1\n\t" \
    "v_max_f32_dpp v173, v173, v173 row_half_mirror row_mask:0xf bank_mask:0xf\n\t" \
    "v_fma_f32 v172, -v173, v29, v172\n\t" \
    "v_fma_f32 v168, -v173, v29, v168\n\t" \
    "v_fma_f32 v169, -v173, v29, v169\n\t" \
    "v_exp_f32 v172, v172\n\t" \
    "v_exp_f32 v168, v168\n\t" \
    "v_exp_f32 v169, v169\n\t" \
    "v_fma_f32 v173, v172, 0.5, v168\n\t" \
    "v_add_f32 v173, v173, v169\n\t" \
    "v_mov_b32_dpp v174, v172 quad_perm:[0,0,2,2] row_mask:0xf bank_mask:0xf\n\t" \
    "v_mov_b32_dpp v175, v172 quad_perm:[1,1,3,3] row_mask:0xf bank_mask:0xf\n\t" \
    "v_add_f32_dpp v173, v173, v173 quad_perm:[1,0,3,2] row_mask:0xf bank_mask:0xf\n\t" \
    "v_mov_b32_dpp v176, v168 quad_perm:[1,0,3,2] row_mask:0xf bank_mask:0xf\n\t" \
    "v_mov_b32_dpp v177, v169 quad_perm:[1,0,3,2] row_mask:0xf bank_mask:0xf\n\t" \
    "v_add_f32_dpp v173, v173, v173 quad_perm:[2,3,0,1] row_mask:0xf bank_mask:0xf\n\t" \
    "v_cndmask_b32 v168, v168, v177, s[48:49]\n\t" \
    "v_cndmask_b32 v169, v176, v169, s[48:49]\n\t" \
    "v_add_f32_dpp v173, v173, v173 row_half_mirror row_mask:0xf bank_mask:0xf\n\t" \
    "s_nop 1\n\t" \
    "v_add_f32_dpp v173, v173, v173 row_mirror row_mask:0xf bank_mask:0xf\n\t" \
    "v_rcp_f32 v173, v173\n\t" \
    "s_nop 0\n\t" \
    "v_pk_mul_f32 v[168:169], v[168:169], v[172:173] op_sel:[0,1] op_sel_hi:[1,1]\n\t" \
    "v_pk_mul_f32 v[174:175], v[174:175], v[172:173] op_sel:[0,1] op_sel_hi:[1,1]\n\t" \
    "v_cvt_pk_bf16_f32 v176, v168, v169\n\t" \
    "global_store_dword v18, v176, s[42:43] sc1\n\t" \
    "global_store_dword v3, v176, s[42:43] sc1\n\t" \
    "v_cvt_pk_bf16_f32 v177, v174, v175\n\t" \
    "global_store_dword v19, v177, s[42:43] sc1\n\t" \
    "global_store_dword v4, v177, s[42:43] sc1\n\t" \
    "s_cmp_eq_u32 s65, 63\n\t" \
    "s_cbranch_scc1 qk_last39_%=\n\t" \
    "s_memtime s[54:55]\n\t" \
    "s_waitcnt vmcnt(4) lgkmcnt(0)\n\t" \
    "s_add_u32 s79, s54, 728\n\t" \
    "v_readfirstlane_b32 s69, v25\n\t" \
    "s_add_u32 s71, s65, 1\n\t" \
    "s_max_i32 s83, s83, s69\n\t" \
    "qk_WF55_%=:\n\t" \
    "s_cmp_ge_i32 s83, s71\n\t" \
    "s_cbranch_scc1 qk_WFok56_%=\n\t" \
    "global_load_dword v160, v24, s[36:37] sc1\n\t" \
    "s_waitcnt vmcnt(0)\n\t" \
    "v_readfirstlane_b32 s83, v160\n\t" \
    "s_nop 1\n\t" \
    "s_cmp_ge_i32 s83, s71\n\t" \
    "s_cbranch_scc1 qk_WFok56_%=\n\t" \
    "s_cmp_eq_u32 s68, 0\n\t" \
    "s_cbranch_scc1 qk_F57_%=\n\t" \
    "s_sub_u32 s68, s68, 1\n\t" \
    "s_sleep 2\n\t" \
    "s_branch qk_WF55_%=\n\t" \
    "qk_F57_%=:\n\t" \
    "v_mov_b32 v160, 6095104\n\t" \
    "v_mov_b32 v161, 9\n\t" \
    "global_store_dword v160, v161, s[36:37] sc1\n\t" \
    "s_waitcnt vmcnt(0)\n\t" \
    "s_branch qk_WFok56_%=\n\t" \
    "qk_WFok56_%=:\n\t" \
    "v_mov_b32 v72, 0\n\t" \
    "v_mov_b32 v73, 0\n\t" \
    "v_mov_b32 v74, 0\n\t" \
    "v_mov_b32 v75, 0\n\t" \
    "v_mov_b32 v76, 0\n\t" \
    "v_mov_b32 v77, 0\n\t" \
    "v_mov_b32 v78, 0\n\t" \
    "v_mov_b32 v79, 0\n\t" \
    "v_mov_b32 v80, 0\n\t" \
    "v_mov_b32 v81, 0\n\t" \
    "v_mov_b32 v82, 0\n\t" \
    "v_mov_b32 v83, 0\n\t" \
    "v_mov_b32 v84, 0\n\t" \
    "v_mov_b32 v85, 0\n\t" \
    "v_mov_b32 v86, 0\n\t" \
    "v_mov_b32 v87, 0\n\t" \
    "v_mov_b32 v88, 0\n\t" \
    "v_mov_b32 v89, 0\n\t" \
    "v_mov_b32 v90, 0\n\t" \
    "v_mov_b32 v91, 0\n\t" \
    "v_mov_b32 v92, 0\n\t" \
    "v_mov_b32 v93, 0\n\t" \
    "v_mov_b32 v94, 0\n\t" \
    "v_mov_b32 v95, 0\n\t" \
    "v_mov_b32 v96, 0\n\t" \
    "v_mov_b32 v97, 0\n\t" \
    "v_mov_b32 v98, 0\n\t" \
    "v_mov_b32 v99, 0\n\t" \
    "s_mov_b32 s70, s79\n\t" \
    "qk_W58_%=:\n\t" \
    "s_memtime s[56:57]\n\t" \
    "s_waitcnt lgkmcnt(0)\n\t" \
    "s_sub_u32 s69, s70, s56\n\t" \
    "s_cmp_gt_i32 s69, 24\n\t" \
    "s_cbranch_scc1 qk_W58_%=\n\t" \
    "global_load_dwordx4 v[100:103], v23, s[38:39] offset:0 sc1\n\t" \
    "global_load_dwordx4 v[104:107], v23, s[38:39] offset:2048 sc1\n\t" \
    "global_load_dwordx4 v[108:111], v15, s[38:39] offset:0 sc1\n\t" \
    "global_load_dwordx4 v[112:115], v15, s[38:39] offset:2048 sc1\n\t" \
    "global_load_dwordx4 v[116:119], v23, s[38:39] offset:1024 sc1\n\t" \
    "global_load_dwordx4 v[120:123], v23, s[38:39] offset:3072 sc1\n\t" \
    "global_load_dwordx4 v[124:127], v15, s[38:39] offset:1024 sc1\n\t" \
    "global_load_dwordx4 v[128:131], v15, s[38:39] offset:3072 sc1\n\t" \
    "s_add_u32 s65, s65, 1\n\t" \
    "v_add_u32 v31, 1, v31\n\t" \
    "s_add_u32 s69, s65, 0\n\t" \
    "s_bitcmp1_b32 s69, 0\n\t" \
    "s_cselect_b32 s70, s63, s62\n\t" \
    "s_bfe_u32 s69, s69, 0x20001\n\t" \
    "s_lshl_b32 s69, s69, 19\n\t" \
    "s_add_u32 s69, s69, s70\n\t" \
    "s_add_u32 s46, s36, s69\n\t" \
    "s_addc_u32 s47, s37, 0\n\t" \
    "s_mov_b64 s[42:43], s[46:47]\n\t" \
    "s_add_u32 s69, s65, 0\n\t" \
    "s_lshr_b32 s69, s69, 1\n\t" \
    "s_xor_b32 s69, s69, s97\n\t" \
    "s_and_b32 s69, s69, 1\n\t" \
    "s_lshl_b32 s69, s69, 14\n\t" \
    "s_or_b32 s46, s46, s69\n\t" \
    "s_add_u32 s69, s65, 3\n\t" \
    "s_cmp_ge_i32 s83, s69\n\t" \
    "s_cselect_b64 vcc, -1, 0\n\t" \
    "v_mov_b32 v9, 6233984\n\t" \
    "v_cndmask_b32 v9, v24, v9, vcc\n\t" \
    "s_mul_i32 s70, s67, 7\n\t" \
    "s_add_u32 s69, s65, 1\n\t" \
    "s_xor_b32 s69, s69, s70\n\t" \
    "s_and_b32 s69, s69, 63\n\t" \
    "s_lshl_b32 s69, s69, 13\n\t" \
    "s_mul_i32 s70, s67, 524288\n\t" \
    "s_add_u32 s69, s69, s70\n\t" \
    "s_add_u32 s69, s69, 23068672\n\t" \
    "s_add_u32 s38, s36, s69\n\t" \
    "s_addc_u32 s39, s37, 0\n\t" \
    "s_waitcnt lgkmcnt(0)\n\t" \
    "s_barrier\n\t" \
    "ds_read_b32 v160, v11 offset:20480\n\t" \
    "s_waitcnt lgkmcnt(0)\n\t" \
    "v_readfirstlane_b32 s79, v160\n\t" \
    "s_nop 1\n\t" \
    "s_branch qk_go44_%=\n\t" \
    "qk_Gbad50_%=:\n\t" \
    "s_waitcnt vmcnt(0) lgkmcnt(0)\n\t" \
    "s_nop 7\n\t" \
    "v_mov_b32 v72, 0\n\t" \
    "v_mov_b32 v73, 0\n\t" \
    "v_mov_b32 v74, 0\n\t" \
    "v_mov_b32 v75, 0\n\t" \
    "v_mov_b32 v76, 0\n\t" \
    "v_mov_b32 v77, 0\n\t" \
    "v_mov_b32 v78, 0\n\t" \
    "v_mov_b32 v79, 0\n\t" \
    "v_mov_b32 v80, 0\n\t" \
    "v_mov_b32 v81, 0\n\t" \
    "v_mov_b32 v82, 0\n\t" \
    "v_mov_b32 v83, 0\n\t" \
    "v_mov_b32 v84, 0\n\t" \
    "v_mov_b32 v85, 0\n\t" \
    "v_mov_b32 v86, 0\n\t" \
    "v_mov_b32 v87, 0\n\t" \
    "s_cmp_eq_u32 s68, 0\n\t" \
    "s_cbranch_scc1 qk_F59_%=\n\t" \
    "s_sub_u32 s68, s68, 1\n\t" \
    "s_branch qk_G49_%=\n\t" \
    "qk_F59_%=:\n\t" \
    "v_mov_b32 v160, 6095104\n\t" \
    "v_mov_b32 v161, 1\n\t" \
    "global_store_dword v160, v161, s[36:37] sc1\n\t" \
    "s_waitcnt vmcnt(0)\n\t" \
    "s_branch qk_Gc52_%=\n\t" \
    "qk_Gbad251_%=:\n\t" \
    "global_load_dwordx4 v[44:47], v2, s[46:47] sc1\n\t" \
    "s_waitcnt vmcnt(0)\n\t" \
    "v_max3_u32 v160, v44, v45, v46\n\t" \
    "v_max_u32 v160, v160, v47\n\t" \
    "v_cmp_eq_u32 vcc, -1, v160\n\t" \
    "s_cbranch_vccz qk_Gre254_%=\n\t" \
    "s_cmp_eq_u32 s68, 0\n\t" \
    "s_cbranch_scc1 qk_F60_%=\n\t" \
    "s_sub_u32 s68, s68, 1\n\t" \
    "s_sleep 1\n\t" \
    "s_branch qk_Gbad251_%=\n\t" \
    "qk_F60_%=:\n\t" \
    "v_mov_b32 v160, 6095104\n\t" \
    "v_mov_b32 v161, 2\n\t" \
    "global_store_dword v160, v161, s[36:37] sc1\n\t" \
    "s_waitcnt vmcnt(0)\n\t" \
    "s_branch qk_Gre254_%=\n\t" \
    "qk_Gre254_%=:\n\t" \
    "v_mov_b32 v72, 0\n\t" \
    "v_mov_b32 v73, 0\n\t" \
    "v_mov_b32 v74, 0\n\t" \
    "v_mov_b32 v75, 0\n\t" \
    "v_mov_b32 v76, 0\n\t" \
    "v_mov_b32 v77, 0\n\t" \
    "v_mov_b32 v78, 0\n\t" \
    "v_mov_b32 v79, 0\n\t" \
    "v_mov_b32 v80, 0\n\t" \
    "v_mov_b32 v81, 0\n\t" \
    "v_mov_b32 v82, 0\n\t" \
    "v_mov_b32 v83, 0\n\t" \
    "v_mov_b32 v84, 0\n\t" \
    "v_mov_b32 v85, 0\n\t" \
    "v_mov_b32 v86, 0\n\t" \
    "v_mov_b32 v87, 0\n\t" \
    "v_smfmac_f32_16x16x64_bf16 v[72:75], v[36:39], a[16:23], v10\n\t" \
    "v_smfmac_f32_16x16x64_bf16 v[76:79], v[36:39], a[88:95], v10\n\t" \
    "v_smfmac_f32_16x16x64_bf16 v[80:83], v[36:39], a[160:167], v10\n\t" \
    "v_smfmac_f32_16x16x64_bf16 v[84:87], v[36:39], a[232:239], v10\n\t" \
    "v_smfmac_f32_16x16x64_bf16 v[72:75], v[40:43], a[24:31], v10\n\t" \
    "v_smfmac_f32_16x16x64_bf16 v[76:79], v[40:43], a[96:103], v10\n\t" \
    "v_smfmac_f32_16x16x64_bf16 v[80:83], v[40:43], a[168:175], v10\n\t" \
    "v_smfmac_f32_16x16x64_bf16 v[84:87], v[40:43], a[240:247], v10\n\t" \
    "v_smfmac_f32_16x16x64_bf16 v[72:75], v[44:47], a[64:71], v10\n\t" \
    "v_smfmac_f32_16x16x64_bf16 v[76:79], v[44:47], a[136:143], v10\n\t" \
    "v_smfmac_f32_16x16x64_bf16 v[80:83], v[44:47], a[208:215], v10\n\t" \
    "v_smfmac_f32_16x16x64_bf16 v[84:87], v[44:47], v[248:255], v10\n\t" \
    "s_branch qk_Gc253_%=\n\t" \
    "qk_k042_%=:\n\t" \
    "qk_WF61_%=:\n\t" \
    "s_cmp_ge_i32 s83, s65\n\t" \
    "s_cbranch_scc1 qk_WFok62_%=\n\t" \
    "global_load_dword v160, v24, s[36:37] sc1\n\t" \
    "s_waitcnt vmcnt(0)\n\t" \
    "v_readfirstlane_b32 s83, v160\n\t" \
    "s_nop 1\n\t" \
    "s_cmp_ge_i32 s83, s65\n\t" \
    "s_cbranch_scc1 qk_WFok62_%=\n\t" \
    "s_cmp_eq_u32 s68, 0\n\t" \
    "s_cbranch_scc1 qk_F63_%=\n\t" \
    "s_sub_u32 s68, s68, 1\n\t" \
    "s_sleep 2\n\t" \
    "s_branch qk_WF61_%=\n\t" \
    "qk_F63_%=:\n\t" \
    "v_mov_b32 v160, 6095104\n\t" \
    "v_mov_b32 v161, 8\n\t" \
    "global_store_dword v160, v161, s[36:37] sc1\n\t" \
    "s_waitcnt vmcnt(0)\n\t" \
    "s_branch qk_WFok62_%=\n\t" \
    "qk_WFok62_%=:\n\t" \
    "global_load_dwordx4 v[100:103], v23, s[50:51] offset:0 sc1\n\t" \
    "global_load_dwordx4 v[104:107], v23, s[50:51] offset:2048 sc1\n\t" \
    "global_load_dwordx4 v[108:111], v15, s[50:51] offset:0 sc1\n\t" \
    "global_load_dwordx4 v[112:115], v15, s[50:51] offset:2048 sc1\n\t" \
    "global_load_dwordx4 v[116:119], v23, s[50:51] offset:1024 sc1\n\t" \
    "global_load_dwordx4 v[120:123], v23, s[50:51] offset:3072 sc1\n\t" \
    "global_load_dwordx4 v[124:127], v15, s[50:51] offset:1024 sc1\n\t" \
    "global_load_dwordx4 v[128:131], v15, s[50:51] offset:3072 sc1\n\t" \
    "s_waitcnt vmcnt(0)\n\t" \
    "s_branch qk_k0b43_%=\n\t" \
    "qk_last39_%=:\n\t" \
    "s_endpgm\n\t" \
    "qk_loop12_%=:\n\t" \
    "v_mov_b32 v72, 0\n\t" \
    "v_mov_b32 v73, 0\n\t" \
    "v_mov_b32 v74, 0\n\t" \
    "v_mov_b32 v75, 0\n\t" \
    "v_mov_b32 v76, 0\n\t" \
    "v_mov_b32 v77, 0\n\t" \
    "v_mov_b32 v78, 0\n\t" \
    "v_mov_b32 v79, 0\n\t" \
    "v_mov_b32 v80, 0\n\t" \
    "v_mov_b32 v81, 0\n\t" \
    "v_mov_b32 v82, 0\n\t" \
    "v_mov_b32 v83, 0\n\t" \
    "v_mov_b32 v84, 0\n\t" \
    "v_mov_b32 v85, 0\n\t" \
    "v_mov_b32 v86, 0\n\t" \
    "v_mov_b32 v87, 0\n\t" \
    "v_add_u32 v31, 1, v31\n\t" \
    "s_add_u32 s69, s65, 0\n\t" \
    "s_bitcmp1_b32 s69, 0\n\t" \
    "s_cselect_b32 s70, s63, s62\n\t" \
    "s_bfe_u32 s69, s69, 0x20001\n\t" \
    "s_lshl_b32 s69, s69, 19\n\t" \
    "s_add_u32 s69, s69, s70\n\t" \
    "s_add_u32 s46, s36, s69\n\t" \
    "s_addc_u32 s47, s37, 0\n\t" \
    "s_mov_b64 s[42:43], s[46:47]\n\t" \
    "s_add_u32 s69, s65, 0\n\t" \
    "s_lshr_b32 s69, s69, 1\n\t" \
    "s_xor_b32 s69, s69, s97\n\t" \
    "s_and_b32 s69, s69, 1\n\t" \
    "s_lshl_b32 s69, s69, 14\n\t" \
    "s_or_b32 s46, s46, s69\n\t" \
    "s_mul_i32 s70, s67, 7\n\t" \
    "s_add_u32 s69, s65, 0\n\t" \
    "s_xor_b32 s69, s69, s70\n\t" \
    "s_and_b32 s69, s69, 63\n\t" \
    "s_lshl_b32 s69, s69, 12\n\t" \
    "s_mul_i32 s70, s67, 262144\n\t" \
    "s_add_u32 s69, s69, s70\n\t" \
    "s_add_u32 s69, s69, 39845888\n\t" \
    "s_add_u32 s40, s36, s69\n\t" \
    "s_addc_u32 s41, s37, 0\n\t" \
    "s_sub_u32 s69, s65, 1\n\t" \
    "v_mov_b32 v25, s69\n\t" \
    "qk_go69_%=:\n\t" \
    "s_mov_b32 s70, s79\n\t" \
    "s_sub_u32 s70, s70, 100\n\t" \
    "s_getpc_b64 s[30:31]\n\t" \
    "qk_Wp71_%=:\n\t" \
    "s_add_u32 s30, s30, qk_We72_%=-qk_Wp71_%=\n\t" \
    "s_addc_u32 s31, s31, 0\n\t" \
    "qk_W70_%=:\n\t" \
    "s_memtime s[56:57]\n\t" \
    "s_waitcnt lgkmcnt(0)\n\t" \
    "s_sub_u32 s69, s70, s56\n\t" \
    "s_cmp_gt_i32 s69, 111\n\t" \
    "s_cbranch_scc1 qk_W70_%=\n\t" \
    "s_max_i32 s69, s69, 0\n\t" \
    "s_lshr_b32 s69, s69, 1\n\t" \
    "s_and_b32 s69, s69, 0x3c\n\t" \
    "s_sub_u32 s30, s30, s69\n\t" \
    "s_subb_u32 s31, s31, 0\n\t" \
    "s_setpc_b64 s[30:31]\n\t" \
    ".p2align 6\n\t" \
    "qk_al73_%=:\n\t" \
    "s_nop 0\n\t" \
    "s_nop 1\n\t" \
    "s_nop 1\n\t" \
    "s_nop 1\n\t" \
    "s_nop 1\n\t" \
    "s_nop 1\n\t" \
    "s_nop 1\n\t" \
    "s_nop 1\n\t" \
    "s_nop 1\n\t" \
    "s_nop 1\n\t" \
    "s_nop 1\n\t" \
    "s_nop 1\n\t" \
    "s_nop 1\n\t" \
    "s_nop 1\n\t" \
    "qk_We72_%=:\n\t" \
    "qk_G74_%=:\n\t" \
    "global_load_dwordx4 v[36:39], v1, s[46:47] sc1\n\t" \
    "s_nop 9\n\t" \
    "global_load_dwordx4 v[40:43], v1, s[46:47] offset:1024 sc1\n\t" \
    "s_nop 9\n\t" \
    "global_load_dwordx4 v[44:47], v2, s[46:47] sc1\n\t" \
    "s_waitcnt vmcnt(2)\n\t" \
    "v_smfmac_f32_16x16x64_bf16 v[72:75], v[36:39], a[32:39], v10\n\t" \
    "ds_write_b128 v0, v[36:39] offset:4096\n\t" \
    "v_smfmac_f32_16x16x64_bf16 v[76:79], v[36:39], a[104:111], v10\n\t" \
    "v_max3_u32 v160, v36, v37, v38\n\t" \
    "v_smfmac_f32_16x16x64_bf16 v[80:83], v[36:39], a[176:183], v10\n\t" \
    "v_smfmac_f32_16x16x64_bf16 v[84:87], v[36:39], a[248:255], v10\n\t" \
    "s_waitcnt vmcnt(1)\n\t" \
    "ds_write_b128 v0, v[40:43] offset:5120\n\t" \
    "v_smfmac_f32_16x16x64_bf16 v[72:75], v[40:43], a[40:47], v10\n\t" \
    "v_max3_u32 v160, v160, v39, v40\n\t" \
    "v_max3_u32 v160, v160, v41, v42\n\t" \
    "v_smfmac_f32_16x16x64_bf16 v[76:79], v[40:43], a[112:119], v10\n\t" \
    "v_max_u32 v160, v160, v43\n\t" \
    "v_cmp_eq_u32 vcc, -1, v160\n\t" \
    "v_smfmac_f32_16x16x64_bf16 v[80:83], v[40:43], a[184:191], v10\n\t" \
    "v_smfmac_f32_16x16x64_bf16 v[84:87], v[40:43], v[224:231], v10\n\t" \
    "s_cbranch_vccnz qk_Gbad75_%=\n\t" \
    "qk_Gc77_%=:\n\t" \
    "s_waitcnt lgkmcnt(0)\n\t" \
    "s_barrier\n\t" \
    "ds_read_b128 v[48:51], v0 offset:6144\n\t" \
    "s_waitcnt vmcnt(0)\n\t" \
    "v_smfmac_f32_16x16x64_bf16 v[72:75], v[44:47], a[64:71], v10\n\t" \
    "v_max3_u32 v160, v44, v45, v46\n\t" \
    "v_smfmac_f32_16x16x64_bf16 v[76:79], v[44:47], a[136:143], v10\n\t" \
    "ds_read_b128 v[52:55], v0 offset:7168\n\t" \
    "v_max_u32 v160, v160, v47\n\t" \
    "v_smfmac_f32_16x16x64_bf16 v[80:83], v[44:47], a[208:215], v10\n\t" \
    "v_cmp_eq_u32 vcc, -1, v160\n\t" \
    "v_smfmac_f32_16x16x64_bf16 v[84:87], v[44:47], v[248:255], v10\n\t" \
    "ds_read_b128 v[56:59], v0 offset:0\n\t" \
    "s_cbranch_vccnz qk_Gbad276_%=\n\t" \
    "qk_Gc278_%=:\n\t" \
    "s_waitcnt lgkmcnt(2)\n\t" \
    "v_smfmac_f32_16x16x64_bf16 v[72:75], v[48:51], a[48:55], v10\n\t" \
    "ds_read_b128 v[60:63], v0 offset:1024\n\t" \
    "global_store_dword v14, v25, s[36:37] sc1\n\t" \
    "v_smfmac_f32_16x16x64_bf16 v[76:79], v[48:51], a[120:127], v10\n\t" \
    "ds_read_b128 v[64:67], v0 offset:2048\n\t" \
    "v_smfmac_f32_16x16x64_bf16 v[80:83], v[48:51], a[192:199], v10\n\t" \
    "ds_read_b128 v[68:71], v0 offset:3072\n\t" \
    "v_smfmac_f32_16x16x64_bf16 v[84:87], v[48:51], v[232:239], v10\n\t" \
    "s_waitcnt lgkmcnt(4)\n\t" \
    "v_smfmac_f32_16x16x64_bf16 v[72:75], v[52:55], a[56:63], v10\n\t" \
    "s_waitcnt lgkmcnt(3)\n\t" \
    "v_smfmac_f32_16x16x64_bf16 v[72:75], v[56:59], a[0:7], v10\n\t" \
    "s_waitcnt lgkmcnt(2)\n\t" \
    "v_smfmac_f32_16x16x64_bf16 v[72:75], v[60:63], a[8:15], v10\n\t" \
    "s_waitcnt lgkmcnt(1)\n\t" \
    "v_smfmac_f32_16x16x64_bf16 v[72:75], v[64:67], a[16:23], v10\n\t" \
    "s_waitcnt lgkmcnt(0)\n\t" \
    "v_smfmac_f32_16x16x64_bf16 v[72:75], v[68:71], a[24:31], v10\n\t" \
    "v_smfmac_f32_16x16x64_bf16 v[76:79], v[52:55], a[128:135], v10\n\t" \
    "s_nop 1\n\t" \
    "s_nop 0\n\t" \
    "v_smfmac_f32_16x16x64_bf16 v[76:79], v[56:59], a[72:79], v10\n\t" \
    "s_nop 1\n\t" \
    "s_nop 0\n\t" \
    "v_smfmac_f32_16x16x64_bf16 v[76:79], v[60:63], a[80:87], v10\n\t" \
    "v_add_f32 v72, v72, v74\n\t" \
    "v_smfmac_f32_16x16x64_bf16 v[76:79], v[64:67], a[88:95], v10\n\t" \
    "v_add_f32 v73, v73, v75\n\t" \
    "v_smfmac_f32_16x16x64_bf16 v[76:79], v[68:71], a[96:103], v10\n\t" \
    "v_cvt_pk_bf16_f32 v156, v72, v73\n\t" \
    "v_smfmac_f32_16x16x64_bf16 v[80:83], v[52:55], a[200:207], v10\n\t" \
    "s_nop 1\n\t" \
    "ds_write_b32 v12, v156 offset:0\n\t" \
    "v_smfmac_f32_16x16x64_bf16 v[80:83], v[56:59], a[144:151], v10\n\t" \
    "s_nop 1\n\t" \
    "s_nop 0\n\t" \
    "v_smfmac_f32_16x16x64_bf16 v[80:83], v[60:63], a[152:159], v10\n\t" \
    "v_add_f32 v76, v76, v78\n\t" \
    "v_smfmac_f32_16x16x64_bf16 v[80:83], v[64:67], a[160:167], v10\n\t" \
    "v_add_f32 v77, v77, v79\n\t" \
    "v_smfmac_f32_16x16x64_bf16 v[80:83], v[68:71], a[168:175], v10\n\t" \
    "v_cvt_pk_bf16_f32 v157, v76, v77\n\t" \
    "v_smfmac_f32_16x16x64_bf16 v[84:87], v[52:55], v[240:247], v10\n\t" \
    "s_nop 1\n\t" \
    "ds_write_b32 v12, v157 offset:64\n\t" \
    "v_smfmac_f32_16x16x64_bf16 v[84:87], v[56:59], a[216:223], v10\n\t" \
    "s_nop 1\n\t" \
    "s_nop 0\n\t" \
    "v_smfmac_f32_16x16x64_bf16 v[84:87], v[60:63], a[224:231], v10\n\t" \
    "v_add_f32 v80, v80, v82\n\t" \
    "v_smfmac_f32_16x16x64_bf16 v[84:87], v[64:67], a[232:239], v10\n\t" \
    "v_add_f32 v81, v81, v83\n\t" \
    "v_smfmac_f32_16x16x64_bf16 v[84:87], v[68:71], a[240:247], v10\n\t" \
    "v_cvt_pk_bf16_f32 v158, v80, v81\n\t" \
    "ds_write_b32 v16, v158 offset:0\n\t" \
    "s_nop 5\n\t" \
    "v_pk_add_f32 v[84:85], v[84:85], v[86:87]\n\t" \
    "v_cvt_pk_bf16_f32 v159, v84, v85\n\t" \
    "ds_write_b32 v16, v159 offset:256\n\t" \
    "s_waitcnt lgkmcnt(0)\n\t" \
    "s_barrier\n\t" \
    "s_sleep 1\n\t" \
    "ds_read_b64_tr_b16 v[140:141], v17 offset:0\n\t" \
    "ds_read_b64_tr_b16 v[142:143], v17 offset:256\n\t" \
    "ds_read_b64_tr_b16 v[144:145], v17 offset:512\n\t" \
    "ds_read_b64_tr_b16 v[146:147], v17 offset:768\n\t" \
    "ds_read_b64_tr_b16 v[148:149], v17 offset:1024\n\t" \
    "ds_read_b64_tr_b16 v[150:151], v17 offset:1280\n\t" \
    "ds_read_b64_tr_b16 v[152:153], v17 offset:1536\n\t" \
    "ds_read_b64_tr_b16 v[154:155], v17 offset:1792\n\t" \
    "s_waitcnt lgkmcnt(0)\n\t" \
    "s_cmp_eq_u32 s65, 63\n\t" \
    "s_cbranch_scc1 qk_last64_%=\n\t" \
    "s_memtime s[54:55]\n\t" \
    "s_waitcnt lgkmcnt(0)\n\t" \
    "s_cmp_ge_u32 s65, 58\n\t" \
    "s_cselect_b32 s69, 0, 0\n\t" \
    "s_sub_u32 s69, 940, s69\n\t" \
    "s_add_u32 s79, s54, s69\n\t" \
    "v_mov_b32 v72, 0\n\t" \
    "v_mov_b32 v73, 0\n\t" \
    "v_mov_b32 v74, 0\n\t" \
    "v_mov_b32 v75, 0\n\t" \
    "v_mov_b32 v76, 0\n\t" \
    "v_mov_b32 v77, 0\n\t" \
    "v_mov_b32 v78, 0\n\t" \
    "v_mov_b32 v79, 0\n\t" \
    "v_mov_b32 v80, 0\n\t" \
    "v_mov_b32 v81, 0\n\t" \
    "v_mov_b32 v82, 0\n\t" \
    "v_mov_b32 v83, 0\n\t" \
    "v_mov_b32 v84, 0\n\t" \
    "v_mov_b32 v85, 0\n\t" \
    "v_mov_b32 v86, 0\n\t" \
    "v_mov_b32 v87, 0\n\t" \
    "s_mov_b32 s70, s79\n\t" \
    "qk_W80_%=:\n\t" \
    "s_memtime s[56:57]\n\t" \
    "s_waitcnt lgkmcnt(0)\n\t" \
    "s_sub_u32 s69, s70, s56\n\t" \
    "s_cmp_gt_i32 s69, 24\n\t" \
    "s_cbranch_scc1 qk_W80_%=\n\t" \
    "s_mov_b64 exec, s[58:59]\n\t" \
    "global_store_dwordx4 v0, v[140:143], s[40:41] offset:0 sc1\n\t" \
    "global_store_dwordx4 v0, v[144:147], s[40:41] offset:1024 sc1\n\t" \
    "global_store_dwordx4 v0, v[148:151], s[40:41] offset:2048 sc1\n\t" \
    "global_store_dwordx4 v0, v[152:155], s[40:41] offset:3072 sc1\n\t" \
    "s_mov_b64 exec, -1\n\t" \
    "s_add_u32 s65, s65, 1\n\t" \
    "v_add_u32 v31, 1, v31\n\t" \
    "s_add_u32 s69, s65, 0\n\t" \
    "s_bitcmp1_b32 s69, 0\n\t" \
    "s_cselect_b32 s70, s63, s62\n\t" \
    "s_bfe_u32 s69, s69, 0x20001\n\t" \
    "s_lshl_b32 s69, s69, 19\n\t" \
    "s_add_u32 s69, s69, s70\n\t" \
    "s_add_u32 s46, s36, s69\n\t" \
    "s_addc_u32 s47, s37, 0\n\t" \
    "s_mov_b64 s[42:43], s[46:47]\n\t" \
    "s_add_u32 s69, s65, 0\n\t" \
    "s_lshr_b32 s69, s69, 1\n\t" \
    "s_xor_b32 s69, s69, s97\n\t" \
    "s_and_b32 s69, s69, 1\n\t" \
    "s_lshl_b32 s69, s69, 14\n\t" \
    "s_or_b32 s46, s46, s69\n\t" \
    "s_mul_i32 s70, s67, 7\n\t" \
    "s_add_u32 s69, s65, 0\n\t" \
    "s_xor_b32 s69, s69, s70\n\t" \
    "s_and_b32 s69, s69, 63\n\t" \
    "s_lshl_b32 s69, s69, 12\n\t" \
    "s_mul_i32 s70, s67, 262144\n\t" \
    "s_add_u32 s69, s69, s70\n\t" \
    "s_add_u32 s69, s69, 39845888\n\t" \
    "s_add_u32 s40, s36, s69\n\t" \
    "s_addc_u32 s41, s37, 0\n\t" \
    "s_sub_u32 s69, s65, 1\n\t" \
    "v_mov_b32 v25, s69\n\t" \
    "s_waitcnt lgkmcnt(0)\n\t" \
    "s_barrier\n\t" \
    "ds_read_b32 v160, v11 offset:20480\n\t" \
    "s_waitcnt lgkmcnt(0)\n\t" \
    "v_readfirstlane_b32 s79, v160\n\t" \
    "s_nop 1\n\t" \
    "s_branch qk_go69_%=\n\t" \
    "qk_Gbad75_%=:\n\t" \
    "s_waitcnt vmcnt(0) lgkmcnt(0)\n\t" \
    "s_nop 7\n\t" \
    "v_mov_b32 v72, 0\n\t" \
    "v_mov_b32 v73, 0\n\t" \
    "v_mov_b32 v74, 0\n\t" \
    "v_mov_b32 v75, 0\n\t" \
    "v_mov_b32 v76, 0\n\t" \
    "v_mov_b32 v77, 0\n\t" \
    "v_mov_b32 v78, 0\n\t" \
    "v_mov_b32 v79, 0\n\t" \
    "v_mov_b32 v80, 0\n\t" \
    "v_mov_b32 v81, 0\n\t" \
    "v_mov_b32 v82, 0\n\t" \
    "v_mov_b32 v83, 0\n\t" \
    "v_mov_b32 v84, 0\n\t" \
    "v_mov_b32 v85, 0\n\t" \
    "v_mov_b32 v86, 0\n\t" \
    "v_mov_b32 v87, 0\n\t" \
    "s_cmp_eq_u32 s68, 0\n\t" \
    "s_cbranch_scc1 qk_F81_%=\n\t" \
    "s_sub_u32 s68, s68, 1\n\t" \
    "s_branch qk_G74_%=\n\t" \
    "qk_F81_%=:\n\t" \
    "v_mov_b32 v160, 6095104\n\t" \
    "v_mov_b32 v161, 1\n\t" \
    "global_store_dword v160, v161, s[36:37] sc1\n\t" \
    "s_waitcnt vmcnt(0)\n\t" \
    "s_branch qk_Gc77_%=\n\t" \
    "qk_Gbad276_%=:\n\t" \
    "global_load_dwordx4 v[44:47], v2, s[46:47] sc1\n\t" \
    "s_waitcnt vmcnt(0)\n\t" \
    "v_max3_u32 v160, v44, v45, v46\n\t" \
    "v_max_u32 v160, v160, v47\n\t" \
    "v_cmp_eq_u32 vcc, -1, v160\n\t" \
    "s_cbranch_vccz qk_Gre279_%=\n\t" \
    "s_cmp_eq_u32 s68, 0\n\t" \
    "s_cbranch_scc1 qk_F82_%=\n\t" \
    "s_sub_u32 s68, s68, 1\n\t" \
    "s_sleep 1\n\t" \
    "s_branch qk_Gbad276_%=\n\t" \
    "qk_F82_%=:\n\t" \
    "v_mov_b32 v160, 6095104\n\t" \
    "v_mov_b32 v161, 2\n\t" \
    "global_store_dword v160, v161, s[36:37] sc1\n\t" \
    "s_waitcnt vmcnt(0)\n\t" \
    "s_branch qk_Gre279_%=\n\t" \
    "qk_Gre279_%=:\n\t" \
    "v_mov_b32 v72, 0\n\t" \
    "v_mov_b32 v73, 0\n\t" \
    "v_mov_b32 v74, 0\n\t" \
    "v_mov_b32 v75, 0\n\t" \
    "v_mov_b32 v76, 0\n\t" \
    "v_mov_b32 v77, 0\n\t" \
    "v_mov_b32 v78, 0\n\t" \
    "v_mov_b32 v79, 0\n\t" \
    "v_mov_b32 v80, 0\n\t" \
    "v_mov_b32 v81, 0\n\t" \
    "v_mov_b32 v82, 0\n\t" \
    "v_mov_b32 v83, 0\n\t" \
    "v_mov_b32 v84, 0\n\t" \
    "v_mov_b32 v85, 0\n\t" \
    "v_mov_b32 v86, 0\n\t" \
    "v_mov_b32 v87, 0\n\t" \
    "v_smfmac_f32_16x16x64_bf16 v[72:75], v[36:39], a[32:39], v10\n\t" \
    "v_smfmac_f32_16x16x64_bf16 v[76:79], v[36:39], a[104:111], v10\n\t" \
    "v_smfmac_f32_16x16x64_bf16 v[80:83], v[36:39], a[176:183], v10\n\t" \
    "v_smfmac_f32_16x16x64_bf16 v[84:87], v[36:39], a[248:255], v10\n\t" \
    "v_smfmac_f32_16x16x64_bf16 v[72:75], v[40:43], a[40:47], v10\n\t" \
    "v_smfmac_f32_16x16x64_bf16 v[76:79], v[40:43], a[112:119], v10\n\t" \
    "v_smfmac_f32_16x16x64_bf16 v[80:83], v[40:43], a[184:191], v10\n\t" \
    "v_smfmac_f32_16x16x64_bf16 v[84:87], v[40:43], v[224:231], v10\n\t" \
    "v_smfmac_f32_16x16x64_bf16 v[72:75], v[44:47], a[64:71], v10\n\t" \
    "v_smfmac_f32_16x16x64_bf16 v[76:79], v[44:47], a[136:143], v10\n\t" \
    "v_smfmac_f32_16x16x64_bf16 v[80:83], v[44:47], a[208:215], v10\n\t" \
    "v_smfmac_f32_16x16x64_bf16 v[84:87], v[44:47], v[248:255], v10\n\t" \
    "s_branch qk_Gc278_%=\n\t" \
    "qk_last64_%=:\n\t" \
    "s_mov_b64 exec, s[58:59]\n\t" \
    "global_store_dwordx4 v0, v[140:143], s[40:41] offset:0 sc1\n\t" \
    "global_store_dwordx4 v0, v[144:147], s[40:41] offset:1024 sc1\n\t" \
    "global_store_dwordx4 v0, v[148:151], s[40:41] offset:2048 sc1\n\t" \
    "global_store_dwordx4 v0, v[152:155], s[40:41] offset:3072 sc1\n\t" \
    "s_mov_b64 exec, -1\n\t" \
    "s_endpgm\n\t" \
    "qk_loop13_%=:\n\t" \
    "v_mov_b32 v72, 0\n\t" \
    "v_mov_b32 v73, 0\n\t" \
    "v_mov_b32 v74, 0\n\t" \
    "v_mov_b32 v75, 0\n\t" \
    "v_mov_b32 v76, 0\n\t" \
    "v_mov_b32 v77, 0\n\t" \
    "v_mov_b32 v78, 0\n\t" \
    "v_mov_b32 v79, 0\n\t" \
    "v_mov_b32 v80, 0\n\t" \
    "v_mov_b32 v81, 0\n\t" \
    "v_mov_b32 v82, 0\n\t" \
    "v_mov_b32 v83, 0\n\t" \
    "v_mov_b32 v84, 0\n\t" \
    "v_mov_b32 v85, 0\n\t" \
    "v_mov_b32 v86, 0\n\t" \
    "v_mov_b32 v87, 0\n\t" \
    "v_add_u32 v31, 1, v31\n\t" \
    "s_add_u32 s69, s65, 0\n\t" \
    "s_bitcmp1_b32 s69, 0\n\t" \
    "s_cselect_b32 s70, s63, s62\n\t" \
    "s_bfe_u32 s69, s69, 0x20001\n\t" \
    "s_lshl_b32 s69, s69, 19\n\t" \
    "s_add_u32 s69, s69, s70\n\t" \
    "s_add_u32 s46, s36, s69\n\t" \
    "s_addc_u32 s47, s37, 0\n\t" \
    "s_mov_b64 s[42:43], s[46:47]\n\t" \
    "s_add_u32 s69, s65, 0\n\t" \
    "s_lshr_b32 s69, s69, 1\n\t" \
    "s_xor_b32 s69, s69, s97\n\t" \
    "s_and_b32 s69, s69, 1\n\t" \
    "s_lshl_b32 s69, s69, 14\n\t" \
    "s_or_b32 s46, s46, s69\n\t" \
    "qk_go88_%=:\n\t" \
    "s_mov_b32 s70, s79\n\t" \
    "s_sub_u32 s70, s70, 100\n\t" \
    "s_getpc_b64 s[30:31]\n\t" \
    "qk_Wp90_%=:\n\t" \
    "s_add_u32 s30, s30, qk_We91_%=-qk_Wp90_%=\n\t" \
    "s_addc_u32 s31, s31, 0\n\t" \
    "qk_W89_%=:\n\t" \
    "s_memtime s[56:57]\n\t" \
    "s_waitcnt lgkmcnt(0)\n\t" \
    "s_sub_u32 s69, s70, s56\n\t" \
    "s_cmp_gt_i32 s69, 111\n\t" \
    "s_cbranch_scc1 qk_W89_%=\n\t" \
    "s_max_i32 s69, s69, 0\n\t" \
    "s_lshr_b32 s69, s69, 1\n\t" \
    "s_and_b32 s69, s69, 0x3c\n\t" \
    "s_sub_u32 s30, s30, s69\n\t" \
    "s_subb_u32 s31, s31, 0\n\t" \
    "s_setpc_b64 s[30:31]\n\t" \
    ".p2align 6\n\t" \
    "qk_al92_%=:\n\t" \
    "s_nop 0\n\t" \
    "s_nop 1\n\t" \
    "s_nop 1\n\t" \
    "s_nop 1\n\t" \
    "s_nop 1\n\t" \
    "s_nop 1\n\t" \
    "s_nop 1\n\t" \
    "s_nop 1\n\t" \
    "s_nop 1\n\t" \
    "s_nop 1\n\t" \
    "s_nop 1\n\t" \
    "s_nop 1\n\t" \
    "s_nop 1\n\t" \
    "s_nop 1\n\t" \
    "qk_We91_%=:\n\t" \
    "qk_G93_%=:\n\t" \
    "global_load_dwordx4 v[36:39], v1, s[46:47] sc1\n\t" \
    "s_nop 9\n\t" \
    "global_load_dwordx4 v[40:43], v1, s[46:47] offset:1024 sc1\n\t" \
    "s_nop 9\n\t" \
    "global_load_dwordx4 v[44:47], v2, s[46:47] sc1\n\t" \
    "s_mul_hi_u32 s69, s84, s78\n\t" \
    "s_add_u32 s33, s74, s69\n\t" \
    "s_lshr_b32 s69, s90, 16\n\t" \
    "s_add_u32 s34, s33, s69\n\t" \
    "s_and_b32 s69, s90, 0xffff\n\t" \
    "s_sub_u32 s35, s80, s74\n\t" \
    "s_sub_u32 s35, s35, s69\n\t" \
    "s_add_u32 s99, s35, 1280\n\t" \
    "s_add_u32 s98, s34, 600\n\t" \
    "s_waitcnt vmcnt(2)\n\t" \
    "v_smfmac_f32_16x16x64_bf16 v[72:75], v[36:39], a[48:55], v10\n\t" \
    "ds_write_b128 v0, v[36:39] offset:6144\n\t" \
    "v_smfmac_f32_16x16x64_bf16 v[76:79], v[36:39], a[120:127], v10\n\t" \
    "v_max3_u32 v160, v36, v37, v38\n\t" \
    "v_smfmac_f32_16x16x64_bf16 v[80:83], v[36:39], a[192:199], v10\n\t" \
    "v_smfmac_f32_16x16x64_bf16 v[84:87], v[36:39], v[232:239], v10\n\t" \
    "s_waitcnt vmcnt(1)\n\t" \
    "ds_write_b128 v0, v[40:43] offset:7168\n\t" \
    "v_smfmac_f32_16x16x64_bf16 v[72:75], v[40:43], a[56:63], v10\n\t" \
    "v_max3_u32 v160, v160, v39, v40\n\t" \
    "v_max3_u32 v160, v160, v41, v42\n\t" \
    "v_smfmac_f32_16x16x64_bf16 v[76:79], v[40:43], a[128:135], v10\n\t" \
    "v_max_u32 v160, v160, v43\n\t" \
    "v_cmp_eq_u32 vcc, -1, v160\n\t" \
    "v_smfmac_f32_16x16x64_bf16 v[80:83], v[40:43], a[200:207], v10\n\t" \
    "v_smfmac_f32_16x16x64_bf16 v[84:87], v[40:43], v[240:247], v10\n\t" \
    "s_cbranch_vccnz qk_Gbad94_%=\n\t" \
    "qk_Gc96_%=:\n\t" \
    "s_waitcnt lgkmcnt(0)\n\t" \
    "s_barrier\n\t" \
    "s_memtime s[56:57]\n\t" \
    "ds_read_b128 v[48:51], v0 offset:0\n\t" \
    "s_waitcnt vmcnt(0)\n\t" \
    "v_smfmac_f32_16x16x64_bf16 v[72:75], v[44:47], a[64:71], v10\n\t" \
    "v_max3_u32 v160, v44, v45, v46\n\t" \
    "v_smfmac_f32_16x16x64_bf16 v[76:79], v[44:47], a[136:143], v10\n\t" \
    "ds_read_b128 v[52:55], v0 offset:1024\n\t" \
    "v_max_u32 v160, v160, v47\n\t" \
    "v_smfmac_f32_16x16x64_bf16 v[80:83], v[44:47], a[208:215], v10\n\t" \
    "v_cmp_eq_u32 vcc, -1, v160\n\t" \
    "s_sub_u32 s69, s56, s73\n\t" \
    "v_smfmac_f32_16x16x64_bf16 v[84:87], v[44:47], v[248:255], v10\n\t" \
    "ds_read_b128 v[56:59], v0 offset:2048\n\t" \
    "s_mul_hi_u32 s69, s69, s78\n\t" \
    "s_cbranch_vccnz qk_Gbad295_%=\n\t" \
    "qk_Gc297_%=:\n\t" \
    "s_waitcnt lgkmcnt(2)\n\t" \
    "v_smfmac_f32_16x16x64_bf16 v[72:75], v[48:51], a[0:7], v10\n\t" \
    "ds_read_b128 v[60:63], v0 offset:3072\n\t" \
    "s_cmp_gt_i32 s69, s35\n\t" \
    "v_smfmac_f32_16x16x64_bf16 v[76:79], v[48:51], a[72:79], v10\n\t" \
    "ds_read_b128 v[64:67], v0 offset:4096\n\t" \
    "s_cselect_b32 s70, s34, s33\n\t" \
    "v_smfmac_f32_16x16x64_bf16 v[80:83], v[48:51], a[144:151], v10\n\t" \
    "ds_read_b128 v[68:71], v0 offset:5120\n\t" \
    "s_cmp_gt_i32 s69, s99\n\t" \
    "v_smfmac_f32_16x16x64_bf16 v[84:87], v[48:51], a[216:223], v10\n\t" \
    "s_cselect_b32 s70, s98, s70\n\t" \
    "s_waitcnt lgkmcnt(4)\n\t" \
    "v_smfmac_f32_16x16x64_bf16 v[72:75], v[52:55], a[8:15], v10\n\t" \
    "s_add_u32 s70, s69, s70\n\t" \
    "s_waitcnt lgkmcnt(3)\n\t" \
    "v_smfmac_f32_16x16x64_bf16 v[72:75], v[56:59], a[16:23], v10\n\t" \
    "v_and_b32_e64 v162, -2, s70\n\t" \
    "s_waitcnt lgkmcnt(2)\n\t" \
    "v_smfmac_f32_16x16x64_bf16 v[72:75], v[60:63], a[24:31], v10\n\t" \
    "s_mov_b64 exec, s[44:45]\n\t" \
    "global_store_dword v21, v162, s[42:43] sc1\n\t" \
    "s_mov_b64 exec, -1\n\t" \
    "s_waitcnt lgkmcnt(1)\n\t" \
    "v_smfmac_f32_16x16x64_bf16 v[72:75], v[64:67], a[32:39], v10\n\t" \
    "s_waitcnt lgkmcnt(0)\n\t" \
    "v_smfmac_f32_16x16x64_bf16 v[72:75], v[68:71], a[40:47], v10\n\t" \
    "v_smfmac_f32_16x16x64_bf16 v[76:79], v[52:55], a[80:87], v10\n\t" \
    "s_nop 1\n\t" \
    "s_nop 0\n\t" \
    "v_smfmac_f32_16x16x64_bf16 v[76:79], v[56:59], a[88:95], v10\n\t" \
    "s_nop 1\n\t" \
    "s_nop 0\n\t" \
    "v_smfmac_f32_16x16x64_bf16 v[76:79], v[60:63], a[96:103], v10\n\t" \
    "v_add_f32 v72, v72, v74\n\t" \
    "v_smfmac_f32_16x16x64_bf16 v[76:79], v[64:67], a[104:111], v10\n\t" \
    "v_add_f32 v73, v73, v75\n\t" \
    "v_smfmac_f32_16x16x64_bf16 v[76:79], v[68:71], a[112:119], v10\n\t" \
    "v_cvt_pk_bf16_f32 v156, v72, v73\n\t" \
    "v_smfmac_f32_16x16x64_bf16 v[80:83], v[52:55], a[152:159], v10\n\t" \
    "s_nop 1\n\t" \
    "ds_write_b32 v12, v156 offset:0\n\t" \
    "v_smfmac_f32_16x16x64_bf16 v[80:83], v[56:59], a[160:167], v10\n\t" \
    "s_nop 1\n\t" \
    "s_nop 0\n\t" \
    "v_smfmac_f32_16x16x64_bf16 v[80:83], v[60:63], a[168:175], v10\n\t" \
    "v_add_f32 v76, v76, v78\n\t" \
    "v_smfmac_f32_16x16x64_bf16 v[80:83], v[64:67], a[176:183], v10\n\t" \
    "v_add_f32 v77, v77, v79\n\t" \
    "v_smfmac_f32_16x16x64_bf16 v[80:83], v[68:71], a[184:191], v10\n\t" \
    "v_cvt_pk_bf16_f32 v157, v76, v77\n\t" \
    "v_smfmac_f32_16x16x64_bf16 v[84:87], v[52:55], a[224:231], v10\n\t" \
    "s_nop 1\n\t" \
    "ds_write_b32 v12, v157 offset:64\n\t" \
    "v_smfmac_f32_16x16x64_bf16 v[84:87], v[56:59], a[232:239], v10\n\t" \
    "s_nop 1\n\t" \
    "s_nop 0\n\t" \
    "v_smfmac_f32_16x16x64_bf16 v[84:87], v[60:63], a[240:247], v10\n\t" \
    "v_add_f32 v80, v80, v82\n\t" \
    "v_smfmac_f32_16x16x64_bf16 v[84:87], v[64:67], a[248:255], v10\n\t" \
    "v_add_f32 v81, v81, v83\n\t" \
    "v_smfmac_f32_16x16x64_bf16 v[84:87], v[68:71], v[224:231], v10\n\t" \
    "v_cvt_pk_bf16_f32 v158, v80, v81\n\t" \
    "ds_write_b32 v16, v158 offset:0\n\t" \
    "s_nop 5\n\t" \
    "v_pk_add_f32 v[84:85], v[84:85], v[86:87]\n\t" \
    "v_cvt_pk_bf16_f32 v159, v84, v85\n\t" \
    "ds_write_b32 v16, v159 offset:256\n\t" \
    "s_waitcnt lgkmcnt(0)\n\t" \
    "s_barrier\n\t" \
    "s_cmp_eq_u32 s65, 63\n\t" \
    "s_cbranch_scc1 qk_last83_%=\n\t" \
    "s_mov_b64 s[44:45], 0xfffff\n\t" \
    "s_sub_u32 s70, s80, s74\n\t" \
    "s_max_i32 s70, s70, 0\n\t" \
    "s_min_i32 s70, s70, 16000\n\t" \
    "s_lshl_b32 s70, s70, 1\n\t" \
    "s_mul_hi_u32 s70, s70, s77\n\t" \
    "s_add_u32 s70, s70, s73\n\t" \
    "qk_W99_%=:\n\t" \
    "s_memtime s[56:57]\n\t" \
    "s_waitcnt lgkmcnt(0)\n\t" \
    "s_sub_u32 s69, s70, s56\n\t" \
    "s_cmp_gt_i32 s69, 24\n\t" \
    "s_cbranch_scc1 qk_W99_%=\n\t" \
    "global_load_dword v160, v20, s[46:47] sc1\n\t" \
    "s_mov_b32 s71, 3\n\t" \
    "qk_E103_%=:\n\t" \
    "s_memtime s[54:55]\n\t" \
    "s_nop 0\n\t" \
    "s_memrealtime s[4:5]\n\t" \
    "s_nop 0\n\t" \
    "s_memrealtime s[6:7]\n\t" \
    "s_nop 0\n\t" \
    "s_memrealtime s[8:9]\n\t" \
    "s_nop 0\n\t" \
    "s_memrealtime s[10:11]\n\t" \
    "s_nop 0\n\t" \
    "s_memrealtime s[12:13]\n\t" \
    "s_nop 0\n\t" \
    "s_memrealtime s[14:15]\n\t" \
    "s_nop 0\n\t" \
    "s_memrealtime s[16:17]\n\t" \
    "s_nop 0\n\t" \
    "s_memrealtime s[18:19]\n\t" \
    "s_nop 0\n\t" \
    "s_memrealtime s[20:21]\n\t" \
    "s_nop 0\n\t" \
    "s_memrealtime s[22:23]\n\t" \
    "s_nop 0\n\t" \
    "s_memrealtime s[24:25]\n\t" \
    "s_nop 0\n\t" \
    "s_memrealtime s[26:27]\n\t" \
    "s_nop 0\n\t" \
    "s_memrealtime s[28:29]\n\t" \
    "s_nop 0\n\t" \
    "s_memtime s[56:57]\n\t" \
    "s_mov_b32 s70, 1\n\t" \
    "s_sub_u32 s71, s71, 1\n\t" \
    "s_cselect_b32 s69, 0, -1\n\t" \
    "s_waitcnt lgkmcnt(0)\n\t" \
    "s_sub_u32 s54, s56, s54\n\t" \
    "s_and_b32 s54, s54, s69\n\t" \
    "s_cmp_gt_u32 s54, 124\n\t" \
    "s_cbranch_scc1 qk_E103_%=\n\t" \
    "s_cmp_eq_u32 s4, s28\n\t" \
    "s_addc_u32 s70, s70, 0\n\t" \
    "s_cmp_eq_u32 s6, s28\n\t" \
    "s_addc_u32 s70, s70, 0\n\t" \
    "s_cmp_eq_u32 s8, s28\n\t" \
    "s_addc_u32 s70, s70, 0\n\t" \
    "s_cmp_eq_u32 s10, s28\n\t" \
    "s_addc_u32 s70, s70, 0\n\t" \
    "s_cmp_eq_u32 s12, s28\n\t" \
    "s_addc_u32 s70, s70, 0\n\t" \
    "s_cmp_eq_u32 s14, s28\n\t" \
    "s_addc_u32 s70, s70, 0\n\t" \
    "s_cmp_eq_u32 s16, s28\n\t" \
    "s_addc_u32 s70, s70, 0\n\t" \
    "s_cmp_eq_u32 s18, s28\n\t" \
    "s_addc_u32 s70, s70, 0\n\t" \
    "s_cmp_eq_u32 s20, s28\n\t" \
    "s_addc_u32 s70, s70, 0\n\t" \
    "s_cmp_eq_u32 s22, s28\n\t" \
    "s_addc_u32 s70, s70, 0\n\t" \
    "s_cmp_eq_u32 s24, s28\n\t" \
    "s_addc_u32 s70, s70, 0\n\t" \
    "s_cmp_eq_u32 s26, s28\n\t" \
    "s_addc_u32 s70, s70, 0\n\t" \
    "s_mov_b32 s75, s73\n\t" \
    "s_mov_b32 s76, s74\n\t" \
    "s_lshl_b32 s70, s70, 3\n\t" \
    "s_sub_u32 s73, s56, s70\n\t" \
    "s_lshl_b32 s74, s28, 4\n\t" \
    "s_sub_u32 s69, s73, s75\n\t" \
    "s_sub_u32 s70, s74, s76\n\t" \
    "s_sub_u32 s71, s70, 1600\n\t" \
    "s_cmp_lt_u32 s71, 400000\n\t" \
    "s_cbranch_scc0 qk_K105_%=\n\t" \
    "v_cvt_f32_u32 v161, s69\n\t" \
    "v_cvt_f32_u32 v162, s70\n\t" \
    "v_rcp_f32 v163, v161\n\t" \
    "v_rcp_f32 v164, v162\n\t" \
    "v_mul_f32 v163, v163, v162\n\t" \
    "v_mul_f32 v164, v164, v161\n\t" \
    "v_mul_f32 v163, 0x4f800000, v163\n\t" \
    "v_mul_f32 v164, 0x4f000000, v164\n\t" \
    "v_cvt_u32_f32 v163, v163\n\t" \
    "v_cvt_u32_f32 v164, v164\n\t" \
    "v_readfirstlane_b32 s69, v163\n\t" \
    "v_readfirstlane_b32 s70, v164\n\t" \
    "s_nop 1\n\t" \
    "s_sub_u32 s69, s69, s78\n\t" \
    "s_ashr_i32 s69, s69, 2\n\t" \
    "s_add_u32 s78, s78, s69\n\t" \
    "s_sub_u32 s70, s70, s77\n\t" \
    "s_ashr_i32 s70, s70, 2\n\t" \
    "s_add_u32 s77, s77, s70\n\t" \
    "qk_K105_%=:\n\t" \
    "s_waitcnt vmcnt(0)\n\t" \
    "v_cmp_eq_u32 vcc, -1, v160\n\t" \
    "s_cbranch_vccz qk_Hok101_%=\n\t" \
    "qk_H100_%=:\n\t" \
    "global_load_dword v160, v20, s[46:47] sc1\n\t" \
    "s_waitcnt vmcnt(0)\n\t" \
    "v_cmp_eq_u32 vcc, -1, v160\n\t" \
    "s_cbranch_vccz qk_Hc102_%=\n\t" \
    "s_cmp_eq_u32 s68, 0\n\t" \
    "s_cbranch_scc1 qk_F106_%=\n\t" \
    "s_sub_u32 s68, s68, 1\n\t" \
    "s_sleep 1\n\t" \
    "s_branch qk_H100_%=\n\t" \
    "qk_F106_%=:\n\t" \
    "v_mov_b32 v161, 6095104\n\t" \
    "v_mov_b32 v162, 5\n\t" \
    "global_store_dword v161, v162, s[36:37] sc1\n\t" \
    "s_waitcnt vmcnt(0)\n\t" \
    "s_branch qk_Hc102_%=\n\t" \
    "qk_Hc102_%=:\n\t" \
    "qk_Hok101_%=:\n\t" \
    "v_subrev_u32 v160, s80, v160\n\t" \
    "v_and_b32 v20, -5, v20\n\t" \
    "s_nop 0\n\t" \
    "v_max_i32_dpp v160, v160, v160 row_ror:1 row_mask:0xf bank_mask:0xf\n\t" \
    "s_nop 1\n\t" \
    "v_max_i32_dpp v160, v160, v160 row_ror:2 row_mask:0xf bank_mask:0xf\n\t" \
    "s_nop 1\n\t" \
    "v_max_i32_dpp v160, v160, v160 row_ror:4 row_mask:0xf bank_mask:0xf\n\t" \
    "s_nop 1\n\t" \
    "v_max_i32_dpp v160, v160, v160 row_ror:8 row_mask:0xf bank_mask:0xf\n\t" \
    "s_nop 0\n\t" \
    "v_readlane_b32 s69, v160, 0\n\t" \
    "v_readlane_b32 s71, v160, 16\n\t" \
    "v_readlane_b32 s70, v160, 32\n\t" \
    "v_readlane_b32 s81, v160, 48\n\t" \
    "s_max_i32 s69, s69, s71\n\t" \
    "s_nop 0\n\t" \
    "s_max_i32 s71, s70, s81\n\t" \
    "s_max_i32 s69, s69, s71\n\t" \
    "s_add_u32 s81, s69, s80\n\t" \
    "v_mov_b32 v72, 0\n\t" \
    "v_mov_b32 v73, 0\n\t" \
    "v_mov_b32 v74, 0\n\t" \
    "v_mov_b32 v75, 0\n\t" \
    "v_mov_b32 v76, 0\n\t" \
    "v_mov_b32 v77, 0\n\t" \
    "v_mov_b32 v78, 0\n\t" \
    "v_mov_b32 v79, 0\n\t" \
    "v_mov_b32 v80, 0\n\t" \
    "v_mov_b32 v81, 0\n\t" \
    "v_mov_b32 v82, 0\n\t" \
    "v_mov_b32 v83, 0\n\t" \
    "v_mov_b32 v84, 0\n\t" \
    "v_mov_b32 v85, 0\n\t" \
    "v_mov_b32 v86, 0\n\t" \
    "v_mov_b32 v87, 0\n\t" \
    "s_cmp_lg_u32 s65, 0\n\t" \
    "s_cbranch_scc1 qk_Oc109_%=\n\t" \
    "s_add_u32 s79, s81, 240\n\t" \
    "s_sub_u32 s70, s79, s74\n\t" \
    "s_max_i32 s70, s70, 0\n\t" \
    "s_min_i32 s70, s70, 16000\n\t" \
    "s_lshl_b32 s70, s70, 1\n\t" \
    "s_mul_hi_u32 s70, s70, s77\n\t" \
    "s_add_u32 s70, s70, s73\n\t" \
    "qk_W110_%=:\n\t" \
    "s_memtime s[56:57]\n\t" \
    "s_waitcnt lgkmcnt(0)\n\t" \
    "s_sub_u32 s69, s70, s56\n\t" \
    "s_cmp_gt_i32 s69, 24\n\t" \
    "s_cbranch_scc1 qk_W110_%=\n\t" \
    "global_load_dword v160, v35, s[46:47] sc1\n\t" \
    "s_waitcnt vmcnt(0)\n\t" \
    "v_cmp_eq_u32 vcc, -1, v160\n\t" \
    "s_cbranch_vccz qk_Ook108_%=\n\t" \
    "qk_O107_%=:\n\t" \
    "global_load_dword v160, v35, s[46:47] offset:1536 sc1\n\t" \
    "s_waitcnt vmcnt(0)\n\t" \
    "v_cmp_eq_u32 vcc, -1, v160\n\t" \
    "s_cbranch_vccz qk_Ook108_%=\n\t" \
    "s_cmp_eq_u32 s68, 0\n\t" \
    "s_cbranch_scc1 qk_F111_%=\n\t" \
    "s_sub_u32 s68, s68, 1\n\t" \
    "s_sleep 3\n\t" \
    "s_branch qk_O107_%=\n\t" \
    "qk_F111_%=:\n\t" \
    "v_mov_b32 v161, 6095104\n\t" \
    "v_mov_b32 v162, 16\n\t" \
    "global_store_dword v161, v162, s[36:37] sc1\n\t" \
    "s_waitcnt vmcnt(0)\n\t" \
    "s_branch qk_Ook108_%=\n\t" \
    "qk_Ook108_%=:\n\t" \
    "v_subrev_u32 v160, s80, v160\n\t" \
    "s_nop 1\n\t" \
    "v_max_i32_dpp v160, v160, v160 row_ror:1 row_mask:0xf bank_mask:0xf\n\t" \
    "s_nop 1\n\t" \
    "v_max_i32_dpp v160, v160, v160 row_ror:2 row_mask:0xf bank_mask:0xf\n\t" \
    "s_nop 1\n\t" \
    "v_max_i32_dpp v160, v160, v160 row_ror:4 row_mask:0xf bank_mask:0xf\n\t" \
    "s_nop 1\n\t" \
    "v_max_i32_dpp v160, v160, v160 row_ror:8 row_mask:0xf bank_mask:0xf\n\t" \
    "s_nop 0\n\t" \
    "v_readlane_b32 s69, v160, 0\n\t" \
    "v_readlane_b32 s71, v160, 16\n\t" \
    "v_readlane_b32 s70, v160, 32\n\t" \
    "v_readlane_b32 s81, v160, 48\n\t" \
    "s_max_i32 s69, s69, s71\n\t" \
    "s_nop 0\n\t" \
    "s_max_i32 s71, s70, s81\n\t" \
    "s_max_i32 s69, s69, s71\n\t" \
    "s_add_u32 s81, s69, s80\n\t" \
    "qk_Oc109_%=:\n\t" \
    "s_mul_hi_u32 s69, s85, s78\n\t" \
    "s_add_u32 s79, s81, s69\n\t" \
    "s_add_u32 s79, s79, s87\n\t" \
    "s_cmp_ge_u32 s65, 36\n\t" \
    "s_cselect_b32 s69, 28, 0\n\t" \
    "s_sub_u32 s79, s79, s69\n\t" \
    "s_cmp_lt_u32 s65, 8\n\t" \
    "s_cselect_b32 s69, 96, 0\n\t" \
    "s_add_u32 s79, s79, s69\n\t" \
    "s_cmp_ge_u32 s65, 57\n\t" \
    "s_cselect_b32 s69, 32, 0\n\t" \
    "s_add_u32 s79, s79, s69\n\t" \
    "s_add_u32 s80, s79, s88\n\t" \
    "s_sub_u32 s79, s79, s74\n\t" \
    "s_max_i32 s79, s79, 0\n\t" \
    "s_min_i32 s79, s79, 16000\n\t" \
    "s_lshl_b32 s79, s79, 1\n\t" \
    "s_mul_hi_u32 s79, s79, s77\n\t" \
    "s_add_u32 s79, s79, s73\n\t" \
    "v_mov_b32 v160, s79\n\t" \
    "ds_write_b32 v11, v160 offset:20480\n\t" \
    "s_add_u32 s65, s65, 1\n\t" \
    "v_add_u32 v31, 1, v31\n\t" \
    "s_add_u32 s69, s65, 0\n\t" \
    "s_bitcmp1_b32 s69, 0\n\t" \
    "s_cselect_b32 s70, s63, s62\n\t" \
    "s_bfe_u32 s69, s69, 0x20001\n\t" \
    "s_lshl_b32 s69, s69, 19\n\t" \
    "s_add_u32 s69, s69, s70\n\t" \
    "s_add_u32 s46, s36, s69\n\t" \
    "s_addc_u32 s47, s37, 0\n\t" \
    "s_mov_b64 s[42:43], s[46:47]\n\t" \
    "s_add_u32 s69, s65, 0\n\t" \
    "s_lshr_b32 s69, s69, 1\n\t" \
    "s_xor_b32 s69, s69, s97\n\t" \
    "s_and_b32 s69, s69, 1\n\t" \
    "s_lshl_b32 s69, s69, 14\n\t" \
    "s_or_b32 s46, s46, s69\n\t" \
    "s_waitcnt lgkmcnt(0)\n\t" \
    "s_barrier\n\t" \
    "s_branch qk_go88_%=\n\t" \
    "qk_Gbad94_%=:\n\t" \
    "s_waitcnt vmcnt(0) lgkmcnt(0)\n\t" \
    "s_nop 7\n\t" \
    "v_mov_b32 v72, 0\n\t" \
    "v_mov_b32 v73, 0\n\t" \
    "v_mov_b32 v74, 0\n\t" \
    "v_mov_b32 v75, 0\n\t" \
    "v_mov_b32 v76, 0\n\t" \
    "v_mov_b32 v77, 0\n\t" \
    "v_mov_b32 v78, 0\n\t" \
    "v_mov_b32 v79, 0\n\t" \
    "v_mov_b32 v80, 0\n\t" \
    "v_mov_b32 v81, 0\n\t" \
    "v_mov_b32 v82, 0\n\t" \
    "v_mov_b32 v83, 0\n\t" \
    "v_mov_b32 v84, 0\n\t" \
    "v_mov_b32 v85, 0\n\t" \
    "v_mov_b32 v86, 0\n\t" \
    "v_mov_b32 v87, 0\n\t" \
    "s_cmp_eq_u32 s68, 0\n\t" \
    "s_cbranch_scc1 qk_F112_%=\n\t" \
    "s_sub_u32 s68, s68, 1\n\t" \
    "s_branch qk_G93_%=\n\t" \
    "qk_F112_%=:\n\t" \
    "v_mov_b32 v160, 6095104\n\t" \
    "v_mov_b32 v161, 1\n\t" \
    "global_store_dword v160, v161, s[36:37] sc1\n\t" \
    "s_waitcnt vmcnt(0)\n\t" \
    "s_branch qk_Gc96_%=\n\t" \
    "qk_Gbad295_%=:\n\t" \
    "global_load_dwordx4 v[44:47], v2, s[46:47] sc1\n\t" \
    "s_waitcnt vmcnt(0)\n\t" \
    "v_max3_u32 v160, v44, v45, v46\n\t" \
    "v_max_u32 v160, v160, v47\n\t" \
    "v_cmp_eq_u32 vcc, -1, v160\n\t" \
    "s_cbranch_vccz qk_Gre298_%=\n\t" \
    "s_cmp_eq_u32 s68, 0\n\t" \
    "s_cbranch_scc1 qk_F113_%=\n\t" \
    "s_sub_u32 s68, s68, 1\n\t" \
    "s_sleep 1\n\t" \
    "s_branch qk_Gbad295_%=\n\t" \
    "qk_F113_%=:\n\t" \
    "v_mov_b32 v160, 6095104\n\t" \
    "v_mov_b32 v161, 2\n\t" \
    "global_store_dword v160, v161, s[36:37] sc1\n\t" \
    "s_waitcnt vmcnt(0)\n\t" \
    "s_branch qk_Gre298_%=\n\t" \
    "qk_Gre298_%=:\n\t" \
    "v_mov_b32 v72, 0\n\t" \
    "v_mov_b32 v73, 0\n\t" \
    "v_mov_b32 v74, 0\n\t" \
    "v_mov_b32 v75, 0\n\t" \
    "v_mov_b32 v76, 0\n\t" \
    "v_mov_b32 v77, 0\n\t" \
    "v_mov_b32 v78, 0\n\t" \
    "v_mov_b32 v79, 0\n\t" \
    "v_mov_b32 v80, 0\n\t" \
    "v_mov_b32 v81, 0\n\t" \
    "v_mov_b32 v82, 0\n\t" \
    "v_mov_b32 v83, 0\n\t" \
    "v_mov_b32 v84, 0\n\t" \
    "v_mov_b32 v85, 0\n\t" \
    "v_mov_b32 v86, 0\n\t" \
    "v_mov_b32 v87, 0\n\t" \
    "v_smfmac_f32_16x16x64_bf16 v[72:75], v[36:39], a[48:55], v10\n\t" \
    "v_smfmac_f32_16x16x64_bf16 v[76:79], v[36:39], a[120:127], v10\n\t" \
    "v_smfmac_f32_16x16x64_bf16 v[80:83], v[36:39], a[192:199], v10\n\t" \
    "v_smfmac_f32_16x16x64_bf16 v[84:87], v[36:39], v[232:239], v10\n\t" \
    "v_smfmac_f32_16x16x64_bf16 v[72:75], v[40:43], a[56:63], v10\n\t" \
    "v_smfmac_f32_16x16x64_bf16 v[76:79], v[40:43], a[128:135], v10\n\t" \
    "v_smfmac_f32_16x16x64_bf16 v[80:83], v[40:43], a[200:207], v10\n\t" \
    "v_smfmac_f32_16x16x64_bf16 v[84:87], v[40:43], v[240:247], v10\n\t" \
    "v_smfmac_f32_16x16x64_bf16 v[72:75], v[44:47], a[64:71], v10\n\t" \
    "v_smfmac_f32_16x16x64_bf16 v[76:79], v[44:47], a[136:143], v10\n\t" \
    "v_smfmac_f32_16x16x64_bf16 v[80:83], v[44:47], a[208:215], v10\n\t" \
    "v_smfmac_f32_16x16x64_bf16 v[84:87], v[44:47], v[248:255], v10\n\t" \
    "s_waitcnt lgkmcnt(0)\n\t" \
    "s_memtime s[56:57]\n\t" \
    "s_waitcnt lgkmcnt(0)\n\t" \
    "s_sub_u32 s69, s56, s73\n\t" \
    "s_mul_hi_u32 s69, s69, s78\n\t" \
    "s_branch qk_Gc297_%=\n\t" \
    "qk_last83_%=:\n\t" \
    "s_endpgm\n\t" \
    "vv_begin_%=:\n\t" \
    "v_mbcnt_lo_u32_b32 v223, -1, 0\n\t" \
    "v_mbcnt_hi_u32_b32 v223, -1, v223\n\t" \
    "s_mov_b32 s68, 262144\n\t" \
    "s_mov_b32 s72, 0\n\t" \
    "s_mov_b32 s83, -1\n\t" \
    "s_mov_b32 s65, 0\n\t" \
    "v_and_b32 v180, 3, v223\n\t" \
    "v_lshlrev_b32 v202, 3, v180\n\t" \
    "v_lshrrev_b32 v181, 5, v223\n\t" \
    "v_lshl_add_u32 v202, v181, 12, v202\n\t" \
    "v_bfe_u32 v182, v223, 2, 2\n\t" \
    "s_mov_b32 s70, 288\n\t" \
    "v_mad_u32_u24 v202, v182, s70, v202\n\t" \
    "v_bfe_u32 v183, v223, 4, 1\n\t" \
    "s_mov_b32 s70, 2176\n\t" \
    "v_mad_u32_u24 v202, v183, s70, v202\n\t" \
    "s_lshl_b32 s69, s66, 6\n\t" \
    "v_xor_b32 v202, s69, v202\n\t" \
    "v_xor_b32 v203, 32, v202\n\t" \
    "s_mul_i32 s69, s66, 9216\n\t" \
    "v_and_b32 v184, 1, v223\n\t" \
    "v_lshl_add_u32 v201, v184, 4, s69\n\t" \
    "v_lshrrev_b32 v185, 3, v223\n\t" \
    "s_mov_b32 s70, 1152\n\t" \
    "v_mad_u32_u24 v201, v185, s70, v201\n\t" \
    "v_bfe_u32 v186, v223, 1, 2\n\t" \
    "v_lshlrev_b32 v204, 5, v186\n\t" \
    "v_lshrrev_b32 v187, 4, v223\n\t" \
    "v_lshlrev_b32 v205, 5, v187\n\t" \
    "v_xor_b32 v204, v204, v205\n\t" \
    "v_add_u32 v201, v201, v204\n\t" \
    "s_mul_i32 s96, s66, 9216\n\t" \
    "v_lshlrev_b32 v207, 3, v180\n\t" \
    "v_bfe_u32 v188, v223, 2, 1\n\t" \
    "v_lshl_add_u32 v207, v188, 7, v207\n\t" \
    "v_bfe_u32 v189, v223, 3, 2\n\t" \
    "s_mov_b32 s70, 288\n\t" \
    "v_mad_u32_u24 v207, v189, s70, v207\n\t" \
    "s_mov_b32 s70, 9216\n\t" \
    "v_mad_u32_u24 v207, v181, s70, v207\n\t" \
    "s_mul_i32 s69, s66, 9\n\t" \
    "s_add_u32 s69, s69, 0\n\t" \
    "s_and_b32 s70, s69, 3\n\t" \
    "s_lshl_b32 s70, s70, 5\n\t" \
    "s_lshr_b32 s69, s69, 2\n\t" \
    "s_lshl_b32 s69, s69, 10\n\t" \
    "v_xor_b32 v204, s70, v207\n\t" \
    "v_add_u32 v204, s69, v204\n\t" \
    "s_mul_i32 s69, s66, 9\n\t" \
    "s_add_u32 s69, s69, 1\n\t" \
    "s_and_b32 s70, s69, 3\n\t" \
    "s_lshl_b32 s70, s70, 5\n\t" \
    "s_lshr_b32 s69, s69, 2\n\t" \
    "s_lshl_b32 s69, s69, 10\n\t" \
    "v_xor_b32 v205, s70, v207\n\t" \
    "v_add_u32 v205, s69, v205\n\t" \
    "s_mul_i32 s69, s66, 9\n\t" \
    "s_add_u32 s69, s69, 2\n\t" \
    "s_and_b32 s70, s69, 3\n\t" \
    "s_lshl_b32 s70, s70, 5\n\t" \
    "s_lshr_b32 s69, s69, 2\n\t" \
    "s_lshl_b32 s69, s69, 10\n\t" \
    "v_xor_b32 v206, s70, v207\n\t" \
    "v_add_u32 v206, s69, v206\n\t" \
    "s_mul_i32 s69, s66, 9\n\t" \
    "s_add_u32 s69, s69, 3\n\t" \
    "s_and_b32 s70, s69, 3\n\t" \
    "s_lshl_b32 s70, s70, 5\n\t" \
    "s_lshr_b32 s69, s69, 2\n\t" \
    "s_lshl_b32 s69, s69, 10\n\t" \
    "v_xor_b32 v207, s70, v207\n\t" \
    "v_add_u32 v207, s69, v207\n\t" \
    "s_waitcnt vmcnt(12) lgkmcnt(0)\n\t" \
    "s_barrier\n\t" \
    "s_add_u32 m0, s82, 65536\n\t" \
    "s_nop 0\n\t" \
    "global_load_lds_dwordx4 v200, s[42:43]\n\t" \
    "v_add_u32 v200, 131072, v200\n\t" \
    "s_add_u32 m0, s82, 69632\n\t" \
    "s_nop 0\n\t" \
    "global_load_lds_dwordx4 v200, s[42:43]\n\t" \
    "v_add_u32 v200, 131072, v200\n\t" \
    "s_add_u32 m0, s82, 73728\n\t" \
    "s_nop 0\n\t" \
    "global_load_lds_dwordx4 v200, s[42:43]\n\t" \
    "v_add_u32 v200, 131072, v200\n\t" \
    "s_add_u32 m0, s82, 77824\n\t" \
    "s_nop 0\n\t" \
    "global_load_lds_dwordx4 v200, s[42:43]\n\t" \
    "v_add_u32 v200, 131072, v200\n\t" \
    "ds_read_b64_tr_b16 a[0:1], v202 offset:36864\n\t" \
    "ds_read_b64_tr_b16 a[2:3], v202 offset:37888\n\t" \
    "ds_read_b64_tr_b16 a[4:5], v202 offset:45056\n\t" \
    "ds_read_b64_tr_b16 a[6:7], v202 offset:46080\n\t" \
    "ds_read_b64_tr_b16 a[72:73], v203 offset:36864\n\t" \
    "ds_read_b64_tr_b16 a[74:75], v203 offset:37888\n\t" \
    "ds_read_b64_tr_b16 a[76:77], v203 offset:45056\n\t" \
    "ds_read_b64_tr_b16 a[78:79], v203 offset:46080\n\t" \
    "v_lshlrev_b32 v0, 4, v223\n\t" \
    "s_mul_i32 s69, s66, 2048\n\t" \
    "s_bfe_u32 s71, s67, 0x10004\n\t" \
    "s_mul_i32 s71, s71, 131072\n\t" \
    "s_add_u32 s69, s69, s71\n\t" \
    "s_add_u32 s69, s69, 65536\n\t" \
    "v_lshl_add_u32 v1, v223, 4, s69\n\t" \
    "s_bfe_u32 s71, s67, 0x10004\n\t" \
    "s_mul_i32 s69, s71, 131072\n\t" \
    "s_add_u32 s69, s69, 102400\n\t" \
    "v_lshl_add_u32 v2, v223, 4, s69\n\t" \
    "v_xor_b32 v34, 131072, v1\n\t" \
    "s_mov_b32 s69, 17476\n\t" \
    "v_bfe_u32 v190, v223, 1, 1\n\t" \
    "v_mov_b32 v10, s69\n\t" \
    "s_mov_b32 s70, 43690\n\t" \
    "v_mad_u32_u24 v10, v190, s70, v10\n\t" \
    "v_mov_b32 v11, 0\n\t" \
    "s_waitcnt vmcnt(12) lgkmcnt(0)\n\t" \
    "s_barrier\n\t" \
    "s_add_u32 m0, s82, 81920\n\t" \
    "s_nop 0\n\t" \
    "global_load_lds_dwordx4 v200, s[42:43]\n\t" \
    "v_add_u32 v200, 131072, v200\n\t" \
    "s_add_u32 m0, s82, 86016\n\t" \
    "s_nop 0\n\t" \
    "global_load_lds_dwordx4 v200, s[42:43]\n\t" \
    "v_add_u32 v200, 131072, v200\n\t" \
    "s_add_u32 m0, s82, 90112\n\t" \
    "s_nop 0\n\t" \
    "global_load_lds_dwordx4 v200, s[42:43]\n\t" \
    "v_add_u32 v200, 131072, v200\n\t" \
    "s_add_u32 m0, s82, 94208\n\t" \
    "s_nop 0\n\t" \
    "global_load_lds_dwordx4 v200, s[42:43]\n\t" \
    "v_add_u32 v200, 131072, v200\n\t" \
    "ds_read_b64_tr_b16 a[8:9], v202 offset:53248\n\t" \
    "ds_read_b64_tr_b16 a[10:11], v202 offset:54272\n\t" \
    "ds_read_b64_tr_b16 a[12:13], v202 offset:61440\n\t" \
    "ds_read_b64_tr_b16 a[14:15], v202 offset:62464\n\t" \
    "ds_read_b64_tr_b16 a[80:81], v203 offset:53248\n\t" \
    "ds_read_b64_tr_b16 a[82:83], v203 offset:54272\n\t" \
    "ds_read_b64_tr_b16 a[84:85], v203 offset:61440\n\t" \
    "ds_read_b64_tr_b16 a[86:87], v203 offset:62464\n\t" \
    "s_mul_i32 s69, s66, 512\n\t" \
    "s_add_u32 s69, s69, 16384\n\t" \
    "v_and_b32 v191, 13, v223\n\t" \
    "v_lshl_add_u32 v12, v191, 5, s69\n\t" \
    "v_lshl_add_u32 v12, v190, 2, v12\n\t" \
    "v_lshl_add_u32 v12, v187, 3, v12\n\t" \
    "s_mov_b32 s69, 16384\n\t" \
    "v_and_b32 v192, 15, v223\n\t" \
    "v_lshl_add_u32 v13, v192, 3, s69\n\t" \
    "v_lshl_add_u32 v13, v187, 8, v13\n\t" \
    "s_bfe_u32 s71, s67, 0x10004\n\t" \
    "s_mul_i32 s69, s71, 32768\n\t" \
    "s_add_u32 s69, s69, 266240\n\t" \
    "v_and_b32 v193, 31, v223\n\t" \
    "v_lshl_add_u32 v20, v193, 5, s69\n\t" \
    "v_lshl_add_u32 v20, v181, 2, v20\n\t" \
    "s_mul_i32 s69, s67, 32\n\t" \
    "s_add_u32 s69, s69, 262144\n\t" \
    "v_lshl_add_u32 v21, v180, 14, s69\n\t" \
    "v_bfe_u32 v194, v223, 2, 3\n\t" \
    "v_lshlrev_b32 v199, 10, v194\n\t" \
    "v_min_u32 v199, 4096, v199\n\t" \
    "v_add_u32 v21, v199, v21\n\t" \
    "s_bfe_u32 s71, s67, 0x10004\n\t" \
    "s_mul_i32 s69, s71, 32768\n\t" \
    "s_add_u32 s69, s69, 267264\n\t" \
    "v_min_u32 v35, 35, v223\n\t" \
    "v_lshl_add_u32 v35, v35, 5, s69\n\t" \
    "v_lshlrev_b32 v22, 2, v223\n\t" \
    "s_waitcnt vmcnt(12) lgkmcnt(0)\n\t" \
    "s_barrier\n\t" \
    "s_add_u32 m0, s82, 98304\n\t" \
    "s_nop 0\n\t" \
    "global_load_lds_dwordx4 v200, s[42:43]\n\t" \
    "v_add_u32 v200, 131072, v200\n\t" \
    "s_add_u32 m0, s82, 102400\n\t" \
    "s_nop 0\n\t" \
    "global_load_lds_dwordx4 v200, s[42:43]\n\t" \
    "v_add_u32 v200, 131072, v200\n\t" \
    "s_add_u32 m0, s82, 106496\n\t" \
    "s_nop 0\n\t" \
    "global_load_lds_dwordx4 v200, s[42:43]\n\t" \
    "v_add_u32 v200, 131072, v200\n\t" \
    "s_add_u32 m0, s82, 110592\n\t" \
    "s_nop 0\n\t" \
    "global_load_lds_dwordx4 v200, s[42:43]\n\t" \
    "v_add_u32 v200, 131072, v200\n\t" \
    "v_add_u32 v202, 69632, v202\n\t" \
    "v_add_u32 v203, 69632, v203\n\t" \
    "ds_read_b64_tr_b16 a[16:17], v202 offset:0\n\t" \
    "ds_read_b64_tr_b16 a[18:19], v202 offset:1024\n\t" \
    "ds_read_b64_tr_b16 a[20:21], v202 offset:8192\n\t" \
    "ds_read_b64_tr_b16 a[22:23], v202 offset:9216\n\t" \
    "ds_read_b64_tr_b16 a[88:89], v203 offset:0\n\t" \
    "ds_read_b64_tr_b16 a[90:91], v203 offset:1024\n\t" \
    "ds_read_b64_tr_b16 a[92:93], v203 offset:8192\n\t" \
    "ds_read_b64_tr_b16 a[94:95], v203 offset:9216\n\t" \
    "s_mul_i32 s69, s66, 147456\n\t" \
    "s_mul_i32 s71, s67, 256\n\t" \
    "s_add_u32 s69, s69, s71\n\t" \
    "v_lshl_add_u32 v14, v192, 4, s69\n\t" \
    "v_lshl_add_u32 v14, v187, 14, v14\n\t" \
    "s_mul_i32 s69, s66, 147456\n\t" \
    "s_mul_i32 s71, s67, 256\n\t" \
    "s_add_u32 s69, s69, s71\n\t" \
    "s_add_u32 s69, s69, 65536\n\t" \
    "v_lshl_add_u32 v15, v192, 4, s69\n\t" \
    "v_lshl_add_u32 v15, v187, 14, v15\n\t" \
    "s_mul_i32 s69, s66, 147456\n\t" \
    "s_mul_i32 s71, s67, 256\n\t" \
    "s_add_u32 s69, s69, s71\n\t" \
    "s_add_u32 s69, s69, 131072\n\t" \
    "v_lshl_add_u32 v16, v192, 4, s69\n\t" \
    "v_lshl_add_u32 v16, v187, 2, v16\n\t" \
    "s_mul_i32 s97, s67, 171\n\t" \
    "s_lshr_b32 s97, s97, 9\n\t" \
    "s_mul_i32 s97, s97, 131072\n\t" \
    "s_mul_i32 s71, s66, 2304\n\t" \
    "s_add_u32 s69, s97, s71\n\t" \
    "s_mul_i32 s71, s67, 655360\n\t" \
    "s_add_u32 s69, s69, s71\n\t" \
    "v_lshl_add_u32 v17, v223, 4, s69\n\t" \
    "s_mul_i32 s71, s66, 2304\n\t" \
    "s_add_u32 s69, s97, s71\n\t" \
    "s_mul_i32 s71, s67, 655360\n\t" \
    "s_add_u32 s69, s69, s71\n\t" \
    "s_add_u32 s69, s69, 2048\n\t" \
    "v_lshl_add_u32 v18, v192, 4, s69\n\t" \
    "v_lshl_add_u32 v18, v187, 2, v18\n\t" \
    "s_waitcnt vmcnt(12) lgkmcnt(0)\n\t" \
    "s_barrier\n\t" \
    "s_add_u32 m0, s82, 0\n\t" \
    "s_nop 0\n\t" \
    "global_load_lds_dwordx4 v200, s[42:43]\n\t" \
    "v_add_u32 v200, 131072, v200\n\t" \
    "s_add_u32 m0, s82, 4096\n\t" \
    "s_nop 0\n\t" \
    "global_load_lds_dwordx4 v200, s[42:43]\n\t" \
    "v_add_u32 v200, 131072, v200\n\t" \
    "s_add_u32 m0, s82, 8192\n\t" \
    "s_nop 0\n\t" \
    "global_load_lds_dwordx4 v200, s[42:43]\n\t" \
    "v_add_u32 v200, 131072, v200\n\t" \
    "s_add_u32 m0, s82, 12288\n\t" \
    "s_nop 0\n\t" \
    "global_load_lds_dwordx4 v200, s[42:43]\n\t" \
    "v_add_u32 v200, 131072, v200\n\t" \
    "ds_read_b64_tr_b16 a[24:25], v202 offset:16384\n\t" \
    "ds_read_b64_tr_b16 a[26:27], v202 offset:17408\n\t" \
    "ds_read_b64_tr_b16 a[28:29], v202 offset:24576\n\t" \
    "ds_read_b64_tr_b16 a[30:31], v202 offset:25600\n\t" \
    "ds_read_b64_tr_b16 a[96:97], v203 offset:16384\n\t" \
    "ds_read_b64_tr_b16 a[98:99], v203 offset:17408\n\t" \
    "ds_read_b64_tr_b16 a[100:101], v203 offset:24576\n\t" \
    "ds_read_b64_tr_b16 a[102:103], v203 offset:25600\n\t" \
    "s_mov_b32 s77, 3019898880\n\t" \
    "s_mov_b32 s78, 3054198966\n\t" \
    "s_mov_b32 s71, 3\n\t" \
    "vv_E1_%=:\n\t" \
    "s_memtime s[54:55]\n\t" \
    "s_nop 0\n\t" \
    "s_memrealtime s[4:5]\n\t" \
    "s_nop 0\n\t" \
    "s_memrealtime s[6:7]\n\t" \
    "s_nop 0\n\t" \
    "s_memrealtime s[8:9]\n\t" \
    "s_nop 0\n\t" \
    "s_memrealtime s[10:11]\n\t" \
    "s_nop 0\n\t" \
    "s_memrealtime s[12:13]\n\t" \
    "s_nop 0\n\t" \
    "s_memrealtime s[14:15]\n\t" \
    "s_nop 0\n\t" \
    "s_memrealtime s[16:17]\n\t" \
    "s_nop 0\n\t" \
    "s_memrealtime s[18:19]\n\t" \
    "s_nop 0\n\t" \
    "s_memrealtime s[20:21]\n\t" \
    "s_nop 0\n\t" \
    "s_memrealtime s[22:23]\n\t" \
    "s_nop 0\n\t" \
    "s_memrealtime s[24:25]\n\t" \
    "s_nop 0\n\t" \
    "s_memrealtime s[26:27]\n\t" \
    "s_nop 0\n\t" \
    "s_memrealtime s[28:29]\n\t" \
    "s_nop 0\n\t" \
    "s_memtime s[56:57]\n\t" \
    "s_mov_b32 s70, 1\n\t" \
    "s_sub_u32 s71, s71, 1\n\t" \
    "s_cselect_b32 s69, 0, -1\n\t" \
    "s_waitcnt lgkmcnt(0)\n\t" \
    "s_sub_u32 s54, s56, s54\n\t" \
    "s_and_b32 s54, s54, s69\n\t" \
    "s_cmp_gt_u32 s54, 124\n\t" \
    "s_cbranch_scc1 vv_E1_%=\n\t" \
    "s_cmp_eq_u32 s4, s28\n\t" \
    "s_addc_u32 s70, s70, 0\n\t" \
    "s_cmp_eq_u32 s6, s28\n\t" \
    "s_addc_u32 s70, s70, 0\n\t" \
    "s_cmp_eq_u32 s8, s28\n\t" \
    "s_addc_u32 s70, s70, 0\n\t" \
    "s_cmp_eq_u32 s10, s28\n\t" \
    "s_addc_u32 s70, s70, 0\n\t" \
    "s_cmp_eq_u32 s12, s28\n\t" \
    "s_addc_u32 s70, s70, 0\n\t" \
    "s_cmp_eq_u32 s14, s28\n\t" \
    "s_addc_u32 s70, s70, 0\n\t" \
    "s_cmp_eq_u32 s16, s28\n\t" \
    "s_addc_u32 s70, s70, 0\n\t" \
    "s_cmp_eq_u32 s18, s28\n\t" \
    "s_addc_u32 s70, s70, 0\n\t" \
    "s_cmp_eq_u32 s20, s28\n\t" \
    "s_addc_u32 s70, s70, 0\n\t" \
    "s_cmp_eq_u32 s22, s28\n\t" \
    "s_addc_u32 s70, s70, 0\n\t" \
    "s_cmp_eq_u32 s24, s28\n\t" \
    "s_addc_u32 s70, s70, 0\n\t" \
    "s_cmp_eq_u32 s26, s28\n\t" \
    "s_addc_u32 s70, s70, 0\n\t" \
    "s_mov_b32 s75, s73\n\t" \
    "s_mov_b32 s76, s74\n\t" \
    "s_lshl_b32 s70, s70, 3\n\t" \
    "s_sub_u32 s73, s56, s70\n\t" \
    "s_lshl_b32 s74, s28, 4\n\t" \
    "s_waitcnt vmcnt(12) lgkmcnt(0)\n\t" \
    "s_barrier\n\t" \
    "s_add_u32 m0, s82, 16384\n\t" \
    "s_nop 0\n\t" \
    "global_load_lds_dwordx4 v200, s[42:43]\n\t" \
    "v_add_u32 v200, 131072, v200\n\t" \
    "s_add_u32 m0, s82, 20480\n\t" \
    "s_nop 0\n\t" \
    "global_load_lds_dwordx4 v200, s[42:43]\n\t" \
    "v_add_u32 v200, 131072, v200\n\t" \
    "s_add_u32 m0, s82, 24576\n\t" \
    "s_nop 0\n\t" \
    "global_load_lds_dwordx4 v200, s[42:43]\n\t" \
    "v_add_u32 v200, 131072, v200\n\t" \
    "s_add_u32 m0, s82, 28672\n\t" \
    "s_nop 0\n\t" \
    "global_load_lds_dwordx4 v200, s[42:43]\n\t" \
    "v_add_u32 v200, 131072, v200\n\t" \
    "ds_read_b64_tr_b16 a[32:33], v202 offset:32768\n\t" \
    "ds_read_b64_tr_b16 a[34:35], v202 offset:33792\n\t" \
    "ds_read_b64_tr_b16 a[36:37], v202 offset:40960\n\t" \
    "ds_read_b64_tr_b16 a[38:39], v202 offset:41984\n\t" \
    "ds_read_b64_tr_b16 a[104:105], v203 offset:32768\n\t" \
    "ds_read_b64_tr_b16 a[106:107], v203 offset:33792\n\t" \
    "ds_read_b64_tr_b16 a[108:109], v203 offset:40960\n\t" \
    "ds_read_b64_tr_b16 a[110:111], v203 offset:41984\n\t" \
    "s_waitcnt vmcnt(12) lgkmcnt(0)\n\t" \
    "s_barrier\n\t" \
    "s_add_u32 m0, s96, 0\n\t" \
    "s_nop 0\n\t" \
    "global_load_lds_dwordx4 v201, s[44:45] offset:0\n\t" \
    "s_add_u32 m0, s96, 896\n\t" \
    "s_nop 0\n\t" \
    "global_load_lds_dwordx4 v201, s[44:45] offset:128\n\t" \
    "s_add_u32 m0, s96, 1792\n\t" \
    "s_nop 0\n\t" \
    "global_load_lds_dwordx4 v201, s[44:45] offset:256\n\t" \
    "s_add_u32 m0, s96, 2688\n\t" \
    "s_nop 0\n\t" \
    "global_load_lds_dwordx4 v201, s[44:45] offset:384\n\t" \
    "s_add_u32 m0, s96, 3584\n\t" \
    "s_nop 0\n\t" \
    "global_load_lds_dwordx4 v201, s[44:45] offset:512\n\t" \
    "s_add_u32 m0, s96, 4480\n\t" \
    "s_nop 0\n\t" \
    "global_load_lds_dwordx4 v201, s[44:45] offset:640\n\t" \
    "s_add_u32 m0, s96, 5376\n\t" \
    "s_nop 0\n\t" \
    "global_load_lds_dwordx4 v201, s[44:45] offset:768\n\t" \
    "s_add_u32 m0, s96, 6272\n\t" \
    "s_nop 0\n\t" \
    "global_load_lds_dwordx4 v201, s[44:45] offset:896\n\t" \
    "s_add_u32 m0, s96, 7168\n\t" \
    "s_nop 0\n\t" \
    "global_load_lds_dwordx4 v201, s[44:45] offset:1024\n\t" \
    "v_add_u32 v201, 36864, v201\n\t" \
    "ds_read_b64_tr_b16 a[40:41], v202 offset:49152\n\t" \
    "ds_read_b64_tr_b16 a[42:43], v202 offset:50176\n\t" \
    "ds_read_b64_tr_b16 a[44:45], v202 offset:57344\n\t" \
    "ds_read_b64_tr_b16 a[46:47], v202 offset:58368\n\t" \
    "ds_read_b64_tr_b16 a[112:113], v203 offset:49152\n\t" \
    "ds_read_b64_tr_b16 a[114:115], v203 offset:50176\n\t" \
    "ds_read_b64_tr_b16 a[116:117], v203 offset:57344\n\t" \
    "ds_read_b64_tr_b16 a[118:119], v203 offset:58368\n\t" \
    "s_waitcnt vmcnt(17) lgkmcnt(0)\n\t" \
    "s_barrier\n\t" \
    "v_add_u32 v202, 65536, v202\n\t" \
    "v_add_u32 v203, 65536, v203\n\t" \
    "ds_read_b64_tr_b16 a[48:49], v202 offset:0\n\t" \
    "ds_read_b64_tr_b16 a[50:51], v202 offset:1024\n\t" \
    "ds_read_b64_tr_b16 a[52:53], v202 offset:8192\n\t" \
    "ds_read_b64_tr_b16 a[54:55], v202 offset:9216\n\t" \
    "ds_read_b64_tr_b16 a[120:121], v203 offset:0\n\t" \
    "ds_read_b64_tr_b16 a[122:123], v203 offset:1024\n\t" \
    "ds_read_b64_tr_b16 a[124:125], v203 offset:8192\n\t" \
    "ds_read_b64_tr_b16 a[126:127], v203 offset:9216\n\t" \
    "s_waitcnt vmcnt(13) lgkmcnt(0)\n\t" \
    "s_barrier\n\t" \
    "s_add_u32 m0, s96, 73728\n\t" \
    "s_nop 0\n\t" \
    "global_load_lds_dwordx4 v201, s[44:45] offset:0\n\t" \
    "s_add_u32 m0, s96, 74624\n\t" \
    "s_nop 0\n\t" \
    "global_load_lds_dwordx4 v201, s[44:45] offset:128\n\t" \
    "s_add_u32 m0, s96, 75520\n\t" \
    "s_nop 0\n\t" \
    "global_load_lds_dwordx4 v201, s[44:45] offset:256\n\t" \
    "s_add_u32 m0, s96, 76416\n\t" \
    "s_nop 0\n\t" \
    "global_load_lds_dwordx4 v201, s[44:45] offset:384\n\t" \
    "s_add_u32 m0, s96, 77312\n\t" \
    "s_nop 0\n\t" \
    "global_load_lds_dwordx4 v201, s[44:45] offset:512\n\t" \
    "s_add_u32 m0, s96, 78208\n\t" \
    "s_nop 0\n\t" \
    "global_load_lds_dwordx4 v201, s[44:45] offset:640\n\t" \
    "s_add_u32 m0, s96, 79104\n\t" \
    "s_nop 0\n\t" \
    "global_load_lds_dwordx4 v201, s[44:45] offset:768\n\t" \
    "s_add_u32 m0, s96, 80000\n\t" \
    "s_nop 0\n\t" \
    "global_load_lds_dwordx4 v201, s[44:45] offset:896\n\t" \
    "s_add_u32 m0, s96, 80896\n\t" \
    "s_nop 0\n\t" \
    "global_load_lds_dwordx4 v201, s[44:45] offset:1024\n\t" \
    "v_add_u32 v201, 36864, v201\n\t" \
    "v_add_u32 v202, -98304, v202\n\t" \
    "v_add_u32 v203, -98304, v203\n\t" \
    "ds_read_b64_tr_b16 a[56:57], v202 offset:0\n\t" \
    "ds_read_b64_tr_b16 a[58:59], v202 offset:1024\n\t" \
    "ds_read_b64_tr_b16 a[60:61], v202 offset:8192\n\t" \
    "ds_read_b64_tr_b16 a[62:63], v202 offset:9216\n\t" \
    "ds_read_b64_tr_b16 a[128:129], v203 offset:0\n\t" \
    "ds_read_b64_tr_b16 a[130:131], v203 offset:1024\n\t" \
    "ds_read_b64_tr_b16 a[132:133], v203 offset:8192\n\t" \
    "ds_read_b64_tr_b16 a[134:135], v203 offset:9216\n\t" \
    "s_waitcnt vmcnt(18) lgkmcnt(0)\n\t" \
    "s_barrier\n\t" \
    "s_add_u32 m0, s96, 110592\n\t" \
    "s_nop 0\n\t" \
    "global_load_lds_dwordx4 v201, s[44:45] offset:0\n\t" \
    "s_add_u32 m0, s96, 111488\n\t" \
    "s_nop 0\n\t" \
    "global_load_lds_dwordx4 v201, s[44:45] offset:128\n\t" \
    "s_add_u32 m0, s96, 112384\n\t" \
    "s_nop 0\n\t" \
    "global_load_lds_dwordx4 v201, s[44:45] offset:256\n\t" \
    "s_add_u32 m0, s96, 113280\n\t" \
    "s_nop 0\n\t" \
    "global_load_lds_dwordx4 v201, s[44:45] offset:384\n\t" \
    "s_add_u32 m0, s96, 114176\n\t" \
    "s_nop 0\n\t" \
    "global_load_lds_dwordx4 v201, s[44:45] offset:512\n\t" \
    "s_add_u32 m0, s96, 115072\n\t" \
    "s_nop 0\n\t" \
    "global_load_lds_dwordx4 v201, s[44:45] offset:640\n\t" \
    "s_add_u32 m0, s96, 115968\n\t" \
    "s_nop 0\n\t" \
    "global_load_lds_dwordx4 v201, s[44:45] offset:768\n\t" \
    "s_add_u32 m0, s96, 116864\n\t" \
    "s_nop 0\n\t" \
    "global_load_lds_dwordx4 v201, s[44:45] offset:896\n\t" \
    "s_add_u32 m0, s96, 117760\n\t" \
    "s_nop 0\n\t" \
    "global_load_lds_dwordx4 v201, s[44:45] offset:1024\n\t" \
    "v_add_u32 v201, 36864, v201\n\t" \
    "ds_read_b64_tr_b16 a[64:65], v202 offset:16384\n\t" \
    "ds_read_b64_tr_b16 a[66:67], v202 offset:17408\n\t" \
    "ds_read_b64_tr_b16 a[68:69], v202 offset:24576\n\t" \
    "ds_read_b64_tr_b16 a[70:71], v202 offset:25600\n\t" \
    "ds_read_b64_tr_b16 a[136:137], v203 offset:16384\n\t" \
    "ds_read_b64_tr_b16 a[138:139], v203 offset:17408\n\t" \
    "ds_read_b64_tr_b16 a[140:141], v203 offset:24576\n\t" \
    "ds_read_b64_tr_b16 a[142:143], v203 offset:25600\n\t" \
    "s_waitcnt vmcnt(18) lgkmcnt(0)\n\t" \
    "s_barrier\n\t" \
    "s_add_u32 m0, s96, 36864\n\t" \
    "s_nop 0\n\t" \
    "global_load_lds_dwordx4 v201, s[44:45] offset:0\n\t" \
    "s_add_u32 m0, s96, 37760\n\t" \
    "s_nop 0\n\t" \
    "global_load_lds_dwordx4 v201, s[44:45] offset:128\n\t" \
    "s_add_u32 m0, s96, 38656\n\t" \
    "s_nop 0\n\t" \
    "global_load_lds_dwordx4 v201, s[44:45] offset:256\n\t" \
    "s_add_u32 m0, s96, 39552\n\t" \
    "s_nop 0\n\t" \
    "global_load_lds_dwordx4 v201, s[44:45] offset:384\n\t" \
    "s_add_u32 m0, s96, 40448\n\t" \
    "s_nop 0\n\t" \
    "global_load_lds_dwordx4 v201, s[44:45] offset:512\n\t" \
    "s_add_u32 m0, s96, 41344\n\t" \
    "s_nop 0\n\t" \
    "global_load_lds_dwordx4 v201, s[44:45] offset:640\n\t" \
    "s_add_u32 m0, s96, 42240\n\t" \
    "s_nop 0\n\t" \
    "global_load_lds_dwordx4 v201, s[44:45] offset:768\n\t" \
    "s_add_u32 m0, s96, 43136\n\t" \
    "s_nop 0\n\t" \
    "global_load_lds_dwordx4 v201, s[44:45] offset:896\n\t" \
    "s_add_u32 m0, s96, 44032\n\t" \
    "s_nop 0\n\t" \
    "global_load_lds_dwordx4 v201, s[44:45] offset:1024\n\t" \
    "v_add_u32 v201, 36864, v201\n\t" \
    "ds_read_b64_tr_b16 a[144:145], v204 offset:0\n\t" \
    "ds_read_b64_tr_b16 a[146:147], v204 offset:18432\n\t" \
    "ds_read_b64_tr_b16 a[160:161], v205 offset:0\n\t" \
    "ds_read_b64_tr_b16 a[162:163], v205 offset:18432\n\t" \
    "ds_read_b64_tr_b16 a[176:177], v206 offset:0\n\t" \
    "ds_read_b64_tr_b16 a[178:179], v206 offset:18432\n\t" \
    "s_waitcnt lgkmcnt(6)\n\t" \
    "ds_read_b64_tr_b16 a[192:193], v207 offset:0\n\t" \
    "ds_read_b64_tr_b16 a[194:195], v207 offset:18432\n\t" \
    "ds_read_b64_tr_b16 a[208:209], v204 offset:1024\n\t" \
    "ds_read_b64_tr_b16 a[210:211], v204 offset:19456\n\t" \
    "ds_read_b64_tr_b16 a[224:225], v205 offset:1024\n\t" \
    "ds_read_b64_tr_b16 a[226:227], v205 offset:19456\n\t" \
    "s_waitcnt lgkmcnt(6)\n\t" \
    "ds_read_b64_tr_b16 a[240:241], v206 offset:1024\n\t" \
    "ds_read_b64_tr_b16 a[242:243], v206 offset:19456\n\t" \
    "ds_read_b64_tr_b16 v[224:225], v207 offset:1024\n\t" \
    "ds_read_b64_tr_b16 v[226:227], v207 offset:19456\n\t" \
    "ds_read_b64_tr_b16 v[240:241], v204 offset:2048\n\t" \
    "ds_read_b64_tr_b16 v[242:243], v204 offset:20480\n\t" \
    "s_waitcnt lgkmcnt(6)\n\t" \
    "s_waitcnt vmcnt(18) lgkmcnt(0)\n\t" \
    "s_barrier\n\t" \
    "v_mbcnt_lo_u32_b32 v140, -1, 0\n\t" \
    "v_mbcnt_hi_u32_b32 v140, -1, v140\n\t" \
    "v_min_u32 v140, 59, v140\n\t" \
    "v_lshlrev_b32 v140, 6, v140\n\t" \
    "v_add_u32 v140, 6258688, v140\n\t" \
    "v_mov_b32 v141, 6266880\n\t" \
    "global_load_dword v141, v141, s[36:37] sc1\n\t" \
    "global_load_dwordx2 v[142:143], v140, s[36:37] sc1\n\t" \
    "v_add_u32 v204, 73728, v204\n\t" \
    "v_add_u32 v205, 73728, v205\n\t" \
    "v_add_u32 v206, 73728, v206\n\t" \
    "v_add_u32 v207, 73728, v207\n\t" \
    "ds_read_b64_tr_b16 a[148:149], v204 offset:0\n\t" \
    "ds_read_b64_tr_b16 a[150:151], v204 offset:18432\n\t" \
    "ds_read_b64_tr_b16 a[164:165], v205 offset:0\n\t" \
    "ds_read_b64_tr_b16 a[166:167], v205 offset:18432\n\t" \
    "ds_read_b64_tr_b16 a[180:181], v206 offset:0\n\t" \
    "ds_read_b64_tr_b16 a[182:183], v206 offset:18432\n\t" \
    "s_waitcnt lgkmcnt(6)\n\t" \
    "ds_read_b64_tr_b16 a[196:197], v207 offset:0\n\t" \
    "ds_read_b64_tr_b16 a[198:199], v207 offset:18432\n\t" \
    "ds_read_b64_tr_b16 a[212:213], v204 offset:1024\n\t" \
    "ds_read_b64_tr_b16 a[214:215], v204 offset:19456\n\t" \
    "ds_read_b64_tr_b16 a[228:229], v205 offset:1024\n\t" \
    "ds_read_b64_tr_b16 a[230:231], v205 offset:19456\n\t" \
    "s_waitcnt lgkmcnt(6)\n\t" \
    "ds_read_b64_tr_b16 a[244:245], v206 offset:1024\n\t" \
    "ds_read_b64_tr_b16 a[246:247], v206 offset:19456\n\t" \
    "ds_read_b64_tr_b16 v[228:229], v207 offset:1024\n\t" \
    "ds_read_b64_tr_b16 v[230:231], v207 offset:19456\n\t" \
    "ds_read_b64_tr_b16 v[244:245], v204 offset:2048\n\t" \
    "ds_read_b64_tr_b16 v[246:247], v204 offset:20480\n\t" \
    "s_waitcnt lgkmcnt(6)\n\t" \
    "s_waitcnt vmcnt(11) lgkmcnt(0)\n\t" \
    "s_barrier\n\t" \
    "ds_read_b64_tr_b16 a[152:153], v204 offset:36864\n\t" \
    "ds_read_b64_tr_b16 a[154:155], v204 offset:55296\n\t" \
    "ds_read_b64_tr_b16 a[168:169], v205 offset:36864\n\t" \
    "ds_read_b64_tr_b16 a[170:171], v205 offset:55296\n\t" \
    "ds_read_b64_tr_b16 a[184:185], v206 offset:36864\n\t" \
    "ds_read_b64_tr_b16 a[186:187], v206 offset:55296\n\t" \
    "s_waitcnt lgkmcnt(6)\n\t" \
    "ds_read_b64_tr_b16 a[200:201], v207 offset:36864\n\t" \
    "ds_read_b64_tr_b16 a[202:203], v207 offset:55296\n\t" \
    "ds_read_b64_tr_b16 a[216:217], v204 offset:37888\n\t" \
    "ds_read_b64_tr_b16 a[218:219], v204 offset:56320\n\t" \
    "ds_read_b64_tr_b16 a[232:233], v205 offset:37888\n\t" \
    "ds_read_b64_tr_b16 a[234:235], v205 offset:56320\n\t" \
    "s_waitcnt lgkmcnt(6)\n\t" \
    "ds_read_b64_tr_b16 a[248:249], v206 offset:37888\n\t" \
    "ds_read_b64_tr_b16 a[250:251], v206 offset:56320\n\t" \
    "ds_read_b64_tr_b16 v[232:233], v207 offset:37888\n\t" \
    "ds_read_b64_tr_b16 v[234:235], v207 offset:56320\n\t" \
    "ds_read_b64_tr_b16 v[248:249], v204 offset:38912\n\t" \
    "ds_read_b64_tr_b16 v[250:251], v204 offset:57344\n\t" \
    "s_waitcnt lgkmcnt(6)\n\t" \
    "s_waitcnt vmcnt(2) lgkmcnt(0)\n\t" \
    "s_barrier\n\t" \
    "v_add_u32 v204, -36864, v204\n\t" \
    "v_add_u32 v205, -36864, v205\n\t" \
    "v_add_u32 v206, -36864, v206\n\t" \
    "v_add_u32 v207, -36864, v207\n\t" \
    "ds_read_b64_tr_b16 a[156:157], v204 offset:0\n\t" \
    "ds_read_b64_tr_b16 a[158:159], v204 offset:18432\n\t" \
    "ds_read_b64_tr_b16 a[172:173], v205 offset:0\n\t" \
    "ds_read_b64_tr_b16 a[174:175], v205 offset:18432\n\t" \
    "ds_read_b64_tr_b16 a[188:189], v206 offset:0\n\t" \
    "ds_read_b64_tr_b16 a[190:191], v206 offset:18432\n\t" \
    "s_waitcnt lgkmcnt(6)\n\t" \
    "ds_read_b64_tr_b16 a[204:205], v207 offset:0\n\t" \
    "ds_read_b64_tr_b16 a[206:207], v207 offset:18432\n\t" \
    "ds_read_b64_tr_b16 a[220:221], v204 offset:1024\n\t" \
    "ds_read_b64_tr_b16 a[222:223], v204 offset:19456\n\t" \
    "ds_read_b64_tr_b16 a[236:237], v205 offset:1024\n\t" \
    "ds_read_b64_tr_b16 a[238:239], v205 offset:19456\n\t" \
    "s_waitcnt lgkmcnt(6)\n\t" \
    "ds_read_b64_tr_b16 a[252:253], v206 offset:1024\n\t" \
    "ds_read_b64_tr_b16 a[254:255], v206 offset:19456\n\t" \
    "ds_read_b64_tr_b16 v[236:237], v207 offset:1024\n\t" \
    "ds_read_b64_tr_b16 v[238:239], v207 offset:19456\n\t" \
    "ds_read_b64_tr_b16 v[252:253], v204 offset:2048\n\t" \
    "ds_read_b64_tr_b16 v[254:255], v204 offset:20480\n\t" \
    "s_waitcnt lgkmcnt(6)\n\t" \
    "s_waitcnt vmcnt(0) lgkmcnt(0)\n\t" \
    "s_barrier\n\t" \
    "v_sub_u32 v142, v142, v141\n\t" \
    "v_cmp_ne_u32 vcc, 1, v142\n\t" \
    "s_cbranch_vccz vv_Cok4_%=\n\t" \
    "vv_C3_%=:\n\t" \
    "global_load_dwordx2 v[142:143], v140, s[36:37] sc1\n\t" \
    "s_waitcnt vmcnt(0)\n\t" \
    "v_sub_u32 v142, v142, v141\n\t" \
    "v_cmp_ne_u32 vcc, 1, v142\n\t" \
    "s_cbranch_vccz vv_Cok4_%=\n\t" \
    "s_cmp_eq_u32 s68, 0\n\t" \
    "s_cbranch_scc1 vv_F5_%=\n\t" \
    "s_sub_u32 s68, s68, 1\n\t" \
    "s_sleep 4\n\t" \
    "s_branch vv_C3_%=\n\t" \
    "vv_F5_%=:\n\t" \
    "v_mov_b32 v144, 6095104\n\t" \
    "v_mov_b32 v145, 4\n\t" \
    "global_store_dword v144, v145, s[36:37] sc1\n\t" \
    "s_waitcnt vmcnt(0)\n\t" \
    "s_branch vv_Cok4_%=\n\t" \
    "vv_Cok4_%=:\n\t" \
    "s_cmp_lg_u32 s66, 0\n\t" \
    "s_cbranch_scc1 vv_Ca6_%=\n\t" \
    "v_mov_b32 v140, 6176768\n\t" \
    "s_mov_b64 exec, 1\n\t" \
    "global_atomic_add v140, v142, s[36:37] sc1\n\t" \
    "s_mov_b64 exec, -1\n\t" \
    "vv_Ca6_%=:\n\t" \
    "v_sub_u32_dpp v142, v143, v143 row_shl:6 row_mask:0xf bank_mask:0xf\n\t" \
    "v_min_u32_dpp v143, v143, v143 row_shl:6 row_mask:0xf bank_mask:0xf\n\t" \
    "v_min_i32 v142, 64, v142\n\t" \
    "v_max_i32 v142, -64, v142\n\t" \
    "s_mov_b32 s97, 0\n\t" \
    "v_readlane_b32 s69, v143, 0\n\t" \
    "v_readlane_b32 s70, v143, 1\n\t" \
    "v_readlane_b32 s54, v142, 0\n\t" \
    "v_readlane_b32 s55, v142, 1\n\t" \
    "s_nop 1\n\t" \
    "s_cmp_lt_u32 s70, s69\n\t" \
    "s_cselect_b32 s62, 8192, 0\n\t" \
    "s_cselect_b32 s54, s55, s54\n\t" \
    "s_cselect_b32 s69, s70, s69\n\t" \
    "s_mov_b32 s71, s69\n\t" \
    "s_add_u32 s97, s97, s54\n\t" \
    "v_readlane_b32 s69, v143, 2\n\t" \
    "v_readlane_b32 s70, v143, 3\n\t" \
    "v_readlane_b32 s54, v142, 2\n\t" \
    "v_readlane_b32 s55, v142, 3\n\t" \
    "s_nop 1\n\t" \
    "s_sub_u32 s55, 0, s55\n\t" \
    "s_cmp_lt_u32 s70, s69\n\t" \
    "s_cselect_b32 s63, 8192, 0\n\t" \
    "s_cselect_b32 s54, s55, s54\n\t" \
    "s_cselect_b32 s69, s70, s69\n\t" \
    "s_min_u32 s71, s69, s71\n\t" \
    "s_add_u32 s97, s97, s54\n\t" \
    "v_readlane_b32 s69, v143, 4\n\t" \
    "v_readlane_b32 s70, v143, 5\n\t" \
    "v_readlane_b32 s54, v142, 4\n\t" \
    "v_readlane_b32 s55, v142, 5\n\t" \
    "s_nop 1\n\t" \
    "s_sub_u32 s54, 0, s54\n\t" \
    "s_sub_u32 s55, 0, s55\n\t" \
    "s_cmp_lt_u32 s70, s69\n\t" \
    "s_cselect_b32 s64, 8192, 0\n\t" \
    "s_cselect_b32 s54, s55, s54\n\t" \
    "s_cselect_b32 s69, s70, s69\n\t" \
    "s_min_u32 s71, s69, s71\n\t" \
    "s_add_u32 s97, s97, s54\n\t" \
    "s_add_u32 s63, s63, 0x200000\n\t" \
    "s_xor_b32 s52, s64, 8192\n\t" \
    "s_cmp_gt_u32 s71, 625\n\t" \
    "s_cselect_b32 s69, 1, 0\n\t" \
    "s_mul_i32 s70, s69, 160\n\t" \
    "s_add_u32 s86, s86, s70\n\t" \
    "s_mul_i32 s70, s69, 536\n\t" \
    "s_add_u32 s87, s87, s70\n\t" \
    "s_mul_i32 s70, s69, 256\n\t" \
    "s_add_u32 s88, s88, s70\n\t" \
    "s_cmp_gt_i32 s97, 0\n\t" \
    "s_cselect_b32 s70, 1, 0\n\t" \
    "s_lshr_b32 s69, s67, 3\n\t" \
    "s_xor_b32 s69, s69, s70\n\t" \
    "s_and_b32 s97, s69, 1\n\t" \
    "v_mov_b32 v148, -1\n\t" \
    "v_mov_b32 v149, -1\n\t" \
    "v_mov_b32 v150, -1\n\t" \
    "v_mov_b32 v151, -1\n\t" \
    "s_mov_b32 s71, 3\n\t" \
    "vv_E7_%=:\n\t" \
    "s_memtime s[54:55]\n\t" \
    "s_nop 0\n\t" \
    "s_memrealtime s[4:5]\n\t" \
    "s_nop 0\n\t" \
    "s_memrealtime s[6:7]\n\t" \
    "s_nop 0\n\t" \
    "s_memrealtime s[8:9]\n\t" \
    "s_nop 0\n\t" \
    "s_memrealtime s[10:11]\n\t" \
    "s_nop 0\n\t" \
    "s_memrealtime s[12:13]\n\t" \
    "s_nop 0\n\t" \
    "s_memrealtime s[14:15]\n\t" \
    "s_nop 0\n\t" \
    "s_memrealtime s[16:17]\n\t" \
    "s_nop 0\n\t" \
    "s_memrealtime s[18:19]\n\t" \
    "s_nop 0\n\t" \
    "s_memrealtime s[20:21]\n\t" \
    "s_nop 0\n\t" \
    "s_memrealtime s[22:23]\n\t" \
    "s_nop 0\n\t" \
    "s_memrealtime s[24:25]\n\t" \
    "s_nop 0\n\t" \
    "s_memrealtime s[26:27]\n\t" \
    "s_nop 0\n\t" \
    "s_memrealtime s[28:29]\n\t" \
    "s_nop 0\n\t" \
    "s_memtime s[56:57]\n\t" \
    "s_mov_b32 s70, 1\n\t" \
    "s_sub_u32 s71, s71, 1\n\t" \
    "s_cselect_b32 s69, 0, -1\n\t" \
    "s_waitcnt lgkmcnt(0)\n\t" \
    "s_sub_u32 s54, s56, s54\n\t" \
    "s_and_b32 s54, s54, s69\n\t" \
    "s_cmp_gt_u32 s54, 124\n\t" \
    "s_cbranch_scc1 vv_E7_%=\n\t" \
    "s_cmp_eq_u32 s4, s28\n\t" \
    "s_addc_u32 s70, s70, 0\n\t" \
    "s_cmp_eq_u32 s6, s28\n\t" \
    "s_addc_u32 s70, s70, 0\n\t" \
    "s_cmp_eq_u32 s8, s28\n\t" \
    "s_addc_u32 s70, s70, 0\n\t" \
    "s_cmp_eq_u32 s10, s28\n\t" \
    "s_addc_u32 s70, s70, 0\n\t" \
    "s_cmp_eq_u32 s12, s28\n\t" \
    "s_addc_u32 s70, s70, 0\n\t" \
    "s_cmp_eq_u32 s14, s28\n\t" \
    "s_addc_u32 s70, s70, 0\n\t" \
    "s_cmp_eq_u32 s16, s28\n\t" \
    "s_addc_u32 s70, s70, 0\n\t" \
    "s_cmp_eq_u32 s18, s28\n\t" \
    "s_addc_u32 s70, s70, 0\n\t" \
    "s_cmp_eq_u32 s20, s28\n\t" \
    "s_addc_u32 s70, s70, 0\n\t" \
    "s_cmp_eq_u32 s22, s28\n\t" \
    "s_addc_u32 s70, s70, 0\n\t" \
    "s_cmp_eq_u32 s24, s28\n\t" \
    "s_addc_u32 s70, s70, 0\n\t" \
    "s_cmp_eq_u32 s26, s28\n\t" \
    "s_addc_u32 s70, s70, 0\n\t" \
    "s_mov_b32 s75, s73\n\t" \
    "s_mov_b32 s76, s74\n\t" \
    "s_lshl_b32 s70, s70, 3\n\t" \
    "s_sub_u32 s73, s56, s70\n\t" \
    "s_lshl_b32 s74, s28, 4\n\t" \
    "s_sub_u32 s69, s73, s75\n\t" \
    "s_sub_u32 s70, s74, s76\n\t" \
    "s_sub_u32 s71, s70, 1600\n\t" \
    "s_cmp_lt_u32 s71, 400000\n\t" \
    "s_cbranch_scc0 vv_K9_%=\n\t" \
    "v_cvt_f32_u32 v140, s69\n\t" \
    "v_cvt_f32_u32 v141, s70\n\t" \
    "v_rcp_f32 v142, v140\n\t" \
    "v_rcp_f32 v143, v141\n\t" \
    "v_mul_f32 v142, v142, v141\n\t" \
    "v_mul_f32 v143, v143, v140\n\t" \
    "v_mul_f32 v142, 0x4f800000, v142\n\t" \
    "v_mul_f32 v143, 0x4f000000, v143\n\t" \
    "v_cvt_u32_f32 v142, v142\n\t" \
    "v_cvt_u32_f32 v143, v143\n\t" \
    "v_readfirstlane_b32 s69, v142\n\t" \
    "v_readfirstlane_b32 s70, v143\n\t" \
    "s_nop 1\n\t" \
    "s_mov_b32 s78, s69\n\t" \
    "s_mov_b32 s77, s70\n\t" \
    "vv_K9_%=:\n\t" \
    "s_mov_b32 s79, s74\n\t" \
    "s_sub_u32 s80, s74, 32000\n\t" \
    "s_cmp_eq_u32 s66, 1\n\t" \
    "s_cbranch_scc1 vv_loop11_%=\n\t" \
    "s_cmp_eq_u32 s66, 2\n\t" \
    "s_cbranch_scc1 vv_loop12_%=\n\t" \
    "s_cmp_eq_u32 s66, 3\n\t" \
    "s_cbranch_scc1 vv_loop13_%=\n\t" \
    "vv_loop10_%=:\n\t" \
    "v_mov_b32 v72, 0\n\t" \
    "v_mov_b32 v73, 0\n\t" \
    "v_mov_b32 v74, 0\n\t" \
    "v_mov_b32 v75, 0\n\t" \
    "v_mov_b32 v76, 0\n\t" \
    "v_mov_b32 v77, 0\n\t" \
    "v_mov_b32 v78, 0\n\t" \
    "v_mov_b32 v79, 0\n\t" \
    "v_mov_b32 v92, 0\n\t" \
    "v_mov_b32 v93, 0\n\t" \
    "v_mov_b32 v94, 0\n\t" \
    "v_mov_b32 v95, 0\n\t" \
    "v_mov_b32 v96, 0\n\t" \
    "v_mov_b32 v97, 0\n\t" \
    "v_mov_b32 v98, 0\n\t" \
    "v_mov_b32 v99, 0\n\t" \
    "v_mov_b32 v100, 0\n\t" \
    "v_mov_b32 v101, 0\n\t" \
    "v_mov_b32 v102, 0\n\t" \
    "v_mov_b32 v103, 0\n\t" \
    "v_mov_b32 v104, 0\n\t" \
    "v_mov_b32 v105, 0\n\t" \
    "v_mov_b32 v106, 0\n\t" \
    "v_mov_b32 v107, 0\n\t" \
    "v_mov_b32 v108, 0\n\t" \
    "v_mov_b32 v109, 0\n\t" \
    "v_mov_b32 v110, 0\n\t" \
    "v_mov_b32 v111, 0\n\t" \
    "v_mov_b32 v112, 0\n\t" \
    "v_mov_b32 v113, 0\n\t" \
    "v_mov_b32 v114, 0\n\t" \
    "v_mov_b32 v115, 0\n\t" \
    "v_mov_b32 v116, 0\n\t" \
    "v_mov_b32 v117, 0\n\t" \
    "v_mov_b32 v118, 0\n\t" \
    "v_mov_b32 v119, 0\n\t" \
    "v_mov_b32 v120, 0\n\t" \
    "v_mov_b32 v121, 0\n\t" \
    "v_mov_b32 v122, 0\n\t" \
    "v_mov_b32 v123, 0\n\t" \
    "v_mov_b32 v124, 0\n\t" \
    "v_mov_b32 v125, 0\n\t" \
    "v_mov_b32 v126, 0\n\t" \
    "v_mov_b32 v127, 0\n\t" \
    "vv_go15_%=:\n\t" \
    "s_add_u32 s69, s65, 0\n\t" \
    "s_bitcmp1_b32 s69, 0\n\t" \
    "s_cselect_b32 s70, s63, s62\n\t" \
    "s_bfe_u32 s69, s69, 0x20001\n\t" \
    "s_lshl_b32 s69, s69, 19\n\t" \
    "s_add_u32 s69, s69, s70\n\t" \
    "s_add_u32 s46, s36, s69\n\t" \
    "s_addc_u32 s47, s37, 0\n\t" \
    "s_add_u32 s69, s65, 0\n\t" \
    "s_lshr_b32 s69, s69, 1\n\t" \
    "s_xor_b32 s69, s69, s97\n\t" \
    "s_and_b32 s69, s69, 1\n\t" \
    "s_lshl_b32 s69, s69, 14\n\t" \
    "s_or_b32 s46, s46, s69\n\t" \
    "s_add_u32 s69, s65, 0\n\t" \
    "s_mul_i32 s70, s69, 171\n\t" \
    "s_lshr_b32 s70, s70, 9\n\t" \
    "s_mul_i32 s70, s70, 3\n\t" \
    "s_sub_u32 s69, s69, s70\n\t" \
    "s_mul_i32 s69, s69, 36\n\t" \
    "s_lshl_b32 s69, s69, 14\n\t" \
    "s_add_u32 s69, s69, s64\n\t" \
    "s_add_u32 s69, s69, 4194304\n\t" \
    "s_add_u32 s38, s36, s69\n\t" \
    "s_addc_u32 s39, s37, 0\n\t" \
    "s_mul_i32 s69, s65, 9216\n\t" \
    "s_add_u32 s69, s69, 123731968\n\t" \
    "s_add_u32 s40, s36, s69\n\t" \
    "s_addc_u32 s41, s37, 0\n\t" \
    "s_sub_u32 s70, s79, s74\n\t" \
    "s_max_i32 s70, s70, 0\n\t" \
    "s_min_i32 s70, s70, 16000\n\t" \
    "s_lshl_b32 s70, s70, 1\n\t" \
    "s_mul_hi_u32 s70, s70, s77\n\t" \
    "s_add_u32 s70, s70, s73\n\t" \
    "s_sub_u32 s70, s70, 100\n\t" \
    "s_getpc_b64 s[30:31]\n\t" \
    "vv_Wp17_%=:\n\t" \
    "s_add_u32 s30, s30, vv_We18_%=-vv_Wp17_%=\n\t" \
    "s_addc_u32 s31, s31, 0\n\t" \
    "vv_W16_%=:\n\t" \
    "s_memtime s[56:57]\n\t" \
    "s_waitcnt lgkmcnt(0)\n\t" \
    "s_sub_u32 s69, s70, s56\n\t" \
    "s_cmp_gt_i32 s69, 111\n\t" \
    "s_cbranch_scc1 vv_W16_%=\n\t" \
    "s_max_i32 s69, s69, 0\n\t" \
    "s_lshr_b32 s69, s69, 1\n\t" \
    "s_and_b32 s69, s69, 0x3c\n\t" \
    "s_sub_u32 s30, s30, s69\n\t" \
    "s_subb_u32 s31, s31, 0\n\t" \
    "s_setpc_b64 s[30:31]\n\t" \
    ".p2align 6\n\t" \
    "vv_al19_%=:\n\t" \
    "s_nop 0\n\t" \
    "s_nop 1\n\t" \
    "s_nop 1\n\t" \
    "s_nop 1\n\t" \
    "s_nop 1\n\t" \
    "s_nop 1\n\t" \
    "s_nop 1\n\t" \
    "s_nop 1\n\t" \
    "s_nop 1\n\t" \
    "s_nop 1\n\t" \
    "s_nop 1\n\t" \
    "s_nop 1\n\t" \
    "s_nop 1\n\t" \
    "s_nop 1\n\t" \
    "vv_We18_%=:\n\t" \
    "vv_G20_%=:\n\t" \
    "global_load_dwordx4 v[36:39], v1, s[46:47] sc1\n\t" \
    "s_nop 9\n\t" \
    "global_load_dwordx4 v[40:43], v1, s[46:47] offset:1024 sc1\n\t" \
    "s_nop 9\n\t" \
    "global_load_dwordx4 v[44:47], v2, s[46:47] sc1\n\t" \
    "s_waitcnt vmcnt(2)\n\t" \
    "v_smfmac_f32_16x16x64_bf16 v[72:75], v[36:39], a[0:7], v10\n\t" \
    "ds_write_b128 v0, v[36:39] offset:0\n\t" \
    "v_smfmac_f32_16x16x64_bf16 v[76:79], v[36:39], a[72:79], v10\n\t" \
    "v_max3_u32 v140, v36, v37, v38\n\t" \
    "s_waitcnt vmcnt(1)\n\t" \
    "ds_write_b128 v0, v[40:43] offset:1024\n\t" \
    "v_smfmac_f32_16x16x64_bf16 v[72:75], v[40:43], a[8:15], v10\n\t" \
    "v_max3_u32 v140, v140, v39, v40\n\t" \
    "v_max3_u32 v140, v140, v41, v42\n\t" \
    "v_max_u32 v140, v140, v43\n\t" \
    "v_cmp_eq_u32 vcc, -1, v140\n\t" \
    "v_smfmac_f32_16x16x64_bf16 v[76:79], v[40:43], a[80:87], v10\n\t" \
    "s_cbranch_vccnz vv_Gbad21_%=\n\t" \
    "vv_Gc23_%=:\n\t" \
    "s_waitcnt lgkmcnt(0)\n\t" \
    "s_barrier\n\t" \
    "ds_read_b128 v[48:51], v0 offset:2048\n\t" \
    "ds_read_b128 v[52:55], v0 offset:3072\n\t" \
    "s_waitcnt vmcnt(0)\n\t" \
    "v_smfmac_f32_16x16x64_bf16 v[72:75], v[44:47], a[64:71], v10\n\t" \
    "ds_read_b128 v[56:59], v0 offset:4096\n\t" \
    "ds_read_b128 v[60:63], v0 offset:5120\n\t" \
    "v_max3_u32 v140, v44, v45, v46\n\t" \
    "v_max_u32 v140, v140, v47\n\t" \
    "v_smfmac_f32_16x16x64_bf16 v[76:79], v[44:47], a[136:143], v10\n\t" \
    "ds_read_b128 v[64:67], v0 offset:6144\n\t" \
    "ds_read_b128 v[68:71], v0 offset:7168\n\t" \
    "v_cmp_eq_u32 vcc, -1, v140\n\t" \
    "s_cbranch_vccnz vv_Gbad222_%=\n\t" \
    "vv_Gc224_%=:\n\t" \
    "s_waitcnt lgkmcnt(5)\n\t" \
    "v_smfmac_f32_16x16x64_bf16 v[72:75], v[48:51], a[16:23], v10\n\t" \
    "s_waitcnt lgkmcnt(4)\n\t" \
    "v_smfmac_f32_16x16x64_bf16 v[72:75], v[52:55], a[24:31], v10\n\t" \
    "s_waitcnt lgkmcnt(3)\n\t" \
    "v_smfmac_f32_16x16x64_bf16 v[72:75], v[56:59], a[32:39], v10\n\t" \
    "s_waitcnt lgkmcnt(2)\n\t" \
    "v_smfmac_f32_16x16x64_bf16 v[72:75], v[60:63], a[40:47], v10\n\t" \
    "s_waitcnt lgkmcnt(1)\n\t" \
    "v_smfmac_f32_16x16x64_bf16 v[72:75], v[64:67], a[48:55], v10\n\t" \
    "s_waitcnt lgkmcnt(0)\n\t" \
    "v_smfmac_f32_16x16x64_bf16 v[72:75], v[68:71], a[56:63], v10\n\t" \
    "v_smfmac_f32_16x16x64_bf16 v[76:79], v[48:51], a[88:95], v10\n\t" \
    "s_nop 1\n\t" \
    "s_nop 0\n\t" \
    "v_smfmac_f32_16x16x64_bf16 v[76:79], v[52:55], a[96:103], v10\n\t" \
    "s_nop 1\n\t" \
    "s_nop 0\n\t" \
    "v_smfmac_f32_16x16x64_bf16 v[76:79], v[56:59], a[104:111], v10\n\t" \
    "v_add_f32 v72, v72, v74\n\t" \
    "v_smfmac_f32_16x16x64_bf16 v[76:79], v[60:63], a[112:119], v10\n\t" \
    "v_add_f32 v73, v73, v75\n\t" \
    "v_smfmac_f32_16x16x64_bf16 v[76:79], v[64:67], a[120:127], v10\n\t" \
    "v_cvt_pk_bf16_f32 v80, v72, v73\n\t" \
    "v_smfmac_f32_16x16x64_bf16 v[76:79], v[68:71], a[128:135], v10\n\t" \
    "ds_write_b32 v12, v80 offset:0\n\t" \
    "s_nop 6\n\t" \
    "v_add_f32 v76, v76, v78\n\t" \
    "v_add_f32 v77, v77, v79\n\t" \
    "v_cvt_pk_bf16_f32 v81, v76, v77\n\t" \
    "ds_write_b32 v12, v81 offset:64\n\t" \
    "s_waitcnt lgkmcnt(0)\n\t" \
    "s_barrier\n\t" \
    "ds_read_b64_tr_b16 v[84:85], v13 offset:0\n\t" \
    "ds_read_b64_tr_b16 v[86:87], v13 offset:128\n\t" \
    "ds_read_b64_tr_b16 v[88:89], v13 offset:1024\n\t" \
    "ds_read_b64_tr_b16 v[90:91], v13 offset:1152\n\t" \
    "s_waitcnt lgkmcnt(2)\n\t" \
    "v_smfmac_f32_16x16x64_bf16 v[92:95], v[84:87], a[144:151], v10\n\t" \
    "s_nop 2\n\t" \
    "s_waitcnt lgkmcnt(0)\n\t" \
    "v_smfmac_f32_16x16x64_bf16 v[92:95], v[88:91], a[152:159], v10\n\t" \
    "s_nop 2\n\t" \
    "v_smfmac_f32_16x16x64_bf16 v[96:99], v[84:87], a[160:167], v10\n\t" \
    "s_nop 2\n\t" \
    "v_smfmac_f32_16x16x64_bf16 v[96:99], v[88:91], a[168:175], v10\n\t" \
    "s_nop 2\n\t" \
    "v_smfmac_f32_16x16x64_bf16 v[100:103], v[84:87], a[176:183], v10\n\t" \
    "s_nop 0\n\t" \
    "v_add_f32 v92, v92, v94\n\t" \
    "v_add_f32 v93, v93, v95\n\t" \
    "v_smfmac_f32_16x16x64_bf16 v[100:103], v[88:91], a[184:191], v10\n\t" \
    "s_nop 1\n\t" \
    "v_cvt_pk_bf16_f32 v128, v92, v93\n\t" \
    "v_smfmac_f32_16x16x64_bf16 v[104:107], v[84:87], a[192:199], v10\n\t" \
    "s_nop 0\n\t" \
    "v_add_f32 v96, v96, v98\n\t" \
    "v_add_f32 v97, v97, v99\n\t" \
    "v_smfmac_f32_16x16x64_bf16 v[104:107], v[88:91], a[200:207], v10\n\t" \
    "s_nop 1\n\t" \
    "v_cvt_pk_bf16_f32 v129, v96, v97\n\t" \
    "v_smfmac_f32_16x16x64_bf16 v[124:127], v[84:87], v[240:247], v10\n\t" \
    "s_nop 0\n\t" \
    "v_add_f32 v100, v100, v102\n\t" \
    "v_add_f32 v101, v101, v103\n\t" \
    "v_smfmac_f32_16x16x64_bf16 v[124:127], v[88:91], v[248:255], v10\n\t" \
    "s_nop 1\n\t" \
    "global_load_dword v140, v20, s[46:47] sc1\n\t" \
    "v_cvt_pk_bf16_f32 v130, v100, v101\n\t" \
    "v_smfmac_f32_16x16x64_bf16 v[108:111], v[84:87], a[208:215], v10\n\t" \
    "s_nop 0\n\t" \
    "v_add_f32 v104, v104, v106\n\t" \
    "v_add_f32 v105, v105, v107\n\t" \
    "v_smfmac_f32_16x16x64_bf16 v[108:111], v[88:91], a[216:223], v10\n\t" \
    "s_nop 0\n\t" \
    "v_cvt_pk_bf16_f32 v131, v104, v105\n\t" \
    "v_permlane32_swap_b32 v128, v130\n\t" \
    "v_smfmac_f32_16x16x64_bf16 v[112:115], v[84:87], a[224:231], v10\n\t" \
    "s_nop 0\n\t" \
    "v_permlane32_swap_b32 v129, v131\n\t" \
    "s_nop 1\n\t" \
    "v_permlane16_swap_b32 v128, v129\n\t" \
    "v_smfmac_f32_16x16x64_bf16 v[112:115], v[88:91], a[232:239], v10\n\t" \
    "v_permlane16_swap_b32 v130, v131\n\t" \
    "global_store_dwordx4 v14, v[128:131], s[38:39] sc1\n\t" \
    "v_add_f32 v124, v124, v126\n\t" \
    "v_smfmac_f32_16x16x64_bf16 v[116:119], v[84:87], a[240:247], v10\n\t" \
    "s_nop 0\n\t" \
    "v_add_f32 v125, v125, v127\n\t" \
    "v_cvt_pk_bf16_f32 v136, v124, v125\n\t" \
    "v_smfmac_f32_16x16x64_bf16 v[116:119], v[88:91], a[248:255], v10\n\t" \
    "global_store_dword v16, v136, s[38:39] sc1\n\t" \
    "v_add_f32 v108, v108, v110\n\t" \
    "v_add_f32 v109, v109, v111\n\t" \
    "v_smfmac_f32_16x16x64_bf16 v[120:123], v[84:87], v[224:231], v10\n\t" \
    "s_nop 0\n\t" \
    "v_cvt_pk_bf16_f32 v132, v108, v109\n\t" \
    "v_add_f32 v112, v112, v114\n\t" \
    "v_smfmac_f32_16x16x64_bf16 v[120:123], v[88:91], v[232:239], v10\n\t" \
    "v_add_f32 v113, v113, v115\n\t" \
    "v_cvt_pk_bf16_f32 v133, v112, v113\n\t" \
    "v_add_f32 v116, v116, v118\n\t" \
    "v_add_f32 v117, v117, v119\n\t" \
    "v_cvt_pk_bf16_f32 v134, v116, v117\n\t" \
    "s_nop 2\n\t" \
    "v_pk_add_f32 v[120:121], v[120:121], v[122:123]\n\t" \
    "v_cvt_pk_bf16_f32 v135, v120, v121\n\t" \
    "v_permlane32_swap_b32 v132, v134\n\t" \
    "s_nop 0\n\t" \
    "v_permlane32_swap_b32 v133, v135\n\t" \
    "s_nop 1\n\t" \
    "v_permlane16_swap_b32 v132, v133\n\t" \
    "v_permlane16_swap_b32 v134, v135\n\t" \
    "global_store_dwordx4 v15, v[132:135], s[38:39] sc1\n\t" \
    "s_nop 1\n\t" \
    "global_store_dwordx4 v17, v[128:131], s[40:41] sc1\n\t" \
    "global_store_dwordx4 v17, v[132:135], s[40:41] offset:1024 sc1\n\t" \
    "global_store_dword v18, v136, s[40:41] sc1\n\t" \
    "s_cmp_eq_u32 s65, 63\n\t" \
    "s_cbranch_scc1 vv_end14_%=\n\t" \
    "s_mov_b32 s71, 3\n\t" \
    "vv_E26_%=:\n\t" \
    "s_memtime s[54:55]\n\t" \
    "s_nop 0\n\t" \
    "s_memrealtime s[4:5]\n\t" \
    "s_nop 0\n\t" \
    "s_memrealtime s[6:7]\n\t" \
    "s_nop 0\n\t" \
    "s_memrealtime s[8:9]\n\t" \
    "s_nop 0\n\t" \
    "s_memrealtime s[10:11]\n\t" \
    "s_nop 0\n\t" \
    "s_memrealtime s[12:13]\n\t" \
    "s_nop 0\n\t" \
    "s_memrealtime s[14:15]\n\t" \
    "s_nop 0\n\t" \
    "s_memrealtime s[16:17]\n\t" \
    "s_nop 0\n\t" \
    "s_memrealtime s[18:19]\n\t" \
    "s_nop 0\n\t" \
    "s_memrealtime s[20:21]\n\t" \
    "s_nop 0\n\t" \
    "s_memrealtime s[22:23]\n\t" \
    "s_nop 0\n\t" \
    "s_memrealtime s[24:25]\n\t" \
    "s_nop 0\n\t" \
    "s_memrealtime s[26:27]\n\t" \
    "s_nop 0\n\t" \
    "s_memrealtime s[28:29]\n\t" \
    "s_nop 0\n\t" \
    "s_memtime s[56:57]\n\t" \
    "s_mov_b32 s70, 1\n\t" \
    "s_sub_u32 s71, s71, 1\n\t" \
    "s_cselect_b32 s69, 0, -1\n\t" \
    "s_waitcnt lgkmcnt(0)\n\t" \
    "s_sub_u32 s54, s56, s54\n\t" \
    "s_and_b32 s54, s54, s69\n\t" \
    "s_cmp_gt_u32 s54, 124\n\t" \
    "s_cbranch_scc1 vv_E26_%=\n\t" \
    "s_cmp_eq_u32 s4, s28\n\t" \
    "s_addc_u32 s70, s70, 0\n\t" \
    "s_cmp_eq_u32 s6, s28\n\t" \
    "s_addc_u32 s70, s70, 0\n\t" \
    "s_cmp_eq_u32 s8, s28\n\t" \
    "s_addc_u32 s70, s70, 0\n\t" \
    "s_cmp_eq_u32 s10, s28\n\t" \
    "s_addc_u32 s70, s70, 0\n\t" \
    "s_cmp_eq_u32 s12, s28\n\t" \
    "s_addc_u32 s70, s70, 0\n\t" \
    "s_cmp_eq_u32 s14, s28\n\t" \
    "s_addc_u32 s70, s70, 0\n\t" \
    "s_cmp_eq_u32 s16, s28\n\t" \
    "s_addc_u32 s70, s70, 0\n\t" \
    "s_cmp_eq_u32 s18, s28\n\t" \
    "s_addc_u32 s70, s70, 0\n\t" \
    "s_cmp_eq_u32 s20, s28\n\t" \
    "s_addc_u32 s70, s70, 0\n\t" \
    "s_cmp_eq_u32 s22, s28\n\t" \
    "s_addc_u32 s70, s70, 0\n\t" \
    "s_cmp_eq_u32 s24, s28\n\t" \
    "s_addc_u32 s70, s70, 0\n\t" \
    "s_cmp_eq_u32 s26, s28\n\t" \
    "s_addc_u32 s70, s70, 0\n\t" \
    "s_mov_b32 s75, s73\n\t" \
    "s_mov_b32 s76, s74\n\t" \
    "s_lshl_b32 s70, s70, 3\n\t" \
    "s_sub_u32 s73, s56, s70\n\t" \
    "s_lshl_b32 s74, s28, 4\n\t" \
    "s_sub_u32 s69, s73, s75\n\t" \
    "s_sub_u32 s70, s74, s76\n\t" \
    "s_sub_u32 s71, s70, 1600\n\t" \
    "s_cmp_lt_u32 s71, 400000\n\t" \
    "s_cbranch_scc0 vv_K28_%=\n\t" \
    "v_cvt_f32_u32 v141, s69\n\t" \
    "v_cvt_f32_u32 v142, s70\n\t" \
    "v_rcp_f32 v143, v141\n\t" \
    "v_rcp_f32 v144, v142\n\t" \
    "v_mul_f32 v143, v143, v142\n\t" \
    "v_mul_f32 v144, v144, v141\n\t" \
    "v_mul_f32 v143, 0x4f800000, v143\n\t" \
    "v_mul_f32 v144, 0x4f000000, v144\n\t" \
    "v_cvt_u32_f32 v143, v143\n\t" \
    "v_cvt_u32_f32 v144, v144\n\t" \
    "v_readfirstlane_b32 s69, v143\n\t" \
    "v_readfirstlane_b32 s70, v144\n\t" \
    "s_nop 1\n\t" \
    "s_sub_u32 s69, s69, s78\n\t" \
    "s_ashr_i32 s69, s69, 2\n\t" \
    "s_add_u32 s78, s78, s69\n\t" \
    "s_sub_u32 s70, s70, s77\n\t" \
    "s_ashr_i32 s70, s70, 2\n\t" \
    "s_add_u32 s77, s77, s70\n\t" \
    "vv_K28_%=:\n\t" \
    "s_waitcnt vmcnt(6)\n\t" \
    "v_cmp_eq_u32 vcc, -1, v140\n\t" \
    "s_cbranch_vccz vv_Hok30_%=\n\t" \
    "vv_H29_%=:\n\t" \
    "global_load_dword v140, v20, s[46:47] sc1\n\t" \
    "s_waitcnt vmcnt(0)\n\t" \
    "v_cmp_eq_u32 vcc, -1, v140\n\t" \
    "s_cbranch_vccz vv_Hc31_%=\n\t" \
    "s_cmp_eq_u32 s68, 0\n\t" \
    "s_cbranch_scc1 vv_F32_%=\n\t" \
    "s_sub_u32 s68, s68, 1\n\t" \
    "s_sleep 5\n\t" \
    "s_branch vv_H29_%=\n\t" \
    "vv_F32_%=:\n\t" \
    "v_mov_b32 v141, 6095104\n\t" \
    "v_mov_b32 v142, 6\n\t" \
    "global_store_dword v141, v142, s[36:37] sc1\n\t" \
    "s_waitcnt vmcnt(0)\n\t" \
    "s_branch vv_Hc31_%=\n\t" \
    "vv_Hc31_%=:\n\t" \
    "vv_Hok30_%=:\n\t" \
    "v_subrev_u32 v140, s80, v140\n\t" \
    "v_and_b32 v20, -5, v20\n\t" \
    "s_nop 0\n\t" \
    "v_max_i32_dpp v140, v140, v140 row_ror:1 row_mask:0xf bank_mask:0xf\n\t" \
    "s_nop 1\n\t" \
    "v_max_i32_dpp v140, v140, v140 row_ror:2 row_mask:0xf bank_mask:0xf\n\t" \
    "s_nop 1\n\t" \
    "v_max_i32_dpp v140, v140, v140 row_ror:4 row_mask:0xf bank_mask:0xf\n\t" \
    "s_nop 1\n\t" \
    "v_max_i32_dpp v140, v140, v140 row_ror:8 row_mask:0xf bank_mask:0xf\n\t" \
    "s_nop 0\n\t" \
    "v_readlane_b32 s69, v140, 0\n\t" \
    "v_readlane_b32 s71, v140, 16\n\t" \
    "v_readlane_b32 s70, v140, 32\n\t" \
    "v_readlane_b32 s81, v140, 48\n\t" \
    "s_max_i32 s69, s69, s71\n\t" \
    "s_nop 0\n\t" \
    "s_max_i32 s71, s70, s81\n\t" \
    "s_max_i32 s69, s69, s71\n\t" \
    "s_add_u32 s81, s69, s80\n\t" \
    "s_cmp_lg_u32 s66, 0\n\t" \
    "s_cbranch_scc1 vv_SF33_%=\n\t" \
    "s_lshl_b32 s69, s67, 7\n\t" \
    "s_add_u32 s69, s69, 6131712\n\t" \
    "s_add_u32 s69, s69, s52\n\t" \
    "s_add_u32 s70, s65, -1\n\t" \
    "v_mov_b32 v140, s69\n\t" \
    "v_mov_b32 v141, s70\n\t" \
    "s_mov_b64 exec, 1\n\t" \
    "global_store_dword v140, v141, s[36:37] sc1\n\t" \
    "s_mov_b64 exec, -1\n\t" \
    "vv_SF33_%=:\n\t" \
    "v_mov_b32 v72, 0\n\t" \
    "v_mov_b32 v73, 0\n\t" \
    "v_mov_b32 v74, 0\n\t" \
    "v_mov_b32 v75, 0\n\t" \
    "v_mov_b32 v76, 0\n\t" \
    "v_mov_b32 v77, 0\n\t" \
    "v_mov_b32 v78, 0\n\t" \
    "v_mov_b32 v79, 0\n\t" \
    "v_mov_b32 v92, 0\n\t" \
    "v_mov_b32 v93, 0\n\t" \
    "v_mov_b32 v94, 0\n\t" \
    "v_mov_b32 v95, 0\n\t" \
    "v_mov_b32 v96, 0\n\t" \
    "v_mov_b32 v97, 0\n\t" \
    "v_mov_b32 v98, 0\n\t" \
    "v_mov_b32 v99, 0\n\t" \
    "v_mov_b32 v100, 0\n\t" \
    "v_mov_b32 v101, 0\n\t" \
    "v_mov_b32 v102, 0\n\t" \
    "v_mov_b32 v103, 0\n\t" \
    "v_mov_b32 v104, 0\n\t" \
    "v_mov_b32 v105, 0\n\t" \
    "v_mov_b32 v106, 0\n\t" \
    "v_mov_b32 v107, 0\n\t" \
    "v_mov_b32 v108, 0\n\t" \
    "v_mov_b32 v109, 0\n\t" \
    "v_mov_b32 v110, 0\n\t" \
    "v_mov_b32 v111, 0\n\t" \
    "v_mov_b32 v112, 0\n\t" \
    "v_mov_b32 v113, 0\n\t" \
    "v_mov_b32 v114, 0\n\t" \
    "v_mov_b32 v115, 0\n\t" \
    "v_mov_b32 v116, 0\n\t" \
    "v_mov_b32 v117, 0\n\t" \
    "v_mov_b32 v118, 0\n\t" \
    "v_mov_b32 v119, 0\n\t" \
    "v_mov_b32 v120, 0\n\t" \
    "v_mov_b32 v121, 0\n\t" \
    "v_mov_b32 v122, 0\n\t" \
    "v_mov_b32 v123, 0\n\t" \
    "v_mov_b32 v124, 0\n\t" \
    "v_mov_b32 v125, 0\n\t" \
    "v_mov_b32 v126, 0\n\t" \
    "v_mov_b32 v127, 0\n\t" \
    "s_cmp_lg_u32 s65, 0\n\t" \
    "s_cbranch_scc1 vv_Oc36_%=\n\t" \
    "s_add_u32 s79, s81, 240\n\t" \
    "s_sub_u32 s70, s79, s74\n\t" \
    "s_max_i32 s70, s70, 0\n\t" \
    "s_min_i32 s70, s70, 16000\n\t" \
    "s_lshl_b32 s70, s70, 1\n\t" \
    "s_mul_hi_u32 s70, s70, s77\n\t" \
    "s_add_u32 s70, s70, s73\n\t" \
    "vv_W37_%=:\n\t" \
    "s_memtime s[56:57]\n\t" \
    "s_waitcnt lgkmcnt(0)\n\t" \
    "s_sub_u32 s69, s70, s56\n\t" \
    "s_cmp_gt_i32 s69, 24\n\t" \
    "s_cbranch_scc1 vv_W37_%=\n\t" \
    "global_load_dword v140, v35, s[46:47] sc1\n\t" \
    "s_waitcnt vmcnt(0)\n\t" \
    "v_cmp_eq_u32 vcc, -1, v140\n\t" \
    "s_cbranch_vccz vv_Ook35_%=\n\t" \
    "vv_O34_%=:\n\t" \
    "global_load_dword v140, v35, s[46:47] offset:1536 sc1\n\t" \
    "s_waitcnt vmcnt(0)\n\t" \
    "v_cmp_eq_u32 vcc, -1, v140\n\t" \
    "s_cbranch_vccz vv_Ook35_%=\n\t" \
    "s_cmp_eq_u32 s68, 0\n\t" \
    "s_cbranch_scc1 vv_F38_%=\n\t" \
    "s_sub_u32 s68, s68, 1\n\t" \
    "s_sleep 3\n\t" \
    "s_branch vv_O34_%=\n\t" \
    "vv_F38_%=:\n\t" \
    "v_mov_b32 v141, 6095104\n\t" \
    "v_mov_b32 v142, 17\n\t" \
    "global_store_dword v141, v142, s[36:37] sc1\n\t" \
    "s_waitcnt vmcnt(0)\n\t" \
    "s_branch vv_Ook35_%=\n\t" \
    "vv_Ook35_%=:\n\t" \
    "v_subrev_u32 v140, s80, v140\n\t" \
    "s_nop 1\n\t" \
    "v_max_i32_dpp v140, v140, v140 row_ror:1 row_mask:0xf bank_mask:0xf\n\t" \
    "s_nop 1\n\t" \
    "v_max_i32_dpp v140, v140, v140 row_ror:2 row_mask:0xf bank_mask:0xf\n\t" \
    "s_nop 1\n\t" \
    "v_max_i32_dpp v140, v140, v140 row_ror:4 row_mask:0xf bank_mask:0xf\n\t" \
    "s_nop 1\n\t" \
    "v_max_i32_dpp v140, v140, v140 row_ror:8 row_mask:0xf bank_mask:0xf\n\t" \
    "s_nop 0\n\t" \
    "v_readlane_b32 s69, v140, 0\n\t" \
    "v_readlane_b32 s71, v140, 16\n\t" \
    "v_readlane_b32 s70, v140, 32\n\t" \
    "v_readlane_b32 s81, v140, 48\n\t" \
    "s_max_i32 s69, s69, s71\n\t" \
    "s_nop 0\n\t" \
    "s_max_i32 s71, s70, s81\n\t" \
    "s_max_i32 s69, s69, s71\n\t" \
    "s_add_u32 s81, s69, s80\n\t" \
    "vv_Oc36_%=:\n\t" \
    "s_mul_hi_u32 s69, s85, s78\n\t" \
    "s_add_u32 s79, s81, s69\n\t" \
    "s_add_u32 s79, s79, s87\n\t" \
    "s_cmp_ge_u32 s65, 36\n\t" \
    "s_cselect_b32 s69, 28, 0\n\t" \
    "s_sub_u32 s79, s79, s69\n\t" \
    "s_cmp_lt_u32 s65, 8\n\t" \
    "s_cselect_b32 s69, 96, 0\n\t" \
    "s_add_u32 s79, s79, s69\n\t" \
    "s_cmp_ge_u32 s65, 57\n\t" \
    "s_cselect_b32 s69, 32, 0\n\t" \
    "s_add_u32 s79, s79, s69\n\t" \
    "s_add_u32 s80, s79, s88\n\t" \
    "s_add_u32 s65, s65, 1\n\t" \
    "s_branch vv_go15_%=\n\t" \
    "vv_Gbad21_%=:\n\t" \
    "s_waitcnt vmcnt(0) lgkmcnt(0)\n\t" \
    "s_nop 7\n\t" \
    "v_mov_b32 v72, 0\n\t" \
    "v_mov_b32 v73, 0\n\t" \
    "v_mov_b32 v74, 0\n\t" \
    "v_mov_b32 v75, 0\n\t" \
    "v_mov_b32 v76, 0\n\t" \
    "v_mov_b32 v77, 0\n\t" \
    "v_mov_b32 v78, 0\n\t" \
    "v_mov_b32 v79, 0\n\t" \
    "s_cmp_eq_u32 s68, 0\n\t" \
    "s_cbranch_scc1 vv_F39_%=\n\t" \
    "s_sub_u32 s68, s68, 1\n\t" \
    "s_branch vv_G20_%=\n\t" \
    "vv_F39_%=:\n\t" \
    "v_mov_b32 v140, 6095104\n\t" \
    "v_mov_b32 v141, 1\n\t" \
    "global_store_dword v140, v141, s[36:37] sc1\n\t" \
    "s_waitcnt vmcnt(0)\n\t" \
    "s_branch vv_Gc23_%=\n\t" \
    "vv_Gbad222_%=:\n\t" \
    "global_load_dwordx4 v[44:47], v2, s[46:47] sc1\n\t" \
    "s_waitcnt vmcnt(0)\n\t" \
    "v_max3_u32 v140, v44, v45, v46\n\t" \
    "v_max_u32 v140, v140, v47\n\t" \
    "v_cmp_eq_u32 vcc, -1, v140\n\t" \
    "s_cbranch_vccz vv_Gre225_%=\n\t" \
    "s_cmp_eq_u32 s68, 0\n\t" \
    "s_cbranch_scc1 vv_F40_%=\n\t" \
    "s_sub_u32 s68, s68, 1\n\t" \
    "s_sleep 1\n\t" \
    "s_branch vv_Gbad222_%=\n\t" \
    "vv_F40_%=:\n\t" \
    "v_mov_b32 v140, 6095104\n\t" \
    "v_mov_b32 v141, 2\n\t" \
    "global_store_dword v140, v141, s[36:37] sc1\n\t" \
    "s_waitcnt vmcnt(0)\n\t" \
    "s_branch vv_Gre225_%=\n\t" \
    "vv_Gre225_%=:\n\t" \
    "v_mov_b32 v72, 0\n\t" \
    "v_mov_b32 v73, 0\n\t" \
    "v_mov_b32 v74, 0\n\t" \
    "v_mov_b32 v75, 0\n\t" \
    "v_mov_b32 v76, 0\n\t" \
    "v_mov_b32 v77, 0\n\t" \
    "v_mov_b32 v78, 0\n\t" \
    "v_mov_b32 v79, 0\n\t" \
    "v_smfmac_f32_16x16x64_bf16 v[72:75], v[36:39], a[0:7], v10\n\t" \
    "s_nop 0\n\t" \
    "v_smfmac_f32_16x16x64_bf16 v[76:79], v[36:39], a[72:79], v10\n\t" \
    "v_smfmac_f32_16x16x64_bf16 v[72:75], v[40:43], a[8:15], v10\n\t" \
    "v_smfmac_f32_16x16x64_bf16 v[76:79], v[40:43], a[80:87], v10\n\t" \
    "v_smfmac_f32_16x16x64_bf16 v[72:75], v[44:47], a[64:71], v10\n\t" \
    "v_smfmac_f32_16x16x64_bf16 v[76:79], v[44:47], a[136:143], v10\n\t" \
    "s_branch vv_Gc224_%=\n\t" \
    "vv_end14_%=:\n\t" \
    "s_endpgm\n\t" \
    "vv_loop11_%=:\n\t" \
    "v_mov_b32 v72, 0\n\t" \
    "v_mov_b32 v73, 0\n\t" \
    "v_mov_b32 v74, 0\n\t" \
    "v_mov_b32 v75, 0\n\t" \
    "v_mov_b32 v76, 0\n\t" \
    "v_mov_b32 v77, 0\n\t" \
    "v_mov_b32 v78, 0\n\t" \
    "v_mov_b32 v79, 0\n\t" \
    "v_mov_b32 v92, 0\n\t" \
    "v_mov_b32 v93, 0\n\t" \
    "v_mov_b32 v94, 0\n\t" \
    "v_mov_b32 v95, 0\n\t" \
    "v_mov_b32 v96, 0\n\t" \
    "v_mov_b32 v97, 0\n\t" \
    "v_mov_b32 v98, 0\n\t" \
    "v_mov_b32 v99, 0\n\t" \
    "v_mov_b32 v100, 0\n\t" \
    "v_mov_b32 v101, 0\n\t" \
    "v_mov_b32 v102, 0\n\t" \
    "v_mov_b32 v103, 0\n\t" \
    "v_mov_b32 v104, 0\n\t" \
    "v_mov_b32 v105, 0\n\t" \
    "v_mov_b32 v106, 0\n\t" \
    "v_mov_b32 v107, 0\n\t" \
    "v_mov_b32 v108, 0\n\t" \
    "v_mov_b32 v109, 0\n\t" \
    "v_mov_b32 v110, 0\n\t" \
    "v_mov_b32 v111, 0\n\t" \
    "v_mov_b32 v112, 0\n\t" \
    "v_mov_b32 v113, 0\n\t" \
    "v_mov_b32 v114, 0\n\t" \
    "v_mov_b32 v115, 0\n\t" \
    "v_mov_b32 v116, 0\n\t" \
    "v_mov_b32 v117, 0\n\t" \
    "v_mov_b32 v118, 0\n\t" \
    "v_mov_b32 v119, 0\n\t" \
    "v_mov_b32 v120, 0\n\t" \
    "v_mov_b32 v121, 0\n\t" \
    "v_mov_b32 v122, 0\n\t" \
    "v_mov_b32 v123, 0\n\t" \
    "v_mov_b32 v124, 0\n\t" \
    "v_mov_b32 v125, 0\n\t" \
    "v_mov_b32 v126, 0\n\t" \
    "v_mov_b32 v127, 0\n\t" \
    "vv_go42_%=:\n\t" \
    "s_add_u32 s69, s65, 0\n\t" \
    "s_bitcmp1_b32 s69, 0\n\t" \
    "s_cselect_b32 s70, s63, s62\n\t" \
    "s_bfe_u32 s69, s69, 0x20001\n\t" \
    "s_lshl_b32 s69, s69, 19\n\t" \
    "s_add_u32 s69, s69, s70\n\t" \
    "s_add_u32 s46, s36, s69\n\t" \
    "s_addc_u32 s47, s37, 0\n\t" \
    "s_add_u32 s69, s65, 0\n\t" \
    "s_lshr_b32 s69, s69, 1\n\t" \
    "s_xor_b32 s69, s69, s97\n\t" \
    "s_and_b32 s69, s69, 1\n\t" \
    "s_lshl_b32 s69, s69, 14\n\t" \
    "s_or_b32 s46, s46, s69\n\t" \
    "s_add_u32 s69, s65, 0\n\t" \
    "s_mul_i32 s70, s69, 171\n\t" \
    "s_lshr_b32 s70, s70, 9\n\t" \
    "s_mul_i32 s70, s70, 3\n\t" \
    "s_sub_u32 s69, s69, s70\n\t" \
    "s_mul_i32 s69, s69, 36\n\t" \
    "s_lshl_b32 s69, s69, 14\n\t" \
    "s_add_u32 s69, s69, s64\n\t" \
    "s_add_u32 s69, s69, 4194304\n\t" \
    "s_add_u32 s38, s36, s69\n\t" \
    "s_addc_u32 s39, s37, 0\n\t" \
    "s_mul_i32 s69, s65, 9216\n\t" \
    "s_add_u32 s69, s69, 123731968\n\t" \
    "s_add_u32 s40, s36, s69\n\t" \
    "s_addc_u32 s41, s37, 0\n\t" \
    "s_sub_u32 s70, s79, s74\n\t" \
    "s_max_i32 s70, s70, 0\n\t" \
    "s_min_i32 s70, s70, 16000\n\t" \
    "s_lshl_b32 s70, s70, 1\n\t" \
    "s_mul_hi_u32 s70, s70, s77\n\t" \
    "s_add_u32 s70, s70, s73\n\t" \
    "s_sub_u32 s70, s70, 100\n\t" \
    "s_getpc_b64 s[30:31]\n\t" \
    "vv_Wp44_%=:\n\t" \
    "s_add_u32 s30, s30, vv_We45_%=-vv_Wp44_%=\n\t" \
    "s_addc_u32 s31, s31, 0\n\t" \
    "vv_W43_%=:\n\t" \
    "s_memtime s[56:57]\n\t" \
    "s_waitcnt lgkmcnt(0)\n\t" \
    "s_sub_u32 s69, s70, s56\n\t" \
    "s_cmp_gt_i32 s69, 111\n\t" \
    "s_cbranch_scc1 vv_W43_%=\n\t" \
    "s_max_i32 s69, s69, 0\n\t" \
    "s_lshr_b32 s69, s69, 1\n\t" \
    "s_and_b32 s69, s69, 0x3c\n\t" \
    "s_sub_u32 s30, s30, s69\n\t" \
    "s_subb_u32 s31, s31, 0\n\t" \
    "s_setpc_b64 s[30:31]\n\t" \
    ".p2align 6\n\t" \
    "vv_al46_%=:\n\t" \
    "s_nop 0\n\t" \
    "s_nop 1\n\t" \
    "s_nop 1\n\t" \
    "s_nop 1\n\t" \
    "s_nop 1\n\t" \
    "s_nop 1\n\t" \
    "s_nop 1\n\t" \
    "s_nop 1\n\t" \
    "s_nop 1\n\t" \
    "s_nop 1\n\t" \
    "s_nop 1\n\t" \
    "s_nop 1\n\t" \
    "s_nop 1\n\t" \
    "s_nop 1\n\t" \
    "vv_We45_%=:\n\t" \
    "vv_G47_%=:\n\t" \
    "global_load_dwordx4 v[36:39], v1, s[46:47] sc1\n\t" \
    "s_nop 9\n\t" \
    "global_load_dwordx4 v[40:43], v1, s[46:47] offset:1024 sc1\n\t" \
    "s_nop 9\n\t" \
    "global_load_dwordx4 v[44:47], v2, s[46:47] sc1\n\t" \
    "s_waitcnt vmcnt(2)\n\t" \
    "v_smfmac_f32_16x16x64_bf16 v[72:75], v[36:39], a[16:23], v10\n\t" \
    "ds_write_b128 v0, v[36:39] offset:2048\n\t" \
    "v_smfmac_f32_16x16x64_bf16 v[76:79], v[36:39], a[88:95], v10\n\t" \
    "v_max3_u32 v140, v36, v37, v38\n\t" \
    "s_waitcnt vmcnt(1)\n\t" \
    "ds_write_b128 v0, v[40:43] offset:3072\n\t" \
    "v_smfmac_f32_16x16x64_bf16 v[72:75], v[40:43], a[24:31], v10\n\t" \
    "v_max3_u32 v140, v140, v39, v40\n\t" \
    "v_max3_u32 v140, v140, v41, v42\n\t" \
    "v_max_u32 v140, v140, v43\n\t" \
    "v_cmp_eq_u32 vcc, -1, v140\n\t" \
    "v_smfmac_f32_16x16x64_bf16 v[76:79], v[40:43], a[96:103], v10\n\t" \
    "s_cbranch_vccnz vv_Gbad48_%=\n\t" \
    "vv_Gc50_%=:\n\t" \
    "s_waitcnt lgkmcnt(0)\n\t" \
    "s_barrier\n\t" \
    "ds_read_b128 v[48:51], v0 offset:4096\n\t" \
    "ds_read_b128 v[52:55], v0 offset:5120\n\t" \
    "s_waitcnt vmcnt(0)\n\t" \
    "v_smfmac_f32_16x16x64_bf16 v[72:75], v[44:47], a[64:71], v10\n\t" \
    "ds_read_b128 v[56:59], v0 offset:6144\n\t" \
    "ds_read_b128 v[60:63], v0 offset:7168\n\t" \
    "v_max3_u32 v140, v44, v45, v46\n\t" \
    "v_max_u32 v140, v140, v47\n\t" \
    "v_smfmac_f32_16x16x64_bf16 v[76:79], v[44:47], a[136:143], v10\n\t" \
    "ds_read_b128 v[64:67], v0 offset:0\n\t" \
    "ds_read_b128 v[68:71], v0 offset:1024\n\t" \
    "v_cmp_eq_u32 vcc, -1, v140\n\t" \
    "s_cbranch_vccnz vv_Gbad249_%=\n\t" \
    "vv_Gc251_%=:\n\t" \
    "s_waitcnt lgkmcnt(5)\n\t" \
    "v_smfmac_f32_16x16x64_bf16 v[72:75], v[48:51], a[32:39], v10\n\t" \
    "s_waitcnt lgkmcnt(4)\n\t" \
    "v_smfmac_f32_16x16x64_bf16 v[72:75], v[52:55], a[40:47], v10\n\t" \
    "s_waitcnt lgkmcnt(3)\n\t" \
    "v_smfmac_f32_16x16x64_bf16 v[72:75], v[56:59], a[48:55], v10\n\t" \
    "s_waitcnt lgkmcnt(2)\n\t" \
    "v_smfmac_f32_16x16x64_bf16 v[72:75], v[60:63], a[56:63], v10\n\t" \
    "s_waitcnt lgkmcnt(1)\n\t" \
    "v_smfmac_f32_16x16x64_bf16 v[72:75], v[64:67], a[0:7], v10\n\t" \
    "s_waitcnt lgkmcnt(0)\n\t" \
    "v_smfmac_f32_16x16x64_bf16 v[72:75], v[68:71], a[8:15], v10\n\t" \
    "v_smfmac_f32_16x16x64_bf16 v[76:79], v[48:51], a[104:111], v10\n\t" \
    "s_nop 1\n\t" \
    "s_nop 0\n\t" \
    "v_smfmac_f32_16x16x64_bf16 v[76:79], v[52:55], a[112:119], v10\n\t" \
    "s_nop 1\n\t" \
    "s_nop 0\n\t" \
    "v_smfmac_f32_16x16x64_bf16 v[76:79], v[56:59], a[120:127], v10\n\t" \
    "v_add_f32 v72, v72, v74\n\t" \
    "v_smfmac_f32_16x16x64_bf16 v[76:79], v[60:63], a[128:135], v10\n\t" \
    "v_add_f32 v73, v73, v75\n\t" \
    "v_smfmac_f32_16x16x64_bf16 v[76:79], v[64:67], a[72:79], v10\n\t" \
    "v_cvt_pk_bf16_f32 v80, v72, v73\n\t" \
    "v_smfmac_f32_16x16x64_bf16 v[76:79], v[68:71], a[80:87], v10\n\t" \
    "ds_write_b32 v12, v80 offset:0\n\t" \
    "s_nop 6\n\t" \
    "v_add_f32 v76, v76, v78\n\t" \
    "v_add_f32 v77, v77, v79\n\t" \
    "v_cvt_pk_bf16_f32 v81, v76, v77\n\t" \
    "ds_write_b32 v12, v81 offset:64\n\t" \
    "s_waitcnt lgkmcnt(0)\n\t" \
    "s_barrier\n\t" \
    "ds_read_b64_tr_b16 v[84:85], v13 offset:0\n\t" \
    "ds_read_b64_tr_b16 v[86:87], v13 offset:128\n\t" \
    "ds_read_b64_tr_b16 v[88:89], v13 offset:1024\n\t" \
    "ds_read_b64_tr_b16 v[90:91], v13 offset:1152\n\t" \
    "s_waitcnt lgkmcnt(2)\n\t" \
    "v_smfmac_f32_16x16x64_bf16 v[92:95], v[84:87], a[144:151], v10\n\t" \
    "s_nop 2\n\t" \
    "s_waitcnt lgkmcnt(0)\n\t" \
    "v_smfmac_f32_16x16x64_bf16 v[92:95], v[88:91], a[152:159], v10\n\t" \
    "s_nop 2\n\t" \
    "v_smfmac_f32_16x16x64_bf16 v[96:99], v[84:87], a[160:167], v10\n\t" \
    "s_nop 2\n\t" \
    "v_smfmac_f32_16x16x64_bf16 v[96:99], v[88:91], a[168:175], v10\n\t" \
    "s_nop 2\n\t" \
    "v_smfmac_f32_16x16x64_bf16 v[100:103], v[84:87], a[176:183], v10\n\t" \
    "s_nop 0\n\t" \
    "v_add_f32 v92, v92, v94\n\t" \
    "v_add_f32 v93, v93, v95\n\t" \
    "v_smfmac_f32_16x16x64_bf16 v[100:103], v[88:91], a[184:191], v10\n\t" \
    "s_nop 1\n\t" \
    "v_cvt_pk_bf16_f32 v128, v92, v93\n\t" \
    "v_smfmac_f32_16x16x64_bf16 v[104:107], v[84:87], a[192:199], v10\n\t" \
    "s_nop 0\n\t" \
    "v_add_f32 v96, v96, v98\n\t" \
    "v_add_f32 v97, v97, v99\n\t" \
    "v_smfmac_f32_16x16x64_bf16 v[104:107], v[88:91], a[200:207], v10\n\t" \
    "s_nop 1\n\t" \
    "v_cvt_pk_bf16_f32 v129, v96, v97\n\t" \
    "v_smfmac_f32_16x16x64_bf16 v[124:127], v[84:87], v[240:247], v10\n\t" \
    "s_nop 0\n\t" \
    "v_add_f32 v100, v100, v102\n\t" \
    "v_add_f32 v101, v101, v103\n\t" \
    "v_smfmac_f32_16x16x64_bf16 v[124:127], v[88:91], v[248:255], v10\n\t" \
    "s_nop 1\n\t" \
    "global_load_dword v140, v20, s[46:47] sc1\n\t" \
    "v_cvt_pk_bf16_f32 v130, v100, v101\n\t" \
    "v_smfmac_f32_16x16x64_bf16 v[108:111], v[84:87], a[208:215], v10\n\t" \
    "s_nop 0\n\t" \
    "v_add_f32 v104, v104, v106\n\t" \
    "v_add_f32 v105, v105, v107\n\t" \
    "v_smfmac_f32_16x16x64_bf16 v[108:111], v[88:91], a[216:223], v10\n\t" \
    "s_nop 0\n\t" \
    "v_cvt_pk_bf16_f32 v131, v104, v105\n\t" \
    "v_permlane32_swap_b32 v128, v130\n\t" \
    "v_smfmac_f32_16x16x64_bf16 v[112:115], v[84:87], a[224:231], v10\n\t" \
    "s_nop 0\n\t" \
    "v_permlane32_swap_b32 v129, v131\n\t" \
    "s_nop 1\n\t" \
    "v_permlane16_swap_b32 v128, v129\n\t" \
    "v_smfmac_f32_16x16x64_bf16 v[112:115], v[88:91], a[232:239], v10\n\t" \
    "v_permlane16_swap_b32 v130, v131\n\t" \
    "global_store_dwordx4 v14, v[128:131], s[38:39] sc1\n\t" \
    "v_add_f32 v124, v124, v126\n\t" \
    "v_smfmac_f32_16x16x64_bf16 v[116:119], v[84:87], a[240:247], v10\n\t" \
    "s_nop 0\n\t" \
    "v_add_f32 v125, v125, v127\n\t" \
    "v_cvt_pk_bf16_f32 v136, v124, v125\n\t" \
    "v_smfmac_f32_16x16x64_bf16 v[116:119], v[88:91], a[248:255], v10\n\t" \
    "global_store_dword v16, v136, s[38:39] sc1\n\t" \
    "v_add_f32 v108, v108, v110\n\t" \
    "v_add_f32 v109, v109, v111\n\t" \
    "v_smfmac_f32_16x16x64_bf16 v[120:123], v[84:87], v[224:231], v10\n\t" \
    "s_nop 0\n\t" \
    "v_cvt_pk_bf16_f32 v132, v108, v109\n\t" \
    "v_add_f32 v112, v112, v114\n\t" \
    "v_smfmac_f32_16x16x64_bf16 v[120:123], v[88:91], v[232:239], v10\n\t" \
    "v_add_f32 v113, v113, v115\n\t" \
    "v_cvt_pk_bf16_f32 v133, v112, v113\n\t" \
    "v_add_f32 v116, v116, v118\n\t" \
    "v_add_f32 v117, v117, v119\n\t" \
    "v_cvt_pk_bf16_f32 v134, v116, v117\n\t" \
    "s_nop 2\n\t" \
    "v_pk_add_f32 v[120:121], v[120:121], v[122:123]\n\t" \
    "v_cvt_pk_bf16_f32 v135, v120, v121\n\t" \
    "v_permlane32_swap_b32 v132, v134\n\t" \
    "s_nop 0\n\t" \
    "v_permlane32_swap_b32 v133, v135\n\t" \
    "s_nop 1\n\t" \
    "v_permlane16_swap_b32 v132, v133\n\t" \
    "v_permlane16_swap_b32 v134, v135\n\t" \
    "global_store_dwordx4 v15, v[132:135], s[38:39] sc1\n\t" \
    "s_nop 1\n\t" \
    "global_store_dwordx4 v17, v[128:131], s[40:41] sc1\n\t" \
    "global_store_dwordx4 v17, v[132:135], s[40:41] offset:1024 sc1\n\t" \
    "global_store_dword v18, v136, s[40:41] sc1\n\t" \
    "s_cmp_eq_u32 s65, 63\n\t" \
    "s_cbranch_scc1 vv_end41_%=\n\t" \
    "s_mov_b32 s71, 3\n\t" \
    "vv_E53_%=:\n\t" \
    "s_memtime s[54:55]\n\t" \
    "s_nop 0\n\t" \
    "s_memrealtime s[4:5]\n\t" \
    "s_nop 0\n\t" \
    "s_memrealtime s[6:7]\n\t" \
    "s_nop 0\n\t" \
    "s_memrealtime s[8:9]\n\t" \
    "s_nop 0\n\t" \
    "s_memrealtime s[10:11]\n\t" \
    "s_nop 0\n\t" \
    "s_memrealtime s[12:13]\n\t" \
    "s_nop 0\n\t" \
    "s_memrealtime s[14:15]\n\t" \
    "s_nop 0\n\t" \
    "s_memrealtime s[16:17]\n\t" \
    "s_nop 0\n\t" \
    "s_memrealtime s[18:19]\n\t" \
    "s_nop 0\n\t" \
    "s_memrealtime s[20:21]\n\t" \
    "s_nop 0\n\t" \
    "s_memrealtime s[22:23]\n\t" \
    "s_nop 0\n\t" \
    "s_memrealtime s[24:25]\n\t" \
    "s_nop 0\n\t" \
    "s_memrealtime s[26:27]\n\t" \
    "s_nop 0\n\t" \
    "s_memrealtime s[28:29]\n\t" \
    "s_nop 0\n\t" \
    "s_memtime s[56:57]\n\t" \
    "s_mov_b32 s70, 1\n\t" \
    "s_sub_u32 s71, s71, 1\n\t" \
    "s_cselect_b32 s69, 0, -1\n\t" \
    "s_waitcnt lgkmcnt(0)\n\t" \
    "s_sub_u32 s54, s56, s54\n\t" \
    "s_and_b32 s54, s54, s69\n\t" \
    "s_cmp_gt_u32 s54, 124\n\t" \
    "s_cbranch_scc1 vv_E53_%=\n\t" \
    "s_cmp_eq_u32 s4, s28\n\t" \
    "s_addc_u32 s70, s70, 0\n\t" \
    "s_cmp_eq_u32 s6, s28\n\t" \
    "s_addc_u32 s70, s70, 0\n\t" \
    "s_cmp_eq_u32 s8, s28\n\t" \
    "s_addc_u32 s70, s70, 0\n\t" \
    "s_cmp_eq_u32 s10, s28\n\t" \
    "s_addc_u32 s70, s70, 0\n\t" \
    "s_cmp_eq_u32 s12, s28\n\t" \
    "s_addc_u32 s70, s70, 0\n\t" \
    "s_cmp_eq_u32 s14, s28\n\t" \
    "s_addc_u32 s70, s70, 0\n\t" \
    "s_cmp_eq_u32 s16, s28\n\t" \
    "s_addc_u32 s70, s70, 0\n\t" \
    "s_cmp_eq_u32 s18, s28\n\t" \
    "s_addc_u32 s70, s70, 0\n\t" \
    "s_cmp_eq_u32 s20, s28\n\t" \
    "s_addc_u32 s70, s70, 0\n\t" \
    "s_cmp_eq_u32 s22, s28\n\t" \
    "s_addc_u32 s70, s70, 0\n\t" \
    "s_cmp_eq_u32 s24, s28\n\t" \
    "s_addc_u32 s70, s70, 0\n\t" \
    "s_cmp_eq_u32 s26, s28\n\t" \
    "s_addc_u32 s70, s70, 0\n\t" \
    "s_mov_b32 s75, s73\n\t" \
    "s_mov_b32 s76, s74\n\t" \
    "s_lshl_b32 s70, s70, 3\n\t" \
    "s_sub_u32 s73, s56, s70\n\t" \
    "s_lshl_b32 s74, s28, 4\n\t" \
    "s_sub_u32 s69, s73, s75\n\t" \
    "s_sub_u32 s70, s74, s76\n\t" \
    "s_sub_u32 s71, s70, 1600\n\t" \
    "s_cmp_lt_u32 s71, 400000\n\t" \
    "s_cbranch_scc0 vv_K55_%=\n\t" \
    "v_cvt_f32_u32 v141, s69\n\t" \
    "v_cvt_f32_u32 v142, s70\n\t" \
    "v_rcp_f32 v143, v141\n\t" \
    "v_rcp_f32 v144, v142\n\t" \
    "v_mul_f32 v143, v143, v142\n\t" \
    "v_mul_f32 v144, v144, v141\n\t" \
    "v_mul_f32 v143, 0x4f800000, v143\n\t" \
    "v_mul_f32 v144, 0x4f000000, v144\n\t" \
    "v_cvt_u32_f32 v143, v143\n\t" \
    "v_cvt_u32_f32 v144, v144\n\t" \
    "v_readfirstlane_b32 s69, v143\n\t" \
    "v_readfirstlane_b32 s70, v144\n\t" \
    "s_nop 1\n\t" \
    "s_sub_u32 s69, s69, s78\n\t" \
    "s_ashr_i32 s69, s69, 2\n\t" \
    "s_add_u32 s78, s78, s69\n\t" \
    "s_sub_u32 s70, s70, s77\n\t" \
    "s_ashr_i32 s70, s70, 2\n\t" \
    "s_add_u32 s77, s77, s70\n\t" \
    "vv_K55_%=:\n\t" \
    "s_waitcnt vmcnt(6)\n\t" \
    "v_cmp_eq_u32 vcc, -1, v140\n\t" \
    "s_cbranch_vccz vv_Hok57_%=\n\t" \
    "vv_H56_%=:\n\t" \
    "global_load_dword v140, v20, s[46:47] sc1\n\t" \
    "s_waitcnt vmcnt(0)\n\t" \
    "v_cmp_eq_u32 vcc, -1, v140\n\t" \
    "s_cbranch_vccz vv_Hc58_%=\n\t" \
    "s_cmp_eq_u32 s68, 0\n\t" \
    "s_cbranch_scc1 vv_F59_%=\n\t" \
    "s_sub_u32 s68, s68, 1\n\t" \
    "s_sleep 5\n\t" \
    "s_branch vv_H56_%=\n\t" \
    "vv_F59_%=:\n\t" \
    "v_mov_b32 v141, 6095104\n\t" \
    "v_mov_b32 v142, 6\n\t" \
    "global_store_dword v141, v142, s[36:37] sc1\n\t" \
    "s_waitcnt vmcnt(0)\n\t" \
    "s_branch vv_Hc58_%=\n\t" \
    "vv_Hc58_%=:\n\t" \
    "vv_Hok57_%=:\n\t" \
    "v_subrev_u32 v140, s80, v140\n\t" \
    "v_and_b32 v20, -5, v20\n\t" \
    "s_nop 0\n\t" \
    "v_max_i32_dpp v140, v140, v140 row_ror:1 row_mask:0xf bank_mask:0xf\n\t" \
    "s_nop 1\n\t" \
    "v_max_i32_dpp v140, v140, v140 row_ror:2 row_mask:0xf bank_mask:0xf\n\t" \
    "s_nop 1\n\t" \
    "v_max_i32_dpp v140, v140, v140 row_ror:4 row_mask:0xf bank_mask:0xf\n\t" \
    "s_nop 1\n\t" \
    "v_max_i32_dpp v140, v140, v140 row_ror:8 row_mask:0xf bank_mask:0xf\n\t" \
    "s_nop 0\n\t" \
    "v_readlane_b32 s69, v140, 0\n\t" \
    "v_readlane_b32 s71, v140, 16\n\t" \
    "v_readlane_b32 s70, v140, 32\n\t" \
    "v_readlane_b32 s81, v140, 48\n\t" \
    "s_max_i32 s69, s69, s71\n\t" \
    "s_nop 0\n\t" \
    "s_max_i32 s71, s70, s81\n\t" \
    "s_max_i32 s69, s69, s71\n\t" \
    "s_add_u32 s81, s69, s80\n\t" \
    "v_mov_b32 v72, 0\n\t" \
    "v_mov_b32 v73, 0\n\t" \
    "v_mov_b32 v74, 0\n\t" \
    "v_mov_b32 v75, 0\n\t" \
    "v_mov_b32 v76, 0\n\t" \
    "v_mov_b32 v77, 0\n\t" \
    "v_mov_b32 v78, 0\n\t" \
    "v_mov_b32 v79, 0\n\t" \
    "v_mov_b32 v92, 0\n\t" \
    "v_mov_b32 v93, 0\n\t" \
    "v_mov_b32 v94, 0\n\t" \
    "v_mov_b32 v95, 0\n\t" \
    "v_mov_b32 v96, 0\n\t" \
    "v_mov_b32 v97, 0\n\t" \
    "v_mov_b32 v98, 0\n\t" \
    "v_mov_b32 v99, 0\n\t" \
    "v_mov_b32 v100, 0\n\t" \
    "v_mov_b32 v101, 0\n\t" \
    "v_mov_b32 v102, 0\n\t" \
    "v_mov_b32 v103, 0\n\t" \
    "v_mov_b32 v104, 0\n\t" \
    "v_mov_b32 v105, 0\n\t" \
    "v_mov_b32 v106, 0\n\t" \
    "v_mov_b32 v107, 0\n\t" \
    "v_mov_b32 v108, 0\n\t" \
    "v_mov_b32 v109, 0\n\t" \
    "v_mov_b32 v110, 0\n\t" \
    "v_mov_b32 v111, 0\n\t" \
    "v_mov_b32 v112, 0\n\t" \
    "v_mov_b32 v113, 0\n\t" \
    "v_mov_b32 v114, 0\n\t" \
    "v_mov_b32 v115, 0\n\t" \
    "v_mov_b32 v116, 0\n\t" \
    "v_mov_b32 v117, 0\n\t" \
    "v_mov_b32 v118, 0\n\t" \
    "v_mov_b32 v119, 0\n\t" \
    "v_mov_b32 v120, 0\n\t" \
    "v_mov_b32 v121, 0\n\t" \
    "v_mov_b32 v122, 0\n\t" \
    "v_mov_b32 v123, 0\n\t" \
    "v_mov_b32 v124, 0\n\t" \
    "v_mov_b32 v125, 0\n\t" \
    "v_mov_b32 v126, 0\n\t" \
    "v_mov_b32 v127, 0\n\t" \
    "s_cmp_lg_u32 s65, 0\n\t" \
    "s_cbranch_scc1 vv_Oc62_%=\n\t" \
    "s_add_u32 s79, s81, 240\n\t" \
    "s_sub_u32 s70, s79, s74\n\t" \
    "s_max_i32 s70, s70, 0\n\t" \
    "s_min_i32 s70, s70, 16000\n\t" \
    "s_lshl_b32 s70, s70, 1\n\t" \
    "s_mul_hi_u32 s70, s70, s77\n\t" \
    "s_add_u32 s70, s70, s73\n\t" \
    "vv_W63_%=:\n\t" \
    "s_memtime s[56:57]\n\t" \
    "s_waitcnt lgkmcnt(0)\n\t" \
    "s_sub_u32 s69, s70, s56\n\t" \
    "s_cmp_gt_i32 s69, 24\n\t" \
    "s_cbranch_scc1 vv_W63_%=\n\t" \
    "global_load_dword v140, v35, s[46:47] sc1\n\t" \
    "s_waitcnt vmcnt(0)\n\t" \
    "v_cmp_eq_u32 vcc, -1, v140\n\t" \
    "s_cbranch_vccz vv_Ook61_%=\n\t" \
    "vv_O60_%=:\n\t" \
    "global_load_dword v140, v35, s[46:47] offset:1536 sc1\n\t" \
    "s_waitcnt vmcnt(0)\n\t" \
    "v_cmp_eq_u32 vcc, -1, v140\n\t" \
    "s_cbranch_vccz vv_Ook61_%=\n\t" \
    "s_cmp_eq_u32 s68, 0\n\t" \
    "s_cbranch_scc1 vv_F64_%=\n\t" \
    "s_sub_u32 s68, s68, 1\n\t" \
    "s_sleep 3\n\t" \
    "s_branch vv_O60_%=\n\t" \
    "vv_F64_%=:\n\t" \
    "v_mov_b32 v141, 6095104\n\t" \
    "v_mov_b32 v142, 17\n\t" \
    "global_store_dword v141, v142, s[36:37] sc1\n\t" \
    "s_waitcnt vmcnt(0)\n\t" \
    "s_branch vv_Ook61_%=\n\t" \
    "vv_Ook61_%=:\n\t" \
    "v_subrev_u32 v140, s80, v140\n\t" \
    "s_nop 1\n\t" \
    "v_max_i32_dpp v140, v140, v140 row_ror:1 row_mask:0xf bank_mask:0xf\n\t" \
    "s_nop 1\n\t" \
    "v_max_i32_dpp v140, v140, v140 row_ror:2 row_mask:0xf bank_mask:0xf\n\t" \
    "s_nop 1\n\t" \
    "v_max_i32_dpp v140, v140, v140 row_ror:4 row_mask:0xf bank_mask:0xf\n\t" \
    "s_nop 1\n\t" \
    "v_max_i32_dpp v140, v140, v140 row_ror:8 row_mask:0xf bank_mask:0xf\n\t" \
    "s_nop 0\n\t" \
    "v_readlane_b32 s69, v140, 0\n\t" \
    "v_readlane_b32 s71, v140, 16\n\t" \
    "v_readlane_b32 s70, v140, 32\n\t" \
    "v_readlane_b32 s81, v140, 48\n\t" \
    "s_max_i32 s69, s69, s71\n\t" \
    "s_nop 0\n\t" \
    "s_max_i32 s71, s70, s81\n\t" \
    "s_max_i32 s69, s69, s71\n\t" \
    "s_add_u32 s81, s69, s80\n\t" \
    "vv_Oc62_%=:\n\t" \
    "s_mul_hi_u32 s69, s85, s78\n\t" \
    "s_add_u32 s79, s81, s69\n\t" \
    "s_add_u32 s79, s79, s87\n\t" \
    "s_cmp_ge_u32 s65, 36\n\t" \
    "s_cselect_b32 s69, 28, 0\n\t" \
    "s_sub_u32 s79, s79, s69\n\t" \
    "s_cmp_lt_u32 s65, 8\n\t" \
    "s_cselect_b32 s69, 96, 0\n\t" \
    "s_add_u32 s79, s79, s69\n\t" \
    "s_cmp_ge_u32 s65, 57\n\t" \
    "s_cselect_b32 s69, 32, 0\n\t" \
    "s_add_u32 s79, s79, s69\n\t" \
    "s_add_u32 s80, s79, s88\n\t" \
    "s_add_u32 s65, s65, 1\n\t" \
    "s_branch vv_go42_%=\n\t" \
    "vv_Gbad48_%=:\n\t" \
    "s_waitcnt vmcnt(0) lgkmcnt(0)\n\t" \
    "s_nop 7\n\t" \
    "v_mov_b32 v72, 0\n\t" \
    "v_mov_b32 v73, 0\n\t" \
    "v_mov_b32 v74, 0\n\t" \
    "v_mov_b32 v75, 0\n\t" \
    "v_mov_b32 v76, 0\n\t" \
    "v_mov_b32 v77, 0\n\t" \
    "v_mov_b32 v78, 0\n\t" \
    "v_mov_b32 v79, 0\n\t" \
    "s_cmp_eq_u32 s68, 0\n\t" \
    "s_cbranch_scc1 vv_F65_%=\n\t" \
    "s_sub_u32 s68, s68, 1\n\t" \
    "s_branch vv_G47_%=\n\t" \
    "vv_F65_%=:\n\t" \
    "v_mov_b32 v140, 6095104\n\t" \
    "v_mov_b32 v141, 1\n\t" \
    "global_store_dword v140, v141, s[36:37] sc1\n\t" \
    "s_waitcnt vmcnt(0)\n\t" \
    "s_branch vv_Gc50_%=\n\t" \
    "vv_Gbad249_%=:\n\t" \
    "global_load_dwordx4 v[44:47], v2, s[46:47] sc1\n\t" \
    "s_waitcnt vmcnt(0)\n\t" \
    "v_max3_u32 v140, v44, v45, v46\n\t" \
    "v_max_u32 v140, v140, v47\n\t" \
    "v_cmp_eq_u32 vcc, -1, v140\n\t" \
    "s_cbranch_vccz vv_Gre252_%=\n\t" \
    "s_cmp_eq_u32 s68, 0\n\t" \
    "s_cbranch_scc1 vv_F66_%=\n\t" \
    "s_sub_u32 s68, s68, 1\n\t" \
    "s_sleep 1\n\t" \
    "s_branch vv_Gbad249_%=\n\t" \
    "vv_F66_%=:\n\t" \
    "v_mov_b32 v140, 6095104\n\t" \
    "v_mov_b32 v141, 2\n\t" \
    "global_store_dword v140, v141, s[36:37] sc1\n\t" \
    "s_waitcnt vmcnt(0)\n\t" \
    "s_branch vv_Gre252_%=\n\t" \
    "vv_Gre252_%=:\n\t" \
    "v_mov_b32 v72, 0\n\t" \
    "v_mov_b32 v73, 0\n\t" \
    "v_mov_b32 v74, 0\n\t" \
    "v_mov_b32 v75, 0\n\t" \
    "v_mov_b32 v76, 0\n\t" \
    "v_mov_b32 v77, 0\n\t" \
    "v_mov_b32 v78, 0\n\t" \
    "v_mov_b32 v79, 0\n\t" \
    "v_smfmac_f32_16x16x64_bf16 v[72:75], v[36:39], a[16:23], v10\n\t" \
    "s_nop 0\n\t" \
    "v_smfmac_f32_16x16x64_bf16 v[76:79], v[36:39], a[88:95], v10\n\t" \
    "v_smfmac_f32_16x16x64_bf16 v[72:75], v[40:43], a[24:31], v10\n\t" \
    "v_smfmac_f32_16x16x64_bf16 v[76:79], v[40:43], a[96:103], v10\n\t" \
    "v_smfmac_f32_16x16x64_bf16 v[72:75], v[44:47], a[64:71], v10\n\t" \
    "v_smfmac_f32_16x16x64_bf16 v[76:79], v[44:47], a[136:143], v10\n\t" \
    "s_branch vv_Gc251_%=\n\t" \
    "vv_end41_%=:\n\t" \
    "s_endpgm\n\t" \
    "vv_loop12_%=:\n\t" \
    "v_mov_b32 v72, 0\n\t" \
    "v_mov_b32 v73, 0\n\t" \
    "v_mov_b32 v74, 0\n\t" \
    "v_mov_b32 v75, 0\n\t" \
    "v_mov_b32 v76, 0\n\t" \
    "v_mov_b32 v77, 0\n\t" \
    "v_mov_b32 v78, 0\n\t" \
    "v_mov_b32 v79, 0\n\t" \
    "v_mov_b32 v92, 0\n\t" \
    "v_mov_b32 v93, 0\n\t" \
    "v_mov_b32 v94, 0\n\t" \
    "v_mov_b32 v95, 0\n\t" \
    "v_mov_b32 v96, 0\n\t" \
    "v_mov_b32 v97, 0\n\t" \
    "v_mov_b32 v98, 0\n\t" \
    "v_mov_b32 v99, 0\n\t" \
    "v_mov_b32 v100, 0\n\t" \
    "v_mov_b32 v101, 0\n\t" \
    "v_mov_b32 v102, 0\n\t" \
    "v_mov_b32 v103, 0\n\t" \
    "v_mov_b32 v104, 0\n\t" \
    "v_mov_b32 v105, 0\n\t" \
    "v_mov_b32 v106, 0\n\t" \
    "v_mov_b32 v107, 0\n\t" \
    "v_mov_b32 v108, 0\n\t" \
    "v_mov_b32 v109, 0\n\t" \
    "v_mov_b32 v110, 0\n\t" \
    "v_mov_b32 v111, 0\n\t" \
    "v_mov_b32 v112, 0\n\t" \
    "v_mov_b32 v113, 0\n\t" \
    "v_mov_b32 v114, 0\n\t" \
    "v_mov_b32 v115, 0\n\t" \
    "v_mov_b32 v116, 0\n\t" \
    "v_mov_b32 v117, 0\n\t" \
    "v_mov_b32 v118, 0\n\t" \
    "v_mov_b32 v119, 0\n\t" \
    "v_mov_b32 v120, 0\n\t" \
    "v_mov_b32 v121, 0\n\t" \
    "v_mov_b32 v122, 0\n\t" \
    "v_mov_b32 v123, 0\n\t" \
    "v_mov_b32 v124, 0\n\t" \
    "v_mov_b32 v125, 0\n\t" \
    "v_mov_b32 v126, 0\n\t" \
    "v_mov_b32 v127, 0\n\t" \
    "vv_go68_%=:\n\t" \
    "s_add_u32 s69, s65, 0\n\t" \
    "s_bitcmp1_b32 s69, 0\n\t" \
    "s_cselect_b32 s70, s63, s62\n\t" \
    "s_bfe_u32 s69, s69, 0x20001\n\t" \
    "s_lshl_b32 s69, s69, 19\n\t" \
    "s_add_u32 s69, s69, s70\n\t" \
    "s_add_u32 s46, s36, s69\n\t" \
    "s_addc_u32 s47, s37, 0\n\t" \
    "s_add_u32 s69, s65, 0\n\t" \
    "s_lshr_b32 s69, s69, 1\n\t" \
    "s_xor_b32 s69, s69, s97\n\t" \
    "s_and_b32 s69, s69, 1\n\t" \
    "s_lshl_b32 s69, s69, 14\n\t" \
    "s_or_b32 s46, s46, s69\n\t" \
    "s_add_u32 s69, s65, 0\n\t" \
    "s_mul_i32 s70, s69, 171\n\t" \
    "s_lshr_b32 s70, s70, 9\n\t" \
    "s_mul_i32 s70, s70, 3\n\t" \
    "s_sub_u32 s69, s69, s70\n\t" \
    "s_mul_i32 s69, s69, 36\n\t" \
    "s_lshl_b32 s69, s69, 14\n\t" \
    "s_add_u32 s69, s69, s64\n\t" \
    "s_add_u32 s69, s69, 4194304\n\t" \
    "s_add_u32 s38, s36, s69\n\t" \
    "s_addc_u32 s39, s37, 0\n\t" \
    "s_mul_i32 s69, s65, 9216\n\t" \
    "s_add_u32 s69, s69, 123731968\n\t" \
    "s_add_u32 s40, s36, s69\n\t" \
    "s_addc_u32 s41, s37, 0\n\t" \
    "s_sub_u32 s70, s79, s74\n\t" \
    "s_max_i32 s70, s70, 0\n\t" \
    "s_min_i32 s70, s70, 16000\n\t" \
    "s_lshl_b32 s70, s70, 1\n\t" \
    "s_mul_hi_u32 s70, s70, s77\n\t" \
    "s_add_u32 s70, s70, s73\n\t" \
    "s_sub_u32 s70, s70, 100\n\t" \
    "s_getpc_b64 s[30:31]\n\t" \
    "vv_Wp70_%=:\n\t" \
    "s_add_u32 s30, s30, vv_We71_%=-vv_Wp70_%=\n\t" \
    "s_addc_u32 s31, s31, 0\n\t" \
    "vv_W69_%=:\n\t" \
    "s_memtime s[56:57]\n\t" \
    "s_waitcnt lgkmcnt(0)\n\t" \
    "s_sub_u32 s69, s70, s56\n\t" \
    "s_cmp_gt_i32 s69, 111\n\t" \
    "s_cbranch_scc1 vv_W69_%=\n\t" \
    "s_max_i32 s69, s69, 0\n\t" \
    "s_lshr_b32 s69, s69, 1\n\t" \
    "s_and_b32 s69, s69, 0x3c\n\t" \
    "s_sub_u32 s30, s30, s69\n\t" \
    "s_subb_u32 s31, s31, 0\n\t" \
    "s_setpc_b64 s[30:31]\n\t" \
    ".p2align 6\n\t" \
    "vv_al72_%=:\n\t" \
    "s_nop 0\n\t" \
    "s_nop 1\n\t" \
    "s_nop 1\n\t" \
    "s_nop 1\n\t" \
    "s_nop 1\n\t" \
    "s_nop 1\n\t" \
    "s_nop 1\n\t" \
    "s_nop 1\n\t" \
    "s_nop 1\n\t" \
    "s_nop 1\n\t" \
    "s_nop 1\n\t" \
    "s_nop 1\n\t" \
    "s_nop 1\n\t" \
    "s_nop 1\n\t" \
    "vv_We71_%=:\n\t" \
    "vv_G73_%=:\n\t" \
    "global_load_dwordx4 v[36:39], v1, s[46:47] sc1\n\t" \
    "s_nop 9\n\t" \
    "global_load_dwordx4 v[40:43], v1, s[46:47] offset:1024 sc1\n\t" \
    "s_nop 9\n\t" \
    "global_load_dwordx4 v[44:47], v2, s[46:47] sc1\n\t" \
    "s_waitcnt vmcnt(2)\n\t" \
    "v_smfmac_f32_16x16x64_bf16 v[72:75], v[36:39], a[32:39], v10\n\t" \
    "ds_write_b128 v0, v[36:39] offset:4096\n\t" \
    "v_smfmac_f32_16x16x64_bf16 v[76:79], v[36:39], a[104:111], v10\n\t" \
    "v_max3_u32 v140, v36, v37, v38\n\t" \
    "s_waitcnt vmcnt(1)\n\t" \
    "ds_write_b128 v0, v[40:43] offset:5120\n\t" \
    "v_smfmac_f32_16x16x64_bf16 v[72:75], v[40:43], a[40:47], v10\n\t" \
    "v_max3_u32 v140, v140, v39, v40\n\t" \
    "v_max3_u32 v140, v140, v41, v42\n\t" \
    "v_max_u32 v140, v140, v43\n\t" \
    "v_cmp_eq_u32 vcc, -1, v140\n\t" \
    "v_smfmac_f32_16x16x64_bf16 v[76:79], v[40:43], a[112:119], v10\n\t" \
    "s_cbranch_vccnz vv_Gbad74_%=\n\t" \
    "vv_Gc76_%=:\n\t" \
    "s_waitcnt lgkmcnt(0)\n\t" \
    "s_barrier\n\t" \
    "ds_read_b128 v[48:51], v0 offset:6144\n\t" \
    "ds_read_b128 v[52:55], v0 offset:7168\n\t" \
    "s_waitcnt vmcnt(0)\n\t" \
    "v_smfmac_f32_16x16x64_bf16 v[72:75], v[44:47], a[64:71], v10\n\t" \
    "ds_read_b128 v[56:59], v0 offset:0\n\t" \
    "ds_read_b128 v[60:63], v0 offset:1024\n\t" \
    "v_max3_u32 v140, v44, v45, v46\n\t" \
    "v_max_u32 v140, v140, v47\n\t" \
    "v_smfmac_f32_16x16x64_bf16 v[76:79], v[44:47], a[136:143], v10\n\t" \
    "ds_read_b128 v[64:67], v0 offset:2048\n\t" \
    "ds_read_b128 v[68:71], v0 offset:3072\n\t" \
    "v_cmp_eq_u32 vcc, -1, v140\n\t" \
    "s_cbranch_vccnz vv_Gbad275_%=\n\t" \
    "vv_Gc277_%=:\n\t" \
    "s_waitcnt lgkmcnt(5)\n\t" \
    "v_smfmac_f32_16x16x64_bf16 v[72:75], v[48:51], a[48:55], v10\n\t" \
    "s_waitcnt lgkmcnt(4)\n\t" \
    "v_smfmac_f32_16x16x64_bf16 v[72:75], v[52:55], a[56:63], v10\n\t" \
    "s_waitcnt lgkmcnt(3)\n\t" \
    "v_smfmac_f32_16x16x64_bf16 v[72:75], v[56:59], a[0:7], v10\n\t" \
    "s_waitcnt lgkmcnt(2)\n\t" \
    "v_smfmac_f32_16x16x64_bf16 v[72:75], v[60:63], a[8:15], v10\n\t" \
    "s_waitcnt lgkmcnt(1)\n\t" \
    "v_smfmac_f32_16x16x64_bf16 v[72:75], v[64:67], a[16:23], v10\n\t" \
    "s_waitcnt lgkmcnt(0)\n\t" \
    "v_smfmac_f32_16x16x64_bf16 v[72:75], v[68:71], a[24:31], v10\n\t" \
    "v_smfmac_f32_16x16x64_bf16 v[76:79], v[48:51], a[120:127], v10\n\t" \
    "s_nop 1\n\t" \
    "s_nop 0\n\t" \
    "v_smfmac_f32_16x16x64_bf16 v[76:79], v[52:55], a[128:135], v10\n\t" \
    "s_nop 1\n\t" \
    "s_nop 0\n\t" \
    "v_smfmac_f32_16x16x64_bf16 v[76:79], v[56:59], a[72:79], v10\n\t" \
    "v_add_f32 v72, v72, v74\n\t" \
    "v_smfmac_f32_16x16x64_bf16 v[76:79], v[60:63], a[80:87], v10\n\t" \
    "v_add_f32 v73, v73, v75\n\t" \
    "v_smfmac_f32_16x16x64_bf16 v[76:79], v[64:67], a[88:95], v10\n\t" \
    "v_cvt_pk_bf16_f32 v80, v72, v73\n\t" \
    "v_smfmac_f32_16x16x64_bf16 v[76:79], v[68:71], a[96:103], v10\n\t" \
    "ds_write_b32 v12, v80 offset:0\n\t" \
    "s_nop 6\n\t" \
    "v_add_f32 v76, v76, v78\n\t" \
    "v_add_f32 v77, v77, v79\n\t" \
    "v_cvt_pk_bf16_f32 v81, v76, v77\n\t" \
    "ds_write_b32 v12, v81 offset:64\n\t" \
    "s_waitcnt lgkmcnt(0)\n\t" \
    "s_barrier\n\t" \
    "ds_read_b64_tr_b16 v[84:85], v13 offset:0\n\t" \
    "ds_read_b64_tr_b16 v[86:87], v13 offset:128\n\t" \
    "ds_read_b64_tr_b16 v[88:89], v13 offset:1024\n\t" \
    "ds_read_b64_tr_b16 v[90:91], v13 offset:1152\n\t" \
    "s_waitcnt lgkmcnt(2)\n\t" \
    "v_smfmac_f32_16x16x64_bf16 v[92:95], v[84:87], a[144:151], v10\n\t" \
    "s_nop 2\n\t" \
    "s_waitcnt lgkmcnt(0)\n\t" \
    "v_smfmac_f32_16x16x64_bf16 v[92:95], v[88:91], a[152:159], v10\n\t" \
    "s_nop 2\n\t" \
    "v_smfmac_f32_16x16x64_bf16 v[96:99], v[84:87], a[160:167], v10\n\t" \
    "s_nop 2\n\t" \
    "v_smfmac_f32_16x16x64_bf16 v[96:99], v[88:91], a[168:175], v10\n\t" \
    "s_nop 2\n\t" \
    "v_smfmac_f32_16x16x64_bf16 v[100:103], v[84:87], a[176:183], v10\n\t" \
    "s_nop 0\n\t" \
    "v_add_f32 v92, v92, v94\n\t" \
    "v_add_f32 v93, v93, v95\n\t" \
    "v_smfmac_f32_16x16x64_bf16 v[100:103], v[88:91], a[184:191], v10\n\t" \
    "s_nop 1\n\t" \
    "v_cvt_pk_bf16_f32 v128, v92, v93\n\t" \
    "v_smfmac_f32_16x16x64_bf16 v[104:107], v[84:87], a[192:199], v10\n\t" \
    "s_nop 0\n\t" \
    "v_add_f32 v96, v96, v98\n\t" \
    "v_add_f32 v97, v97, v99\n\t" \
    "v_smfmac_f32_16x16x64_bf16 v[104:107], v[88:91], a[200:207], v10\n\t" \
    "s_nop 1\n\t" \
    "v_cvt_pk_bf16_f32 v129, v96, v97\n\t" \
    "v_smfmac_f32_16x16x64_bf16 v[124:127], v[84:87], v[240:247], v10\n\t" \
    "s_nop 0\n\t" \
    "v_add_f32 v100, v100, v102\n\t" \
    "v_add_f32 v101, v101, v103\n\t" \
    "v_smfmac_f32_16x16x64_bf16 v[124:127], v[88:91], v[248:255], v10\n\t" \
    "s_nop 1\n\t" \
    "global_load_dword v140, v20, s[46:47] sc1\n\t" \
    "v_cvt_pk_bf16_f32 v130, v100, v101\n\t" \
    "v_smfmac_f32_16x16x64_bf16 v[108:111], v[84:87], a[208:215], v10\n\t" \
    "s_nop 0\n\t" \
    "v_add_f32 v104, v104, v106\n\t" \
    "v_add_f32 v105, v105, v107\n\t" \
    "v_smfmac_f32_16x16x64_bf16 v[108:111], v[88:91], a[216:223], v10\n\t" \
    "s_nop 0\n\t" \
    "v_cvt_pk_bf16_f32 v131, v104, v105\n\t" \
    "v_permlane32_swap_b32 v128, v130\n\t" \
    "v_smfmac_f32_16x16x64_bf16 v[112:115], v[84:87], a[224:231], v10\n\t" \
    "s_nop 0\n\t" \
    "v_permlane32_swap_b32 v129, v131\n\t" \
    "s_nop 1\n\t" \
    "v_permlane16_swap_b32 v128, v129\n\t" \
    "v_smfmac_f32_16x16x64_bf16 v[112:115], v[88:91], a[232:239], v10\n\t" \
    "v_permlane16_swap_b32 v130, v131\n\t" \
    "global_store_dwordx4 v14, v[128:131], s[38:39] sc1\n\t" \
    "v_add_f32 v124, v124, v126\n\t" \
    "v_smfmac_f32_16x16x64_bf16 v[116:119], v[84:87], a[240:247], v10\n\t" \
    "s_nop 0\n\t" \
    "v_add_f32 v125, v125, v127\n\t" \
    "v_cvt_pk_bf16_f32 v136, v124, v125\n\t" \
    "v_smfmac_f32_16x16x64_bf16 v[116:119], v[88:91], a[248:255], v10\n\t" \
    "global_store_dword v16, v136, s[38:39] sc1\n\t" \
    "v_add_f32 v108, v108, v110\n\t" \
    "v_add_f32 v109, v109, v111\n\t" \
    "v_smfmac_f32_16x16x64_bf16 v[120:123], v[84:87], v[224:231], v10\n\t" \
    "s_nop 0\n\t" \
    "v_cvt_pk_bf16_f32 v132, v108, v109\n\t" \
    "v_add_f32 v112, v112, v114\n\t" \
    "v_smfmac_f32_16x16x64_bf16 v[120:123], v[88:91], v[232:239], v10\n\t" \
    "v_add_f32 v113, v113, v115\n\t" \
    "v_cvt_pk_bf16_f32 v133, v112, v113\n\t" \
    "v_add_f32 v116, v116, v118\n\t" \
    "v_add_f32 v117, v117, v119\n\t" \
    "v_cvt_pk_bf16_f32 v134, v116, v117\n\t" \
    "s_nop 2\n\t" \
    "v_pk_add_f32 v[120:121], v[120:121], v[122:123]\n\t" \
    "v_cvt_pk_bf16_f32 v135, v120, v121\n\t" \
    "v_permlane32_swap_b32 v132, v134\n\t" \
    "s_nop 0\n\t" \
    "v_permlane32_swap_b32 v133, v135\n\t" \
    "s_nop 1\n\t" \
    "v_permlane16_swap_b32 v132, v133\n\t" \
    "v_permlane16_swap_b32 v134, v135\n\t" \
    "global_store_dwordx4 v15, v[132:135], s[38:39] sc1\n\t" \
    "s_nop 1\n\t" \
    "global_store_dwordx4 v17, v[128:131], s[40:41] sc1\n\t" \
    "global_store_dwordx4 v17, v[132:135], s[40:41] offset:1024 sc1\n\t" \
    "global_store_dword v18, v136, s[40:41] sc1\n\t" \
    "s_cmp_eq_u32 s65, 63\n\t" \
    "s_cbranch_scc1 vv_end67_%=\n\t" \
    "s_mov_b32 s71, 3\n\t" \
    "vv_E79_%=:\n\t" \
    "s_memtime s[54:55]\n\t" \
    "s_nop 0\n\t" \
    "s_memrealtime s[4:5]\n\t" \
    "s_nop 0\n\t" \
    "s_memrealtime s[6:7]\n\t" \
    "s_nop 0\n\t" \
    "s_memrealtime s[8:9]\n\t" \
    "s_nop 0\n\t" \
    "s_memrealtime s[10:11]\n\t" \
    "s_nop 0\n\t" \
    "s_memrealtime s[12:13]\n\t" \
    "s_nop 0\n\t" \
    "s_memrealtime s[14:15]\n\t" \
    "s_nop 0\n\t" \
    "s_memrealtime s[16:17]\n\t" \
    "s_nop 0\n\t" \
    "s_memrealtime s[18:19]\n\t" \
    "s_nop 0\n\t" \
    "s_memrealtime s[20:21]\n\t" \
    "s_nop 0\n\t" \
    "s_memrealtime s[22:23]\n\t" \
    "s_nop 0\n\t" \
    "s_memrealtime s[24:25]\n\t" \
    "s_nop 0\n\t" \
    "s_memrealtime s[26:27]\n\t" \
    "s_nop 0\n\t" \
    "s_memrealtime s[28:29]\n\t" \
    "s_nop 0\n\t" \
    "s_memtime s[56:57]\n\t" \
    "s_mov_b32 s70, 1\n\t" \
    "s_sub_u32 s71, s71, 1\n\t" \
    "s_cselect_b32 s69, 0, -1\n\t" \
    "s_waitcnt lgkmcnt(0)\n\t" \
    "s_sub_u32 s54, s56, s54\n\t" \
    "s_and_b32 s54, s54, s69\n\t" \
    "s_cmp_gt_u32 s54, 124\n\t" \
    "s_cbranch_scc1 vv_E79_%=\n\t" \
    "s_cmp_eq_u32 s4, s28\n\t" \
    "s_addc_u32 s70, s70, 0\n\t" \
    "s_cmp_eq_u32 s6, s28\n\t" \
    "s_addc_u32 s70, s70, 0\n\t" \
    "s_cmp_eq_u32 s8, s28\n\t" \
    "s_addc_u32 s70, s70, 0\n\t" \
    "s_cmp_eq_u32 s10, s28\n\t" \
    "s_addc_u32 s70, s70, 0\n\t" \
    "s_cmp_eq_u32 s12, s28\n\t" \
    "s_addc_u32 s70, s70, 0\n\t" \
    "s_cmp_eq_u32 s14, s28\n\t" \
    "s_addc_u32 s70, s70, 0\n\t" \
    "s_cmp_eq_u32 s16, s28\n\t" \
    "s_addc_u32 s70, s70, 0\n\t" \
    "s_cmp_eq_u32 s18, s28\n\t" \
    "s_addc_u32 s70, s70, 0\n\t" \
    "s_cmp_eq_u32 s20, s28\n\t" \
    "s_addc_u32 s70, s70, 0\n\t" \
    "s_cmp_eq_u32 s22, s28\n\t" \
    "s_addc_u32 s70, s70, 0\n\t" \
    "s_cmp_eq_u32 s24, s28\n\t" \
    "s_addc_u32 s70, s70, 0\n\t" \
    "s_cmp_eq_u32 s26, s28\n\t" \
    "s_addc_u32 s70, s70, 0\n\t" \
    "s_mov_b32 s75, s73\n\t" \
    "s_mov_b32 s76, s74\n\t" \
    "s_lshl_b32 s70, s70, 3\n\t" \
    "s_sub_u32 s73, s56, s70\n\t" \
    "s_lshl_b32 s74, s28, 4\n\t" \
    "s_sub_u32 s69, s73, s75\n\t" \
    "s_sub_u32 s70, s74, s76\n\t" \
    "s_sub_u32 s71, s70, 1600\n\t" \
    "s_cmp_lt_u32 s71, 400000\n\t" \
    "s_cbranch_scc0 vv_K81_%=\n\t" \
    "v_cvt_f32_u32 v141, s69\n\t" \
    "v_cvt_f32_u32 v142, s70\n\t" \
    "v_rcp_f32 v143, v141\n\t" \
    "v_rcp_f32 v144, v142\n\t" \
    "v_mul_f32 v143, v143, v142\n\t" \
    "v_mul_f32 v144, v144, v141\n\t" \
    "v_mul_f32 v143, 0x4f800000, v143\n\t" \
    "v_mul_f32 v144, 0x4f000000, v144\n\t" \
    "v_cvt_u32_f32 v143, v143\n\t" \
    "v_cvt_u32_f32 v144, v144\n\t" \
    "v_readfirstlane_b32 s69, v143\n\t" \
    "v_readfirstlane_b32 s70, v144\n\t" \
    "s_nop 1\n\t" \
    "s_sub_u32 s69, s69, s78\n\t" \
    "s_ashr_i32 s69, s69, 2\n\t" \
    "s_add_u32 s78, s78, s69\n\t" \
    "s_sub_u32 s70, s70, s77\n\t" \
    "s_ashr_i32 s70, s70, 2\n\t" \
    "s_add_u32 s77, s77, s70\n\t" \
    "vv_K81_%=:\n\t" \
    "s_waitcnt vmcnt(6)\n\t" \
    "v_cmp_eq_u32 vcc, -1, v140\n\t" \
    "s_cbranch_vccz vv_Hok83_%=\n\t" \
    "vv_H82_%=:\n\t" \
    "global_load_dword v140, v20, s[46:47] sc1\n\t" \
    "s_waitcnt vmcnt(0)\n\t" \
    "v_cmp_eq_u32 vcc, -1, v140\n\t" \
    "s_cbranch_vccz vv_Hc84_%=\n\t" \
    "s_cmp_eq_u32 s68, 0\n\t" \
    "s_cbranch_scc1 vv_F85_%=\n\t" \
    "s_sub_u32 s68, s68, 1\n\t" \
    "s_sleep 5\n\t" \
    "s_branch vv_H82_%=\n\t" \
    "vv_F85_%=:\n\t" \
    "v_mov_b32 v141, 6095104\n\t" \
    "v_mov_b32 v142, 6\n\t" \
    "global_store_dword v141, v142, s[36:37] sc1\n\t" \
    "s_waitcnt vmcnt(0)\n\t" \
    "s_branch vv_Hc84_%=\n\t" \
    "vv_Hc84_%=:\n\t" \
    "vv_Hok83_%=:\n\t" \
    "v_subrev_u32 v140, s80, v140\n\t" \
    "v_and_b32 v20, -5, v20\n\t" \
    "s_nop 0\n\t" \
    "v_max_i32_dpp v140, v140, v140 row_ror:1 row_mask:0xf bank_mask:0xf\n\t" \
    "s_nop 1\n\t" \
    "v_max_i32_dpp v140, v140, v140 row_ror:2 row_mask:0xf bank_mask:0xf\n\t" \
    "s_nop 1\n\t" \
    "v_max_i32_dpp v140, v140, v140 row_ror:4 row_mask:0xf bank_mask:0xf\n\t" \
    "s_nop 1\n\t" \
    "v_max_i32_dpp v140, v140, v140 row_ror:8 row_mask:0xf bank_mask:0xf\n\t" \
    "s_nop 0\n\t" \
    "v_readlane_b32 s69, v140, 0\n\t" \
    "v_readlane_b32 s71, v140, 16\n\t" \
    "v_readlane_b32 s70, v140, 32\n\t" \
    "v_readlane_b32 s81, v140, 48\n\t" \
    "s_max_i32 s69, s69, s71\n\t" \
    "s_nop 0\n\t" \
    "s_max_i32 s71, s70, s81\n\t" \
    "s_max_i32 s69, s69, s71\n\t" \
    "s_add_u32 s81, s69, s80\n\t" \
    "v_mov_b32 v72, 0\n\t" \
    "v_mov_b32 v73, 0\n\t" \
    "v_mov_b32 v74, 0\n\t" \
    "v_mov_b32 v75, 0\n\t" \
    "v_mov_b32 v76, 0\n\t" \
    "v_mov_b32 v77, 0\n\t" \
    "v_mov_b32 v78, 0\n\t" \
    "v_mov_b32 v79, 0\n\t" \
    "v_mov_b32 v92, 0\n\t" \
    "v_mov_b32 v93, 0\n\t" \
    "v_mov_b32 v94, 0\n\t" \
    "v_mov_b32 v95, 0\n\t" \
    "v_mov_b32 v96, 0\n\t" \
    "v_mov_b32 v97, 0\n\t" \
    "v_mov_b32 v98, 0\n\t" \
    "v_mov_b32 v99, 0\n\t" \
    "v_mov_b32 v100, 0\n\t" \
    "v_mov_b32 v101, 0\n\t" \
    "v_mov_b32 v102, 0\n\t" \
    "v_mov_b32 v103, 0\n\t" \
    "v_mov_b32 v104, 0\n\t" \
    "v_mov_b32 v105, 0\n\t" \
    "v_mov_b32 v106, 0\n\t" \
    "v_mov_b32 v107, 0\n\t" \
    "v_mov_b32 v108, 0\n\t" \
    "v_mov_b32 v109, 0\n\t" \
    "v_mov_b32 v110, 0\n\t" \
    "v_mov_b32 v111, 0\n\t" \
    "v_mov_b32 v112, 0\n\t" \
    "v_mov_b32 v113, 0\n\t" \
    "v_mov_b32 v114, 0\n\t" \
    "v_mov_b32 v115, 0\n\t" \
    "v_mov_b32 v116, 0\n\t" \
    "v_mov_b32 v117, 0\n\t" \
    "v_mov_b32 v118, 0\n\t" \
    "v_mov_b32 v119, 0\n\t" \
    "v_mov_b32 v120, 0\n\t" \
    "v_mov_b32 v121, 0\n\t" \
    "v_mov_b32 v122, 0\n\t" \
    "v_mov_b32 v123, 0\n\t" \
    "v_mov_b32 v124, 0\n\t" \
    "v_mov_b32 v125, 0\n\t" \
    "v_mov_b32 v126, 0\n\t" \
    "v_mov_b32 v127, 0\n\t" \
    "s_cmp_lg_u32 s65, 0\n\t" \
    "s_cbranch_scc1 vv_Oc88_%=\n\t" \
    "s_add_u32 s79, s81, 240\n\t" \
    "s_sub_u32 s70, s79, s74\n\t" \
    "s_max_i32 s70, s70, 0\n\t" \
    "s_min_i32 s70, s70, 16000\n\t" \
    "s_lshl_b32 s70, s70, 1\n\t" \
    "s_mul_hi_u32 s70, s70, s77\n\t" \
    "s_add_u32 s70, s70, s73\n\t" \
    "vv_W89_%=:\n\t" \
    "s_memtime s[56:57]\n\t" \
    "s_waitcnt lgkmcnt(0)\n\t" \
    "s_sub_u32 s69, s70, s56\n\t" \
    "s_cmp_gt_i32 s69, 24\n\t" \
    "s_cbranch_scc1 vv_W89_%=\n\t" \
    "global_load_dword v140, v35, s[46:47] sc1\n\t" \
    "s_waitcnt vmcnt(0)\n\t" \
    "v_cmp_eq_u32 vcc, -1, v140\n\t" \
    "s_cbranch_vccz vv_Ook87_%=\n\t" \
    "vv_O86_%=:\n\t" \
    "global_load_dword v140, v35, s[46:47] offset:1536 sc1\n\t" \
    "s_waitcnt vmcnt(0)\n\t" \
    "v_cmp_eq_u32 vcc, -1, v140\n\t" \
    "s_cbranch_vccz vv_Ook87_%=\n\t" \
    "s_cmp_eq_u32 s68, 0\n\t" \
    "s_cbranch_scc1 vv_F90_%=\n\t" \
    "s_sub_u32 s68, s68, 1\n\t" \
    "s_sleep 3\n\t" \
    "s_branch vv_O86_%=\n\t" \
    "vv_F90_%=:\n\t" \
    "v_mov_b32 v141, 6095104\n\t" \
    "v_mov_b32 v142, 17\n\t" \
    "global_store_dword v141, v142, s[36:37] sc1\n\t" \
    "s_waitcnt vmcnt(0)\n\t" \
    "s_branch vv_Ook87_%=\n\t" \
    "vv_Ook87_%=:\n\t" \
    "v_subrev_u32 v140, s80, v140\n\t" \
    "s_nop 1\n\t" \
    "v_max_i32_dpp v140, v140, v140 row_ror:1 row_mask:0xf bank_mask:0xf\n\t" \
    "s_nop 1\n\t" \
    "v_max_i32_dpp v140, v140, v140 row_ror:2 row_mask:0xf bank_mask:0xf\n\t" \
    "s_nop 1\n\t" \
    "v_max_i32_dpp v140, v140, v140 row_ror:4 row_mask:0xf bank_mask:0xf\n\t" \
    "s_nop 1\n\t" \
    "v_max_i32_dpp v140, v140, v140 row_ror:8 row_mask:0xf bank_mask:0xf\n\t" \
    "s_nop 0\n\t" \
    "v_readlane_b32 s69, v140, 0\n\t" \
    "v_readlane_b32 s71, v140, 16\n\t" \
    "v_readlane_b32 s70, v140, 32\n\t" \
    "v_readlane_b32 s81, v140, 48\n\t" \
    "s_max_i32 s69, s69, s71\n\t" \
    "s_nop 0\n\t" \
    "s_max_i32 s71, s70, s81\n\t" \
    "s_max_i32 s69, s69, s71\n\t" \
    "s_add_u32 s81, s69, s80\n\t" \
    "vv_Oc88_%=:\n\t" \
    "s_mul_hi_u32 s69, s85, s78\n\t" \
    "s_add_u32 s79, s81, s69\n\t" \
    "s_add_u32 s79, s79, s87\n\t" \
    "s_cmp_ge_u32 s65, 36\n\t" \
    "s_cselect_b32 s69, 28, 0\n\t" \
    "s_sub_u32 s79, s79, s69\n\t" \
    "s_cmp_lt_u32 s65, 8\n\t" \
    "s_cselect_b32 s69, 96, 0\n\t" \
    "s_add_u32 s79, s79, s69\n\t" \
    "s_cmp_ge_u32 s65, 57\n\t" \
    "s_cselect_b32 s69, 32, 0\n\t" \
    "s_add_u32 s79, s79, s69\n\t" \
    "s_add_u32 s80, s79, s88\n\t" \
    "s_add_u32 s65, s65, 1\n\t" \
    "s_branch vv_go68_%=\n\t" \
    "vv_Gbad74_%=:\n\t" \
    "s_waitcnt vmcnt(0) lgkmcnt(0)\n\t" \
    "s_nop 7\n\t" \
    "v_mov_b32 v72, 0\n\t" \
    "v_mov_b32 v73, 0\n\t" \
    "v_mov_b32 v74, 0\n\t" \
    "v_mov_b32 v75, 0\n\t" \
    "v_mov_b32 v76, 0\n\t" \
    "v_mov_b32 v77, 0\n\t" \
    "v_mov_b32 v78, 0\n\t" \
    "v_mov_b32 v79, 0\n\t" \
    "s_cmp_eq_u32 s68, 0\n\t" \
    "s_cbranch_scc1 vv_F91_%=\n\t" \
    "s_sub_u32 s68, s68, 1\n\t" \
    "s_branch vv_G73_%=\n\t" \
    "vv_F91_%=:\n\t" \
    "v_mov_b32 v140, 6095104\n\t" \
    "v_mov_b32 v141, 1\n\t" \
    "global_store_dword v140, v141, s[36:37] sc1\n\t" \
    "s_waitcnt vmcnt(0)\n\t" \
    "s_branch vv_Gc76_%=\n\t" \
    "vv_Gbad275_%=:\n\t" \
    "global_load_dwordx4 v[44:47], v2, s[46:47] sc1\n\t" \
    "s_waitcnt vmcnt(0)\n\t" \
    "v_max3_u32 v140, v44, v45, v46\n\t" \
    "v_max_u32 v140, v140, v47\n\t" \
    "v_cmp_eq_u32 vcc, -1, v140\n\t" \
    "s_cbranch_vccz vv_Gre278_%=\n\t" \
    "s_cmp_eq_u32 s68, 0\n\t" \
    "s_cbranch_scc1 vv_F92_%=\n\t" \
    "s_sub_u32 s68, s68, 1\n\t" \
    "s_sleep 1\n\t" \
    "s_branch vv_Gbad275_%=\n\t" \
    "vv_F92_%=:\n\t" \
    "v_mov_b32 v140, 6095104\n\t" \
    "v_mov_b32 v141, 2\n\t" \
    "global_store_dword v140, v141, s[36:37] sc1\n\t" \
    "s_waitcnt vmcnt(0)\n\t" \
    "s_branch vv_Gre278_%=\n\t" \
    "vv_Gre278_%=:\n\t" \
    "v_mov_b32 v72, 0\n\t" \
    "v_mov_b32 v73, 0\n\t" \
    "v_mov_b32 v74, 0\n\t" \
    "v_mov_b32 v75, 0\n\t" \
    "v_mov_b32 v76, 0\n\t" \
    "v_mov_b32 v77, 0\n\t" \
    "v_mov_b32 v78, 0\n\t" \
    "v_mov_b32 v79, 0\n\t" \
    "v_smfmac_f32_16x16x64_bf16 v[72:75], v[36:39], a[32:39], v10\n\t" \
    "s_nop 0\n\t" \
    "v_smfmac_f32_16x16x64_bf16 v[76:79], v[36:39], a[104:111], v10\n\t" \
    "v_smfmac_f32_16x16x64_bf16 v[72:75], v[40:43], a[40:47], v10\n\t" \
    "v_smfmac_f32_16x16x64_bf16 v[76:79], v[40:43], a[112:119], v10\n\t" \
    "v_smfmac_f32_16x16x64_bf16 v[72:75], v[44:47], a[64:71], v10\n\t" \
    "v_smfmac_f32_16x16x64_bf16 v[76:79], v[44:47], a[136:143], v10\n\t" \
    "s_branch vv_Gc277_%=\n\t" \
    "vv_end67_%=:\n\t" \
    "s_endpgm\n\t" \
    "vv_loop13_%=:\n\t" \
    "v_mov_b32 v72, 0\n\t" \
    "v_mov_b32 v73, 0\n\t" \
    "v_mov_b32 v74, 0\n\t" \
    "v_mov_b32 v75, 0\n\t" \
    "v_mov_b32 v76, 0\n\t" \
    "v_mov_b32 v77, 0\n\t" \
    "v_mov_b32 v78, 0\n\t" \
    "v_mov_b32 v79, 0\n\t" \
    "v_mov_b32 v92, 0\n\t" \
    "v_mov_b32 v93, 0\n\t" \
    "v_mov_b32 v94, 0\n\t" \
    "v_mov_b32 v95, 0\n\t" \
    "v_mov_b32 v96, 0\n\t" \
    "v_mov_b32 v97, 0\n\t" \
    "v_mov_b32 v98, 0\n\t" \
    "v_mov_b32 v99, 0\n\t" \
    "v_mov_b32 v100, 0\n\t" \
    "v_mov_b32 v101, 0\n\t" \
    "v_mov_b32 v102, 0\n\t" \
    "v_mov_b32 v103, 0\n\t" \
    "v_mov_b32 v104, 0\n\t" \
    "v_mov_b32 v105, 0\n\t" \
    "v_mov_b32 v106, 0\n\t" \
    "v_mov_b32 v107, 0\n\t" \
    "v_mov_b32 v108, 0\n\t" \
    "v_mov_b32 v109, 0\n\t" \
    "v_mov_b32 v110, 0\n\t" \
    "v_mov_b32 v111, 0\n\t" \
    "v_mov_b32 v112, 0\n\t" \
    "v_mov_b32 v113, 0\n\t" \
    "v_mov_b32 v114, 0\n\t" \
    "v_mov_b32 v115, 0\n\t" \
    "v_mov_b32 v116, 0\n\t" \
    "v_mov_b32 v117, 0\n\t" \
    "v_mov_b32 v118, 0\n\t" \
    "v_mov_b32 v119, 0\n\t" \
    "v_mov_b32 v120, 0\n\t" \
    "v_mov_b32 v121, 0\n\t" \
    "v_mov_b32 v122, 0\n\t" \
    "v_mov_b32 v123, 0\n\t" \
    "v_mov_b32 v124, 0\n\t" \
    "v_mov_b32 v125, 0\n\t" \
    "v_mov_b32 v126, 0\n\t" \
    "v_mov_b32 v127, 0\n\t" \
    "vv_go94_%=:\n\t" \
    "s_add_u32 s69, s65, 0\n\t" \
    "s_bitcmp1_b32 s69, 0\n\t" \
    "s_cselect_b32 s70, s63, s62\n\t" \
    "s_bfe_u32 s69, s69, 0x20001\n\t" \
    "s_lshl_b32 s69, s69, 19\n\t" \
    "s_add_u32 s69, s69, s70\n\t" \
    "s_add_u32 s46, s36, s69\n\t" \
    "s_addc_u32 s47, s37, 0\n\t" \
    "s_add_u32 s69, s65, 0\n\t" \
    "s_lshr_b32 s69, s69, 1\n\t" \
    "s_xor_b32 s69, s69, s97\n\t" \
    "s_and_b32 s69, s69, 1\n\t" \
    "s_lshl_b32 s69, s69, 14\n\t" \
    "s_or_b32 s46, s46, s69\n\t" \
    "s_add_u32 s69, s65, 0\n\t" \
    "s_mul_i32 s70, s69, 171\n\t" \
    "s_lshr_b32 s70, s70, 9\n\t" \
    "s_mul_i32 s70, s70, 3\n\t" \
    "s_sub_u32 s69, s69, s70\n\t" \
    "s_mul_i32 s69, s69, 36\n\t" \
    "s_lshl_b32 s69, s69, 14\n\t" \
    "s_add_u32 s69, s69, s64\n\t" \
    "s_add_u32 s69, s69, 4194304\n\t" \
    "s_add_u32 s38, s36, s69\n\t" \
    "s_addc_u32 s39, s37, 0\n\t" \
    "s_mul_i32 s69, s65, 9216\n\t" \
    "s_add_u32 s69, s69, 123731968\n\t" \
    "s_add_u32 s40, s36, s69\n\t" \
    "s_addc_u32 s41, s37, 0\n\t" \
    "s_sub_u32 s70, s79, s74\n\t" \
    "s_max_i32 s70, s70, 0\n\t" \
    "s_min_i32 s70, s70, 16000\n\t" \
    "s_lshl_b32 s70, s70, 1\n\t" \
    "s_mul_hi_u32 s70, s70, s77\n\t" \
    "s_add_u32 s70, s70, s73\n\t" \
    "s_sub_u32 s70, s70, 100\n\t" \
    "s_getpc_b64 s[30:31]\n\t" \
    "vv_Wp96_%=:\n\t" \
    "s_add_u32 s30, s30, vv_We97_%=-vv_Wp96_%=\n\t" \
    "s_addc_u32 s31, s31, 0\n\t" \
    "vv_W95_%=:\n\t" \
    "s_memtime s[56:57]\n\t" \
    "s_waitcnt lgkmcnt(0)\n\t" \
    "s_sub_u32 s69, s70, s56\n\t" \
    "s_cmp_gt_i32 s69, 111\n\t" \
    "s_cbranch_scc1 vv_W95_%=\n\t" \
    "s_max_i32 s69, s69, 0\n\t" \
    "s_lshr_b32 s69, s69, 1\n\t" \
    "s_and_b32 s69, s69, 0x3c\n\t" \
    "s_sub_u32 s30, s30, s69\n\t" \
    "s_subb_u32 s31, s31, 0\n\t" \
    "s_setpc_b64 s[30:31]\n\t" \
    ".p2align 6\n\t" \
    "vv_al98_%=:\n\t" \
    "s_nop 0\n\t" \
    "s_nop 1\n\t" \
    "s_nop 1\n\t" \
    "s_nop 1\n\t" \
    "s_nop 1\n\t" \
    "s_nop 1\n\t" \
    "s_nop 1\n\t" \
    "s_nop 1\n\t" \
    "s_nop 1\n\t" \
    "s_nop 1\n\t" \
    "s_nop 1\n\t" \
    "s_nop 1\n\t" \
    "s_nop 1\n\t" \
    "s_nop 1\n\t" \
    "vv_We97_%=:\n\t" \
    "vv_G99_%=:\n\t" \
    "global_load_dwordx4 v[36:39], v1, s[46:47] sc1\n\t" \
    "s_nop 9\n\t" \
    "global_load_dwordx4 v[40:43], v1, s[46:47] offset:1024 sc1\n\t" \
    "s_nop 9\n\t" \
    "global_load_dwordx4 v[44:47], v2, s[46:47] sc1\n\t" \
    "s_waitcnt vmcnt(2)\n\t" \
    "v_smfmac_f32_16x16x64_bf16 v[72:75], v[36:39], a[48:55], v10\n\t" \
    "ds_write_b128 v0, v[36:39] offset:6144\n\t" \
    "v_smfmac_f32_16x16x64_bf16 v[76:79], v[36:39], a[120:127], v10\n\t" \
    "v_max3_u32 v140, v36, v37, v38\n\t" \
    "s_waitcnt vmcnt(1)\n\t" \
    "ds_write_b128 v0, v[40:43] offset:7168\n\t" \
    "v_smfmac_f32_16x16x64_bf16 v[72:75], v[40:43], a[56:63], v10\n\t" \
    "v_max3_u32 v140, v140, v39, v40\n\t" \
    "v_max3_u32 v140, v140, v41, v42\n\t" \
    "v_max_u32 v140, v140, v43\n\t" \
    "v_cmp_eq_u32 vcc, -1, v140\n\t" \
    "v_smfmac_f32_16x16x64_bf16 v[76:79], v[40:43], a[128:135], v10\n\t" \
    "s_cbranch_vccnz vv_Gbad100_%=\n\t" \
    "vv_Gc102_%=:\n\t" \
    "s_waitcnt lgkmcnt(0)\n\t" \
    "s_barrier\n\t" \
    "ds_read_b128 v[48:51], v0 offset:0\n\t" \
    "ds_read_b128 v[52:55], v0 offset:1024\n\t" \
    "s_waitcnt vmcnt(0)\n\t" \
    "v_smfmac_f32_16x16x64_bf16 v[72:75], v[44:47], a[64:71], v10\n\t" \
    "ds_read_b128 v[56:59], v0 offset:2048\n\t" \
    "ds_read_b128 v[60:63], v0 offset:3072\n\t" \
    "v_max3_u32 v140, v44, v45, v46\n\t" \
    "v_max_u32 v140, v140, v47\n\t" \
    "v_smfmac_f32_16x16x64_bf16 v[76:79], v[44:47], a[136:143], v10\n\t" \
    "ds_read_b128 v[64:67], v0 offset:4096\n\t" \
    "ds_read_b128 v[68:71], v0 offset:5120\n\t" \
    "v_cmp_eq_u32 vcc, -1, v140\n\t" \
    "s_cbranch_vccnz vv_Gbad2101_%=\n\t" \
    "vv_Gc2103_%=:\n\t" \
    "s_waitcnt lgkmcnt(5)\n\t" \
    "v_smfmac_f32_16x16x64_bf16 v[72:75], v[48:51], a[0:7], v10\n\t" \
    "s_waitcnt lgkmcnt(4)\n\t" \
    "v_smfmac_f32_16x16x64_bf16 v[72:75], v[52:55], a[8:15], v10\n\t" \
    "s_waitcnt lgkmcnt(3)\n\t" \
    "v_smfmac_f32_16x16x64_bf16 v[72:75], v[56:59], a[16:23], v10\n\t" \
    "s_waitcnt lgkmcnt(2)\n\t" \
    "v_smfmac_f32_16x16x64_bf16 v[72:75], v[60:63], a[24:31], v10\n\t" \
    "s_waitcnt lgkmcnt(1)\n\t" \
    "v_smfmac_f32_16x16x64_bf16 v[72:75], v[64:67], a[32:39], v10\n\t" \
    "s_waitcnt lgkmcnt(0)\n\t" \
    "v_smfmac_f32_16x16x64_bf16 v[72:75], v[68:71], a[40:47], v10\n\t" \
    "v_smfmac_f32_16x16x64_bf16 v[76:79], v[48:51], a[72:79], v10\n\t" \
    "s_nop 1\n\t" \
    "s_nop 0\n\t" \
    "v_smfmac_f32_16x16x64_bf16 v[76:79], v[52:55], a[80:87], v10\n\t" \
    "s_nop 1\n\t" \
    "s_nop 0\n\t" \
    "v_smfmac_f32_16x16x64_bf16 v[76:79], v[56:59], a[88:95], v10\n\t" \
    "v_add_f32 v72, v72, v74\n\t" \
    "v_smfmac_f32_16x16x64_bf16 v[76:79], v[60:63], a[96:103], v10\n\t" \
    "v_add_f32 v73, v73, v75\n\t" \
    "v_smfmac_f32_16x16x64_bf16 v[76:79], v[64:67], a[104:111], v10\n\t" \
    "v_cvt_pk_bf16_f32 v80, v72, v73\n\t" \
    "v_smfmac_f32_16x16x64_bf16 v[76:79], v[68:71], a[112:119], v10\n\t" \
    "ds_write_b32 v12, v80 offset:0\n\t" \
    "s_nop 6\n\t" \
    "v_add_f32 v76, v76, v78\n\t" \
    "v_add_f32 v77, v77, v79\n\t" \
    "v_cvt_pk_bf16_f32 v81, v76, v77\n\t" \
    "ds_write_b32 v12, v81 offset:64\n\t" \
    "s_waitcnt lgkmcnt(0)\n\t" \
    "s_barrier\n\t" \
    "ds_read_b64_tr_b16 v[84:85], v13 offset:0\n\t" \
    "ds_read_b64_tr_b16 v[86:87], v13 offset:128\n\t" \
    "ds_read_b64_tr_b16 v[88:89], v13 offset:1024\n\t" \
    "ds_read_b64_tr_b16 v[90:91], v13 offset:1152\n\t" \
    "s_waitcnt lgkmcnt(2)\n\t" \
    "v_smfmac_f32_16x16x64_bf16 v[92:95], v[84:87], a[144:151], v10\n\t" \
    "s_nop 2\n\t" \
    "s_waitcnt lgkmcnt(0)\n\t" \
    "v_smfmac_f32_16x16x64_bf16 v[92:95], v[88:91], a[152:159], v10\n\t" \
    "s_nop 2\n\t" \
    "v_smfmac_f32_16x16x64_bf16 v[96:99], v[84:87], a[160:167], v10\n\t" \
    "s_nop 2\n\t" \
    "v_smfmac_f32_16x16x64_bf16 v[96:99], v[88:91], a[168:175], v10\n\t" \
    "s_nop 2\n\t" \
    "v_smfmac_f32_16x16x64_bf16 v[100:103], v[84:87], a[176:183], v10\n\t" \
    "s_nop 0\n\t" \
    "v_add_f32 v92, v92, v94\n\t" \
    "v_add_f32 v93, v93, v95\n\t" \
    "v_smfmac_f32_16x16x64_bf16 v[100:103], v[88:91], a[184:191], v10\n\t" \
    "s_nop 1\n\t" \
    "v_cvt_pk_bf16_f32 v128, v92, v93\n\t" \
    "v_smfmac_f32_16x16x64_bf16 v[104:107], v[84:87], a[192:199], v10\n\t" \
    "s_nop 0\n\t" \
    "v_add_f32 v96, v96, v98\n\t" \
    "v_add_f32 v97, v97, v99\n\t" \
    "v_smfmac_f32_16x16x64_bf16 v[104:107], v[88:91], a[200:207], v10\n\t" \
    "s_nop 1\n\t" \
    "v_cvt_pk_bf16_f32 v129, v96, v97\n\t" \
    "v_smfmac_f32_16x16x64_bf16 v[124:127], v[84:87], v[240:247], v10\n\t" \
    "s_nop 0\n\t" \
    "v_add_f32 v100, v100, v102\n\t" \
    "v_add_f32 v101, v101, v103\n\t" \
    "v_smfmac_f32_16x16x64_bf16 v[124:127], v[88:91], v[248:255], v10\n\t" \
    "s_nop 1\n\t" \
    "global_load_dword v140, v20, s[46:47] sc1\n\t" \
    "v_cvt_pk_bf16_f32 v130, v100, v101\n\t" \
    "v_smfmac_f32_16x16x64_bf16 v[108:111], v[84:87], a[208:215], v10\n\t" \
    "s_nop 0\n\t" \
    "v_add_f32 v104, v104, v106\n\t" \
    "v_add_f32 v105, v105, v107\n\t" \
    "v_smfmac_f32_16x16x64_bf16 v[108:111], v[88:91], a[216:223], v10\n\t" \
    "s_nop 0\n\t" \
    "v_cvt_pk_bf16_f32 v131, v104, v105\n\t" \
    "v_permlane32_swap_b32 v128, v130\n\t" \
    "v_smfmac_f32_16x16x64_bf16 v[112:115], v[84:87], a[224:231], v10\n\t" \
    "s_nop 0\n\t" \
    "v_permlane32_swap_b32 v129, v131\n\t" \
    "s_nop 1\n\t" \
    "v_permlane16_swap_b32 v128, v129\n\t" \
    "v_smfmac_f32_16x16x64_bf16 v[112:115], v[88:91], a[232:239], v10\n\t" \
    "v_permlane16_swap_b32 v130, v131\n\t" \
    "global_store_dwordx4 v14, v[128:131], s[38:39] sc1\n\t" \
    "v_add_f32 v124, v124, v126\n\t" \
    "v_smfmac_f32_16x16x64_bf16 v[116:119], v[84:87], a[240:247], v10\n\t" \
    "s_nop 0\n\t" \
    "v_add_f32 v125, v125, v127\n\t" \
    "v_cvt_pk_bf16_f32 v136, v124, v125\n\t" \
    "v_smfmac_f32_16x16x64_bf16 v[116:119], v[88:91], a[248:255], v10\n\t" \
    "global_store_dword v16, v136, s[38:39] sc1\n\t" \
    "v_add_f32 v108, v108, v110\n\t" \
    "v_add_f32 v109, v109, v111\n\t" \
    "v_smfmac_f32_16x16x64_bf16 v[120:123], v[84:87], v[224:231], v10\n\t" \
    "s_nop 0\n\t" \
    "v_cvt_pk_bf16_f32 v132, v108, v109\n\t" \
    "v_add_f32 v112, v112, v114\n\t" \
    "v_smfmac_f32_16x16x64_bf16 v[120:123], v[88:91], v[232:239], v10\n\t" \
    "v_add_f32 v113, v113, v115\n\t" \
    "v_cvt_pk_bf16_f32 v133, v112, v113\n\t" \
    "v_add_f32 v116, v116, v118\n\t" \
    "v_add_f32 v117, v117, v119\n\t" \
    "v_cvt_pk_bf16_f32 v134, v116, v117\n\t" \
    "s_nop 2\n\t" \
    "v_pk_add_f32 v[120:121], v[120:121], v[122:123]\n\t" \
    "v_cvt_pk_bf16_f32 v135, v120, v121\n\t" \
    "v_permlane32_swap_b32 v132, v134\n\t" \
    "s_nop 0\n\t" \
    "v_permlane32_swap_b32 v133, v135\n\t" \
    "s_nop 1\n\t" \
    "v_permlane16_swap_b32 v132, v133\n\t" \
    "v_permlane16_swap_b32 v134, v135\n\t" \
    "global_store_dwordx4 v15, v[132:135], s[38:39] sc1\n\t" \
    "s_nop 1\n\t" \
    "global_store_dwordx4 v17, v[128:131], s[40:41] sc1\n\t" \
    "global_store_dwordx4 v17, v[132:135], s[40:41] offset:1024 sc1\n\t" \
    "global_store_dword v18, v136, s[40:41] sc1\n\t" \
    "s_cmp_eq_u32 s65, 63\n\t" \
    "s_cbranch_scc1 vv_end93_%=\n\t" \
    "s_mov_b32 s71, 3\n\t" \
    "vv_E105_%=:\n\t" \
    "s_memtime s[54:55]\n\t" \
    "s_nop 0\n\t" \
    "s_memrealtime s[4:5]\n\t" \
    "s_nop 0\n\t" \
    "s_memrealtime s[6:7]\n\t" \
    "s_nop 0\n\t" \
    "s_memrealtime s[8:9]\n\t" \
    "s_nop 0\n\t" \
    "s_memrealtime s[10:11]\n\t" \
    "s_nop 0\n\t" \
    "s_memrealtime s[12:13]\n\t" \
    "s_nop 0\n\t" \
    "s_memrealtime s[14:15]\n\t" \
    "s_nop 0\n\t" \
    "s_memrealtime s[16:17]\n\t" \
    "s_nop 0\n\t" \
    "s_memrealtime s[18:19]\n\t" \
    "s_nop 0\n\t" \
    "s_memrealtime s[20:21]\n\t" \
    "s_nop 0\n\t" \
    "s_memrealtime s[22:23]\n\t" \
    "s_nop 0\n\t" \
    "s_memrealtime s[24:25]\n\t" \
    "s_nop 0\n\t" \
    "s_memrealtime s[26:27]\n\t" \
    "s_nop 0\n\t" \
    "s_memrealtime s[28:29]\n\t" \
    "s_nop 0\n\t" \
    "s_memtime s[56:57]\n\t" \
    "s_mov_b32 s70, 1\n\t" \
    "s_sub_u32 s71, s71, 1\n\t" \
    "s_cselect_b32 s69, 0, -1\n\t" \
    "s_waitcnt lgkmcnt(0)\n\t" \
    "s_sub_u32 s54, s56, s54\n\t" \
    "s_and_b32 s54, s54, s69\n\t" \
    "s_cmp_gt_u32 s54, 124\n\t" \
    "s_cbranch_scc1 vv_E105_%=\n\t" \
    "s_cmp_eq_u32 s4, s28\n\t" \
    "s_addc_u32 s70, s70, 0\n\t" \
    "s_cmp_eq_u32 s6, s28\n\t" \
    "s_addc_u32 s70, s70, 0\n\t" \
    "s_cmp_eq_u32 s8, s28\n\t" \
    "s_addc_u32 s70, s70, 0\n\t" \
    "s_cmp_eq_u32 s10, s28\n\t" \
    "s_addc_u32 s70, s70, 0\n\t" \
    "s_cmp_eq_u32 s12, s28\n\t" \
    "s_addc_u32 s70, s70, 0\n\t" \
    "s_cmp_eq_u32 s14, s28\n\t" \
    "s_addc_u32 s70, s70, 0\n\t" \
    "s_cmp_eq_u32 s16, s28\n\t" \
    "s_addc_u32 s70, s70, 0\n\t" \
    "s_cmp_eq_u32 s18, s28\n\t" \
    "s_addc_u32 s70, s70, 0\n\t" \
    "s_cmp_eq_u32 s20, s28\n\t" \
    "s_addc_u32 s70, s70, 0\n\t" \
    "s_cmp_eq_u32 s22, s28\n\t" \
    "s_addc_u32 s70, s70, 0\n\t" \
    "s_cmp_eq_u32 s24, s28\n\t" \
    "s_addc_u32 s70, s70, 0\n\t" \
    "s_cmp_eq_u32 s26, s28\n\t" \
    "s_addc_u32 s70, s70, 0\n\t" \
    "s_mov_b32 s75, s73\n\t" \
    "s_mov_b32 s76, s74\n\t" \
    "s_lshl_b32 s70, s70, 3\n\t" \
    "s_sub_u32 s73, s56, s70\n\t" \
    "s_lshl_b32 s74, s28, 4\n\t" \
    "s_sub_u32 s69, s73, s75\n\t" \
    "s_sub_u32 s70, s74, s76\n\t" \
    "s_sub_u32 s71, s70, 1600\n\t" \
    "s_cmp_lt_u32 s71, 400000\n\t" \
    "s_cbranch_scc0 vv_K107_%=\n\t" \
    "v_cvt_f32_u32 v141, s69\n\t" \
    "v_cvt_f32_u32 v142, s70\n\t" \
    "v_rcp_f32 v143, v141\n\t" \
    "v_rcp_f32 v144, v142\n\t" \
    "v_mul_f32 v143, v143, v142\n\t" \
    "v_mul_f32 v144, v144, v141\n\t" \
    "v_mul_f32 v143, 0x4f800000, v143\n\t" \
    "v_mul_f32 v144, 0x4f000000, v144\n\t" \
    "v_cvt_u32_f32 v143, v143\n\t" \
    "v_cvt_u32_f32 v144, v144\n\t" \
    "v_readfirstlane_b32 s69, v143\n\t" \
    "v_readfirstlane_b32 s70, v144\n\t" \
    "s_nop 1\n\t" \
    "s_sub_u32 s69, s69, s78\n\t" \
    "s_ashr_i32 s69, s69, 2\n\t" \
    "s_add_u32 s78, s78, s69\n\t" \
    "s_sub_u32 s70, s70, s77\n\t" \
    "s_ashr_i32 s70, s70, 2\n\t" \
    "s_add_u32 s77, s77, s70\n\t" \
    "vv_K107_%=:\n\t" \
    "s_waitcnt vmcnt(6)\n\t" \
    "v_cmp_eq_u32 vcc, -1, v140\n\t" \
    "s_cbranch_vccz vv_Hok109_%=\n\t" \
    "vv_H108_%=:\n\t" \
    "global_load_dword v140, v20, s[46:47] sc1\n\t" \
    "s_waitcnt vmcnt(0)\n\t" \
    "v_cmp_eq_u32 vcc, -1, v140\n\t" \
    "s_cbranch_vccz vv_Hc110_%=\n\t" \
    "s_cmp_eq_u32 s68, 0\n\t" \
    "s_cbranch_scc1 vv_F111_%=\n\t" \
    "s_sub_u32 s68, s68, 1\n\t" \
    "s_sleep 5\n\t" \
    "s_branch vv_H108_%=\n\t" \
    "vv_F111_%=:\n\t" \
    "v_mov_b32 v141, 6095104\n\t" \
    "v_mov_b32 v142, 6\n\t" \
    "global_store_dword v141, v142, s[36:37] sc1\n\t" \
    "s_waitcnt vmcnt(0)\n\t" \
    "s_branch vv_Hc110_%=\n\t" \
    "vv_Hc110_%=:\n\t" \
    "vv_Hok109_%=:\n\t" \
    "v_subrev_u32 v140, s80, v140\n\t" \
    "v_and_b32 v20, -5, v20\n\t" \
    "s_nop 0\n\t" \
    "v_max_i32_dpp v140, v140, v140 row_ror:1 row_mask:0xf bank_mask:0xf\n\t" \
    "s_nop 1\n\t" \
    "v_max_i32_dpp v140, v140, v140 row_ror:2 row_mask:0xf bank_mask:0xf\n\t" \
    "s_nop 1\n\t" \
    "v_max_i32_dpp v140, v140, v140 row_ror:4 row_mask:0xf bank_mask:0xf\n\t" \
    "s_nop 1\n\t" \
    "v_max_i32_dpp v140, v140, v140 row_ror:8 row_mask:0xf bank_mask:0xf\n\t" \
    "s_nop 0\n\t" \
    "v_readlane_b32 s69, v140, 0\n\t" \
    "v_readlane_b32 s71, v140, 16\n\t" \
    "v_readlane_b32 s70, v140, 32\n\t" \
    "v_readlane_b32 s81, v140, 48\n\t" \
    "s_max_i32 s69, s69, s71\n\t" \
    "s_nop 0\n\t" \
    "s_max_i32 s71, s70, s81\n\t" \
    "s_max_i32 s69, s69, s71\n\t" \
    "s_add_u32 s81, s69, s80\n\t" \
    "v_mov_b32 v72, 0\n\t" \
    "v_mov_b32 v73, 0\n\t" \
    "v_mov_b32 v74, 0\n\t" \
    "v_mov_b32 v75, 0\n\t" \
    "v_mov_b32 v76, 0\n\t" \
    "v_mov_b32 v77, 0\n\t" \
    "v_mov_b32 v78, 0\n\t" \
    "v_mov_b32 v79, 0\n\t" \
    "v_mov_b32 v92, 0\n\t" \
    "v_mov_b32 v93, 0\n\t" \
    "v_mov_b32 v94, 0\n\t" \
    "v_mov_b32 v95, 0\n\t" \
    "v_mov_b32 v96, 0\n\t" \
    "v_mov_b32 v97, 0\n\t" \
    "v_mov_b32 v98, 0\n\t" \
    "v_mov_b32 v99, 0\n\t" \
    "v_mov_b32 v100, 0\n\t" \
    "v_mov_b32 v101, 0\n\t" \
    "v_mov_b32 v102, 0\n\t" \
    "v_mov_b32 v103, 0\n\t" \
    "v_mov_b32 v104, 0\n\t" \
    "v_mov_b32 v105, 0\n\t" \
    "v_mov_b32 v106, 0\n\t" \
    "v_mov_b32 v107, 0\n\t" \
    "v_mov_b32 v108, 0\n\t" \
    "v_mov_b32 v109, 0\n\t" \
    "v_mov_b32 v110, 0\n\t" \
    "v_mov_b32 v111, 0\n\t" \
    "v_mov_b32 v112, 0\n\t" \
    "v_mov_b32 v113, 0\n\t" \
    "v_mov_b32 v114, 0\n\t" \
    "v_mov_b32 v115, 0\n\t" \
    "v_mov_b32 v116, 0\n\t" \
    "v_mov_b32 v117, 0\n\t" \
    "v_mov_b32 v118, 0\n\t" \
    "v_mov_b32 v119, 0\n\t" \
    "v_mov_b32 v120, 0\n\t" \
    "v_mov_b32 v121, 0\n\t" \
    "v_mov_b32 v122, 0\n\t" \
    "v_mov_b32 v123, 0\n\t" \
    "v_mov_b32 v124, 0\n\t" \
    "v_mov_b32 v125, 0\n\t" \
    "v_mov_b32 v126, 0\n\t" \
    "v_mov_b32 v127, 0\n\t" \
    "s_cmp_lg_u32 s65, 0\n\t" \
    "s_cbranch_scc1 vv_Oc114_%=\n\t" \
    "s_add_u32 s79, s81, 240\n\t" \
    "s_sub_u32 s70, s79, s74\n\t" \
    "s_max_i32 s70, s70, 0\n\t" \
    "s_min_i32 s70, s70, 16000\n\t" \
    "s_lshl_b32 s70, s70, 1\n\t" \
    "s_mul_hi_u32 s70, s70, s77\n\t" \
    "s_add_u32 s70, s70, s73\n\t" \
    "vv_W115_%=:\n\t" \
    "s_memtime s[56:57]\n\t" \
    "s_waitcnt lgkmcnt(0)\n\t" \
    "s_sub_u32 s69, s70, s56\n\t" \
    "s_cmp_gt_i32 s69, 24\n\t" \
    "s_cbranch_scc1 vv_W115_%=\n\t" \
    "global_load_dword v140, v35, s[46:47] sc1\n\t" \
    "s_waitcnt vmcnt(0)\n\t" \
    "v_cmp_eq_u32 vcc, -1, v140\n\t" \
    "s_cbranch_vccz vv_Ook113_%=\n\t" \
    "vv_O112_%=:\n\t" \
    "global_load_dword v140, v35, s[46:47] offset:1536 sc1\n\t" \
    "s_waitcnt vmcnt(0)\n\t" \
    "v_cmp_eq_u32 vcc, -1, v140\n\t" \
    "s_cbranch_vccz vv_Ook113_%=\n\t" \
    "s_cmp_eq_u32 s68, 0\n\t" \
    "s_cbranch_scc1 vv_F116_%=\n\t" \
    "s_sub_u32 s68, s68, 1\n\t" \
    "s_sleep 3\n\t" \
    "s_branch vv_O112_%=\n\t" \
    "vv_F116_%=:\n\t" \
    "v_mov_b32 v141, 6095104\n\t" \
    "v_mov_b32 v142, 17\n\t" \
    "global_store_dword v141, v142, s[36:37] sc1\n\t" \
    "s_waitcnt vmcnt(0)\n\t" \
    "s_branch vv_Ook113_%=\n\t" \
    "vv_Ook113_%=:\n\t" \
    "v_subrev_u32 v140, s80, v140\n\t" \
    "s_nop 1\n\t" \
    "v_max_i32_dpp v140, v140, v140 row_ror:1 row_mask:0xf bank_mask:0xf\n\t" \
    "s_nop 1\n\t" \
    "v_max_i32_dpp v140, v140, v140 row_ror:2 row_mask:0xf bank_mask:0xf\n\t" \
    "s_nop 1\n\t" \
    "v_max_i32_dpp v140, v140, v140 row_ror:4 row_mask:0xf bank_mask:0xf\n\t" \
    "s_nop 1\n\t" \
    "v_max_i32_dpp v140, v140, v140 row_ror:8 row_mask:0xf bank_mask:0xf\n\t" \
    "s_nop 0\n\t" \
    "v_readlane_b32 s69, v140, 0\n\t" \
    "v_readlane_b32 s71, v140, 16\n\t" \
    "v_readlane_b32 s70, v140, 32\n\t" \
    "v_readlane_b32 s81, v140, 48\n\t" \
    "s_max_i32 s69, s69, s71\n\t" \
    "s_nop 0\n\t" \
    "s_max_i32 s71, s70, s81\n\t" \
    "s_max_i32 s69, s69, s71\n\t" \
    "s_add_u32 s81, s69, s80\n\t" \
    "vv_Oc114_%=:\n\t" \
    "s_mul_hi_u32 s69, s85, s78\n\t" \
    "s_add_u32 s79, s81, s69\n\t" \
    "s_add_u32 s79, s79, s87\n\t" \
    "s_cmp_ge_u32 s65, 36\n\t" \
    "s_cselect_b32 s69, 28, 0\n\t" \
    "s_sub_u32 s79, s79, s69\n\t" \
    "s_cmp_lt_u32 s65, 8\n\t" \
    "s_cselect_b32 s69, 96, 0\n\t" \
    "s_add_u32 s79, s79, s69\n\t" \
    "s_cmp_ge_u32 s65, 57\n\t" \
    "s_cselect_b32 s69, 32, 0\n\t" \
    "s_add_u32 s79, s79, s69\n\t" \
    "s_add_u32 s80, s79, s88\n\t" \
    "s_add_u32 s65, s65, 1\n\t" \
    "s_branch vv_go94_%=\n\t" \
    "vv_Gbad100_%=:\n\t" \
    "s_waitcnt vmcnt(0) lgkmcnt(0)\n\t" \
    "s_nop 7\n\t" \
    "v_mov_b32 v72, 0\n\t" \
    "v_mov_b32 v73, 0\n\t" \
    "v_mov_b32 v74, 0\n\t" \
    "v_mov_b32 v75, 0\n\t" \
    "v_mov_b32 v76, 0\n\t" \
    "v_mov_b32 v77, 0\n\t" \
    "v_mov_b32 v78, 0\n\t" \
    "v_mov_b32 v79, 0\n\t" \
    "s_cmp_eq_u32 s68, 0\n\t" \
    "s_cbranch_scc1 vv_F117_%=\n\t" \
    "s_sub_u32 s68, s68, 1\n\t" \
    "s_branch vv_G99_%=\n\t" \
    "vv_F117_%=:\n\t" \
    "v_mov_b32 v140, 6095104\n\t" \
    "v_mov_b32 v141, 1\n\t" \
    "global_store_dword v140, v141, s[36:37] sc1\n\t" \
    "s_waitcnt vmcnt(0)\n\t" \
    "s_branch vv_Gc102_%=\n\t" \
    "vv_Gbad2101_%=:\n\t" \
    "global_load_dwordx4 v[44:47], v2, s[46:47] sc1\n\t" \
    "s_waitcnt vmcnt(0)\n\t" \
    "v_max3_u32 v140, v44, v45, v46\n\t" \
    "v_max_u32 v140, v140, v47\n\t" \
    "v_cmp_eq_u32 vcc, -1, v140\n\t" \
    "s_cbranch_vccz vv_Gre2104_%=\n\t" \
    "s_cmp_eq_u32 s68, 0\n\t" \
    "s_cbranch_scc1 vv_F118_%=\n\t" \
    "s_sub_u32 s68, s68, 1\n\t" \
    "s_sleep 1\n\t" \
    "s_branch vv_Gbad2101_%=\n\t" \
    "vv_F118_%=:\n\t" \
    "v_mov_b32 v140, 6095104\n\t" \
    "v_mov_b32 v141, 2\n\t" \
    "global_store_dword v140, v141, s[36:37] sc1\n\t" \
    "s_waitcnt vmcnt(0)\n\t" \
    "s_branch vv_Gre2104_%=\n\t" \
    "vv_Gre2104_%=:\n\t" \
    "v_mov_b32 v72, 0\n\t" \
    "v_mov_b32 v73, 0\n\t" \
    "v_mov_b32 v74, 0\n\t" \
    "v_mov_b32 v75, 0\n\t" \
    "v_mov_b32 v76, 0\n\t" \
    "v_mov_b32 v77, 0\n\t" \
    "v_mov_b32 v78, 0\n\t" \
    "v_mov_b32 v79, 0\n\t" \
    "v_smfmac_f32_16x16x64_bf16 v[72:75], v[36:39], a[48:55], v10\n\t" \
    "s_nop 0\n\t" \
    "v_smfmac_f32_16x16x64_bf16 v[76:79], v[36:39], a[120:127], v10\n\t" \
    "v_smfmac_f32_16x16x64_bf16 v[72:75], v[40:43], a[56:63], v10\n\t" \
    "v_smfmac_f32_16x16x64_bf16 v[76:79], v[40:43], a[128:135], v10\n\t" \
    "v_smfmac_f32_16x16x64_bf16 v[72:75], v[44:47], a[64:71], v10\n\t" \
    "v_smfmac_f32_16x16x64_bf16 v[76:79], v[44:47], a[136:143], v10\n\t" \
    "s_branch vv_Gc2103_%=\n\t" \
    "vv_end93_%=:\n\t" \
    "s_endpgm\n\t" \
    "gx_begin_%=:\n\t" \
    "v_mbcnt_lo_u32_b32 v223, -1, 0\n\t" \
    "v_mbcnt_hi_u32_b32 v223, -1, v223\n\t" \
    "s_mov_b32 s68, 262144\n\t" \
    "s_mov_b32 s72, 0\n\t" \
    "s_mov_b32 s83, -1\n\t" \
    "s_mov_b32 s65, 0\n\t" \
    "v_and_b32 v180, 3, v223\n\t" \
    "v_lshlrev_b32 v202, 3, v180\n\t" \
    "v_lshrrev_b32 v181, 5, v223\n\t" \
    "v_lshl_add_u32 v202, v181, 12, v202\n\t" \
    "v_bfe_u32 v182, v223, 2, 2\n\t" \
    "s_mov_b32 s70, 288\n\t" \
    "v_mad_u32_u24 v202, v182, s70, v202\n\t" \
    "v_bfe_u32 v183, v223, 4, 1\n\t" \
    "s_mov_b32 s70, 2176\n\t" \
    "v_mad_u32_u24 v202, v183, s70, v202\n\t" \
    "s_lshl_b32 s69, s66, 6\n\t" \
    "v_xor_b32 v202, s69, v202\n\t" \
    "v_xor_b32 v203, 32, v202\n\t" \
    "v_lshlrev_b32 v3, 4, v180\n\t" \
    "v_lshlrev_b32 v2, 5, v181\n\t" \
    "v_xor_b32 v3, v3, v2\n\t" \
    "s_bfe_u32 s71, s66, 0x10000\n\t" \
    "s_mul_i32 s69, s71, 576\n\t" \
    "s_bfe_u32 s71, s66, 0x10001\n\t" \
    "s_mul_i32 s71, s71, 18432\n\t" \
    "s_add_u32 s69, s69, s71\n\t" \
    "v_lshrrev_b32 v184, 2, v223\n\t" \
    "v_mov_b32 v1, s69\n\t" \
    "s_mov_b32 s70, 1152\n\t" \
    "v_mad_u32_u24 v1, v184, s70, v1\n\t" \
    "v_add_u32 v1, v1, v3\n\t" \
    "v_and_b32 v185, 15, v223\n\t" \
    "v_lshlrev_b32 v3, 6, v185\n\t" \
    "v_lshrrev_b32 v186, 4, v223\n\t" \
    "v_lshl_add_u32 v3, v186, 4, v3\n\t" \
    "v_bfe_u32 v187, v223, 3, 1\n\t" \
    "v_lshlrev_b32 v2, 5, v187\n\t" \
    "v_xor_b32 v3, v3, v2\n\t" \
    "v_mov_b32 v2, v3\n\t" \
    "s_mul_i32 s89, s66, 9216\n\t" \
    "s_mov_b32 s90, 0\n\t" \
    "s_waitcnt vmcnt(12) lgkmcnt(0)\n\t" \
    "s_barrier\n\t" \
    "s_add_u32 m0, s82, 65536\n\t" \
    "s_nop 0\n\t" \
    "global_load_lds_dwordx4 v200, s[42:43]\n\t" \
    "v_add_u32 v200, 131072, v200\n\t" \
    "s_add_u32 m0, s82, 69632\n\t" \
    "s_nop 0\n\t" \
    "global_load_lds_dwordx4 v200, s[42:43]\n\t" \
    "v_add_u32 v200, 131072, v200\n\t" \
    "s_add_u32 m0, s82, 73728\n\t" \
    "s_nop 0\n\t" \
    "global_load_lds_dwordx4 v200, s[42:43]\n\t" \
    "v_add_u32 v200, 131072, v200\n\t" \
    "s_add_u32 m0, s82, 77824\n\t" \
    "s_nop 0\n\t" \
    "global_load_lds_dwordx4 v200, s[42:43]\n\t" \
    "v_add_u32 v200, 131072, v200\n\t" \
    "v_add_u32 v202, 73728, v202\n\t" \
    "v_add_u32 v203, 73728, v203\n\t" \
    "ds_read_b64_tr_b16 a[144:145], v202 offset:0\n\t" \
    "ds_read_b64_tr_b16 a[146:147], v202 offset:1024\n\t" \
    "ds_read_b64_tr_b16 a[148:149], v202 offset:8192\n\t" \
    "ds_read_b64_tr_b16 a[150:151], v202 offset:9216\n\t" \
    "ds_read_b64_tr_b16 a[216:217], v203 offset:0\n\t" \
    "ds_read_b64_tr_b16 a[218:219], v203 offset:1024\n\t" \
    "ds_read_b64_tr_b16 a[220:221], v203 offset:8192\n\t" \
    "ds_read_b64_tr_b16 a[222:223], v203 offset:9216\n\t" \
    "v_lshlrev_b32 v0, 4, v223\n\t" \
    "s_mul_i32 s69, s66, 2048\n\t" \
    "s_add_u32 s69, s69, 147456\n\t" \
    "v_lshl_add_u32 v4, v223, 4, s69\n\t" \
    "s_mov_b32 s69, 147456\n\t" \
    "v_lshl_add_u32 v5, v223, 4, s69\n\t" \
    "v_lshlrev_b32 v6, 2, v223\n\t" \
    "v_mov_b32 v7, 0\n\t" \
    "s_mul_i32 s97, s67, 7\n\t" \
    "s_and_b32 s97, s97, 63\n\t" \
    "s_lshl_b32 s97, s97, 13\n\t" \
    "s_mul_i32 s71, s66, 2048\n\t" \
    "s_add_u32 s69, s97, s71\n\t" \
    "s_mul_i32 s71, s67, 524288\n\t" \
    "s_add_u32 s69, s69, s71\n\t" \
    "v_lshl_add_u32 v8, v223, 4, s69\n\t" \
    "s_add_u32 s38, s36, 6291456\n\t" \
    "s_addc_u32 s39, s37, 0\n\t" \
    "s_add_u32 s40, s36, 23068672\n\t" \
    "s_addc_u32 s41, s37, 0\n\t" \
    "s_mul_i32 s69, s67, 128\n\t" \
    "s_add_u32 s69, s69, 6098944\n\t" \
    "v_and_b32 v188, 1, v223\n\t" \
    "v_mov_b32 v10, s69\n\t" \
    "s_mov_b32 s70, 61440\n\t" \
    "v_mad_u32_u24 v10, v188, s70, v10\n\t" \
    "v_lshlrev_b32 v11, 0, v188\n\t" \
    "s_cmp_eq_u32 s66, 0\n\t" \
    "s_cselect_b32 s58, 3, 0\n\t" \
    "s_mov_b32 s59, 0\n\t" \
    "s_waitcnt vmcnt(12) lgkmcnt(0)\n\t" \
    "s_barrier\n\t" \
    "s_add_u32 m0, s82, 0\n\t" \
    "s_nop 0\n\t" \
    "global_load_lds_dwordx4 v200, s[42:43]\n\t" \
    "v_add_u32 v200, 131072, v200\n\t" \
    "s_add_u32 m0, s82, 4096\n\t" \
    "s_nop 0\n\t" \
    "global_load_lds_dwordx4 v200, s[42:43]\n\t" \
    "v_add_u32 v200, 131072, v200\n\t" \
    "s_add_u32 m0, s82, 8192\n\t" \
    "s_nop 0\n\t" \
    "global_load_lds_dwordx4 v200, s[42:43]\n\t" \
    "v_add_u32 v200, 131072, v200\n\t" \
    "s_add_u32 m0, s82, 12288\n\t" \
    "s_nop 0\n\t" \
    "global_load_lds_dwordx4 v200, s[42:43]\n\t" \
    "v_add_u32 v200, 131072, v200\n\t" \
    "ds_read_b64_tr_b16 a[152:153], v202 offset:16384\n\t" \
    "ds_read_b64_tr_b16 a[154:155], v202 offset:17408\n\t" \
    "ds_read_b64_tr_b16 a[156:157], v202 offset:24576\n\t" \
    "ds_read_b64_tr_b16 a[158:159], v202 offset:25600\n\t" \
    "ds_read_b64_tr_b16 a[224:225], v203 offset:16384\n\t" \
    "ds_read_b64_tr_b16 a[226:227], v203 offset:17408\n\t" \
    "ds_read_b64_tr_b16 a[228:229], v203 offset:24576\n\t" \
    "ds_read_b64_tr_b16 a[230:231], v203 offset:25600\n\t" \
    "s_waitcnt vmcnt(12) lgkmcnt(0)\n\t" \
    "s_barrier\n\t" \
    "s_add_u32 m0, s82, 16384\n\t" \
    "s_nop 0\n\t" \
    "global_load_lds_dwordx4 v200, s[42:43]\n\t" \
    "v_add_u32 v200, 131072, v200\n\t" \
    "s_add_u32 m0, s82, 20480\n\t" \
    "s_nop 0\n\t" \
    "global_load_lds_dwordx4 v200, s[42:43]\n\t" \
    "v_add_u32 v200, 131072, v200\n\t" \
    "s_add_u32 m0, s82, 24576\n\t" \
    "s_nop 0\n\t" \
    "global_load_lds_dwordx4 v200, s[42:43]\n\t" \
    "v_add_u32 v200, 131072, v200\n\t" \
    "s_add_u32 m0, s82, 28672\n\t" \
    "s_nop 0\n\t" \
    "global_load_lds_dwordx4 v200, s[42:43]\n\t" \
    "v_add_u32 v200, 131072, v200\n\t" \
    "s_add_u32 m0, s89, 0\n\t" \
    "s_nop 0\n\t" \
    "global_load_lds_dwordx4 v1, s[50:51] offset:0\n\t" \
    "s_add_u32 m0, s89, 960\n\t" \
    "s_nop 0\n\t" \
    "global_load_lds_dwordx4 v1, s[50:51] offset:64\n\t" \
    "s_add_u32 m0, s89, 1920\n\t" \
    "s_nop 0\n\t" \
    "global_load_lds_dwordx4 v1, s[50:51] offset:128\n\t" \
    "s_add_u32 m0, s89, 2880\n\t" \
    "s_nop 0\n\t" \
    "global_load_lds_dwordx4 v1, s[50:51] offset:192\n\t" \
    "s_add_u32 m0, s89, 3840\n\t" \
    "s_nop 0\n\t" \
    "global_load_lds_dwordx4 v1, s[50:51] offset:256\n\t" \
    "s_add_u32 m0, s89, 4800\n\t" \
    "s_nop 0\n\t" \
    "global_load_lds_dwordx4 v1, s[50:51] offset:320\n\t" \
    "s_add_u32 m0, s89, 5760\n\t" \
    "s_nop 0\n\t" \
    "global_load_lds_dwordx4 v1, s[50:51] offset:384\n\t" \
    "s_add_u32 m0, s89, 6720\n\t" \
    "s_nop 0\n\t" \
    "global_load_lds_dwordx4 v1, s[50:51] offset:448\n\t" \
    "s_add_u32 m0, s89, 7680\n\t" \
    "s_nop 0\n\t" \
    "global_load_lds_dwordx4 v1, s[50:51] offset:512\n\t" \
    "ds_read_b64_tr_b16 a[160:161], v202 offset:32768\n\t" \
    "ds_read_b64_tr_b16 a[162:163], v202 offset:33792\n\t" \
    "ds_read_b64_tr_b16 a[164:165], v202 offset:40960\n\t" \
    "ds_read_b64_tr_b16 a[166:167], v202 offset:41984\n\t" \
    "ds_read_b64_tr_b16 a[232:233], v203 offset:32768\n\t" \
    "ds_read_b64_tr_b16 a[234:235], v203 offset:33792\n\t" \
    "ds_read_b64_tr_b16 a[236:237], v203 offset:40960\n\t" \
    "ds_read_b64_tr_b16 a[238:239], v203 offset:41984\n\t" \
    "s_waitcnt vmcnt(21) lgkmcnt(0)\n\t" \
    "s_barrier\n\t" \
    "s_add_u32 m0, s82, 32768\n\t" \
    "s_nop 0\n\t" \
    "global_load_lds_dwordx4 v200, s[42:43]\n\t" \
    "v_add_u32 v200, 131072, v200\n\t" \
    "s_add_u32 m0, s82, 36864\n\t" \
    "s_nop 0\n\t" \
    "global_load_lds_dwordx4 v200, s[42:43]\n\t" \
    "v_add_u32 v200, 131072, v200\n\t" \
    "s_add_u32 m0, s82, 40960\n\t" \
    "s_nop 0\n\t" \
    "global_load_lds_dwordx4 v200, s[42:43]\n\t" \
    "v_add_u32 v200, 131072, v200\n\t" \
    "s_add_u32 m0, s82, 45056\n\t" \
    "s_nop 0\n\t" \
    "global_load_lds_dwordx4 v200, s[42:43]\n\t" \
    "v_add_u32 v200, 131072, v200\n\t" \
    "s_add_u32 s50, s50, 36864\n\t" \
    "s_addc_u32 s51, s51, 0\n\t" \
    "s_add_u32 m0, s89, 36864\n\t" \
    "s_nop 0\n\t" \
    "global_load_lds_dwordx4 v1, s[50:51] offset:0\n\t" \
    "s_add_u32 m0, s89, 37824\n\t" \
    "s_nop 0\n\t" \
    "global_load_lds_dwordx4 v1, s[50:51] offset:64\n\t" \
    "s_add_u32 m0, s89, 38784\n\t" \
    "s_nop 0\n\t" \
    "global_load_lds_dwordx4 v1, s[50:51] offset:128\n\t" \
    "s_add_u32 m0, s89, 39744\n\t" \
    "s_nop 0\n\t" \
    "global_load_lds_dwordx4 v1, s[50:51] offset:192\n\t" \
    "s_add_u32 m0, s89, 40704\n\t" \
    "s_nop 0\n\t" \
    "global_load_lds_dwordx4 v1, s[50:51] offset:256\n\t" \
    "s_add_u32 m0, s89, 41664\n\t" \
    "s_nop 0\n\t" \
    "global_load_lds_dwordx4 v1, s[50:51] offset:320\n\t" \
    "s_add_u32 m0, s89, 42624\n\t" \
    "s_nop 0\n\t" \
    "global_load_lds_dwordx4 v1, s[50:51] offset:384\n\t" \
    "s_add_u32 m0, s89, 43584\n\t" \
    "s_nop 0\n\t" \
    "global_load_lds_dwordx4 v1, s[50:51] offset:448\n\t" \
    "s_add_u32 m0, s89, 44544\n\t" \
    "s_nop 0\n\t" \
    "global_load_lds_dwordx4 v1, s[50:51] offset:512\n\t" \
    "ds_read_b64_tr_b16 a[168:169], v202 offset:49152\n\t" \
    "ds_read_b64_tr_b16 a[170:171], v202 offset:50176\n\t" \
    "ds_read_b64_tr_b16 a[172:173], v202 offset:57344\n\t" \
    "ds_read_b64_tr_b16 a[174:175], v202 offset:58368\n\t" \
    "ds_read_b64_tr_b16 a[240:241], v203 offset:49152\n\t" \
    "ds_read_b64_tr_b16 a[242:243], v203 offset:50176\n\t" \
    "ds_read_b64_tr_b16 a[244:245], v203 offset:57344\n\t" \
    "ds_read_b64_tr_b16 a[246:247], v203 offset:58368\n\t" \
    "s_waitcnt vmcnt(30) lgkmcnt(0)\n\t" \
    "s_barrier\n\t" \
    "s_add_u32 m0, s82, 49152\n\t" \
    "s_nop 0\n\t" \
    "global_load_lds_dwordx4 v200, s[42:43]\n\t" \
    "v_add_u32 v200, 131072, v200\n\t" \
    "s_add_u32 m0, s82, 53248\n\t" \
    "s_nop 0\n\t" \
    "global_load_lds_dwordx4 v200, s[42:43]\n\t" \
    "v_add_u32 v200, 131072, v200\n\t" \
    "s_add_u32 m0, s82, 57344\n\t" \
    "s_nop 0\n\t" \
    "global_load_lds_dwordx4 v200, s[42:43]\n\t" \
    "v_add_u32 v200, 131072, v200\n\t" \
    "s_add_u32 m0, s82, 61440\n\t" \
    "s_nop 0\n\t" \
    "global_load_lds_dwordx4 v200, s[42:43]\n\t" \
    "v_add_u32 v200, 131072, v200\n\t" \
    "v_add_u32 v202, 65536, v202\n\t" \
    "v_add_u32 v203, 65536, v203\n\t" \
    "ds_read_b64_tr_b16 a[176:177], v202 offset:0\n\t" \
    "ds_read_b64_tr_b16 a[178:179], v202 offset:1024\n\t" \
    "ds_read_b64_tr_b16 a[180:181], v202 offset:8192\n\t" \
    "ds_read_b64_tr_b16 a[182:183], v202 offset:9216\n\t" \
    "ds_read_b64_tr_b16 a[248:249], v203 offset:0\n\t" \
    "ds_read_b64_tr_b16 a[250:251], v203 offset:1024\n\t" \
    "ds_read_b64_tr_b16 a[252:253], v203 offset:8192\n\t" \
    "ds_read_b64_tr_b16 a[254:255], v203 offset:9216\n\t" \
    "s_waitcnt vmcnt(30) lgkmcnt(0)\n\t" \
    "s_barrier\n\t" \
    "v_subrev_u32 v200, 4718592, v200\n\t" \
    "s_add_u32 m0, s82, 65536\n\t" \
    "s_nop 0\n\t" \
    "global_load_lds_dwordx4 v200, s[44:45]\n\t" \
    "v_add_u32 v200, 131072, v200\n\t" \
    "s_add_u32 m0, s82, 69632\n\t" \
    "s_nop 0\n\t" \
    "global_load_lds_dwordx4 v200, s[44:45]\n\t" \
    "v_add_u32 v200, 131072, v200\n\t" \
    "s_add_u32 m0, s82, 73728\n\t" \
    "s_nop 0\n\t" \
    "global_load_lds_dwordx4 v200, s[44:45]\n\t" \
    "v_add_u32 v200, 131072, v200\n\t" \
    "s_add_u32 m0, s82, 77824\n\t" \
    "s_nop 0\n\t" \
    "global_load_lds_dwordx4 v200, s[44:45]\n\t" \
    "v_add_u32 v200, 131072, v200\n\t" \
    "v_add_u32 v202, -65536, v202\n\t" \
    "v_add_u32 v203, -65536, v203\n\t" \
    "ds_read_b64_tr_b16 a[184:185], v202 offset:0\n\t" \
    "ds_read_b64_tr_b16 a[186:187], v202 offset:1024\n\t" \
    "ds_read_b64_tr_b16 a[188:189], v202 offset:8192\n\t" \
    "ds_read_b64_tr_b16 a[190:191], v202 offset:9216\n\t" \
    "ds_read_b64_tr_b16 v[224:225], v203 offset:0\n\t" \
    "ds_read_b64_tr_b16 v[226:227], v203 offset:1024\n\t" \
    "ds_read_b64_tr_b16 v[228:229], v203 offset:8192\n\t" \
    "ds_read_b64_tr_b16 v[230:231], v203 offset:9216\n\t" \
    "s_waitcnt vmcnt(30) lgkmcnt(0)\n\t" \
    "s_barrier\n\t" \
    "s_add_u32 m0, s82, 0\n\t" \
    "s_nop 0\n\t" \
    "global_load_lds_dwordx4 v200, s[44:45]\n\t" \
    "v_add_u32 v200, 131072, v200\n\t" \
    "s_add_u32 m0, s82, 4096\n\t" \
    "s_nop 0\n\t" \
    "global_load_lds_dwordx4 v200, s[44:45]\n\t" \
    "v_add_u32 v200, 131072, v200\n\t" \
    "s_add_u32 m0, s82, 8192\n\t" \
    "s_nop 0\n\t" \
    "global_load_lds_dwordx4 v200, s[44:45]\n\t" \
    "v_add_u32 v200, 131072, v200\n\t" \
    "s_add_u32 m0, s82, 12288\n\t" \
    "s_nop 0\n\t" \
    "global_load_lds_dwordx4 v200, s[44:45]\n\t" \
    "v_add_u32 v200, 131072, v200\n\t" \
    "ds_read_b64_tr_b16 a[192:193], v202 offset:16384\n\t" \
    "ds_read_b64_tr_b16 a[194:195], v202 offset:17408\n\t" \
    "ds_read_b64_tr_b16 a[196:197], v202 offset:24576\n\t" \
    "ds_read_b64_tr_b16 a[198:199], v202 offset:25600\n\t" \
    "ds_read_b64_tr_b16 v[232:233], v203 offset:16384\n\t" \
    "ds_read_b64_tr_b16 v[234:235], v203 offset:17408\n\t" \
    "ds_read_b64_tr_b16 v[236:237], v203 offset:24576\n\t" \
    "ds_read_b64_tr_b16 v[238:239], v203 offset:25600\n\t" \
    "s_waitcnt vmcnt(21) lgkmcnt(0)\n\t" \
    "s_barrier\n\t" \
    "s_add_u32 m0, s82, 16384\n\t" \
    "s_nop 0\n\t" \
    "global_load_lds_dwordx4 v200, s[44:45]\n\t" \
    "v_add_u32 v200, 131072, v200\n\t" \
    "s_add_u32 m0, s82, 20480\n\t" \
    "s_nop 0\n\t" \
    "global_load_lds_dwordx4 v200, s[44:45]\n\t" \
    "v_add_u32 v200, 131072, v200\n\t" \
    "s_add_u32 m0, s82, 24576\n\t" \
    "s_nop 0\n\t" \
    "global_load_lds_dwordx4 v200, s[44:45]\n\t" \
    "v_add_u32 v200, 131072, v200\n\t" \
    "s_add_u32 m0, s82, 28672\n\t" \
    "s_nop 0\n\t" \
    "global_load_lds_dwordx4 v200, s[44:45]\n\t" \
    "v_add_u32 v200, 131072, v200\n\t" \
    "ds_read_b64_tr_b16 a[200:201], v202 offset:32768\n\t" \
    "ds_read_b64_tr_b16 a[202:203], v202 offset:33792\n\t" \
    "ds_read_b64_tr_b16 a[204:205], v202 offset:40960\n\t" \
    "ds_read_b64_tr_b16 a[206:207], v202 offset:41984\n\t" \
    "ds_read_b64_tr_b16 v[240:241], v203 offset:32768\n\t" \
    "ds_read_b64_tr_b16 v[242:243], v203 offset:33792\n\t" \
    "ds_read_b64_tr_b16 v[244:245], v203 offset:40960\n\t" \
    "ds_read_b64_tr_b16 v[246:247], v203 offset:41984\n\t" \
    "s_waitcnt vmcnt(12) lgkmcnt(0)\n\t" \
    "s_barrier\n\t" \
    "s_add_u32 m0, s82, 32768\n\t" \
    "s_nop 0\n\t" \
    "global_load_lds_dwordx4 v200, s[44:45]\n\t" \
    "v_add_u32 v200, 131072, v200\n\t" \
    "s_add_u32 m0, s82, 36864\n\t" \
    "s_nop 0\n\t" \
    "global_load_lds_dwordx4 v200, s[44:45]\n\t" \
    "v_add_u32 v200, 131072, v200\n\t" \
    "s_add_u32 m0, s82, 40960\n\t" \
    "s_nop 0\n\t" \
    "global_load_lds_dwordx4 v200, s[44:45]\n\t" \
    "v_add_u32 v200, 131072, v200\n\t" \
    "s_add_u32 m0, s82, 45056\n\t" \
    "s_nop 0\n\t" \
    "global_load_lds_dwordx4 v200, s[44:45]\n\t" \
    "v_add_u32 v200, 131072, v200\n\t" \
    "v_mbcnt_lo_u32_b32 v164, -1, 0\n\t" \
    "v_mbcnt_hi_u32_b32 v164, -1, v164\n\t" \
    "s_cmp_eq_u32 s66, 0\n\t" \
    "s_cselect_b32 s69, 59, 11\n\t" \
    "v_min_u32 v164, s69, v164\n\t" \
    "v_lshlrev_b32 v164, 6, v164\n\t" \
    "v_add_u32 v164, 6258688, v164\n\t" \
    "v_mov_b32 v165, 6266880\n\t" \
    "global_load_dword v165, v165, s[36:37] sc1\n\t" \
    "global_load_dwordx2 v[166:167], v164, s[36:37] sc1\n\t" \
    "ds_read_b64_tr_b16 a[208:209], v202 offset:49152\n\t" \
    "ds_read_b64_tr_b16 a[210:211], v202 offset:50176\n\t" \
    "ds_read_b64_tr_b16 a[212:213], v202 offset:57344\n\t" \
    "ds_read_b64_tr_b16 a[214:215], v202 offset:58368\n\t" \
    "ds_read_b64_tr_b16 v[248:249], v203 offset:49152\n\t" \
    "ds_read_b64_tr_b16 v[250:251], v203 offset:50176\n\t" \
    "ds_read_b64_tr_b16 v[252:253], v203 offset:57344\n\t" \
    "ds_read_b64_tr_b16 v[254:255], v203 offset:58368\n\t" \
    "s_waitcnt lgkmcnt(0)\n\t" \
    "s_waitcnt lgkmcnt(6)\n\t" \
    "v_add_u32 v2, 36864, v3\n\t" \
    "ds_read_b128 v[24:27], v2 offset:0\n\t" \
    "ds_read_b128 v[28:31], v2 offset:18432\n\t" \
    "ds_read_b128 v[32:35], v2 offset:1024\n\t" \
    "ds_read_b128 v[36:39], v2 offset:19456\n\t" \
    "s_waitcnt lgkmcnt(3)\n\t" \
    "v_mfma_f32_16x16x32_bf16 v[88:91], a[144:147], v[24:27], 0\n\t" \
    "ds_read_b128 v[40:43], v2 offset:2048\n\t" \
    "s_waitcnt lgkmcnt(3)\n\t" \
    "v_mfma_f32_16x16x32_bf16 v[92:95], a[144:147], v[28:31], 0\n\t" \
    "ds_read_b128 v[44:47], v2 offset:20480\n\t" \
    "v_mfma_f32_16x16x32_bf16 v[96:99], a[216:219], v[24:27], 0\n\t" \
    "v_mfma_f32_16x16x32_bf16 v[100:103], a[216:219], v[28:31], 0\n\t" \
    "s_waitcnt lgkmcnt(3)\n\t" \
    "v_mfma_f32_16x16x32_bf16 v[88:91], a[148:151], v[32:35], v[88:91]\n\t" \
    "ds_read_b128 v[48:51], v2 offset:3072\n\t" \
    "s_waitcnt lgkmcnt(3)\n\t" \
    "v_mfma_f32_16x16x32_bf16 v[92:95], a[148:151], v[36:39], v[92:95]\n\t" \
    "ds_read_b128 v[52:55], v2 offset:21504\n\t" \
    "v_mfma_f32_16x16x32_bf16 v[96:99], a[220:223], v[32:35], v[96:99]\n\t" \
    "v_mfma_f32_16x16x32_bf16 v[100:103], a[220:223], v[36:39], v[100:103]\n\t" \
    "s_waitcnt lgkmcnt(3)\n\t" \
    "v_mfma_f32_16x16x32_bf16 v[88:91], a[152:155], v[40:43], v[88:91]\n\t" \
    "ds_read_b128 v[56:59], v2 offset:4096\n\t" \
    "s_waitcnt lgkmcnt(3)\n\t" \
    "v_mfma_f32_16x16x32_bf16 v[92:95], a[152:155], v[44:47], v[92:95]\n\t" \
    "ds_read_b128 v[60:63], v2 offset:22528\n\t" \
    "v_mfma_f32_16x16x32_bf16 v[96:99], a[224:227], v[40:43], v[96:99]\n\t" \
    "v_mfma_f32_16x16x32_bf16 v[100:103], a[224:227], v[44:47], v[100:103]\n\t" \
    "s_waitcnt lgkmcnt(3)\n\t" \
    "v_mfma_f32_16x16x32_bf16 v[88:91], a[156:159], v[48:51], v[88:91]\n\t" \
    "ds_read_b128 v[64:67], v2 offset:5120\n\t" \
    "s_waitcnt lgkmcnt(3)\n\t" \
    "v_mfma_f32_16x16x32_bf16 v[92:95], a[156:159], v[52:55], v[92:95]\n\t" \
    "ds_read_b128 v[68:71], v2 offset:23552\n\t" \
    "v_mfma_f32_16x16x32_bf16 v[96:99], a[228:231], v[48:51], v[96:99]\n\t" \
    "v_mfma_f32_16x16x32_bf16 v[100:103], a[228:231], v[52:55], v[100:103]\n\t" \
    "s_waitcnt lgkmcnt(3)\n\t" \
    "v_mfma_f32_16x16x32_bf16 v[88:91], a[160:163], v[56:59], v[88:91]\n\t" \
    "ds_read_b128 v[24:27], v2 offset:6144\n\t" \
    "s_waitcnt lgkmcnt(3)\n\t" \
    "v_mfma_f32_16x16x32_bf16 v[92:95], a[160:163], v[60:63], v[92:95]\n\t" \
    "ds_read_b128 v[28:31], v2 offset:24576\n\t" \
    "v_mfma_f32_16x16x32_bf16 v[96:99], a[232:235], v[56:59], v[96:99]\n\t" \
    "v_mfma_f32_16x16x32_bf16 v[100:103], a[232:235], v[60:63], v[100:103]\n\t" \
    "s_waitcnt lgkmcnt(3)\n\t" \
    "v_mfma_f32_16x16x32_bf16 v[88:91], a[164:167], v[64:67], v[88:91]\n\t" \
    "ds_read_b128 v[32:35], v2 offset:7168\n\t" \
    "s_waitcnt lgkmcnt(3)\n\t" \
    "v_mfma_f32_16x16x32_bf16 v[92:95], a[164:167], v[68:71], v[92:95]\n\t" \
    "ds_read_b128 v[36:39], v2 offset:25600\n\t" \
    "v_mfma_f32_16x16x32_bf16 v[96:99], a[236:239], v[64:67], v[96:99]\n\t" \
    "v_mfma_f32_16x16x32_bf16 v[100:103], a[236:239], v[68:71], v[100:103]\n\t" \
    "s_waitcnt lgkmcnt(3)\n\t" \
    "v_mfma_f32_16x16x32_bf16 v[88:91], a[168:171], v[24:27], v[88:91]\n\t" \
    "ds_read_b128 v[40:43], v2 offset:8192\n\t" \
    "s_waitcnt lgkmcnt(3)\n\t" \
    "v_mfma_f32_16x16x32_bf16 v[92:95], a[168:171], v[28:31], v[92:95]\n\t" \
    "ds_read_b128 v[44:47], v2 offset:26624\n\t" \
    "v_mfma_f32_16x16x32_bf16 v[96:99], a[240:243], v[24:27], v[96:99]\n\t" \
    "v_mfma_f32_16x16x32_bf16 v[100:103], a[240:243], v[28:31], v[100:103]\n\t" \
    "s_waitcnt lgkmcnt(3)\n\t" \
    "v_mfma_f32_16x16x32_bf16 v[88:91], a[172:175], v[32:35], v[88:91]\n\t" \
    "ds_read_b128 v[48:51], v2 offset:9216\n\t" \
    "s_waitcnt lgkmcnt(3)\n\t" \
    "v_mfma_f32_16x16x32_bf16 v[92:95], a[172:175], v[36:39], v[92:95]\n\t" \
    "ds_read_b128 v[52:55], v2 offset:27648\n\t" \
    "v_mfma_f32_16x16x32_bf16 v[96:99], a[244:247], v[32:35], v[96:99]\n\t" \
    "v_mfma_f32_16x16x32_bf16 v[100:103], a[244:247], v[36:39], v[100:103]\n\t" \
    "s_waitcnt lgkmcnt(3)\n\t" \
    "v_mfma_f32_16x16x32_bf16 v[88:91], a[176:179], v[40:43], v[88:91]\n\t" \
    "ds_read_b128 v[56:59], v2 offset:10240\n\t" \
    "s_waitcnt lgkmcnt(3)\n\t" \
    "v_mfma_f32_16x16x32_bf16 v[92:95], a[176:179], v[44:47], v[92:95]\n\t" \
    "ds_read_b128 v[60:63], v2 offset:28672\n\t" \
    "v_mfma_f32_16x16x32_bf16 v[96:99], a[248:251], v[40:43], v[96:99]\n\t" \
    "v_mfma_f32_16x16x32_bf16 v[100:103], a[248:251], v[44:47], v[100:103]\n\t" \
    "s_waitcnt lgkmcnt(3)\n\t" \
    "v_mfma_f32_16x16x32_bf16 v[88:91], a[180:183], v[48:51], v[88:91]\n\t" \
    "ds_read_b128 v[64:67], v2 offset:11264\n\t" \
    "s_waitcnt lgkmcnt(3)\n\t" \
    "v_mfma_f32_16x16x32_bf16 v[92:95], a[180:183], v[52:55], v[92:95]\n\t" \
    "ds_read_b128 v[68:71], v2 offset:29696\n\t" \
    "v_mfma_f32_16x16x32_bf16 v[96:99], a[252:255], v[48:51], v[96:99]\n\t" \
    "v_mfma_f32_16x16x32_bf16 v[100:103], a[252:255], v[52:55], v[100:103]\n\t" \
    "s_waitcnt lgkmcnt(3)\n\t" \
    "v_mfma_f32_16x16x32_bf16 v[88:91], a[184:187], v[56:59], v[88:91]\n\t" \
    "ds_read_b128 v[24:27], v2 offset:12288\n\t" \
    "s_waitcnt lgkmcnt(3)\n\t" \
    "v_mfma_f32_16x16x32_bf16 v[92:95], a[184:187], v[60:63], v[92:95]\n\t" \
    "ds_read_b128 v[28:31], v2 offset:30720\n\t" \
    "v_mfma_f32_16x16x32_bf16 v[96:99], v[224:227], v[56:59], v[96:99]\n\t" \
    "v_mfma_f32_16x16x32_bf16 v[100:103], v[224:227], v[60:63], v[100:103]\n\t" \
    "s_waitcnt lgkmcnt(3)\n\t" \
    "v_mfma_f32_16x16x32_bf16 v[88:91], a[188:191], v[64:67], v[88:91]\n\t" \
    "ds_read_b128 v[32:35], v2 offset:13312\n\t" \
    "s_waitcnt lgkmcnt(3)\n\t" \
    "v_mfma_f32_16x16x32_bf16 v[92:95], a[188:191], v[68:71], v[92:95]\n\t" \
    "ds_read_b128 v[36:39], v2 offset:31744\n\t" \
    "v_mfma_f32_16x16x32_bf16 v[96:99], v[228:231], v[64:67], v[96:99]\n\t" \
    "v_mfma_f32_16x16x32_bf16 v[100:103], v[228:231], v[68:71], v[100:103]\n\t" \
    "s_waitcnt lgkmcnt(3)\n\t" \
    "v_mfma_f32_16x16x32_bf16 v[88:91], a[192:195], v[24:27], v[88:91]\n\t" \
    "ds_read_b128 v[40:43], v2 offset:14336\n\t" \
    "s_waitcnt lgkmcnt(3)\n\t" \
    "v_mfma_f32_16x16x32_bf16 v[92:95], a[192:195], v[28:31], v[92:95]\n\t" \
    "ds_read_b128 v[44:47], v2 offset:32768\n\t" \
    "v_mfma_f32_16x16x32_bf16 v[96:99], v[232:235], v[24:27], v[96:99]\n\t" \
    "v_mfma_f32_16x16x32_bf16 v[100:103], v[232:235], v[28:31], v[100:103]\n\t" \
    "s_waitcnt lgkmcnt(3)\n\t" \
    "v_mfma_f32_16x16x32_bf16 v[88:91], a[196:199], v[32:35], v[88:91]\n\t" \
    "ds_read_b128 v[48:51], v2 offset:15360\n\t" \
    "s_waitcnt lgkmcnt(3)\n\t" \
    "v_mfma_f32_16x16x32_bf16 v[92:95], a[196:199], v[36:39], v[92:95]\n\t" \
    "ds_read_b128 v[52:55], v2 offset:33792\n\t" \
    "v_mfma_f32_16x16x32_bf16 v[96:99], v[236:239], v[32:35], v[96:99]\n\t" \
    "v_mfma_f32_16x16x32_bf16 v[100:103], v[236:239], v[36:39], v[100:103]\n\t" \
    "s_waitcnt lgkmcnt(3)\n\t" \
    "v_mfma_f32_16x16x32_bf16 v[88:91], a[200:203], v[40:43], v[88:91]\n\t" \
    "ds_read_b128 v[56:59], v2 offset:16384\n\t" \
    "s_waitcnt lgkmcnt(3)\n\t" \
    "v_mfma_f32_16x16x32_bf16 v[92:95], a[200:203], v[44:47], v[92:95]\n\t" \
    "ds_read_b128 v[60:63], v2 offset:34816\n\t" \
    "v_mfma_f32_16x16x32_bf16 v[96:99], v[240:243], v[40:43], v[96:99]\n\t" \
    "v_mfma_f32_16x16x32_bf16 v[100:103], v[240:243], v[44:47], v[100:103]\n\t" \
    "s_waitcnt lgkmcnt(3)\n\t" \
    "v_mfma_f32_16x16x32_bf16 v[88:91], a[204:207], v[48:51], v[88:91]\n\t" \
    "ds_read_b128 v[64:67], v2 offset:17408\n\t" \
    "s_waitcnt lgkmcnt(3)\n\t" \
    "v_mfma_f32_16x16x32_bf16 v[92:95], a[204:207], v[52:55], v[92:95]\n\t" \
    "ds_read_b128 v[68:71], v2 offset:35840\n\t" \
    "v_mfma_f32_16x16x32_bf16 v[96:99], v[244:247], v[48:51], v[96:99]\n\t" \
    "v_mfma_f32_16x16x32_bf16 v[100:103], v[244:247], v[52:55], v[100:103]\n\t" \
    "s_waitcnt lgkmcnt(3)\n\t" \
    "v_mfma_f32_16x16x32_bf16 v[88:91], a[208:211], v[56:59], v[88:91]\n\t" \
    "s_waitcnt lgkmcnt(2)\n\t" \
    "v_mfma_f32_16x16x32_bf16 v[92:95], a[208:211], v[60:63], v[92:95]\n\t" \
    "v_mfma_f32_16x16x32_bf16 v[96:99], v[248:251], v[56:59], v[96:99]\n\t" \
    "v_mfma_f32_16x16x32_bf16 v[100:103], v[248:251], v[60:63], v[100:103]\n\t" \
    "s_waitcnt lgkmcnt(1)\n\t" \
    "v_mfma_f32_16x16x32_bf16 v[88:91], a[212:215], v[64:67], v[88:91]\n\t" \
    "s_waitcnt lgkmcnt(0)\n\t" \
    "v_mfma_f32_16x16x32_bf16 v[92:95], a[212:215], v[68:71], v[92:95]\n\t" \
    "v_mfma_f32_16x16x32_bf16 v[96:99], v[252:255], v[64:67], v[96:99]\n\t" \
    "v_mfma_f32_16x16x32_bf16 v[100:103], v[252:255], v[68:71], v[100:103]\n\t" \
    "v_xor_b32 v9, 8192, v8\n\t" \
    "s_nop 2\n\t" \
    "v_cvt_pk_bf16_f32 v112, v88, v89\n\t" \
    "v_cvt_pk_bf16_f32 v113, v90, v91\n\t" \
    "s_nop 0\n\t" \
    "v_cvt_pk_bf16_f32 v114, v96, v97\n\t" \
    "v_cvt_pk_bf16_f32 v115, v98, v99\n\t" \
    "global_store_dwordx4 v9, v[112:115], s[40:41] offset:0 sc1\n\t" \
    "v_cvt_pk_bf16_f32 v116, v92, v93\n\t" \
    "v_cvt_pk_bf16_f32 v117, v94, v95\n\t" \
    "v_cvt_pk_bf16_f32 v118, v100, v101\n\t" \
    "v_cvt_pk_bf16_f32 v119, v102, v103\n\t" \
    "global_store_dwordx4 v9, v[116:119], s[40:41] offset:1024 sc1\n\t" \
    "s_waitcnt lgkmcnt(6)\n\t" \
    "v_add_u32 v2, 0, v3\n\t" \
    "ds_read_b128 v[24:27], v2 offset:0\n\t" \
    "ds_read_b128 v[28:31], v2 offset:18432\n\t" \
    "ds_read_b128 v[32:35], v2 offset:1024\n\t" \
    "ds_read_b128 v[36:39], v2 offset:19456\n\t" \
    "s_waitcnt lgkmcnt(3)\n\t" \
    "v_mfma_f32_16x16x32_bf16 v[88:91], a[144:147], v[24:27], 0\n\t" \
    "ds_read_b128 v[40:43], v2 offset:2048\n\t" \
    "s_waitcnt lgkmcnt(3)\n\t" \
    "v_mfma_f32_16x16x32_bf16 v[92:95], a[144:147], v[28:31], 0\n\t" \
    "ds_read_b128 v[44:47], v2 offset:20480\n\t" \
    "v_mfma_f32_16x16x32_bf16 v[96:99], a[216:219], v[24:27], 0\n\t" \
    "v_mfma_f32_16x16x32_bf16 v[100:103], a[216:219], v[28:31], 0\n\t" \
    "s_waitcnt lgkmcnt(3)\n\t" \
    "v_mfma_f32_16x16x32_bf16 v[88:91], a[148:151], v[32:35], v[88:91]\n\t" \
    "ds_read_b128 v[48:51], v2 offset:3072\n\t" \
    "s_waitcnt lgkmcnt(3)\n\t" \
    "v_mfma_f32_16x16x32_bf16 v[92:95], a[148:151], v[36:39], v[92:95]\n\t" \
    "ds_read_b128 v[52:55], v2 offset:21504\n\t" \
    "v_mfma_f32_16x16x32_bf16 v[96:99], a[220:223], v[32:35], v[96:99]\n\t" \
    "v_mfma_f32_16x16x32_bf16 v[100:103], a[220:223], v[36:39], v[100:103]\n\t" \
    "s_waitcnt lgkmcnt(3)\n\t" \
    "v_mfma_f32_16x16x32_bf16 v[88:91], a[152:155], v[40:43], v[88:91]\n\t" \
    "ds_read_b128 v[56:59], v2 offset:4096\n\t" \
    "s_waitcnt lgkmcnt(3)\n\t" \
    "v_mfma_f32_16x16x32_bf16 v[92:95], a[152:155], v[44:47], v[92:95]\n\t" \
    "ds_read_b128 v[60:63], v2 offset:22528\n\t" \
    "v_mfma_f32_16x16x32_bf16 v[96:99], a[224:227], v[40:43], v[96:99]\n\t" \
    "v_mfma_f32_16x16x32_bf16 v[100:103], a[224:227], v[44:47], v[100:103]\n\t" \
    "s_waitcnt lgkmcnt(3)\n\t" \
    "v_mfma_f32_16x16x32_bf16 v[88:91], a[156:159], v[48:51], v[88:91]\n\t" \
    "ds_read_b128 v[64:67], v2 offset:5120\n\t" \
    "s_waitcnt lgkmcnt(3)\n\t" \
    "v_mfma_f32_16x16x32_bf16 v[92:95], a[156:159], v[52:55], v[92:95]\n\t" \
    "ds_read_b128 v[68:71], v2 offset:23552\n\t" \
    "v_mfma_f32_16x16x32_bf16 v[96:99], a[228:231], v[48:51], v[96:99]\n\t" \
    "v_mfma_f32_16x16x32_bf16 v[100:103], a[228:231], v[52:55], v[100:103]\n\t" \
    "s_waitcnt lgkmcnt(3)\n\t" \
    "v_mfma_f32_16x16x32_bf16 v[88:91], a[160:163], v[56:59], v[88:91]\n\t" \
    "ds_read_b128 v[24:27], v2 offset:6144\n\t" \
    "s_waitcnt lgkmcnt(3)\n\t" \
    "v_mfma_f32_16x16x32_bf16 v[92:95], a[160:163], v[60:63], v[92:95]\n\t" \
    "ds_read_b128 v[28:31], v2 offset:24576\n\t" \
    "v_mfma_f32_16x16x32_bf16 v[96:99], a[232:235], v[56:59], v[96:99]\n\t" \
    "v_mfma_f32_16x16x32_bf16 v[100:103], a[232:235], v[60:63], v[100:103]\n\t" \
    "s_waitcnt lgkmcnt(3)\n\t" \
    "v_mfma_f32_16x16x32_bf16 v[88:91], a[164:167], v[64:67], v[88:91]\n\t" \
    "ds_read_b128 v[32:35], v2 offset:7168\n\t" \
    "s_waitcnt lgkmcnt(3)\n\t" \
    "v_mfma_f32_16x16x32_bf16 v[92:95], a[164:167], v[68:71], v[92:95]\n\t" \
    "ds_read_b128 v[36:39], v2 offset:25600\n\t" \
    "v_mfma_f32_16x16x32_bf16 v[96:99], a[236:239], v[64:67], v[96:99]\n\t" \
    "v_mfma_f32_16x16x32_bf16 v[100:103], a[236:239], v[68:71], v[100:103]\n\t" \
    "s_waitcnt lgkmcnt(3)\n\t" \
    "v_mfma_f32_16x16x32_bf16 v[88:91], a[168:171], v[24:27], v[88:91]\n\t" \
    "ds_read_b128 v[40:43], v2 offset:8192\n\t" \
    "s_waitcnt lgkmcnt(3)\n\t" \
    "v_mfma_f32_16x16x32_bf16 v[92:95], a[168:171], v[28:31], v[92:95]\n\t" \
    "ds_read_b128 v[44:47], v2 offset:26624\n\t" \
    "v_mfma_f32_16x16x32_bf16 v[96:99], a[240:243], v[24:27], v[96:99]\n\t" \
    "v_mfma_f32_16x16x32_bf16 v[100:103], a[240:243], v[28:31], v[100:103]\n\t" \
    "s_waitcnt lgkmcnt(3)\n\t" \
    "v_mfma_f32_16x16x32_bf16 v[88:91], a[172:175], v[32:35], v[88:91]\n\t" \
    "ds_read_b128 v[48:51], v2 offset:9216\n\t" \
    "s_waitcnt lgkmcnt(3)\n\t" \
    "v_mfma_f32_16x16x32_bf16 v[92:95], a[172:175], v[36:39], v[92:95]\n\t" \
    "ds_read_b128 v[52:55], v2 offset:27648\n\t" \
    "v_mfma_f32_16x16x32_bf16 v[96:99], a[244:247], v[32:35], v[96:99]\n\t" \
    "v_mfma_f32_16x16x32_bf16 v[100:103], a[244:247], v[36:39], v[100:103]\n\t" \
    "s_waitcnt lgkmcnt(3)\n\t" \
    "v_mfma_f32_16x16x32_bf16 v[88:91], a[176:179], v[40:43], v[88:91]\n\t" \
    "ds_read_b128 v[56:59], v2 offset:10240\n\t" \
    "s_waitcnt lgkmcnt(3)\n\t" \
    "v_mfma_f32_16x16x32_bf16 v[92:95], a[176:179], v[44:47], v[92:95]\n\t" \
    "ds_read_b128 v[60:63], v2 offset:28672\n\t" \
    "v_mfma_f32_16x16x32_bf16 v[96:99], a[248:251], v[40:43], v[96:99]\n\t" \
    "v_mfma_f32_16x16x32_bf16 v[100:103], a[248:251], v[44:47], v[100:103]\n\t" \
    "s_waitcnt lgkmcnt(3)\n\t" \
    "v_mfma_f32_16x16x32_bf16 v[88:91], a[180:183], v[48:51], v[88:91]\n\t" \
    "ds_read_b128 v[64:67], v2 offset:11264\n\t" \
    "s_waitcnt lgkmcnt(3)\n\t" \
    "v_mfma_f32_16x16x32_bf16 v[92:95], a[180:183], v[52:55], v[92:95]\n\t" \
    "ds_read_b128 v[68:71], v2 offset:29696\n\t" \
    "v_mfma_f32_16x16x32_bf16 v[96:99], a[252:255], v[48:51], v[96:99]\n\t" \
    "v_mfma_f32_16x16x32_bf16 v[100:103], a[252:255], v[52:55], v[100:103]\n\t" \
    "s_waitcnt lgkmcnt(3)\n\t" \
    "v_mfma_f32_16x16x32_bf16 v[88:91], a[184:187], v[56:59], v[88:91]\n\t" \
    "ds_read_b128 v[24:27], v2 offset:12288\n\t" \
    "s_waitcnt lgkmcnt(3)\n\t" \
    "v_mfma_f32_16x16x32_bf16 v[92:95], a[184:187], v[60:63], v[92:95]\n\t" \
    "ds_read_b128 v[28:31], v2 offset:30720\n\t" \
    "v_mfma_f32_16x16x32_bf16 v[96:99], v[224:227], v[56:59], v[96:99]\n\t" \
    "v_mfma_f32_16x16x32_bf16 v[100:103], v[224:227], v[60:63], v[100:103]\n\t" \
    "s_waitcnt lgkmcnt(3)\n\t" \
    "v_mfma_f32_16x16x32_bf16 v[88:91], a[188:191], v[64:67], v[88:91]\n\t" \
    "ds_read_b128 v[32:35], v2 offset:13312\n\t" \
    "s_waitcnt lgkmcnt(3)\n\t" \
    "v_mfma_f32_16x16x32_bf16 v[92:95], a[188:191], v[68:71], v[92:95]\n\t" \
    "ds_read_b128 v[36:39], v2 offset:31744\n\t" \
    "v_mfma_f32_16x16x32_bf16 v[96:99], v[228:231], v[64:67], v[96:99]\n\t" \
    "v_mfma_f32_16x16x32_bf16 v[100:103], v[228:231], v[68:71], v[100:103]\n\t" \
    "s_waitcnt lgkmcnt(3)\n\t" \
    "v_mfma_f32_16x16x32_bf16 v[88:91], a[192:195], v[24:27], v[88:91]\n\t" \
    "ds_read_b128 v[40:43], v2 offset:14336\n\t" \
    "s_waitcnt lgkmcnt(3)\n\t" \
    "v_mfma_f32_16x16x32_bf16 v[92:95], a[192:195], v[28:31], v[92:95]\n\t" \
    "ds_read_b128 v[44:47], v2 offset:32768\n\t" \
    "v_mfma_f32_16x16x32_bf16 v[96:99], v[232:235], v[24:27], v[96:99]\n\t" \
    "v_mfma_f32_16x16x32_bf16 v[100:103], v[232:235], v[28:31], v[100:103]\n\t" \
    "s_waitcnt lgkmcnt(3)\n\t" \
    "v_mfma_f32_16x16x32_bf16 v[88:91], a[196:199], v[32:35], v[88:91]\n\t" \
    "ds_read_b128 v[48:51], v2 offset:15360\n\t" \
    "s_waitcnt lgkmcnt(3)\n\t" \
    "v_mfma_f32_16x16x32_bf16 v[92:95], a[196:199], v[36:39], v[92:95]\n\t" \
    "ds_read_b128 v[52:55], v2 offset:33792\n\t" \
    "v_mfma_f32_16x16x32_bf16 v[96:99], v[236:239], v[32:35], v[96:99]\n\t" \
    "v_mfma_f32_16x16x32_bf16 v[100:103], v[236:239], v[36:39], v[100:103]\n\t" \
    "s_waitcnt lgkmcnt(3)\n\t" \
    "v_mfma_f32_16x16x32_bf16 v[88:91], a[200:203], v[40:43], v[88:91]\n\t" \
    "ds_read_b128 v[56:59], v2 offset:16384\n\t" \
    "s_waitcnt lgkmcnt(3)\n\t" \
    "v_mfma_f32_16x16x32_bf16 v[92:95], a[200:203], v[44:47], v[92:95]\n\t" \
    "ds_read_b128 v[60:63], v2 offset:34816\n\t" \
    "v_mfma_f32_16x16x32_bf16 v[96:99], v[240:243], v[40:43], v[96:99]\n\t" \
    "v_mfma_f32_16x16x32_bf16 v[100:103], v[240:243], v[44:47], v[100:103]\n\t" \
    "s_waitcnt lgkmcnt(3)\n\t" \
    "v_mfma_f32_16x16x32_bf16 v[88:91], a[204:207], v[48:51], v[88:91]\n\t" \
    "ds_read_b128 v[64:67], v2 offset:17408\n\t" \
    "s_waitcnt lgkmcnt(3)\n\t" \
    "v_mfma_f32_16x16x32_bf16 v[92:95], a[204:207], v[52:55], v[92:95]\n\t" \
    "ds_read_b128 v[68:71], v2 offset:35840\n\t" \
    "v_mfma_f32_16x16x32_bf16 v[96:99], v[244:247], v[48:51], v[96:99]\n\t" \
    "v_mfma_f32_16x16x32_bf16 v[100:103], v[244:247], v[52:55], v[100:103]\n\t" \
    "s_waitcnt lgkmcnt(3)\n\t" \
    "v_mfma_f32_16x16x32_bf16 v[88:91], a[208:211], v[56:59], v[88:91]\n\t" \
    "s_waitcnt lgkmcnt(2)\n\t" \
    "v_mfma_f32_16x16x32_bf16 v[92:95], a[208:211], v[60:63], v[92:95]\n\t" \
    "v_mfma_f32_16x16x32_bf16 v[96:99], v[248:251], v[56:59], v[96:99]\n\t" \
    "v_mfma_f32_16x16x32_bf16 v[100:103], v[248:251], v[60:63], v[100:103]\n\t" \
    "s_waitcnt lgkmcnt(1)\n\t" \
    "v_mfma_f32_16x16x32_bf16 v[88:91], a[212:215], v[64:67], v[88:91]\n\t" \
    "s_waitcnt lgkmcnt(0)\n\t" \
    "v_mfma_f32_16x16x32_bf16 v[92:95], a[212:215], v[68:71], v[92:95]\n\t" \
    "v_mfma_f32_16x16x32_bf16 v[96:99], v[252:255], v[64:67], v[96:99]\n\t" \
    "v_mfma_f32_16x16x32_bf16 v[100:103], v[252:255], v[68:71], v[100:103]\n\t" \
    "v_xor_b32 v9, 0, v8\n\t" \
    "s_nop 2\n\t" \
    "v_cvt_pk_bf16_f32 v112, v88, v89\n\t" \
    "v_cvt_pk_bf16_f32 v113, v90, v91\n\t" \
    "s_nop 0\n\t" \
    "v_cvt_pk_bf16_f32 v114, v96, v97\n\t" \
    "v_cvt_pk_bf16_f32 v115, v98, v99\n\t" \
    "global_store_dwordx4 v9, v[112:115], s[40:41] offset:0 sc1\n\t" \
    "v_cvt_pk_bf16_f32 v116, v92, v93\n\t" \
    "v_cvt_pk_bf16_f32 v117, v94, v95\n\t" \
    "v_cvt_pk_bf16_f32 v118, v100, v101\n\t" \
    "v_cvt_pk_bf16_f32 v119, v102, v103\n\t" \
    "global_store_dwordx4 v9, v[116:119], s[40:41] offset:1024 sc1\n\t" \
    "s_waitcnt vmcnt(18) lgkmcnt(0)\n\t" \
    "s_barrier\n\t" \
    "s_add_u32 m0, s82, 49152\n\t" \
    "s_nop 0\n\t" \
    "global_load_lds_dwordx4 v200, s[44:45]\n\t" \
    "v_add_u32 v200, 131072, v200\n\t" \
    "s_add_u32 m0, s82, 53248\n\t" \
    "s_nop 0\n\t" \
    "global_load_lds_dwordx4 v200, s[44:45]\n\t" \
    "v_add_u32 v200, 131072, v200\n\t" \
    "s_add_u32 m0, s82, 57344\n\t" \
    "s_nop 0\n\t" \
    "global_load_lds_dwordx4 v200, s[44:45]\n\t" \
    "v_add_u32 v200, 131072, v200\n\t" \
    "s_add_u32 m0, s82, 61440\n\t" \
    "s_nop 0\n\t" \
    "global_load_lds_dwordx4 v200, s[44:45]\n\t" \
    "v_add_u32 v200, 131072, v200\n\t" \
    "v_add_u32 v202, 65536, v202\n\t" \
    "v_add_u32 v203, 65536, v203\n\t" \
    "ds_read_b64_tr_b16 a[0:1], v202 offset:0\n\t" \
    "ds_read_b64_tr_b16 a[2:3], v202 offset:1024\n\t" \
    "ds_read_b64_tr_b16 a[4:5], v202 offset:8192\n\t" \
    "ds_read_b64_tr_b16 a[6:7], v202 offset:9216\n\t" \
    "ds_read_b64_tr_b16 a[72:73], v203 offset:0\n\t" \
    "ds_read_b64_tr_b16 a[74:75], v203 offset:1024\n\t" \
    "ds_read_b64_tr_b16 a[76:77], v203 offset:8192\n\t" \
    "ds_read_b64_tr_b16 a[78:79], v203 offset:9216\n\t" \
    "s_waitcnt vmcnt(18) lgkmcnt(0)\n\t" \
    "s_barrier\n\t" \
    "s_add_u32 m0, s82, 65536\n\t" \
    "s_nop 0\n\t" \
    "global_load_lds_dwordx4 v200, s[44:45]\n\t" \
    "v_add_u32 v200, 131072, v200\n\t" \
    "s_add_u32 m0, s82, 69632\n\t" \
    "s_nop 0\n\t" \
    "global_load_lds_dwordx4 v200, s[44:45]\n\t" \
    "v_add_u32 v200, 131072, v200\n\t" \
    "s_add_u32 m0, s82, 73728\n\t" \
    "s_nop 0\n\t" \
    "global_load_lds_dwordx4 v200, s[44:45]\n\t" \
    "v_add_u32 v200, 131072, v200\n\t" \
    "s_add_u32 m0, s82, 77824\n\t" \
    "s_nop 0\n\t" \
    "global_load_lds_dwordx4 v200, s[44:45]\n\t" \
    "v_add_u32 v200, 131072, v200\n\t" \
    "v_add_u32 v202, -65536, v202\n\t" \
    "v_add_u32 v203, -65536, v203\n\t" \
    "ds_read_b64_tr_b16 a[8:9], v202 offset:0\n\t" \
    "ds_read_b64_tr_b16 a[10:11], v202 offset:1024\n\t" \
    "ds_read_b64_tr_b16 a[12:13], v202 offset:8192\n\t" \
    "ds_read_b64_tr_b16 a[14:15], v202 offset:9216\n\t" \
    "ds_read_b64_tr_b16 a[80:81], v203 offset:0\n\t" \
    "ds_read_b64_tr_b16 a[82:83], v203 offset:1024\n\t" \
    "ds_read_b64_tr_b16 a[84:85], v203 offset:8192\n\t" \
    "ds_read_b64_tr_b16 a[86:87], v203 offset:9216\n\t" \
    "s_waitcnt vmcnt(18) lgkmcnt(0)\n\t" \
    "s_barrier\n\t" \
    "s_add_u32 m0, s82, 0\n\t" \
    "s_nop 0\n\t" \
    "global_load_lds_dwordx4 v200, s[44:45]\n\t" \
    "v_add_u32 v200, 131072, v200\n\t" \
    "s_add_u32 m0, s82, 4096\n\t" \
    "s_nop 0\n\t" \
    "global_load_lds_dwordx4 v200, s[44:45]\n\t" \
    "v_add_u32 v200, 131072, v200\n\t" \
    "s_add_u32 m0, s82, 8192\n\t" \
    "s_nop 0\n\t" \
    "global_load_lds_dwordx4 v200, s[44:45]\n\t" \
    "v_add_u32 v200, 131072, v200\n\t" \
    "s_add_u32 m0, s82, 12288\n\t" \
    "s_nop 0\n\t" \
    "global_load_lds_dwordx4 v200, s[44:45]\n\t" \
    "v_add_u32 v200, 131072, v200\n\t" \
    "ds_read_b64_tr_b16 a[16:17], v202 offset:16384\n\t" \
    "ds_read_b64_tr_b16 a[18:19], v202 offset:17408\n\t" \
    "ds_read_b64_tr_b16 a[20:21], v202 offset:24576\n\t" \
    "ds_read_b64_tr_b16 a[22:23], v202 offset:25600\n\t" \
    "ds_read_b64_tr_b16 a[88:89], v203 offset:16384\n\t" \
    "ds_read_b64_tr_b16 a[90:91], v203 offset:17408\n\t" \
    "ds_read_b64_tr_b16 a[92:93], v203 offset:24576\n\t" \
    "ds_read_b64_tr_b16 a[94:95], v203 offset:25600\n\t" \
    "s_waitcnt vmcnt(18) lgkmcnt(0)\n\t" \
    "s_barrier\n\t" \
    "s_add_u32 m0, s82, 16384\n\t" \
    "s_nop 0\n\t" \
    "global_load_lds_dwordx4 v200, s[44:45]\n\t" \
    "v_add_u32 v200, 131072, v200\n\t" \
    "s_add_u32 m0, s82, 20480\n\t" \
    "s_nop 0\n\t" \
    "global_load_lds_dwordx4 v200, s[44:45]\n\t" \
    "v_add_u32 v200, 131072, v200\n\t" \
    "s_add_u32 m0, s82, 24576\n\t" \
    "s_nop 0\n\t" \
    "global_load_lds_dwordx4 v200, s[44:45]\n\t" \
    "v_add_u32 v200, 131072, v200\n\t" \
    "s_add_u32 m0, s82, 28672\n\t" \
    "s_nop 0\n\t" \
    "global_load_lds_dwordx4 v200, s[44:45]\n\t" \
    "v_add_u32 v200, 131072, v200\n\t" \
    "ds_read_b64_tr_b16 a[24:25], v202 offset:32768\n\t" \
    "ds_read_b64_tr_b16 a[26:27], v202 offset:33792\n\t" \
    "ds_read_b64_tr_b16 a[28:29], v202 offset:40960\n\t" \
    "ds_read_b64_tr_b16 a[30:31], v202 offset:41984\n\t" \
    "ds_read_b64_tr_b16 a[96:97], v203 offset:32768\n\t" \
    "ds_read_b64_tr_b16 a[98:99], v203 offset:33792\n\t" \
    "ds_read_b64_tr_b16 a[100:101], v203 offset:40960\n\t" \
    "ds_read_b64_tr_b16 a[102:103], v203 offset:41984\n\t" \
    "s_waitcnt vmcnt(12) lgkmcnt(0)\n\t" \
    "s_barrier\n\t" \
    "s_add_u32 m0, s82, 32768\n\t" \
    "s_nop 0\n\t" \
    "global_load_lds_dwordx4 v200, s[44:45]\n\t" \
    "v_add_u32 v200, 131072, v200\n\t" \
    "s_add_u32 m0, s82, 36864\n\t" \
    "s_nop 0\n\t" \
    "global_load_lds_dwordx4 v200, s[44:45]\n\t" \
    "v_add_u32 v200, 131072, v200\n\t" \
    "s_add_u32 m0, s82, 40960\n\t" \
    "s_nop 0\n\t" \
    "global_load_lds_dwordx4 v200, s[44:45]\n\t" \
    "v_add_u32 v200, 131072, v200\n\t" \
    "s_add_u32 m0, s82, 45056\n\t" \
    "s_nop 0\n\t" \
    "global_load_lds_dwordx4 v200, s[44:45]\n\t" \
    "v_add_u32 v200, 131072, v200\n\t" \
    "v_sub_u32 v166, v166, v165\n\t" \
    "v_cmp_ne_u32 vcc, 1, v166\n\t" \
    "s_cbranch_vccz gx_Cok2_%=\n\t" \
    "gx_C1_%=:\n\t" \
    "global_load_dwordx2 v[166:167], v164, s[36:37] sc1\n\t" \
    "s_waitcnt vmcnt(0)\n\t" \
    "v_sub_u32 v166, v166, v165\n\t" \
    "v_cmp_ne_u32 vcc, 1, v166\n\t" \
    "s_cbranch_vccz gx_Cok2_%=\n\t" \
    "s_cmp_eq_u32 s68, 0\n\t" \
    "s_cbranch_scc1 gx_F3_%=\n\t" \
    "s_sub_u32 s68, s68, 1\n\t" \
    "s_sleep 4\n\t" \
    "s_branch gx_C1_%=\n\t" \
    "gx_F3_%=:\n\t" \
    "v_mov_b32 v168, 6095104\n\t" \
    "v_mov_b32 v169, 4\n\t" \
    "global_store_dword v168, v169, s[36:37] sc1\n\t" \
    "s_waitcnt vmcnt(0)\n\t" \
    "s_branch gx_Cok2_%=\n\t" \
    "gx_Cok2_%=:\n\t" \
    "s_cmp_lg_u32 s66, 0\n\t" \
    "s_cbranch_scc1 gx_Ca4_%=\n\t" \
    "v_mov_b32 v164, 6176768\n\t" \
    "s_mov_b64 exec, 1\n\t" \
    "global_atomic_add v164, v166, s[36:37] sc1\n\t" \
    "s_mov_b64 exec, -1\n\t" \
    "gx_Ca4_%=:\n\t" \
    "v_min_u32_dpp v167, v167, v167 row_shl:6 row_mask:0xf bank_mask:0xf\n\t" \
    "s_nop 0\n\t" \
    "v_readlane_b32 s69, v167, 4\n\t" \
    "v_readlane_b32 s70, v167, 5\n\t" \
    "s_nop 1\n\t" \
    "s_cmp_lt_u32 s70, s69\n\t" \
    "s_cselect_b32 s52, 8192, 0\n\t" \
    "s_xor_b32 s52, s52, 8192\n\t" \
    "s_cmp_lg_u32 s66, 0\n\t" \
    "s_cbranch_scc1 gx_SF5_%=\n\t" \
    "s_lshl_b32 s69, s67, 7\n\t" \
    "s_add_u32 s69, s69, 6160384\n\t" \
    "s_add_u32 s69, s69, s52\n\t" \
    "s_add_u32 s70, s65, 1\n\t" \
    "v_mov_b32 v156, s69\n\t" \
    "v_mov_b32 v157, s70\n\t" \
    "s_mov_b64 exec, 1\n\t" \
    "global_store_dword v156, v157, s[36:37] sc1\n\t" \
    "s_mov_b64 exec, -1\n\t" \
    "gx_SF5_%=:\n\t" \
    "v_add_u32 v10, s52, v10\n\t" \
    "ds_read_b64_tr_b16 a[32:33], v202 offset:49152\n\t" \
    "ds_read_b64_tr_b16 a[34:35], v202 offset:50176\n\t" \
    "ds_read_b64_tr_b16 a[36:37], v202 offset:57344\n\t" \
    "ds_read_b64_tr_b16 a[38:39], v202 offset:58368\n\t" \
    "ds_read_b64_tr_b16 a[104:105], v203 offset:49152\n\t" \
    "ds_read_b64_tr_b16 a[106:107], v203 offset:50176\n\t" \
    "ds_read_b64_tr_b16 a[108:109], v203 offset:57344\n\t" \
    "ds_read_b64_tr_b16 a[110:111], v203 offset:58368\n\t" \
    "s_waitcnt vmcnt(12) lgkmcnt(0)\n\t" \
    "s_barrier\n\t" \
    "v_add_u32 v202, 65536, v202\n\t" \
    "v_add_u32 v203, 65536, v203\n\t" \
    "ds_read_b64_tr_b16 a[40:41], v202 offset:0\n\t" \
    "ds_read_b64_tr_b16 a[42:43], v202 offset:1024\n\t" \
    "ds_read_b64_tr_b16 a[44:45], v202 offset:8192\n\t" \
    "ds_read_b64_tr_b16 a[46:47], v202 offset:9216\n\t" \
    "ds_read_b64_tr_b16 a[112:113], v203 offset:0\n\t" \
    "ds_read_b64_tr_b16 a[114:115], v203 offset:1024\n\t" \
    "ds_read_b64_tr_b16 a[116:117], v203 offset:8192\n\t" \
    "ds_read_b64_tr_b16 a[118:119], v203 offset:9216\n\t" \
    "s_waitcnt vmcnt(8) lgkmcnt(0)\n\t" \
    "s_barrier\n\t" \
    "v_add_u32 v202, -65536, v202\n\t" \
    "v_add_u32 v203, -65536, v203\n\t" \
    "ds_read_b64_tr_b16 a[48:49], v202 offset:0\n\t" \
    "ds_read_b64_tr_b16 a[50:51], v202 offset:1024\n\t" \
    "ds_read_b64_tr_b16 a[52:53], v202 offset:8192\n\t" \
    "ds_read_b64_tr_b16 a[54:55], v202 offset:9216\n\t" \
    "ds_read_b64_tr_b16 a[120:121], v203 offset:0\n\t" \
    "ds_read_b64_tr_b16 a[122:123], v203 offset:1024\n\t" \
    "ds_read_b64_tr_b16 a[124:125], v203 offset:8192\n\t" \
    "ds_read_b64_tr_b16 a[126:127], v203 offset:9216\n\t" \
    "s_waitcnt vmcnt(4) lgkmcnt(0)\n\t" \
    "s_barrier\n\t" \
    "ds_read_b64_tr_b16 a[56:57], v202 offset:16384\n\t" \
    "ds_read_b64_tr_b16 a[58:59], v202 offset:17408\n\t" \
    "ds_read_b64_tr_b16 a[60:61], v202 offset:24576\n\t" \
    "ds_read_b64_tr_b16 a[62:63], v202 offset:25600\n\t" \
    "ds_read_b64_tr_b16 a[128:129], v203 offset:16384\n\t" \
    "ds_read_b64_tr_b16 a[130:131], v203 offset:17408\n\t" \
    "ds_read_b64_tr_b16 a[132:133], v203 offset:24576\n\t" \
    "ds_read_b64_tr_b16 a[134:135], v203 offset:25600\n\t" \
    "s_waitcnt vmcnt(0) lgkmcnt(0)\n\t" \
    "s_barrier\n\t" \
    "ds_read_b64_tr_b16 a[64:65], v202 offset:32768\n\t" \
    "ds_read_b64_tr_b16 a[66:67], v202 offset:33792\n\t" \
    "ds_read_b64_tr_b16 a[68:69], v202 offset:40960\n\t" \
    "ds_read_b64_tr_b16 a[70:71], v202 offset:41984\n\t" \
    "ds_read_b64_tr_b16 a[136:137], v203 offset:32768\n\t" \
    "ds_read_b64_tr_b16 a[138:139], v203 offset:33792\n\t" \
    "ds_read_b64_tr_b16 a[140:141], v203 offset:40960\n\t" \
    "ds_read_b64_tr_b16 a[142:143], v203 offset:41984\n\t" \
    "s_waitcnt lgkmcnt(0)\n\t" \
    "s_barrier\n\t" \
    "ds_read_b128 v[24:27], v2 offset:0\n\t" \
    "ds_read_b128 v[28:31], v2 offset:18432\n\t" \
    "ds_read_b128 v[32:35], v2 offset:1024\n\t" \
    "ds_read_b128 v[36:39], v2 offset:19456\n\t" \
    "ds_read_b128 v[40:43], v2 offset:2048\n\t" \
    "ds_read_b128 v[44:47], v2 offset:20480\n\t" \
    "s_cmp_lt_u32 s65, 62\n\t" \
    "s_cselect_b32 s69, 36864, 0\n\t" \
    "s_add_u32 s50, s50, s69\n\t" \
    "s_addc_u32 s51, s51, 0\n\t" \
    "s_add_u32 s90, s90, 36864\n\t" \
    "s_cmp_eq_u32 s90, 147456\n\t" \
    "s_cselect_b32 s90, 0, s90\n\t" \
    "s_add_u32 s91, s90, 36864\n\t" \
    "s_cmp_eq_u32 s91, 147456\n\t" \
    "s_cselect_b32 s91, 0, s91\n\t" \
    "s_add_u32 s91, s91, s89\n\t" \
    "v_add_u32 v17, s90, v3\n\t" \
    "s_branch gx_mid8_%=\n\t" \
    ".p2align 6\n\t" \
    "gx_al9_%=:\n\t" \
    "s_nop 0\n\t" \
    "s_nop 0\n\t" \
    "gx_loop6_%=:\n\t" \
    "s_waitcnt lgkmcnt(5)\n\t" \
    "v_mfma_f32_16x16x32_bf16 v[88:91], a[144:147], v[24:27], 0\n\t" \
    "ds_read_b128 v[48:51], v2 offset:3072\n\t" \
    "s_waitcnt lgkmcnt(5)\n\t" \
    "v_mfma_f32_16x16x32_bf16 v[92:95], a[144:147], v[28:31], 0\n\t" \
    "ds_read_b128 v[52:55], v2 offset:21504\n\t" \
    "v_mfma_f32_16x16x32_bf16 v[96:99], a[216:219], v[24:27], 0\n\t" \
    "s_sub_u32 s70, s65, 1\n\t" \
    "v_max_i32 v157, s70, v11\n\t" \
    "s_cmp_lt_u32 s65, 62\n\t" \
    "v_mfma_f32_16x16x32_bf16 v[100:103], a[216:219], v[28:31], 0\n\t" \
    "s_cselect_b32 s69, 36864, 0\n\t" \
    "s_add_u32 s50, s50, s69\n\t" \
    "s_addc_u32 s51, s51, 0\n\t" \
    "s_waitcnt lgkmcnt(5)\n\t" \
    "v_mfma_f32_16x16x32_bf16 v[88:91], a[148:151], v[32:35], v[88:91]\n\t" \
    "ds_read_b128 v[56:59], v2 offset:4096\n\t" \
    "s_waitcnt lgkmcnt(5)\n\t" \
    "v_mfma_f32_16x16x32_bf16 v[92:95], a[148:151], v[36:39], v[92:95]\n\t" \
    "ds_read_b128 v[60:63], v2 offset:22528\n\t" \
    "v_mfma_f32_16x16x32_bf16 v[96:99], a[220:223], v[32:35], v[96:99]\n\t" \
    "s_add_u32 s90, s90, 36864\n\t" \
    "s_cmp_eq_u32 s90, 147456\n\t" \
    "s_cselect_b32 s90, 0, s90\n\t" \
    "v_mfma_f32_16x16x32_bf16 v[100:103], a[220:223], v[36:39], v[100:103]\n\t" \
    "s_add_u32 s91, s90, 36864\n\t" \
    "s_cmp_eq_u32 s91, 147456\n\t" \
    "s_cselect_b32 s91, 0, s91\n\t" \
    "s_waitcnt lgkmcnt(5)\n\t" \
    "v_mfma_f32_16x16x32_bf16 v[88:91], a[152:155], v[40:43], v[88:91]\n\t" \
    "ds_read_b128 v[64:67], v2 offset:5120\n\t" \
    "s_waitcnt lgkmcnt(5)\n\t" \
    "v_mfma_f32_16x16x32_bf16 v[92:95], a[152:155], v[44:47], v[92:95]\n\t" \
    "ds_read_b128 v[68:71], v2 offset:23552\n\t" \
    "v_mfma_f32_16x16x32_bf16 v[96:99], a[224:227], v[40:43], v[96:99]\n\t" \
    "s_add_u32 s91, s91, s89\n\t" \
    "v_add_u32 v17, s90, v3\n\t" \
    "v_mfma_f32_16x16x32_bf16 v[100:103], a[224:227], v[44:47], v[100:103]\n\t" \
    "s_waitcnt lgkmcnt(5)\n\t" \
    "v_mfma_f32_16x16x32_bf16 v[88:91], a[156:159], v[48:51], v[88:91]\n\t" \
    "ds_read_b128 v[24:27], v2 offset:6144\n\t" \
    "s_waitcnt lgkmcnt(5)\n\t" \
    "v_mfma_f32_16x16x32_bf16 v[92:95], a[156:159], v[52:55], v[92:95]\n\t" \
    "ds_read_b128 v[28:31], v2 offset:24576\n\t" \
    "v_mfma_f32_16x16x32_bf16 v[96:99], a[228:231], v[48:51], v[96:99]\n\t" \
    "s_waitcnt vmcnt(0)\n\t" \
    "s_barrier\n\t" \
    "v_mfma_f32_16x16x32_bf16 v[100:103], a[228:231], v[52:55], v[100:103]\n\t" \
    "s_mov_b64 exec, s[58:59]\n\t" \
    "global_store_dword v10, v157, s[36:37] sc1\n\t" \
    "s_mov_b64 exec, -1\n\t" \
    "s_waitcnt lgkmcnt(5)\n\t" \
    "v_mfma_f32_16x16x32_bf16 v[88:91], a[160:163], v[56:59], v[88:91]\n\t" \
    "ds_read_b128 v[32:35], v2 offset:7168\n\t" \
    "s_waitcnt lgkmcnt(5)\n\t" \
    "v_mfma_f32_16x16x32_bf16 v[92:95], a[160:163], v[60:63], v[92:95]\n\t" \
    "ds_read_b128 v[36:39], v2 offset:25600\n\t" \
    "v_mfma_f32_16x16x32_bf16 v[96:99], a[232:235], v[56:59], v[96:99]\n\t" \
    "v_cvt_pk_bf16_f32 v104, v72, v73\n\t" \
    "v_mfma_f32_16x16x32_bf16 v[100:103], a[232:235], v[60:63], v[100:103]\n\t" \
    "v_cvt_pk_bf16_f32 v105, v74, v75\n\t" \
    "s_waitcnt lgkmcnt(5)\n\t" \
    "v_mfma_f32_16x16x32_bf16 v[88:91], a[164:167], v[64:67], v[88:91]\n\t" \
    "ds_read_b128 v[40:43], v2 offset:8192\n\t" \
    "s_waitcnt lgkmcnt(5)\n\t" \
    "v_mfma_f32_16x16x32_bf16 v[92:95], a[164:167], v[68:71], v[92:95]\n\t" \
    "ds_read_b128 v[44:47], v2 offset:26624\n\t" \
    "v_mfma_f32_16x16x32_bf16 v[96:99], a[236:239], v[64:67], v[96:99]\n\t" \
    "v_cvt_pk_bf16_f32 v106, v80, v81\n\t" \
    "v_mfma_f32_16x16x32_bf16 v[100:103], a[236:239], v[68:71], v[100:103]\n\t" \
    "v_cvt_pk_bf16_f32 v107, v82, v83\n\t" \
    "s_waitcnt lgkmcnt(5)\n\t" \
    "v_mfma_f32_16x16x32_bf16 v[88:91], a[168:171], v[24:27], v[88:91]\n\t" \
    "ds_read_b128 v[48:51], v2 offset:9216\n\t" \
    "s_waitcnt lgkmcnt(5)\n\t" \
    "v_mfma_f32_16x16x32_bf16 v[92:95], a[168:171], v[28:31], v[92:95]\n\t" \
    "ds_read_b128 v[52:55], v2 offset:27648\n\t" \
    "v_mfma_f32_16x16x32_bf16 v[96:99], a[240:243], v[24:27], v[96:99]\n\t" \
    "global_store_dwordx4 v9, v[104:107], s[38:39] offset:0 sc1\n\t" \
    "v_mfma_f32_16x16x32_bf16 v[100:103], a[240:243], v[28:31], v[100:103]\n\t" \
    "v_cvt_pk_bf16_f32 v108, v76, v77\n\t" \
    "s_waitcnt lgkmcnt(5)\n\t" \
    "v_mfma_f32_16x16x32_bf16 v[88:91], a[172:175], v[32:35], v[88:91]\n\t" \
    "ds_read_b128 v[56:59], v2 offset:10240\n\t" \
    "s_waitcnt lgkmcnt(5)\n\t" \
    "v_mfma_f32_16x16x32_bf16 v[92:95], a[172:175], v[36:39], v[92:95]\n\t" \
    "ds_read_b128 v[60:63], v2 offset:28672\n\t" \
    "v_mfma_f32_16x16x32_bf16 v[96:99], a[244:247], v[32:35], v[96:99]\n\t" \
    "v_cvt_pk_bf16_f32 v109, v78, v79\n\t" \
    "v_mfma_f32_16x16x32_bf16 v[100:103], a[244:247], v[36:39], v[100:103]\n\t" \
    "v_cvt_pk_bf16_f32 v110, v84, v85\n\t" \
    "s_waitcnt lgkmcnt(5)\n\t" \
    "v_mfma_f32_16x16x32_bf16 v[88:91], a[176:179], v[40:43], v[88:91]\n\t" \
    "ds_read_b128 v[64:67], v2 offset:11264\n\t" \
    "s_waitcnt lgkmcnt(5)\n\t" \
    "v_mfma_f32_16x16x32_bf16 v[92:95], a[176:179], v[44:47], v[92:95]\n\t" \
    "ds_read_b128 v[68:71], v2 offset:29696\n\t" \
    "v_mfma_f32_16x16x32_bf16 v[96:99], a[248:251], v[40:43], v[96:99]\n\t" \
    "v_cvt_pk_bf16_f32 v111, v86, v87\n\t" \
    "v_mfma_f32_16x16x32_bf16 v[100:103], a[248:251], v[44:47], v[100:103]\n\t" \
    "global_store_dwordx4 v9, v[108:111], s[38:39] offset:1024 sc1\n\t" \
    "s_waitcnt lgkmcnt(5)\n\t" \
    "v_mfma_f32_16x16x32_bf16 v[88:91], a[180:183], v[48:51], v[88:91]\n\t" \
    "ds_read_b128 v[24:27], v2 offset:12288\n\t" \
    "s_waitcnt lgkmcnt(5)\n\t" \
    "v_mfma_f32_16x16x32_bf16 v[92:95], a[180:183], v[52:55], v[92:95]\n\t" \
    "ds_read_b128 v[28:31], v2 offset:30720\n\t" \
    "v_mfma_f32_16x16x32_bf16 v[96:99], a[252:255], v[48:51], v[96:99]\n\t" \
    "s_add_u32 m0, s91, 0\n\t" \
    "v_mfma_f32_16x16x32_bf16 v[100:103], a[252:255], v[52:55], v[100:103]\n\t" \
    "global_load_lds_dwordx4 v1, s[50:51] offset:0\n\t" \
    "s_waitcnt lgkmcnt(5)\n\t" \
    "v_mfma_f32_16x16x32_bf16 v[88:91], a[184:187], v[56:59], v[88:91]\n\t" \
    "ds_read_b128 v[32:35], v2 offset:13312\n\t" \
    "s_waitcnt lgkmcnt(5)\n\t" \
    "v_mfma_f32_16x16x32_bf16 v[92:95], a[184:187], v[60:63], v[92:95]\n\t" \
    "ds_read_b128 v[36:39], v2 offset:31744\n\t" \
    "v_mfma_f32_16x16x32_bf16 v[96:99], v[224:227], v[56:59], v[96:99]\n\t" \
    "s_add_u32 m0, s91, 960\n\t" \
    "v_mfma_f32_16x16x32_bf16 v[100:103], v[224:227], v[60:63], v[100:103]\n\t" \
    "global_load_lds_dwordx4 v1, s[50:51] offset:64\n\t" \
    "s_waitcnt lgkmcnt(5)\n\t" \
    "v_mfma_f32_16x16x32_bf16 v[88:91], a[188:191], v[64:67], v[88:91]\n\t" \
    "ds_read_b128 v[40:43], v2 offset:14336\n\t" \
    "s_waitcnt lgkmcnt(5)\n\t" \
    "v_mfma_f32_16x16x32_bf16 v[92:95], a[188:191], v[68:71], v[92:95]\n\t" \
    "ds_read_b128 v[44:47], v2 offset:32768\n\t" \
    "v_mfma_f32_16x16x32_bf16 v[96:99], v[228:231], v[64:67], v[96:99]\n\t" \
    "s_add_u32 m0, s91, 1920\n\t" \
    "v_mfma_f32_16x16x32_bf16 v[100:103], v[228:231], v[68:71], v[100:103]\n\t" \
    "global_load_lds_dwordx4 v1, s[50:51] offset:128\n\t" \
    "s_waitcnt lgkmcnt(5)\n\t" \
    "v_mfma_f32_16x16x32_bf16 v[88:91], a[192:195], v[24:27], v[88:91]\n\t" \
    "ds_read_b128 v[48:51], v2 offset:15360\n\t" \
    "s_waitcnt lgkmcnt(5)\n\t" \
    "v_mfma_f32_16x16x32_bf16 v[92:95], a[192:195], v[28:31], v[92:95]\n\t" \
    "ds_read_b128 v[52:55], v2 offset:33792\n\t" \
    "v_mfma_f32_16x16x32_bf16 v[96:99], v[232:235], v[24:27], v[96:99]\n\t" \
    "s_add_u32 m0, s91, 2880\n\t" \
    "v_mfma_f32_16x16x32_bf16 v[100:103], v[232:235], v[28:31], v[100:103]\n\t" \
    "global_load_lds_dwordx4 v1, s[50:51] offset:192\n\t" \
    "s_waitcnt lgkmcnt(5)\n\t" \
    "v_mfma_f32_16x16x32_bf16 v[88:91], a[196:199], v[32:35], v[88:91]\n\t" \
    "ds_read_b128 v[56:59], v2 offset:16384\n\t" \
    "s_waitcnt lgkmcnt(5)\n\t" \
    "v_mfma_f32_16x16x32_bf16 v[92:95], a[196:199], v[36:39], v[92:95]\n\t" \
    "ds_read_b128 v[60:63], v2 offset:34816\n\t" \
    "v_mfma_f32_16x16x32_bf16 v[96:99], v[236:239], v[32:35], v[96:99]\n\t" \
    "s_add_u32 m0, s91, 3840\n\t" \
    "v_mfma_f32_16x16x32_bf16 v[100:103], v[236:239], v[36:39], v[100:103]\n\t" \
    "global_load_lds_dwordx4 v1, s[50:51] offset:256\n\t" \
    "s_waitcnt lgkmcnt(5)\n\t" \
    "v_mfma_f32_16x16x32_bf16 v[88:91], a[200:203], v[40:43], v[88:91]\n\t" \
    "ds_read_b128 v[64:67], v2 offset:17408\n\t" \
    "s_waitcnt lgkmcnt(5)\n\t" \
    "v_mfma_f32_16x16x32_bf16 v[92:95], a[200:203], v[44:47], v[92:95]\n\t" \
    "ds_read_b128 v[68:71], v2 offset:35840\n\t" \
    "v_mfma_f32_16x16x32_bf16 v[96:99], v[240:243], v[40:43], v[96:99]\n\t" \
    "s_add_u32 m0, s91, 4800\n\t" \
    "v_mfma_f32_16x16x32_bf16 v[100:103], v[240:243], v[44:47], v[100:103]\n\t" \
    "global_load_lds_dwordx4 v1, s[50:51] offset:320\n\t" \
    "s_waitcnt lgkmcnt(5)\n\t" \
    "v_mfma_f32_16x16x32_bf16 v[88:91], a[204:207], v[48:51], v[88:91]\n\t" \
    "ds_read_b128 v[24:27], v2 offset:0\n\t" \
    "s_waitcnt lgkmcnt(5)\n\t" \
    "v_mfma_f32_16x16x32_bf16 v[92:95], a[204:207], v[52:55], v[92:95]\n\t" \
    "ds_read_b128 v[28:31], v2 offset:18432\n\t" \
    "v_mfma_f32_16x16x32_bf16 v[96:99], v[244:247], v[48:51], v[96:99]\n\t" \
    "s_add_u32 m0, s91, 5760\n\t" \
    "v_mfma_f32_16x16x32_bf16 v[100:103], v[244:247], v[52:55], v[100:103]\n\t" \
    "global_load_lds_dwordx4 v1, s[50:51] offset:384\n\t" \
    "s_waitcnt lgkmcnt(5)\n\t" \
    "v_mfma_f32_16x16x32_bf16 v[88:91], a[208:211], v[56:59], v[88:91]\n\t" \
    "ds_read_b128 v[32:35], v2 offset:1024\n\t" \
    "s_waitcnt lgkmcnt(5)\n\t" \
    "v_mfma_f32_16x16x32_bf16 v[92:95], a[208:211], v[60:63], v[92:95]\n\t" \
    "ds_read_b128 v[36:39], v2 offset:19456\n\t" \
    "v_mfma_f32_16x16x32_bf16 v[96:99], v[248:251], v[56:59], v[96:99]\n\t" \
    "s_add_u32 m0, s91, 6720\n\t" \
    "v_mfma_f32_16x16x32_bf16 v[100:103], v[248:251], v[60:63], v[100:103]\n\t" \
    "global_load_lds_dwordx4 v1, s[50:51] offset:448\n\t" \
    "s_waitcnt lgkmcnt(5)\n\t" \
    "v_mfma_f32_16x16x32_bf16 v[88:91], a[212:215], v[64:67], v[88:91]\n\t" \
    "ds_read_b128 v[40:43], v2 offset:2048\n\t" \
    "s_waitcnt lgkmcnt(5)\n\t" \
    "v_mfma_f32_16x16x32_bf16 v[92:95], a[212:215], v[68:71], v[92:95]\n\t" \
    "ds_read_b128 v[44:47], v2 offset:20480\n\t" \
    "v_mfma_f32_16x16x32_bf16 v[96:99], v[252:255], v[64:67], v[96:99]\n\t" \
    "s_add_u32 m0, s91, 7680\n\t" \
    "v_mfma_f32_16x16x32_bf16 v[100:103], v[252:255], v[68:71], v[100:103]\n\t" \
    "global_load_lds_dwordx4 v1, s[50:51] offset:512\n\t" \
    "gx_mid8_%=:\n\t" \
    "s_waitcnt lgkmcnt(5)\n\t" \
    "v_mfma_f32_16x16x32_bf16 v[72:75], a[0:3], v[24:27], 0\n\t" \
    "ds_read_b128 v[48:51], v2 offset:3072\n\t" \
    "s_waitcnt lgkmcnt(5)\n\t" \
    "v_mfma_f32_16x16x32_bf16 v[76:79], a[0:3], v[28:31], 0\n\t" \
    "ds_read_b128 v[52:55], v2 offset:21504\n\t" \
    "v_mfma_f32_16x16x32_bf16 v[80:83], a[72:75], v[24:27], 0\n\t" \
    "s_lshl_b32 s69, s65, 13\n\t" \
    "v_xor_b32 v9, s69, v8\n\t" \
    "v_mfma_f32_16x16x32_bf16 v[84:87], a[72:75], v[28:31], 0\n\t" \
    "s_waitcnt lgkmcnt(5)\n\t" \
    "v_mfma_f32_16x16x32_bf16 v[72:75], a[4:7], v[32:35], v[72:75]\n\t" \
    "ds_read_b128 v[56:59], v2 offset:4096\n\t" \
    "s_waitcnt lgkmcnt(5)\n\t" \
    "v_mfma_f32_16x16x32_bf16 v[76:79], a[4:7], v[36:39], v[76:79]\n\t" \
    "ds_read_b128 v[60:63], v2 offset:22528\n\t" \
    "v_mfma_f32_16x16x32_bf16 v[80:83], a[76:79], v[32:35], v[80:83]\n\t" \
    "v_mfma_f32_16x16x32_bf16 v[84:87], a[76:79], v[36:39], v[84:87]\n\t" \
    "s_waitcnt lgkmcnt(5)\n\t" \
    "v_mfma_f32_16x16x32_bf16 v[72:75], a[8:11], v[40:43], v[72:75]\n\t" \
    "ds_read_b128 v[64:67], v2 offset:5120\n\t" \
    "s_waitcnt lgkmcnt(5)\n\t" \
    "v_mfma_f32_16x16x32_bf16 v[76:79], a[8:11], v[44:47], v[76:79]\n\t" \
    "ds_read_b128 v[68:71], v2 offset:23552\n\t" \
    "v_mfma_f32_16x16x32_bf16 v[80:83], a[80:83], v[40:43], v[80:83]\n\t" \
    "v_cvt_pk_bf16_f32 v112, v88, v89\n\t" \
    "v_mfma_f32_16x16x32_bf16 v[84:87], a[80:83], v[44:47], v[84:87]\n\t" \
    "v_cvt_pk_bf16_f32 v113, v90, v91\n\t" \
    "s_waitcnt lgkmcnt(5)\n\t" \
    "v_mfma_f32_16x16x32_bf16 v[72:75], a[12:15], v[48:51], v[72:75]\n\t" \
    "ds_read_b128 v[24:27], v2 offset:6144\n\t" \
    "s_waitcnt lgkmcnt(5)\n\t" \
    "v_mfma_f32_16x16x32_bf16 v[76:79], a[12:15], v[52:55], v[76:79]\n\t" \
    "ds_read_b128 v[28:31], v2 offset:24576\n\t" \
    "v_mfma_f32_16x16x32_bf16 v[80:83], a[84:87], v[48:51], v[80:83]\n\t" \
    "v_cvt_pk_bf16_f32 v114, v96, v97\n\t" \
    "v_mfma_f32_16x16x32_bf16 v[84:87], a[84:87], v[52:55], v[84:87]\n\t" \
    "v_cvt_pk_bf16_f32 v115, v98, v99\n\t" \
    "s_waitcnt lgkmcnt(5)\n\t" \
    "v_mfma_f32_16x16x32_bf16 v[72:75], a[16:19], v[56:59], v[72:75]\n\t" \
    "ds_read_b128 v[32:35], v2 offset:7168\n\t" \
    "s_waitcnt lgkmcnt(5)\n\t" \
    "v_mfma_f32_16x16x32_bf16 v[76:79], a[16:19], v[60:63], v[76:79]\n\t" \
    "ds_read_b128 v[36:39], v2 offset:25600\n\t" \
    "v_mfma_f32_16x16x32_bf16 v[80:83], a[88:91], v[56:59], v[80:83]\n\t" \
    "global_store_dwordx4 v9, v[112:115], s[40:41] offset:0 sc1\n\t" \
    "v_mfma_f32_16x16x32_bf16 v[84:87], a[88:91], v[60:63], v[84:87]\n\t" \
    "v_cvt_pk_bf16_f32 v116, v92, v93\n\t" \
    "s_waitcnt lgkmcnt(5)\n\t" \
    "v_mfma_f32_16x16x32_bf16 v[72:75], a[20:23], v[64:67], v[72:75]\n\t" \
    "ds_read_b128 v[40:43], v2 offset:8192\n\t" \
    "s_waitcnt lgkmcnt(5)\n\t" \
    "v_mfma_f32_16x16x32_bf16 v[76:79], a[20:23], v[68:71], v[76:79]\n\t" \
    "ds_read_b128 v[44:47], v2 offset:26624\n\t" \
    "v_mfma_f32_16x16x32_bf16 v[80:83], a[92:95], v[64:67], v[80:83]\n\t" \
    "v_cvt_pk_bf16_f32 v117, v94, v95\n\t" \
    "v_mfma_f32_16x16x32_bf16 v[84:87], a[92:95], v[68:71], v[84:87]\n\t" \
    "v_cvt_pk_bf16_f32 v118, v100, v101\n\t" \
    "s_waitcnt lgkmcnt(5)\n\t" \
    "v_mfma_f32_16x16x32_bf16 v[72:75], a[24:27], v[24:27], v[72:75]\n\t" \
    "ds_read_b128 v[48:51], v2 offset:9216\n\t" \
    "s_waitcnt lgkmcnt(5)\n\t" \
    "v_mfma_f32_16x16x32_bf16 v[76:79], a[24:27], v[28:31], v[76:79]\n\t" \
    "ds_read_b128 v[52:55], v2 offset:27648\n\t" \
    "v_mfma_f32_16x16x32_bf16 v[80:83], a[96:99], v[24:27], v[80:83]\n\t" \
    "v_cvt_pk_bf16_f32 v119, v102, v103\n\t" \
    "v_mfma_f32_16x16x32_bf16 v[84:87], a[96:99], v[28:31], v[84:87]\n\t" \
    "global_store_dwordx4 v9, v[116:119], s[40:41] offset:1024 sc1\n\t" \
    "s_waitcnt lgkmcnt(5)\n\t" \
    "v_mfma_f32_16x16x32_bf16 v[72:75], a[28:31], v[32:35], v[72:75]\n\t" \
    "ds_read_b128 v[56:59], v2 offset:10240\n\t" \
    "s_waitcnt lgkmcnt(5)\n\t" \
    "v_mfma_f32_16x16x32_bf16 v[76:79], a[28:31], v[36:39], v[76:79]\n\t" \
    "ds_read_b128 v[60:63], v2 offset:28672\n\t" \
    "v_mfma_f32_16x16x32_bf16 v[80:83], a[100:103], v[32:35], v[80:83]\n\t" \
    "v_mfma_f32_16x16x32_bf16 v[84:87], a[100:103], v[36:39], v[84:87]\n\t" \
    "s_waitcnt lgkmcnt(5)\n\t" \
    "v_mfma_f32_16x16x32_bf16 v[72:75], a[32:35], v[40:43], v[72:75]\n\t" \
    "ds_read_b128 v[64:67], v2 offset:11264\n\t" \
    "s_waitcnt lgkmcnt(5)\n\t" \
    "v_mfma_f32_16x16x32_bf16 v[76:79], a[32:35], v[44:47], v[76:79]\n\t" \
    "ds_read_b128 v[68:71], v2 offset:29696\n\t" \
    "v_mfma_f32_16x16x32_bf16 v[80:83], a[104:107], v[40:43], v[80:83]\n\t" \
    "v_mfma_f32_16x16x32_bf16 v[84:87], a[104:107], v[44:47], v[84:87]\n\t" \
    "s_waitcnt lgkmcnt(5)\n\t" \
    "v_mfma_f32_16x16x32_bf16 v[72:75], a[36:39], v[48:51], v[72:75]\n\t" \
    "ds_read_b128 v[24:27], v2 offset:12288\n\t" \
    "s_waitcnt lgkmcnt(5)\n\t" \
    "v_mfma_f32_16x16x32_bf16 v[76:79], a[36:39], v[52:55], v[76:79]\n\t" \
    "ds_read_b128 v[28:31], v2 offset:30720\n\t" \
    "v_mfma_f32_16x16x32_bf16 v[80:83], a[108:111], v[48:51], v[80:83]\n\t" \
    "v_mfma_f32_16x16x32_bf16 v[84:87], a[108:111], v[52:55], v[84:87]\n\t" \
    "s_waitcnt lgkmcnt(5)\n\t" \
    "v_mfma_f32_16x16x32_bf16 v[72:75], a[40:43], v[56:59], v[72:75]\n\t" \
    "ds_read_b128 v[32:35], v2 offset:13312\n\t" \
    "s_waitcnt lgkmcnt(5)\n\t" \
    "v_mfma_f32_16x16x32_bf16 v[76:79], a[40:43], v[60:63], v[76:79]\n\t" \
    "ds_read_b128 v[36:39], v2 offset:31744\n\t" \
    "v_mfma_f32_16x16x32_bf16 v[80:83], a[112:115], v[56:59], v[80:83]\n\t" \
    "v_mfma_f32_16x16x32_bf16 v[84:87], a[112:115], v[60:63], v[84:87]\n\t" \
    "s_waitcnt lgkmcnt(5)\n\t" \
    "v_mfma_f32_16x16x32_bf16 v[72:75], a[44:47], v[64:67], v[72:75]\n\t" \
    "ds_read_b128 v[40:43], v2 offset:14336\n\t" \
    "s_waitcnt lgkmcnt(5)\n\t" \
    "v_mfma_f32_16x16x32_bf16 v[76:79], a[44:47], v[68:71], v[76:79]\n\t" \
    "ds_read_b128 v[44:47], v2 offset:32768\n\t" \
    "v_mfma_f32_16x16x32_bf16 v[80:83], a[116:119], v[64:67], v[80:83]\n\t" \
    "v_mfma_f32_16x16x32_bf16 v[84:87], a[116:119], v[68:71], v[84:87]\n\t" \
    "s_waitcnt lgkmcnt(5)\n\t" \
    "v_mfma_f32_16x16x32_bf16 v[72:75], a[48:51], v[24:27], v[72:75]\n\t" \
    "ds_read_b128 v[48:51], v2 offset:15360\n\t" \
    "s_waitcnt lgkmcnt(5)\n\t" \
    "v_mfma_f32_16x16x32_bf16 v[76:79], a[48:51], v[28:31], v[76:79]\n\t" \
    "ds_read_b128 v[52:55], v2 offset:33792\n\t" \
    "v_mfma_f32_16x16x32_bf16 v[80:83], a[120:123], v[24:27], v[80:83]\n\t" \
    "v_mfma_f32_16x16x32_bf16 v[84:87], a[120:123], v[28:31], v[84:87]\n\t" \
    "s_waitcnt lgkmcnt(5)\n\t" \
    "v_mfma_f32_16x16x32_bf16 v[72:75], a[52:55], v[32:35], v[72:75]\n\t" \
    "ds_read_b128 v[56:59], v2 offset:16384\n\t" \
    "s_waitcnt lgkmcnt(5)\n\t" \
    "v_mfma_f32_16x16x32_bf16 v[76:79], a[52:55], v[36:39], v[76:79]\n\t" \
    "ds_read_b128 v[60:63], v2 offset:34816\n\t" \
    "v_mfma_f32_16x16x32_bf16 v[80:83], a[124:127], v[32:35], v[80:83]\n\t" \
    "v_mfma_f32_16x16x32_bf16 v[84:87], a[124:127], v[36:39], v[84:87]\n\t" \
    "s_cmp_eq_u32 s65, 0\n\t" \
    "s_cbranch_scc1 gx_g110_%=\n\t" \
    "gx_g1b11_%=:\n\t" \
    "s_waitcnt lgkmcnt(5)\n\t" \
    "v_mfma_f32_16x16x32_bf16 v[72:75], a[56:59], v[40:43], v[72:75]\n\t" \
    "ds_read_b128 v[64:67], v2 offset:17408\n\t" \
    "s_waitcnt lgkmcnt(5)\n\t" \
    "v_mfma_f32_16x16x32_bf16 v[76:79], a[56:59], v[44:47], v[76:79]\n\t" \
    "ds_read_b128 v[68:71], v2 offset:35840\n\t" \
    "v_mfma_f32_16x16x32_bf16 v[80:83], a[128:131], v[40:43], v[80:83]\n\t" \
    "v_mfma_f32_16x16x32_bf16 v[84:87], a[128:131], v[44:47], v[84:87]\n\t" \
    "s_waitcnt lgkmcnt(5)\n\t" \
    "v_mfma_f32_16x16x32_bf16 v[72:75], a[60:63], v[48:51], v[72:75]\n\t" \
    "ds_read_b128 v[24:27], v17 offset:0\n\t" \
    "s_waitcnt lgkmcnt(5)\n\t" \
    "v_mfma_f32_16x16x32_bf16 v[76:79], a[60:63], v[52:55], v[76:79]\n\t" \
    "ds_read_b128 v[28:31], v17 offset:18432\n\t" \
    "v_mfma_f32_16x16x32_bf16 v[80:83], a[132:135], v[48:51], v[80:83]\n\t" \
    "v_mov_b32 v2, v17\n\t" \
    "v_mfma_f32_16x16x32_bf16 v[84:87], a[132:135], v[52:55], v[84:87]\n\t" \
    "s_waitcnt lgkmcnt(5)\n\t" \
    "v_mfma_f32_16x16x32_bf16 v[72:75], a[64:67], v[56:59], v[72:75]\n\t" \
    "ds_read_b128 v[32:35], v17 offset:1024\n\t" \
    "s_waitcnt lgkmcnt(5)\n\t" \
    "v_mfma_f32_16x16x32_bf16 v[76:79], a[64:67], v[60:63], v[76:79]\n\t" \
    "ds_read_b128 v[36:39], v17 offset:19456\n\t" \
    "v_mfma_f32_16x16x32_bf16 v[80:83], a[136:139], v[56:59], v[80:83]\n\t" \
    "v_mfma_f32_16x16x32_bf16 v[84:87], a[136:139], v[60:63], v[84:87]\n\t" \
    "s_waitcnt lgkmcnt(5)\n\t" \
    "v_mfma_f32_16x16x32_bf16 v[72:75], a[68:71], v[64:67], v[72:75]\n\t" \
    "ds_read_b128 v[40:43], v17 offset:2048\n\t" \
    "s_waitcnt lgkmcnt(5)\n\t" \
    "v_mfma_f32_16x16x32_bf16 v[76:79], a[68:71], v[68:71], v[76:79]\n\t" \
    "ds_read_b128 v[44:47], v17 offset:20480\n\t" \
    "v_mfma_f32_16x16x32_bf16 v[80:83], a[140:143], v[64:67], v[80:83]\n\t" \
    "s_add_u32 s65, s65, 1\n\t" \
    "s_cmp_lt_u32 s65, 64\n\t" \
    "v_mfma_f32_16x16x32_bf16 v[84:87], a[140:143], v[68:71], v[84:87]\n\t" \
    "s_cbranch_scc1 gx_loop6_%=\n\t" \
    "s_nop 7\n\t" \
    "v_cvt_pk_bf16_f32 v104, v72, v73\n\t" \
    "v_cvt_pk_bf16_f32 v105, v74, v75\n\t" \
    "v_cvt_pk_bf16_f32 v106, v80, v81\n\t" \
    "v_cvt_pk_bf16_f32 v107, v82, v83\n\t" \
    "global_store_dwordx4 v9, v[104:107], s[38:39] offset:0 sc1\n\t" \
    "v_cvt_pk_bf16_f32 v108, v76, v77\n\t" \
    "v_cvt_pk_bf16_f32 v109, v78, v79\n\t" \
    "v_cvt_pk_bf16_f32 v110, v84, v85\n\t" \
    "v_cvt_pk_bf16_f32 v111, v86, v87\n\t" \
    "global_store_dwordx4 v9, v[108:111], s[38:39] offset:1024 sc1\n\t" \
    "s_waitcnt vmcnt(0) lgkmcnt(0)\n\t" \
    "s_barrier\n\t" \
    "s_cmp_lg_u32 s66, 0\n\t" \
    "s_cbranch_scc1 gx_SF12_%=\n\t" \
    "s_lshl_b32 s69, s67, 7\n\t" \
    "s_add_u32 s69, s69, 6098944\n\t" \
    "s_add_u32 s69, s69, s52\n\t" \
    "s_add_u32 s70, s65, 0\n\t" \
    "v_mov_b32 v156, s69\n\t" \
    "v_mov_b32 v157, s70\n\t" \
    "s_add_u32 s69, s69, 61440\n\t" \
    "s_max_i32 s70, s70, 1\n\t" \
    "v_writelane_b32 v156, s69, 1\n\t" \
    "v_writelane_b32 v157, s70, 1\n\t" \
    "s_mov_b64 exec, 3\n\t" \
    "global_store_dword v156, v157, s[36:37] sc1\n\t" \
    "s_mov_b64 exec, -1\n\t" \
    "gx_SF12_%=:\n\t" \
    "s_waitcnt vmcnt(0)\n\t" \
    "s_branch ea_done_%=\n\t" \
    "gx_g110_%=:\n\t" \
    "s_add_u32 m0, s91, 0\n\t" \
    "s_nop 0\n\t" \
    "global_load_lds_dwordx4 v1, s[50:51] offset:0\n\t" \
    "s_add_u32 m0, s91, 960\n\t" \
    "s_nop 0\n\t" \
    "global_load_lds_dwordx4 v1, s[50:51] offset:64\n\t" \
    "s_add_u32 m0, s91, 1920\n\t" \
    "s_nop 0\n\t" \
    "global_load_lds_dwordx4 v1, s[50:51] offset:128\n\t" \
    "s_add_u32 m0, s91, 2880\n\t" \
    "s_nop 0\n\t" \
    "global_load_lds_dwordx4 v1, s[50:51] offset:192\n\t" \
    "s_add_u32 m0, s91, 3840\n\t" \
    "s_nop 0\n\t" \
    "global_load_lds_dwordx4 v1, s[50:51] offset:256\n\t" \
    "s_add_u32 m0, s91, 4800\n\t" \
    "s_nop 0\n\t" \
    "global_load_lds_dwordx4 v1, s[50:51] offset:320\n\t" \
    "s_add_u32 m0, s91, 5760\n\t" \
    "s_nop 0\n\t" \
    "global_load_lds_dwordx4 v1, s[50:51] offset:384\n\t" \
    "s_add_u32 m0, s91, 6720\n\t" \
    "s_nop 0\n\t" \
    "global_load_lds_dwordx4 v1, s[50:51] offset:448\n\t" \
    "s_add_u32 m0, s91, 7680\n\t" \
    "s_nop 0\n\t" \
    "global_load_lds_dwordx4 v1, s[50:51] offset:512\n\t" \
    "s_branch gx_g1b11_%=\n\t" \
    "gv_begin_%=:\n\t" \
    "v_mbcnt_lo_u32_b32 v223, -1, 0\n\t" \
    "v_mbcnt_hi_u32_b32 v223, -1, v223\n\t" \
    "s_mov_b32 s68, 262144\n\t" \
    "s_mov_b32 s72, 0\n\t" \
    "s_mov_b32 s83, -1\n\t" \
    "s_mov_b32 s65, 0\n\t" \
    "v_and_b32 v180, 3, v223\n\t" \
    "v_lshlrev_b32 v202, 3, v180\n\t" \
    "v_lshrrev_b32 v181, 5, v223\n\t" \
    "v_lshl_add_u32 v202, v181, 12, v202\n\t" \
    "v_bfe_u32 v182, v223, 2, 2\n\t" \
    "s_mov_b32 s70, 288\n\t" \
    "v_mad_u32_u24 v202, v182, s70, v202\n\t" \
    "v_bfe_u32 v183, v223, 4, 1\n\t" \
    "s_mov_b32 s70, 2176\n\t" \
    "v_mad_u32_u24 v202, v183, s70, v202\n\t" \
    "s_lshl_b32 s69, s66, 6\n\t" \
    "v_xor_b32 v202, s69, v202\n\t" \
    "v_xor_b32 v203, 32, v202\n\t" \
    "s_mul_i32 s69, s66, 9216\n\t" \
    "v_and_b32 v184, 1, v223\n\t" \
    "v_lshl_add_u32 v201, v184, 4, s69\n\t" \
    "v_lshrrev_b32 v185, 3, v223\n\t" \
    "s_mov_b32 s70, 1152\n\t" \
    "v_mad_u32_u24 v201, v185, s70, v201\n\t" \
    "v_bfe_u32 v186, v223, 1, 2\n\t" \
    "v_lshlrev_b32 v204, 5, v186\n\t" \
    "v_lshrrev_b32 v187, 4, v223\n\t" \
    "v_lshlrev_b32 v205, 5, v187\n\t" \
    "v_xor_b32 v204, v204, v205\n\t" \
    "v_add_u32 v201, v201, v204\n\t" \
    "s_mul_i32 s96, s66, 9216\n\t" \
    "v_lshlrev_b32 v207, 3, v180\n\t" \
    "v_bfe_u32 v188, v223, 2, 1\n\t" \
    "v_lshl_add_u32 v207, v188, 7, v207\n\t" \
    "v_bfe_u32 v189, v223, 3, 2\n\t" \
    "s_mov_b32 s70, 288\n\t" \
    "v_mad_u32_u24 v207, v189, s70, v207\n\t" \
    "s_mov_b32 s70, 9216\n\t" \
    "v_mad_u32_u24 v207, v181, s70, v207\n\t" \
    "s_mul_i32 s69, s66, 9\n\t" \
    "s_add_u32 s69, s69, 0\n\t" \
    "s_and_b32 s70, s69, 3\n\t" \
    "s_lshl_b32 s70, s70, 5\n\t" \
    "s_lshr_b32 s69, s69, 2\n\t" \
    "s_lshl_b32 s69, s69, 10\n\t" \
    "v_xor_b32 v204, s70, v207\n\t" \
    "v_add_u32 v204, s69, v204\n\t" \
    "s_mul_i32 s69, s66, 9\n\t" \
    "s_add_u32 s69, s69, 1\n\t" \
    "s_and_b32 s70, s69, 3\n\t" \
    "s_lshl_b32 s70, s70, 5\n\t" \
    "s_lshr_b32 s69, s69, 2\n\t" \
    "s_lshl_b32 s69, s69, 10\n\t" \
    "v_xor_b32 v205, s70, v207\n\t" \
    "v_add_u32 v205, s69, v205\n\t" \
    "s_mul_i32 s69, s66, 9\n\t" \
    "s_add_u32 s69, s69, 2\n\t" \
    "s_and_b32 s70, s69, 3\n\t" \
    "s_lshl_b32 s70, s70, 5\n\t" \
    "s_lshr_b32 s69, s69, 2\n\t" \
    "s_lshl_b32 s69, s69, 10\n\t" \
    "v_xor_b32 v206, s70, v207\n\t" \
    "v_add_u32 v206, s69, v206\n\t" \
    "s_mul_i32 s69, s66, 9\n\t" \
    "s_add_u32 s69, s69, 3\n\t" \
    "s_and_b32 s70, s69, 3\n\t" \
    "s_lshl_b32 s70, s70, 5\n\t" \
    "s_lshr_b32 s69, s69, 2\n\t" \
    "s_lshl_b32 s69, s69, 10\n\t" \
    "v_xor_b32 v207, s70, v207\n\t" \
    "v_add_u32 v207, s69, v207\n\t" \
    "s_waitcnt vmcnt(12) lgkmcnt(0)\n\t" \
    "s_barrier\n\t" \
    "s_add_u32 m0, s82, 65536\n\t" \
    "s_nop 0\n\t" \
    "global_load_lds_dwordx4 v200, s[42:43]\n\t" \
    "v_add_u32 v200, 131072, v200\n\t" \
    "s_add_u32 m0, s82, 69632\n\t" \
    "s_nop 0\n\t" \
    "global_load_lds_dwordx4 v200, s[42:43]\n\t" \
    "v_add_u32 v200, 131072, v200\n\t" \
    "s_add_u32 m0, s82, 73728\n\t" \
    "s_nop 0\n\t" \
    "global_load_lds_dwordx4 v200, s[42:43]\n\t" \
    "v_add_u32 v200, 131072, v200\n\t" \
    "s_add_u32 m0, s82, 77824\n\t" \
    "s_nop 0\n\t" \
    "global_load_lds_dwordx4 v200, s[42:43]\n\t" \
    "v_add_u32 v200, 131072, v200\n\t" \
    "ds_read_b64_tr_b16 a[0:1], v202 offset:36864\n\t" \
    "ds_read_b64_tr_b16 a[2:3], v202 offset:37888\n\t" \
    "ds_read_b64_tr_b16 a[4:5], v202 offset:45056\n\t" \
    "ds_read_b64_tr_b16 a[6:7], v202 offset:46080\n\t" \
    "ds_read_b64_tr_b16 a[72:73], v203 offset:36864\n\t" \
    "ds_read_b64_tr_b16 a[74:75], v203 offset:37888\n\t" \
    "ds_read_b64_tr_b16 a[76:77], v203 offset:45056\n\t" \
    "ds_read_b64_tr_b16 a[78:79], v203 offset:46080\n\t" \
    "v_lshlrev_b32 v3, 4, v180\n\t" \
    "v_lshlrev_b32 v2, 5, v181\n\t" \
    "v_xor_b32 v3, v3, v2\n\t" \
    "s_bfe_u32 s71, s66, 0x10000\n\t" \
    "s_mul_i32 s69, s71, 576\n\t" \
    "s_bfe_u32 s71, s66, 0x10001\n\t" \
    "s_mul_i32 s71, s71, 18432\n\t" \
    "s_add_u32 s69, s69, s71\n\t" \
    "v_lshrrev_b32 v190, 2, v223\n\t" \
    "v_mov_b32 v1, s69\n\t" \
    "s_mov_b32 s70, 1152\n\t" \
    "v_mad_u32_u24 v1, v190, s70, v1\n\t" \
    "v_add_u32 v1, v1, v3\n\t" \
    "v_and_b32 v191, 15, v223\n\t" \
    "v_lshlrev_b32 v3, 6, v191\n\t" \
    "v_lshl_add_u32 v3, v187, 4, v3\n\t" \
    "v_bfe_u32 v192, v223, 3, 1\n\t" \
    "v_lshlrev_b32 v2, 5, v192\n\t" \
    "v_xor_b32 v3, v3, v2\n\t" \
    "v_mov_b32 v2, v3\n\t" \
    "s_lshr_b32 s65, s67, 5\n\t" \
    "s_mul_i32 s69, s65, 36864\n\t" \
    "s_add_u32 s50, s50, s69\n\t" \
    "s_addc_u32 s51, s51, 0\n\t" \
    "s_add_u32 s38, s36, 48234496\n\t" \
    "s_addc_u32 s39, s37, 0\n\t" \
    "s_mul_i32 s89, s66, 9216\n\t" \
    "s_mov_b32 s90, 0\n\t" \
    "s_waitcnt vmcnt(12) lgkmcnt(0)\n\t" \
    "s_barrier\n\t" \
    "s_add_u32 m0, s82, 81920\n\t" \
    "s_nop 0\n\t" \
    "global_load_lds_dwordx4 v200, s[42:43]\n\t" \
    "v_add_u32 v200, 131072, v200\n\t" \
    "s_add_u32 m0, s82, 86016\n\t" \
    "s_nop 0\n\t" \
    "global_load_lds_dwordx4 v200, s[42:43]\n\t" \
    "v_add_u32 v200, 131072, v200\n\t" \
    "s_add_u32 m0, s82, 90112\n\t" \
    "s_nop 0\n\t" \
    "global_load_lds_dwordx4 v200, s[42:43]\n\t" \
    "v_add_u32 v200, 131072, v200\n\t" \
    "s_add_u32 m0, s82, 94208\n\t" \
    "s_nop 0\n\t" \
    "global_load_lds_dwordx4 v200, s[42:43]\n\t" \
    "v_add_u32 v200, 131072, v200\n\t" \
    "ds_read_b64_tr_b16 a[8:9], v202 offset:53248\n\t" \
    "ds_read_b64_tr_b16 a[10:11], v202 offset:54272\n\t" \
    "ds_read_b64_tr_b16 a[12:13], v202 offset:61440\n\t" \
    "ds_read_b64_tr_b16 a[14:15], v202 offset:62464\n\t" \
    "ds_read_b64_tr_b16 a[80:81], v203 offset:53248\n\t" \
    "ds_read_b64_tr_b16 a[82:83], v203 offset:54272\n\t" \
    "ds_read_b64_tr_b16 a[84:85], v203 offset:61440\n\t" \
    "ds_read_b64_tr_b16 a[86:87], v203 offset:62464\n\t" \
    "v_lshlrev_b32 v0, 4, v223\n\t" \
    "s_mul_i32 s69, s66, 2048\n\t" \
    "s_add_u32 s69, s69, 147456\n\t" \
    "v_lshl_add_u32 v4, v223, 4, s69\n\t" \
    "s_mov_b32 s69, 147456\n\t" \
    "v_lshl_add_u32 v5, v223, 4, s69\n\t" \
    "v_lshlrev_b32 v6, 2, v223\n\t" \
    "v_mov_b32 v7, 0\n\t" \
    "s_cmp_eq_u32 s66, 0\n\t" \
    "s_cselect_b32 s58, 31, 0\n\t" \
    "s_mov_b32 s59, 0\n\t" \
    "s_waitcnt vmcnt(12) lgkmcnt(0)\n\t" \
    "s_barrier\n\t" \
    "s_add_u32 m0, s82, 98304\n\t" \
    "s_nop 0\n\t" \
    "global_load_lds_dwordx4 v200, s[42:43]\n\t" \
    "v_add_u32 v200, 131072, v200\n\t" \
    "s_add_u32 m0, s82, 102400\n\t" \
    "s_nop 0\n\t" \
    "global_load_lds_dwordx4 v200, s[42:43]\n\t" \
    "v_add_u32 v200, 131072, v200\n\t" \
    "s_add_u32 m0, s82, 106496\n\t" \
    "s_nop 0\n\t" \
    "global_load_lds_dwordx4 v200, s[42:43]\n\t" \
    "v_add_u32 v200, 131072, v200\n\t" \
    "s_add_u32 m0, s82, 110592\n\t" \
    "s_nop 0\n\t" \
    "global_load_lds_dwordx4 v200, s[42:43]\n\t" \
    "v_add_u32 v200, 131072, v200\n\t" \
    "v_add_u32 v202, 69632, v202\n\t" \
    "v_add_u32 v203, 69632, v203\n\t" \
    "ds_read_b64_tr_b16 a[16:17], v202 offset:0\n\t" \
    "ds_read_b64_tr_b16 a[18:19], v202 offset:1024\n\t" \
    "ds_read_b64_tr_b16 a[20:21], v202 offset:8192\n\t" \
    "ds_read_b64_tr_b16 a[22:23], v202 offset:9216\n\t" \
    "ds_read_b64_tr_b16 a[88:89], v203 offset:0\n\t" \
    "ds_read_b64_tr_b16 a[90:91], v203 offset:1024\n\t" \
    "ds_read_b64_tr_b16 a[92:93], v203 offset:8192\n\t" \
    "ds_read_b64_tr_b16 a[94:95], v203 offset:9216\n\t" \
    "s_bfe_u32 s71, s67, 0x50000\n\t" \
    "s_mul_i32 s69, s71, 32\n\t" \
    "s_add_u32 s69, s69, 262148\n\t" \
    "v_lshl_add_u32 v19, v180, 14, s69\n\t" \
    "v_lshl_add_u32 v19, v181, 13, v19\n\t" \
    "v_bfe_u32 v193, v223, 2, 3\n\t" \
    "v_lshlrev_b32 v199, 10, v193\n\t" \
    "v_min_u32 v199, 4096, v199\n\t" \
    "v_add_u32 v19, v199, v19\n\t" \
    "s_mul_i32 s69, s67, 32\n\t" \
    "s_add_u32 s69, s69, 6193152\n\t" \
    "v_lshl_add_u32 v20, v180, 11, s69\n\t" \
    "s_lshl_b32 s69, s67, 7\n\t" \
    "s_add_u32 s69, s69, 6111232\n\t" \
    "v_writelane_b32 v20, s69, 4\n\t" \
    "s_bfe_u32 s71, s67, 0x50000\n\t" \
    "s_mul_i32 s69, s71, 1024\n\t" \
    "v_lshl_add_u32 v17, v223, 4, s69\n\t" \
    "s_mul_i32 s69, s66, 9\n\t" \
    "s_add_u32 s69, s69, 0\n\t" \
    "s_mul_i32 s70, s69, 7\n\t" \
    "s_and_b32 s70, s70, 63\n\t" \
    "s_lshl_b32 s70, s70, 15\n\t" \
    "s_mul_i32 s69, s69, 2097152\n\t" \
    "s_add_u32 s69, s69, s70\n\t" \
    "v_add_u32 v8, s69, v17\n\t" \
    "s_mul_i32 s69, s66, 9\n\t" \
    "s_add_u32 s69, s69, 1\n\t" \
    "s_mul_i32 s70, s69, 7\n\t" \
    "s_and_b32 s70, s70, 63\n\t" \
    "s_lshl_b32 s70, s70, 15\n\t" \
    "s_mul_i32 s69, s69, 2097152\n\t" \
    "s_add_u32 s69, s69, s70\n\t" \
    "v_add_u32 v9, s69, v17\n\t" \
    "s_mul_i32 s69, s66, 9\n\t" \
    "s_add_u32 s69, s69, 2\n\t" \
    "s_mul_i32 s70, s69, 7\n\t" \
    "s_and_b32 s70, s70, 63\n\t" \
    "s_lshl_b32 s70, s70, 15\n\t" \
    "s_mul_i32 s69, s69, 2097152\n\t" \
    "s_add_u32 s69, s69, s70\n\t" \
    "v_add_u32 v10, s69, v17\n\t" \
    "s_mul_i32 s69, s66, 9\n\t" \
    "s_add_u32 s69, s69, 3\n\t" \
    "s_mul_i32 s70, s69, 7\n\t" \
    "s_and_b32 s70, s70, 63\n\t" \
    "s_lshl_b32 s70, s70, 15\n\t" \
    "s_mul_i32 s69, s69, 2097152\n\t" \
    "s_add_u32 s69, s69, s70\n\t" \
    "v_add_u32 v11, s69, v17\n\t" \
    "s_mul_i32 s69, s66, 9\n\t" \
    "s_add_u32 s69, s69, 4\n\t" \
    "s_mul_i32 s70, s69, 7\n\t" \
    "s_and_b32 s70, s70, 63\n\t" \
    "s_lshl_b32 s70, s70, 15\n\t" \
    "s_mul_i32 s69, s69, 2097152\n\t" \
    "s_add_u32 s69, s69, s70\n\t" \
    "v_add_u32 v12, s69, v17\n\t" \
    "s_mul_i32 s69, s66, 9\n\t" \
    "s_add_u32 s69, s69, 5\n\t" \
    "s_mul_i32 s70, s69, 7\n\t" \
    "s_and_b32 s70, s70, 63\n\t" \
    "s_lshl_b32 s70, s70, 15\n\t" \
    "s_mul_i32 s69, s69, 2097152\n\t" \
    "s_add_u32 s69, s69, s70\n\t" \
    "v_add_u32 v13, s69, v17\n\t" \
    "s_mul_i32 s69, s66, 9\n\t" \
    "s_add_u32 s69, s69, 6\n\t" \
    "s_mul_i32 s70, s69, 7\n\t" \
    "s_and_b32 s70, s70, 63\n\t" \
    "s_lshl_b32 s70, s70, 15\n\t" \
    "s_mul_i32 s69, s69, 2097152\n\t" \
    "s_add_u32 s69, s69, s70\n\t" \
    "v_add_u32 v14, s69, v17\n\t" \
    "s_mul_i32 s69, s66, 9\n\t" \
    "s_add_u32 s69, s69, 7\n\t" \
    "s_mul_i32 s70, s69, 7\n\t" \
    "s_and_b32 s70, s70, 63\n\t" \
    "s_lshl_b32 s70, s70, 15\n\t" \
    "s_mul_i32 s69, s69, 2097152\n\t" \
    "s_add_u32 s69, s69, s70\n\t" \
    "v_add_u32 v15, s69, v17\n\t" \
    "s_mul_i32 s69, s66, 9\n\t" \
    "s_add_u32 s69, s69, 8\n\t" \
    "s_mul_i32 s70, s69, 7\n\t" \
    "s_and_b32 s70, s70, 63\n\t" \
    "s_lshl_b32 s70, s70, 15\n\t" \
    "s_mul_i32 s69, s69, 2097152\n\t" \
    "s_add_u32 s69, s69, s70\n\t" \
    "v_add_u32 v16, s69, v17\n\t" \
    "s_waitcnt vmcnt(12) lgkmcnt(0)\n\t" \
    "s_barrier\n\t" \
    "s_add_u32 m0, s82, 0\n\t" \
    "s_nop 0\n\t" \
    "global_load_lds_dwordx4 v200, s[42:43]\n\t" \
    "v_add_u32 v200, 131072, v200\n\t" \
    "s_add_u32 m0, s82, 4096\n\t" \
    "s_nop 0\n\t" \
    "global_load_lds_dwordx4 v200, s[42:43]\n\t" \
    "v_add_u32 v200, 131072, v200\n\t" \
    "s_add_u32 m0, s82, 8192\n\t" \
    "s_nop 0\n\t" \
    "global_load_lds_dwordx4 v200, s[42:43]\n\t" \
    "v_add_u32 v200, 131072, v200\n\t" \
    "s_add_u32 m0, s82, 12288\n\t" \
    "s_nop 0\n\t" \
    "global_load_lds_dwordx4 v200, s[42:43]\n\t" \
    "v_add_u32 v200, 131072, v200\n\t" \
    "ds_read_b64_tr_b16 a[24:25], v202 offset:16384\n\t" \
    "ds_read_b64_tr_b16 a[26:27], v202 offset:17408\n\t" \
    "ds_read_b64_tr_b16 a[28:29], v202 offset:24576\n\t" \
    "ds_read_b64_tr_b16 a[30:31], v202 offset:25600\n\t" \
    "ds_read_b64_tr_b16 a[96:97], v203 offset:16384\n\t" \
    "ds_read_b64_tr_b16 a[98:99], v203 offset:17408\n\t" \
    "ds_read_b64_tr_b16 a[100:101], v203 offset:24576\n\t" \
    "ds_read_b64_tr_b16 a[102:103], v203 offset:25600\n\t" \
    "s_waitcnt vmcnt(12) lgkmcnt(0)\n\t" \
    "s_barrier\n\t" \
    "s_add_u32 m0, s82, 16384\n\t" \
    "s_nop 0\n\t" \
    "global_load_lds_dwordx4 v200, s[42:43]\n\t" \
    "v_add_u32 v200, 131072, v200\n\t" \
    "s_add_u32 m0, s82, 20480\n\t" \
    "s_nop 0\n\t" \
    "global_load_lds_dwordx4 v200, s[42:43]\n\t" \
    "v_add_u32 v200, 131072, v200\n\t" \
    "s_add_u32 m0, s82, 24576\n\t" \
    "s_nop 0\n\t" \
    "global_load_lds_dwordx4 v200, s[42:43]\n\t" \
    "v_add_u32 v200, 131072, v200\n\t" \
    "s_add_u32 m0, s82, 28672\n\t" \
    "s_nop 0\n\t" \
    "global_load_lds_dwordx4 v200, s[42:43]\n\t" \
    "v_add_u32 v200, 131072, v200\n\t" \
    "s_add_u32 m0, s89, 0\n\t" \
    "s_nop 0\n\t" \
    "global_load_lds_dwordx4 v1, s[50:51] offset:0\n\t" \
    "s_add_u32 m0, s89, 960\n\t" \
    "s_nop 0\n\t" \
    "global_load_lds_dwordx4 v1, s[50:51] offset:64\n\t" \
    "s_add_u32 m0, s89, 1920\n\t" \
    "s_nop 0\n\t" \
    "global_load_lds_dwordx4 v1, s[50:51] offset:128\n\t" \
    "s_add_u32 m0, s89, 2880\n\t" \
    "s_nop 0\n\t" \
    "global_load_lds_dwordx4 v1, s[50:51] offset:192\n\t" \
    "s_add_u32 m0, s89, 3840\n\t" \
    "s_nop 0\n\t" \
    "global_load_lds_dwordx4 v1, s[50:51] offset:256\n\t" \
    "s_add_u32 m0, s89, 4800\n\t" \
    "s_nop 0\n\t" \
    "global_load_lds_dwordx4 v1, s[50:51] offset:320\n\t" \
    "s_add_u32 m0, s89, 5760\n\t" \
    "s_nop 0\n\t" \
    "global_load_lds_dwordx4 v1, s[50:51] offset:384\n\t" \
    "s_add_u32 m0, s89, 6720\n\t" \
    "s_nop 0\n\t" \
    "global_load_lds_dwordx4 v1, s[50:51] offset:448\n\t" \
    "s_add_u32 m0, s89, 7680\n\t" \
    "s_nop 0\n\t" \
    "global_load_lds_dwordx4 v1, s[50:51] offset:512\n\t" \
    "ds_read_b64_tr_b16 a[32:33], v202 offset:32768\n\t" \
    "ds_read_b64_tr_b16 a[34:35], v202 offset:33792\n\t" \
    "ds_read_b64_tr_b16 a[36:37], v202 offset:40960\n\t" \
    "ds_read_b64_tr_b16 a[38:39], v202 offset:41984\n\t" \
    "ds_read_b64_tr_b16 a[104:105], v203 offset:32768\n\t" \
    "ds_read_b64_tr_b16 a[106:107], v203 offset:33792\n\t" \
    "ds_read_b64_tr_b16 a[108:109], v203 offset:40960\n\t" \
    "ds_read_b64_tr_b16 a[110:111], v203 offset:41984\n\t" \
    "s_waitcnt vmcnt(21) lgkmcnt(0)\n\t" \
    "s_barrier\n\t" \
    "s_add_u32 m0, s96, 73728\n\t" \
    "s_nop 0\n\t" \
    "global_load_lds_dwordx4 v201, s[44:45] offset:0\n\t" \
    "s_add_u32 m0, s96, 74624\n\t" \
    "s_nop 0\n\t" \
    "global_load_lds_dwordx4 v201, s[44:45] offset:128\n\t" \
    "s_add_u32 m0, s96, 75520\n\t" \
    "s_nop 0\n\t" \
    "global_load_lds_dwordx4 v201, s[44:45] offset:256\n\t" \
    "s_add_u32 m0, s96, 76416\n\t" \
    "s_nop 0\n\t" \
    "global_load_lds_dwordx4 v201, s[44:45] offset:384\n\t" \
    "s_add_u32 m0, s96, 77312\n\t" \
    "s_nop 0\n\t" \
    "global_load_lds_dwordx4 v201, s[44:45] offset:512\n\t" \
    "s_add_u32 m0, s96, 78208\n\t" \
    "s_nop 0\n\t" \
    "global_load_lds_dwordx4 v201, s[44:45] offset:640\n\t" \
    "s_add_u32 m0, s96, 79104\n\t" \
    "s_nop 0\n\t" \
    "global_load_lds_dwordx4 v201, s[44:45] offset:768\n\t" \
    "s_add_u32 m0, s96, 80000\n\t" \
    "s_nop 0\n\t" \
    "global_load_lds_dwordx4 v201, s[44:45] offset:896\n\t" \
    "s_add_u32 m0, s96, 80896\n\t" \
    "s_nop 0\n\t" \
    "global_load_lds_dwordx4 v201, s[44:45] offset:1024\n\t" \
    "v_add_u32 v201, 36864, v201\n\t" \
    "ds_read_b64_tr_b16 a[40:41], v202 offset:49152\n\t" \
    "ds_read_b64_tr_b16 a[42:43], v202 offset:50176\n\t" \
    "ds_read_b64_tr_b16 a[44:45], v202 offset:57344\n\t" \
    "ds_read_b64_tr_b16 a[46:47], v202 offset:58368\n\t" \
    "ds_read_b64_tr_b16 a[112:113], v203 offset:49152\n\t" \
    "ds_read_b64_tr_b16 a[114:115], v203 offset:50176\n\t" \
    "ds_read_b64_tr_b16 a[116:117], v203 offset:57344\n\t" \
    "ds_read_b64_tr_b16 a[118:119], v203 offset:58368\n\t" \
    "s_waitcnt vmcnt(26) lgkmcnt(0)\n\t" \
    "s_barrier\n\t" \
    "v_add_u32 v202, 65536, v202\n\t" \
    "v_add_u32 v203, 65536, v203\n\t" \
    "ds_read_b64_tr_b16 a[48:49], v202 offset:0\n\t" \
    "ds_read_b64_tr_b16 a[50:51], v202 offset:1024\n\t" \
    "ds_read_b64_tr_b16 a[52:53], v202 offset:8192\n\t" \
    "ds_read_b64_tr_b16 a[54:55], v202 offset:9216\n\t" \
    "ds_read_b64_tr_b16 a[120:121], v203 offset:0\n\t" \
    "ds_read_b64_tr_b16 a[122:123], v203 offset:1024\n\t" \
    "ds_read_b64_tr_b16 a[124:125], v203 offset:8192\n\t" \
    "ds_read_b64_tr_b16 a[126:127], v203 offset:9216\n\t" \
    "s_waitcnt vmcnt(22) lgkmcnt(0)\n\t" \
    "s_barrier\n\t" \
    "s_add_u32 m0, s96, 110592\n\t" \
    "s_nop 0\n\t" \
    "global_load_lds_dwordx4 v201, s[44:45] offset:0\n\t" \
    "s_add_u32 m0, s96, 111488\n\t" \
    "s_nop 0\n\t" \
    "global_load_lds_dwordx4 v201, s[44:45] offset:128\n\t" \
    "s_add_u32 m0, s96, 112384\n\t" \
    "s_nop 0\n\t" \
    "global_load_lds_dwordx4 v201, s[44:45] offset:256\n\t" \
    "s_add_u32 m0, s96, 113280\n\t" \
    "s_nop 0\n\t" \
    "global_load_lds_dwordx4 v201, s[44:45] offset:384\n\t" \
    "s_add_u32 m0, s96, 114176\n\t" \
    "s_nop 0\n\t" \
    "global_load_lds_dwordx4 v201, s[44:45] offset:512\n\t" \
    "s_add_u32 m0, s96, 115072\n\t" \
    "s_nop 0\n\t" \
    "global_load_lds_dwordx4 v201, s[44:45] offset:640\n\t" \
    "s_add_u32 m0, s96, 115968\n\t" \
    "s_nop 0\n\t" \
    "global_load_lds_dwordx4 v201, s[44:45] offset:768\n\t" \
    "s_add_u32 m0, s96, 116864\n\t" \
    "s_nop 0\n\t" \
    "global_load_lds_dwordx4 v201, s[44:45] offset:896\n\t" \
    "s_add_u32 m0, s96, 117760\n\t" \
    "s_nop 0\n\t" \
    "global_load_lds_dwordx4 v201, s[44:45] offset:1024\n\t" \
    "v_add_u32 v201, 36864, v201\n\t" \
    "v_add_u32 v202, -98304, v202\n\t" \
    "v_add_u32 v203, -98304, v203\n\t" \
    "ds_read_b64_tr_b16 a[56:57], v202 offset:0\n\t" \
    "ds_read_b64_tr_b16 a[58:59], v202 offset:1024\n\t" \
    "ds_read_b64_tr_b16 a[60:61], v202 offset:8192\n\t" \
    "ds_read_b64_tr_b16 a[62:63], v202 offset:9216\n\t" \
    "ds_read_b64_tr_b16 a[128:129], v203 offset:0\n\t" \
    "ds_read_b64_tr_b16 a[130:131], v203 offset:1024\n\t" \
    "ds_read_b64_tr_b16 a[132:133], v203 offset:8192\n\t" \
    "ds_read_b64_tr_b16 a[134:135], v203 offset:9216\n\t" \
    "s_waitcnt vmcnt(27) lgkmcnt(0)\n\t" \
    "s_barrier\n\t" \
    "ds_read_b64_tr_b16 a[64:65], v202 offset:16384\n\t" \
    "ds_read_b64_tr_b16 a[66:67], v202 offset:17408\n\t" \
    "ds_read_b64_tr_b16 a[68:69], v202 offset:24576\n\t" \
    "ds_read_b64_tr_b16 a[70:71], v202 offset:25600\n\t" \
    "ds_read_b64_tr_b16 a[136:137], v203 offset:16384\n\t" \
    "ds_read_b64_tr_b16 a[138:139], v203 offset:17408\n\t" \
    "ds_read_b64_tr_b16 a[140:141], v203 offset:24576\n\t" \
    "ds_read_b64_tr_b16 a[142:143], v203 offset:25600\n\t" \
    "s_waitcnt vmcnt(9) lgkmcnt(0)\n\t" \
    "s_barrier\n\t" \
    "s_add_u32 m0, s96, 36864\n\t" \
    "s_nop 0\n\t" \
    "global_load_lds_dwordx4 v201, s[44:45] offset:0\n\t" \
    "s_add_u32 m0, s96, 37760\n\t" \
    "s_nop 0\n\t" \
    "global_load_lds_dwordx4 v201, s[44:45] offset:128\n\t" \
    "s_add_u32 m0, s96, 38656\n\t" \
    "s_nop 0\n\t" \
    "global_load_lds_dwordx4 v201, s[44:45] offset:256\n\t" \
    "s_add_u32 m0, s96, 39552\n\t" \
    "s_nop 0\n\t" \
    "global_load_lds_dwordx4 v201, s[44:45] offset:384\n\t" \
    "s_add_u32 m0, s96, 40448\n\t" \
    "s_nop 0\n\t" \
    "global_load_lds_dwordx4 v201, s[44:45] offset:512\n\t" \
    "s_add_u32 m0, s96, 41344\n\t" \
    "s_nop 0\n\t" \
    "global_load_lds_dwordx4 v201, s[44:45] offset:640\n\t" \
    "s_add_u32 m0, s96, 42240\n\t" \
    "s_nop 0\n\t" \
    "global_load_lds_dwordx4 v201, s[44:45] offset:768\n\t" \
    "s_add_u32 m0, s96, 43136\n\t" \
    "s_nop 0\n\t" \
    "global_load_lds_dwordx4 v201, s[44:45] offset:896\n\t" \
    "s_add_u32 m0, s96, 44032\n\t" \
    "s_nop 0\n\t" \
    "global_load_lds_dwordx4 v201, s[44:45] offset:1024\n\t" \
    "v_add_u32 v201, 36864, v201\n\t" \
    "v_add_u32 v204, 73728, v204\n\t" \
    "v_add_u32 v205, 73728, v205\n\t" \
    "v_add_u32 v206, 73728, v206\n\t" \
    "v_add_u32 v207, 73728, v207\n\t" \
    "ds_read_b64_tr_b16 a[144:145], v204 offset:0\n\t" \
    "ds_read_b64_tr_b16 a[146:147], v204 offset:18432\n\t" \
    "ds_read_b64_tr_b16 a[160:161], v205 offset:0\n\t" \
    "ds_read_b64_tr_b16 a[162:163], v205 offset:18432\n\t" \
    "ds_read_b64_tr_b16 a[176:177], v206 offset:0\n\t" \
    "ds_read_b64_tr_b16 a[178:179], v206 offset:18432\n\t" \
    "s_waitcnt lgkmcnt(6)\n\t" \
    "ds_read_b64_tr_b16 a[192:193], v207 offset:0\n\t" \
    "ds_read_b64_tr_b16 a[194:195], v207 offset:18432\n\t" \
    "ds_read_b64_tr_b16 a[208:209], v204 offset:1024\n\t" \
    "ds_read_b64_tr_b16 a[210:211], v204 offset:19456\n\t" \
    "ds_read_b64_tr_b16 a[224:225], v205 offset:1024\n\t" \
    "ds_read_b64_tr_b16 a[226:227], v205 offset:19456\n\t" \
    "s_waitcnt lgkmcnt(6)\n\t" \
    "ds_read_b64_tr_b16 a[240:241], v206 offset:1024\n\t" \
    "ds_read_b64_tr_b16 a[242:243], v206 offset:19456\n\t" \
    "ds_read_b64_tr_b16 v[224:225], v207 offset:1024\n\t" \
    "ds_read_b64_tr_b16 v[226:227], v207 offset:19456\n\t" \
    "ds_read_b64_tr_b16 v[240:241], v204 offset:2048\n\t" \
    "ds_read_b64_tr_b16 v[242:243], v204 offset:20480\n\t" \
    "s_waitcnt lgkmcnt(6)\n\t" \
    "s_waitcnt lgkmcnt(6)\n\t" \
    "ds_read_b128 v[24:27], v2 offset:0\n\t" \
    "ds_read_b128 v[28:31], v2 offset:18432\n\t" \
    "ds_read_b128 v[32:35], v2 offset:1024\n\t" \
    "ds_read_b128 v[36:39], v2 offset:19456\n\t" \
    "s_waitcnt lgkmcnt(3)\n\t" \
    "v_mfma_f32_16x16x32_bf16 v[72:75], a[0:3], v[24:27], 0\n\t" \
    "ds_read_b128 v[40:43], v2 offset:2048\n\t" \
    "s_waitcnt lgkmcnt(3)\n\t" \
    "v_mfma_f32_16x16x32_bf16 v[76:79], a[0:3], v[28:31], 0\n\t" \
    "ds_read_b128 v[44:47], v2 offset:20480\n\t" \
    "v_mfma_f32_16x16x32_bf16 v[80:83], a[72:75], v[24:27], 0\n\t" \
    "v_mfma_f32_16x16x32_bf16 v[84:87], a[72:75], v[28:31], 0\n\t" \
    "s_waitcnt lgkmcnt(3)\n\t" \
    "v_mfma_f32_16x16x32_bf16 v[72:75], a[4:7], v[32:35], v[72:75]\n\t" \
    "ds_read_b128 v[48:51], v2 offset:3072\n\t" \
    "s_waitcnt lgkmcnt(3)\n\t" \
    "v_mfma_f32_16x16x32_bf16 v[76:79], a[4:7], v[36:39], v[76:79]\n\t" \
    "ds_read_b128 v[52:55], v2 offset:21504\n\t" \
    "v_mfma_f32_16x16x32_bf16 v[80:83], a[76:79], v[32:35], v[80:83]\n\t" \
    "v_mfma_f32_16x16x32_bf16 v[84:87], a[76:79], v[36:39], v[84:87]\n\t" \
    "s_waitcnt lgkmcnt(3)\n\t" \
    "v_mfma_f32_16x16x32_bf16 v[72:75], a[8:11], v[40:43], v[72:75]\n\t" \
    "ds_read_b128 v[56:59], v2 offset:4096\n\t" \
    "s_waitcnt lgkmcnt(3)\n\t" \
    "v_mfma_f32_16x16x32_bf16 v[76:79], a[8:11], v[44:47], v[76:79]\n\t" \
    "ds_read_b128 v[60:63], v2 offset:22528\n\t" \
    "v_mfma_f32_16x16x32_bf16 v[80:83], a[80:83], v[40:43], v[80:83]\n\t" \
    "v_mfma_f32_16x16x32_bf16 v[84:87], a[80:83], v[44:47], v[84:87]\n\t" \
    "s_waitcnt lgkmcnt(3)\n\t" \
    "v_mfma_f32_16x16x32_bf16 v[72:75], a[12:15], v[48:51], v[72:75]\n\t" \
    "ds_read_b128 v[64:67], v2 offset:5120\n\t" \
    "s_waitcnt lgkmcnt(3)\n\t" \
    "v_mfma_f32_16x16x32_bf16 v[76:79], a[12:15], v[52:55], v[76:79]\n\t" \
    "ds_read_b128 v[68:71], v2 offset:23552\n\t" \
    "v_mfma_f32_16x16x32_bf16 v[80:83], a[84:87], v[48:51], v[80:83]\n\t" \
    "v_mfma_f32_16x16x32_bf16 v[84:87], a[84:87], v[52:55], v[84:87]\n\t" \
    "s_waitcnt lgkmcnt(3)\n\t" \
    "v_mfma_f32_16x16x32_bf16 v[72:75], a[16:19], v[56:59], v[72:75]\n\t" \
    "s_waitcnt lgkmcnt(2)\n\t" \
    "v_mfma_f32_16x16x32_bf16 v[76:79], a[16:19], v[60:63], v[76:79]\n\t" \
    "v_mfma_f32_16x16x32_bf16 v[80:83], a[88:91], v[56:59], v[80:83]\n\t" \
    "v_mfma_f32_16x16x32_bf16 v[84:87], a[88:91], v[60:63], v[84:87]\n\t" \
    "s_waitcnt lgkmcnt(1)\n\t" \
    "v_mfma_f32_16x16x32_bf16 v[72:75], a[20:23], v[64:67], v[72:75]\n\t" \
    "s_waitcnt lgkmcnt(0)\n\t" \
    "v_mfma_f32_16x16x32_bf16 v[76:79], a[20:23], v[68:71], v[76:79]\n\t" \
    "v_mfma_f32_16x16x32_bf16 v[80:83], a[92:95], v[64:67], v[80:83]\n\t" \
    "v_mfma_f32_16x16x32_bf16 v[84:87], a[92:95], v[68:71], v[84:87]\n\t" \
    "s_waitcnt vmcnt(9) lgkmcnt(0)\n\t" \
    "s_barrier\n\t" \
    "s_add_u32 m0, s96, 73728\n\t" \
    "s_nop 0\n\t" \
    "global_load_lds_dwordx4 v201, s[44:45] offset:0\n\t" \
    "s_add_u32 m0, s96, 74624\n\t" \
    "s_nop 0\n\t" \
    "global_load_lds_dwordx4 v201, s[44:45] offset:128\n\t" \
    "s_add_u32 m0, s96, 75520\n\t" \
    "s_nop 0\n\t" \
    "global_load_lds_dwordx4 v201, s[44:45] offset:256\n\t" \
    "s_add_u32 m0, s96, 76416\n\t" \
    "s_nop 0\n\t" \
    "global_load_lds_dwordx4 v201, s[44:45] offset:384\n\t" \
    "s_add_u32 m0, s96, 77312\n\t" \
    "s_nop 0\n\t" \
    "global_load_lds_dwordx4 v201, s[44:45] offset:512\n\t" \
    "s_add_u32 m0, s96, 78208\n\t" \
    "s_nop 0\n\t" \
    "global_load_lds_dwordx4 v201, s[44:45] offset:640\n\t" \
    "s_add_u32 m0, s96, 79104\n\t" \
    "s_nop 0\n\t" \
    "global_load_lds_dwordx4 v201, s[44:45] offset:768\n\t" \
    "s_add_u32 m0, s96, 80000\n\t" \
    "s_nop 0\n\t" \
    "global_load_lds_dwordx4 v201, s[44:45] offset:896\n\t" \
    "s_add_u32 m0, s96, 80896\n\t" \
    "s_nop 0\n\t" \
    "global_load_lds_dwordx4 v201, s[44:45] offset:1024\n\t" \
    "v_add_u32 v201, 36864, v201\n\t" \
    "v_mbcnt_lo_u32_b32 v164, -1, 0\n\t" \
    "v_mbcnt_hi_u32_b32 v164, -1, v164\n\t" \
    "s_cmp_eq_u32 s66, 0\n\t" \
    "s_cselect_b32 s69, 59, 11\n\t" \
    "v_min_u32 v164, s69, v164\n\t" \
    "v_lshlrev_b32 v164, 6, v164\n\t" \
    "v_add_u32 v164, 6258688, v164\n\t" \
    "v_mov_b32 v165, 6266880\n\t" \
    "global_load_dword v165, v165, s[36:37] sc1\n\t" \
    "global_load_dwordx2 v[166:167], v164, s[36:37] sc1\n\t" \
    "ds_read_b64_tr_b16 a[148:149], v204 offset:36864\n\t" \
    "ds_read_b64_tr_b16 a[150:151], v204 offset:55296\n\t" \
    "ds_read_b64_tr_b16 a[164:165], v205 offset:36864\n\t" \
    "ds_read_b64_tr_b16 a[166:167], v205 offset:55296\n\t" \
    "ds_read_b64_tr_b16 a[180:181], v206 offset:36864\n\t" \
    "ds_read_b64_tr_b16 a[182:183], v206 offset:55296\n\t" \
    "s_waitcnt lgkmcnt(6)\n\t" \
    "ds_read_b64_tr_b16 a[196:197], v207 offset:36864\n\t" \
    "ds_read_b64_tr_b16 a[198:199], v207 offset:55296\n\t" \
    "ds_read_b64_tr_b16 a[212:213], v204 offset:37888\n\t" \
    "ds_read_b64_tr_b16 a[214:215], v204 offset:56320\n\t" \
    "ds_read_b64_tr_b16 a[228:229], v205 offset:37888\n\t" \
    "ds_read_b64_tr_b16 a[230:231], v205 offset:56320\n\t" \
    "s_waitcnt lgkmcnt(6)\n\t" \
    "ds_read_b64_tr_b16 a[244:245], v206 offset:37888\n\t" \
    "ds_read_b64_tr_b16 a[246:247], v206 offset:56320\n\t" \
    "ds_read_b64_tr_b16 v[228:229], v207 offset:37888\n\t" \
    "ds_read_b64_tr_b16 v[230:231], v207 offset:56320\n\t" \
    "ds_read_b64_tr_b16 v[244:245], v204 offset:38912\n\t" \
    "ds_read_b64_tr_b16 v[246:247], v204 offset:57344\n\t" \
    "s_waitcnt lgkmcnt(6)\n\t" \
    "s_waitcnt lgkmcnt(6)\n\t" \
    "ds_read_b128 v[24:27], v2 offset:6144\n\t" \
    "ds_read_b128 v[28:31], v2 offset:24576\n\t" \
    "ds_read_b128 v[32:35], v2 offset:7168\n\t" \
    "ds_read_b128 v[36:39], v2 offset:25600\n\t" \
    "s_waitcnt lgkmcnt(3)\n\t" \
    "v_mfma_f32_16x16x32_bf16 v[72:75], a[24:27], v[24:27], v[72:75]\n\t" \
    "ds_read_b128 v[40:43], v2 offset:8192\n\t" \
    "s_waitcnt lgkmcnt(3)\n\t" \
    "v_mfma_f32_16x16x32_bf16 v[76:79], a[24:27], v[28:31], v[76:79]\n\t" \
    "ds_read_b128 v[44:47], v2 offset:26624\n\t" \
    "v_mfma_f32_16x16x32_bf16 v[80:83], a[96:99], v[24:27], v[80:83]\n\t" \
    "v_mfma_f32_16x16x32_bf16 v[84:87], a[96:99], v[28:31], v[84:87]\n\t" \
    "s_waitcnt lgkmcnt(3)\n\t" \
    "v_mfma_f32_16x16x32_bf16 v[72:75], a[28:31], v[32:35], v[72:75]\n\t" \
    "ds_read_b128 v[48:51], v2 offset:9216\n\t" \
    "s_waitcnt lgkmcnt(3)\n\t" \
    "v_mfma_f32_16x16x32_bf16 v[76:79], a[28:31], v[36:39], v[76:79]\n\t" \
    "ds_read_b128 v[52:55], v2 offset:27648\n\t" \
    "v_mfma_f32_16x16x32_bf16 v[80:83], a[100:103], v[32:35], v[80:83]\n\t" \
    "v_mfma_f32_16x16x32_bf16 v[84:87], a[100:103], v[36:39], v[84:87]\n\t" \
    "s_waitcnt lgkmcnt(3)\n\t" \
    "v_mfma_f32_16x16x32_bf16 v[72:75], a[32:35], v[40:43], v[72:75]\n\t" \
    "ds_read_b128 v[56:59], v2 offset:10240\n\t" \
    "s_waitcnt lgkmcnt(3)\n\t" \
    "v_mfma_f32_16x16x32_bf16 v[76:79], a[32:35], v[44:47], v[76:79]\n\t" \
    "ds_read_b128 v[60:63], v2 offset:28672\n\t" \
    "v_mfma_f32_16x16x32_bf16 v[80:83], a[104:107], v[40:43], v[80:83]\n\t" \
    "v_mfma_f32_16x16x32_bf16 v[84:87], a[104:107], v[44:47], v[84:87]\n\t" \
    "s_waitcnt lgkmcnt(3)\n\t" \
    "v_mfma_f32_16x16x32_bf16 v[72:75], a[36:39], v[48:51], v[72:75]\n\t" \
    "ds_read_b128 v[64:67], v2 offset:11264\n\t" \
    "s_waitcnt lgkmcnt(3)\n\t" \
    "v_mfma_f32_16x16x32_bf16 v[76:79], a[36:39], v[52:55], v[76:79]\n\t" \
    "ds_read_b128 v[68:71], v2 offset:29696\n\t" \
    "v_mfma_f32_16x16x32_bf16 v[80:83], a[108:111], v[48:51], v[80:83]\n\t" \
    "v_mfma_f32_16x16x32_bf16 v[84:87], a[108:111], v[52:55], v[84:87]\n\t" \
    "s_waitcnt lgkmcnt(3)\n\t" \
    "v_mfma_f32_16x16x32_bf16 v[72:75], a[40:43], v[56:59], v[72:75]\n\t" \
    "s_waitcnt lgkmcnt(2)\n\t" \
    "v_mfma_f32_16x16x32_bf16 v[76:79], a[40:43], v[60:63], v[76:79]\n\t" \
    "v_mfma_f32_16x16x32_bf16 v[80:83], a[112:115], v[56:59], v[80:83]\n\t" \
    "v_mfma_f32_16x16x32_bf16 v[84:87], a[112:115], v[60:63], v[84:87]\n\t" \
    "s_waitcnt lgkmcnt(1)\n\t" \
    "v_mfma_f32_16x16x32_bf16 v[72:75], a[44:47], v[64:67], v[72:75]\n\t" \
    "s_waitcnt lgkmcnt(0)\n\t" \
    "v_mfma_f32_16x16x32_bf16 v[76:79], a[44:47], v[68:71], v[76:79]\n\t" \
    "v_mfma_f32_16x16x32_bf16 v[80:83], a[116:119], v[64:67], v[80:83]\n\t" \
    "v_mfma_f32_16x16x32_bf16 v[84:87], a[116:119], v[68:71], v[84:87]\n\t" \
    "s_waitcnt vmcnt(11) lgkmcnt(0)\n\t" \
    "s_barrier\n\t" \
    "s_add_u32 s50, s50, 73728\n\t" \
    "s_addc_u32 s51, s51, 0\n\t" \
    "s_add_u32 m0, s89, 110592\n\t" \
    "s_nop 0\n\t" \
    "global_load_lds_dwordx4 v1, s[50:51] offset:0\n\t" \
    "s_add_u32 m0, s89, 111552\n\t" \
    "s_nop 0\n\t" \
    "global_load_lds_dwordx4 v1, s[50:51] offset:64\n\t" \
    "s_add_u32 m0, s89, 112512\n\t" \
    "s_nop 0\n\t" \
    "global_load_lds_dwordx4 v1, s[50:51] offset:128\n\t" \
    "s_add_u32 m0, s89, 113472\n\t" \
    "s_nop 0\n\t" \
    "global_load_lds_dwordx4 v1, s[50:51] offset:192\n\t" \
    "s_add_u32 m0, s89, 114432\n\t" \
    "s_nop 0\n\t" \
    "global_load_lds_dwordx4 v1, s[50:51] offset:256\n\t" \
    "s_add_u32 m0, s89, 115392\n\t" \
    "s_nop 0\n\t" \
    "global_load_lds_dwordx4 v1, s[50:51] offset:320\n\t" \
    "s_add_u32 m0, s89, 116352\n\t" \
    "s_nop 0\n\t" \
    "global_load_lds_dwordx4 v1, s[50:51] offset:384\n\t" \
    "s_add_u32 m0, s89, 117312\n\t" \
    "s_nop 0\n\t" \
    "global_load_lds_dwordx4 v1, s[50:51] offset:448\n\t" \
    "s_add_u32 m0, s89, 118272\n\t" \
    "s_nop 0\n\t" \
    "global_load_lds_dwordx4 v1, s[50:51] offset:512\n\t" \
    "v_add_u32 v204, -36864, v204\n\t" \
    "v_add_u32 v205, -36864, v205\n\t" \
    "v_add_u32 v206, -36864, v206\n\t" \
    "v_add_u32 v207, -36864, v207\n\t" \
    "ds_read_b64_tr_b16 a[152:153], v204 offset:0\n\t" \
    "ds_read_b64_tr_b16 a[154:155], v204 offset:18432\n\t" \
    "ds_read_b64_tr_b16 a[168:169], v205 offset:0\n\t" \
    "ds_read_b64_tr_b16 a[170:171], v205 offset:18432\n\t" \
    "ds_read_b64_tr_b16 a[184:185], v206 offset:0\n\t" \
    "ds_read_b64_tr_b16 a[186:187], v206 offset:18432\n\t" \
    "s_waitcnt lgkmcnt(6)\n\t" \
    "ds_read_b64_tr_b16 a[200:201], v207 offset:0\n\t" \
    "ds_read_b64_tr_b16 a[202:203], v207 offset:18432\n\t" \
    "ds_read_b64_tr_b16 a[216:217], v204 offset:1024\n\t" \
    "ds_read_b64_tr_b16 a[218:219], v204 offset:19456\n\t" \
    "ds_read_b64_tr_b16 a[232:233], v205 offset:1024\n\t" \
    "ds_read_b64_tr_b16 a[234:235], v205 offset:19456\n\t" \
    "s_waitcnt lgkmcnt(6)\n\t" \
    "ds_read_b64_tr_b16 a[248:249], v206 offset:1024\n\t" \
    "ds_read_b64_tr_b16 a[250:251], v206 offset:19456\n\t" \
    "ds_read_b64_tr_b16 v[232:233], v207 offset:1024\n\t" \
    "ds_read_b64_tr_b16 v[234:235], v207 offset:19456\n\t" \
    "ds_read_b64_tr_b16 v[248:249], v204 offset:2048\n\t" \
    "ds_read_b64_tr_b16 v[250:251], v204 offset:20480\n\t" \
    "s_waitcnt lgkmcnt(6)\n\t" \
    "s_waitcnt lgkmcnt(6)\n\t" \
    "ds_read_b128 v[24:27], v2 offset:12288\n\t" \
    "ds_read_b128 v[28:31], v2 offset:30720\n\t" \
    "ds_read_b128 v[32:35], v2 offset:13312\n\t" \
    "ds_read_b128 v[36:39], v2 offset:31744\n\t" \
    "s_waitcnt lgkmcnt(3)\n\t" \
    "v_mfma_f32_16x16x32_bf16 v[72:75], a[48:51], v[24:27], v[72:75]\n\t" \
    "ds_read_b128 v[40:43], v2 offset:14336\n\t" \
    "s_waitcnt lgkmcnt(3)\n\t" \
    "v_mfma_f32_16x16x32_bf16 v[76:79], a[48:51], v[28:31], v[76:79]\n\t" \
    "ds_read_b128 v[44:47], v2 offset:32768\n\t" \
    "v_mfma_f32_16x16x32_bf16 v[80:83], a[120:123], v[24:27], v[80:83]\n\t" \
    "v_mfma_f32_16x16x32_bf16 v[84:87], a[120:123], v[28:31], v[84:87]\n\t" \
    "s_waitcnt lgkmcnt(3)\n\t" \
    "v_mfma_f32_16x16x32_bf16 v[72:75], a[52:55], v[32:35], v[72:75]\n\t" \
    "ds_read_b128 v[48:51], v2 offset:15360\n\t" \
    "s_waitcnt lgkmcnt(3)\n\t" \
    "v_mfma_f32_16x16x32_bf16 v[76:79], a[52:55], v[36:39], v[76:79]\n\t" \
    "ds_read_b128 v[52:55], v2 offset:33792\n\t" \
    "v_mfma_f32_16x16x32_bf16 v[80:83], a[124:127], v[32:35], v[80:83]\n\t" \
    "v_mfma_f32_16x16x32_bf16 v[84:87], a[124:127], v[36:39], v[84:87]\n\t" \
    "s_waitcnt lgkmcnt(3)\n\t" \
    "v_mfma_f32_16x16x32_bf16 v[72:75], a[56:59], v[40:43], v[72:75]\n\t" \
    "ds_read_b128 v[56:59], v2 offset:16384\n\t" \
    "s_waitcnt lgkmcnt(3)\n\t" \
    "v_mfma_f32_16x16x32_bf16 v[76:79], a[56:59], v[44:47], v[76:79]\n\t" \
    "ds_read_b128 v[60:63], v2 offset:34816\n\t" \
    "v_mfma_f32_16x16x32_bf16 v[80:83], a[128:131], v[40:43], v[80:83]\n\t" \
    "v_mfma_f32_16x16x32_bf16 v[84:87], a[128:131], v[44:47], v[84:87]\n\t" \
    "s_waitcnt lgkmcnt(3)\n\t" \
    "v_mfma_f32_16x16x32_bf16 v[72:75], a[60:63], v[48:51], v[72:75]\n\t" \
    "ds_read_b128 v[64:67], v2 offset:17408\n\t" \
    "s_waitcnt lgkmcnt(3)\n\t" \
    "v_mfma_f32_16x16x32_bf16 v[76:79], a[60:63], v[52:55], v[76:79]\n\t" \
    "ds_read_b128 v[68:71], v2 offset:35840\n\t" \
    "v_mfma_f32_16x16x32_bf16 v[80:83], a[132:135], v[48:51], v[80:83]\n\t" \
    "v_mfma_f32_16x16x32_bf16 v[84:87], a[132:135], v[52:55], v[84:87]\n\t" \
    "s_waitcnt lgkmcnt(3)\n\t" \
    "v_mfma_f32_16x16x32_bf16 v[72:75], a[64:67], v[56:59], v[72:75]\n\t" \
    "s_waitcnt lgkmcnt(2)\n\t" \
    "v_mfma_f32_16x16x32_bf16 v[76:79], a[64:67], v[60:63], v[76:79]\n\t" \
    "v_mfma_f32_16x16x32_bf16 v[80:83], a[136:139], v[56:59], v[80:83]\n\t" \
    "v_mfma_f32_16x16x32_bf16 v[84:87], a[136:139], v[60:63], v[84:87]\n\t" \
    "s_waitcnt lgkmcnt(1)\n\t" \
    "v_mfma_f32_16x16x32_bf16 v[72:75], a[68:71], v[64:67], v[72:75]\n\t" \
    "s_waitcnt lgkmcnt(0)\n\t" \
    "v_mfma_f32_16x16x32_bf16 v[76:79], a[68:71], v[68:71], v[76:79]\n\t" \
    "v_mfma_f32_16x16x32_bf16 v[80:83], a[140:143], v[64:67], v[80:83]\n\t" \
    "v_mfma_f32_16x16x32_bf16 v[84:87], a[140:143], v[68:71], v[84:87]\n\t" \
    "s_waitcnt vmcnt(11) lgkmcnt(0)\n\t" \
    "s_barrier\n\t" \
    "ds_read_b64_tr_b16 a[156:157], v204 offset:36864\n\t" \
    "ds_read_b64_tr_b16 a[158:159], v204 offset:55296\n\t" \
    "ds_read_b64_tr_b16 a[172:173], v205 offset:36864\n\t" \
    "ds_read_b64_tr_b16 a[174:175], v205 offset:55296\n\t" \
    "ds_read_b64_tr_b16 a[188:189], v206 offset:36864\n\t" \
    "ds_read_b64_tr_b16 a[190:191], v206 offset:55296\n\t" \
    "s_waitcnt lgkmcnt(6)\n\t" \
    "ds_read_b64_tr_b16 a[204:205], v207 offset:36864\n\t" \
    "ds_read_b64_tr_b16 a[206:207], v207 offset:55296\n\t" \
    "ds_read_b64_tr_b16 a[220:221], v204 offset:37888\n\t" \
    "ds_read_b64_tr_b16 a[222:223], v204 offset:56320\n\t" \
    "ds_read_b64_tr_b16 a[236:237], v205 offset:37888\n\t" \
    "ds_read_b64_tr_b16 a[238:239], v205 offset:56320\n\t" \
    "s_waitcnt lgkmcnt(6)\n\t" \
    "ds_read_b64_tr_b16 a[252:253], v206 offset:37888\n\t" \
    "ds_read_b64_tr_b16 a[254:255], v206 offset:56320\n\t" \
    "ds_read_b64_tr_b16 v[236:237], v207 offset:37888\n\t" \
    "ds_read_b64_tr_b16 v[238:239], v207 offset:56320\n\t" \
    "ds_read_b64_tr_b16 v[252:253], v204 offset:38912\n\t" \
    "ds_read_b64_tr_b16 v[254:255], v204 offset:57344\n\t" \
    "s_waitcnt lgkmcnt(6)\n\t" \
    "s_waitcnt vmcnt(9) lgkmcnt(0)\n\t" \
    "s_barrier\n\t" \
    "v_sub_u32 v166, v166, v165\n\t" \
    "v_cmp_ne_u32 vcc, 1, v166\n\t" \
    "s_cbranch_vccz gv_Cok2_%=\n\t" \
    "gv_C1_%=:\n\t" \
    "global_load_dwordx2 v[166:167], v164, s[36:37] sc1\n\t" \
    "s_waitcnt vmcnt(0)\n\t" \
    "v_sub_u32 v166, v166, v165\n\t" \
    "v_cmp_ne_u32 vcc, 1, v166\n\t" \
    "s_cbranch_vccz gv_Cok2_%=\n\t" \
    "s_cmp_eq_u32 s68, 0\n\t" \
    "s_cbranch_scc1 gv_F3_%=\n\t" \
    "s_sub_u32 s68, s68, 1\n\t" \
    "s_sleep 4\n\t" \
    "s_branch gv_C1_%=\n\t" \
    "gv_F3_%=:\n\t" \
    "v_mov_b32 v168, 6095104\n\t" \
    "v_mov_b32 v169, 4\n\t" \
    "global_store_dword v168, v169, s[36:37] sc1\n\t" \
    "s_waitcnt vmcnt(0)\n\t" \
    "s_branch gv_Cok2_%=\n\t" \
    "gv_Cok2_%=:\n\t" \
    "s_cmp_lg_u32 s66, 0\n\t" \
    "s_cbranch_scc1 gv_Ca4_%=\n\t" \
    "v_mov_b32 v164, 6176768\n\t" \
    "s_mov_b64 exec, 1\n\t" \
    "global_atomic_add v164, v166, s[36:37] sc1\n\t" \
    "s_mov_b64 exec, -1\n\t" \
    "gv_Ca4_%=:\n\t" \
    "v_min_u32_dpp v167, v167, v167 row_shl:6 row_mask:0xf bank_mask:0xf\n\t" \
    "s_nop 0\n\t" \
    "v_readlane_b32 s69, v167, 4\n\t" \
    "v_readlane_b32 s70, v167, 5\n\t" \
    "s_nop 1\n\t" \
    "s_cmp_lt_u32 s70, s69\n\t" \
    "s_cselect_b32 s52, 8192, 0\n\t" \
    "s_xor_b32 s52, s52, 8192\n\t" \
    "v_xor_b32 v20, s52, v20\n\t" \
    "s_lshr_b32 s69, s67, 5\n\t" \
    "s_or_b32 s69, s69, s66\n\t" \
    "s_cbranch_scc1 gv_npr5_%=\n\t" \
    "s_memrealtime s[54:55]\n\t" \
    "s_waitcnt lgkmcnt(0)\n\t" \
    "s_lshl_b32 s70, s54, 4\n\t" \
    "s_add_u32 s70, s70, 3840\n\t" \
    "v_mov_b32 v157, s70\n\t" \
    "s_mov_b32 exec_lo, 0xf00f0\n\t" \
    "s_mov_b32 exec_hi, 0xf00f0\n\t" \
    "global_store_dword v19, v157, s[36:37] sc1\n\t" \
    "s_mov_b64 exec, -1\n\t" \
    "gv_npr5_%=:\n\t" \
    "s_cmp_lt_u32 s65, 60\n\t" \
    "s_cselect_b32 s69, 73728, 0\n\t" \
    "s_add_u32 s50, s50, s69\n\t" \
    "s_addc_u32 s51, s51, 0\n\t" \
    "s_sub_u32 s90, s90, 36864\n\t" \
    "s_cselect_b32 s90, 110592, s90\n\t" \
    "s_sub_u32 s91, s90, 36864\n\t" \
    "s_cselect_b32 s91, 110592, s91\n\t" \
    "s_add_u32 s91, s91, s89\n\t" \
    "v_add_u32 v17, s90, v3\n\t" \
    "s_lshl_b32 s40, s65, 15\n\t" \
    "v_mov_b32 v2, v17\n\t" \
    "s_mov_b64 s[46:47], 0\n\t" \
    "s_branch gv_mid8_%=\n\t" \
    ".p2align 6\n\t" \
    "gv_al9_%=:\n\t" \
    "s_nop 0\n\t" \
    "s_nop 0\n\t" \
    "gv_loop6_%=:\n\t" \
    "s_waitcnt lgkmcnt(5)\n\t" \
    "v_mfma_f32_16x16x32_bf16 v[72:75], a[0:3], v[24:27], 0\n\t" \
    "ds_read_b128 v[48:51], v2 offset:3072\n\t" \
    "s_waitcnt lgkmcnt(5)\n\t" \
    "v_mfma_f32_16x16x32_bf16 v[76:79], a[0:3], v[28:31], 0\n\t" \
    "ds_read_b128 v[52:55], v2 offset:21504\n\t" \
    "v_mfma_f32_16x16x32_bf16 v[80:83], a[72:75], v[24:27], 0\n\t" \
    "s_cmp_lt_u32 s65, 60\n\t" \
    "s_cselect_b32 s69, 73728, 0\n\t" \
    "s_add_u32 s50, s50, s69\n\t" \
    "v_mfma_f32_16x16x32_bf16 v[84:87], a[72:75], v[28:31], 0\n\t" \
    "s_addc_u32 s51, s51, 0\n\t" \
    "s_sub_u32 s90, s90, 36864\n\t" \
    "s_cselect_b32 s90, 110592, s90\n\t" \
    "s_waitcnt lgkmcnt(5)\n\t" \
    "v_mfma_f32_16x16x32_bf16 v[72:75], a[4:7], v[32:35], v[72:75]\n\t" \
    "ds_read_b128 v[56:59], v2 offset:4096\n\t" \
    "s_waitcnt lgkmcnt(5)\n\t" \
    "v_mfma_f32_16x16x32_bf16 v[76:79], a[4:7], v[36:39], v[76:79]\n\t" \
    "ds_read_b128 v[60:63], v2 offset:22528\n\t" \
    "v_mfma_f32_16x16x32_bf16 v[80:83], a[76:79], v[32:35], v[80:83]\n\t" \
    "s_sub_u32 s91, s90, 36864\n\t" \
    "s_cselect_b32 s91, 110592, s91\n\t" \
    "s_add_u32 s91, s91, s89\n\t" \
    "v_mfma_f32_16x16x32_bf16 v[84:87], a[76:79], v[36:39], v[84:87]\n\t" \
    "v_add_u32 v17, s90, v3\n\t" \
    "s_lshl_b32 s40, s65, 15\n\t" \
    "s_sub_u32 s70, s65, 4\n\t" \
    "s_waitcnt lgkmcnt(5)\n\t" \
    "v_mfma_f32_16x16x32_bf16 v[72:75], a[8:11], v[40:43], v[72:75]\n\t" \
    "ds_read_b128 v[64:67], v2 offset:5120\n\t" \
    "s_waitcnt lgkmcnt(5)\n\t" \
    "v_mfma_f32_16x16x32_bf16 v[76:79], a[8:11], v[44:47], v[76:79]\n\t" \
    "ds_read_b128 v[68:71], v2 offset:23552\n\t" \
    "v_mfma_f32_16x16x32_bf16 v[80:83], a[80:83], v[40:43], v[80:83]\n\t" \
    "v_mov_b32 v157, s70\n\t" \
    "s_cmp_lt_u32 s65, 14\n\t" \
    "s_cselect_b64 s[46:47], 0, s[58:59]\n\t" \
    "v_mfma_f32_16x16x32_bf16 v[84:87], a[80:83], v[44:47], v[84:87]\n\t" \
    "s_waitcnt lgkmcnt(5)\n\t" \
    "v_mfma_f32_16x16x32_bf16 v[72:75], a[12:15], v[48:51], v[72:75]\n\t" \
    "ds_read_b128 v[24:27], v2 offset:6144\n\t" \
    "s_waitcnt lgkmcnt(5)\n\t" \
    "v_mfma_f32_16x16x32_bf16 v[76:79], a[12:15], v[52:55], v[76:79]\n\t" \
    "ds_read_b128 v[28:31], v2 offset:24576\n\t" \
    "v_mfma_f32_16x16x32_bf16 v[80:83], a[84:87], v[48:51], v[80:83]\n\t" \
    "s_add_u32 m0, s91, 0\n\t" \
    "v_mfma_f32_16x16x32_bf16 v[84:87], a[84:87], v[52:55], v[84:87]\n\t" \
    "global_load_lds_dwordx4 v1, s[50:51] offset:0\n\t" \
    "s_waitcnt lgkmcnt(5)\n\t" \
    "v_mfma_f32_16x16x32_bf16 v[72:75], a[16:19], v[56:59], v[72:75]\n\t" \
    "ds_read_b128 v[32:35], v2 offset:7168\n\t" \
    "s_waitcnt lgkmcnt(5)\n\t" \
    "v_mfma_f32_16x16x32_bf16 v[76:79], a[16:19], v[60:63], v[76:79]\n\t" \
    "ds_read_b128 v[36:39], v2 offset:25600\n\t" \
    "v_mfma_f32_16x16x32_bf16 v[80:83], a[88:91], v[56:59], v[80:83]\n\t" \
    "s_add_u32 m0, s91, 960\n\t" \
    "v_mfma_f32_16x16x32_bf16 v[84:87], a[88:91], v[60:63], v[84:87]\n\t" \
    "global_load_lds_dwordx4 v1, s[50:51] offset:64\n\t" \
    "s_waitcnt lgkmcnt(5)\n\t" \
    "v_mfma_f32_16x16x32_bf16 v[72:75], a[20:23], v[64:67], v[72:75]\n\t" \
    "ds_read_b128 v[40:43], v2 offset:8192\n\t" \
    "s_waitcnt lgkmcnt(5)\n\t" \
    "v_mfma_f32_16x16x32_bf16 v[76:79], a[20:23], v[68:71], v[76:79]\n\t" \
    "ds_read_b128 v[44:47], v2 offset:26624\n\t" \
    "v_mfma_f32_16x16x32_bf16 v[80:83], a[92:95], v[64:67], v[80:83]\n\t" \
    "s_add_u32 m0, s91, 1920\n\t" \
    "v_mfma_f32_16x16x32_bf16 v[84:87], a[92:95], v[68:71], v[84:87]\n\t" \
    "global_load_lds_dwordx4 v1, s[50:51] offset:128\n\t" \
    "s_waitcnt lgkmcnt(5)\n\t" \
    "v_mfma_f32_16x16x32_bf16 v[72:75], a[24:27], v[24:27], v[72:75]\n\t" \
    "ds_read_b128 v[48:51], v2 offset:9216\n\t" \
    "s_waitcnt lgkmcnt(5)\n\t" \
    "v_mfma_f32_16x16x32_bf16 v[76:79], a[24:27], v[28:31], v[76:79]\n\t" \
    "ds_read_b128 v[52:55], v2 offset:27648\n\t" \
    "v_mfma_f32_16x16x32_bf16 v[80:83], a[96:99], v[24:27], v[80:83]\n\t" \
    "s_add_u32 m0, s91, 2880\n\t" \
    "v_mfma_f32_16x16x32_bf16 v[84:87], a[96:99], v[28:31], v[84:87]\n\t" \
    "global_load_lds_dwordx4 v1, s[50:51] offset:192\n\t" \
    "s_waitcnt lgkmcnt(5)\n\t" \
    "v_mfma_f32_16x16x32_bf16 v[72:75], a[28:31], v[32:35], v[72:75]\n\t" \
    "ds_read_b128 v[56:59], v2 offset:10240\n\t" \
    "s_waitcnt lgkmcnt(5)\n\t" \
    "v_mfma_f32_16x16x32_bf16 v[76:79], a[28:31], v[36:39], v[76:79]\n\t" \
    "ds_read_b128 v[60:63], v2 offset:28672\n\t" \
    "v_mfma_f32_16x16x32_bf16 v[80:83], a[100:103], v[32:35], v[80:83]\n\t" \
    "s_add_u32 m0, s91, 3840\n\t" \
    "v_mfma_f32_16x16x32_bf16 v[84:87], a[100:103], v[36:39], v[84:87]\n\t" \
    "global_load_lds_dwordx4 v1, s[50:51] offset:256\n\t" \
    "s_waitcnt lgkmcnt(5)\n\t" \
    "v_mfma_f32_16x16x32_bf16 v[72:75], a[32:35], v[40:43], v[72:75]\n\t" \
    "ds_read_b128 v[64:67], v2 offset:11264\n\t" \
    "s_waitcnt lgkmcnt(5)\n\t" \
    "v_mfma_f32_16x16x32_bf16 v[76:79], a[32:35], v[44:47], v[76:79]\n\t" \
    "ds_read_b128 v[68:71], v2 offset:29696\n\t" \
    "v_mfma_f32_16x16x32_bf16 v[80:83], a[104:107], v[40:43], v[80:83]\n\t" \
    "s_add_u32 m0, s91, 4800\n\t" \
    "v_mfma_f32_16x16x32_bf16 v[84:87], a[104:107], v[44:47], v[84:87]\n\t" \
    "global_load_lds_dwordx4 v1, s[50:51] offset:320\n\t" \
    "s_waitcnt lgkmcnt(5)\n\t" \
    "v_mfma_f32_16x16x32_bf16 v[72:75], a[36:39], v[48:51], v[72:75]\n\t" \
    "ds_read_b128 v[24:27], v2 offset:12288\n\t" \
    "s_waitcnt lgkmcnt(5)\n\t" \
    "v_mfma_f32_16x16x32_bf16 v[76:79], a[36:39], v[52:55], v[76:79]\n\t" \
    "ds_read_b128 v[28:31], v2 offset:30720\n\t" \
    "v_mfma_f32_16x16x32_bf16 v[80:83], a[108:111], v[48:51], v[80:83]\n\t" \
    "s_add_u32 m0, s91, 5760\n\t" \
    "v_mfma_f32_16x16x32_bf16 v[84:87], a[108:111], v[52:55], v[84:87]\n\t" \
    "global_load_lds_dwordx4 v1, s[50:51] offset:384\n\t" \
    "s_waitcnt lgkmcnt(5)\n\t" \
    "v_mfma_f32_16x16x32_bf16 v[72:75], a[40:43], v[56:59], v[72:75]\n\t" \
    "ds_read_b128 v[32:35], v2 offset:13312\n\t" \
    "s_waitcnt lgkmcnt(5)\n\t" \
    "v_mfma_f32_16x16x32_bf16 v[76:79], a[40:43], v[60:63], v[76:79]\n\t" \
    "ds_read_b128 v[36:39], v2 offset:31744\n\t" \
    "v_mfma_f32_16x16x32_bf16 v[80:83], a[112:115], v[56:59], v[80:83]\n\t" \
    "s_add_u32 m0, s91, 6720\n\t" \
    "v_mfma_f32_16x16x32_bf16 v[84:87], a[112:115], v[60:63], v[84:87]\n\t" \
    "global_load_lds_dwordx4 v1, s[50:51] offset:448\n\t" \
    "s_waitcnt lgkmcnt(5)\n\t" \
    "v_mfma_f32_16x16x32_bf16 v[72:75], a[44:47], v[64:67], v[72:75]\n\t" \
    "ds_read_b128 v[40:43], v2 offset:14336\n\t" \
    "s_waitcnt lgkmcnt(5)\n\t" \
    "v_mfma_f32_16x16x32_bf16 v[76:79], a[44:47], v[68:71], v[76:79]\n\t" \
    "ds_read_b128 v[44:47], v2 offset:32768\n\t" \
    "v_mfma_f32_16x16x32_bf16 v[80:83], a[116:119], v[64:67], v[80:83]\n\t" \
    "s_add_u32 m0, s91, 7680\n\t" \
    "v_mfma_f32_16x16x32_bf16 v[84:87], a[116:119], v[68:71], v[84:87]\n\t" \
    "global_load_lds_dwordx4 v1, s[50:51] offset:512\n\t" \
    "s_waitcnt lgkmcnt(5)\n\t" \
    "v_mfma_f32_16x16x32_bf16 v[72:75], a[48:51], v[24:27], v[72:75]\n\t" \
    "ds_read_b128 v[48:51], v2 offset:15360\n\t" \
    "s_waitcnt lgkmcnt(5)\n\t" \
    "v_mfma_f32_16x16x32_bf16 v[76:79], a[48:51], v[28:31], v[76:79]\n\t" \
    "ds_read_b128 v[52:55], v2 offset:33792\n\t" \
    "v_mfma_f32_16x16x32_bf16 v[80:83], a[120:123], v[24:27], v[80:83]\n\t" \
    "v_mfma_f32_16x16x32_bf16 v[84:87], a[120:123], v[28:31], v[84:87]\n\t" \
    "s_waitcnt lgkmcnt(5)\n\t" \
    "v_mfma_f32_16x16x32_bf16 v[72:75], a[52:55], v[32:35], v[72:75]\n\t" \
    "ds_read_b128 v[56:59], v2 offset:16384\n\t" \
    "s_waitcnt lgkmcnt(5)\n\t" \
    "v_mfma_f32_16x16x32_bf16 v[76:79], a[52:55], v[36:39], v[76:79]\n\t" \
    "ds_read_b128 v[60:63], v2 offset:34816\n\t" \
    "v_mfma_f32_16x16x32_bf16 v[80:83], a[124:127], v[32:35], v[80:83]\n\t" \
    "v_mfma_f32_16x16x32_bf16 v[84:87], a[124:127], v[36:39], v[84:87]\n\t" \
    "s_waitcnt lgkmcnt(5)\n\t" \
    "v_mfma_f32_16x16x32_bf16 v[72:75], a[56:59], v[40:43], v[72:75]\n\t" \
    "ds_read_b128 v[64:67], v2 offset:17408\n\t" \
    "s_waitcnt lgkmcnt(5)\n\t" \
    "v_mfma_f32_16x16x32_bf16 v[76:79], a[56:59], v[44:47], v[76:79]\n\t" \
    "ds_read_b128 v[68:71], v2 offset:35840\n\t" \
    "v_mfma_f32_16x16x32_bf16 v[80:83], a[128:131], v[40:43], v[80:83]\n\t" \
    "v_mfma_f32_16x16x32_bf16 v[84:87], a[128:131], v[44:47], v[84:87]\n\t" \
    "s_waitcnt lgkmcnt(5)\n\t" \
    "v_mfma_f32_16x16x32_bf16 v[72:75], a[60:63], v[48:51], v[72:75]\n\t" \
    "s_waitcnt lgkmcnt(4)\n\t" \
    "v_mfma_f32_16x16x32_bf16 v[76:79], a[60:63], v[52:55], v[76:79]\n\t" \
    "v_mfma_f32_16x16x32_bf16 v[80:83], a[132:135], v[48:51], v[80:83]\n\t" \
    "v_mfma_f32_16x16x32_bf16 v[84:87], a[132:135], v[52:55], v[84:87]\n\t" \
    "s_waitcnt lgkmcnt(3)\n\t" \
    "v_mfma_f32_16x16x32_bf16 v[72:75], a[64:67], v[56:59], v[72:75]\n\t" \
    "s_waitcnt lgkmcnt(2)\n\t" \
    "v_mfma_f32_16x16x32_bf16 v[76:79], a[64:67], v[60:63], v[76:79]\n\t" \
    "v_mfma_f32_16x16x32_bf16 v[80:83], a[136:139], v[56:59], v[80:83]\n\t" \
    "v_mov_b32 v2, v17\n\t" \
    "v_mfma_f32_16x16x32_bf16 v[84:87], a[136:139], v[60:63], v[84:87]\n\t" \
    "s_waitcnt lgkmcnt(1)\n\t" \
    "v_mfma_f32_16x16x32_bf16 v[72:75], a[68:71], v[64:67], v[72:75]\n\t" \
    "s_waitcnt lgkmcnt(0)\n\t" \
    "v_mfma_f32_16x16x32_bf16 v[76:79], a[68:71], v[68:71], v[76:79]\n\t" \
    "v_mfma_f32_16x16x32_bf16 v[80:83], a[140:143], v[64:67], v[80:83]\n\t" \
    "v_mfma_f32_16x16x32_bf16 v[84:87], a[140:143], v[68:71], v[84:87]\n\t" \
    "gv_mid8_%=:\n\t" \
    "s_nop 3\n\t" \
    "v_cvt_pk_bf16_f32 v104, v72, v73\n\t" \
    "v_cvt_pk_bf16_f32 v105, v74, v75\n\t" \
    "s_nop 0\n\t" \
    "v_cvt_pk_bf16_f32 v106, v80, v81\n\t" \
    "v_cvt_pk_bf16_f32 v107, v82, v83\n\t" \
    "ds_write_b128 v4, v[104:107] offset:0\n\t" \
    "v_cvt_pk_bf16_f32 v108, v76, v77\n\t" \
    "v_cvt_pk_bf16_f32 v109, v78, v79\n\t" \
    "v_cvt_pk_bf16_f32 v110, v84, v85\n\t" \
    "v_cvt_pk_bf16_f32 v111, v86, v87\n\t" \
    "ds_write_b128 v4, v[108:111] offset:1024\n\t" \
    "s_waitcnt vmcnt(18) lgkmcnt(0)\n\t" \
    "s_barrier\n\t" \
    "ds_read_b128 v[164:167], v5 offset:0\n\t" \
    "ds_read_b128 v[168:171], v5 offset:1024\n\t" \
    "ds_read_b128 v[172:175], v5 offset:2048\n\t" \
    "ds_read_b128 v[176:179], v5 offset:3072\n\t" \
    "ds_read_b128 v[180:183], v5 offset:4096\n\t" \
    "ds_read_b128 v[184:187], v5 offset:5120\n\t" \
    "ds_read_b128 v[188:191], v5 offset:6144\n\t" \
    "ds_read_b128 v[192:195], v5 offset:7168\n\t" \
    "s_mov_b64 exec, s[46:47]\n\t" \
    "global_store_dword v20, v157, s[36:37] sc1\n\t" \
    "s_mov_b64 exec, -1\n\t" \
    "v_xor_b32 v4, 0x2000, v4\n\t" \
    "v_xor_b32 v5, 0x2000, v5\n\t" \
    "s_waitcnt lgkmcnt(7)\n\t" \
    "v_mfma_f32_16x16x32_bf16 v[72:75], v[164:167], a[144:147], 0\n\t" \
    "s_waitcnt lgkmcnt(6)\n\t" \
    "v_mfma_f32_16x16x32_bf16 v[76:79], v[168:171], a[144:147], 0\n\t" \
    "s_waitcnt lgkmcnt(5)\n\t" \
    "v_mfma_f32_16x16x32_bf16 v[72:75], v[172:175], a[148:151], v[72:75]\n\t" \
    "s_waitcnt lgkmcnt(4)\n\t" \
    "v_mfma_f32_16x16x32_bf16 v[76:79], v[176:179], a[148:151], v[76:79]\n\t" \
    "s_waitcnt lgkmcnt(3)\n\t" \
    "v_mfma_f32_16x16x32_bf16 v[72:75], v[180:183], a[152:155], v[72:75]\n\t" \
    "s_waitcnt lgkmcnt(2)\n\t" \
    "v_mfma_f32_16x16x32_bf16 v[76:79], v[184:187], a[152:155], v[76:79]\n\t" \
    "s_waitcnt lgkmcnt(1)\n\t" \
    "v_mfma_f32_16x16x32_bf16 v[72:75], v[188:191], a[156:159], v[72:75]\n\t" \
    "s_waitcnt lgkmcnt(0)\n\t" \
    "v_mfma_f32_16x16x32_bf16 v[76:79], v[192:195], a[156:159], v[76:79]\n\t" \
    "v_mfma_f32_16x16x32_bf16 v[80:83], v[164:167], a[160:163], 0\n\t" \
    "v_mfma_f32_16x16x32_bf16 v[84:87], v[168:171], a[160:163], 0\n\t" \
    "v_mfma_f32_16x16x32_bf16 v[80:83], v[172:175], a[164:167], v[80:83]\n\t" \
    "v_mfma_f32_16x16x32_bf16 v[84:87], v[176:179], a[164:167], v[84:87]\n\t" \
    "v_mfma_f32_16x16x32_bf16 v[80:83], v[180:183], a[168:171], v[80:83]\n\t" \
    "v_mfma_f32_16x16x32_bf16 v[84:87], v[184:187], a[168:171], v[84:87]\n\t" \
    "v_cvt_pk_bf16_f32 v104, v72, v73\n\t" \
    "v_mfma_f32_16x16x32_bf16 v[80:83], v[188:191], a[172:175], v[80:83]\n\t" \
    "v_cvt_pk_bf16_f32 v105, v74, v75\n\t" \
    "v_mfma_f32_16x16x32_bf16 v[84:87], v[192:195], a[172:175], v[84:87]\n\t" \
    "v_cvt_pk_bf16_f32 v106, v76, v77\n\t" \
    "v_mfma_f32_16x16x32_bf16 v[88:91], v[164:167], a[176:179], 0\n\t" \
    "v_cvt_pk_bf16_f32 v107, v78, v79\n\t" \
    "v_mfma_f32_16x16x32_bf16 v[92:95], v[168:171], a[176:179], 0\n\t" \
    "v_xor_b32 v18, s40, v8\n\t" \
    "v_mfma_f32_16x16x32_bf16 v[88:91], v[172:175], a[180:183], v[88:91]\n\t" \
    "global_store_dwordx4 v18, v[104:107], s[38:39] sc1\n\t" \
    "v_mfma_f32_16x16x32_bf16 v[92:95], v[176:179], a[180:183], v[92:95]\n\t" \
    "v_mfma_f32_16x16x32_bf16 v[88:91], v[180:183], a[184:187], v[88:91]\n\t" \
    "v_mfma_f32_16x16x32_bf16 v[92:95], v[184:187], a[184:187], v[92:95]\n\t" \
    "v_cvt_pk_bf16_f32 v108, v80, v81\n\t" \
    "v_mfma_f32_16x16x32_bf16 v[88:91], v[188:191], a[188:191], v[88:91]\n\t" \
    "v_cvt_pk_bf16_f32 v109, v82, v83\n\t" \
    "v_mfma_f32_16x16x32_bf16 v[92:95], v[192:195], a[188:191], v[92:95]\n\t" \
    "v_cvt_pk_bf16_f32 v110, v84, v85\n\t" \
    "v_mfma_f32_16x16x32_bf16 v[72:75], v[164:167], a[192:195], 0\n\t" \
    "v_cvt_pk_bf16_f32 v111, v86, v87\n\t" \
    "v_mfma_f32_16x16x32_bf16 v[76:79], v[168:171], a[192:195], 0\n\t" \
    "v_xor_b32 v18, s40, v9\n\t" \
    "v_mfma_f32_16x16x32_bf16 v[72:75], v[172:175], a[196:199], v[72:75]\n\t" \
    "global_store_dwordx4 v18, v[108:111], s[38:39] sc1\n\t" \
    "v_mfma_f32_16x16x32_bf16 v[76:79], v[176:179], a[196:199], v[76:79]\n\t" \
    "v_mfma_f32_16x16x32_bf16 v[72:75], v[180:183], a[200:203], v[72:75]\n\t" \
    "v_mfma_f32_16x16x32_bf16 v[76:79], v[184:187], a[200:203], v[76:79]\n\t" \
    "v_cvt_pk_bf16_f32 v104, v88, v89\n\t" \
    "v_mfma_f32_16x16x32_bf16 v[72:75], v[188:191], a[204:207], v[72:75]\n\t" \
    "v_cvt_pk_bf16_f32 v105, v90, v91\n\t" \
    "v_mfma_f32_16x16x32_bf16 v[76:79], v[192:195], a[204:207], v[76:79]\n\t" \
    "v_cvt_pk_bf16_f32 v106, v92, v93\n\t" \
    "v_mfma_f32_16x16x32_bf16 v[80:83], v[164:167], a[208:211], 0\n\t" \
    "v_cvt_pk_bf16_f32 v107, v94, v95\n\t" \
    "v_mfma_f32_16x16x32_bf16 v[84:87], v[168:171], a[208:211], 0\n\t" \
    "v_xor_b32 v18, s40, v10\n\t" \
    "v_mfma_f32_16x16x32_bf16 v[80:83], v[172:175], a[212:215], v[80:83]\n\t" \
    "global_store_dwordx4 v18, v[104:107], s[38:39] sc1\n\t" \
    "v_mfma_f32_16x16x32_bf16 v[84:87], v[176:179], a[212:215], v[84:87]\n\t" \
    "v_mfma_f32_16x16x32_bf16 v[80:83], v[180:183], a[216:219], v[80:83]\n\t" \
    "v_mfma_f32_16x16x32_bf16 v[84:87], v[184:187], a[216:219], v[84:87]\n\t" \
    "v_cvt_pk_bf16_f32 v108, v72, v73\n\t" \
    "v_mfma_f32_16x16x32_bf16 v[80:83], v[188:191], a[220:223], v[80:83]\n\t" \
    "v_cvt_pk_bf16_f32 v109, v74, v75\n\t" \
    "v_mfma_f32_16x16x32_bf16 v[84:87], v[192:195], a[220:223], v[84:87]\n\t" \
    "v_cvt_pk_bf16_f32 v110, v76, v77\n\t" \
    "v_mfma_f32_16x16x32_bf16 v[88:91], v[164:167], a[224:227], 0\n\t" \
    "v_cvt_pk_bf16_f32 v111, v78, v79\n\t" \
    "v_mfma_f32_16x16x32_bf16 v[92:95], v[168:171], a[224:227], 0\n\t" \
    "v_xor_b32 v18, s40, v11\n\t" \
    "v_mfma_f32_16x16x32_bf16 v[88:91], v[172:175], a[228:231], v[88:91]\n\t" \
    "global_store_dwordx4 v18, v[108:111], s[38:39] sc1\n\t" \
    "v_mfma_f32_16x16x32_bf16 v[92:95], v[176:179], a[228:231], v[92:95]\n\t" \
    "v_mfma_f32_16x16x32_bf16 v[88:91], v[180:183], a[232:235], v[88:91]\n\t" \
    "v_mfma_f32_16x16x32_bf16 v[92:95], v[184:187], a[232:235], v[92:95]\n\t" \
    "v_cvt_pk_bf16_f32 v104, v80, v81\n\t" \
    "v_mfma_f32_16x16x32_bf16 v[88:91], v[188:191], a[236:239], v[88:91]\n\t" \
    "v_cvt_pk_bf16_f32 v105, v82, v83\n\t" \
    "v_mfma_f32_16x16x32_bf16 v[92:95], v[192:195], a[236:239], v[92:95]\n\t" \
    "v_cvt_pk_bf16_f32 v106, v84, v85\n\t" \
    "v_mfma_f32_16x16x32_bf16 v[72:75], v[164:167], a[240:243], 0\n\t" \
    "v_cvt_pk_bf16_f32 v107, v86, v87\n\t" \
    "v_mfma_f32_16x16x32_bf16 v[76:79], v[168:171], a[240:243], 0\n\t" \
    "v_xor_b32 v18, s40, v12\n\t" \
    "v_mfma_f32_16x16x32_bf16 v[72:75], v[172:175], a[244:247], v[72:75]\n\t" \
    "global_store_dwordx4 v18, v[104:107], s[38:39] sc1\n\t" \
    "v_mfma_f32_16x16x32_bf16 v[76:79], v[176:179], a[244:247], v[76:79]\n\t" \
    "v_mfma_f32_16x16x32_bf16 v[72:75], v[180:183], a[248:251], v[72:75]\n\t" \
    "v_mfma_f32_16x16x32_bf16 v[76:79], v[184:187], a[248:251], v[76:79]\n\t" \
    "v_cvt_pk_bf16_f32 v108, v88, v89\n\t" \
    "v_mfma_f32_16x16x32_bf16 v[72:75], v[188:191], a[252:255], v[72:75]\n\t" \
    "v_cvt_pk_bf16_f32 v109, v90, v91\n\t" \
    "v_mfma_f32_16x16x32_bf16 v[76:79], v[192:195], a[252:255], v[76:79]\n\t" \
    "v_cvt_pk_bf16_f32 v110, v92, v93\n\t" \
    "v_mfma_f32_16x16x32_bf16 v[80:83], v[164:167], v[224:227], 0\n\t" \
    "v_cvt_pk_bf16_f32 v111, v94, v95\n\t" \
    "v_mfma_f32_16x16x32_bf16 v[84:87], v[168:171], v[224:227], 0\n\t" \
    "v_xor_b32 v18, s40, v13\n\t" \
    "v_mfma_f32_16x16x32_bf16 v[80:83], v[172:175], v[228:231], v[80:83]\n\t" \
    "global_store_dwordx4 v18, v[108:111], s[38:39] sc1\n\t" \
    "v_mfma_f32_16x16x32_bf16 v[84:87], v[176:179], v[228:231], v[84:87]\n\t" \
    "v_mfma_f32_16x16x32_bf16 v[80:83], v[180:183], v[232:235], v[80:83]\n\t" \
    "v_mfma_f32_16x16x32_bf16 v[84:87], v[184:187], v[232:235], v[84:87]\n\t" \
    "v_cvt_pk_bf16_f32 v104, v72, v73\n\t" \
    "v_mfma_f32_16x16x32_bf16 v[80:83], v[188:191], v[236:239], v[80:83]\n\t" \
    "v_cvt_pk_bf16_f32 v105, v74, v75\n\t" \
    "v_mfma_f32_16x16x32_bf16 v[84:87], v[192:195], v[236:239], v[84:87]\n\t" \
    "v_cvt_pk_bf16_f32 v106, v76, v77\n\t" \
    "s_cmp_lt_u32 s65, 4\n\t" \
    "s_cbranch_scc1 gv_f1st10_%=\n\t" \
    "gv_f1b11_%=:\n\t" \
    "v_mfma_f32_16x16x32_bf16 v[88:91], v[164:167], v[240:243], 0\n\t" \
    "v_cvt_pk_bf16_f32 v107, v78, v79\n\t" \
    "ds_read_b128 v[24:27], v17 offset:0\n\t" \
    "v_mfma_f32_16x16x32_bf16 v[92:95], v[168:171], v[240:243], 0\n\t" \
    "v_xor_b32 v18, s40, v14\n\t" \
    "ds_read_b128 v[28:31], v17 offset:18432\n\t" \
    "v_mfma_f32_16x16x32_bf16 v[88:91], v[172:175], v[244:247], v[88:91]\n\t" \
    "global_store_dwordx4 v18, v[104:107], s[38:39] sc1\n\t" \
    "ds_read_b128 v[32:35], v17 offset:1024\n\t" \
    "v_mfma_f32_16x16x32_bf16 v[92:95], v[176:179], v[244:247], v[92:95]\n\t" \
    "ds_read_b128 v[36:39], v17 offset:19456\n\t" \
    "v_mfma_f32_16x16x32_bf16 v[88:91], v[180:183], v[248:251], v[88:91]\n\t" \
    "ds_read_b128 v[40:43], v17 offset:2048\n\t" \
    "v_mfma_f32_16x16x32_bf16 v[92:95], v[184:187], v[248:251], v[92:95]\n\t" \
    "v_cvt_pk_bf16_f32 v108, v80, v81\n\t" \
    "ds_read_b128 v[44:47], v17 offset:20480\n\t" \
    "v_mfma_f32_16x16x32_bf16 v[88:91], v[188:191], v[252:255], v[88:91]\n\t" \
    "v_cvt_pk_bf16_f32 v109, v82, v83\n\t" \
    "v_mfma_f32_16x16x32_bf16 v[92:95], v[192:195], v[252:255], v[92:95]\n\t" \
    "v_cvt_pk_bf16_f32 v110, v84, v85\n\t" \
    "v_cvt_pk_bf16_f32 v111, v86, v87\n\t" \
    "v_xor_b32 v18, s40, v15\n\t" \
    "global_store_dwordx4 v18, v[108:111], s[38:39] sc1\n\t" \
    "s_nop 1\n\t" \
    "v_cvt_pk_bf16_f32 v104, v88, v89\n\t" \
    "v_cvt_pk_bf16_f32 v105, v90, v91\n\t" \
    "v_cvt_pk_bf16_f32 v106, v92, v93\n\t" \
    "v_cvt_pk_bf16_f32 v107, v94, v95\n\t" \
    "v_xor_b32 v18, s40, v16\n\t" \
    "global_store_dwordx4 v18, v[104:107], s[38:39] sc1\n\t" \
    "s_sub_u32 s69, s65, 10\n\t" \
    "s_cmp_ge_u32 s69, 52\n\t" \
    "s_cbranch_scc1 gv_fast12_%=\n\t" \
    "gv_nofast7_%=:\n\t" \
    "s_add_u32 s65, s65, 2\n\t" \
    "s_cmp_lt_u32 s65, 64\n\t" \
    "s_cbranch_scc1 gv_loop6_%=\n\t" \
    "s_waitcnt vmcnt(0) lgkmcnt(0)\n\t" \
    "s_barrier\n\t" \
    "s_branch ea_done_%=\n\t" \
    "gv_f1st10_%=:\n\t" \
    "s_waitcnt vmcnt(6)\n\t" \
    "s_barrier\n\t" \
    "s_branch gv_f1b11_%=\n\t" \
    "gv_fast12_%=:\n\t" \
    "s_waitcnt vmcnt(0)\n\t" \
    "s_barrier\n\t" \
    "s_cmp_lg_u32 s66, 0\n\t" \
    "s_cbranch_scc1 gv_SF13_%=\n\t" \
    "s_add_u32 s70, s65, 0\n\t" \
    "v_mov_b32 v157, s70\n\t" \
    "s_mov_b64 exec, 31\n\t" \
    "global_store_dword v20, v157, s[36:37] sc1\n\t" \
    "s_mov_b64 exec, -1\n\t" \
    "gv_SF13_%=:\n\t" \
    "s_cmp_ge_u32 s65, 2\n\t" \
    "s_cbranch_scc1 gv_nofast7_%=\n\t" \
    "s_add_u32 m0, s91, 0\n\t" \
    "s_nop 0\n\t" \
    "global_load_lds_dwordx4 v1, s[50:51] offset:0\n\t" \
    "s_add_u32 m0, s91, 960\n\t" \
    "s_nop 0\n\t" \
    "global_load_lds_dwordx4 v1, s[50:51] offset:64\n\t" \
    "s_add_u32 m0, s91, 1920\n\t" \
    "s_nop 0\n\t" \
    "global_load_lds_dwordx4 v1, s[50:51] offset:128\n\t" \
    "s_add_u32 m0, s91, 2880\n\t" \
    "s_nop 0\n\t" \
    "global_load_lds_dwordx4 v1, s[50:51] offset:192\n\t" \
    "s_add_u32 m0, s91, 3840\n\t" \
    "s_nop 0\n\t" \
    "global_load_lds_dwordx4 v1, s[50:51] offset:256\n\t" \
    "s_add_u32 m0, s91, 4800\n\t" \
    "s_nop 0\n\t" \
    "global_load_lds_dwordx4 v1, s[50:51] offset:320\n\t" \
    "s_add_u32 m0, s91, 5760\n\t" \
    "s_nop 0\n\t" \
    "global_load_lds_dwordx4 v1, s[50:51] offset:384\n\t" \
    "s_add_u32 m0, s91, 6720\n\t" \
    "s_nop 0\n\t" \
    "global_load_lds_dwordx4 v1, s[50:51] offset:448\n\t" \
    "s_add_u32 m0, s91, 7680\n\t" \
    "s_nop 0\n\t" \
    "global_load_lds_dwordx4 v1, s[50:51] offset:512\n\t" \
    "s_branch gv_nofast7_%=\n\t" \
    "ea_done_%=:\n\t" \
    "s_waitcnt lgkmcnt(0)\n\t" \
    "ea_last_%=:\n\t" \
    ""
#define ASM_R \
    "s_mov_b64 s[36:37], %0\n\t" \
    "s_mov_b64 s[42:43], %1\n\t" \
    "s_mov_b64 s[44:45], %2\n\t" \
    "s_mov_b32 s66, %3\n\t" \
    "s_mov_b32 s67, %4\n\t" \
    "s_mov_b64 s[50:51], %5\n\t" \
    "s_mov_b32 s82, %7\n\t" \
    "s_load_dwordx8 s[84:91], %6, 0\n\t" \
    "v_mbcnt_lo_u32_b32 v223, -1, 0\n\t" \
    "v_mbcnt_hi_u32_b32 v223, -1, v223\n\t" \
    "s_getpc_b64 s[46:47]\n\t" \
    "rr_pc_%=:\n\t" \
    "s_lshl_b32 s69, s66, 13\n\t" \
    "v_lshl_add_u32 v222, v223, 7, s69\n\t" \
    "v_min_u32 v222, rr_last_%=-rr_pc_%=-4, v222\n\t" \
    "global_load_dword v222, v222, s[46:47]\n\t" \
    "v_lshlrev_b32 v223, 2, v223\n\t" \
    "v_add_u32 v223, s82, v223\n\t" \
    "ds_read_b32 v0, v223 offset:0\n\t" \
    "ds_read_b32 v1, v223 offset:256\n\t" \
    "ds_read_b32 v2, v223 offset:512\n\t" \
    "ds_read_b32 v3, v223 offset:768\n\t" \
    "ds_read_b32 v4, v223 offset:1024\n\t" \
    "ds_read_b32 v5, v223 offset:1280\n\t" \
    "ds_read_b32 v6, v223 offset:1536\n\t" \
    "ds_read_b32 v7, v223 offset:1792\n\t" \
    "ds_read_b32 v8, v223 offset:2048\n\t" \
    "ds_read_b32 v9, v223 offset:2304\n\t" \
    "ds_read_b32 v10, v223 offset:2560\n\t" \
    "ds_read_b32 v11, v223 offset:2816\n\t" \
    "s_waitcnt lgkmcnt(0)\n\t" \
    "ds_read_b32 v12, v223 offset:3072\n\t" \
    "ds_read_b32 v13, v223 offset:3328\n\t" \
    "ds_read_b32 v14, v223 offset:3584\n\t" \
    "ds_read_b32 v15, v223 offset:3840\n\t" \
    "ds_read_b32 v16, v223 offset:4096\n\t" \
    "ds_read_b32 v17, v223 offset:4352\n\t" \
    "ds_read_b32 v18, v223 offset:4608\n\t" \
    "ds_read_b32 v19, v223 offset:4864\n\t" \
    "ds_read_b32 v20, v223 offset:5120\n\t" \
    "ds_read_b32 v21, v223 offset:5376\n\t" \
    "ds_read_b32 v22, v223 offset:5632\n\t" \
    "ds_read_b32 v23, v223 offset:5888\n\t" \
    "s_waitcnt lgkmcnt(0)\n\t" \
    "ds_read_b32 v24, v223 offset:6144\n\t" \
    "ds_read_b32 v25, v223 offset:6400\n\t" \
    "ds_read_b32 v26, v223 offset:6656\n\t" \
    "ds_read_b32 v27, v223 offset:6912\n\t" \
    "ds_read_b32 v28, v223 offset:7168\n\t" \
    "ds_read_b32 v29, v223 offset:7424\n\t" \
    "ds_read_b32 v30, v223 offset:7680\n\t" \
    "ds_read_b32 v31, v223 offset:7936\n\t" \
    "ds_read_b32 v32, v223 offset:8192\n\t" \
    "ds_read_b32 v33, v223 offset:8448\n\t" \
    "ds_read_b32 v34, v223 offset:8704\n\t" \
    "ds_read_b32 v35, v223 offset:8960\n\t" \
    "s_waitcnt lgkmcnt(0)\n\t" \
    "ds_read_b32 v36, v223 offset:9216\n\t" \
    "ds_read_b32 v37, v223 offset:9472\n\t" \
    "ds_read_b32 v38, v223 offset:9728\n\t" \
    "ds_read_b32 v39, v223 offset:9984\n\t" \
    "ds_read_b32 v40, v223 offset:10240\n\t" \
    "ds_read_b32 v41, v223 offset:10496\n\t" \
    "ds_read_b32 v42, v223 offset:10752\n\t" \
    "ds_read_b32 v43, v223 offset:11008\n\t" \
    "ds_read_b32 v44, v223 offset:11264\n\t" \
    "ds_read_b32 v45, v223 offset:11520\n\t" \
    "ds_read_b32 v46, v223 offset:11776\n\t" \
    "ds_read_b32 v164, v223 offset:12032\n\t" \
    "s_waitcnt lgkmcnt(0)\n\t" \
    "ds_read_b32 v165, v223 offset:12288\n\t" \
    "ds_read_b32 v166, v223 offset:12544\n\t" \
    "ds_read_b32 v167, v223 offset:12800\n\t" \
    "ds_read_b32 v196, v223 offset:13312\n\t" \
    "ds_read_b32 v197, v223 offset:13568\n\t" \
    "ds_read_b32 v198, v223 offset:13824\n\t" \
    "ds_read_b32 v199, v223 offset:14080\n\t" \
    "ds_read_b32 v200, v223 offset:14336\n\t" \
    "s_waitcnt lgkmcnt(0)\n\t" \
    "s_mov_b32 s68, 262144\n\t" \
    "s_mov_b32 s72, 0\n\t" \
    "s_mov_b32 s83, -1\n\t" \
    "s_mov_b32 s65, 0\n\t" \
    "v_add_u32 v181, 16384, v2\n\t" \
    "v_add_u32 v182, 131072, v2\n\t" \
    "v_add_u32 v183, 147456, v2\n\t" \
    "s_mov_b32 s77, 3019898880\n\t" \
    "s_mov_b32 s78, 3054198966\n\t" \
    "s_mov_b32 s71, 3\n\t" \
    "rr_E1_%=:\n\t" \
    "s_memtime s[54:55]\n\t" \
    "s_nop 0\n\t" \
    "s_memrealtime s[4:5]\n\t" \
    "s_nop 0\n\t" \
    "s_memrealtime s[6:7]\n\t" \
    "s_nop 0\n\t" \
    "s_memrealtime s[8:9]\n\t" \
    "s_nop 0\n\t" \
    "s_memrealtime s[10:11]\n\t" \
    "s_nop 0\n\t" \
    "s_memrealtime s[12:13]\n\t" \
    "s_nop 0\n\t" \
    "s_memrealtime s[14:15]\n\t" \
    "s_nop 0\n\t" \
    "s_memrealtime s[16:17]\n\t" \
    "s_nop 0\n\t" \
    "s_memrealtime s[18:19]\n\t" \
    "s_nop 0\n\t" \
    "s_memrealtime s[20:21]\n\t" \
    "s_nop 0\n\t" \
    "s_memrealtime s[22:23]\n\t" \
    "s_nop 0\n\t" \
    "s_memrealtime s[24:25]\n\t" \
    "s_nop 0\n\t" \
    "s_memrealtime s[26:27]\n\t" \
    "s_nop 0\n\t" \
    "s_memrealtime s[28:29]\n\t" \
    "s_nop 0\n\t" \
    "s_memtime s[56:57]\n\t" \
    "s_mov_b32 s70, 1\n\t" \
    "s_sub_u32 s71, s71, 1\n\t" \
    "s_cselect_b32 s69, 0, -1\n\t" \
    "s_waitcnt lgkmcnt(0)\n\t" \
    "s_sub_u32 s54, s56, s54\n\t" \
    "s_and_b32 s54, s54, s69\n\t" \
    "s_cmp_gt_u32 s54, 124\n\t" \
    "s_cbranch_scc1 rr_E1_%=\n\t" \
    "s_cmp_eq_u32 s4, s28\n\t" \
    "s_addc_u32 s70, s70, 0\n\t" \
    "s_cmp_eq_u32 s6, s28\n\t" \
    "s_addc_u32 s70, s70, 0\n\t" \
    "s_cmp_eq_u32 s8, s28\n\t" \
    "s_addc_u32 s70, s70, 0\n\t" \
    "s_cmp_eq_u32 s10, s28\n\t" \
    "s_addc_u32 s70, s70, 0\n\t" \
    "s_cmp_eq_u32 s12, s28\n\t" \
    "s_addc_u32 s70, s70, 0\n\t" \
    "s_cmp_eq_u32 s14, s28\n\t" \
    "s_addc_u32 s70, s70, 0\n\t" \
    "s_cmp_eq_u32 s16, s28\n\t" \
    "s_addc_u32 s70, s70, 0\n\t" \
    "s_cmp_eq_u32 s18, s28\n\t" \
    "s_addc_u32 s70, s70, 0\n\t" \
    "s_cmp_eq_u32 s20, s28\n\t" \
    "s_addc_u32 s70, s70, 0\n\t" \
    "s_cmp_eq_u32 s22, s28\n\t" \
    "s_addc_u32 s70, s70, 0\n\t" \
    "s_cmp_eq_u32 s24, s28\n\t" \
    "s_addc_u32 s70, s70, 0\n\t" \
    "s_cmp_eq_u32 s26, s28\n\t" \
    "s_addc_u32 s70, s70, 0\n\t" \
    "s_mov_b32 s75, s73\n\t" \
    "s_mov_b32 s76, s74\n\t" \
    "s_lshl_b32 s70, s70, 3\n\t" \
    "s_sub_u32 s73, s56, s70\n\t" \
    "s_lshl_b32 s74, s28, 4\n\t" \
    "v_mov_b32 v172, 0x1f0000\n\t" \
    "v_mov_b32 v173, 0x3f0000\n\t" \
    "global_load_dword v172, v172, s[36:37]\n\t" \
    "global_load_dword v173, v173, s[36:37]\n\t" \
    "s_mov_b32 s65, 63\n\t" \
    "s_mul_i32 s70, s67, 7\n\t" \
    "s_add_u32 s69, s65, 0\n\t" \
    "s_xor_b32 s69, s69, s70\n\t" \
    "s_and_b32 s69, s69, 63\n\t" \
    "s_lshl_b32 s69, s69, 15\n\t" \
    "s_mul_i32 s70, s67, 2097152\n\t" \
    "s_add_u32 s69, s69, s70\n\t" \
    "s_add_u32 s69, s69, 48234496\n\t" \
    "s_add_u32 s38, s36, s69\n\t" \
    "s_addc_u32 s39, s37, 0\n\t" \
    "v_mov_b32 v174, 0\n\t" \
    "global_load_dword v174, v174, s[38:39]\n\t" \
    "v_mbcnt_lo_u32_b32 v184, -1, 0\n\t" \
    "v_mbcnt_hi_u32_b32 v184, -1, v184\n\t" \
    "v_min_u32 v184, 59, v184\n\t" \
    "v_lshlrev_b32 v184, 6, v184\n\t" \
    "v_add_u32 v184, 6258688, v184\n\t" \
    "v_mov_b32 v185, 6266880\n\t" \
    "global_load_dword v185, v185, s[36:37] sc1\n\t" \
    "global_load_dwordx2 v[186:187], v184, s[36:37] sc1\n\t" \
    "s_waitcnt vmcnt(0)\n\t" \
    "v_sub_u32 v186, v186, v185\n\t" \
    "v_cmp_ne_u32 vcc, 1, v186\n\t" \
    "s_cbranch_vccz rr_Cok4_%=\n\t" \
    "rr_C3_%=:\n\t" \
    "global_load_dwordx2 v[186:187], v184, s[36:37] sc1\n\t" \
    "s_waitcnt vmcnt(0)\n\t" \
    "v_sub_u32 v186, v186, v185\n\t" \
    "v_cmp_ne_u32 vcc, 1, v186\n\t" \
    "s_cbranch_vccz rr_Cok4_%=\n\t" \
    "s_cmp_eq_u32 s68, 0\n\t" \
    "s_cbranch_scc1 rr_F5_%=\n\t" \
    "s_sub_u32 s68, s68, 1\n\t" \
    "s_sleep 4\n\t" \
    "s_branch rr_C3_%=\n\t" \
    "rr_F5_%=:\n\t" \
    "v_mov_b32 v188, 6095104\n\t" \
    "v_mov_b32 v189, 4\n\t" \
    "global_store_dword v188, v189, s[36:37] sc1\n\t" \
    "s_waitcnt vmcnt(0)\n\t" \
    "s_branch rr_Cok4_%=\n\t" \
    "rr_Cok4_%=:\n\t" \
    "v_sub_u32_dpp v186, v187, v187 row_shl:6 row_mask:0xf bank_mask:0xf\n\t" \
    "v_min_u32_dpp v187, v187, v187 row_shl:6 row_mask:0xf bank_mask:0xf\n\t" \
    "v_min_i32 v186, 64, v186\n\t" \
    "v_max_i32 v186, -64, v186\n\t" \
    "s_mov_b32 s33, 0\n\t" \
    "v_readlane_b32 s69, v187, 0\n\t" \
    "v_readlane_b32 s70, v187, 1\n\t" \
    "v_readlane_b32 s54, v186, 0\n\t" \
    "v_readlane_b32 s55, v186, 1\n\t" \
    "s_nop 1\n\t" \
    "s_cmp_lt_u32 s70, s69\n\t" \
    "s_cselect_b32 s62, 8192, 0\n\t" \
    "s_cselect_b32 s54, s55, s54\n\t" \
    "s_cselect_b32 s69, s70, s69\n\t" \
    "s_mov_b32 s71, s69\n\t" \
    "s_add_u32 s33, s33, s54\n\t" \
    "v_readlane_b32 s69, v187, 2\n\t" \
    "v_readlane_b32 s70, v187, 3\n\t" \
    "v_readlane_b32 s54, v186, 2\n\t" \
    "v_readlane_b32 s55, v186, 3\n\t" \
    "s_nop 1\n\t" \
    "s_sub_u32 s55, 0, s55\n\t" \
    "s_cmp_lt_u32 s70, s69\n\t" \
    "s_cselect_b32 s63, 8192, 0\n\t" \
    "s_cselect_b32 s54, s55, s54\n\t" \
    "s_cselect_b32 s69, s70, s69\n\t" \
    "s_min_u32 s71, s69, s71\n\t" \
    "s_add_u32 s33, s33, s54\n\t" \
    "v_readlane_b32 s69, v187, 4\n\t" \
    "v_readlane_b32 s70, v187, 5\n\t" \
    "v_readlane_b32 s54, v186, 4\n\t" \
    "v_readlane_b32 s55, v186, 5\n\t" \
    "s_nop 1\n\t" \
    "s_sub_u32 s54, 0, s54\n\t" \
    "s_sub_u32 s55, 0, s55\n\t" \
    "s_cmp_lt_u32 s70, s69\n\t" \
    "s_cselect_b32 s64, 8192, 0\n\t" \
    "s_cselect_b32 s54, s55, s54\n\t" \
    "s_cselect_b32 s69, s70, s69\n\t" \
    "s_min_u32 s71, s69, s71\n\t" \
    "s_add_u32 s33, s33, s54\n\t" \
    "s_add_u32 s63, s63, 0x200000\n\t" \
    "s_xor_b32 s52, s64, 8192\n\t" \
    "s_cmp_gt_u32 s71, 625\n\t" \
    "s_cselect_b32 s69, 1, 0\n\t" \
    "s_mul_i32 s70, s69, 160\n\t" \
    "s_add_u32 s86, s86, s70\n\t" \
    "s_mul_i32 s70, s69, 536\n\t" \
    "s_add_u32 s87, s87, s70\n\t" \
    "s_mul_i32 s70, s69, 256\n\t" \
    "s_add_u32 s88, s88, s70\n\t" \
    "v_xor_b32 v6, s52, v6\n\t" \
    "v_lshlrev_b32 v178, 3, v0\n\t" \
    "v_and_b32 v178, 0xf80, v178\n\t" \
    "s_add_u32 s69, s52, 6148096\n\t" \
    "v_add_u32 v178, s69, v178\n\t" \
    "v_mov_b32 v172, -1\n\t" \
    "v_mov_b32 v173, -1\n\t" \
    "v_mov_b32 v174, -1\n\t" \
    "v_mov_b32 v175, -1\n\t" \
    "v_mov_b32 v170, 0\n\t" \
    "s_mov_b32 s65, s66\n\t" \
    "s_sub_u32 s80, s74, 32000\n\t" \
    "v_mov_b32 v177, -1\n\t" \
    "s_cmp_lg_u32 s51, 0\n\t" \
    "s_cselect_b32 s42, 0, 0\n\t" \
    "s_cselect_b32 s43, 32, 0\n\t" \
    "s_cmp_le_u32 s51, s65\n\t" \
    "s_cbranch_scc1 rr_n316_%=\n\t" \
    "s_add_u32 s69, s65, 2\n\t" \
    "s_mul_i32 s70, s69, 171\n\t" \
    "s_lshr_b32 s70, s70, 9\n\t" \
    "s_mul_i32 s70, s70, 3\n\t" \
    "s_sub_u32 s69, s69, s70\n\t" \
    "s_mul_i32 s69, s69, 36\n\t" \
    "s_lshl_b32 s69, s69, 14\n\t" \
    "s_add_u32 s69, s69, s64\n\t" \
    "s_add_u32 s69, s69, 4194304\n\t" \
    "s_add_u32 s40, s36, s69\n\t" \
    "s_addc_u32 s41, s37, 0\n\t" \
    "global_store_dwordx4 v196, v[172:175], s[40:41] sc1\n\t" \
    "global_store_dwordx4 v197, v[172:175], s[40:41] sc1\n\t" \
    "global_store_dwordx4 v198, v[172:175], s[40:41] sc1\n\t" \
    "s_add_u32 s69, s65, 2\n\t" \
    "s_mul_i32 s70, s69, 171\n\t" \
    "s_lshr_b32 s70, s70, 9\n\t" \
    "s_mul_i32 s70, s70, 3\n\t" \
    "s_sub_u32 s69, s69, s70\n\t" \
    "s_mul_i32 s69, s69, 36\n\t" \
    "s_add_u32 s69, s69, s67\n\t" \
    "s_lshl_b32 s69, s69, 14\n\t" \
    "s_add_u32 s69, s69, s64\n\t" \
    "s_add_u32 s69, s69, 4194304\n\t" \
    "s_add_u32 s40, s36, s69\n\t" \
    "s_addc_u32 s41, s37, 0\n\t" \
    "global_store_dwordx4 v0, v[172:175], s[40:41] offset:0 sc1\n\t" \
    "global_store_dwordx4 v0, v[172:175], s[40:41] offset:1024 sc1\n\t" \
    "global_store_dwordx4 v0, v[172:175], s[40:41] offset:2048 sc1\n\t" \
    "global_store_dwordx4 v0, v[172:175], s[40:41] offset:3072 sc1\n\t" \
    "global_store_dwordx4 v1, v[172:175], s[40:41] offset:0 sc1\n\t" \
    "global_store_dwordx4 v1, v[172:175], s[40:41] offset:1024 sc1\n\t" \
    "global_store_dwordx4 v1, v[172:175], s[40:41] offset:2048 sc1\n\t" \
    "global_store_dwordx4 v1, v[172:175], s[40:41] offset:3072 sc1\n\t" \
    "rr_n316_%=:\n\t" \
    "s_cmp_eq_u32 s51, 0\n\t" \
    "s_cbranch_scc1 rr_arm17_%=\n\t" \
    "s_cmp_gt_u32 s65, 61\n\t" \
    "s_cbranch_scc1 rr_arm17_%=\n\t" \
    "s_add_u32 s69, s65, 2\n\t" \
    "s_bitcmp1_b32 s69, 0\n\t" \
    "s_cselect_b32 s70, s63, s62\n\t" \
    "s_bfe_u32 s69, s69, 0x20001\n\t" \
    "s_lshl_b32 s69, s69, 19\n\t" \
    "s_add_u32 s69, s69, s70\n\t" \
    "s_add_u32 s46, s36, s69\n\t" \
    "s_addc_u32 s47, s37, 0\n\t" \
    "global_store_dwordx4 v164, v[172:175], s[46:47] sc1\n\t" \
    "global_store_dwordx4 v165, v[172:175], s[46:47] sc1\n\t" \
    "global_store_dwordx4 v166, v[172:175], s[46:47] sc1\n\t" \
    "global_store_dwordx4 v167, v[172:175], s[46:47] sc1\n\t" \
    "rr_arm17_%=:\n\t" \
    "s_waitcnt vmcnt(0)\n\t" \
    "s_cmp_gt_i32 s33, 0\n\t" \
    "s_cselect_b32 s70, 1, 0\n\t" \
    "s_mul_i32 s69, s67, 57\n\t" \
    "s_lshr_b32 s69, s69, 9\n\t" \
    "s_xor_b32 s69, s69, s70\n\t" \
    "s_and_b32 s33, s69, 1\n\t" \
    "s_and_b32 s69, s33, 1\n\t" \
    "s_lshl_b32 s69, s69, 14\n\t" \
    "s_add_u32 s69, s69, s62\n\t" \
    "s_add_u32 s52, s36, s69\n\t" \
    "s_addc_u32 s53, s37, 0\n\t" \
    "v_lshrrev_b32 v187, 9, v0\n\t" \
    "v_mul_i32_i24 v187, -2044, v187\n\t" \
    "v_add_u32 v187, v187, v7\n\t" \
    "v_add_u32 v187, 3072, v187\n\t" \
    "v_mov_b32 v189, -1\n\t" \
    "s_sleep 40\n\t" \
    "s_mov_b32 s71, 3\n\t" \
    "rr_E18_%=:\n\t" \
    "s_memtime s[54:55]\n\t" \
    "s_nop 0\n\t" \
    "s_memrealtime s[4:5]\n\t" \
    "s_nop 0\n\t" \
    "s_memrealtime s[6:7]\n\t" \
    "s_nop 0\n\t" \
    "s_memrealtime s[8:9]\n\t" \
    "s_nop 0\n\t" \
    "s_memrealtime s[10:11]\n\t" \
    "s_nop 0\n\t" \
    "s_memrealtime s[12:13]\n\t" \
    "s_nop 0\n\t" \
    "s_memrealtime s[14:15]\n\t" \
    "s_nop 0\n\t" \
    "s_memrealtime s[16:17]\n\t" \
    "s_nop 0\n\t" \
    "s_memrealtime s[18:19]\n\t" \
    "s_nop 0\n\t" \
    "s_memrealtime s[20:21]\n\t" \
    "s_nop 0\n\t" \
    "s_memrealtime s[22:23]\n\t" \
    "s_nop 0\n\t" \
    "s_memrealtime s[24:25]\n\t" \
    "s_nop 0\n\t" \
    "s_memrealtime s[26:27]\n\t" \
    "s_nop 0\n\t" \
    "s_memrealtime s[28:29]\n\t" \
    "s_nop 0\n\t" \
    "s_memtime s[56:57]\n\t" \
    "s_mov_b32 s70, 1\n\t" \
    "s_sub_u32 s71, s71, 1\n\t" \
    "s_cselect_b32 s69, 0, -1\n\t" \
    "s_waitcnt lgkmcnt(0)\n\t" \
    "s_sub_u32 s54, s56, s54\n\t" \
    "s_and_b32 s54, s54, s69\n\t" \
    "s_cmp_gt_u32 s54, 124\n\t" \
    "s_cbranch_scc1 rr_E18_%=\n\t" \
    "s_cmp_eq_u32 s4, s28\n\t" \
    "s_addc_u32 s70, s70, 0\n\t" \
    "s_cmp_eq_u32 s6, s28\n\t" \
    "s_addc_u32 s70, s70, 0\n\t" \
    "s_cmp_eq_u32 s8, s28\n\t" \
    "s_addc_u32 s70, s70, 0\n\t" \
    "s_cmp_eq_u32 s10, s28\n\t" \
    "s_addc_u32 s70, s70, 0\n\t" \
    "s_cmp_eq_u32 s12, s28\n\t" \
    "s_addc_u32 s70, s70, 0\n\t" \
    "s_cmp_eq_u32 s14, s28\n\t" \
    "s_addc_u32 s70, s70, 0\n\t" \
    "s_cmp_eq_u32 s16, s28\n\t" \
    "s_addc_u32 s70, s70, 0\n\t" \
    "s_cmp_eq_u32 s18, s28\n\t" \
    "s_addc_u32 s70, s70, 0\n\t" \
    "s_cmp_eq_u32 s20, s28\n\t" \
    "s_addc_u32 s70, s70, 0\n\t" \
    "s_cmp_eq_u32 s22, s28\n\t" \
    "s_addc_u32 s70, s70, 0\n\t" \
    "s_cmp_eq_u32 s24, s28\n\t" \
    "s_addc_u32 s70, s70, 0\n\t" \
    "s_cmp_eq_u32 s26, s28\n\t" \
    "s_addc_u32 s70, s70, 0\n\t" \
    "s_mov_b32 s75, s73\n\t" \
    "s_mov_b32 s76, s74\n\t" \
    "s_lshl_b32 s70, s70, 3\n\t" \
    "s_sub_u32 s73, s56, s70\n\t" \
    "s_lshl_b32 s74, s28, 4\n\t" \
    "s_sub_u32 s69, s73, s75\n\t" \
    "s_sub_u32 s70, s74, s76\n\t" \
    "s_sub_u32 s71, s70, 1600\n\t" \
    "s_cmp_lt_u32 s71, 400000\n\t" \
    "s_cbranch_scc0 rr_K20_%=\n\t" \
    "v_cvt_f32_u32 v190, s69\n\t" \
    "v_cvt_f32_u32 v191, s70\n\t" \
    "v_rcp_f32 v192, v190\n\t" \
    "v_rcp_f32 v193, v191\n\t" \
    "v_mul_f32 v192, v192, v191\n\t" \
    "v_mul_f32 v193, v193, v190\n\t" \
    "v_mul_f32 v192, 0x4f800000, v192\n\t" \
    "v_mul_f32 v193, 0x4f000000, v193\n\t" \
    "v_cvt_u32_f32 v192, v192\n\t" \
    "v_cvt_u32_f32 v193, v193\n\t" \
    "v_readfirstlane_b32 s69, v192\n\t" \
    "v_readfirstlane_b32 s70, v193\n\t" \
    "s_nop 1\n\t" \
    "s_mov_b32 s78, s69\n\t" \
    "s_mov_b32 s77, s70\n\t" \
    "rr_K20_%=:\n\t" \
    "s_add_u32 s69, s65, 0\n\t" \
    "s_bitcmp1_b32 s69, 0\n\t" \
    "s_cselect_b32 s70, s63, s62\n\t" \
    "s_bfe_u32 s69, s69, 0x20001\n\t" \
    "s_lshl_b32 s69, s69, 19\n\t" \
    "s_add_u32 s69, s69, s70\n\t" \
    "s_add_u32 s46, s36, s69\n\t" \
    "s_addc_u32 s47, s37, 0\n\t" \
    "s_add_u32 s69, s65, 0\n\t" \
    "s_lshr_b32 s69, s69, 1\n\t" \
    "s_xor_b32 s69, s69, s33\n\t" \
    "s_and_b32 s69, s69, 1\n\t" \
    "s_lshl_b32 s69, s69, 14\n\t" \
    "s_or_b32 s46, s46, s69\n\t" \
    "s_add_u32 s69, s65, 1\n\t" \
    "s_bitcmp1_b32 s69, 0\n\t" \
    "s_cselect_b32 s70, s63, s62\n\t" \
    "s_bfe_u32 s69, s69, 0x20001\n\t" \
    "s_lshl_b32 s69, s69, 19\n\t" \
    "s_add_u32 s69, s69, s70\n\t" \
    "s_add_u32 s48, s36, s69\n\t" \
    "s_addc_u32 s49, s37, 0\n\t" \
    "s_mul_i32 s70, s67, 7\n\t" \
    "s_add_u32 s69, s65, 0\n\t" \
    "s_xor_b32 s69, s69, s70\n\t" \
    "s_and_b32 s69, s69, 63\n\t" \
    "s_lshl_b32 s69, s69, 15\n\t" \
    "s_mul_i32 s70, s67, 2097152\n\t" \
    "s_add_u32 s69, s69, s70\n\t" \
    "s_add_u32 s69, s69, 48234496\n\t" \
    "s_add_u32 s38, s36, s69\n\t" \
    "s_addc_u32 s39, s37, 0\n\t" \
    "s_add_u32 s69, s65, 0\n\t" \
    "s_mul_i32 s70, s69, 171\n\t" \
    "s_lshr_b32 s70, s70, 9\n\t" \
    "s_mul_i32 s70, s70, 3\n\t" \
    "s_sub_u32 s69, s69, s70\n\t" \
    "s_mul_i32 s69, s69, 36\n\t" \
    "s_add_u32 s69, s69, s67\n\t" \
    "s_lshl_b32 s69, s69, 14\n\t" \
    "s_add_u32 s69, s69, s64\n\t" \
    "s_add_u32 s69, s69, 4194304\n\t" \
    "s_add_u32 s40, s36, s69\n\t" \
    "s_addc_u32 s41, s37, 0\n\t" \
    "s_mov_b32 s91, 0\n\t" \
    "s_mov_b32 s96, 0x55555555\n\t" \
    "s_mov_b32 s58, 0\n\t" \
    "s_mov_b32 s59, -1\n\t" \
    "v_mov_b32 v160, 0\n\t" \
    "v_mov_b32 v161, 0\n\t" \
    "v_mov_b32 v162, 0\n\t" \
    "v_mov_b32 v163, 0\n\t" \
    "v_mov_b32 v185, 0\n\t" \
    "s_mov_b32 s91, 3\n\t" \
    "s_mov_b64 exec, 0\n\t" \
    "s_branch rr_h025_%=\n\t" \
    "rr_dry28_%=:\n\t" \
    "s_mov_b64 exec, -1\n\t" \
    "s_mov_b32 s91, 0\n\t" \
    "s_waitcnt vmcnt(0) lgkmcnt(0)\n\t" \
    "s_nop 7\n\t" \
    "v_mov_b32 v160, 0\n\t" \
    "v_mov_b32 v161, 0\n\t" \
    "v_mov_b32 v162, 0\n\t" \
    "v_mov_b32 v163, 0\n\t" \
    "rr_p129_%=:\n\t" \
    "s_mov_b32 exec_lo, 0\n\t" \
    "global_load_dword v189, v187, s[52:53] sc1\n\t" \
    "s_mov_b64 exec, -1\n\t" \
    "s_waitcnt vmcnt(0)\n\t" \
    "v_cmp_eq_u32 vcc, -1, v189\n\t" \
    "s_nop 1\n\t" \
    "s_cmp_eq_u32 vcc_hi, 0\n\t" \
    "s_cbranch_scc1 rr_p1x30_%=\n\t" \
    "s_cmp_eq_u32 s68, 0\n\t" \
    "s_cbranch_scc1 rr_F31_%=\n\t" \
    "s_sub_u32 s68, s68, 1\n\t" \
    "s_sleep 10\n\t" \
    "s_branch rr_p129_%=\n\t" \
    "rr_F31_%=:\n\t" \
    "v_mov_b32 v184, 6095104\n\t" \
    "v_mov_b32 v185, 13\n\t" \
    "global_store_dword v184, v185, s[36:37] sc1\n\t" \
    "s_waitcnt vmcnt(0)\n\t" \
    "s_branch rr_p1x30_%=\n\t" \
    "rr_p1x30_%=:\n\t" \
    "s_mov_b32 s58, 1\n\t" \
    "rr_pg21_%=:\n\t" \
    "global_load_dword v177, v6, s[36:37] sc1\n\t" \
    "s_waitcnt vmcnt(0)\n\t" \
    "v_cmp_le_i32 vcc, s65, v177\n\t" \
    "s_nop 1\n\t" \
    "s_lshr_b32 s69, vcc_hi, 1\n\t" \
    "s_and_b32 s69, s69, vcc_hi\n\t" \
    "s_and_b32 s54, s69, s96\n\t" \
    "s_andn2_b32 s96, s96, s54\n\t" \
    "s_mov_b32 s55, s54\n\t" \
    "s_lshr_b64 s[54:55], s[54:55], s50\n\t" \
    "s_and_b32 s69, s54, 85\n\t" \
    "s_cbranch_scc0 rr_pq32_%=\n\t" \
    "s_bitcmp1_b32 s54, 0\n\t" \
    "s_cbranch_scc0 rr_pk33_%=\n\t" \
    "global_load_dwordx4 a[0:3], v26, s[38:39] offset:0 sc1\n\t" \
    "global_load_dwordx4 a[4:7], v26, s[38:39] offset:1024 sc1\n\t" \
    "rr_pk33_%=:\n\t" \
    "s_bitcmp1_b32 s54, 2\n\t" \
    "s_cbranch_scc0 rr_pk34_%=\n\t" \
    "global_load_dwordx4 a[8:11], v27, s[38:39] offset:0 sc1\n\t" \
    "global_load_dwordx4 a[12:15], v27, s[38:39] offset:1024 sc1\n\t" \
    "rr_pk34_%=:\n\t" \
    "s_bitcmp1_b32 s54, 4\n\t" \
    "s_cbranch_scc0 rr_pk35_%=\n\t" \
    "global_load_dwordx4 a[16:19], v28, s[38:39] offset:0 sc1\n\t" \
    "global_load_dwordx4 a[20:23], v28, s[38:39] offset:1024 sc1\n\t" \
    "rr_pk35_%=:\n\t" \
    "s_bitcmp1_b32 s54, 6\n\t" \
    "s_cbranch_scc0 rr_pk36_%=\n\t" \
    "global_load_dwordx4 a[24:27], v29, s[38:39] offset:0 sc1\n\t" \
    "global_load_dwordx4 a[28:31], v29, s[38:39] offset:1024 sc1\n\t" \
    "rr_pk36_%=:\n\t" \
    "rr_pq32_%=:\n\t" \
    "s_and_b32 s69, s54, 21760\n\t" \
    "s_cbranch_scc0 rr_pq37_%=\n\t" \
    "s_bitcmp1_b32 s54, 8\n\t" \
    "s_cbranch_scc0 rr_pk38_%=\n\t" \
    "global_load_dwordx4 a[32:35], v30, s[38:39] offset:0 sc1\n\t" \
    "global_load_dwordx4 a[36:39], v30, s[38:39] offset:1024 sc1\n\t" \
    "rr_pk38_%=:\n\t" \
    "s_bitcmp1_b32 s54, 10\n\t" \
    "s_cbranch_scc0 rr_pk39_%=\n\t" \
    "global_load_dwordx4 a[40:43], v31, s[38:39] offset:0 sc1\n\t" \
    "global_load_dwordx4 a[44:47], v31, s[38:39] offset:1024 sc1\n\t" \
    "rr_pk39_%=:\n\t" \
    "s_bitcmp1_b32 s54, 12\n\t" \
    "s_cbranch_scc0 rr_pk40_%=\n\t" \
    "global_load_dwordx4 a[48:51], v32, s[38:39] offset:0 sc1\n\t" \
    "global_load_dwordx4 a[52:55], v32, s[38:39] offset:1024 sc1\n\t" \
    "rr_pk40_%=:\n\t" \
    "s_bitcmp1_b32 s54, 14\n\t" \
    "s_cbranch_scc0 rr_pk41_%=\n\t" \
    "global_load_dwordx4 a[56:59], v33, s[38:39] offset:0 sc1\n\t" \
    "global_load_dwordx4 a[60:63], v33, s[38:39] offset:1024 sc1\n\t" \
    "rr_pk41_%=:\n\t" \
    "rr_pq37_%=:\n\t" \
    "s_and_b32 s69, s54, 5570560\n\t" \
    "s_cbranch_scc0 rr_pq42_%=\n\t" \
    "s_bitcmp1_b32 s54, 16\n\t" \
    "s_cbranch_scc0 rr_pk43_%=\n\t" \
    "global_load_dwordx4 a[64:67], v34, s[38:39] offset:0 sc1\n\t" \
    "global_load_dwordx4 a[68:71], v34, s[38:39] offset:1024 sc1\n\t" \
    "rr_pk43_%=:\n\t" \
    "s_bitcmp1_b32 s54, 18\n\t" \
    "s_cbranch_scc0 rr_pk44_%=\n\t" \
    "global_load_dwordx4 a[72:75], v35, s[38:39] offset:0 sc1\n\t" \
    "global_load_dwordx4 a[76:79], v35, s[38:39] offset:1024 sc1\n\t" \
    "rr_pk44_%=:\n\t" \
    "s_bitcmp1_b32 s54, 20\n\t" \
    "s_cbranch_scc0 rr_pk45_%=\n\t" \
    "global_load_dwordx4 a[80:83], v36, s[38:39] offset:0 sc1\n\t" \
    "global_load_dwordx4 a[84:87], v36, s[38:39] offset:1024 sc1\n\t" \
    "rr_pk45_%=:\n\t" \
    "s_bitcmp1_b32 s54, 22\n\t" \
    "s_cbranch_scc0 rr_pk46_%=\n\t" \
    "global_load_dwordx4 a[88:91], v37, s[38:39] offset:0 sc1\n\t" \
    "global_load_dwordx4 a[92:95], v37, s[38:39] offset:1024 sc1\n\t" \
    "rr_pk46_%=:\n\t" \
    "rr_pq42_%=:\n\t" \
    "s_and_b32 s69, s54, 1426063360\n\t" \
    "s_cbranch_scc0 rr_pq47_%=\n\t" \
    "s_bitcmp1_b32 s54, 24\n\t" \
    "s_cbranch_scc0 rr_pk48_%=\n\t" \
    "global_load_dwordx4 a[96:99], v38, s[38:39] offset:0 sc1\n\t" \
    "global_load_dwordx4 a[100:103], v38, s[38:39] offset:1024 sc1\n\t" \
    "rr_pk48_%=:\n\t" \
    "s_bitcmp1_b32 s54, 26\n\t" \
    "s_cbranch_scc0 rr_pk49_%=\n\t" \
    "global_load_dwordx4 a[104:107], v39, s[38:39] offset:0 sc1\n\t" \
    "global_load_dwordx4 a[108:111], v39, s[38:39] offset:1024 sc1\n\t" \
    "rr_pk49_%=:\n\t" \
    "s_bitcmp1_b32 s54, 28\n\t" \
    "s_cbranch_scc0 rr_pk50_%=\n\t" \
    "global_load_dwordx4 a[112:115], v40, s[38:39] offset:0 sc1\n\t" \
    "global_load_dwordx4 a[116:119], v40, s[38:39] offset:1024 sc1\n\t" \
    "rr_pk50_%=:\n\t" \
    "s_bitcmp1_b32 s54, 30\n\t" \
    "s_cbranch_scc0 rr_pk51_%=\n\t" \
    "global_load_dwordx4 a[120:123], v41, s[38:39] offset:0 sc1\n\t" \
    "global_load_dwordx4 a[124:127], v41, s[38:39] offset:1024 sc1\n\t" \
    "rr_pk51_%=:\n\t" \
    "rr_pq47_%=:\n\t" \
    "s_cmp_eq_u32 s58, 0\n\t" \
    "s_cbranch_scc1 rr_pn22_%=\n\t" \
    "v_cmp_eq_u32 vcc, -1, v189\n\t" \
    "s_cbranch_vccz rr_p2x52_%=\n\t" \
    "rr_p253_%=:\n\t" \
    "s_mov_b32 exec_hi, 0\n\t" \
    "global_load_dword v189, v187, s[52:53] sc1\n\t" \
    "s_mov_b64 exec, -1\n\t" \
    "s_waitcnt vmcnt(0)\n\t" \
    "v_cmp_eq_u32 vcc, -1, v189\n\t" \
    "s_cbranch_vccz rr_p2x52_%=\n\t" \
    "s_cmp_eq_u32 s68, 0\n\t" \
    "s_cbranch_scc1 rr_F54_%=\n\t" \
    "s_sub_u32 s68, s68, 1\n\t" \
    "s_sleep 10\n\t" \
    "s_branch rr_p253_%=\n\t" \
    "rr_F54_%=:\n\t" \
    "v_mov_b32 v184, 6095104\n\t" \
    "v_mov_b32 v185, 13\n\t" \
    "global_store_dword v184, v185, s[36:37] sc1\n\t" \
    "s_waitcnt vmcnt(0)\n\t" \
    "s_branch rr_p2x52_%=\n\t" \
    "rr_p2x52_%=:\n\t" \
    "s_mov_b32 s71, 3\n\t" \
    "rr_E55_%=:\n\t" \
    "s_memtime s[54:55]\n\t" \
    "s_nop 0\n\t" \
    "s_memrealtime s[4:5]\n\t" \
    "s_nop 0\n\t" \
    "s_memrealtime s[6:7]\n\t" \
    "s_nop 0\n\t" \
    "s_memrealtime s[8:9]\n\t" \
    "s_nop 0\n\t" \
    "s_memrealtime s[10:11]\n\t" \
    "s_nop 0\n\t" \
    "s_memrealtime s[12:13]\n\t" \
    "s_nop 0\n\t" \
    "s_memrealtime s[14:15]\n\t" \
    "s_nop 0\n\t" \
    "s_memrealtime s[16:17]\n\t" \
    "s_nop 0\n\t" \
    "s_memrealtime s[18:19]\n\t" \
    "s_nop 0\n\t" \
    "s_memrealtime s[20:21]\n\t" \
    "s_nop 0\n\t" \
    "s_memrealtime s[22:23]\n\t" \
    "s_nop 0\n\t" \
    "s_memrealtime s[24:25]\n\t" \
    "s_nop 0\n\t" \
    "s_memrealtime s[26:27]\n\t" \
    "s_nop 0\n\t" \
    "s_memrealtime s[28:29]\n\t" \
    "s_nop 0\n\t" \
    "s_memtime s[56:57]\n\t" \
    "s_mov_b32 s70, 1\n\t" \
    "s_sub_u32 s71, s71, 1\n\t" \
    "s_cselect_b32 s69, 0, -1\n\t" \
    "s_waitcnt lgkmcnt(0)\n\t" \
    "s_sub_u32 s54, s56, s54\n\t" \
    "s_and_b32 s54, s54, s69\n\t" \
    "s_cmp_gt_u32 s54, 124\n\t" \
    "s_cbranch_scc1 rr_E55_%=\n\t" \
    "s_cmp_eq_u32 s4, s28\n\t" \
    "s_addc_u32 s70, s70, 0\n\t" \
    "s_cmp_eq_u32 s6, s28\n\t" \
    "s_addc_u32 s70, s70, 0\n\t" \
    "s_cmp_eq_u32 s8, s28\n\t" \
    "s_addc_u32 s70, s70, 0\n\t" \
    "s_cmp_eq_u32 s10, s28\n\t" \
    "s_addc_u32 s70, s70, 0\n\t" \
    "s_cmp_eq_u32 s12, s28\n\t" \
    "s_addc_u32 s70, s70, 0\n\t" \
    "s_cmp_eq_u32 s14, s28\n\t" \
    "s_addc_u32 s70, s70, 0\n\t" \
    "s_cmp_eq_u32 s16, s28\n\t" \
    "s_addc_u32 s70, s70, 0\n\t" \
    "s_cmp_eq_u32 s18, s28\n\t" \
    "s_addc_u32 s70, s70, 0\n\t" \
    "s_cmp_eq_u32 s20, s28\n\t" \
    "s_addc_u32 s70, s70, 0\n\t" \
    "s_cmp_eq_u32 s22, s28\n\t" \
    "s_addc_u32 s70, s70, 0\n\t" \
    "s_cmp_eq_u32 s24, s28\n\t" \
    "s_addc_u32 s70, s70, 0\n\t" \
    "s_cmp_eq_u32 s26, s28\n\t" \
    "s_addc_u32 s70, s70, 0\n\t" \
    "s_mov_b32 s75, s73\n\t" \
    "s_mov_b32 s76, s74\n\t" \
    "s_lshl_b32 s70, s70, 3\n\t" \
    "s_sub_u32 s73, s56, s70\n\t" \
    "s_lshl_b32 s74, s28, 4\n\t" \
    "s_sub_u32 s69, s73, s75\n\t" \
    "s_sub_u32 s70, s74, s76\n\t" \
    "s_sub_u32 s71, s70, 1600\n\t" \
    "s_cmp_lt_u32 s71, 400000\n\t" \
    "s_cbranch_scc0 rr_K57_%=\n\t" \
    "v_cvt_f32_u32 v190, s69\n\t" \
    "v_cvt_f32_u32 v191, s70\n\t" \
    "v_rcp_f32 v192, v190\n\t" \
    "v_rcp_f32 v193, v191\n\t" \
    "v_mul_f32 v192, v192, v191\n\t" \
    "v_mul_f32 v193, v193, v190\n\t" \
    "v_mul_f32 v192, 0x4f800000, v192\n\t" \
    "v_mul_f32 v193, 0x4f000000, v193\n\t" \
    "v_cvt_u32_f32 v192, v192\n\t" \
    "v_cvt_u32_f32 v193, v193\n\t" \
    "v_readfirstlane_b32 s69, v192\n\t" \
    "v_readfirstlane_b32 s70, v193\n\t" \
    "s_nop 1\n\t" \
    "s_sub_u32 s69, s69, s78\n\t" \
    "s_ashr_i32 s69, s69, 2\n\t" \
    "s_add_u32 s78, s78, s69\n\t" \
    "s_sub_u32 s70, s70, s77\n\t" \
    "s_ashr_i32 s70, s70, 2\n\t" \
    "s_add_u32 s77, s77, s70\n\t" \
    "rr_K57_%=:\n\t" \
    "v_mov_b32 v184, v189\n\t" \
    "v_subrev_u32 v184, s80, v184\n\t" \
    "s_nop 1\n\t" \
    "v_max_i32_dpp v184, v184, v184 row_ror:1 row_mask:0xf bank_mask:0xf\n\t" \
    "s_nop 1\n\t" \
    "v_max_i32_dpp v184, v184, v184 row_ror:2 row_mask:0xf bank_mask:0xf\n\t" \
    "s_nop 1\n\t" \
    "v_max_i32_dpp v184, v184, v184 row_ror:4 row_mask:0xf bank_mask:0xf\n\t" \
    "s_nop 1\n\t" \
    "v_max_i32_dpp v184, v184, v184 row_ror:8 row_mask:0xf bank_mask:0xf\n\t" \
    "s_nop 0\n\t" \
    "v_readlane_b32 s69, v184, 0\n\t" \
    "v_readlane_b32 s71, v184, 16\n\t" \
    "v_readlane_b32 s70, v184, 32\n\t" \
    "v_readlane_b32 s81, v184, 48\n\t" \
    "s_max_i32 s69, s69, s71\n\t" \
    "s_nop 0\n\t" \
    "s_max_i32 s71, s70, s81\n\t" \
    "s_max_i32 s69, s69, s71\n\t" \
    "s_add_u32 s81, s69, s80\n\t" \
    "s_mov_b32 s58, 0\n\t" \
    "s_cmp_eq_u32 s96, 0\n\t" \
    "s_cbranch_scc1 rr_pe23_%=\n\t" \
    "s_add_u32 s79, s71, s80\n\t" \
    "s_sub_u32 s79, s79, 880\n\t" \
    "s_sub_u32 s70, s79, s74\n\t" \
    "s_max_i32 s70, s70, 0\n\t" \
    "s_min_i32 s70, s70, 16000\n\t" \
    "s_lshl_b32 s70, s70, 1\n\t" \
    "s_mul_hi_u32 s70, s70, s77\n\t" \
    "s_add_u32 s70, s70, s73\n\t" \
    "rr_W58_%=:\n\t" \
    "s_memtime s[56:57]\n\t" \
    "s_waitcnt lgkmcnt(0)\n\t" \
    "s_sub_u32 s69, s70, s56\n\t" \
    "s_cmp_gt_i32 s69, 24\n\t" \
    "s_cbranch_scc1 rr_W58_%=\n\t" \
    "s_branch rr_pg21_%=\n\t" \
    "rr_pn22_%=:\n\t" \
    "s_cmp_eq_u32 s96, 0\n\t" \
    "s_cbranch_scc1 rr_pe23_%=\n\t" \
    "s_cmp_eq_u32 s68, 0\n\t" \
    "s_cbranch_scc1 rr_F59_%=\n\t" \
    "s_sub_u32 s68, s68, 1\n\t" \
    "s_sleep 2\n\t" \
    "s_branch rr_pg21_%=\n\t" \
    "rr_F59_%=:\n\t" \
    "v_mov_b32 v184, 6095104\n\t" \
    "v_mov_b32 v185, 10\n\t" \
    "global_store_dword v184, v185, s[36:37] sc1\n\t" \
    "s_waitcnt vmcnt(0)\n\t" \
    "s_branch rr_pe23_%=\n\t" \
    "rr_pe23_%=:\n\t" \
    "s_mov_b32 s83, s65\n\t" \
    "s_cmp_lg_u32 s65, 0\n\t" \
    "s_cbranch_scc1 rr_u227_%=\n\t" \
    "s_memtime s[56:57]\n\t" \
    "v_mov_b32 v184, v189\n\t" \
    "v_subrev_u32 v184, s80, v184\n\t" \
    "s_nop 1\n\t" \
    "v_max_i32_dpp v184, v184, v184 row_ror:1 row_mask:0xf bank_mask:0xf\n\t" \
    "s_nop 1\n\t" \
    "v_max_i32_dpp v184, v184, v184 row_ror:2 row_mask:0xf bank_mask:0xf\n\t" \
    "s_nop 1\n\t" \
    "v_max_i32_dpp v184, v184, v184 row_ror:4 row_mask:0xf bank_mask:0xf\n\t" \
    "s_nop 1\n\t" \
    "v_max_i32_dpp v184, v184, v184 row_ror:8 row_mask:0xf bank_mask:0xf\n\t" \
    "s_nop 0\n\t" \
    "v_readlane_b32 s69, v184, 0\n\t" \
    "v_readlane_b32 s71, v184, 16\n\t" \
    "s_nop 1\n\t" \
    "s_max_i32 s69, s69, s71\n\t" \
    "s_add_u32 s81, s69, s80\n\t" \
    "s_waitcnt lgkmcnt(0)\n\t" \
    "s_sub_u32 s69, s56, s73\n\t" \
    "s_mul_hi_u32 s69, s69, s78\n\t" \
    "s_add_u32 s69, s69, s74\n\t" \
    "s_add_u32 s69, s69, 208\n\t" \
    "s_sub_u32 s69, s69, s81\n\t" \
    "s_max_i32 s69, s69, 0\n\t" \
    "s_add_u32 s69, s69, s81\n\t" \
    "v_and_b32_e64 v184, -2, s69\n\t" \
    "s_add_u32 s69, s69, 160\n\t" \
    "v_and_b32_e64 v185, -2, s69\n\t" \
    "s_add_u32 s69, s65, 0\n\t" \
    "s_bitcmp1_b32 s69, 0\n\t" \
    "s_cselect_b32 s70, s63, s62\n\t" \
    "s_bfe_u32 s69, s69, 0x20001\n\t" \
    "s_lshl_b32 s69, s69, 19\n\t" \
    "s_add_u32 s69, s69, s70\n\t" \
    "s_add_u32 s52, s36, s69\n\t" \
    "s_addc_u32 s53, s37, 0\n\t" \
    "global_store_dword v3, v185, s[52:53] sc1\n\t" \
    "s_branch rr_h126_%=\n\t" \
    "rr_pf13_%=:\n\t" \
    "s_cmp_gt_u32 s65, 62\n\t" \
    "s_cbranch_scc1 rr_end8_%=\n\t" \
    "v_min_i32_dpp v177, v177, v177 row_ror:1 row_mask:0xf bank_mask:0xf\n\t" \
    "s_nop 1\n\t" \
    "v_min_i32_dpp v177, v177, v177 row_ror:2 row_mask:0xf bank_mask:0xf\n\t" \
    "s_nop 1\n\t" \
    "v_min_i32_dpp v177, v177, v177 row_ror:4 row_mask:0xf bank_mask:0xf\n\t" \
    "s_nop 1\n\t" \
    "v_min_i32_dpp v177, v177, v177 row_ror:8 row_mask:0xf bank_mask:0xf\n\t" \
    "s_nop 0\n\t" \
    "v_readlane_b32 s69, v177, 0\n\t" \
    "v_readlane_b32 s70, v177, 16\n\t" \
    "s_nop 1\n\t" \
    "s_min_i32 s69, s69, s70\n\t" \
    "s_max_i32 s83, s83, s69\n\t" \
    "rr_WF60_%=:\n\t" \
    "s_cmp_ge_i32 s83, s65\n\t" \
    "s_cbranch_scc1 rr_WFok61_%=\n\t" \
    "global_load_dword v184, v6, s[36:37] sc1\n\t" \
    "s_waitcnt vmcnt(0)\n\t" \
    "v_min_i32_dpp v184, v184, v184 row_ror:1 row_mask:0xf bank_mask:0xf\n\t" \
    "s_nop 1\n\t" \
    "v_min_i32_dpp v184, v184, v184 row_ror:2 row_mask:0xf bank_mask:0xf\n\t" \
    "s_nop 1\n\t" \
    "v_min_i32_dpp v184, v184, v184 row_ror:4 row_mask:0xf bank_mask:0xf\n\t" \
    "s_nop 1\n\t" \
    "v_min_i32_dpp v184, v184, v184 row_ror:8 row_mask:0xf bank_mask:0xf\n\t" \
    "s_nop 0\n\t" \
    "v_readlane_b32 s69, v184, 0\n\t" \
    "v_readlane_b32 s70, v184, 16\n\t" \
    "s_nop 1\n\t" \
    "s_min_i32 s83, s69, s70\n\t" \
    "s_cmp_ge_i32 s83, s65\n\t" \
    "s_cbranch_scc1 rr_WFok61_%=\n\t" \
    "s_cmp_eq_u32 s68, 0\n\t" \
    "s_cbranch_scc1 rr_F62_%=\n\t" \
    "s_sub_u32 s68, s68, 1\n\t" \
    "s_sleep 2\n\t" \
    "s_branch rr_WF60_%=\n\t" \
    "rr_F62_%=:\n\t" \
    "v_mov_b32 v184, 6095104\n\t" \
    "v_mov_b32 v185, 10\n\t" \
    "global_store_dword v184, v185, s[36:37] sc1\n\t" \
    "s_waitcnt vmcnt(0)\n\t" \
    "s_branch rr_WFok61_%=\n\t" \
    "rr_WFok61_%=:\n\t" \
    "s_mul_i32 s70, s67, 7\n\t" \
    "s_add_u32 s69, s65, 0\n\t" \
    "s_xor_b32 s69, s69, s70\n\t" \
    "s_and_b32 s69, s69, 63\n\t" \
    "s_lshl_b32 s69, s69, 15\n\t" \
    "s_mul_i32 s70, s67, 2097152\n\t" \
    "s_add_u32 s69, s69, s70\n\t" \
    "s_add_u32 s69, s69, 48234496\n\t" \
    "s_add_u32 s38, s36, s69\n\t" \
    "s_addc_u32 s39, s37, 0\n\t" \
    "s_mul_hi_u32 s69, s85, s78\n\t" \
    "s_add_u32 s79, s81, s69\n\t" \
    "s_add_u32 s79, s79, s87\n\t" \
    "s_cmp_ge_u32 s65, 38\n\t" \
    "s_cselect_b32 s69, 28, 0\n\t" \
    "s_sub_u32 s79, s79, s69\n\t" \
    "s_cmp_lt_u32 s65, 10\n\t" \
    "s_cselect_b32 s69, 96, 0\n\t" \
    "s_add_u32 s79, s79, s69\n\t" \
    "s_cmp_ge_u32 s65, 59\n\t" \
    "s_cselect_b32 s69, 32, 0\n\t" \
    "s_add_u32 s79, s79, s69\n\t" \
    "s_add_u32 s80, s79, s88\n\t" \
    "s_add_u32 s79, s79, -96\n\t" \
    "s_add_u32 s79, s79, s42\n\t" \
    "s_sub_u32 s70, s79, s74\n\t" \
    "s_max_i32 s70, s70, 0\n\t" \
    "s_min_i32 s70, s70, 16000\n\t" \
    "s_lshl_b32 s70, s70, 1\n\t" \
    "s_mul_hi_u32 s70, s70, s77\n\t" \
    "s_add_u32 s70, s70, s73\n\t" \
    "rr_W63_%=:\n\t" \
    "s_memtime s[56:57]\n\t" \
    "s_waitcnt lgkmcnt(0)\n\t" \
    "s_sub_u32 s69, s70, s56\n\t" \
    "s_cmp_gt_i32 s69, 24\n\t" \
    "s_cbranch_scc1 rr_W63_%=\n\t" \
    "s_add_u32 s69, s65, 4\n\t" \
    "s_min_i32 s69, s69, 62\n\t" \
    "s_cmp_ge_i32 s83, s69\n\t" \
    "s_cbranch_scc1 rr_nfl64_%=\n\t" \
    "global_load_dword v177, v6, s[36:37] sc1\n\t" \
    "rr_nfl64_%=:\n\t" \
    "global_load_dwordx4 a[0:3], v26, s[38:39] offset:0 sc1\n\t" \
    "global_load_dwordx4 a[4:7], v26, s[38:39] offset:1024 sc1\n\t" \
    "global_load_dwordx4 a[8:11], v27, s[38:39] offset:0 sc1\n\t" \
    "global_load_dwordx4 a[12:15], v27, s[38:39] offset:1024 sc1\n\t" \
    "global_load_dwordx4 a[16:19], v28, s[38:39] offset:0 sc1\n\t" \
    "global_load_dwordx4 a[20:23], v28, s[38:39] offset:1024 sc1\n\t" \
    "global_load_dwordx4 a[24:27], v29, s[38:39] offset:0 sc1\n\t" \
    "global_load_dwordx4 a[28:31], v29, s[38:39] offset:1024 sc1\n\t" \
    "global_load_dwordx4 a[32:35], v30, s[38:39] offset:0 sc1\n\t" \
    "global_load_dwordx4 a[36:39], v30, s[38:39] offset:1024 sc1\n\t" \
    "global_load_dwordx4 a[40:43], v31, s[38:39] offset:0 sc1\n\t" \
    "global_load_dwordx4 a[44:47], v31, s[38:39] offset:1024 sc1\n\t" \
    "global_load_dwordx4 a[48:51], v32, s[38:39] offset:0 sc1\n\t" \
    "global_load_dwordx4 a[52:55], v32, s[38:39] offset:1024 sc1\n\t" \
    "global_load_dwordx4 a[56:59], v33, s[38:39] offset:0 sc1\n\t" \
    "global_load_dwordx4 a[60:63], v33, s[38:39] offset:1024 sc1\n\t" \
    "global_load_dwordx4 a[64:67], v34, s[38:39] offset:0 sc1\n\t" \
    "global_load_dwordx4 a[68:71], v34, s[38:39] offset:1024 sc1\n\t" \
    "global_load_dwordx4 a[72:75], v35, s[38:39] offset:0 sc1\n\t" \
    "global_load_dwordx4 a[76:79], v35, s[38:39] offset:1024 sc1\n\t" \
    "global_load_dwordx4 a[80:83], v36, s[38:39] offset:0 sc1\n\t" \
    "global_load_dwordx4 a[84:87], v36, s[38:39] offset:1024 sc1\n\t" \
    "global_load_dwordx4 a[88:91], v37, s[38:39] offset:0 sc1\n\t" \
    "global_load_dwordx4 a[92:95], v37, s[38:39] offset:1024 sc1\n\t" \
    "global_load_dwordx4 a[96:99], v38, s[38:39] offset:0 sc1\n\t" \
    "global_load_dwordx4 a[100:103], v38, s[38:39] offset:1024 sc1\n\t" \
    "global_load_dwordx4 a[104:107], v39, s[38:39] offset:0 sc1\n\t" \
    "global_load_dwordx4 a[108:111], v39, s[38:39] offset:1024 sc1\n\t" \
    "global_load_dwordx4 a[112:115], v40, s[38:39] offset:0 sc1\n\t" \
    "global_load_dwordx4 a[116:119], v40, s[38:39] offset:1024 sc1\n\t" \
    "global_load_dwordx4 a[120:123], v41, s[38:39] offset:0 sc1\n\t" \
    "global_load_dwordx4 a[124:127], v41, s[38:39] offset:1024 sc1\n\t" \
    "s_cmp_eq_u32 s51, 0\n\t" \
    "s_cbranch_scc1 rr_nb65_%=\n\t" \
    "s_add_u32 s69, s65, 1\n\t" \
    "s_lshl_b32 s69, s69, 15\n\t" \
    "v_xor_b32 v201, s69, v199\n\t" \
    "v_xor_b32 v202, s69, v200\n\t" \
    "global_load_dword v201, v201, s[36:37] sc1\n\t" \
    "global_load_dword v202, v202, s[36:37] sc1\n\t" \
    "rr_nb65_%=:\n\t" \
    "s_mul_hi_u32 s69, s85, s78\n\t" \
    "s_add_u32 s79, s81, s69\n\t" \
    "s_add_u32 s79, s79, s87\n\t" \
    "s_cmp_ge_u32 s65, 38\n\t" \
    "s_cselect_b32 s69, 28, 0\n\t" \
    "s_sub_u32 s79, s79, s69\n\t" \
    "s_cmp_lt_u32 s65, 10\n\t" \
    "s_cselect_b32 s69, 96, 0\n\t" \
    "s_add_u32 s79, s79, s69\n\t" \
    "s_cmp_ge_u32 s65, 59\n\t" \
    "s_cselect_b32 s69, 32, 0\n\t" \
    "s_add_u32 s79, s79, s69\n\t" \
    "s_add_u32 s80, s79, s88\n\t" \
    "s_add_u32 s79, s79, 320\n\t" \
    "s_add_u32 s79, s79, s43\n\t" \
    "s_sub_u32 s70, s79, s74\n\t" \
    "s_max_i32 s70, s70, 0\n\t" \
    "s_min_i32 s70, s70, 16000\n\t" \
    "s_lshl_b32 s70, s70, 1\n\t" \
    "s_mul_hi_u32 s70, s70, s77\n\t" \
    "s_add_u32 s70, s70, s73\n\t" \
    "rr_W66_%=:\n\t" \
    "s_memtime s[56:57]\n\t" \
    "s_waitcnt lgkmcnt(0)\n\t" \
    "s_sub_u32 s69, s70, s56\n\t" \
    "s_cmp_gt_i32 s69, 24\n\t" \
    "s_cbranch_scc1 rr_W66_%=\n\t" \
    "s_cmp_eq_u32 s51, 0\n\t" \
    "s_cbranch_scc1 rr_arm67_%=\n\t" \
    "global_store_dwordx4 v0, v[172:175], s[40:41] offset:0 sc1\n\t" \
    "global_store_dwordx4 v0, v[172:175], s[40:41] offset:1024 sc1\n\t" \
    "global_store_dwordx4 v0, v[172:175], s[40:41] offset:2048 sc1\n\t" \
    "global_store_dwordx4 v0, v[172:175], s[40:41] offset:3072 sc1\n\t" \
    "global_store_dwordx4 v1, v[172:175], s[40:41] offset:0 sc1\n\t" \
    "global_store_dwordx4 v1, v[172:175], s[40:41] offset:1024 sc1\n\t" \
    "global_store_dwordx4 v1, v[172:175], s[40:41] offset:2048 sc1\n\t" \
    "global_store_dwordx4 v1, v[172:175], s[40:41] offset:3072 sc1\n\t" \
    "s_cmp_gt_u32 s65, 61\n\t" \
    "s_cbranch_scc1 rr_arm67_%=\n\t" \
    "s_add_u32 s69, s65, 2\n\t" \
    "s_bitcmp1_b32 s69, 0\n\t" \
    "s_cselect_b32 s70, s63, s62\n\t" \
    "s_bfe_u32 s69, s69, 0x20001\n\t" \
    "s_lshl_b32 s69, s69, 19\n\t" \
    "s_add_u32 s69, s69, s70\n\t" \
    "s_add_u32 s46, s36, s69\n\t" \
    "s_addc_u32 s47, s37, 0\n\t" \
    "global_store_dwordx4 v164, v[172:175], s[46:47] sc1\n\t" \
    "global_store_dwordx4 v165, v[172:175], s[46:47] sc1\n\t" \
    "global_store_dwordx4 v166, v[172:175], s[46:47] sc1\n\t" \
    "global_store_dwordx4 v167, v[172:175], s[46:47] sc1\n\t" \
    "rr_arm67_%=:\n\t" \
    "s_waitcnt vmcnt(0)\n\t" \
    "v_min_i32_dpp v177, v177, v177 row_ror:1 row_mask:0xf bank_mask:0xf\n\t" \
    "s_nop 1\n\t" \
    "v_min_i32_dpp v177, v177, v177 row_ror:2 row_mask:0xf bank_mask:0xf\n\t" \
    "s_nop 1\n\t" \
    "v_min_i32_dpp v177, v177, v177 row_ror:4 row_mask:0xf bank_mask:0xf\n\t" \
    "s_nop 1\n\t" \
    "v_min_i32_dpp v177, v177, v177 row_ror:8 row_mask:0xf bank_mask:0xf\n\t" \
    "s_nop 0\n\t" \
    "v_readlane_b32 s69, v177, 0\n\t" \
    "v_readlane_b32 s70, v177, 16\n\t" \
    "s_nop 1\n\t" \
    "s_min_i32 s69, s69, s70\n\t" \
    "s_max_i32 s83, s83, s69\n\t" \
    "s_cmp_lg_u32 s65, 40\n\t" \
    "s_cbranch_scc1 rr_f268_%=\n\t" \
    "s_cmp_lg_u32 s67, 35\n\t" \
    "s_cbranch_scc1 rr_f268_%=\n\t" \
    "rr_f2l69_%=:\n\t" \
    "global_load_dword v186, v178, s[36:37] sc1\n\t" \
    "s_waitcnt vmcnt(0)\n\t" \
    "v_cmp_ne_u32 vcc, 1, v186\n\t" \
    "s_cbranch_vccz rr_f268_%=\n\t" \
    "s_cmp_eq_u32 s68, 0\n\t" \
    "s_cbranch_scc1 rr_F70_%=\n\t" \
    "s_sub_u32 s68, s68, 1\n\t" \
    "s_sleep 2\n\t" \
    "s_branch rr_f2l69_%=\n\t" \
    "rr_F70_%=:\n\t" \
    "v_mov_b32 v187, 6095104\n\t" \
    "v_mov_b32 v188, 11\n\t" \
    "global_store_dword v187, v188, s[36:37] sc1\n\t" \
    "s_waitcnt vmcnt(0)\n\t" \
    "s_branch rr_f268_%=\n\t" \
    "rr_f268_%=:\n\t" \
    "s_cmp_eq_u32 s51, 0\n\t" \
    "s_cbranch_scc1 rr_sl24_%=\n\t" \
    "s_add_u32 s69, s65, -1\n\t" \
    "s_bitcmp1_b32 s69, 0\n\t" \
    "s_cselect_b32 s70, s63, s62\n\t" \
    "s_bfe_u32 s69, s69, 0x20001\n\t" \
    "s_lshl_b32 s69, s69, 19\n\t" \
    "s_add_u32 s69, s69, s70\n\t" \
    "s_add_u32 s46, s36, s69\n\t" \
    "s_addc_u32 s47, s37, 0\n\t" \
    "s_add_u32 s69, s65, -1\n\t" \
    "s_lshr_b32 s69, s69, 1\n\t" \
    "s_xor_b32 s69, s69, s33\n\t" \
    "s_and_b32 s69, s69, 1\n\t" \
    "s_lshl_b32 s69, s69, 14\n\t" \
    "s_or_b32 s46, s46, s69\n\t" \
    "s_add_u32 s79, s80, 480\n\t" \
    "s_sub_u32 s70, s79, s74\n\t" \
    "s_max_i32 s70, s70, 0\n\t" \
    "s_min_i32 s70, s70, 16000\n\t" \
    "s_lshl_b32 s70, s70, 1\n\t" \
    "s_mul_hi_u32 s70, s70, s77\n\t" \
    "s_add_u32 s70, s70, s73\n\t" \
    "rr_W71_%=:\n\t" \
    "s_memtime s[56:57]\n\t" \
    "s_waitcnt lgkmcnt(0)\n\t" \
    "s_sub_u32 s69, s70, s56\n\t" \
    "s_cmp_gt_i32 s69, 24\n\t" \
    "s_cbranch_scc1 rr_W71_%=\n\t" \
    "v_add_u32 v187, 4096, v7\n\t" \
    "global_load_dword v184, v187, s[46:47] sc1\n\t" \
    "s_waitcnt vmcnt(0)\n\t" \
    "v_cmp_eq_u32 vcc, -1, v184\n\t" \
    "s_cbranch_vccz rr_Hok73_%=\n\t" \
    "rr_H72_%=:\n\t" \
    "global_load_dword v184, v187, s[46:47] sc1\n\t" \
    "s_waitcnt vmcnt(0)\n\t" \
    "v_cmp_eq_u32 vcc, -1, v184\n\t" \
    "s_cbranch_vccz rr_Hc74_%=\n\t" \
    "s_cmp_eq_u32 s68, 0\n\t" \
    "s_cbranch_scc1 rr_F75_%=\n\t" \
    "s_sub_u32 s68, s68, 1\n\t" \
    "s_sleep 4\n\t" \
    "s_branch rr_H72_%=\n\t" \
    "rr_F75_%=:\n\t" \
    "v_mov_b32 v185, 6095104\n\t" \
    "v_mov_b32 v186, 19\n\t" \
    "global_store_dword v185, v186, s[36:37] sc1\n\t" \
    "s_waitcnt vmcnt(0)\n\t" \
    "s_branch rr_Hc74_%=\n\t" \
    "rr_Hc74_%=:\n\t" \
    "rr_Hok73_%=:\n\t" \
    "v_subrev_u32 v184, s80, v184\n\t" \
    "s_nop 1\n\t" \
    "v_max_i32_dpp v184, v184, v184 row_ror:1 row_mask:0xf bank_mask:0xf\n\t" \
    "s_nop 1\n\t" \
    "v_max_i32_dpp v184, v184, v184 row_ror:2 row_mask:0xf bank_mask:0xf\n\t" \
    "s_nop 1\n\t" \
    "v_max_i32_dpp v184, v184, v184 row_ror:4 row_mask:0xf bank_mask:0xf\n\t" \
    "s_nop 1\n\t" \
    "v_max_i32_dpp v184, v184, v184 row_ror:8 row_mask:0xf bank_mask:0xf\n\t" \
    "s_nop 0\n\t" \
    "v_readlane_b32 s69, v184, 0\n\t" \
    "v_readlane_b32 s71, v184, 16\n\t" \
    "s_nop 1\n\t" \
    "s_max_i32 s69, s69, s71\n\t" \
    "s_add_u32 s81, s69, s80\n\t" \
    "s_mul_hi_u32 s69, s85, s78\n\t" \
    "s_add_u32 s79, s81, s69\n\t" \
    "s_add_u32 s79, s79, s87\n\t" \
    "s_cmp_ge_u32 s65, 37\n\t" \
    "s_cselect_b32 s69, 28, 0\n\t" \
    "s_sub_u32 s79, s79, s69\n\t" \
    "s_cmp_lt_u32 s65, 9\n\t" \
    "s_cselect_b32 s69, 96, 0\n\t" \
    "s_add_u32 s79, s79, s69\n\t" \
    "s_cmp_ge_u32 s65, 58\n\t" \
    "s_cselect_b32 s69, 32, 0\n\t" \
    "s_add_u32 s79, s79, s69\n\t" \
    "s_add_u32 s80, s79, s88\n\t" \
    "s_add_u32 s79, s79, 96\n\t" \
    "s_add_u32 s69, s65, 1\n\t" \
    "s_mul_i32 s70, s69, 171\n\t" \
    "s_lshr_b32 s70, s70, 9\n\t" \
    "s_mul_i32 s70, s70, 3\n\t" \
    "s_sub_u32 s69, s69, s70\n\t" \
    "s_mul_i32 s69, s69, 36\n\t" \
    "s_lshl_b32 s69, s69, 14\n\t" \
    "s_add_u32 s69, s69, s64\n\t" \
    "s_add_u32 s69, s69, 4194304\n\t" \
    "s_add_u32 s40, s36, s69\n\t" \
    "s_addc_u32 s41, s37, 0\n\t" \
    "s_sub_u32 s70, s79, s74\n\t" \
    "s_max_i32 s70, s70, 0\n\t" \
    "s_min_i32 s70, s70, 16000\n\t" \
    "s_lshl_b32 s70, s70, 1\n\t" \
    "s_mul_hi_u32 s70, s70, s77\n\t" \
    "s_add_u32 s70, s70, s73\n\t" \
    "rr_W76_%=:\n\t" \
    "s_memtime s[56:57]\n\t" \
    "s_waitcnt lgkmcnt(0)\n\t" \
    "s_sub_u32 s69, s70, s56\n\t" \
    "s_cmp_gt_i32 s69, 24\n\t" \
    "s_cbranch_scc1 rr_W76_%=\n\t" \
    "global_store_dwordx4 v196, v[172:175], s[40:41] sc1\n\t" \
    "global_store_dwordx4 v197, v[172:175], s[40:41] sc1\n\t" \
    "global_store_dwordx4 v198, v[172:175], s[40:41] sc1\n\t" \
    "s_branch rr_u227_%=\n\t" \
    "rr_sl24_%=:\n\t" \
    "ds_read_b64 v[184:185], v9\n\t" \
    "s_sub_u32 s70, s65, 1\n\t" \
    "s_waitcnt lgkmcnt(0)\n\t" \
    "v_readfirstlane_b32 s69, v185\n\t" \
    "v_readfirstlane_b32 s81, v184\n\t" \
    "s_nop 0\n\t" \
    "s_cmp_eq_u32 s69, s70\n\t" \
    "s_cbranch_scc1 rr_u227_%=\n\t" \
    "s_cmp_eq_u32 s68, 0\n\t" \
    "s_cbranch_scc1 rr_F77_%=\n\t" \
    "s_sub_u32 s68, s68, 1\n\t" \
    "s_sleep 2\n\t" \
    "s_branch rr_sl24_%=\n\t" \
    "rr_F77_%=:\n\t" \
    "v_mov_b32 v184, 6095104\n\t" \
    "v_mov_b32 v185, 14\n\t" \
    "global_store_dword v184, v185, s[36:37] sc1\n\t" \
    "s_waitcnt vmcnt(0)\n\t" \
    "s_branch rr_u227_%=\n\t" \
    "rr_u227_%=:\n\t" \
    "s_add_u32 s69, s65, -1\n\t" \
    "s_bitcmp1_b32 s69, 0\n\t" \
    "s_cselect_b32 s70, s63, s62\n\t" \
    "s_bfe_u32 s69, s69, 0x20001\n\t" \
    "s_lshl_b32 s69, s69, 19\n\t" \
    "s_add_u32 s69, s69, s70\n\t" \
    "s_add_u32 s46, s36, s69\n\t" \
    "s_addc_u32 s47, s37, 0\n\t" \
    "s_add_u32 s69, s65, -1\n\t" \
    "s_lshr_b32 s69, s69, 1\n\t" \
    "s_xor_b32 s69, s69, s33\n\t" \
    "s_and_b32 s69, s69, 1\n\t" \
    "s_lshl_b32 s69, s69, 14\n\t" \
    "s_or_b32 s46, s46, s69\n\t" \
    "s_cmp_lg_u32 s65, 1\n\t" \
    "s_cbranch_scc1 rr_Oc80_%=\n\t" \
    "s_add_u32 s79, s81, 480\n\t" \
    "s_sub_u32 s70, s79, s74\n\t" \
    "s_max_i32 s70, s70, 0\n\t" \
    "s_min_i32 s70, s70, 16000\n\t" \
    "s_lshl_b32 s70, s70, 1\n\t" \
    "s_mul_hi_u32 s70, s70, s77\n\t" \
    "s_add_u32 s70, s70, s73\n\t" \
    "rr_W81_%=:\n\t" \
    "s_memtime s[56:57]\n\t" \
    "s_waitcnt lgkmcnt(0)\n\t" \
    "s_sub_u32 s69, s70, s56\n\t" \
    "s_cmp_gt_i32 s69, 24\n\t" \
    "s_cbranch_scc1 rr_W81_%=\n\t" \
    "global_load_dword v184, v46, s[46:47] sc1\n\t" \
    "s_waitcnt vmcnt(0)\n\t" \
    "v_cmp_eq_u32 vcc, -1, v184\n\t" \
    "s_cbranch_vccz rr_Ook79_%=\n\t" \
    "rr_O78_%=:\n\t" \
    "global_load_dword v184, v46, s[46:47] offset:1536 sc1\n\t" \
    "s_waitcnt vmcnt(0)\n\t" \
    "v_cmp_eq_u32 vcc, -1, v184\n\t" \
    "s_cbranch_vccz rr_Ook79_%=\n\t" \
    "s_cmp_eq_u32 s68, 0\n\t" \
    "s_cbranch_scc1 rr_F82_%=\n\t" \
    "s_sub_u32 s68, s68, 1\n\t" \
    "s_sleep 3\n\t" \
    "s_branch rr_O78_%=\n\t" \
    "rr_F82_%=:\n\t" \
    "v_mov_b32 v185, 6095104\n\t" \
    "v_mov_b32 v186, 18\n\t" \
    "global_store_dword v185, v186, s[36:37] sc1\n\t" \
    "s_waitcnt vmcnt(0)\n\t" \
    "s_branch rr_Ook79_%=\n\t" \
    "rr_Ook79_%=:\n\t" \
    "v_subrev_u32 v184, s80, v184\n\t" \
    "s_nop 1\n\t" \
    "v_max_i32_dpp v184, v184, v184 row_ror:1 row_mask:0xf bank_mask:0xf\n\t" \
    "s_nop 1\n\t" \
    "v_max_i32_dpp v184, v184, v184 row_ror:2 row_mask:0xf bank_mask:0xf\n\t" \
    "s_nop 1\n\t" \
    "v_max_i32_dpp v184, v184, v184 row_ror:4 row_mask:0xf bank_mask:0xf\n\t" \
    "s_nop 1\n\t" \
    "v_max_i32_dpp v184, v184, v184 row_ror:8 row_mask:0xf bank_mask:0xf\n\t" \
    "s_nop 0\n\t" \
    "v_readlane_b32 s69, v184, 0\n\t" \
    "v_readlane_b32 s71, v184, 16\n\t" \
    "v_readlane_b32 s70, v184, 32\n\t" \
    "v_readlane_b32 s81, v184, 48\n\t" \
    "s_max_i32 s69, s69, s71\n\t" \
    "s_nop 0\n\t" \
    "s_max_i32 s71, s70, s81\n\t" \
    "s_max_i32 s69, s69, s71\n\t" \
    "s_add_u32 s81, s69, s80\n\t" \
    "rr_Oc80_%=:\n\t" \
    "s_mul_hi_u32 s69, s85, s78\n\t" \
    "s_add_u32 s79, s81, s69\n\t" \
    "s_add_u32 s79, s79, s87\n\t" \
    "s_cmp_ge_u32 s65, 37\n\t" \
    "s_cselect_b32 s69, 28, 0\n\t" \
    "s_sub_u32 s79, s79, s69\n\t" \
    "s_cmp_lt_u32 s65, 9\n\t" \
    "s_cselect_b32 s69, 96, 0\n\t" \
    "s_add_u32 s79, s79, s69\n\t" \
    "s_cmp_ge_u32 s65, 58\n\t" \
    "s_cselect_b32 s69, 32, 0\n\t" \
    "s_add_u32 s79, s79, s69\n\t" \
    "s_add_u32 s80, s79, s88\n\t" \
    "s_sub_u32 s70, s79, s74\n\t" \
    "s_max_i32 s70, s70, 0\n\t" \
    "s_min_i32 s70, s70, 16000\n\t" \
    "s_lshl_b32 s70, s70, 1\n\t" \
    "s_mul_hi_u32 s70, s70, s77\n\t" \
    "s_add_u32 s70, s70, s73\n\t" \
    "rr_W83_%=:\n\t" \
    "s_memtime s[56:57]\n\t" \
    "s_waitcnt lgkmcnt(0)\n\t" \
    "s_sub_u32 s69, s70, s56\n\t" \
    "s_cmp_gt_i32 s69, 24\n\t" \
    "s_cbranch_scc1 rr_W83_%=\n\t" \
    "rr_act7_%=:\n\t" \
    "s_add_u32 s69, s65, 0\n\t" \
    "s_bitcmp1_b32 s69, 0\n\t" \
    "s_cselect_b32 s70, s63, s62\n\t" \
    "s_bfe_u32 s69, s69, 0x20001\n\t" \
    "s_lshl_b32 s69, s69, 19\n\t" \
    "s_add_u32 s69, s69, s70\n\t" \
    "s_add_u32 s46, s36, s69\n\t" \
    "s_addc_u32 s47, s37, 0\n\t" \
    "s_add_u32 s69, s65, 0\n\t" \
    "s_lshr_b32 s69, s69, 1\n\t" \
    "s_xor_b32 s69, s69, s33\n\t" \
    "s_and_b32 s69, s69, 1\n\t" \
    "s_lshl_b32 s69, s69, 14\n\t" \
    "s_or_b32 s46, s46, s69\n\t" \
    "s_add_u32 s69, s65, 1\n\t" \
    "s_bitcmp1_b32 s69, 0\n\t" \
    "s_cselect_b32 s70, s63, s62\n\t" \
    "s_bfe_u32 s69, s69, 0x20001\n\t" \
    "s_lshl_b32 s69, s69, 19\n\t" \
    "s_add_u32 s69, s69, s70\n\t" \
    "s_add_u32 s48, s36, s69\n\t" \
    "s_addc_u32 s49, s37, 0\n\t" \
    "s_add_u32 s69, s65, 0\n\t" \
    "s_mul_i32 s70, s69, 171\n\t" \
    "s_lshr_b32 s70, s70, 9\n\t" \
    "s_mul_i32 s70, s70, 3\n\t" \
    "s_sub_u32 s69, s69, s70\n\t" \
    "s_mul_i32 s69, s69, 36\n\t" \
    "s_add_u32 s69, s69, s67\n\t" \
    "s_lshl_b32 s69, s69, 14\n\t" \
    "s_add_u32 s69, s69, s64\n\t" \
    "s_add_u32 s69, s69, 4194304\n\t" \
    "s_add_u32 s40, s36, s69\n\t" \
    "s_addc_u32 s41, s37, 0\n\t" \
    "s_add_u32 s69, s65, 2\n\t" \
    "s_cmp_ge_i32 s83, s69\n\t" \
    "s_cbranch_scc1 rr_nf84_%=\n\t" \
    "global_load_dword v177, v6, s[36:37] sc1\n\t" \
    "rr_nf84_%=:\n\t" \
    "s_mov_b32 s71, 3\n\t" \
    "rr_E85_%=:\n\t" \
    "s_memtime s[54:55]\n\t" \
    "s_nop 0\n\t" \
    "s_memrealtime s[4:5]\n\t" \
    "s_nop 0\n\t" \
    "s_memrealtime s[6:7]\n\t" \
    "s_nop 0\n\t" \
    "s_memrealtime s[8:9]\n\t" \
    "s_nop 0\n\t" \
    "s_memrealtime s[10:11]\n\t" \
    "s_nop 0\n\t" \
    "s_memrealtime s[12:13]\n\t" \
    "s_nop 0\n\t" \
    "s_memrealtime s[14:15]\n\t" \
    "s_nop 0\n\t" \
    "s_memrealtime s[16:17]\n\t" \
    "s_nop 0\n\t" \
    "s_memrealtime s[18:19]\n\t" \
    "s_nop 0\n\t" \
    "s_memrealtime s[20:21]\n\t" \
    "s_nop 0\n\t" \
    "s_memrealtime s[22:23]\n\t" \
    "s_nop 0\n\t" \
    "s_memrealtime s[24:25]\n\t" \
    "s_nop 0\n\t" \
    "s_memrealtime s[26:27]\n\t" \
    "s_nop 0\n\t" \
    "s_memrealtime s[28:29]\n\t" \
    "s_nop 0\n\t" \
    "s_memtime s[56:57]\n\t" \
    "s_mov_b32 s70, 1\n\t" \
    "s_sub_u32 s71, s71, 1\n\t" \
    "s_cselect_b32 s69, 0, -1\n\t" \
    "s_waitcnt lgkmcnt(0)\n\t" \
    "s_sub_u32 s54, s56, s54\n\t" \
    "s_and_b32 s54, s54, s69\n\t" \
    "s_cmp_gt_u32 s54, 124\n\t" \
    "s_cbranch_scc1 rr_E85_%=\n\t" \
    "s_cmp_eq_u32 s4, s28\n\t" \
    "s_addc_u32 s70, s70, 0\n\t" \
    "s_cmp_eq_u32 s6, s28\n\t" \
    "s_addc_u32 s70, s70, 0\n\t" \
    "s_cmp_eq_u32 s8, s28\n\t" \
    "s_addc_u32 s70, s70, 0\n\t" \
    "s_cmp_eq_u32 s10, s28\n\t" \
    "s_addc_u32 s70, s70, 0\n\t" \
    "s_cmp_eq_u32 s12, s28\n\t" \
    "s_addc_u32 s70, s70, 0\n\t" \
    "s_cmp_eq_u32 s14, s28\n\t" \
    "s_addc_u32 s70, s70, 0\n\t" \
    "s_cmp_eq_u32 s16, s28\n\t" \
    "s_addc_u32 s70, s70, 0\n\t" \
    "s_cmp_eq_u32 s18, s28\n\t" \
    "s_addc_u32 s70, s70, 0\n\t" \
    "s_cmp_eq_u32 s20, s28\n\t" \
    "s_addc_u32 s70, s70, 0\n\t" \
    "s_cmp_eq_u32 s22, s28\n\t" \
    "s_addc_u32 s70, s70, 0\n\t" \
    "s_cmp_eq_u32 s24, s28\n\t" \
    "s_addc_u32 s70, s70, 0\n\t" \
    "s_cmp_eq_u32 s26, s28\n\t" \
    "s_addc_u32 s70, s70, 0\n\t" \
    "s_mov_b32 s75, s73\n\t" \
    "s_mov_b32 s76, s74\n\t" \
    "s_lshl_b32 s70, s70, 3\n\t" \
    "s_sub_u32 s73, s56, s70\n\t" \
    "s_lshl_b32 s74, s28, 4\n\t" \
    "s_sub_u32 s69, s73, s75\n\t" \
    "s_sub_u32 s70, s74, s76\n\t" \
    "s_sub_u32 s71, s70, 1600\n\t" \
    "s_cmp_lt_u32 s71, 400000\n\t" \
    "s_cbranch_scc0 rr_K87_%=\n\t" \
    "v_cvt_f32_u32 v184, s69\n\t" \
    "v_cvt_f32_u32 v185, s70\n\t" \
    "v_rcp_f32 v186, v184\n\t" \
    "v_rcp_f32 v187, v185\n\t" \
    "v_mul_f32 v186, v186, v185\n\t" \
    "v_mul_f32 v187, v187, v184\n\t" \
    "v_mul_f32 v186, 0x4f800000, v186\n\t" \
    "v_mul_f32 v187, 0x4f000000, v187\n\t" \
    "v_cvt_u32_f32 v186, v186\n\t" \
    "v_cvt_u32_f32 v187, v187\n\t" \
    "v_readfirstlane_b32 s69, v186\n\t" \
    "v_readfirstlane_b32 s70, v187\n\t" \
    "s_nop 1\n\t" \
    "s_sub_u32 s69, s69, s78\n\t" \
    "s_ashr_i32 s69, s69, 2\n\t" \
    "s_add_u32 s78, s78, s69\n\t" \
    "s_sub_u32 s70, s70, s77\n\t" \
    "s_ashr_i32 s70, s70, 2\n\t" \
    "s_add_u32 s77, s77, s70\n\t" \
    "rr_K87_%=:\n\t" \
    "s_mov_b32 s91, 0\n\t" \
    "v_mov_b32 v160, 0\n\t" \
    "v_mov_b32 v161, 0\n\t" \
    "v_mov_b32 v162, 0\n\t" \
    "v_mov_b32 v163, 0\n\t" \
    "s_sub_u32 s70, s80, s74\n\t" \
    "s_max_i32 s70, s70, 0\n\t" \
    "s_min_i32 s70, s70, 16000\n\t" \
    "s_lshl_b32 s70, s70, 1\n\t" \
    "s_mul_hi_u32 s70, s70, s77\n\t" \
    "s_add_u32 s70, s70, s73\n\t" \
    "s_sub_u32 s70, s70, 100\n\t" \
    "s_getpc_b64 s[30:31]\n\t" \
    "rr_Wp94_%=:\n\t" \
    "s_add_u32 s30, s30, rr_We95_%=-rr_Wp94_%=\n\t" \
    "s_addc_u32 s31, s31, 0\n\t" \
    "rr_W93_%=:\n\t" \
    "s_memtime s[56:57]\n\t" \
    "s_waitcnt lgkmcnt(0)\n\t" \
    "s_sub_u32 s69, s70, s56\n\t" \
    "s_cmp_gt_i32 s69, 111\n\t" \
    "s_cbranch_scc1 rr_W93_%=\n\t" \
    "s_max_i32 s69, s69, 0\n\t" \
    "s_lshr_b32 s69, s69, 1\n\t" \
    "s_and_b32 s69, s69, 0x3c\n\t" \
    "s_sub_u32 s30, s30, s69\n\t" \
    "s_subb_u32 s31, s31, 0\n\t" \
    "s_setpc_b64 s[30:31]\n\t" \
    ".p2align 6\n\t" \
    "rr_al96_%=:\n\t" \
    "s_nop 0\n\t" \
    "s_nop 0\n\t" \
    "s_nop 1\n\t" \
    "s_nop 1\n\t" \
    "s_nop 1\n\t" \
    "s_nop 1\n\t" \
    "s_nop 1\n\t" \
    "s_nop 1\n\t" \
    "s_nop 1\n\t" \
    "s_nop 1\n\t" \
    "s_nop 1\n\t" \
    "s_nop 1\n\t" \
    "s_nop 1\n\t" \
    "s_nop 1\n\t" \
    "s_nop 1\n\t" \
    "rr_We95_%=:\n\t" \
    "global_load_dword v184, v7, s[46:47] sc1\n\t" \
    "s_sub_u32 s97, s74, s86\n\t" \
    "s_sub_u32 s71, s73, 190\n\t" \
    "s_getpc_b64 s[30:31]\n\t" \
    "rr_wp97_%=:\n\t" \
    "s_add_u32 s30, s30, rr_again9_%=-rr_wp97_%=\n\t" \
    "s_addc_u32 s31, s31, (rr_again9_%=-rr_wp97_%=)>>32\n\t" \
    "s_waitcnt vmcnt(0) lgkmcnt(0)\n\t" \
    "rr_hok88_%=:\n\t" \
    "s_memtime s[56:57]\n\t" \
    "v_cmp_eq_u32 vcc, -1, v184\n\t" \
    "v_subrev_u32 v185, s97, v184\n\t" \
    "s_nop 1\n\t" \
    "v_max_i32_dpp v185, v185, v185 row_ror:1 row_mask:0xf bank_mask:0xf\n\t" \
    "s_cbranch_vccnz rr_hlate89_%=\n\t" \
    "s_nop 0\n\t" \
    "v_max_i32_dpp v185, v185, v185 row_ror:2 row_mask:0xf bank_mask:0xf\n\t" \
    "s_nop 1\n\t" \
    "v_max_i32_dpp v185, v185, v185 row_ror:4 row_mask:0xf bank_mask:0xf\n\t" \
    "s_nop 1\n\t" \
    "v_max_i32_dpp v185, v185, v185 row_ror:8 row_mask:0xf bank_mask:0xf\n\t" \
    "s_nop 0\n\t" \
    "v_readlane_b32 s69, v185, 0\n\t" \
    "v_readlane_b32 s70, v185, 16\n\t" \
    "s_waitcnt lgkmcnt(0)\n\t" \
    "s_sub_u32 s71, s71, s56\n\t" \
    "s_max_i32 s81, s69, s70\n\t" \
    "s_max_i32 s69, s81, 0\n\t" \
    "s_lshl_b32 s69, s69, 1\n\t" \
    "s_mul_hi_u32 s69, s69, s77\n\t" \
    "s_add_u32 s69, s69, s71\n\t" \
    "s_cmp_gt_i32 s69, 111\n\t" \
    "s_cbranch_scc1 rr_wl90_%=\n\t" \
    "rr_wf92_%=:\n\t" \
    "s_max_i32 s69, s69, 0\n\t" \
    "s_lshr_b32 s69, s69, 1\n\t" \
    "s_and_b32 s69, s69, 0x3c\n\t" \
    "s_sub_u32 s30, s30, s69\n\t" \
    "s_subb_u32 s31, s31, 0\n\t" \
    "s_setpc_b64 s[30:31]\n\t" \
    "rr_wl90_%=:\n\t" \
    "s_min_i32 s69, s69, 24000\n\t" \
    "s_add_u32 s70, s69, s56\n\t" \
    "s_add_u32 s70, s70, 62\n\t" \
    "rr_wp91_%=:\n\t" \
    "s_memtime s[56:57]\n\t" \
    "s_waitcnt lgkmcnt(0)\n\t" \
    "s_sub_u32 s69, s70, s56\n\t" \
    "s_cmp_gt_i32 s69, 111\n\t" \
    "s_cbranch_scc1 rr_wp91_%=\n\t" \
    "s_branch rr_wf92_%=\n\t" \
    ".p2align 6\n\t" \
    "rr_al98_%=:\n\t" \
    "s_nop 0\n\t" \
    "s_nop 0\n\t" \
    "s_nop 1\n\t" \
    "s_nop 1\n\t" \
    "s_nop 1\n\t" \
    "s_nop 1\n\t" \
    "s_nop 1\n\t" \
    "s_nop 1\n\t" \
    "s_nop 1\n\t" \
    "s_nop 1\n\t" \
    "s_nop 1\n\t" \
    "s_nop 1\n\t" \
    "s_nop 1\n\t" \
    "s_nop 1\n\t" \
    "s_nop 1\n\t" \
    "rr_again9_%=:\n\t" \
    "global_load_dwordx4 v[48:51], v10, s[46:47] sc1\n\t" \
    "global_load_dwordx4 v[52:55], v11, s[46:47] sc1\n\t" \
    "global_load_dwordx4 v[56:59], v12, s[46:47] sc1\n\t" \
    "global_load_dwordx4 v[60:63], v13, s[46:47] sc1\n\t" \
    "global_load_dwordx4 v[64:67], v14, s[46:47] sc1\n\t" \
    "global_load_dwordx4 v[68:71], v15, s[46:47] sc1\n\t" \
    "global_load_dwordx4 v[72:75], v16, s[46:47] sc1\n\t" \
    "global_load_dwordx4 v[76:79], v17, s[46:47] sc1\n\t" \
    "global_load_dwordx4 v[80:83], v18, s[46:47] sc1\n\t" \
    "global_load_dwordx4 v[84:87], v19, s[46:47] sc1\n\t" \
    "global_load_dwordx4 v[88:91], v20, s[46:47] sc1\n\t" \
    "global_load_dwordx4 v[92:95], v21, s[46:47] sc1\n\t" \
    "global_load_dwordx4 v[96:99], v22, s[46:47] sc1\n\t" \
    "global_load_dwordx4 v[100:103], v23, s[46:47] sc1\n\t" \
    "global_load_dwordx4 v[104:107], v24, s[46:47] sc1\n\t" \
    "global_load_dwordx4 v[108:111], v25, s[46:47] sc1\n\t" \
    "global_load_dwordx4 v[112:115], v42, s[46:47] sc1\n\t" \
    "global_load_dwordx4 v[116:119], v43, s[46:47] sc1\n\t" \
    "global_load_dwordx4 v[120:123], v44, s[46:47] sc1\n\t" \
    "global_load_dwordx4 v[124:127], v45, s[46:47] sc1\n\t" \
    "global_load_dwordx4 v[128:131], v0, s[40:41] offset:0 sc1\n\t" \
    "global_load_dwordx4 v[132:135], v0, s[40:41] offset:1024 sc1\n\t" \
    "global_load_dwordx4 v[136:139], v0, s[40:41] offset:2048 sc1\n\t" \
    "global_load_dwordx4 v[140:143], v0, s[40:41] offset:3072 sc1\n\t" \
    "global_load_dwordx4 v[144:147], v1, s[40:41] offset:0 sc1\n\t" \
    "global_load_dwordx4 v[148:151], v1, s[40:41] offset:1024 sc1\n\t" \
    "global_load_dwordx4 v[152:155], v1, s[40:41] offset:2048 sc1\n\t" \
    "global_load_dwordx4 v[156:159], v1, s[40:41] offset:3072 sc1\n\t" \
    "s_cmp_lg_u32 s91, 0\n\t" \
    "s_cbranch_scc1 rr_nob12_%=\n\t" \
    "s_add_u32 s81, s81, s97\n\t" \
    "v_mov_b32 v184, s81\n\t" \
    "v_mov_b32 v185, s65\n\t" \
    "ds_write_b64 v9, v[184:185]\n\t" \
    "rr_nob12_%=:\n\t" \
    "s_waitcnt vmcnt(27)\n\t" \
    "v_smfmac_f32_16x16x64_bf16 v[160:163], v[48:51], a[0:7], v4\n\t" \
    "s_waitcnt vmcnt(26)\n\t" \
    "v_smfmac_f32_16x16x64_bf16 v[160:163], v[52:55], a[8:15], v4\n\t" \
    "s_waitcnt vmcnt(25)\n\t" \
    "v_smfmac_f32_16x16x64_bf16 v[160:163], v[56:59], a[16:23], v4\n\t" \
    "s_waitcnt vmcnt(24)\n\t" \
    "v_smfmac_f32_16x16x64_bf16 v[160:163], v[60:63], a[24:31], v4\n\t" \
    "s_waitcnt vmcnt(23)\n\t" \
    "v_smfmac_f32_16x16x64_bf16 v[160:163], v[64:67], a[32:39], v4\n\t" \
    "s_waitcnt vmcnt(22)\n\t" \
    "v_smfmac_f32_16x16x64_bf16 v[160:163], v[68:71], a[40:47], v4\n\t" \
    "s_waitcnt vmcnt(21)\n\t" \
    "v_smfmac_f32_16x16x64_bf16 v[160:163], v[72:75], a[48:55], v4\n\t" \
    "s_waitcnt vmcnt(20)\n\t" \
    "v_smfmac_f32_16x16x64_bf16 v[160:163], v[76:79], a[56:63], v4\n\t" \
    "s_waitcnt vmcnt(19)\n\t" \
    "v_smfmac_f32_16x16x64_bf16 v[160:163], v[80:83], a[64:71], v4\n\t" \
    "s_waitcnt vmcnt(18)\n\t" \
    "v_smfmac_f32_16x16x64_bf16 v[160:163], v[84:87], a[72:79], v4\n\t" \
    "s_waitcnt vmcnt(17)\n\t" \
    "v_smfmac_f32_16x16x64_bf16 v[160:163], v[88:91], a[80:87], v4\n\t" \
    "s_waitcnt vmcnt(16)\n\t" \
    "v_smfmac_f32_16x16x64_bf16 v[160:163], v[92:95], a[88:95], v4\n\t" \
    "s_waitcnt vmcnt(15)\n\t" \
    "v_smfmac_f32_16x16x64_bf16 v[160:163], v[96:99], a[96:103], v4\n\t" \
    "s_waitcnt vmcnt(14)\n\t" \
    "v_smfmac_f32_16x16x64_bf16 v[160:163], v[100:103], a[104:111], v4\n\t" \
    "s_waitcnt vmcnt(13)\n\t" \
    "v_smfmac_f32_16x16x64_bf16 v[160:163], v[104:107], a[112:119], v4\n\t" \
    "s_waitcnt vmcnt(12)\n\t" \
    "v_smfmac_f32_16x16x64_bf16 v[160:163], v[108:111], a[120:127], v4\n\t" \
    "s_waitcnt vmcnt(6)\n\t" \
    "v_smfmac_f32_16x16x64_bf16 v[160:163], v[112:115], v[128:135], v4\n\t" \
    "s_waitcnt vmcnt(4)\n\t" \
    "v_smfmac_f32_16x16x64_bf16 v[160:163], v[116:119], v[136:143], v4\n\t" \
    "s_waitcnt vmcnt(2)\n\t" \
    "v_smfmac_f32_16x16x64_bf16 v[160:163], v[120:123], v[144:151], v4\n\t" \
    "s_waitcnt vmcnt(0)\n\t" \
    "v_smfmac_f32_16x16x64_bf16 v[160:163], v[124:127], v[152:159], v4\n\t" \
    "s_nop 7\n\t" \
    "v_pk_add_f32 v[160:161], v[160:161], v[162:163]\n\t" \
    "v_cvt_pk_bf16_f32 v184, v160, v161\n\t" \
    "v_cmp_u_f32 vcc, v160, v161\n\t" \
    "s_cbranch_vccnz rr_bad10_%=\n\t" \
    "rr_cont11_%=:\n\t" \
    "v_mov_b32_dpp v185, v184 quad_perm:[1,0,3,2] row_mask:0xf bank_mask:0xf\n\t" \
    "v_perm_b32 v184, v185, v184, v5\n\t" \
    "global_store_dword v2, v184, s[48:49] sc1\n\t" \
    "global_store_dword v181, v184, s[48:49] sc1\n\t" \
    "global_store_dword v182, v184, s[48:49] sc1\n\t" \
    "global_store_dword v183, v184, s[48:49] sc1\n\t" \
    "s_cmp_eq_u32 s91, 3\n\t" \
    "s_cbranch_scc1 rr_dry28_%=\n\t" \
    "s_cmp_eq_u32 s65, 62\n\t" \
    "s_cbranch_scc1 rr_end8_%=\n\t" \
    "s_add_u32 s65, s65, 2\n\t" \
    "s_branch rr_pf13_%=\n\t" \
    "rr_bad10_%=:\n\t" \
    "s_waitcnt lgkmcnt(0)\n\t" \
    "v_max_u32 v185, v48, v49\n\t" \
    "v_max3_u32 v185, v185, v50, v51\n\t" \
    "v_max3_u32 v185, v185, v52, v53\n\t" \
    "v_max3_u32 v185, v185, v54, v55\n\t" \
    "v_max3_u32 v185, v185, v56, v57\n\t" \
    "v_max3_u32 v185, v185, v58, v59\n\t" \
    "v_max3_u32 v185, v185, v60, v61\n\t" \
    "v_max3_u32 v185, v185, v62, v63\n\t" \
    "v_max3_u32 v185, v185, v64, v65\n\t" \
    "v_max3_u32 v185, v185, v66, v67\n\t" \
    "v_max3_u32 v185, v185, v68, v69\n\t" \
    "v_max3_u32 v185, v185, v70, v71\n\t" \
    "v_max3_u32 v185, v185, v72, v73\n\t" \
    "v_max3_u32 v185, v185, v74, v75\n\t" \
    "v_max3_u32 v185, v185, v76, v77\n\t" \
    "v_max3_u32 v185, v185, v78, v79\n\t" \
    "v_max3_u32 v185, v185, v80, v81\n\t" \
    "v_max3_u32 v185, v185, v82, v83\n\t" \
    "v_max3_u32 v185, v185, v84, v85\n\t" \
    "v_max3_u32 v185, v185, v86, v87\n\t" \
    "v_max3_u32 v185, v185, v88, v89\n\t" \
    "v_max3_u32 v185, v185, v90, v91\n\t" \
    "v_max3_u32 v185, v185, v92, v93\n\t" \
    "v_max3_u32 v185, v185, v94, v95\n\t" \
    "v_max3_u32 v185, v185, v96, v97\n\t" \
    "v_max3_u32 v185, v185, v98, v99\n\t" \
    "v_max3_u32 v185, v185, v100, v101\n\t" \
    "v_max3_u32 v185, v185, v102, v103\n\t" \
    "v_max3_u32 v185, v185, v104, v105\n\t" \
    "v_max3_u32 v185, v185, v106, v107\n\t" \
    "v_max3_u32 v185, v185, v108, v109\n\t" \
    "v_max3_u32 v185, v185, v110, v111\n\t" \
    "v_max3_u32 v185, v185, v112, v113\n\t" \
    "v_max3_u32 v185, v185, v114, v115\n\t" \
    "v_max3_u32 v185, v185, v116, v117\n\t" \
    "v_max3_u32 v185, v185, v118, v119\n\t" \
    "v_max3_u32 v185, v185, v120, v121\n\t" \
    "v_max3_u32 v185, v185, v122, v123\n\t" \
    "v_max3_u32 v185, v185, v124, v125\n\t" \
    "v_max3_u32 v185, v185, v126, v127\n\t" \
    "v_max_u32 v186, v128, v129\n\t" \
    "v_max3_u32 v186, v186, v130, v131\n\t" \
    "v_max3_u32 v186, v186, v132, v133\n\t" \
    "v_max3_u32 v186, v186, v134, v135\n\t" \
    "v_max3_u32 v186, v186, v136, v137\n\t" \
    "v_max3_u32 v186, v186, v138, v139\n\t" \
    "v_max3_u32 v186, v186, v140, v141\n\t" \
    "v_max3_u32 v186, v186, v142, v143\n\t" \
    "v_max3_u32 v186, v186, v144, v145\n\t" \
    "v_max3_u32 v186, v186, v146, v147\n\t" \
    "v_max3_u32 v186, v186, v148, v149\n\t" \
    "v_max3_u32 v186, v186, v150, v151\n\t" \
    "v_max3_u32 v186, v186, v152, v153\n\t" \
    "v_max3_u32 v186, v186, v154, v155\n\t" \
    "v_max3_u32 v186, v186, v156, v157\n\t" \
    "v_max3_u32 v186, v186, v158, v159\n\t" \
    "v_max_u32 v185, v185, v186\n\t" \
    "v_cmp_eq_u32 vcc, -1, v185\n\t" \
    "s_cbranch_vccz rr_cont11_%=\n\t" \
    "s_mov_b32 s91, 1\n\t" \
    "v_mov_b32 v160, 0\n\t" \
    "v_mov_b32 v161, 0\n\t" \
    "v_mov_b32 v162, 0\n\t" \
    "v_mov_b32 v163, 0\n\t" \
    "s_cmp_eq_u32 s68, 0\n\t" \
    "s_cbranch_scc1 rr_F99_%=\n\t" \
    "s_sub_u32 s68, s68, 1\n\t" \
    "s_branch rr_again9_%=\n\t" \
    "rr_F99_%=:\n\t" \
    "v_mov_b32 v186, 6095104\n\t" \
    "v_mov_b32 v187, 3\n\t" \
    "global_store_dword v186, v187, s[36:37] sc1\n\t" \
    "s_waitcnt vmcnt(0)\n\t" \
    "s_branch rr_cont11_%=\n\t" \
    "rr_hlate89_%=:\n\t" \
    "s_waitcnt lgkmcnt(0)\n\t" \
    "v_add_u32 v189, 1024, v7\n\t" \
    "global_load_dword v186, v189, s[46:47] sc1\n\t" \
    "s_sleep 4\n\t" \
    "v_add_u32 v189, 2048, v7\n\t" \
    "global_load_dword v187, v189, s[46:47] sc1\n\t" \
    "s_waitcnt vmcnt(1)\n\t" \
    "v_cmp_eq_u32 vcc, -1, v186\n\t" \
    "s_cbranch_vccz rr_hg100_%=\n\t" \
    "v_add_u32 v189, 3072, v7\n\t" \
    "global_load_dword v186, v189, s[46:47] sc1\n\t" \
    "s_waitcnt vmcnt(1)\n\t" \
    "v_cmp_eq_u32 vcc, -1, v187\n\t" \
    "s_cbranch_vccz rr_hg101_%=\n\t" \
    "s_waitcnt vmcnt(0)\n\t" \
    "v_cmp_eq_u32 vcc, -1, v186\n\t" \
    "s_cbranch_vccz rr_hg100_%=\n\t" \
    "rr_hp102_%=:\n\t" \
    "global_load_dword v184, v189, s[46:47] sc1\n\t" \
    "s_waitcnt vmcnt(0)\n\t" \
    "v_cmp_eq_u32 vcc, -1, v184\n\t" \
    "s_cbranch_vccz rr_hpd103_%=\n\t" \
    "s_cmp_eq_u32 s68, 0\n\t" \
    "s_cbranch_scc1 rr_F104_%=\n\t" \
    "s_sub_u32 s68, s68, 1\n\t" \
    "s_sleep 1\n\t" \
    "s_branch rr_hp102_%=\n\t" \
    "rr_F104_%=:\n\t" \
    "v_mov_b32 v186, 6095104\n\t" \
    "v_mov_b32 v187, 7\n\t" \
    "global_store_dword v186, v187, s[36:37] sc1\n\t" \
    "s_waitcnt vmcnt(0)\n\t" \
    "s_branch rr_hpd103_%=\n\t" \
    "rr_hpd103_%=:\n\t" \
    "rr_h025_%=:\n\t" \
    "v_and_b32 v184, -2, v184\n\t" \
    "s_mov_b32 s71, 3\n\t" \
    "rr_E105_%=:\n\t" \
    "s_memtime s[54:55]\n\t" \
    "s_nop 0\n\t" \
    "s_memrealtime s[4:5]\n\t" \
    "s_nop 0\n\t" \
    "s_memrealtime s[6:7]\n\t" \
    "s_nop 0\n\t" \
    "s_memrealtime s[8:9]\n\t" \
    "s_nop 0\n\t" \
    "s_memrealtime s[10:11]\n\t" \
    "s_nop 0\n\t" \
    "s_memrealtime s[12:13]\n\t" \
    "s_nop 0\n\t" \
    "s_memrealtime s[14:15]\n\t" \
    "s_nop 0\n\t" \
    "s_memrealtime s[16:17]\n\t" \
    "s_nop 0\n\t" \
    "s_memrealtime s[18:19]\n\t" \
    "s_nop 0\n\t" \
    "s_memrealtime s[20:21]\n\t" \
    "s_nop 0\n\t" \
    "s_memrealtime s[22:23]\n\t" \
    "s_nop 0\n\t" \
    "s_memrealtime s[24:25]\n\t" \
    "s_nop 0\n\t" \
    "s_memrealtime s[26:27]\n\t" \
    "s_nop 0\n\t" \
    "s_memrealtime s[28:29]\n\t" \
    "s_nop 0\n\t" \
    "s_memtime s[56:57]\n\t" \
    "s_mov_b32 s70, 1\n\t" \
    "s_sub_u32 s71, s71, 1\n\t" \
    "s_cselect_b32 s69, 0, -1\n\t" \
    "s_waitcnt lgkmcnt(0)\n\t" \
    "s_sub_u32 s54, s56, s54\n\t" \
    "s_and_b32 s54, s54, s69\n\t" \
    "s_cmp_gt_u32 s54, 124\n\t" \
    "s_cbranch_scc1 rr_E105_%=\n\t" \
    "s_cmp_eq_u32 s4, s28\n\t" \
    "s_addc_u32 s70, s70, 0\n\t" \
    "s_cmp_eq_u32 s6, s28\n\t" \
    "s_addc_u32 s70, s70, 0\n\t" \
    "s_cmp_eq_u32 s8, s28\n\t" \
    "s_addc_u32 s70, s70, 0\n\t" \
    "s_cmp_eq_u32 s10, s28\n\t" \
    "s_addc_u32 s70, s70, 0\n\t" \
    "s_cmp_eq_u32 s12, s28\n\t" \
    "s_addc_u32 s70, s70, 0\n\t" \
    "s_cmp_eq_u32 s14, s28\n\t" \
    "s_addc_u32 s70, s70, 0\n\t" \
    "s_cmp_eq_u32 s16, s28\n\t" \
    "s_addc_u32 s70, s70, 0\n\t" \
    "s_cmp_eq_u32 s18, s28\n\t" \
    "s_addc_u32 s70, s70, 0\n\t" \
    "s_cmp_eq_u32 s20, s28\n\t" \
    "s_addc_u32 s70, s70, 0\n\t" \
    "s_cmp_eq_u32 s22, s28\n\t" \
    "s_addc_u32 s70, s70, 0\n\t" \
    "s_cmp_eq_u32 s24, s28\n\t" \
    "s_addc_u32 s70, s70, 0\n\t" \
    "s_cmp_eq_u32 s26, s28\n\t" \
    "s_addc_u32 s70, s70, 0\n\t" \
    "s_mov_b32 s75, s73\n\t" \
    "s_mov_b32 s76, s74\n\t" \
    "s_lshl_b32 s70, s70, 3\n\t" \
    "s_sub_u32 s73, s56, s70\n\t" \
    "s_lshl_b32 s74, s28, 4\n\t" \
    "rr_h126_%=:\n\t" \
    "s_sub_u32 s97, s74, s86\n\t" \
    "s_sub_u32 s71, s73, 190\n\t" \
    "s_getpc_b64 s[30:31]\n\t" \
    "rr_wp107_%=:\n\t" \
    "s_add_u32 s30, s30, rr_again9_%=-rr_wp107_%=\n\t" \
    "s_addc_u32 s31, s31, (rr_again9_%=-rr_wp107_%=)>>32\n\t" \
    "s_branch rr_hok88_%=\n\t" \
    "rr_hg100_%=:\n\t" \
    "v_mov_b32 v184, v186\n\t" \
    "s_branch rr_hok88_%=\n\t" \
    "rr_hg101_%=:\n\t" \
    "v_mov_b32 v184, v187\n\t" \
    "s_branch rr_hok88_%=\n\t" \
    "rr_end8_%=:\n\t" \
    "s_waitcnt vmcnt(0) lgkmcnt(0)\n\t" \
    "s_endpgm\n\t" \
    "rr_last_%=:\n\t" \
    ""
#define ASM_O \
    "s_mov_b64 s[36:37], %0\n\t" \
    "s_mov_b64 s[42:43], %1\n\t" \
    "s_mov_b64 s[44:45], %2\n\t" \
    "s_mov_b32 s66, %3\n\t" \
    "s_mov_b32 s67, %4\n\t" \
    "s_mov_b64 s[50:51], %5\n\t" \
    "s_mov_b32 s82, %7\n\t" \
    "v_mbcnt_lo_u32_b32 v223, -1, 0\n\t" \
    "v_mbcnt_hi_u32_b32 v223, -1, v223\n\t" \
    "s_getpc_b64 s[46:47]\n\t" \
    "oo_pc_%=:\n\t" \
    "s_lshl_b32 s69, s66, 13\n\t" \
    "v_lshl_add_u32 v222, v223, 7, s69\n\t" \
    "v_min_u32 v222, oo_last_%=-oo_pc_%=-4, v222\n\t" \
    "global_load_dword v222, v222, s[46:47]\n\t" \
    "v_lshlrev_b32 v223, 2, v223\n\t" \
    "v_add_u32 v223, s82, v223\n\t" \
    "ds_read_b32 v0, v223 offset:0\n\t" \
    "ds_read_b32 v1, v223 offset:256\n\t" \
    "ds_read_b32 v2, v223 offset:512\n\t" \
    "ds_read_b32 v3, v223 offset:768\n\t" \
    "ds_read_b32 v4, v223 offset:1024\n\t" \
    "ds_read_b32 v5, v223 offset:1280\n\t" \
    "ds_read_b32 v6, v223 offset:1536\n\t" \
    "ds_read_b32 v7, v223 offset:1792\n\t" \
    "ds_read_b32 v8, v223 offset:2048\n\t" \
    "ds_read_b32 v9, v223 offset:2304\n\t" \
    "ds_read_b32 v10, v223 offset:2560\n\t" \
    "ds_read_b32 v11, v223 offset:2816\n\t" \
    "s_waitcnt lgkmcnt(0)\n\t" \
    "ds_read_b32 v12, v223 offset:3072\n\t" \
    "ds_read_b32 v13, v223 offset:3328\n\t" \
    "ds_read_b32 v14, v223 offset:3584\n\t" \
    "ds_read_b32 v15, v223 offset:3840\n\t" \
    "ds_read_b32 v16, v223 offset:4096\n\t" \
    "ds_read_b32 v17, v223 offset:4352\n\t" \
    "s_waitcnt lgkmcnt(0)\n\t" \
    "s_mov_b32 s68, 262144\n\t" \
    "s_mov_b32 s72, 0\n\t" \
    "s_mov_b32 s83, -1\n\t" \
    "s_mov_b32 s65, 0\n\t" \
    "s_barrier\n\t" \
    "s_mov_b32 s65, s67\n\t" \
    "s_mov_b32 s96, s67\n\t" \
    "s_add_u32 s38, s36, 48234496\n\t" \
    "s_addc_u32 s39, s37, 0\n\t" \
    "oo_poll1_%=:\n\t" \
    "s_cmp_gt_u32 s96, 34\n\t" \
    "s_cselect_b64 vcc, -1, 0\n\t" \
    "v_cndmask_b32 v25, v12, v13, vcc\n\t" \
    "global_load_dword v24, v25, s[36:37] sc1\n\t" \
    "s_waitcnt vmcnt(0)\n\t" \
    "v_cmp_gt_i32 vcc, s96, v24\n\t" \
    "s_cbranch_vccz oo_go2_%=\n\t" \
    "s_cmp_eq_u32 s68, 0\n\t" \
    "s_cbranch_scc1 oo_F7_%=\n\t" \
    "s_sub_u32 s68, s68, 1\n\t" \
    "s_sleep 10\n\t" \
    "s_branch oo_poll1_%=\n\t" \
    "oo_F7_%=:\n\t" \
    "v_mov_b32 v25, 6095104\n\t" \
    "v_mov_b32 v26, 40\n\t" \
    "global_store_dword v25, v26, s[36:37] sc1\n\t" \
    "s_waitcnt vmcnt(0)\n\t" \
    "s_branch oo_go2_%=\n\t" \
    "oo_go2_%=:\n\t" \
    "s_mul_i32 s69, s96, 65536\n\t" \
    "s_add_u32 s69, s69, 146800640\n\t" \
    "s_add_u32 s40, s36, s69\n\t" \
    "s_addc_u32 s41, s37, 0\n\t" \
    "s_lshl_b32 s69, s96, 15\n\t" \
    "v_xor_b32 v18, s69, v0\n\t" \
    "v_xor_b32 v19, s69, v1\n\t" \
    "v_xor_b32 v20, s69, v2\n\t" \
    "v_xor_b32 v21, s69, v3\n\t" \
    "v_xor_b32 v22, s69, v4\n\t" \
    "v_xor_b32 v23, s69, v5\n\t" \
    "global_load_dwordx4 v[32:35], v6, s[40:41] offset:-4096 sc1\n\t" \
    "global_load_dwordx4 v[36:39], v6, s[40:41] offset:-3072 sc1\n\t" \
    "global_load_dwordx4 a[0:3], v18, s[38:39] offset:-4096 sc1\n\t" \
    "global_load_dwordx4 a[4:7], v19, s[38:39] offset:-4096 sc1\n\t" \
    "global_load_dwordx4 a[8:11], v20, s[38:39] offset:-4096 sc1\n\t" \
    "global_load_dwordx4 a[12:15], v21, s[38:39] offset:-4096 sc1\n\t" \
    "global_load_dwordx4 a[16:19], v22, s[38:39] offset:-4096 sc1\n\t" \
    "global_load_dwordx4 a[20:23], v23, s[38:39] offset:-4096 sc1\n\t" \
    "global_load_dwordx4 v[40:43], v6, s[40:41] offset:-2048 sc1\n\t" \
    "global_load_dwordx4 v[44:47], v6, s[40:41] offset:-1024 sc1\n\t" \
    "global_load_dwordx4 a[24:27], v18, s[38:39] offset:-3072 sc1\n\t" \
    "global_load_dwordx4 a[28:31], v19, s[38:39] offset:-3072 sc1\n\t" \
    "global_load_dwordx4 a[32:35], v20, s[38:39] offset:-3072 sc1\n\t" \
    "global_load_dwordx4 a[36:39], v21, s[38:39] offset:-3072 sc1\n\t" \
    "global_load_dwordx4 a[40:43], v22, s[38:39] offset:-3072 sc1\n\t" \
    "global_load_dwordx4 a[44:47], v23, s[38:39] offset:-3072 sc1\n\t" \
    "global_load_dwordx4 v[48:51], v6, s[40:41] offset:0 sc1\n\t" \
    "global_load_dwordx4 v[52:55], v6, s[40:41] offset:1024 sc1\n\t" \
    "global_load_dwordx4 a[48:51], v18, s[38:39] offset:-2048 sc1\n\t" \
    "global_load_dwordx4 a[52:55], v19, s[38:39] offset:-2048 sc1\n\t" \
    "global_load_dwordx4 a[56:59], v20, s[38:39] offset:-2048 sc1\n\t" \
    "global_load_dwordx4 a[60:63], v21, s[38:39] offset:-2048 sc1\n\t" \
    "global_load_dwordx4 a[64:67], v22, s[38:39] offset:-2048 sc1\n\t" \
    "global_load_dwordx4 a[68:71], v23, s[38:39] offset:-2048 sc1\n\t" \
    "global_load_dwordx4 v[56:59], v6, s[40:41] offset:2048 sc1\n\t" \
    "global_load_dwordx4 v[60:63], v6, s[40:41] offset:3072 sc1\n\t" \
    "global_load_dwordx4 a[72:75], v18, s[38:39] offset:-1024 sc1\n\t" \
    "global_load_dwordx4 a[76:79], v19, s[38:39] offset:-1024 sc1\n\t" \
    "global_load_dwordx4 a[80:83], v20, s[38:39] offset:-1024 sc1\n\t" \
    "global_load_dwordx4 a[84:87], v21, s[38:39] offset:-1024 sc1\n\t" \
    "global_load_dwordx4 a[88:91], v22, s[38:39] offset:-1024 sc1\n\t" \
    "global_load_dwordx4 a[92:95], v23, s[38:39] offset:-1024 sc1\n\t" \
    "oo_loop3_%=:\n\t" \
    "s_mul_i32 s69, s96, 16384\n\t" \
    "s_add_u32 s69, s69, 150994944\n\t" \
    "s_add_u32 s46, s36, s69\n\t" \
    "s_addc_u32 s47, s37, 0\n\t" \
    "s_mul_i32 s69, s96, 9216\n\t" \
    "s_add_u32 s69, s69, 0\n\t" \
    "s_add_u32 s48, s36, s69\n\t" \
    "s_addc_u32 s49, s37, 0\n\t" \
    "s_waitcnt vmcnt(29)\n\t" \
    "v_mfma_f32_16x16x32_bf16 v[72:75], a[0:3], v[32:35], 0\n\t" \
    "global_load_dwordx4 v[64:67], v7, s[40:41] offset:-4096 sc1\n\t" \
    "v_mfma_f32_16x16x32_bf16 v[76:79], a[0:3], v[36:39], 0\n\t" \
    "global_load_dwordx4 v[68:71], v7, s[40:41] offset:-3072 sc1\n\t" \
    "s_waitcnt vmcnt(30)\n\t" \
    "v_mfma_f32_16x16x32_bf16 v[80:83], a[4:7], v[32:35], 0\n\t" \
    "global_load_dwordx4 a[96:99], v18, s[38:39] offset:0 sc1\n\t" \
    "v_mfma_f32_16x16x32_bf16 v[84:87], a[4:7], v[36:39], 0\n\t" \
    "global_load_dwordx4 a[100:103], v19, s[38:39] offset:0 sc1\n\t" \
    "s_waitcnt vmcnt(31)\n\t" \
    "v_mfma_f32_16x16x32_bf16 v[88:91], a[8:11], v[32:35], 0\n\t" \
    "global_load_dwordx4 a[104:107], v20, s[38:39] offset:0 sc1\n\t" \
    "v_mfma_f32_16x16x32_bf16 v[92:95], a[8:11], v[36:39], 0\n\t" \
    "global_load_dwordx4 a[108:111], v21, s[38:39] offset:0 sc1\n\t" \
    "s_waitcnt vmcnt(32)\n\t" \
    "v_mfma_f32_16x16x32_bf16 v[96:99], a[12:15], v[32:35], 0\n\t" \
    "global_load_dwordx4 a[112:115], v22, s[38:39] offset:0 sc1\n\t" \
    "v_mfma_f32_16x16x32_bf16 v[100:103], a[12:15], v[36:39], 0\n\t" \
    "global_load_dwordx4 a[116:119], v23, s[38:39] offset:0 sc1\n\t" \
    "s_waitcnt vmcnt(33)\n\t" \
    "v_mfma_f32_16x16x32_bf16 v[104:107], a[16:19], v[32:35], 0\n\t" \
    "v_mfma_f32_16x16x32_bf16 v[108:111], a[16:19], v[36:39], 0\n\t" \
    "s_waitcnt vmcnt(32)\n\t" \
    "v_mfma_f32_16x16x32_bf16 v[112:115], a[20:23], v[32:35], 0\n\t" \
    "v_mfma_f32_16x16x32_bf16 v[116:119], a[20:23], v[36:39], 0\n\t" \
    "s_add_u32 s96, s96, 4\n\t" \
    "s_cmp_gt_u32 s96, 34\n\t" \
    "s_cselect_b64 vcc, -1, 0\n\t" \
    "v_cndmask_b32 v25, v12, v13, vcc\n\t" \
    "global_load_dword v24, v25, s[36:37] sc1\n\t" \
    "s_waitcnt vmcnt(30)\n\t" \
    "v_mfma_f32_16x16x32_bf16 v[72:75], a[24:27], v[40:43], v[72:75]\n\t" \
    "global_load_dwordx4 v[32:35], v7, s[40:41] offset:-2048 sc1\n\t" \
    "v_mfma_f32_16x16x32_bf16 v[76:79], a[24:27], v[44:47], v[76:79]\n\t" \
    "global_load_dwordx4 v[36:39], v7, s[40:41] offset:-1024 sc1\n\t" \
    "s_waitcnt vmcnt(31)\n\t" \
    "v_mfma_f32_16x16x32_bf16 v[80:83], a[28:31], v[40:43], v[80:83]\n\t" \
    "global_load_dwordx4 a[0:3], v18, s[38:39] offset:1024 sc1\n\t" \
    "v_mfma_f32_16x16x32_bf16 v[84:87], a[28:31], v[44:47], v[84:87]\n\t" \
    "global_load_dwordx4 a[4:7], v19, s[38:39] offset:1024 sc1\n\t" \
    "s_waitcnt vmcnt(32)\n\t" \
    "v_mfma_f32_16x16x32_bf16 v[88:91], a[32:35], v[40:43], v[88:91]\n\t" \
    "global_load_dwordx4 a[8:11], v20, s[38:39] offset:1024 sc1\n\t" \
    "v_mfma_f32_16x16x32_bf16 v[92:95], a[32:35], v[44:47], v[92:95]\n\t" \
    "global_load_dwordx4 a[12:15], v21, s[38:39] offset:1024 sc1\n\t" \
    "s_waitcnt vmcnt(33)\n\t" \
    "v_mfma_f32_16x16x32_bf16 v[96:99], a[36:39], v[40:43], v[96:99]\n\t" \
    "global_load_dwordx4 a[16:19], v22, s[38:39] offset:1024 sc1\n\t" \
    "v_mfma_f32_16x16x32_bf16 v[100:103], a[36:39], v[44:47], v[100:103]\n\t" \
    "global_load_dwordx4 a[20:23], v23, s[38:39] offset:1024 sc1\n\t" \
    "s_waitcnt vmcnt(34)\n\t" \
    "v_mfma_f32_16x16x32_bf16 v[104:107], a[40:43], v[40:43], v[104:107]\n\t" \
    "v_mfma_f32_16x16x32_bf16 v[108:111], a[40:43], v[44:47], v[108:111]\n\t" \
    "s_waitcnt vmcnt(33)\n\t" \
    "v_mfma_f32_16x16x32_bf16 v[112:115], a[44:47], v[40:43], v[112:115]\n\t" \
    "v_mfma_f32_16x16x32_bf16 v[116:119], a[44:47], v[44:47], v[116:119]\n\t" \
    "s_waitcnt vmcnt(30)\n\t" \
    "v_mfma_f32_16x16x32_bf16 v[72:75], a[48:51], v[48:51], v[72:75]\n\t" \
    "global_load_dwordx4 v[40:43], v7, s[40:41] offset:0 sc1\n\t" \
    "v_mfma_f32_16x16x32_bf16 v[76:79], a[48:51], v[52:55], v[76:79]\n\t" \
    "global_load_dwordx4 v[44:47], v7, s[40:41] offset:1024 sc1\n\t" \
    "s_waitcnt vmcnt(31)\n\t" \
    "v_mfma_f32_16x16x32_bf16 v[80:83], a[52:55], v[48:51], v[80:83]\n\t" \
    "global_load_dwordx4 a[24:27], v18, s[38:39] offset:2048 sc1\n\t" \
    "v_mfma_f32_16x16x32_bf16 v[84:87], a[52:55], v[52:55], v[84:87]\n\t" \
    "global_load_dwordx4 a[28:31], v19, s[38:39] offset:2048 sc1\n\t" \
    "s_waitcnt vmcnt(32)\n\t" \
    "v_mfma_f32_16x16x32_bf16 v[88:91], a[56:59], v[48:51], v[88:91]\n\t" \
    "global_load_dwordx4 a[32:35], v20, s[38:39] offset:2048 sc1\n\t" \
    "v_mfma_f32_16x16x32_bf16 v[92:95], a[56:59], v[52:55], v[92:95]\n\t" \
    "global_load_dwordx4 a[36:39], v21, s[38:39] offset:2048 sc1\n\t" \
    "s_waitcnt vmcnt(33)\n\t" \
    "v_mfma_f32_16x16x32_bf16 v[96:99], a[60:63], v[48:51], v[96:99]\n\t" \
    "global_load_dwordx4 a[40:43], v22, s[38:39] offset:2048 sc1\n\t" \
    "v_mfma_f32_16x16x32_bf16 v[100:103], a[60:63], v[52:55], v[100:103]\n\t" \
    "global_load_dwordx4 a[44:47], v23, s[38:39] offset:2048 sc1\n\t" \
    "s_waitcnt vmcnt(34)\n\t" \
    "v_mfma_f32_16x16x32_bf16 v[104:107], a[64:67], v[48:51], v[104:107]\n\t" \
    "v_mfma_f32_16x16x32_bf16 v[108:111], a[64:67], v[52:55], v[108:111]\n\t" \
    "s_waitcnt vmcnt(33)\n\t" \
    "v_mfma_f32_16x16x32_bf16 v[112:115], a[68:71], v[48:51], v[112:115]\n\t" \
    "v_mfma_f32_16x16x32_bf16 v[116:119], a[68:71], v[52:55], v[116:119]\n\t" \
    "s_waitcnt vmcnt(30)\n\t" \
    "v_mfma_f32_16x16x32_bf16 v[72:75], a[72:75], v[56:59], v[72:75]\n\t" \
    "global_load_dwordx4 v[48:51], v7, s[40:41] offset:2048 sc1\n\t" \
    "v_mfma_f32_16x16x32_bf16 v[76:79], a[72:75], v[60:63], v[76:79]\n\t" \
    "global_load_dwordx4 v[52:55], v7, s[40:41] offset:3072 sc1\n\t" \
    "s_waitcnt vmcnt(31)\n\t" \
    "v_mfma_f32_16x16x32_bf16 v[80:83], a[76:79], v[56:59], v[80:83]\n\t" \
    "global_load_dwordx4 a[48:51], v18, s[38:39] offset:3072 sc1\n\t" \
    "v_mfma_f32_16x16x32_bf16 v[84:87], a[76:79], v[60:63], v[84:87]\n\t" \
    "global_load_dwordx4 a[52:55], v19, s[38:39] offset:3072 sc1\n\t" \
    "s_waitcnt vmcnt(32)\n\t" \
    "v_mfma_f32_16x16x32_bf16 v[88:91], a[80:83], v[56:59], v[88:91]\n\t" \
    "global_load_dwordx4 a[56:59], v20, s[38:39] offset:3072 sc1\n\t" \
    "v_mfma_f32_16x16x32_bf16 v[92:95], a[80:83], v[60:63], v[92:95]\n\t" \
    "global_load_dwordx4 a[60:63], v21, s[38:39] offset:3072 sc1\n\t" \
    "s_waitcnt vmcnt(33)\n\t" \
    "v_mfma_f32_16x16x32_bf16 v[96:99], a[84:87], v[56:59], v[96:99]\n\t" \
    "global_load_dwordx4 a[64:67], v22, s[38:39] offset:3072 sc1\n\t" \
    "v_mfma_f32_16x16x32_bf16 v[100:103], a[84:87], v[60:63], v[100:103]\n\t" \
    "global_load_dwordx4 a[68:71], v23, s[38:39] offset:3072 sc1\n\t" \
    "s_waitcnt vmcnt(34)\n\t" \
    "v_mfma_f32_16x16x32_bf16 v[104:107], a[88:91], v[56:59], v[104:107]\n\t" \
    "v_mfma_f32_16x16x32_bf16 v[108:111], a[88:91], v[60:63], v[108:111]\n\t" \
    "s_waitcnt vmcnt(33)\n\t" \
    "v_mfma_f32_16x16x32_bf16 v[112:115], a[92:95], v[56:59], v[112:115]\n\t" \
    "v_mfma_f32_16x16x32_bf16 v[116:119], a[92:95], v[60:63], v[116:119]\n\t" \
    "s_waitcnt vmcnt(30)\n\t" \
    "v_mfma_f32_16x16x32_bf16 v[72:75], a[96:99], v[64:67], v[72:75]\n\t" \
    "global_load_dwordx4 v[56:59], v8, s[46:47] offset:0 sc1\n\t" \
    "v_mfma_f32_16x16x32_bf16 v[76:79], a[96:99], v[68:71], v[76:79]\n\t" \
    "global_load_dwordx4 v[60:63], v9, s[46:47] offset:0 sc1\n\t" \
    "s_waitcnt vmcnt(31)\n\t" \
    "v_mfma_f32_16x16x32_bf16 v[80:83], a[100:103], v[64:67], v[80:83]\n\t" \
    "global_load_dwordx4 a[72:75], v10, s[48:49] offset:0 sc1\n\t" \
    "v_mfma_f32_16x16x32_bf16 v[84:87], a[100:103], v[68:71], v[84:87]\n\t" \
    "global_load_dwordx4 a[76:79], v10, s[48:49] offset:256 sc1\n\t" \
    "s_waitcnt vmcnt(32)\n\t" \
    "v_mfma_f32_16x16x32_bf16 v[88:91], a[104:107], v[64:67], v[88:91]\n\t" \
    "global_load_dwordx4 a[80:83], v10, s[48:49] offset:512 sc1\n\t" \
    "v_mfma_f32_16x16x32_bf16 v[92:95], a[104:107], v[68:71], v[92:95]\n\t" \
    "global_load_dwordx4 a[84:87], v10, s[48:49] offset:768 sc1\n\t" \
    "s_waitcnt vmcnt(33)\n\t" \
    "v_mfma_f32_16x16x32_bf16 v[96:99], a[108:111], v[64:67], v[96:99]\n\t" \
    "global_load_dwordx4 a[88:91], v10, s[48:49] offset:1024 sc1\n\t" \
    "v_mfma_f32_16x16x32_bf16 v[100:103], a[108:111], v[68:71], v[100:103]\n\t" \
    "global_load_dwordx4 a[92:95], v10, s[48:49] offset:1280 sc1\n\t" \
    "s_waitcnt vmcnt(34)\n\t" \
    "v_mfma_f32_16x16x32_bf16 v[104:107], a[112:115], v[64:67], v[104:107]\n\t" \
    "v_mfma_f32_16x16x32_bf16 v[108:111], a[112:115], v[68:71], v[108:111]\n\t" \
    "s_waitcnt vmcnt(33)\n\t" \
    "v_mfma_f32_16x16x32_bf16 v[112:115], a[116:119], v[64:67], v[112:115]\n\t" \
    "v_mfma_f32_16x16x32_bf16 v[116:119], a[116:119], v[68:71], v[116:119]\n\t" \
    "s_waitcnt vmcnt(29)\n\t" \
    "v_mfma_f32_16x16x32_bf16 v[72:75], a[0:3], v[32:35], v[72:75]\n\t" \
    "global_load_dwordx4 v[64:67], v8, s[46:47] offset:1024 sc1\n\t" \
    "v_mfma_f32_16x16x32_bf16 v[76:79], a[0:3], v[36:39], v[76:79]\n\t" \
    "global_load_dwordx4 v[68:71], v9, s[46:47] offset:1024 sc1\n\t" \
    "s_waitcnt vmcnt(30)\n\t" \
    "v_mfma_f32_16x16x32_bf16 v[80:83], a[4:7], v[32:35], v[80:83]\n\t" \
    "global_load_dwordx4 a[96:99], v11, s[48:49] offset:0 sc1\n\t" \
    "v_mfma_f32_16x16x32_bf16 v[84:87], a[4:7], v[36:39], v[84:87]\n\t" \
    "global_load_dwordx4 a[100:103], v11, s[48:49] offset:256 sc1\n\t" \
    "s_waitcnt vmcnt(31)\n\t" \
    "v_mfma_f32_16x16x32_bf16 v[88:91], a[8:11], v[32:35], v[88:91]\n\t" \
    "global_load_dwordx4 a[104:107], v11, s[48:49] offset:512 sc1\n\t" \
    "v_mfma_f32_16x16x32_bf16 v[92:95], a[8:11], v[36:39], v[92:95]\n\t" \
    "global_load_dwordx4 a[108:111], v11, s[48:49] offset:768 sc1\n\t" \
    "s_waitcnt vmcnt(32)\n\t" \
    "v_mfma_f32_16x16x32_bf16 v[96:99], a[12:15], v[32:35], v[96:99]\n\t" \
    "global_load_dwordx4 a[112:115], v11, s[48:49] offset:1024 sc1\n\t" \
    "v_mfma_f32_16x16x32_bf16 v[100:103], a[12:15], v[36:39], v[100:103]\n\t" \
    "global_load_dwordx4 a[116:119], v11, s[48:49] offset:1280 sc1\n\t" \
    "s_waitcnt vmcnt(33)\n\t" \
    "v_mfma_f32_16x16x32_bf16 v[104:107], a[16:19], v[32:35], v[104:107]\n\t" \
    "v_mfma_f32_16x16x32_bf16 v[108:111], a[16:19], v[36:39], v[108:111]\n\t" \
    "s_waitcnt vmcnt(32)\n\t" \
    "v_mfma_f32_16x16x32_bf16 v[112:115], a[20:23], v[32:35], v[112:115]\n\t" \
    "v_mfma_f32_16x16x32_bf16 v[116:119], a[20:23], v[36:39], v[116:119]\n\t" \
    "v_cmp_gt_i32 vcc, s96, v24\n\t" \
    "s_cmp_ge_u32 s96, 58\n\t" \
    "s_cbranch_scc1 oo_stop4_%=\n\t" \
    "s_cbranch_vccnz oo_stop4_%=\n\t" \
    "s_mul_i32 s69, s96, 65536\n\t" \
    "s_add_u32 s69, s69, 146800640\n\t" \
    "s_add_u32 s40, s36, s69\n\t" \
    "s_addc_u32 s41, s37, 0\n\t" \
    "s_lshl_b32 s69, s96, 15\n\t" \
    "v_xor_b32 v18, s69, v0\n\t" \
    "v_xor_b32 v19, s69, v1\n\t" \
    "v_xor_b32 v20, s69, v2\n\t" \
    "v_xor_b32 v21, s69, v3\n\t" \
    "v_xor_b32 v22, s69, v4\n\t" \
    "v_xor_b32 v23, s69, v5\n\t" \
    "s_waitcnt vmcnt(29)\n\t" \
    "v_mfma_f32_16x16x32_bf16 v[72:75], a[24:27], v[40:43], v[72:75]\n\t" \
    "global_load_dwordx4 v[32:35], v6, s[40:41] offset:-4096 sc1\n\t" \
    "v_mfma_f32_16x16x32_bf16 v[76:79], a[24:27], v[44:47], v[76:79]\n\t" \
    "global_load_dwordx4 v[36:39], v6, s[40:41] offset:-3072 sc1\n\t" \
    "s_waitcnt vmcnt(30)\n\t" \
    "v_mfma_f32_16x16x32_bf16 v[80:83], a[28:31], v[40:43], v[80:83]\n\t" \
    "global_load_dwordx4 a[0:3], v18, s[38:39] offset:-4096 sc1\n\t" \
    "v_mfma_f32_16x16x32_bf16 v[84:87], a[28:31], v[44:47], v[84:87]\n\t" \
    "global_load_dwordx4 a[4:7], v19, s[38:39] offset:-4096 sc1\n\t" \
    "s_waitcnt vmcnt(31)\n\t" \
    "v_mfma_f32_16x16x32_bf16 v[88:91], a[32:35], v[40:43], v[88:91]\n\t" \
    "global_load_dwordx4 a[8:11], v20, s[38:39] offset:-4096 sc1\n\t" \
    "v_mfma_f32_16x16x32_bf16 v[92:95], a[32:35], v[44:47], v[92:95]\n\t" \
    "global_load_dwordx4 a[12:15], v21, s[38:39] offset:-4096 sc1\n\t" \
    "s_waitcnt vmcnt(32)\n\t" \
    "v_mfma_f32_16x16x32_bf16 v[96:99], a[36:39], v[40:43], v[96:99]\n\t" \
    "global_load_dwordx4 a[16:19], v22, s[38:39] offset:-4096 sc1\n\t" \
    "v_mfma_f32_16x16x32_bf16 v[100:103], a[36:39], v[44:47], v[100:103]\n\t" \
    "global_load_dwordx4 a[20:23], v23, s[38:39] offset:-4096 sc1\n\t" \
    "s_waitcnt vmcnt(33)\n\t" \
    "v_mfma_f32_16x16x32_bf16 v[104:107], a[40:43], v[40:43], v[104:107]\n\t" \
    "v_mfma_f32_16x16x32_bf16 v[108:111], a[40:43], v[44:47], v[108:111]\n\t" \
    "s_waitcnt vmcnt(32)\n\t" \
    "v_mfma_f32_16x16x32_bf16 v[112:115], a[44:47], v[40:43], v[112:115]\n\t" \
    "v_mfma_f32_16x16x32_bf16 v[116:119], a[44:47], v[44:47], v[116:119]\n\t" \
    "s_waitcnt vmcnt(29)\n\t" \
    "v_mfma_f32_16x16x32_bf16 v[72:75], a[48:51], v[48:51], v[72:75]\n\t" \
    "global_load_dwordx4 v[40:43], v6, s[40:41] offset:-2048 sc1\n\t" \
    "v_mfma_f32_16x16x32_bf16 v[76:79], a[48:51], v[52:55], v[76:79]\n\t" \
    "global_load_dwordx4 v[44:47], v6, s[40:41] offset:-1024 sc1\n\t" \
    "s_waitcnt vmcnt(30)\n\t" \
    "v_mfma_f32_16x16x32_bf16 v[80:83], a[52:55], v[48:51], v[80:83]\n\t" \
    "global_load_dwordx4 a[24:27], v18, s[38:39] offset:-3072 sc1\n\t" \
    "v_mfma_f32_16x16x32_bf16 v[84:87], a[52:55], v[52:55], v[84:87]\n\t" \
    "global_load_dwordx4 a[28:31], v19, s[38:39] offset:-3072 sc1\n\t" \
    "s_waitcnt vmcnt(31)\n\t" \
    "v_mfma_f32_16x16x32_bf16 v[88:91], a[56:59], v[48:51], v[88:91]\n\t" \
    "global_load_dwordx4 a[32:35], v20, s[38:39] offset:-3072 sc1\n\t" \
    "v_mfma_f32_16x16x32_bf16 v[92:95], a[56:59], v[52:55], v[92:95]\n\t" \
    "global_load_dwordx4 a[36:39], v21, s[38:39] offset:-3072 sc1\n\t" \
    "s_waitcnt vmcnt(32)\n\t" \
    "v_mfma_f32_16x16x32_bf16 v[96:99], a[60:63], v[48:51], v[96:99]\n\t" \
    "global_load_dwordx4 a[40:43], v22, s[38:39] offset:-3072 sc1\n\t" \
    "v_mfma_f32_16x16x32_bf16 v[100:103], a[60:63], v[52:55], v[100:103]\n\t" \
    "global_load_dwordx4 a[44:47], v23, s[38:39] offset:-3072 sc1\n\t" \
    "s_waitcnt vmcnt(33)\n\t" \
    "v_mfma_f32_16x16x32_bf16 v[104:107], a[64:67], v[48:51], v[104:107]\n\t" \
    "v_mfma_f32_16x16x32_bf16 v[108:111], a[64:67], v[52:55], v[108:111]\n\t" \
    "s_waitcnt vmcnt(32)\n\t" \
    "v_mfma_f32_16x16x32_bf16 v[112:115], a[68:71], v[48:51], v[112:115]\n\t" \
    "v_mfma_f32_16x16x32_bf16 v[116:119], a[68:71], v[52:55], v[116:119]\n\t" \
    "s_waitcnt vmcnt(29)\n\t" \
    "v_mfma_f32_16x16x32_bf16 v[72:75], a[72:75], v[56:59], v[72:75]\n\t" \
    "global_load_dwordx4 v[48:51], v6, s[40:41] offset:0 sc1\n\t" \
    "v_mfma_f32_16x16x32_bf16 v[76:79], a[72:75], v[60:63], v[76:79]\n\t" \
    "global_load_dwordx4 v[52:55], v6, s[40:41] offset:1024 sc1\n\t" \
    "s_waitcnt vmcnt(30)\n\t" \
    "v_mfma_f32_16x16x32_bf16 v[80:83], a[76:79], v[56:59], v[80:83]\n\t" \
    "global_load_dwordx4 a[48:51], v18, s[38:39] offset:-2048 sc1\n\t" \
    "v_mfma_f32_16x16x32_bf16 v[84:87], a[76:79], v[60:63], v[84:87]\n\t" \
    "global_load_dwordx4 a[52:55], v19, s[38:39] offset:-2048 sc1\n\t" \
    "s_waitcnt vmcnt(31)\n\t" \
    "v_mfma_f32_16x16x32_bf16 v[88:91], a[80:83], v[56:59], v[88:91]\n\t" \
    "global_load_dwordx4 a[56:59], v20, s[38:39] offset:-2048 sc1\n\t" \
    "v_mfma_f32_16x16x32_bf16 v[92:95], a[80:83], v[60:63], v[92:95]\n\t" \
    "global_load_dwordx4 a[60:63], v21, s[38:39] offset:-2048 sc1\n\t" \
    "s_waitcnt vmcnt(32)\n\t" \
    "v_mfma_f32_16x16x32_bf16 v[96:99], a[84:87], v[56:59], v[96:99]\n\t" \
    "global_load_dwordx4 a[64:67], v22, s[38:39] offset:-2048 sc1\n\t" \
    "v_mfma_f32_16x16x32_bf16 v[100:103], a[84:87], v[60:63], v[100:103]\n\t" \
    "global_load_dwordx4 a[68:71], v23, s[38:39] offset:-2048 sc1\n\t" \
    "s_waitcnt vmcnt(33)\n\t" \
    "v_mfma_f32_16x16x32_bf16 v[104:107], a[88:91], v[56:59], v[104:107]\n\t" \
    "v_mfma_f32_16x16x32_bf16 v[108:111], a[88:91], v[60:63], v[108:111]\n\t" \
    "s_waitcnt vmcnt(32)\n\t" \
    "v_mfma_f32_16x16x32_bf16 v[112:115], a[92:95], v[56:59], v[112:115]\n\t" \
    "v_mfma_f32_16x16x32_bf16 v[116:119], a[92:95], v[60:63], v[116:119]\n\t" \
    "s_waitcnt vmcnt(29)\n\t" \
    "v_mfma_f32_16x16x32_bf16 v[72:75], a[96:99], v[64:67], v[72:75]\n\t" \
    "global_load_dwordx4 v[56:59], v6, s[40:41] offset:2048 sc1\n\t" \
    "v_mfma_f32_16x16x32_bf16 v[76:79], a[96:99], v[68:71], v[76:79]\n\t" \
    "global_load_dwordx4 v[60:63], v6, s[40:41] offset:3072 sc1\n\t" \
    "s_waitcnt vmcnt(30)\n\t" \
    "v_mfma_f32_16x16x32_bf16 v[80:83], a[100:103], v[64:67], v[80:83]\n\t" \
    "global_load_dwordx4 a[72:75], v18, s[38:39] offset:-1024 sc1\n\t" \
    "v_mfma_f32_16x16x32_bf16 v[84:87], a[100:103], v[68:71], v[84:87]\n\t" \
    "global_load_dwordx4 a[76:79], v19, s[38:39] offset:-1024 sc1\n\t" \
    "s_waitcnt vmcnt(31)\n\t" \
    "v_mfma_f32_16x16x32_bf16 v[88:91], a[104:107], v[64:67], v[88:91]\n\t" \
    "global_load_dwordx4 a[80:83], v20, s[38:39] offset:-1024 sc1\n\t" \
    "v_mfma_f32_16x16x32_bf16 v[92:95], a[104:107], v[68:71], v[92:95]\n\t" \
    "global_load_dwordx4 a[84:87], v21, s[38:39] offset:-1024 sc1\n\t" \
    "s_waitcnt vmcnt(32)\n\t" \
    "v_mfma_f32_16x16x32_bf16 v[96:99], a[108:111], v[64:67], v[96:99]\n\t" \
    "global_load_dwordx4 a[88:91], v22, s[38:39] offset:-1024 sc1\n\t" \
    "v_mfma_f32_16x16x32_bf16 v[100:103], a[108:111], v[68:71], v[100:103]\n\t" \
    "global_load_dwordx4 a[92:95], v23, s[38:39] offset:-1024 sc1\n\t" \
    "s_waitcnt vmcnt(33)\n\t" \
    "v_mfma_f32_16x16x32_bf16 v[104:107], a[112:115], v[64:67], v[104:107]\n\t" \
    "v_mfma_f32_16x16x32_bf16 v[108:111], a[112:115], v[68:71], v[108:111]\n\t" \
    "s_waitcnt vmcnt(32)\n\t" \
    "v_mfma_f32_16x16x32_bf16 v[112:115], a[116:119], v[64:67], v[112:115]\n\t" \
    "v_mfma_f32_16x16x32_bf16 v[116:119], a[116:119], v[68:71], v[116:119]\n\t" \
    "s_mov_b32 s97, 1\n\t" \
    "s_branch oo_epi5_%=\n\t" \
    "oo_stop4_%=:\n\t" \
    "s_waitcnt vmcnt(29)\n\t" \
    "v_mfma_f32_16x16x32_bf16 v[72:75], a[24:27], v[40:43], v[72:75]\n\t" \
    "v_mfma_f32_16x16x32_bf16 v[76:79], a[24:27], v[44:47], v[76:79]\n\t" \
    "s_waitcnt vmcnt(28)\n\t" \
    "v_mfma_f32_16x16x32_bf16 v[80:83], a[28:31], v[40:43], v[80:83]\n\t" \
    "v_mfma_f32_16x16x32_bf16 v[84:87], a[28:31], v[44:47], v[84:87]\n\t" \
    "s_waitcnt vmcnt(27)\n\t" \
    "v_mfma_f32_16x16x32_bf16 v[88:91], a[32:35], v[40:43], v[88:91]\n\t" \
    "v_mfma_f32_16x16x32_bf16 v[92:95], a[32:35], v[44:47], v[92:95]\n\t" \
    "s_waitcnt vmcnt(26)\n\t" \
    "v_mfma_f32_16x16x32_bf16 v[96:99], a[36:39], v[40:43], v[96:99]\n\t" \
    "v_mfma_f32_16x16x32_bf16 v[100:103], a[36:39], v[44:47], v[100:103]\n\t" \
    "s_waitcnt vmcnt(25)\n\t" \
    "v_mfma_f32_16x16x32_bf16 v[104:107], a[40:43], v[40:43], v[104:107]\n\t" \
    "v_mfma_f32_16x16x32_bf16 v[108:111], a[40:43], v[44:47], v[108:111]\n\t" \
    "s_waitcnt vmcnt(24)\n\t" \
    "v_mfma_f32_16x16x32_bf16 v[112:115], a[44:47], v[40:43], v[112:115]\n\t" \
    "v_mfma_f32_16x16x32_bf16 v[116:119], a[44:47], v[44:47], v[116:119]\n\t" \
    "s_waitcnt vmcnt(21)\n\t" \
    "v_mfma_f32_16x16x32_bf16 v[72:75], a[48:51], v[48:51], v[72:75]\n\t" \
    "v_mfma_f32_16x16x32_bf16 v[76:79], a[48:51], v[52:55], v[76:79]\n\t" \
    "s_waitcnt vmcnt(20)\n\t" \
    "v_mfma_f32_16x16x32_bf16 v[80:83], a[52:55], v[48:51], v[80:83]\n\t" \
    "v_mfma_f32_16x16x32_bf16 v[84:87], a[52:55], v[52:55], v[84:87]\n\t" \
    "s_waitcnt vmcnt(19)\n\t" \
    "v_mfma_f32_16x16x32_bf16 v[88:91], a[56:59], v[48:51], v[88:91]\n\t" \
    "v_mfma_f32_16x16x32_bf16 v[92:95], a[56:59], v[52:55], v[92:95]\n\t" \
    "s_waitcnt vmcnt(18)\n\t" \
    "v_mfma_f32_16x16x32_bf16 v[96:99], a[60:63], v[48:51], v[96:99]\n\t" \
    "v_mfma_f32_16x16x32_bf16 v[100:103], a[60:63], v[52:55], v[100:103]\n\t" \
    "s_waitcnt vmcnt(17)\n\t" \
    "v_mfma_f32_16x16x32_bf16 v[104:107], a[64:67], v[48:51], v[104:107]\n\t" \
    "v_mfma_f32_16x16x32_bf16 v[108:111], a[64:67], v[52:55], v[108:111]\n\t" \
    "s_waitcnt vmcnt(16)\n\t" \
    "v_mfma_f32_16x16x32_bf16 v[112:115], a[68:71], v[48:51], v[112:115]\n\t" \
    "v_mfma_f32_16x16x32_bf16 v[116:119], a[68:71], v[52:55], v[116:119]\n\t" \
    "s_waitcnt vmcnt(13)\n\t" \
    "v_mfma_f32_16x16x32_bf16 v[72:75], a[72:75], v[56:59], v[72:75]\n\t" \
    "v_mfma_f32_16x16x32_bf16 v[76:79], a[72:75], v[60:63], v[76:79]\n\t" \
    "s_waitcnt vmcnt(12)\n\t" \
    "v_mfma_f32_16x16x32_bf16 v[80:83], a[76:79], v[56:59], v[80:83]\n\t" \
    "v_mfma_f32_16x16x32_bf16 v[84:87], a[76:79], v[60:63], v[84:87]\n\t" \
    "s_waitcnt vmcnt(11)\n\t" \
    "v_mfma_f32_16x16x32_bf16 v[88:91], a[80:83], v[56:59], v[88:91]\n\t" \
    "v_mfma_f32_16x16x32_bf16 v[92:95], a[80:83], v[60:63], v[92:95]\n\t" \
    "s_waitcnt vmcnt(10)\n\t" \
    "v_mfma_f32_16x16x32_bf16 v[96:99], a[84:87], v[56:59], v[96:99]\n\t" \
    "v_mfma_f32_16x16x32_bf16 v[100:103], a[84:87], v[60:63], v[100:103]\n\t" \
    "s_waitcnt vmcnt(9)\n\t" \
    "v_mfma_f32_16x16x32_bf16 v[104:107], a[88:91], v[56:59], v[104:107]\n\t" \
    "v_mfma_f32_16x16x32_bf16 v[108:111], a[88:91], v[60:63], v[108:111]\n\t" \
    "s_waitcnt vmcnt(8)\n\t" \
    "v_mfma_f32_16x16x32_bf16 v[112:115], a[92:95], v[56:59], v[112:115]\n\t" \
    "v_mfma_f32_16x16x32_bf16 v[116:119], a[92:95], v[60:63], v[116:119]\n\t" \
    "s_waitcnt vmcnt(5)\n\t" \
    "v_mfma_f32_16x16x32_bf16 v[72:75], a[96:99], v[64:67], v[72:75]\n\t" \
    "v_mfma_f32_16x16x32_bf16 v[76:79], a[96:99], v[68:71], v[76:79]\n\t" \
    "s_waitcnt vmcnt(4)\n\t" \
    "v_mfma_f32_16x16x32_bf16 v[80:83], a[100:103], v[64:67], v[80:83]\n\t" \
    "v_mfma_f32_16x16x32_bf16 v[84:87], a[100:103], v[68:71], v[84:87]\n\t" \
    "s_waitcnt vmcnt(3)\n\t" \
    "v_mfma_f32_16x16x32_bf16 v[88:91], a[104:107], v[64:67], v[88:91]\n\t" \
    "v_mfma_f32_16x16x32_bf16 v[92:95], a[104:107], v[68:71], v[92:95]\n\t" \
    "s_waitcnt vmcnt(2)\n\t" \
    "v_mfma_f32_16x16x32_bf16 v[96:99], a[108:111], v[64:67], v[96:99]\n\t" \
    "v_mfma_f32_16x16x32_bf16 v[100:103], a[108:111], v[68:71], v[100:103]\n\t" \
    "s_waitcnt vmcnt(1)\n\t" \
    "v_mfma_f32_16x16x32_bf16 v[104:107], a[112:115], v[64:67], v[104:107]\n\t" \
    "v_mfma_f32_16x16x32_bf16 v[108:111], a[112:115], v[68:71], v[108:111]\n\t" \
    "s_waitcnt vmcnt(0)\n\t" \
    "v_mfma_f32_16x16x32_bf16 v[112:115], a[116:119], v[64:67], v[112:115]\n\t" \
    "v_mfma_f32_16x16x32_bf16 v[116:119], a[116:119], v[68:71], v[116:119]\n\t" \
    "s_mov_b32 s97, 0\n\t" \
    "oo_epi5_%=:\n\t" \
    "s_mul_i32 s69, s65, 73728\n\t" \
    "s_add_u32 s44, s42, s69\n\t" \
    "s_addc_u32 s45, s43, 0\n\t" \
    "s_nop 7\n\t" \
    "ds_write_b128 v14, v[72:75] offset:0\n\t" \
    "ds_write_b128 v14, v[76:79] offset:1024\n\t" \
    "ds_write_b128 v14, v[80:83] offset:2048\n\t" \
    "ds_write_b128 v14, v[84:87] offset:3072\n\t" \
    "ds_write_b128 v14, v[88:91] offset:4096\n\t" \
    "ds_write_b128 v14, v[92:95] offset:5120\n\t" \
    "ds_write_b128 v14, v[96:99] offset:6144\n\t" \
    "ds_write_b128 v14, v[100:103] offset:7168\n\t" \
    "ds_write_b128 v14, v[104:107] offset:8192\n\t" \
    "ds_write_b128 v14, v[108:111] offset:9216\n\t" \
    "ds_write_b128 v14, v[112:115] offset:10240\n\t" \
    "ds_write_b128 v14, v[116:119] offset:11264\n\t" \
    "s_waitcnt lgkmcnt(0)\n\t" \
    "s_barrier\n\t" \
    "ds_read_b128 v[120:123], v15 offset:0\n\t" \
    "ds_read_b128 v[124:127], v15 offset:12288\n\t" \
    "ds_read_b128 v[128:131], v15 offset:24576\n\t" \
    "ds_read_b128 v[132:135], v15 offset:36864\n\t" \
    "ds_read_b128 v[136:139], v15 offset:4096\n\t" \
    "ds_read_b128 v[140:143], v15 offset:16384\n\t" \
    "ds_read_b128 v[144:147], v15 offset:28672\n\t" \
    "ds_read_b128 v[148:151], v15 offset:40960\n\t" \
    "ds_read_b128 v[152:155], v15 offset:8192\n\t" \
    "ds_read_b128 v[156:159], v15 offset:20480\n\t" \
    "ds_read_b128 v[160:163], v15 offset:32768\n\t" \
    "ds_read_b128 v[164:167], v15 offset:45056\n\t" \
    "v_xor_b32 v14, 0x10000, v14\n\t" \
    "v_xor_b32 v15, 0x10000, v15\n\t" \
    "s_waitcnt lgkmcnt(10)\n\t" \
    "v_pk_add_f32 v[120:121], v[120:121], v[124:125]\n\t" \
    "v_pk_add_f32 v[122:123], v[122:123], v[126:127]\n\t" \
    "s_waitcnt lgkmcnt(8)\n\t" \
    "v_pk_add_f32 v[128:129], v[128:129], v[132:133]\n\t" \
    "v_pk_add_f32 v[130:131], v[130:131], v[134:135]\n\t" \
    "v_pk_add_f32 v[120:121], v[120:121], v[128:129]\n\t" \
    "v_pk_add_f32 v[122:123], v[122:123], v[130:131]\n\t" \
    "v_cvt_pk_bf16_f32 v120, v120, v121\n\t" \
    "v_cvt_pk_bf16_f32 v121, v122, v123\n\t" \
    "global_store_dwordx2 v16, v[120:121], s[44:45] offset:0\n\t" \
    "s_waitcnt lgkmcnt(6)\n\t" \
    "v_pk_add_f32 v[136:137], v[136:137], v[140:141]\n\t" \
    "v_pk_add_f32 v[138:139], v[138:139], v[142:143]\n\t" \
    "s_waitcnt lgkmcnt(4)\n\t" \
    "v_pk_add_f32 v[144:145], v[144:145], v[148:149]\n\t" \
    "v_pk_add_f32 v[146:147], v[146:147], v[150:151]\n\t" \
    "v_pk_add_f32 v[136:137], v[136:137], v[144:145]\n\t" \
    "v_pk_add_f32 v[138:139], v[138:139], v[146:147]\n\t" \
    "v_cvt_pk_bf16_f32 v136, v136, v137\n\t" \
    "v_cvt_pk_bf16_f32 v137, v138, v139\n\t" \
    "global_store_dwordx2 v16, v[136:137], s[44:45] offset:64\n\t" \
    "s_waitcnt lgkmcnt(2)\n\t" \
    "v_pk_add_f32 v[152:153], v[152:153], v[156:157]\n\t" \
    "v_pk_add_f32 v[154:155], v[154:155], v[158:159]\n\t" \
    "s_waitcnt lgkmcnt(0)\n\t" \
    "v_pk_add_f32 v[160:161], v[160:161], v[164:165]\n\t" \
    "v_pk_add_f32 v[162:163], v[162:163], v[166:167]\n\t" \
    "v_pk_add_f32 v[152:153], v[152:153], v[160:161]\n\t" \
    "v_pk_add_f32 v[154:155], v[154:155], v[162:163]\n\t" \
    "v_cvt_pk_bf16_f32 v152, v152, v153\n\t" \
    "v_cvt_pk_bf16_f32 v153, v154, v155\n\t" \
    "global_store_dwordx2 v16, v[152:153], s[44:45] offset:128\n\t" \
    "s_mov_b32 s65, s96\n\t" \
    "s_cmp_lg_u32 s97, 0\n\t" \
    "s_cbranch_scc1 oo_loop3_%=\n\t" \
    "s_cmp_lt_u32 s65, 58\n\t" \
    "s_cbranch_scc1 oo_poll1_%=\n\t" \
    "s_waitcnt vmcnt(0) lgkmcnt(0)\n\t" \
    "s_barrier\n\t" \
    "oo_last_%=:\n\t" \
    ""
#define ASM_CV \
    "v_mov_b32 v0, %0\n\t" \
    "v_mov_b32 v2, %2\n\t" \
    "v_subrev_u32 v1, 768, %1\n\t" \
    "v_mov_b32 v4, 0\n\t" \
    "v_mov_b32 v5, 0\n\t" \
    "v_mov_b32 v6, 0\n\t" \
    "v_mov_b32 v7, 0\n\t" \
    "v_mov_b32 v8, 0\n\t" \
    "v_mov_b32 v9, 0\n\t" \
    "v_mov_b32 v10, 0\n\t" \
    "v_mov_b32 v11, 0\n\t" \
    "v_mov_b32 v12, 0\n\t" \
    "v_mov_b32 v13, 0\n\t" \
    "v_mov_b32 v14, 0\n\t" \
    "v_mov_b32 v15, 0\n\t" \
    "v_mov_b32 v16, 0\n\t" \
    "v_mov_b32 v17, 0\n\t" \
    "v_mov_b32 v18, 0\n\t" \
    "v_mov_b32 v19, 0\n\t" \
    "v_mov_b32 v20, 0\n\t" \
    "v_mov_b32 v21, 0\n\t" \
    "v_mov_b32 v22, 0\n\t" \
    "v_mov_b32 v23, 0\n\t" \
    "v_mov_b32 v24, 0\n\t" \
    "v_mov_b32 v25, 0\n\t" \
    "v_mov_b32 v26, 0\n\t" \
    "v_mov_b32 v27, 0\n\t" \
    "v_mov_b32 v28, 0\n\t" \
    "v_mov_b32 v29, 0\n\t" \
    "v_mov_b32 v30, 0\n\t" \
    "v_mov_b32 v31, 0\n\t" \
    "v_mov_b32 v32, 0\n\t" \
    "v_mov_b32 v33, 0\n\t" \
    "v_mov_b32 v34, 0\n\t" \
    "v_mov_b32 v35, 0\n\t" \
    "s_waitcnt lgkmcnt(0)\n\t" \
    "ds_read_b64 v[36:37], v0 offset:0\n\t" \
    "ds_read_b64 v[38:39], v0 offset:8\n\t" \
    "ds_read_b128 v[44:47], v1 offset:768\n\t" \
    "ds_read_b64 v[40:41], v0 offset:32\n\t" \
    "ds_read_b64 v[42:43], v0 offset:40\n\t" \
    "ds_read_b128 v[48:51], v1 offset:2304\n\t" \
    "ds_read_b128 v[52:55], v1 offset:3840\n\t" \
    "ds_read_b128 v[56:59], v1 offset:5376\n\t" \
    "s_mov_b32 s69, 4\n\t" \
    "cv_c1_%=:\n\t" \
    "s_waitcnt lgkmcnt(5)\n\t" \
    "v_mfma_f32_16x16x32_f16 v[4:7], v[36:39], v[44:47], v[4:7]\n\t" \
    "ds_read_b64 v[60:61], v0 offset:64\n\t" \
    "s_waitcnt lgkmcnt(4)\n\t" \
    "v_mfma_f32_16x16x32_f16 v[20:23], v[40:43], v[44:47], v[20:23]\n\t" \
    "ds_read_b64 v[62:63], v0 offset:72\n\t" \
    "s_waitcnt lgkmcnt(4)\n\t" \
    "v_mfma_f32_16x16x32_f16 v[8:11], v[36:39], v[48:51], v[8:11]\n\t" \
    "ds_read_b128 v[68:71], v1 offset:672\n\t" \
    "v_mfma_f32_16x16x32_f16 v[24:27], v[40:43], v[48:51], v[24:27]\n\t" \
    "ds_read_b64 v[64:65], v0 offset:96\n\t" \
    "s_waitcnt lgkmcnt(5)\n\t" \
    "v_mfma_f32_16x16x32_f16 v[12:15], v[36:39], v[52:55], v[12:15]\n\t" \
    "ds_read_b64 v[66:67], v0 offset:104\n\t" \
    "v_mfma_f32_16x16x32_f16 v[28:31], v[40:43], v[52:55], v[28:31]\n\t" \
    "ds_read_b128 v[72:75], v1 offset:2208\n\t" \
    "s_waitcnt lgkmcnt(6)\n\t" \
    "v_mfma_f32_16x16x32_f16 v[16:19], v[36:39], v[56:59], v[16:19]\n\t" \
    "ds_read_b128 v[76:79], v1 offset:3744\n\t" \
    "v_mfma_f32_16x16x32_f16 v[32:35], v[40:43], v[56:59], v[32:35]\n\t" \
    "ds_read_b128 v[80:83], v1 offset:5280\n\t" \
    "s_waitcnt lgkmcnt(5)\n\t" \
    "v_mfma_f32_16x16x32_f16 v[4:7], v[60:63], v[68:71], v[4:7]\n\t" \
    "ds_read_b64 v[84:85], v0 offset:128\n\t" \
    "s_waitcnt lgkmcnt(4)\n\t" \
    "v_mfma_f32_16x16x32_f16 v[20:23], v[64:67], v[68:71], v[20:23]\n\t" \
    "ds_read_b64 v[86:87], v0 offset:136\n\t" \
    "s_waitcnt lgkmcnt(4)\n\t" \
    "v_mfma_f32_16x16x32_f16 v[8:11], v[60:63], v[72:75], v[8:11]\n\t" \
    "ds_read_b128 v[92:95], v1 offset:576\n\t" \
    "v_mfma_f32_16x16x32_f16 v[24:27], v[64:67], v[72:75], v[24:27]\n\t" \
    "ds_read_b64 v[88:89], v0 offset:160\n\t" \
    "s_waitcnt lgkmcnt(5)\n\t" \
    "v_mfma_f32_16x16x32_f16 v[12:15], v[60:63], v[76:79], v[12:15]\n\t" \
    "ds_read_b64 v[90:91], v0 offset:168\n\t" \
    "v_mfma_f32_16x16x32_f16 v[28:31], v[64:67], v[76:79], v[28:31]\n\t" \
    "ds_read_b128 v[96:99], v1 offset:2112\n\t" \
    "s_waitcnt lgkmcnt(6)\n\t" \
    "v_mfma_f32_16x16x32_f16 v[16:19], v[60:63], v[80:83], v[16:19]\n\t" \
    "ds_read_b128 v[100:103], v1 offset:3648\n\t" \
    "v_mfma_f32_16x16x32_f16 v[32:35], v[64:67], v[80:83], v[32:35]\n\t" \
    "ds_read_b128 v[104:107], v1 offset:5184\n\t" \
    "s_waitcnt lgkmcnt(5)\n\t" \
    "v_mfma_f32_16x16x32_f16 v[4:7], v[84:87], v[92:95], v[4:7]\n\t" \
    "ds_read_b64 v[108:109], v0 offset:192\n\t" \
    "s_waitcnt lgkmcnt(4)\n\t" \
    "v_mfma_f32_16x16x32_f16 v[20:23], v[88:91], v[92:95], v[20:23]\n\t" \
    "ds_read_b64 v[110:111], v0 offset:200\n\t" \
    "s_waitcnt lgkmcnt(4)\n\t" \
    "v_mfma_f32_16x16x32_f16 v[8:11], v[84:87], v[96:99], v[8:11]\n\t" \
    "ds_read_b128 v[116:119], v1 offset:480\n\t" \
    "v_mfma_f32_16x16x32_f16 v[24:27], v[88:91], v[96:99], v[24:27]\n\t" \
    "ds_read_b64 v[112:113], v0 offset:224\n\t" \
    "s_waitcnt lgkmcnt(5)\n\t" \
    "v_mfma_f32_16x16x32_f16 v[12:15], v[84:87], v[100:103], v[12:15]\n\t" \
    "ds_read_b64 v[114:115], v0 offset:232\n\t" \
    "v_mfma_f32_16x16x32_f16 v[28:31], v[88:91], v[100:103], v[28:31]\n\t" \
    "ds_read_b128 v[120:123], v1 offset:2016\n\t" \
    "s_waitcnt lgkmcnt(6)\n\t" \
    "v_mfma_f32_16x16x32_f16 v[16:19], v[84:87], v[104:107], v[16:19]\n\t" \
    "ds_read_b128 v[124:127], v1 offset:3552\n\t" \
    "v_mfma_f32_16x16x32_f16 v[32:35], v[88:91], v[104:107], v[32:35]\n\t" \
    "ds_read_b128 v[128:131], v1 offset:5088\n\t" \
    "s_waitcnt lgkmcnt(5)\n\t" \
    "v_mfma_f32_16x16x32_f16 v[4:7], v[108:111], v[116:119], v[4:7]\n\t" \
    "ds_read_b64 v[36:37], v0 offset:256\n\t" \
    "s_waitcnt lgkmcnt(4)\n\t" \
    "v_mfma_f32_16x16x32_f16 v[20:23], v[112:115], v[116:119], v[20:23]\n\t" \
    "ds_read_b64 v[38:39], v0 offset:264\n\t" \
    "s_waitcnt lgkmcnt(4)\n\t" \
    "v_mfma_f32_16x16x32_f16 v[8:11], v[108:111], v[120:123], v[8:11]\n\t" \
    "ds_read_b128 v[44:47], v1 offset:384\n\t" \
    "v_mfma_f32_16x16x32_f16 v[24:27], v[112:115], v[120:123], v[24:27]\n\t" \
    "ds_read_b64 v[40:41], v0 offset:288\n\t" \
    "s_waitcnt lgkmcnt(5)\n\t" \
    "v_mfma_f32_16x16x32_f16 v[12:15], v[108:111], v[124:127], v[12:15]\n\t" \
    "ds_read_b64 v[42:43], v0 offset:296\n\t" \
    "v_mfma_f32_16x16x32_f16 v[28:31], v[112:115], v[124:127], v[28:31]\n\t" \
    "ds_read_b128 v[48:51], v1 offset:1920\n\t" \
    "s_waitcnt lgkmcnt(6)\n\t" \
    "v_mfma_f32_16x16x32_f16 v[16:19], v[108:111], v[128:131], v[16:19]\n\t" \
    "ds_read_b128 v[52:55], v1 offset:3456\n\t" \
    "v_mfma_f32_16x16x32_f16 v[32:35], v[112:115], v[128:131], v[32:35]\n\t" \
    "ds_read_b128 v[56:59], v1 offset:4992\n\t" \
    "v_add_u32 v0, 256, v0\n\t" \
    "v_subrev_u32 v1, 384, v1\n\t" \
    "s_sub_u32 s69, s69, 1\n\t" \
    "s_cmp_lg_u32 s69, 0\n\t" \
    "s_cbranch_scc1 cv_c1_%=\n\t" \
    "s_waitcnt lgkmcnt(0)\n\t" \
    "ds_read_b64 v[36:37], v0 offset:0\n\t" \
    "ds_read_b64 v[38:39], v0 offset:8\n\t" \
    "ds_read_b128 v[48:51], v1 offset:2304\n\t" \
    "ds_read_b64 v[40:41], v0 offset:32\n\t" \
    "ds_read_b64 v[42:43], v0 offset:40\n\t" \
    "ds_read_b128 v[52:55], v1 offset:3840\n\t" \
    "ds_read_b128 v[56:59], v1 offset:5376\n\t" \
    "ds_read_b64 v[60:61], v0 offset:64\n\t" \
    "ds_read_b64 v[62:63], v0 offset:72\n\t" \
    "ds_read_b128 v[72:75], v1 offset:2208\n\t" \
    "ds_read_b64 v[64:65], v0 offset:96\n\t" \
    "ds_read_b64 v[66:67], v0 offset:104\n\t" \
    "ds_read_b128 v[76:79], v1 offset:3744\n\t" \
    "ds_read_b128 v[80:83], v1 offset:5280\n\t" \
    "s_mov_b32 s69, 4\n\t" \
    "cv_c2_%=:\n\t" \
    "s_waitcnt lgkmcnt(11)\n\t" \
    "v_mfma_f32_16x16x32_f16 v[8:11], v[36:39], v[48:51], v[8:11]\n\t" \
    "ds_read_b64 v[84:85], v0 offset:128\n\t" \
    "ds_read_b64 v[86:87], v0 offset:136\n\t" \
    "s_waitcnt lgkmcnt(11)\n\t" \
    "v_mfma_f32_16x16x32_f16 v[24:27], v[40:43], v[48:51], v[24:27]\n\t" \
    "ds_read_b128 v[96:99], v1 offset:2112\n\t" \
    "ds_read_b64 v[88:89], v0 offset:160\n\t" \
    "s_waitcnt lgkmcnt(12)\n\t" \
    "v_mfma_f32_16x16x32_f16 v[12:15], v[36:39], v[52:55], v[12:15]\n\t" \
    "ds_read_b64 v[90:91], v0 offset:168\n\t" \
    "ds_read_b128 v[100:103], v1 offset:3648\n\t" \
    "v_mfma_f32_16x16x32_f16 v[28:31], v[40:43], v[52:55], v[28:31]\n\t" \
    "ds_read_b128 v[104:107], v1 offset:5184\n\t" \
    "s_waitcnt lgkmcnt(14)\n\t" \
    "v_mfma_f32_16x16x32_f16 v[16:19], v[36:39], v[56:59], v[16:19]\n\t" \
    "v_mfma_f32_16x16x32_f16 v[32:35], v[40:43], v[56:59], v[32:35]\n\t" \
    "s_waitcnt lgkmcnt(11)\n\t" \
    "v_mfma_f32_16x16x32_f16 v[8:11], v[60:63], v[72:75], v[8:11]\n\t" \
    "ds_read_b64 v[108:109], v0 offset:192\n\t" \
    "ds_read_b64 v[110:111], v0 offset:200\n\t" \
    "s_waitcnt lgkmcnt(11)\n\t" \
    "v_mfma_f32_16x16x32_f16 v[24:27], v[64:67], v[72:75], v[24:27]\n\t" \
    "ds_read_b128 v[120:123], v1 offset:2016\n\t" \
    "ds_read_b64 v[112:113], v0 offset:224\n\t" \
    "s_waitcnt lgkmcnt(12)\n\t" \
    "v_mfma_f32_16x16x32_f16 v[12:15], v[60:63], v[76:79], v[12:15]\n\t" \
    "ds_read_b64 v[114:115], v0 offset:232\n\t" \
    "ds_read_b128 v[124:127], v1 offset:3552\n\t" \
    "v_mfma_f32_16x16x32_f16 v[28:31], v[64:67], v[76:79], v[28:31]\n\t" \
    "ds_read_b128 v[128:131], v1 offset:5088\n\t" \
    "s_waitcnt lgkmcnt(14)\n\t" \
    "v_mfma_f32_16x16x32_f16 v[16:19], v[60:63], v[80:83], v[16:19]\n\t" \
    "v_mfma_f32_16x16x32_f16 v[32:35], v[64:67], v[80:83], v[32:35]\n\t" \
    "s_waitcnt lgkmcnt(11)\n\t" \
    "v_mfma_f32_16x16x32_f16 v[8:11], v[84:87], v[96:99], v[8:11]\n\t" \
    "ds_read_b64 v[36:37], v0 offset:256\n\t" \
    "ds_read_b64 v[38:39], v0 offset:264\n\t" \
    "s_waitcnt lgkmcnt(11)\n\t" \
    "v_mfma_f32_16x16x32_f16 v[24:27], v[88:91], v[96:99], v[24:27]\n\t" \
    "ds_read_b128 v[48:51], v1 offset:1920\n\t" \
    "ds_read_b64 v[40:41], v0 offset:288\n\t" \
    "s_waitcnt lgkmcnt(12)\n\t" \
    "v_mfma_f32_16x16x32_f16 v[12:15], v[84:87], v[100:103], v[12:15]\n\t" \
    "ds_read_b64 v[42:43], v0 offset:296\n\t" \
    "ds_read_b128 v[52:55], v1 offset:3456\n\t" \
    "v_mfma_f32_16x16x32_f16 v[28:31], v[88:91], v[100:103], v[28:31]\n\t" \
    "ds_read_b128 v[56:59], v1 offset:4992\n\t" \
    "s_waitcnt lgkmcnt(14)\n\t" \
    "v_mfma_f32_16x16x32_f16 v[16:19], v[84:87], v[104:107], v[16:19]\n\t" \
    "v_mfma_f32_16x16x32_f16 v[32:35], v[88:91], v[104:107], v[32:35]\n\t" \
    "s_waitcnt lgkmcnt(11)\n\t" \
    "v_mfma_f32_16x16x32_f16 v[8:11], v[108:111], v[120:123], v[8:11]\n\t" \
    "ds_read_b64 v[60:61], v0 offset:320\n\t" \
    "ds_read_b64 v[62:63], v0 offset:328\n\t" \
    "s_waitcnt lgkmcnt(11)\n\t" \
    "v_mfma_f32_16x16x32_f16 v[24:27], v[112:115], v[120:123], v[24:27]\n\t" \
    "ds_read_b128 v[72:75], v1 offset:1824\n\t" \
    "ds_read_b64 v[64:65], v0 offset:352\n\t" \
    "s_waitcnt lgkmcnt(12)\n\t" \
    "v_mfma_f32_16x16x32_f16 v[12:15], v[108:111], v[124:127], v[12:15]\n\t" \
    "ds_read_b64 v[66:67], v0 offset:360\n\t" \
    "ds_read_b128 v[76:79], v1 offset:3360\n\t" \
    "v_mfma_f32_16x16x32_f16 v[28:31], v[112:115], v[124:127], v[28:31]\n\t" \
    "ds_read_b128 v[80:83], v1 offset:4896\n\t" \
    "s_waitcnt lgkmcnt(14)\n\t" \
    "v_mfma_f32_16x16x32_f16 v[16:19], v[108:111], v[128:131], v[16:19]\n\t" \
    "v_mfma_f32_16x16x32_f16 v[32:35], v[112:115], v[128:131], v[32:35]\n\t" \
    "v_add_u32 v0, 256, v0\n\t" \
    "v_subrev_u32 v1, 384, v1\n\t" \
    "s_sub_u32 s69, s69, 1\n\t" \
    "s_cmp_lg_u32 s69, 0\n\t" \
    "s_cbranch_scc1 cv_c2_%=\n\t" \
    "s_waitcnt lgkmcnt(0)\n\t" \
    "ds_read_b64 v[36:37], v0 offset:0\n\t" \
    "ds_read_b64 v[38:39], v0 offset:8\n\t" \
    "ds_read_b128 v[52:55], v1 offset:3840\n\t" \
    "ds_read_b64 v[40:41], v0 offset:32\n\t" \
    "ds_read_b64 v[42:43], v0 offset:40\n\t" \
    "ds_read_b128 v[56:59], v1 offset:5376\n\t" \
    "ds_read_b64 v[60:61], v0 offset:64\n\t" \
    "ds_read_b64 v[62:63], v0 offset:72\n\t" \
    "ds_read_b128 v[76:79], v1 offset:3744\n\t" \
    "ds_read_b64 v[64:65], v0 offset:96\n\t" \
    "ds_read_b64 v[66:67], v0 offset:104\n\t" \
    "ds_read_b128 v[80:83], v1 offset:5280\n\t" \
    "s_mov_b32 s69, 4\n\t" \
    "cv_c3_%=:\n\t" \
    "s_waitcnt lgkmcnt(9)\n\t" \
    "v_mfma_f32_16x16x32_f16 v[12:15], v[36:39], v[52:55], v[12:15]\n\t" \
    "ds_read_b64 v[84:85], v0 offset:128\n\t" \
    "ds_read_b64 v[86:87], v0 offset:136\n\t" \
    "s_waitcnt lgkmcnt(9)\n\t" \
    "v_mfma_f32_16x16x32_f16 v[28:31], v[40:43], v[52:55], v[28:31]\n\t" \
    "ds_read_b128 v[100:103], v1 offset:3648\n\t" \
    "ds_read_b64 v[88:89], v0 offset:160\n\t" \
    "s_waitcnt lgkmcnt(10)\n\t" \
    "v_mfma_f32_16x16x32_f16 v[16:19], v[36:39], v[56:59], v[16:19]\n\t" \
    "ds_read_b64 v[90:91], v0 offset:168\n\t" \
    "ds_read_b128 v[104:107], v1 offset:5184\n\t" \
    "v_mfma_f32_16x16x32_f16 v[32:35], v[40:43], v[56:59], v[32:35]\n\t" \
    "s_waitcnt lgkmcnt(9)\n\t" \
    "v_mfma_f32_16x16x32_f16 v[12:15], v[60:63], v[76:79], v[12:15]\n\t" \
    "ds_read_b64 v[108:109], v0 offset:192\n\t" \
    "ds_read_b64 v[110:111], v0 offset:200\n\t" \
    "s_waitcnt lgkmcnt(9)\n\t" \
    "v_mfma_f32_16x16x32_f16 v[28:31], v[64:67], v[76:79], v[28:31]\n\t" \
    "ds_read_b128 v[124:127], v1 offset:3552\n\t" \
    "ds_read_b64 v[112:113], v0 offset:224\n\t" \
    "s_waitcnt lgkmcnt(10)\n\t" \
    "v_mfma_f32_16x16x32_f16 v[16:19], v[60:63], v[80:83], v[16:19]\n\t" \
    "ds_read_b64 v[114:115], v0 offset:232\n\t" \
    "ds_read_b128 v[128:131], v1 offset:5088\n\t" \
    "v_mfma_f32_16x16x32_f16 v[32:35], v[64:67], v[80:83], v[32:35]\n\t" \
    "s_waitcnt lgkmcnt(9)\n\t" \
    "v_mfma_f32_16x16x32_f16 v[12:15], v[84:87], v[100:103], v[12:15]\n\t" \
    "ds_read_b64 v[36:37], v0 offset:256\n\t" \
    "ds_read_b64 v[38:39], v0 offset:264\n\t" \
    "s_waitcnt lgkmcnt(9)\n\t" \
    "v_mfma_f32_16x16x32_f16 v[28:31], v[88:91], v[100:103], v[28:31]\n\t" \
    "ds_read_b128 v[52:55], v1 offset:3456\n\t" \
    "ds_read_b64 v[40:41], v0 offset:288\n\t" \
    "s_waitcnt lgkmcnt(10)\n\t" \
    "v_mfma_f32_16x16x32_f16 v[16:19], v[84:87], v[104:107], v[16:19]\n\t" \
    "ds_read_b64 v[42:43], v0 offset:296\n\t" \
    "ds_read_b128 v[56:59], v1 offset:4992\n\t" \
    "v_mfma_f32_16x16x32_f16 v[32:35], v[88:91], v[104:107], v[32:35]\n\t" \
    "s_waitcnt lgkmcnt(9)\n\t" \
    "v_mfma_f32_16x16x32_f16 v[12:15], v[108:111], v[124:127], v[12:15]\n\t" \
    "ds_read_b64 v[60:61], v0 offset:320\n\t" \
    "ds_read_b64 v[62:63], v0 offset:328\n\t" \
    "s_waitcnt lgkmcnt(9)\n\t" \
    "v_mfma_f32_16x16x32_f16 v[28:31], v[112:115], v[124:127], v[28:31]\n\t" \
    "ds_read_b128 v[76:79], v1 offset:3360\n\t" \
    "ds_read_b64 v[64:65], v0 offset:352\n\t" \
    "s_waitcnt lgkmcnt(10)\n\t" \
    "v_mfma_f32_16x16x32_f16 v[16:19], v[108:111], v[128:131], v[16:19]\n\t" \
    "ds_read_b64 v[66:67], v0 offset:360\n\t" \
    "ds_read_b128 v[80:83], v1 offset:4896\n\t" \
    "v_mfma_f32_16x16x32_f16 v[32:35], v[112:115], v[128:131], v[32:35]\n\t" \
    "v_add_u32 v0, 256, v0\n\t" \
    "v_subrev_u32 v1, 384, v1\n\t" \
    "s_sub_u32 s69, s69, 1\n\t" \
    "s_cmp_lg_u32 s69, 0\n\t" \
    "s_cbranch_scc1 cv_c3_%=\n\t" \
    "s_waitcnt lgkmcnt(0)\n\t" \
    "ds_read_b64 v[36:37], v0 offset:0\n\t" \
    "ds_read_b64 v[38:39], v0 offset:8\n\t" \
    "ds_read_b128 v[56:59], v1 offset:5376\n\t" \
    "ds_read_b64 v[40:41], v0 offset:32\n\t" \
    "ds_read_b64 v[42:43], v0 offset:40\n\t" \
    "ds_read_b64 v[60:61], v0 offset:64\n\t" \
    "ds_read_b64 v[62:63], v0 offset:72\n\t" \
    "ds_read_b128 v[80:83], v1 offset:5280\n\t" \
    "ds_read_b64 v[64:65], v0 offset:96\n\t" \
    "ds_read_b64 v[66:67], v0 offset:104\n\t" \
    "ds_read_b64 v[84:85], v0 offset:128\n\t" \
    "ds_read_b64 v[86:87], v0 offset:136\n\t" \
    "ds_read_b128 v[104:107], v1 offset:5184\n\t" \
    "ds_read_b64 v[88:89], v0 offset:160\n\t" \
    "ds_read_b64 v[90:91], v0 offset:168\n\t" \
    "s_mov_b32 s69, 4\n\t" \
    "cv_c4_%=:\n\t" \
    "s_waitcnt lgkmcnt(12)\n\t" \
    "v_mfma_f32_16x16x32_f16 v[16:19], v[36:39], v[56:59], v[16:19]\n\t" \
    "ds_read_b64 v[108:109], v0 offset:192\n\t" \
    "ds_read_b64 v[110:111], v0 offset:200\n\t" \
    "ds_read_b128 v[128:131], v1 offset:5088\n\t" \
    "s_waitcnt lgkmcnt(13)\n\t" \
    "v_mfma_f32_16x16x32_f16 v[32:35], v[40:43], v[56:59], v[32:35]\n\t" \
    "ds_read_b64 v[112:113], v0 offset:224\n\t" \
    "ds_read_b64 v[114:115], v0 offset:232\n\t" \
    "s_waitcnt lgkmcnt(12)\n\t" \
    "v_mfma_f32_16x16x32_f16 v[16:19], v[60:63], v[80:83], v[16:19]\n\t" \
    "ds_read_b64 v[36:37], v0 offset:256\n\t" \
    "ds_read_b64 v[38:39], v0 offset:264\n\t" \
    "ds_read_b128 v[56:59], v1 offset:4992\n\t" \
    "s_waitcnt lgkmcnt(13)\n\t" \
    "v_mfma_f32_16x16x32_f16 v[32:35], v[64:67], v[80:83], v[32:35]\n\t" \
    "ds_read_b64 v[40:41], v0 offset:288\n\t" \
    "ds_read_b64 v[42:43], v0 offset:296\n\t" \
    "s_waitcnt lgkmcnt(12)\n\t" \
    "v_mfma_f32_16x16x32_f16 v[16:19], v[84:87], v[104:107], v[16:19]\n\t" \
    "ds_read_b64 v[60:61], v0 offset:320\n\t" \
    "ds_read_b64 v[62:63], v0 offset:328\n\t" \
    "ds_read_b128 v[80:83], v1 offset:4896\n\t" \
    "s_waitcnt lgkmcnt(13)\n\t" \
    "v_mfma_f32_16x16x32_f16 v[32:35], v[88:91], v[104:107], v[32:35]\n\t" \
    "ds_read_b64 v[64:65], v0 offset:352\n\t" \
    "ds_read_b64 v[66:67], v0 offset:360\n\t" \
    "s_waitcnt lgkmcnt(12)\n\t" \
    "v_mfma_f32_16x16x32_f16 v[16:19], v[108:111], v[128:131], v[16:19]\n\t" \
    "ds_read_b64 v[84:85], v0 offset:384\n\t" \
    "ds_read_b64 v[86:87], v0 offset:392\n\t" \
    "ds_read_b128 v[104:107], v1 offset:4800\n\t" \
    "s_waitcnt lgkmcnt(13)\n\t" \
    "v_mfma_f32_16x16x32_f16 v[32:35], v[112:115], v[128:131], v[32:35]\n\t" \
    "ds_read_b64 v[88:89], v0 offset:416\n\t" \
    "ds_read_b64 v[90:91], v0 offset:424\n\t" \
    "v_add_u32 v0, 256, v0\n\t" \
    "v_subrev_u32 v1, 384, v1\n\t" \
    "s_sub_u32 s69, s69, 1\n\t" \
    "s_cmp_lg_u32 s69, 0\n\t" \
    "s_cbranch_scc1 cv_c4_%=\n\t" \
    "s_waitcnt lgkmcnt(0)\n\t" \
    "s_nop 7\n\t" \
    "ds_write_b128 v2, v[4:7] offset:0\n\t" \
    "ds_write_b128 v2, v[8:11] offset:1024\n\t" \
    "ds_write_b128 v2, v[12:15] offset:2048\n\t" \
    "ds_write_b128 v2, v[16:19] offset:3072\n\t" \
    "ds_write_b128 v2, v[20:23] offset:4096\n\t" \
    "ds_write_b128 v2, v[24:27] offset:5120\n\t" \
    "ds_write_b128 v2, v[28:31] offset:6144\n\t" \
    "ds_write_b128 v2, v[32:35] offset:7168\n\t" \
    "s_waitcnt lgkmcnt(0)\n\t" \
    "cv_last_%=:\n\t" \
    ""
#define ASM_CLOB "v0", "v1", "v2", "v3", "v4", "v5", "v6", "v7", "v8", "v9", "v10", "v11", "v12", "v13", "v14", "v15", "v16", "v17", "v18", "v19", "v20", "v21", "v22", "v23", "v24", "v25", "v26", "v27", "v28", "v29", "v30", "v31", "v32", "v33", "v34", "v35", "v36", "v37", "v38", "v39", "v40", "v41", "v42", "v43", "v44", "v45", "v46", "v47", "v48", "v49", "v50", "v51", "v52", "v53", "v54", "v55", "v56", "v57", "v58", "v59", "v60", "v61", "v62", "v63", "v64", "v65", "v66", "v67", "v68", "v69", "v70", "v71", "v72", "v73", "v74", "v75", "v76", "v77", "v78", "v79", "v80", "v81", "v82", "v83", "v84", "v85", "v86", "v87", "v88", "v89", "v90", "v91", "v92", "v93", "v94", "v95", "v96", "v97", "v98", "v99", "v100", "v101", "v102", "v103", "v104", "v105", "v106", "v107", "v108", "v109", "v110", "v111", "v112", "v113", "v114", "v115", "v116", "v117", "v118", "v119", "v120", "v121", "v122", "v123", "v124", "v125", "v126", "v127", "v128", "v129", "v130", "v131", "v132", "v133", "v134", "v135", "v136", "v137", "v138", "v139", "v140", "v141", "v142", "v143", "v144", "v145", "v146", "v147", "v148", "v149", "v150", "v151", "v152", "v153", "v154", "v155", "v156", "v157", "v158", "v159", "v160", "v161", "v162", "v163", "v164", "v165", "v166", "v167", "v168", "v169", "v170", "v171", "v172", "v173", "v174", "v175", "v176", "v177", "v178", "v179", "v180", "v181", "v182", "v183", "v184", "v185", "v186", "v187", "v188", "v189", "v190", "v191", "v192", "v193", "v194", "v195", "v196", "v197", "v198", "v199", "v200", "v201", "v202", "v203", "v204", "v205", "v206", "v207", "v208", "v209", "v210", "v211", "v212", "v213", "v214", "v215", "v222", "v223", "v224", "v225", "v226", "v227", "v228", "v229", "v230", "v231", "v232", "v233", "v234", "v235", "v236", "v237", "v238", "v239", "v240", "v241", "v242", "v243", "v244", "v245", "v246", "v247", "v248", "v249", "v250", "v251", "v252", "v253", "v254", "v255", "a0", "a1", "a2", "a3", "a4", "a5", "a6", "a7", "a8", "a9", "a10", "a11", "a12", "a13", "a14", "a15", "a16", "a17", "a18", "a19", "a20", "a21", "a22", "a23", "a24", "a25", "a26", "a27", "a28", "a29", "a30", "a31", "a32", "a33", "a34", "a35", "a36", "a37", "a38", "a39", "a40", "a41", "a42", "a43", "a44", "a45", "a46", "a47", "a48", "a49", "a50", "a51", "a52", "a53", "a54", "a55", "a56", "a57", "a58", "a59", "a60", "a61", "a62", "a63", "a64", "a65", "a66", "a67", "a68", "a69", "a70", "a71", "a72", "a73", "a74", "a75", "a76", "a77", "a78", "a79", "a80", "a81", "a82", "a83", "a84", "a85", "a86", "a87", "a88", "a89", "a90", "a91", "a92", "a93", "a94", "a95", "a96", "a97", "a98", "a99", "a100", "a101", "a102", "a103", "a104", "a105", "a106", "a107", "a108", "a109", "a110", "a111", "a112", "a113", "a114", "a115", "a116", "a117", "a118", "a119", "a120", "a121", "a122", "a123", "a124", "a125", "a126", "a127", "a128", "a129", "a130", "a131", "a132", "a133", "a134", "a135", "a136", "a137", "a138", "a139", "a140", "a141", "a142", "a143", "a144", "a145", "a146", "a147", "a148", "a149", "a150", "a151", "a152", "a153", "a154", "a155", "a156", "a157", "a158", "a159", "a160", "a161", "a162", "a163", "a164", "a165", "a166", "a167", "a168", "a169", "a170", "a171", "a172", "a173", "a174", "a175", "a176", "a177", "a178", "a179", "a180", "a181", "a182", "a183", "a184", "a185", "a186", "a187", "a188", "a189", "a190", "a191", "a192", "a193", "a194", "a195", "a196", "a197", "a198", "a199", "a200", "a201", "a202", "a203", "a204", "a205", "a206", "a207", "a208", "a209", "a210", "a211", "a212", "a213", "a214", "a215", "a216", "a217", "a218", "a219", "a220", "a221", "a222", "a223", "a224", "a225", "a226", "a227", "a228", "a229", "a230", "a231", "a232", "a233", "a234", "a235", "a236", "a237", "a238", "a239", "a240", "a241", "a242", "a243", "a244", "a245", "a246", "a247", "a248", "a249", "a250", "a251", "a252", "a253", "a254", "a255", "s33", "s34", "s35", "s36", "s37", "s38", "s39", "s40", "s41", "s42", "s43", "s44", "s45", "s46", "s47", "s48", "s49", "s50", "s51", "s52", "s53", "s54", "s55", "s56", "s57", "s58", "s59", "s60", "s61", "s62", "s63", "s64", "s65", "s66", "s67", "s68", "s69", "s70", "s71", "s72", "s73", "s74", "s75", "s76", "s77", "s78", "s79", "s80", "s81", "s82", "s83", "s84", "s85", "s86", "s87", "s88", "s89", "s90", "s91", "s92", "s93", "s94", "s95", "s96", "s97", "s98", "s99", "vcc", "scc", "memory"
#define CV_CLOB "v0", "v1", "v2", "v3", "v4", "v5", "v6", "v7", "v8", "v9", "v10", "v11", "v12", "v13", "v14", "v15", "v16", "v17", "v18", "v19", "v20", "v21", "v22", "v23", "v24", "v25", "v26", "v27", "v28", "v29", "v30", "v31", "v32", "v33", "v34", "v35", "v36", "v37", "v38", "v39", "v40", "v41", "v42", "v43", "v44", "v45", "v46", "v47", "v48", "v49", "v50", "v51", "v52", "v53", "v54", "v55", "v56", "v57", "v58", "v59", "v60", "v61", "v62", "v63", "v64", "v65", "v66", "v67", "v68", "v69", "v70", "v71", "v72", "v73", "v74", "v75", "v76", "v77", "v78", "v79", "v80", "v81", "v82", "v83", "v84", "v85", "v86", "v87", "v88", "v89", "v90", "v91", "v92", "v93", "v94", "v95", "v96", "v97", "v98", "v99", "v100", "v101", "v102", "v103", "v104", "v105", "v106", "v107", "v108", "v109", "v110", "v111", "v112", "v113", "v114", "v115", "v116", "v117", "v118", "v119", "v120", "v121", "v122", "v123", "v124", "v125", "v126", "v127", "v128", "v129", "v130", "v131", "s69", "scc", "memory"
static constexpr unsigned G_STEP = 0x80000;
static constexpr unsigned G_HOFF = 0x40000;
static constexpr unsigned G_RM = 0x4;
static constexpr unsigned G_RP = 0x2;
static constexpr unsigned G_HP = 0x20;
static constexpr unsigned G_HKB = 0x8;
static constexpr unsigned G_RHO = 0x1400;
static constexpr unsigned G_RHS = 0x600;
static constexpr unsigned G_RARM = 0x1;
static constexpr unsigned G_NARM = 0x4;
static constexpr unsigned G_WS_UM = 0x400000;
static constexpr unsigned G_WS_CTL = 0x5d0000;
static constexpr unsigned G_WS_DONE = 0x5f8000;
static constexpr unsigned G_WS_EP = 0x5fa000;
static constexpr unsigned G_WS_STAMPS = 0x10000000;
static constexpr unsigned G_C_TASK = 0x200;
static constexpr unsigned G_C_GX = 0x1000;
static constexpr unsigned G_C_GV = 0x4000;
static constexpr unsigned G_C_KM = 0x8000;
static constexpr unsigned G_C_UM = 0x9000;
static constexpr unsigned G_C_PC = 0xc000;
static constexpr unsigned G_C_UT = 0xd000;
static constexpr unsigned G_C_GK = 0x10000;
static constexpr unsigned G_C_YC = 0x11000;
static constexpr unsigned G_C_ERR = 0x100;
static constexpr unsigned G_C_ARR = 0x14000;
static constexpr unsigned G_C_KG = 0x1c000;
static constexpr unsigned G_C_END = 0x22000;
static constexpr unsigned G_CTF = 0x2000;
static constexpr unsigned G_GVC = 0x2;
static constexpr unsigned G_GVH = 0x14000;
static constexpr unsigned G_HS8 = 0x80000;
static constexpr unsigned G_HS4 = 0x40000;
static constexpr unsigned G_UXS = 0x200000;
static constexpr unsigned G_UMH = 0xa0000;
static constexpr unsigned G_D_QX = 0x600000;
static constexpr unsigned G_D_KX = 0x1600000;
static constexpr unsigned G_D_KM = 0x2600000;
static constexpr unsigned G_D_UX = 0x2e00000;
static constexpr unsigned G_D_UMX = 0x7600000;
static constexpr unsigned G_D_PX = 0x8c00000;
static constexpr unsigned G_D_PM = 0x9000000;
static constexpr unsigned G_D_KT = 0x9100000;
static constexpr unsigned G_D_UT = 0x9600000;
static constexpr unsigned G_D_YB = 0x9900000;
static constexpr unsigned G_D_END = 0x9c00000;
static constexpr unsigned G_SKM = 0x7;
static constexpr unsigned G_GVN = 0x2;
static constexpr unsigned G_STG = 0x9000;
static constexpr unsigned G_NA_ = 0x22;
static constexpr unsigned G_KROW = 0x30;
#define FLG(x_) (kDefault[5])
#define FLG2(x_) (kDefault[7])
static constexpr int kDefault[8] = {PRM_DEFAULT};
#define MFMA32(a_, b_, c_) __builtin_amdgcn_mfma_f32_16x16x32_bf16(__builtin_bit_cast(bf16x8, a_), __builtin_bit_cast(bf16x8, b_), c_, 0, 0, 0)
#define MFMA16(a_, b_, c_) __builtin_amdgcn_mfma_f32_16x16x16bf16_1k(__builtin_bit_cast(s16x4, a_), __builtin_bit_cast(s16x4, b_), c_, 0, 0, 0)
#define WLD8(d_, voff_, base_, off_) asm volatile("s_nop 4\n\tglobal_load_dwordx2 %0, %1, %2 offset:%3 sc1" : "=v"(d_) : "v"(voff_), "s"(base_), "i"(off_) : "memory")
#define WLD16(d_, voff_, base_, off_) asm volatile("s_nop 4\n\tglobal_load_dwordx4 %0, %1, %2 offset:%3 sc1" : "=v"(d_) : "v"(voff_), "s"(base_), "i"(off_) : "memory")
#define WST16(voff_, d_, base_, off_) asm volatile("s_nop 4\n\tglobal_store_dwordx4 %0, %1, %2 offset:%3 sc1\n\ts_nop 1" :: "v"(voff_), "v"(d_), "s"(base_), "i"(off_) : "memory")
#define WLD16X4(d0, d1, d2, d3, voff_, base_) asm volatile("s_nop 4\n\tglobal_load_dwordx4 %0, %4, %5 sc1\n\tglobal_load_dwordx4 %1, %4, %5 offset:1024 sc1\n\t" \
    "global_load_dwordx4 %2, %4, %5 offset:2048 sc1\n\tglobal_load_dwordx4 %3, %4, %5 offset:3072 sc1" \
    : "=&v"(d0), "=&v"(d1), "=&v"(d2), "=&v"(d3) : "v"(voff_), "s"(base_) : "memory")

__device__ __forceinline__ u32x4 pack8(const f32x4& a, const f32x4& b) {
    return u32x4{pk2bf(a[0], a[1]), pk2bf(a[2], a[3]), pk2bf(b[0], b[1]), pk2bf(b[2], b[3])};
}
template <class T> __device__ __forceinline__ T* uni(T* q) {
    const u64 v = (u64)q;
    unsigned lo = __builtin_amdgcn_readfirstlane((unsigned)v), hi = __builtin_amdgcn_readfirstlane((unsigned)(v >> 32));
    asm volatile("s_nop 4" : "+s"(lo), "+s"(hi));
    return (T*)(((u64)hi << 32) | lo);
}
#define CTLO (G_WS_CTL + (unsigned)p.prm[0])
template <bool FAST = false>
__device__ __forceinline__ void wait_word(const MainP& p, const char* f, int want, unsigned code) {
    unsigned tries = 0;
    while (__any((int)__hip_atomic_load((const gu32*)f, RLX_AGENT) < want)) {
        if (++tries > kWaitCap) { __hip_atomic_store((gu32*)(p.ws + G_WS_CTL + G_C_ERR), code, RLX_AGENT); break; }
        if ((tries & 1023u) == 0u && __hip_atomic_load((const gu32*)(p.ws + G_WS_CTL + G_C_ERR), RLX_AGENT) != 0xffffffffu) break;
        if (FAST) __builtin_amdgcn_s_sleep(1); else __builtin_amdgcn_s_sleep(10);
    }
}

__device__ __forceinline__ int skew(int s, int id) { return (s ^ (G_SKM * id)) & 63; }
__device__ __forceinline__ const char* qx_ptr(const MainP& p, int s, int h) {
    return p.ws + G_D_QX + (size_t)h * G_HS8 + skew(s, h) * 8192;
}
__device__ __forceinline__ unsigned umx_base(int h) { return G_D_UMX + (h / 3) * 0x200000 + (h % 3) * G_UMH; }
typedef GAS unsigned long long gu64;
__device__ __forceinline__ void init_done(char* ws, int slot, unsigned val) {
    const unsigned e = __hip_atomic_load((const gu32*)(ws + G_WS_EP), RLX_AGENT);
    __hip_atomic_store((gu64*)(ws + G_WS_DONE + slot * 64), ((u64)val << 32) | (e + 1u), RLX_AGENT);
}
__device__ __forceinline__ void init_seen(char* ws, int lane, unsigned e) {
    for (unsigned tries = 0; tries < kWaitCap; ++tries) {
        if (!__any(__hip_atomic_load((const gu32*)(ws + G_WS_DONE + min(lane, 59) * 64), RLX_AGENT) != e + 1u)) break;
        __builtin_amdgcn_s_sleep(8);
    }
}
__device__ __forceinline__ void init_count(char* ws, int lane) { if (lane == 0) __hip_atomic_fetch_add((gu32*)(ws + G_WS_CTL + G_C_ARR), 1u, RLX_AGENT); }
__device__ __forceinline__ void fill_block(char* ws, int b, int tid, const u32x4& v0, const u32x4& v1) {
    const unsigned o = b * 8192 + tid * 16, o2 = o + 4096;
    asm volatile("global_store_dwordx4 %0, %1, %2 sc1\n\ts_nop 1" :: "v"(o), "v"(v0), "s"(ws) : "memory");
    asm volatile("global_store_dwordx4 %0, %1, %2 sc1\n\ts_nop 1" :: "v"(o2), "v"(v1), "s"(ws) : "memory");
}
__device__ __forceinline__ void init_fill(char* ws, const u16* mem0, int f, int tid) {
    const u32x4 sent = {0xffffffffu, 0xffffffffu, 0xffffffffu, 0xffffffffu};
    u32x4 v0 = sent, v1 = sent;
    if (f < 16) {
#pragma unroll
        for (int k = 0; k < 2; ++k) {
            const int q = tid + 256 * k + ((f & 2) ? 256 : 0), m = q & 15;
            if (!(f & 2) || (q >= 512 && q < 576)) {
                const unsigned* src = (const unsigned*)(mem0 + (2 * (m >> 2) + (m & 1)) * DM + 16 * (q >> 4) + (m & 2));
                const u32x4 o = {min(src[0], 0xfffffffeu), min(src[2], 0xfffffffeu), min(src[4], 0xfffffffeu), min(src[6], 0xfffffffeu)};
                if (k) v1 = o; else v0 = o;
            }
        }
    }
    static_assert(G_HKB == 8 && G_RP == 2 && G_RM == 4, "layout");
    constexpr int NH = 48, NP = NH + 16, NU = 144;
#pragma unroll 1
    for (int q = f; q < NP + NU + (int)(G_C_END >> 13); q += 48) {
        const int u = q - 32, y = q - NH;
        fill_block(ws, q < 32 ? (q >> 4) * 256 + ((q >> 3) & 1) * 16 + ((q >> 2) & 1) * 2 + 8 + (q & 2) * 2 + (q & 1) : q < NH ? (u >> 3) * 256 + (u & 7) : q < NP ? (y >> 3) * 256 + 32 + (y & 7) : q < NP + NU ? q - NP + 512 : q - NP - NU + (int)(G_WS_CTL >> 13), tid, v0, v1);
        v0 = sent; v1 = sent;
    }
    const unsigned e = __hip_atomic_load((const gu32*)(ws + G_WS_EP), RLX_AGENT);
    asm volatile("s_waitcnt vmcnt(0)" :: "v"(e) : "memory");
    __syncthreads();
    if (tid == 0) __hip_atomic_store((gu64*)(ws + G_WS_DONE + (12 + f) * 64), (u64)(e + 1u), RLX_AGENT);
}
__device__ __forceinline__ void init_cal(char* ws, char* smem, int id, int tid, int n) {
    const int sl = id % 6, w = tid >> 6;
    char* a = ws + (size_t)(sl >> 1) * 0x200000 + 0x1B0000 + (id / 6) * 16384 + (sl & 1) * 8192 + w * 2048;
    volatile unsigned* lds = (volatile unsigned*)smem;
    const u32x4 z = {0u, 0u, 0u, 0u};
    asm volatile("global_store_dwordx4 %0, %1, off sc1\n\tglobal_store_dwordx4 %0, %1, off offset:1024 sc1\n\ts_waitcnt vmcnt(0)" :: "v"(a + (tid & 63) * 16), "v"(z) : "memory");
    __syncthreads();
    if ((tid & 63) == 0) {
        unsigned best = 0xfffffffeu;
#pragma unroll 1
        for (int it = 0; it < 3 * n; ++it) {
            const unsigned c0 = (unsigned)__builtin_amdgcn_s_memtime();
            const unsigned v = __hip_atomic_fetch_or((gu32*)(a + it * 128), 0u, RLX_AGENT);
            asm volatile("s_waitcnt vmcnt(0)" :: "v"(v) : "memory");
            best = min(best, (unsigned)__builtin_amdgcn_s_memtime() - c0);
            if (it + 1 >= 2 * n && best > 560u) break;
        }
        lds[w] = best;
    }
    __syncthreads();
    if (tid == 0) init_done(ws, id, min(min(lds[0], lds[1]), min(lds[2], lds[3])));
    __syncthreads();
}

#define TST(slot_, i_)
#define PIN8(a) asm volatile("" : "+v"(a[0]), "+v"(a[1]), "+v"(a[2]), "+v"(a[3]), "+v"(a[4]), "+v"(a[5]), "+v"(a[6]), "+v"(a[7]))
struct AOps { u32x4 q[4], k[8]; int fl; };
struct APtr { const char* q; const char* k; const char* m; unsigned voff, vkm, voffm, vfl; };
__device__ __forceinline__ void attn_batch(const MainP& p, AOps& o, const APtr& a) {
    const char* q2 = a.q + 4096; const char* k2 = a.k + 4096;
    WLD16(o.q[0], a.voff, a.q, 0); WLD16(o.q[1], a.voff, a.q, 2048); WLD16(o.q[2], a.voff, q2, 0); WLD16(o.q[3], a.voff, q2, 2048);
    WLD16X4(o.k[0], o.k[1], o.k[2], o.k[3], a.voff, a.k); WLD16X4(o.k[4], o.k[5], o.k[6], o.k[7], a.voff, k2);
    asm volatile("global_load_dword %0, %1, %2 sc1" : "=v"(o.fl) : "v"(a.vfl), "s"(p.ws) : "memory");
}
__device__ __forceinline__ void attn_pin(AOps& o) {
    asm volatile("" : "+v"(o.fl), "+v"(o.q[0]), "+v"(o.q[1]), "+v"(o.q[2]), "+v"(o.q[3])); PIN8(o.k);
}
static constexpr int NFINE = 6;
static constexpr int NG1 = 64 - NFINE;
__device__ __forceinline__ void score_round4(float* r, const f32x4& x) {
    asm volatile("v_cvt_pk_bf16_f32 %0, 0, %4\n\tv_cvt_pk_bf16_f32 %1, 0, %5\n\tv_cvt_pk_bf16_f32 %2, 0, %6\n\tv_cvt_pk_bf16_f32 %3, 0, %7\n\t"
                 "v_mul_f32 %0, 0x3db504f3, %0\n\tv_mul_f32 %1, 0x3db504f3, %1\n\tv_mul_f32 %2, 0x3db504f3, %2\n\tv_mul_f32 %3, 0x3db504f3, %3\n\t"
                 "v_cvt_pk_bf16_f32 %0, 0, %0\n\tv_cvt_pk_bf16_f32 %1, 0, %1\n\tv_cvt_pk_bf16_f32 %2, 0, %2\n\tv_cvt_pk_bf16_f32 %3, 0, %3"
                 : "=&v"(r[0]), "=&v"(r[1]), "=&v"(r[2]), "=&v"(r[3]) : "v"(x[0]), "v"(x[1]), "v"(x[2]), "v"(x[3]));
}
__device__ __forceinline__ float vmax3(float a, float b, float c) { float r; asm("v_max3_f32 %0, %1, %2, %3" : "=v"(r) : "v"(a), "v"(b), "v"(c)); return r; }
template <int N>
__device__ __forceinline__ void attn_soft(float (&q)[12], u32x2& Pm, u32x4& Px) {
    float m = vmax3(q[0], q[1], q[2]);
#pragma unroll
    for (int i = 3; i < N; i += 2) m = vmax3(m, q[i], q[i + 1 < N ? i + 1 : i]);
    { auto a = __builtin_amdgcn_permlane16_swap(__float_as_uint(m), __float_as_uint(m), false, false); m = vmax3(m, __uint_as_float(a[0]), __uint_as_float(a[1])); }
    { auto a = __builtin_amdgcn_permlane32_swap(__float_as_uint(m), __float_as_uint(m), false, false); m = vmax3(m, __uint_as_float(a[0]), __uint_as_float(a[1])); }
    const float ml = -m * 1.4426950408889634f;
#pragma unroll
    for (int i = 0; i < N; ++i) q[i] = __builtin_amdgcn_exp2f(fmaf(q[i], 1.4426950408889634f, ml));
    float sum = 0.5f * ((q[0] + q[1]) + (q[2] + q[3])) + ((q[4] + q[5]) + (q[6] + q[7]));
    if (N > 8) sum += (q[8] + q[9]) + (q[10] + q[11]);
    sum = rowsum4(sum);
    const float inv = __builtin_amdgcn_rcpf(sum);
    Pm = u32x2{pk2bf(q[0] * inv, q[1] * inv), pk2bf(q[2] * inv, q[3] * inv)};
    Px = u32x4{pk2bf(q[4] * inv, q[5] * inv), pk2bf(q[6] * inv, q[7] * inv), N > 8 ? pk2bf(q[8] * inv, q[9] * inv) : 0u, N > 8 ? pk2bf(q[10] * inv, q[11] * inv) : 0u};
}
static constexpr int NA = G_NA_;
#define NPRE_ 38
static constexpr int NPRE = NPRE_;
__device__ __forceinline__ int attn_next(int s) { return s < NA ? s + 1 : s + 2; }
__device__ __forceinline__ unsigned attn_flag(const MainP& p, int s, int hq) { return CTLO + G_C_PC + ((s > NA && (s & 1)) ? 1024 : 0) + hq * 128; }
struct BOps { u32x4 q[8], k[8], m[4]; int fl; };
#define WLD16X4A(d0, d1, d2, d3, voff_, base_) asm volatile("s_nop 4\n\tglobal_load_dwordx4 %0, %4, %5 sc1\n\tglobal_load_dwordx4 %1, %4, %5 offset:1024 sc1\n\t" \
    "global_load_dwordx4 %2, %4, %5 offset:2048 sc1\n\tglobal_load_dwordx4 %3, %4, %5 offset:3072 sc1" \
    : "=&a"(d0), "=&a"(d1), "=&a"(d2), "=&a"(d3) : "v"(voff_), "s"(base_) : "memory")
#define PIN8A(a) asm volatile("" : "+a"(a[0]), "+a"(a[1]), "+a"(a[2]), "+a"(a[3]), "+a"(a[4]), "+a"(a[5]), "+a"(a[6]), "+a"(a[7]))
#define MFMA_AV0(acc, a_, b_) asm volatile("v_mfma_f32_16x16x32_bf16 %0, %1, %2, 0" : "=&v"(acc) : "a"(a_), "v"(b_))
#define MFMA_AV(acc, a_, b_) asm volatile("v_mfma_f32_16x16x32_bf16 %0, %1, %2, %0" : "+v"(acc) : "a"(a_), "v"(b_))
__device__ __forceinline__ void attn_batch8(const MainP& p, BOps& o, const APtr& a) {
    const char* q2 = a.q + 4096; const char* k2 = a.k + 4096;
    WLD16X4(o.q[0], o.q[1], o.q[2], o.q[3], a.voff, a.q); WLD16X4(o.q[4], o.q[5], o.q[6], o.q[7], a.voff, q2);
    WLD16X4A(o.k[0], o.k[1], o.k[2], o.k[3], a.voff, a.k); WLD16X4A(o.k[4], o.k[5], o.k[6], o.k[7], a.voff, k2);
}
template <bool PRE>
__device__ __forceinline__ void attn_step(const MainP& p, BOps& o, BOps& n1, BOps& n2, APtr& a, int& kg, int& kk, int s, int prev, int h, int w, int lane) {
    const int g = lane >> 4, c = lane & 15;
    const float scale = 0.08838834764831845f;
    TST(s * 64 + h * 2, 0);
    const int s2 = attn_next(attn_next(s));
    const int sm = PRE ? attn_next(s) : s;
    if (kg <= s2 || kk < sm) {
        VMC0();
        wait_word<true>(p, p.ws + a.vfl, (lane & 1) ? sm : s2 + 1, 13u);
    }
    TST(s * 64 + h * 2, 1);
    a.q = qx_ptr(p, s2, h); a.k = a.q + (G_D_KX - G_D_QX); a.m = p.ws + G_D_KM + (size_t)h * G_HS4 + skew(sm, h) * 4096;
    if (PRE) {
        asm volatile("s_waitcnt vmcnt(20)\n\ts_barrier" : "+a"(o.m[0]), "+a"(o.m[1]), "+a"(o.m[2]), "+a"(o.m[3]), "+v"(o.fl) :: "memory");
        PIN8(o.q); PIN8A(o.k);
    }
    __builtin_amdgcn_sched_barrier(0);
    BOps& d = PRE ? n1 : o;
    u32x4 (&km)[4] = o.m;
    int& fl = o.fl;
    WLD16X4A(d.m[0], d.m[1], d.m[2], d.m[3], a.vkm, a.m);
    asm volatile("global_load_dword %0, %1, %2 sc1" : "=v"(d.fl) : "v"(a.vfl), "s"(p.ws) : "memory");
    const char* q2 = a.q + 4096; const char* k2 = a.k + 4096;
#define SPREAD(x_) do { if (PRE) { __builtin_amdgcn_sched_barrier(0); x_; __builtin_amdgcn_sched_barrier(0); } } while (0)
    if (!PRE) attn_batch8(p, n2, a);
    f32x4 y[5];
#pragma unroll
    for (int wc = 0; wc < 4; ++wc)
#pragma unroll
        for (int i = 0; i < 3; ++i) { if (wc == 0) MFMA_AV0(y[i], o.k[i >> 1], o.q[(i + 1) >> 1]); else MFMA_AV(y[i], o.k[wc * 2 + (i >> 1)], o.q[wc * 2 + ((i + 1) >> 1)]); }
    asm volatile("s_nop 7" : "+v"(y[0]), "+v"(y[1]), "+v"(y[2]));
    SPREAD(WLD16X4(n2.q[0], n2.q[1], n2.q[2], n2.q[3], a.voff, a.q));
    float q0[12], q1[12];
    score_round4(q0 + 4, y[0]); score_round4(q1 + 4, y[1]); score_round4(q1 + 8, y[2]);
#pragma unroll
    for (int i = 0; i < 4; ++i) {
        const bool vis = 4 * g + i <= c;
        q0[4 + i] = vis ? q0[4 + i] : -INFINITY; q1[8 + i] = vis ? q1[8 + i] : -INFINITY;
    }
    __builtin_amdgcn_sched_barrier(0);
    if (!PRE) {
        asm volatile("s_waitcnt vmcnt(16)\n\ts_barrier" : "+a"(km[0]), "+a"(km[1]), "+a"(km[2]), "+a"(km[3]), "+v"(fl) :: "memory");
        PIN8(n1.q); PIN8A(n1.k);
    }
    { const unsigned fo = attn_flag(p, s, h >> 2); const int fv = PRE ? prev - 1 : s - 1;
      asm volatile("s_cmp_lg_u32 %3, 0\n\ts_cbranch_scc1 1f\n\tglobal_store_dword %0, %1, %2 sc1\n1:" :: "v"(fo), "v"(fv), "s"(p.ws), "s"(w) : "scc", "memory"); }
#pragma unroll
    for (int wc = 0; wc < 4; ++wc)
#pragma unroll
        for (int i = 0; i < 2; ++i) { if (wc == 0) MFMA_AV0(y[3 + i], km[0], o.q[i]); else MFMA_AV(y[3 + i], km[wc], o.q[wc * 2 + i]); }
    asm volatile("s_nop 7" : "+v"(y[3]), "+v"(y[4]));
    kg = __builtin_amdgcn_readlane(fl, 0); kk = __builtin_amdgcn_readlane(fl, 1);
    SPREAD(WLD16X4(n2.q[4], n2.q[5], n2.q[6], n2.q[7], a.voff, q2));
    score_round4(q0, y[3]); score_round4(q1, y[4]);
    u32x2 Pm0, Pm1; u32x4 Px0, Px1;
    attn_soft<8>(q0, Pm0, Px0);
    SPREAD(WLD16X4A(n2.k[0], n2.k[1], n2.k[2], n2.k[3], a.voff, a.k));
    attn_soft<12>(q1, Pm1, Px1);
    SPREAD(WLD16X4A(n2.k[4], n2.k[5], n2.k[6], n2.k[7], a.voff, k2));
    char* pxb = p.ws + G_D_PX + (size_t)(s * 32 + h) * 2048;
    char* pmb = p.ws + G_D_PM + (size_t)s * 16384 + h * 256; char* pm2 = pmb + 8192;
    WST16(a.voff, Px0, pxb, 0); WST16(a.voff, Px1, pxb, 1024);
    asm volatile("global_store_dwordx2 %0, %1, %3 sc1\n\tglobal_store_dwordx2 %0, %2, %4 sc1" :: "v"(a.voffm), "v"(Pm0), "v"(Pm1), "s"(pmb), "s"(pm2) : "memory");
    if (!PRE) { const unsigned fo = attn_flag(p, s, h >> 2);
      asm volatile("s_cmp_lt_u32 %4, 46\n\ts_cbranch_scc1 1f\n\ts_waitcnt vmcnt(0)\n\ts_barrier\n\ts_cmp_lg_u32 %3, 0\n\ts_cbranch_scc1 1f\n\tglobal_store_dword %0, %1, %2 sc1\n1:"
                   :: "v"(fo), "v"(s), "s"(p.ws), "s"(w), "s"(s) : "scc", "memory"); }
    TST(s * 64 + h * 2, 2);
}
__device__ __forceinline__ void attn_role(const MainP& p, int s, int h, int w, int lane) {
    APtr a;
    a.q = qx_ptr(p, s, h); a.k = a.q + (G_D_KX - G_D_QX);
    a.voff = lane * 16; a.vkm = (lane & 55) * 16; a.voffm = (lane & 15) * 16 + ((lane >> 4) & 1) * 8;
    a.vfl = CTLO + ((lane & 1) ? G_C_KM : G_C_GX) + h * 128;
    BOps o0, o1, o2;
    wait_word(p, p.ws + a.vfl, (lane & 1) ? s : attn_next(s) + 1, 10u);
    attn_batch8(p, o0, a);
    a.q = qx_ptr(p, attn_next(s), h); a.k = a.q + (G_D_KX - G_D_QX); a.m = p.ws + G_D_KM + (size_t)h * G_HS4 + skew(s, h) * 4096;
    attn_batch8(p, o1, a);
    WLD16X4A(o0.m[0], o0.m[1], o0.m[2], o0.m[3], a.vkm, a.m);
    VMC0();
    o0.fl = -1;
    PIN8(o0.q); PIN8A(o0.k); PIN8(o1.q); PIN8A(o1.k);
    int kg = -1, kk = -1, pv = s;
    static_assert(NA % 2 == 0 && NG1 + 3 < NSEG && NPRE + 6 < 46, "ranges");
#pragma unroll 1
    while (s < NPRE) {
        attn_step<true>(p, o0, o1, o2, a, kg, kk, s, pv, h, w, lane); pv = s; s = attn_next(s);
        attn_step<true>(p, o1, o2, o0, a, kg, kk, s, pv, h, w, lane); pv = s; s = attn_next(s);
        attn_step<true>(p, o2, o0, o1, a, kg, kk, s, pv, h, w, lane); pv = s; s = attn_next(s);
    }
    asm volatile("s_waitcnt vmcnt(0)" : "+a"(o0.m[0]), "+a"(o0.m[1]), "+a"(o0.m[2]), "+a"(o0.m[3]), "+v"(o0.fl) :: "memory");
    PIN8(o0.q); PIN8A(o0.k); PIN8(o1.q); PIN8A(o1.k);
#pragma unroll 1
    for (;;) {
        attn_step<false>(p, o0, o1, o2, a, kg, kk, s, 0, h, w, lane);
        if ((s = attn_next(s)) >= NG1) break;
        attn_step<false>(p, o1, o2, o0, a, kg, kk, s, 0, h, w, lane);
        if ((s = attn_next(s)) >= NG1) break;
        attn_step<false>(p, o2, o0, o1, a, kg, kk, s, 0, h, w, lane);
        if ((s = attn_next(s)) >= NG1) break;
    }
    VMC0();
}
__device__ __forceinline__ unsigned umax4(const u32x4& v) { return max(max(v[0], v[1]), max(v[2], v[3])); }
__device__ __forceinline__ bool give_up(const MainP& p, unsigned& tries, unsigned code) {
    if (++tries <= kWaitCap / 4) return false;
    __hip_atomic_store((gu32*)(p.ws + G_WS_CTL + G_C_ERR), code, RLX_AGENT);
    return true;
}
__device__ __forceinline__ void task_attn_tail(const MainP& p, int s, int h, int w, int lane) {
    const int g = lane >> 4, c = lane & 15, qt = w & 1;
    const float scale = 0.08838834764831845f;
    APtr a;
    a.q = qx_ptr(p, s, h) + qt * 1024; a.k = qx_ptr(p, s, h) + (G_D_KX - G_D_QX); a.m = p.ws + G_D_KM + (size_t)h * G_HS4 + skew(s, h) * 4096;
    a.voff = lane * 16; a.vkm = (lane & 55) * 16; a.voffm = c * 16 + (g & 1) * 8; a.vfl = G_WS_CTL;
    AOps o;
    wait_word(p, p.ws + CTLO + G_C_GX + h * 128, s + 1, 14u);
    attn_batch(p, o, a);
    VMC0();
    attn_pin(o);
    float q[12];
    u32x4 km[4];
#pragma unroll
    for (int kt = 1; kt >= -1; --kt) {
        if (kt < 0) {
            TST(s * 64 + h * 2 + qt, 0);
            wait_word(p, p.ws + CTLO + G_C_KM + h * 128, s - 2, 15u);
            if (FLG(p.prm[5]) & 2) wait_word<true>(p, p.ws + CTLO + G_C_KM + h * 128, s - 1, 15u);
            if (s < 63) for (int i = 0; i < (int)((FLG2(p.prm[7]) >> 3) & 31); ++i) __builtin_amdgcn_s_sleep(2);
            for (unsigned tries = 0;;) {
                WLD16X4(km[0], km[1], km[2], km[3], a.vkm, a.m);
                asm volatile("s_waitcnt vmcnt(0)" : "+v"(km[0]), "+v"(km[1]), "+v"(km[2]), "+v"(km[3]) :: "memory");
                if (!__any(max(max(umax4(km[0]), umax4(km[1])), max(umax4(km[2]), umax4(km[3]))) == 0xffffffffu) || give_up(p, tries, 16u)) break;
            }
            TST(s * 64 + h * 2 + qt, 1);
        }
        f32x4 y = {0.f, 0.f, 0.f, 0.f};
#pragma unroll
        for (int wc = 0; wc < 4; ++wc) y = MFMA32(kt < 0 ? km[wc] : o.k[wc * 2 + (kt < 0 ? 0 : kt)], o.q[wc], y);
#pragma unroll
        for (int i = 0; i < 4; ++i) q[4 + 4 * kt + i] = (16 * kt + 4 * g + i <= 16 * qt + c) ? bfr(bfr(y[i]) * scale) : -INFINITY;
    }
    u32x2 Pm; u32x4 Px;
    attn_soft<12>(q, Pm, Px);
    char* pxb = p.ws + G_D_PX + (size_t)(s * 32 + h) * 2048 + qt * 1024;
    char* pmb = p.ws + G_D_PM + (size_t)s * 16384 + h * 256 + qt * 8192;
    WST16(a.voff, Px, pxb, 0);
    asm volatile("global_store_dwordx2 %0, %1, %2 sc1" :: "v"(a.voffm), "v"(Pm), "s"(pmb) : "memory");
    TST(s * 64 + h * 2 + qt, 2);
}

#define RLD(d_, voff_, base_, off_) asm volatile("global_load_dwordx4 %0, %1, %2 offset:%3 sc1" : "=v"(d_) : "v"(voff_), "s"(base_), "i"(off_) : "memory")
#define RLDA(d_, voff_, base_, off_) asm volatile("global_load_dwordx4 %0, %1, %2 offset:%3 sc1" : "=a"(d_) : "v"(voff_), "s"(base_), "i"(off_) : "memory")
#define MFMA_ACC_A(acc, a_, b_) asm volatile("v_mfma_f32_16x16x32_bf16 %0, %1, %2, %0" : "+v"(acc) : "a"(a_), "v"(b_))
#define VMW(n_) asm volatile("s_waitcnt vmcnt(%0)" :: "i"(n_) : "memory")
template <int NT, int DEPTH, bool TAIL>
__device__ __forceinline__ void task_out(const MainP& p, char* smem, int s, int ct0, int w, int lane) {
    const int g = lane >> 4, c = lane & 15;
    const unsigned voff = lane * 16;
    f32x4 acc[NT][2];
#pragma unroll
    for (int t = 0; t < NT; ++t) { acc[t][0] = f32x4{0.f, 0.f, 0.f, 0.f}; acc[t][1] = acc[t][0]; asm volatile("" : "+v"(acc[t][0]), "+v"(acc[t][1])); }
    const char* ub[NT];
#pragma unroll
    for (int t = 0; t < NT; ++t) ub[t] = uni(p.ws + G_D_UX + (size_t)(ct0 + t) * G_UXS + skew(s, ct0 + t) * 32768 + w * 8192);
    const char* pb = uni(p.ws + G_D_PX + (size_t)(s * 32 + w * 8) * 2048);
    const char* um = uni(p.ws + s * 9216 + ct0 * 256);
    const unsigned vu0 = umx_base(w * 8 + g) + c * 16, vu1 = umx_base(w * 8 + 4 + g) + c * 16;
    const char* pm = uni(p.ws + G_D_PM + (size_t)s * 16384 + w * 2048);
    constexpr int NR = DEPTH + 1 > 10 ? 10 : DEPTH + 1;
    u32x4 a[NR][NT], b[10][2];
    unsigned vb = voff, va = voff;
    TST(4096 + (s * 36 + ct0) * 4 + w, 0);
    const int f0 = (int)__hip_atomic_load((const gu32*)(p.ws + CTLO + (lane < 8 ? G_C_UM + (w * 8 + lane) * 128 : lane < 16 ? G_C_GV + ((s & (G_GVN - 1)) * 32 + w * 8 + lane - 8) * 128
                                                                                                                                : attn_flag(p, s, w * 2 + (lane & 1)) - CTLO)), RLX_AGENT);
    const bool early = !TAIL && !__any(f0 < s);
    if (!early) wait_word(p, p.ws + CTLO + (lane < 8 ? G_C_GV + ((s & (G_GVN - 1)) * 32 + w * 8 + lane) * 128 : G_C_UM + w * 8 * 128), lane < 8 ? s : s - 3, 21u);
#define ALOAD(q) do { if ((q) < 8) { _Pragma("unroll") for (int t = 0; t < NT; ++t) RLDA(a[(q) % NR][t], va, ub[t], 0);  \
                                     asm volatile("v_add_u32 %0, 0x400, %0" : "+v"(va)); }                                            \
                      else { _Pragma("unroll") for (int t = 0; t < NT; ++t) RLDA(a[(q) % NR][t], ((q) & 1) ? vu1 : vu0, um, t * 256); } } while (0)
    constexpr int D0 = DEPTH < 8 ? DEPTH : 8;
    asm volatile("s_nop 4");
#pragma unroll
    for (int q = 0; q < D0; ++q) ALOAD(q);
    asm volatile("s_cmp_lg_u32 %0, 0\n\ts_cbranch_scc1 1f\n\ts_waitcnt vmcnt(0)\n1:\n\ts_waitcnt vmcnt(30)" :: "s"(__builtin_amdgcn_readfirstlane((int)early)) : "scc", "memory");
    unsigned tries = 0;
    if (TAIL) {
        const char* sp = pm + (lane & 7) * 256 + ((lane >> 3) & 1) * 8192;
        const int dl = (FLG(p.prm[5]) >> 26) & 31;
        if (dl) {
            wait_word<true>(p, p.ws + CTLO + G_C_KM + ((ct0 >> 1) + w) * 128, s - 1, 25u);
            for (int i = 0; i < dl + (s < 63 ? (int)((FLG2(p.prm[7]) >> 8) & 31) : 0); ++i) __builtin_amdgcn_s_sleep(2);
        } else
        while (__any(__hip_atomic_load((const gu32*)sp, RLX_AGENT) == 0xffffffffu) && !give_up(p, tries, 23u)) __builtin_amdgcn_s_sleep(1);
    } else if (!early) wait_word(p, p.ws + CTLO + (lane < 8 ? G_C_UM + (w * 8 + lane) * 128 : attn_flag(p, s, w * 2 + (lane & 1)) - CTLO), s, 22u);
    if (!early) TST(4096 + (s * 36 + ct0) * 4 + w, 1);
    for (;;) {
    asm volatile("s_nop 4");
#pragma unroll
    for (int q = 0; q < 8; ++q) { RLD(b[q][0], vb, pb, 0); RLD(b[q][1], vb, pb, 1024); asm volatile("v_add_u32 %0, 0x800, %0" : "+v"(vb)); }
#pragma unroll
    for (int q = 8; q < 10; ++q) { RLD(b[q][0], voff, pm, (q & 1) * 1024); RLD(b[q][1], voff, pm + 8192, (q & 1) * 1024); }
#pragma unroll
    for (int q = D0; q < NR; ++q) ALOAD(q);
#pragma unroll
    for (int q = 0; q < 10; ++q) {
        const int issuedA = (NR + q < 10 ? NR + q : 10) - D0;
        if (q < D0) VMW(2 * (9 - q) + issuedA * NT); else VMW((issuedA - (q - D0 + 1)) * NT);
#pragma unroll
        for (int t = 0; t < NT; ++t) { MFMA_ACC_A(acc[t][0], a[q % NR][t], b[q][0]); MFMA_ACC_A(acc[t][1], a[q % NR][t], b[q][1]); }
        if (NR + q < 10) ALOAD(NR + q);
    }
    asm volatile("s_nop 7" ::: "memory");
#pragma unroll
    for (int t = 0; t < NT; ++t) asm volatile("" : "+v"(acc[t][0]), "+v"(acc[t][1]));
    if (!TAIL) break;
    f32x4 chk = acc[0][0];
#pragma unroll
    for (int t = 0; t < NT; ++t) chk += acc[t][1] + acc[t][0];
    const float ck = (chk[0] + chk[1]) + (chk[2] + chk[3]);
    if (!__any(ck != ck)) break;
    unsigned mx = 0;
#pragma unroll
    for (int q = 0; q < 10; ++q) { asm volatile("" : "+v"(b[q][0]), "+v"(b[q][1])); mx = max(mx, max(umax4(b[q][0]), umax4(b[q][1]))); }
#pragma unroll
    for (int t = 0; t < NT; ++t) { asm volatile("" : "+a"(a[8 % NR][t]), "+a"(a[9 % NR][t])); mx = max(mx, max(umax4(a[8 % NR][t]), umax4(a[9 % NR][t]))); }
    if (!__any(mx == 0xffffffffu) || give_up(p, tries, 24u)) break;
    vb = voff;
#pragma unroll
    for (int t = 0; t < NT; ++t) { acc[t][0] = f32x4{0.f, 0.f, 0.f, 0.f}; acc[t][1] = acc[t][0]; asm volatile("" : "+v"(acc[t][0]), "+v"(acc[t][1])); }
    }
    TST(4096 + (s * 36 + ct0) * 4 + w, 2);
    f32x4* red = (f32x4*)smem;
#pragma unroll
    for (int t = 0; t < NT; ++t) { red[((w * NT + t) * 2 + 0) * 64 + lane] = acc[t][0]; red[((w * NT + t) * 2 + 1) * 64 + lane] = acc[t][1]; }
    __syncthreads();
    u16* orow = p.out + (size_t)(32 * s + 16 * (w & 1) + c) * OUTW + 16 * ct0 + 4 * g;
#pragma unroll
    for (int t = 0; t < NT; ++t)
        if ((t & 1) == (w >> 1)) {
            const int m = w & 1;
            const f32x4 v = (red[((0 * NT + t) * 2 + m) * 64 + lane] + red[((1 * NT + t) * 2 + m) * 64 + lane]) +
                            (red[((2 * NT + t) * 2 + m) * 64 + lane] + red[((3 * NT + t) * 2 + m) * 64 + lane]);
            *(u32x2*)(orow + 16 * t) = u32x2{pk2bf(v[0], v[1]), pk2bf(v[2], v[3])};
        }
}

static constexpr int KROW = G_KROW;
static constexpr int KARR = 80 * KROW;
static constexpr int UCS = 2208;
struct BCh {
    u16 uc[4][UCS];
    f32x4 cst[NST];
    u16 Kh[KARR + 256];
    u16 ub[SL];
};
static_assert(4 * sizeof(BCh) <= LDS_TOTAL - 32, "LDS");
typedef __attribute__((ext_vector_type(8))) _Float16 f16x8;
typedef __attribute__((ext_vector_type(2))) _Float16 f16x2;
__device__ __forceinline__ unsigned wave_umax(unsigned m) {
#pragma unroll
    for (int o = 32; o; o >>= 1) m = max(m, (unsigned)__shfl_xor((int)m, o));
    return m;
}
__device__ __forceinline__ unsigned scale_exp(unsigned absbits) { return min(max(absbits >> 23, 15u), 254u); }
__device__ __forceinline__ unsigned pk2h(float a, float b) { unsigned r; asm("v_cvt_pk_f16_f32 %0, %1, %2" : "=v"(r) : "v"(a), "v"(b)); return r; }
__device__ __forceinline__ unsigned prm(unsigned hi, unsigned lo, unsigned sel) { return __builtin_amdgcn_perm(hi, lo, sel); }
#define BST(i)
#define BST2(i)
__device__ __forceinline__ void task_ut(const MainP& p, char* smem, int rb, int w, int lane) {
    const int tid = w * 64 + lane, g = lane >> 4, c = lane & 15;
    const u32x4* src = (const u32x4*)(p.U + (size_t)rb * 64 * DM);
    u32x4 v[18];
#pragma unroll
    for (int i = 0; i < 18; ++i) {
        v[i] = src[tid + 256 * i];
    }
    BST2(0);
#pragma unroll
    for (int i = 0; i < 18; ++i) { const int ch = tid + 256 * i, row = ch / 72; *(u32x4*)(smem + row * 1184 + (ch - 72 * row) * 16) = v[i]; }
    __syncthreads();
    const unsigned ad = (unsigned)(uintptr_t)smem + (16 * g + (c >> 2)) * 1184 + (w * 144 + 4 * (c & 3)) * 2;
#pragma unroll
    for (int i = 0; i < 9; ++i) {
        u32x2 x0, x1, x2, x3;
        asm volatile("ds_read_b64_tr_b16 %0, %4 offset:%5\n\tds_read_b64_tr_b16 %1, %4 offset:%6\n\tds_read_b64_tr_b16 %2, %4 offset:%7\n\tds_read_b64_tr_b16 %3, %4 offset:%8\n\ts_waitcnt lgkmcnt(0)"
                     : "=&v"(x0), "=&v"(x1), "=&v"(x2), "=&v"(x3) : "v"(ad), "i"(i * 32), "i"(4 * 1184 + i * 32), "i"(8 * 1184 + i * 32), "i"(12 * 1184 + i * 32) : "memory");
        v[2 * i] = u32x4{x0[0], x0[1], x1[0], x1[1]}; v[2 * i + 1] = u32x4{x2[0], x2[1], x3[0], x3[1]};
    }
    __syncthreads();
    BST2(1);
    char* tw = smem + w * (144 * 144);
#pragma unroll
    for (int i = 0; i < 9; ++i) { *(u32x4*)(tw + (16 * i + c) * 144 + g * 32) = v[2 * i]; *(u32x4*)(tw + (16 * i + c) * 144 + g * 32 + 16) = v[2 * i + 1]; }
    const char* dst = uni(p.ws + G_D_UT + (size_t)rb * 73728 + w * 18432);
#pragma unroll
    for (int i = 0; i < 18; ++i) {
        const u32x4 o = *(const u32x4*)(tw + (8 * i + (lane >> 3)) * 144 + (lane & 7) * 16);
        const unsigned vi = lane * 16 + i * 1024;
        WST16(vi, o, dst, 0);
    }
    BST2(2);
    const u32x4 sent = {0xffffffffu, 0xffffffffu, 0xffffffffu, 0xffffffffu};
#pragma unroll 1
    for (int sg = NG1; sg < 64; ++sg) {
        const unsigned ov = umx_base(rb) + sg * 9216 + tid * 16, ok = G_D_KM + rb * G_HS4 + skew(sg, rb) * 4096 + tid * 16;
        const unsigned op = tid < 128 ? G_D_PX + (sg * 32 + rb) * 2048 + tid * 16
                          : tid < 192 ? ov + 6144 : G_D_PM + sg * 16384 + ((tid >> 4) & 1) * 8192 + rb * 256 + (tid & 15) * 16;
        asm volatile("global_store_dwordx4 %0, %4, %5 sc1\n\tglobal_store_dwordx4 %1, %4, %5 sc1\n\tglobal_store_dwordx4 %2, %4, %5 sc1\n\t"
                     "global_store_dwordx4 %3, %4, %5 sc1\n\ts_nop 1" :: "v"(ov), "v"(ov + 4096), "v"(ok), "v"(op), "v"(sent), "s"(p.ws) : "memory");
    }
    BST2(3);
    VMC0();
    BST2(4);
    __syncthreads();
    if (tid == 0) __hip_atomic_store((gu32*)(p.ws + CTLO + G_C_UT + rb * 128), 1u, RLX_AGENT);
}
struct KPar { u16 ldt, pa[2][4]; };
__device__ __forceinline__ KPar kgen_par(const MainP& p, int d, int lane) {
    KPar k;
    k.ldt = p.logdt[d];
#pragma unroll
    for (int q = 0; q < 2; ++q) { const int i = d * NST + lane + 64 * q; k.pa[q][0] = p.logA[i]; k.pa[q][1] = p.Aim[i]; k.pa[q][2] = p.Cre[i]; k.pa[q][3] = p.Cim[i]; }
    return k;
}
struct Pull { const char* a; const char* b; const char* c; unsigned at; };
__device__ __forceinline__ void pull_inputs(Pull& q, int w, bool now) {
    if (q.a && (now || (int)((unsigned)__builtin_amdgcn_s_memrealtime() - q.at) >= 0)) {
#pragma unroll
        for (int i = 0; i < 3; ++i)
            if (i < 2 || w == 0) asm volatile("s_mov_b32 m0, %3\n\ts_nop 0\n\tglobal_load_lds_dword %0, %1\n\tglobal_load_lds_dword %0, %2" :: "v"(threadIdx.x * 128 + i * 32768), "s"(q.a), "s"(q.b), "s"(155648) : "memory");
        asm volatile("global_load_lds_dword %0, %1" :: "v"(min(threadIdx.x, 143u) * 128), "s"(q.c) : "memory");
        q.a = nullptr;
    }
}
__device__ __forceinline__ void task_kgen(const MainP& p, char* smem, int d, int w, int lane, const KPar& kp, bool first, unsigned ep, Pull& pl) {
    BCh& L = reinterpret_cast<BCh*>(smem)[w];
    const int g = lane >> 4, c = lane & 15, d0 = d;
    const int tid = (d & 3) * 64 + lane;
    unsigned* Kd = (unsigned*)L.uc;
    unsigned ek;
    BST(10);
    const auto& pa = kp.pa;
    const float dt = bfr(expf(bf2f(kp.ldt)));
    f32x4 cs[2];
#pragma unroll
    for (int q = 0; q < 2; ++q) {
        const float ar = -bfr(expf(bf2f(pa[q][0])));
        const float cr = bf2f(pa[q][2]) * dt, ci = bf2f(pa[q][3]) * dt;
        cs[q] = f32x4{ar * dt * 1.4426950408889634f, bf2f(pa[q][1]) * dt * 0.15915494309189535f, __log2f(hypotf(cr, ci)), atan2f(ci, cr) * 0.15915494309189535f};
    }
    const int sh = 134 - (int)wave_umax((unsigned)(int)ceilf(fminf(fmaxf(fmaxf(cs[0][2], cs[1][2]), -120.f), 120.f) + 120.f));
#pragma unroll
    for (int q = 0; q < 2; ++q) { cs[q][2] += (float)sh; L.cst[lane + 64 * q] = cs[q]; }
    BST(11);
    {
        f32x4 acc[3];
#pragma unroll
        for (int v = 0; v < 3; ++v) acc[v] = f32x4{0.f, 0.f, 0.f, 0.f};
        auto base_of = [](int v, int r) { return v == 0 ? (r & 7) + 128 * (r >> 3) : v == 1 ? (r < 8 ? 256 + 2 * (r & 3) + 128 * (r >> 2) : 512 + 4 * (r & 1) + 128 * ((r - 8) >> 1))
                                                                                           : 1024 + 128 * min(r, 8); };
        const float b0 = (float)base_of(0, c), b1 = (float)base_of(1, c), b2 = (float)base_of(2, c), off = (float)(8 * c);
#define KG_POLAR(lg_, f_, a_, b_) const float m_ = __builtin_amdgcn_exp2f(fmaf(cc[0], b_, lg_)), p_ = __builtin_amdgcn_fractf(fmaf(a_, b_, f_)); \
            const float re_ = m_ * __builtin_amdgcn_cosf(p_), im_ = m_ * __builtin_amdgcn_sinf(p_)
#define MFMA32H(a_, b_, c_) __builtin_amdgcn_mfma_f32_16x16x32_f16(__builtin_bit_cast(f16x8, a_), __builtin_bit_cast(f16x8, b_), c_, 0, 0, 0)
#pragma unroll 1
        for (int t = 0; t < NST / 16; ++t) {
            u32x4 fh, fl, ah[3];
            pull_inputs(pl, w, false);
#pragma unroll
            for (int j = 0; j < 4; ++j) {
                const f32x4 cc = L.cst[16 * t + 4 * g + j];
                { KG_POLAR(12.f, 0.f, -cc[1], off); const unsigned hh = pk2h(re_, im_); const f16x2 h2 = __builtin_bit_cast(f16x2, hh); fh[j] = hh; fl[j] = pk2h(re_ - (float)h2[0], im_ - (float)h2[1]); }
                { KG_POLAR(cc[2], cc[3], cc[1], b0); ah[0][j] = pk2h(re_, im_); }
                { KG_POLAR(cc[2], cc[3], cc[1], b1); ah[1][j] = pk2h(re_, im_); }
                { KG_POLAR(cc[2], cc[3], cc[1], b2); ah[2][j] = pk2h(re_, im_); }
            }
            asm volatile("s_nop 1" : "+v"(fh), "+v"(fl), "+v"(ah[0]), "+v"(ah[1]), "+v"(ah[2]));
#pragma unroll
            for (int v = 0; v < 3; ++v) { acc[v] = MFMA32H(ah[v], fl, acc[v]); acc[v] = MFMA32H(ah[v], fh, acc[v]); }
        }
        BST(12);
        unsigned mx = 0;
#pragma unroll
        for (int v = 0; v < 3; ++v)
#pragma unroll
            for (int i = 0; i < 4; ++i)
                if (base_of(v, 4 * g + i) + 8 * c <= 2048) mx = max(mx, __float_as_uint(acc[v][i]) & 0x7fffffffu);
        ek = scale_exp(wave_umax(mx));
        const float sk = __uint_as_float((268u - ek) << 23);
#pragma unroll
        for (int v = 0; v < 3; ++v)
#pragma unroll
            for (int i = 0; i < 4; ++i) {
                const int pos = base_of(v, 4 * g + i) + 8 * c;
                if (pos <= 2048) Kd[pos] = pk2h(acc[v][i] * sk, 0.f);
            }
    }
    char* kt = p.ws + G_D_KT + (size_t)d * 8192;
    char* kt2 = kt + 4096;
    const unsigned voff = lane * 16;
#pragma unroll
    for (int q = 0; q < 4; ++q) {
        const int e = lane + 64 * q;
        unsigned t[8], k[8];
        const unsigned k0 = Kd[8 * e], k8 = Kd[8 * e + 8];
        if (q == 0) {
            const u32x4 a = *(const u32x4*)&Kd[8 * e], b = *(const u32x4*)&Kd[8 * e + 4];
            const bool two = lane >= 32;
            t[0] = a[0]; t[1] = two ? a[0] : a[1]; t[2] = a[2]; t[3] = two ? b[0] : a[3]; t[4] = b[0]; t[5] = two ? b[0] : b[1]; t[6] = b[2]; t[7] = two ? k8 : b[3];
        } else if (q == 1) {
            const unsigned k4 = Kd[8 * e + 4];
            t[0] = t[1] = t[2] = k0; t[3] = t[4] = t[5] = k4; t[6] = t[7] = k8;
        } else {
            t[0] = t[1] = t[2] = t[3] = k0; t[4] = (lane & 1) ? k8 : k0; t[5] = t[6] = t[7] = k8;
        }
#pragma unroll
        for (int j = 0; j < 8; ++j) k[j] = t[7 - j];
        u32x4 kh;
#pragma unroll
        for (int j = 0; j < 4; ++j) kh[j] = prm(k[2 * j + 1], k[2 * j], 0x05040100u);
        WST16(voff, kh, kt, q * 1024);
    }
    ek = (unsigned)min(max((int)ek - sh - 12, 15), 253);
    if (lane < 8) { const u32x4 e4 = {ek, ek, ek, ek}; WST16(voff, e4, kt2, 0); }
    BST(13);
    VMC0();
    BST(14);
    if (first) init_seen(p.ws, lane, ep);
    BST(15);
    if (lane == 0) __hip_atomic_fetch_add((gu32*)(p.ws + G_WS_CTL + G_C_KG + (d >> 2) * 128), 1u, RLX_AGENT);
}
__device__ __forceinline__ void task_s4d(const MainP& p, char* smem, int d0, int w, int lane, int& nb, bool more) {
    BCh* LL = reinterpret_cast<BCh*>(smem);
    BCh& L = LL[w];
    const int tid = w * 64 + lane, g = lane >> 4, c = lane & 15, d = d0 + w;
    BST(0);
    wait_word(p, p.ws + (lane < 32 ? CTLO + G_C_UT + lane * 128 : G_WS_CTL + G_C_KG + (d0 >> 2) * 128), lane < 32 ? 1 : 3, 30u);
    const float dsk = bf2f(p.Dskip[d]);
    const char* kt = p.ws + G_D_KT + (size_t)d * 8192;
    const char* kt2 = kt + 4096;
    const char* ut = p.ws + G_D_UT + d * 128;
    const unsigned voff = lane * 16;
    u32x4 kh[4], uu[4];
    unsigned ek;
#pragma unroll
    for (int q = 0; q < 4; ++q) { const unsigned vu = (8 * q + (lane >> 3)) * 73728 + (lane & 7) * 16; WLD16(uu[q], vu, ut, 0); }
    WLD16X4(kh[0], kh[1], kh[2], kh[3], voff, kt);
    asm volatile("global_load_dword %0, %1, %2 sc1" : "=v"(ek) : "v"(0u), "s"(kt2) : "memory");
    const u32x4 z4 = {0u, 0u, 0u, 0u};
    *(u32x4*)&L.Kh[8 * lane] = z4;
    *(u32x4*)&L.Kh[512 + 8 * (lane & 31)] = z4;
    *(u32x4*)&L.uc[0][64 + SL] = z4;
    *(u32x4*)&L.uc[(lane >> 3) & 3][8 * (lane & 7)] = z4;
    asm volatile("s_waitcnt vmcnt(0)" : "+v"(kh[0]), "+v"(kh[1]), "+v"(kh[2]), "+v"(kh[3]), "+v"(ek),
                 "+v"(uu[0]), "+v"(uu[1]), "+v"(uu[2]), "+v"(uu[3]) :: "memory");
    BST(1);
    unsigned mu = 0;
#pragma unroll
    for (int q = 0; q < 4; ++q)
#pragma unroll
        for (int j = 0; j < 4; ++j) mu = max(mu, max(uu[q][j] & 0x7fff0000u, (uu[q][j] << 16) & 0x7fff0000u));
    const unsigned eu = scale_exp(wave_umax(mu));
    const float su = __uint_as_float((268u - eu) << 23), iu = __uint_as_float((eu - 14u) << 23), ik = __uint_as_float((ek - 13u) << 23);
#pragma unroll
    for (int q = 0; q < 4; ++q) {
        const int e = lane + 64 * q, at = (16 + (e >> 2)) * KROW + 24 - 8 * (e & 3);
        u32x4 h;
#pragma unroll
        for (int j = 0; j < 4; ++j) h[j] = pk2h(lo2f(uu[q][j]) * su, hi2f(uu[q][j]) * su);
        *(u32x4*)&L.uc[0][64 + 8 * e] = h;
        *(u32x4*)&L.ub[8 * e] = uu[q];
        *(u32x4*)&L.Kh[at] = kh[q];
    }
    BST(2);
    BST(3);
#pragma unroll
    for (int q = 0; q < 4; ++q) {
        const int e = lane + 64 * q;
        const int ue = 56 + 8 * e;
        const u32x4 a = *(const u32x4*)&L.uc[0][ue];
        const u32x2 b = *(const u32x2*)&L.uc[0][ue + 8];
        const unsigned s0 = __builtin_amdgcn_alignbit(a[1], a[0], 16), s1 = __builtin_amdgcn_alignbit(a[2], a[1], 16), s2 = __builtin_amdgcn_alignbit(a[3], a[2], 16),
                       s3 = __builtin_amdgcn_alignbit(b[0], a[3], 16), s4 = __builtin_amdgcn_alignbit(b[1], b[0], 16);
        *(u32x4*)&L.uc[1][ue] = u32x4{s0, s1, s2, s3};
        *(u32x4*)&L.uc[2][ue] = u32x4{a[1], a[2], a[3], b[0]};
        *(u32x4*)&L.uc[3][ue] = u32x4{s1, s2, s3, s4};
    }
    if (lane == 0) {
        const u32x4 a = *(const u32x4*)&L.uc[0][56 + SL];
        const unsigned s0 = __builtin_amdgcn_alignbit(a[1], a[0], 16), s1 = __builtin_amdgcn_alignbit(a[2], a[1], 16), s2 = __builtin_amdgcn_alignbit(a[3], a[2], 16), s3 = a[3] >> 16;
        *(u32x4*)&L.uc[1][56 + SL] = u32x4{s0, s1, s2, s3};
        *(u32x4*)&L.uc[2][56 + SL] = u32x4{a[1], a[2], a[3], 0u};
        *(u32x4*)&L.uc[3][56 + SL] = u32x4{s1, s2, s3, 0u};
    }
    BST(4);
    {
        const int o = 33 + c + 8 * g;
        const unsigned ua = (unsigned)(uintptr_t)&L.uc[o & 3][o & ~3], ka = (unsigned)(uintptr_t)&L.Kh[(16 + c) * KROW + 8 * g];
        const f32x4* res = (const f32x4*)L.Kh + lane;
        asm volatile(ASM_CV :: "v"(ua), "v"(ka), "v"((unsigned)(uintptr_t)res) : CV_CLOB);
        BST(5);
#pragma unroll
        for (int par = 0; par < 2; ++par)
#pragma unroll
            for (int t = 0; t < 4; ++t) {
                const int n = 16 * (par + 2 * (16 * t + c)) + 4 * g;
                const u32x2 uu = *(const u32x2*)&L.ub[n];
                const f32x4 y4 = res[(par * 4 + t) * 64] * fminf(ik, iu) * fmaxf(ik, iu);
                const float r0 = bfr(y4[0]) + bfr(lo2f(uu[0]) * dsk), r1 = bfr(y4[1]) + bfr(hi2f(uu[0]) * dsk);
                const float r2 = bfr(y4[2]) + bfr(lo2f(uu[1]) * dsk), r3 = bfr(y4[3]) + bfr(hi2f(uu[1]) * dsk);
                *(u32x2*)&L.uc[1][n] = u32x2{pk2bf(r0, r1), pk2bf(r2, r3)};
            }
    }
    __syncthreads();
    BST(6);
    u32x4 y[4];
#pragma unroll
    for (int ch = 0; ch < 4; ++ch) y[ch] = *(const u32x4*)&LL[ch].uc[1][8 * tid];
    const unsigned vt = tid * 16;
    char* yb = p.ws + G_D_YB + (size_t)(d0 >> 2) * 16384;
#pragma unroll
    for (int i = 0; i < 4; ++i) {
        const u32x4 o = {prm(y[1][i], y[0][i], 0x05040100u), prm(y[3][i], y[2][i], 0x05040100u), prm(y[1][i], y[0][i], 0x07060302u), prm(y[3][i], y[2][i], 0x07060302u)};
        char* yi = yb + i * 4096;
        WST16(vt, o, yi, 0);
    }
    BST(7);
    if (tid == 0 && more) nb = (int)(__hip_atomic_fetch_add((gu32*)(p.ws + CTLO + G_C_TASK + 128), 1u, RLX_AGENT) + 1u);
    VMC0();
    __syncthreads();
    if (tid == 0) __hip_atomic_fetch_add((gu32*)(p.ws + CTLO + G_C_YC + (d0 >> 6) * 128), 1u, RLX_AGENT);
    BST(8);
}
__device__ __forceinline__ void task_merge(const MainP& p, char* smem, int d0, int i, int w, int lane) {
    const unsigned vt = (w * 64 + lane) * 16;
    wait_word(p, p.ws + CTLO + G_C_YC + (d0 >> 6) * 128, 15, 31u);
    {
        const char* sb = p.ws + G_D_YB + (size_t)(d0 >> 6) * 262144 + i * 4096;
        u32x4 z[16];
#pragma unroll
        for (int j = 0; j < 16; ++j) { const char* sj = sb + j * 16384; WLD16(z[j], vt, sj, 0); }
        asm volatile("s_waitcnt vmcnt(0)" : "+v"(z[0]), "+v"(z[1]), "+v"(z[2]), "+v"(z[3]), "+v"(z[4]), "+v"(z[5]), "+v"(z[6]), "+v"(z[7]),
                     "+v"(z[8]), "+v"(z[9]), "+v"(z[10]), "+v"(z[11]), "+v"(z[12]), "+v"(z[13]), "+v"(z[14]), "+v"(z[15]) :: "memory");
        char* tw = smem + w * 18432;
#pragma unroll
        for (int j = 0; j < 8; ++j) {
            *(u32x4*)(tw + lane * 144 + j * 16) = u32x4{z[2 * j][0], z[2 * j][1], z[2 * j + 1][0], z[2 * j + 1][1]};
            *(u32x4*)(tw + (64 + lane) * 144 + j * 16) = u32x4{z[2 * j][2], z[2 * j][3], z[2 * j + 1][2], z[2 * j + 1][3]};
        }
#pragma unroll
        for (int k = 0; k < 16; ++k) {
            const int q = 8 * k + (lane >> 3);
            const int row = 8 * (64 * w + (q & 63)) + 2 * i + (q >> 6);
            *(u32x4*)(p.out + (size_t)row * OUTW + DM + (d0 & ~63) + 8 * (lane & 7)) = *(const u32x4*)(tw + q * 144 + (lane & 7) * 16);
        }
    }
}

static constexpr int NKG = 144, E_B = NKG + 36;
static constexpr int Q1 = 0, Q2 = Q1 + (NFINE - 2) * 25;
static constexpr int NTASK = E_B + Q2 + 68;

__device__ __forceinline__ void worker_body(const MainP& p, char* smem, const int w, const int lane, int quota) {
    const int tid = w * 64 + lane;
    int* slot = (int*)(smem + LDS_TOTAL - 16);
    int nb = -1;
    int nst = 0;
    gu32* qa = (gu32*)(p.ws + CTLO + G_C_TASK);
    for (;;) {
        if (tid == 0) {
            int t = E_B;
            if (quota > 0) {
                const int k = nb >= 0 ? nb : (int)(__hip_atomic_fetch_add(qa + 32, 1u, RLX_AGENT) + 1u);
                if (k < E_B) t = k;
                nb = -1;
            }
            if (t >= E_B) t = E_B + (int)(__hip_atomic_fetch_add(qa, 1u, RLX_AGENT) + 1u);
            *slot = t;
        }
        __syncthreads();
        const int t = __builtin_amdgcn_readfirstlane(*slot);
        if (t >= NTASK) break;
        quota = t < E_B ? quota - 1 : 0;
        if (t < E_B) {
            if (t < 128 || (t >= 144 && t < 160)) task_s4d(p, smem, 4 * (t < 128 ? t : t - 16), w, lane, nb, quota > 0);
            else task_merge(p, smem, 64 * ((t < 144 ? t - 128 : t - 144) >> 2), t & 3, w, lane);
        } else {
            const int u = t - E_B;
            const bool last2 = u >= Q2;
            const int sg = last2 ? 62 + (u - Q2) / 34 : NG1 + (u - Q1) / 25, r = last2 ? (u - Q2) % 34 : (u - Q1) % 25;
            if (r < 16) task_attn_tail(p, sg, 2 * r + (w >> 1), w, lane);
            else if (last2) task_out<2, 9, true>(p, smem, sg, 2 * (r - 16), w, lane);
            else task_out<4, 9, true>(p, smem, sg, 4 * (r - 16), w, lane);
            if (last2) break;
        }
        __syncthreads();
    }
}

__host__ __device__ constexpr unsigned phys(unsigned x) { return (x >> 13) * 32768 + (x & 8191); }
__host__ __device__ constexpr unsigned cbase(int c) { return (c >> 1) * 131072 + (c & 1) * 16384; }
__host__ __device__ constexpr unsigned hbase(int c) { return G_HOFF + (c >> 1) * 32768 + (c & 1) * 16384; }
__host__ __device__ constexpr unsigned aMB(int f, int b) { return phys(f < 8 ? 16384 + f * 1024 + b : 28672 + b); }
__host__ __device__ constexpr unsigned aPX(int pr, int b) { return phys(pr * 1024 + b); }
__host__ __device__ constexpr unsigned aPM(int u, int b) { return phys(24576 + u * 1024 + b); }
#define IST(k)

static constexpr int L_RW = 160000;
__device__ __forceinline__ int lane_id() { int l; asm volatile("v_mbcnt_lo_u32_b32 %0, -1, 0\n\tv_mbcnt_hi_u32_b32 %0, -1, %0" : "=v"(l)); return l; }
typedef const __attribute__((address_space(4))) MainP* KArg;

__global__ __launch_bounds__(256, 1) void main_kernel(MainP p_) {
    __shared__ __attribute__((aligned(16))) char smem[LDS_TOTAL];
    KArg ka = (KArg)__builtin_amdgcn_kernarg_segment_ptr();
    static_assert(offsetof(MainP, W) == 0 && offsetof(MainP, ws) == 0x20 && offsetof(MainP, X) == 0x28 && offsetof(MainP, prm) == 0x40, "argument layout");
    asm volatile(ASM_EA :: "s"(ka), "s"(blockIdx.x), "v"(threadIdx.x) : ASM_CLOB);
    asm volatile("" : "+s"(ka));
    const unsigned t00 = (unsigned)__builtin_amdgcn_s_memrealtime();
    const int b = blockIdx.x, x = b & 7, ix = b >> 3;
    const int w = __builtin_amdgcn_readfirstlane(threadIdx.x >> 6);
    const bool isr = x < 4 && ix >= 16;
    if (ix == (x < 4 ? 24 : 31) && (FLG(ka->prm[5]) & 8)) {
        const unsigned t = threadIdx.x;
        const char* pg = t < (G_D_END >> 21) ? ka->ws + (size_t)t * 0x200000 + 0x1FFF80 : t < 128 ? ka->ws : (const char*)(t < 192 ? ka->out : t < 224 ? ka->X : ka->U) + min((t & 31) * 0x100000u, t < 192 ? 4718588u : 2359292u);
        asm volatile("s_mov_b32 m0, %1\n\ts_nop 0\n\tglobal_load_lds_dword %0, off" :: "v"(pg), "s"(155648) : "memory");
    }
    if (x < 4 && ix >= 29) init_cal(ka->ws, smem, x * 3 + ix - 29, threadIdx.x, min((FLG(ka->prm[5]) >> 16) & 7, 5));
    if (x >= 4 ? ix >= 24 : (ix >= 25 && ix < 29)) init_fill(ka->ws, ka->mem0, x >= 4 ? (ix - 24) * 4 + (x & 3) : 32 + (ix - 25) * 4 + x, threadIdx.x);
    IST(0);
    const int NS = (FLG(ka->prm[5]) >> 8) & 3, NF = (FLG(ka->prm[5]) >> 21) & 7, RK = (FLG(ka->prm[5]) >> 10) & 3;
    const int NSP = G_RARM ? 7 : FLG2(ka->prm[7]) & 7;
    const int NA1 = 18 + 4 * NSP, NB1 = NA1 - 3 * max(NSP - 4, 0), N1 = RK ? NA1 + (RK - 1) * NB1 : 0, N2 = N1 + (28 - 4 * NSP) * NS, NKE = min(N2 + max(24 * NF - 8, 0), NKG);
    if (ix >= (x < 4 ? 16 : 24)) {
        MainP p;
        p.ws = ka->ws; p.logA = ka->logA; p.Aim = ka->Aim; p.Cre = ka->Cre; p.Cim = ka->Cim; p.logdt = ka->logdt;
        Pull pl = {nullptr, nullptr, nullptr, t00 + 400 + 50 * ((FLG(ka->prm[5]) >> 4) & 15)};
        const bool own = x < 4 && ix < 25 + NSP;
        int d0, nr, dd;
        if (own) {
            const int jt = ix < 25 ? ix - 16 : ix - 25, solo = jt < NSP;
            const int k = ix >= 25 ? 18 + NSP + 3 * jt + w - 1 : solo ? 3 * jt + w - 1 : NSP + 2 * jt + w - 2;
            d0 = k < NB1 ? x * NB1 + k : 4 * NB1 * RK + x * (NA1 - NB1) + k - NB1; nr = w < 2 - solo ? 0 : k < NB1 ? RK : min(RK, 1); dd = 4 * NB1;
            if (threadIdx.x < 8) ((unsigned*)(smem + L_RW))[threadIdx.x] = 0xffffffffu;
        } else if (x < 4) { d0 = 4 * (N1 + (ix - 25 - NSP) * 4 + x) + w; nr = NS; dd = 4 * (28 - 4 * NSP); }
        else {
            d0 = 4 * (N2 + (ix - 24) * 4 + (x & 3)) + w; nr = ix < 28 ? NF : ix < 30 ? max(NF - 1, 0) : 0; dd = 96;
            const size_t o = (size_t)((ix - 24) * 4 + (x & 3)) * 73728; pl.a = (const char*)ka->X + o; pl.b = (const char*)ka->U + o;
            pl.c = (const char*)((x & 2) ? ((x & 1) ? ka->Cim : ka->Cre) : ((x & 1) ? ka->Aim : ka->logA)) + (ix - 24) * 18432;
        }
        while (nr > 0 && d0 + dd * (nr - 1) >= 4 * NKG) --nr;
        KPar kn;
        if (nr > 0) kn = kgen_par(p, d0, threadIdx.x & 63);
        __builtin_amdgcn_sched_barrier(0);
        const unsigned ep = __hip_atomic_load((const gu32*)(p.ws + G_WS_EP), RLX_AGENT);
        asm volatile("s_waitcnt vmcnt(0)" :: "v"(ep) : "memory");
        __syncthreads();
        if (x >= 4 && ix == 24) {
            const char* xs = (const char*)ka->X + threadIdx.x * 128;
#pragma unroll
            for (int i = 0; i < 7; ++i) asm volatile("s_mov_b32 m0, %1\n\ts_nop 0\n\tglobal_load_lds_dword %0, off" :: "v"(xs + i * 32768), "s"(155648) : "memory");
        }
        {
            const int q = x < 4 ? ix - 25 : ix - 26;
            if (q >= 0 && q < 4 && (FLG(ka->prm[5]) & 1)) {
                const int hs = (x & 3) * 8 + 4 * (q & 1) + ((1 - (int)((u64)ka->W[q & 2] >> 8)) & 3);
                const char* src = (q & 2) ? (const char*)ka->W[3] + (size_t)hs * 147456 + threadIdx.x * 128 : (const char*)ka->W[x < 4] + hs * 256 + (threadIdx.x >> 1) * 8192 + (threadIdx.x & 1) * 128;
                const unsigned st = (q & 2) ? 32768u : 1048576u;
#pragma unroll
                for (int i = 0; i < 5; ++i)
                    if (i < 4 || w < 2) {
                        if (FLG(ka->prm[5]) & 4) asm volatile("s_mov_b32 m0, %1\n\ts_nop 0\n\tglobal_load_lds_dword %0, off nt" :: "v"(src + (size_t)i * st), "s"(155648) : "memory");
                        else asm volatile("s_mov_b32 m0, %1\n\ts_nop 0\n\tglobal_load_lds_dword %0, off" :: "v"(src + (size_t)i * st), "s"(155648) : "memory");
                    }
            }
        }
        if (nr == 0) init_seen(p.ws, threadIdx.x & 63, ep);
#pragma unroll 1
        for (int r = 0; r < nr; ++r) {
            const KPar kp = kn;
            kn = kgen_par(p, d0 + dd * min(r + 1, nr - 1), threadIdx.x & 63);
            __builtin_amdgcn_sched_barrier(0);
            task_kgen(p, smem, d0 + dd * r, w, threadIdx.x & 63, kp, r == 0, ep, pl);
        }
        if (w == 0) init_count(p.ws, threadIdx.x & 63);
        while (pl.a) { pull_inputs(pl, w, false); __builtin_amdgcn_s_sleep(8); }
        if (x < 4 && !own) return;
        asm volatile("" : "+s"(ka));
    }
    if (isr) {
        const int lane = threadIdx.x & 63, g = lane >> 4, c = lane & 15;
        const int jt = ix < 25 ? ix - 16 : ix - 25, solo = jt < NSP, par = solo ? ix >= 25 : w;
        const unsigned lb = w * G_STG;
        unsigned* ct = (unsigned*)(smem + lb) + lane;
        asm volatile("" :: "v"(ct) : "memory");
#define CTW(k, val) ct[(k) * 64] = (unsigned)(val)
        const u64 ws = (u64)ka->ws, prm = (u64)ka + 0x40;
        const u64 stb = 0;
        IST(1);
        CTW(0, lane * 16);
        const int j = x * 9 + jt;
        const int rot = (jt * 16) / 9;
        if (w >= 2 - solo) return;
        CTW(1, lane * 16 + 4096);
        CTW(2, aMB(j >> 2, (j & 3) * 256 + (4 * g + 2 * ((c >> 1) & 1) + (c & 1)) * 16 + (c >> 2) * 4));
        for (int k = 0; k < (int)G_NARM; ++k) {
            constexpr int N0 = G_HKB, N1 = N0 + (9 * G_RM + 3) / 4, N2 = N1 + 5 * G_RP;
            constexpr int NO = G_RARM ? 7 : 9;
            static_assert(N2 <= NO * G_NARM && G_NARM <= 5, "shares");
            const int q = min(jt + NO * k, N2 - 1), qm = min(x * (N1 - N0) + q - N0, (int)(9 * G_RM) - 1), qp = x * 5 * G_RP + q - N1;
            const int cp = q < N0 ? 4 + x : q < N1 ? qm / 9 : qp / 20, kb = q < N0 ? q : q < N1 ? (qm % 9 == 8 ? 28 : 16 + qm % 9) : (qp % 20 < 16 ? qp % 20 : 8 + qp % 20);
            CTW(47 + k, (cp >= 4 ? hbase(x) : cbase(cp)) + phys(kb * 1024 + lane * 16));
        }
        for (int k = 0; k < 3; ++k) { const int q = min(jt + 7 * k, 15); CTW(52 + k, (x * 9 + 7 + (q >> 3)) * 16384 + (q & 7) * 1024 + lane * 16); }
        for (int k = 0; k < 2; ++k) { const int L = min(jt * 74 + min(k * 64 + lane, 73), 511), tl = x * 9 + 7 + (L >> 8); CTW(55 + k, G_D_UX + tl * G_UXS + skew(0, tl) * 32768 + (L & 255) * 128); }
        const unsigned pc = 0;
        CTW(3, hbase(lane & 3) + G_RHO + ((lane >> 2) & 1) * G_RHS + j * 32); CTW(46, hbase(x & 2) + G_RHO + min(lane, 35) * 32);
        CTW(4, (c & 2) ? 0xEEEE : 0x4444); CTW(5, (c & 1) ? 0x03020706u : 0x05040100u);
        CTW(6, G_GVC ? G_WS_CTL + G_C_GV + G_GVH + x * 2048 + (par * 32 + (lane & 31)) * 32 : G_WS_CTL + G_C_GV + (par * 32 + (lane & 31)) * 128); CTW(7, hbase(x & 2) + (lane & 31) * G_HP); CTW(8, lane * 4); CTW(9, L_RW);
        for (int k = 0; k < 16; ++k) { const int i = (k + rot) & 15; CTW(10 + k, pc + aPX(i, lane * 16)); CTW(26 + k, i * 2048 + lane * 16); }
        for (int k = 0; k < 4; ++k) CTW(42 + k, pc + aPM(k, lane * 16));
        IST(2);
        asm volatile(ASM_R :: "s"(ws), "s"(ws), "s"((u64)(t00 << 4)), "s"(__builtin_amdgcn_readfirstlane(par)), "s"(__builtin_amdgcn_readfirstlane(j)), "s"((u64)__builtin_amdgcn_readfirstlane(2 * rot) + ((u64)__builtin_amdgcn_readfirstlane(solo) << 32)), "s"(prm), "s"(lb), "s"(stb) : ASM_CLOB);
    }
    asm volatile("" : "+s"(ka));
    MainP p;
    p.prm[5] = ka->prm[5]; p.prm[7] = ka->prm[7];
    p.U = ka->U; p.ws = ka->ws; p.logA = ka->logA; p.Aim = ka->Aim; p.Cre = ka->Cre; p.Cim = ka->Cim; p.logdt = ka->logdt; p.Dskip = ka->Dskip; p.out = ka->out;
    {
        auto cal = [&](int k) { return __hip_atomic_load((const gu32*)(p.ws + G_WS_DONE + k * 64 + 4), RLX_AGENT); };
        p.prm[0] = __builtin_amdgcn_readfirstlane((min(cal(5), cal(11)) < min(cal(4), cal(10)) ? 8192 : 0) ^ G_CTF);
    }
    const bool s4 = ix < 22 - ((FLG(p.prm[5]) >> 12) & 15);
    if (s4 && ix >= 8 && ix < 24) for (int i = 0; i < (int)(((ix & 7) * 4 + (x & 3)) * ((FLG(p.prm[5]) >> 24) & 3)); ++i) __builtin_amdgcn_s_sleep(3);
    if (x >= 4 && ix >= 8 && ix < 22) {
#pragma unroll 1
        for (int q = NKE + (ix - 8) * 4 + (x & 3); q < NKG; q += 56) {
            Pull pl = {nullptr, nullptr, nullptr, 0u};
            task_kgen(p, smem, 4 * q + w, w, lane_id(), kgen_par(p, 4 * q + w, lane_id()), false, 0u, pl);
        }
    }
    if (ix >= 24 && ix < 30) {
#pragma unroll 1
        for (int rb = (ix - 24) * 4 + (x & 3); rb < 32; rb += 8) {
            __syncthreads();
            task_ut(p, smem, rb, w, lane_id());
            if (ix < 28) break;
        }
        __syncthreads();
        const int lane = lane_id(), g = lane >> 4, c = lane & 15, ct0 = 6 * (ix - 24), s0 = x & 3;
        const unsigned lb = w * G_STG;
        unsigned* ct = (unsigned*)(smem + lb) + lane;
        asm volatile("" :: "v"(ct) : "memory");
        for (int t = 0; t < 6; ++t) CTW(t, (ct0 + t) * G_UXS + skew(0, ct0 + t) * 32768 + w * 8192 + 4096 + lane * 16);
        CTW(6, w * 16384 + 4096 + lane * 16); CTW(7, w * 16384 + 12288 + lane * 16); CTW(8, w * 2048 + lane * 16); CTW(9, w * 2048 + 8192 + lane * 16);
        CTW(10, umx_base(w * 8 + g) + ct0 * 256 + c * 16); CTW(11, umx_base(w * 8 + 4 + g) + ct0 * 256 + c * 16);
        const unsigned fa = lane < 8 ? CTLO + G_C_UM + (w * 8 + lane) * 128 : CTLO + G_C_GV + ((s0 & 1) * 32 + w * 8 + (lane & 7)) * 128;
        CTW(12, lane < 16 ? fa : attn_flag(p, s0, w * 2 + (lane & 1))); CTW(13, lane < 16 ? fa : attn_flag(p, s0 + 60, w * 2 + (lane & 1)));
        CTW(14, w * 12288 + lane * 16); CTW(15, w * 1024 + lane * 16);
        CTW(16, (16 * (w & 1) + c) * (OUTW * 2) + (16 * ct0 + 16 * (w >> 1) + 4 * g) * 2); CTW(17, lane * 4);
        static_assert(NG1 == 58, "as in the generated block");
        const u64 ws = (u64)p.ws;
        const u64 stb = 0;
        asm volatile(ASM_O :: "s"(ws), "s"((u64)p.out), "s"(ws), "s"(w), "s"(s0), "s"(ws), "s"(ws), "s"(lb), "s"(stb) : ASM_CLOB);
        asm volatile("" : "+s"(ka));
        p.prm[5] = ka->prm[5]; p.prm[7] = ka->prm[7];
        p.U = ka->U; p.ws = ka->ws; p.logA = ka->logA; p.Aim = ka->Aim; p.Cre = ka->Cre; p.Cim = ka->Cim; p.logdt = ka->logdt; p.Dskip = ka->Dskip; p.out = ka->out;
    }
    if ((ix & 22) == 22 && x >= 4) { attn_role(p, ix >= 30 ? 0 : NA + 1, (x & 3) * 8 + (ix & 1) * 4 + w, w, lane_id());
        if (b == 255) {
            wait_word(p, p.ws + G_WS_CTL + G_C_ARR, 255, 50u);
            if (w == 0 && lane_id() == 0) __hip_atomic_fetch_add((gu32*)(p.ws + G_WS_EP), 1u, RLX_AGENT);
        }
        __syncthreads();
    }
    worker_body(p, smem, w, lane_id(), s4 ? 1000 : 0);
}

extern "C" __global__ void code_end_marker() {}

static constexpr int g_prm[8] = {PRM_DEFAULT};

extern "C" void kernel_launch(void* const* d_in, const int* in_sizes, int n_in, void* d_out, int out_size,
                              void* d_ws, size_t ws_size, hipStream_t stream) {
    char* ws = (char*)(((uintptr_t)d_ws + 0x1FFFFF) & ~(uintptr_t)0x1FFFFF);
    MainP p;
    for (int i = 0; i < 8; ++i) p.prm[i] = g_prm[i];
    p.mem0 = (const u16*)d_in[1];
    for (int i = 0; i < 4; ++i) p.W[i] = (const u16*)d_in[2 + i];
    p.X = (const u16*)d_in[0]; p.U = (const u16*)d_in[7]; p.ws = ws;
    p.logA = (const u16*)d_in[8]; p.Aim = (const u16*)d_in[9]; p.Cre = (const u16*)d_in[10]; p.Cim = (const u16*)d_in[11];
    p.logdt = (const u16*)d_in[12]; p.Dskip = (const u16*)d_in[13]; p.out = (u16*)d_out;
    main_kernel<<<256, 256, 0, stream>>>(p);
}
```

```cpp
#include <hip/hip_runtime.h>
#include <hip/hip_bf16.h>
#include <stdint.h>
#include <stddef.h>

typedef __attribute__((ext_vector_type(8))) __bf16 bf16x8;
typedef __attribute__((ext_vector_type(4))) short s16x4;
typedef __attribute__((ext_vector_type(4))) float f32x4;
typedef __attribute__((ext_vector_type(4))) unsigned u32x4;
typedef __attribute__((ext_vector_type(2))) unsigned u32x2;
typedef __attribute__((ext_vector_type(2))) float f32x2;
typedef unsigned short u16;
typedef unsigned long long u64;

#define GAS __attribute__((address_space(1)))
typedef GAS unsigned gu32;
#define RLX_AGENT __ATOMIC_RELAXED, __HIP_MEMORY_SCOPE_AGENT

static constexpr int DM = 576, NH = 32, DH = 128, MM = 8, SEG = 32, NSEG = 64, SL = 2048;
static constexpr int HD = NH * DH;
static constexpr int OUTW = 2 * DM;
static constexpr int NST = 128;

__device__ __forceinline__ float bf2f(u16 h) { return __uint_as_float(((unsigned)h) << 16); }
typedef __attribute__((ext_vector_type(2))) __bf16 bf16x2_t;
__device__ __forceinline__ unsigned pk2bf(float a, float b) {
    bf16x2_t v; v[0] = (__bf16)a; v[1] = (__bf16)b;
    return __builtin_bit_cast(unsigned, v);
}
__device__ __forceinline__ float bfr(float f) { return __uint_as_float(pk2bf(f, f) << 16); }
__device__ __forceinline__ float lo2f(unsigned u) { return __uint_as_float(u << 16); }
__device__ __forceinline__ float hi2f(unsigned u) { return __uint_as_float(u & 0xffff0000u); }
__device__ __forceinline__ float rowmax4(float x) {
    auto a = __builtin_amdgcn_permlane16_swap(__float_as_uint(x), __float_as_uint(x), false, false);
    x = fmaxf(__uint_as_float(a[0]), __uint_as_float(a[1]));
    auto b = __builtin_amdgcn_permlane32_swap(__float_as_uint(x), __float_as_uint(x), false, false);
    return fmaxf(__uint_as_float(b[0]), __uint_as_float(b[1]));
}
__device__ __forceinline__ float rowsum4(float x) {
    auto a = __builtin_amdgcn_permlane16_swap(__float_as_uint(x), __float_as_uint(x), false, false);
    x = __uint_as_float(a[0]) + __uint_as_float(a[1]);
    auto b = __builtin_amdgcn_permlane32_swap(__float_as_uint(x), __float_as_uint(x), false, false);
    return __uint_as_float(b[0]) + __uint_as_float(b[1]);
}

struct MainP {
    const u16* W[4];
    char* ws; const u16* X; const u16* mem0; const u16* U;
    int prm[8];
    const u16* logA; const u16* Aim; const u16* Cre; const u16* Cim; const u16* logdt; const u16* Dskip;
    u16* out;
};
static constexpr int LDS_TOTAL = 163840;
static constexpr unsigned kWaitCap = 1u << 18;
#define VMC0() asm volatile("s_waitcnt vmcnt(0)" ::: "memory")
#define PRM_DEFAULT 1050, 600, 118, 665, 760, 1615006531, 15728816, 3143
#define ASM_EA \
    "s_load_dwordx8 s[40:47], %0, 0\n\t" \
    "s_load_dwordx4 s[36:39], %0, 0x20\n\t" \
    "s_load_dwordx8 s[84:91], %0, 0x40\n\t" \
    "s_getpc_b64 s[54:55]\n\t" \
    "ea_pc_%=:\n\t" \
    "s_bfe_u32 s69, %1, 0x20003\n\t" \
    "s_lshl_b32 s69, s69, 15\n\t" \
    "v_lshl_add_u32 v1, %2, 7, s69\n\t" \
    "v_min_u32 v1, code_end_marker-ea_pc_%=, v1\n\t" \
    "s_mov_b32 m0, 155648\n\t" \
    "v_readfirstlane_b32 s66, %2\n\t" \
    "global_load_lds_dword v1, s[54:55]\n\t" \
    "s_bitcmp1_b32 %1, 2\n\t" \
    "s_cselect_b32 s69, 24, 16\n\t" \
    "s_lshr_b32 s70, %1, 3\n\t" \
    "s_cmp_ge_u32 s70, s69\n\t" \
    "s_cbranch_scc1 ea_done_%=\n\t" \
    "s_lshr_b32 s66, s66, 6\n\t" \
    "s_and_b32 s69, %1, 0xc4\n\t" \
    "s_cmp_eq_u32 s69, 4\n\t" \
    "s_cselect_b32 s69, 72, 36\n\t" \
    "s_add_u32 s82, s66, s69\n\t" \
    "s_lshl_b32 s82, s82, 10\n\t" \
    "s_mov_b32 m0, s82\n\t" \
    "s_and_b32 s67, %1, 3\n\t" \
    "s_lshl_b32 s67, s67, 3\n\t" \
    "s_and_b32 s69, s70, 7\n\t" \
    "s_add_u32 s67, s67, s69\n\t" \
    "s_lshl_b32 s69, s67, 8\n\t" \
    "v_lshrrev_b32 v1, 4, %2\n\t" \
    "v_lshrrev_b32 v2, 3, %2\n\t" \
    "v_and_b32 v2, 6, v2\n\t" \
    "v_and_or_b32 v2, v1, 8, v2\n\t" \
    "v_xor_b32 v2, v2, %2\n\t" \
    "v_and_b32 v2, 15, v2\n\t" \
    "v_lshl_add_u32 v2, v2, 4, s69\n\t" \
    "v_lshl_add_u32 v200, v1, 13, v2\n\t" \
    "s_mov_b32 s71, 16\n\t" \
    "s_cmp_eq_u32 0, 0\n\t" \
    "s_waitcnt lgkmcnt(0)\n\t" \
    "s_cselect_b64 s[48:49], s[42:43], s[40:41]\n\t" \
    "s_cselect_b64 s[40:41], s[40:41], s[42:43]\n\t" \
    "s_cmp_ge_u32 s70, 8\n\t" \
    "s_cselect_b64 s[42:43], s[44:45], s[48:49]\n\t" \
    "s_cselect_b64 s[44:45], s[46:47], s[40:41]\n\t" \
    "s_cbranch_scc1 ea_nd2_%=\n\t" \
    "s_lshr_b32 s69, s42, 8\n\t" \
    "s_add_u32 s69, s69, s67\n\t" \
    "s_bitcmp1_b32 0xa2222222, s69\n\t" \
    "s_cbranch_scc1 ea_nd2_%=\n\t" \
    "s_bitcmp1_b32 %1, 2\n\t" \
    "s_cselect_b32 s69, 7, 8\n\t" \
    "ea_dl3_%=:\n\t" \
    "s_sub_u32 s69, s69, 1\n\t" \
    "s_cbranch_scc1 ea_nd2_%=\n\t" \
    "s_sleep 9\n\t" \
    "s_branch ea_dl3_%=\n\t" \
    "ea_nd2_%=:\n\t" \
    "ea_lp1_%=:\n\t" \
    "global_load_lds_dwordx4 v200, s[42:43]\n\t" \
    "v_add_u32 v200, 131072, v200\n\t" \
    "s_add_u32 m0, m0, 4096\n\t" \
    "s_sub_u32 s71, s71, 1\n\t" \
    "s_cmp_lg_u32 s71, 0\n\t" \
    "s_cbranch_scc1 ea_lp1_%=\n\t" \
    "s_mov_b64 s[50:51], s[38:39]\n\t" \
    "s_cmp_lt_u32 s70, 8\n\t" \
    "s_cbranch_scc1 ea_w_%=\n\t" \
    "s_mul_i32 s69, s67, 147456\n\t" \
    "s_add_u32 s44, s44, s69\n\t" \
    "s_addc_u32 s45, s45, 0\n\t" \
    "s_and_b32 s69, s70, 16\n\t" \
    "s_lshl_b32 s69, s69, 1\n\t" \
    "s_add_u32 s67, s67, s69\n\t" \
    "s_bitcmp1_b32 %1, 2\n\t" \
    "s_cbranch_scc1 gv_begin_%=\n\t" \
    "s_branch vv_begin_%=\n\t" \
    "ea_w_%=:\n\t" \
    "s_bitcmp1_b32 %1, 2\n\t" \
    "s_cbranch_scc1 gx_begin_%=\n\t" \
    "qk_begin_%=:\n\t" \
    "v_mbcnt_lo_u32_b32 v223, -1, 0\n\t" \
    "v_mbcnt_hi_u32_b32 v223, -1, v223\n\t" \
    "s_mov_b32 s68, 262144\n\t" \
    "s_mov_b32 s72, 0\n\t" \
    "s_mov_b32 s83, -1\n\t" \
    "s_mov_b32 s65, 0\n\t" \
    "v_and_b32 v180, 3, v223\n\t" \
    "v_lshlrev_b32 v202, 3, v180\n\t" \
    "v_lshrrev_b32 v181, 5, v223\n\t" \
    "v_lshl_add_u32 v202, v181, 12, v202\n\t" \
    "v_bfe_u32 v182, v223, 2, 2\n\t" \
    "s_mov_b32 s70, 288\n\t" \
    "v_mad_u32_u24 v202, v182, s70, v202\n\t" \
    "v_bfe_u32 v183, v223, 4, 1\n\t" \
    "s_mov_b32 s70, 2176\n\t" \
    "v_mad_u32_u24 v202, v183, s70, v202\n\t" \
    "s_lshl_b32 s69, s66, 6\n\t" \
    "v_xor_b32 v202, s69, v202\n\t" \
    "v_xor_b32 v203, 32, v202\n\t" \
    "v_lshlrev_b32 v0, 4, v223\n\t" \
    "s_mul_i32 s69, s66, 2048\n\t" \
    "s_bfe_u32 s71, s67, 0x10004\n\t" \
    "s_mul_i32 s71, s71, 131072\n\t" \
    "s_add_u32 s69, s69, s71\n\t" \
    "s_add_u32 s69, s69, 65536\n\t" \
    "v_lshl_add_u32 v1, v223, 4, s69\n\t" \
    "s_bfe_u32 s71, s67, 0x10004\n\t" \
    "s_mul_i32 s69, s71, 131072\n\t" \
    "s_add_u32 s69, s69, 102400\n\t" \
    "v_lshl_add_u32 v2, v223, 4, s69\n\t" \
    "v_xor_b32 v34, 131072, v1\n\t" \
    "s_mov_b32 s69, 17476\n\t" \
    "v_bfe_u32 v184, v223, 1, 1\n\t" \
    "v_mov_b32 v10, s69\n\t" \
    "s_mov_b32 s70, 43690\n\t" \
    "v_mad_u32_u24 v10, v184, s70, v10\n\t" \
    "v_mov_b32 v11, 0\n\t" \
    "s_waitcnt vmcnt(12) lgkmcnt(0)\n\t" \
    "s_barrier\n\t" \
    "s_add_u32 m0, s82, 65536\n\t" \
    "s_nop 0\n\t" \
    "global_load_lds_dwordx4 v200, s[42:43]\n\t" \
    "v_add_u32 v200, 131072, v200\n\t" \
    "s_add_u32 m0, s82, 69632\n\t" \
    "s_nop 0\n\t" \
    "global_load_lds_dwordx4 v200, s[42:43]\n\t" \
    "v_add_u32 v200, 131072, v200\n\t" \
    "s_add_u32 m0, s82, 73728\n\t" \
    "s_nop 0\n\t" \
    "global_load_lds_dwordx4 v200, s[42:43]\n\t" \
    "v_add_u32 v200, 131072, v200\n\t" \
    "s_add_u32 m0, s82, 77824\n\t" \
    "s_nop 0\n\t" \
    "global_load_lds_dwordx4 v200, s[42:43]\n\t" \
    "v_add_u32 v200, 131072, v200\n\t" \
    "ds_read_b64_tr_b16 a[144:145], v202 offset:36864\n\t" \
    "ds_read_b64_tr_b16 a[146:147], v202 offset:37888\n\t" \
    "ds_read_b64_tr_b16 a[148:149], v202 offset:45056\n\t" \
    "ds_read_b64_tr_b16 a[150:151], v202 offset:46080\n\t" \
    "ds_read_b64_tr_b16 a[216:217], v203 offset:36864\n\t" \
    "ds_read_b64_tr_b16 a[218:219], v203 offset:37888\n\t" \
    "ds_read_b64_tr_b16 a[220:221], v203 offset:45056\n\t" \
    "ds_read_b64_tr_b16 a[222:223], v203 offset:46080\n\t" \
    "s_mul_i32 s69, s66, 512\n\t" \
    "s_add_u32 s69, s69, 16384\n\t" \
    "v_and_b32 v185, 13, v223\n\t" \
    "v_lshl_add_u32 v12, v185, 5, s69\n\t" \
    "v_lshl_add_u32 v12, v184, 2, v12\n\t" \
    "v_lshrrev_b32 v186, 4, v223\n\t" \
    "v_lshl_add_u32 v12, v186, 3, v12\n\t" \
    "s_mov_b32 s69, 16384\n\t" \
    "v_and_b32 v187, 15, v223\n\t" \
    "v_lshl_add_u32 v13, v187, 3, s69\n\t" \
    "v_lshl_add_u32 v13, v186, 8, v13\n\t" \
    "s_bfe_u32 s71, s67, 0x10004\n\t" \
    "s_mul_i32 s69, s71, 32768\n\t" \
    "s_add_u32 s69, s69, 266240\n\t" \
    "v_and_b32 v188, 31, v223\n\t" \
    "v_lshl_add_u32 v20, v188, 5, s69\n\t" \
    "v_lshl_add_u32 v20, v181, 2, v20\n\t" \
    "s_mul_i32 s69, s67, 32\n\t" \
    "s_add_u32 s69, s69, 262144\n\t" \
    "v_lshl_add_u32 v21, v180, 14, s69\n\t" \
    "v_bfe_u32 v189, v223, 2, 3\n\t" \
    "v_lshlrev_b32 v199, 10, v189\n\t" \
    "v_min_u32 v199, 4096, v199\n\t" \
    "v_add_u32 v21, v199, v21\n\t" \
    "s_bfe_u32 s71, s67, 0x10004\n\t" \
    "s_mul_i32 s69, s71, 32768\n\t" \
    "s_add_u32 s69, s69, 267264\n\t" \
    "v_min_u32 v35, 35, v223\n\t" \
    "v_lshl_add_u32 v35, v35, 5, s69\n\t" \
    "v_lshlrev_b32 v22, 2, v223\n\t" \
    "s_waitcnt vmcnt(12) lgkmcnt(0)\n\t" \
    "s_barrier\n\t" \
    "s_add_u32 m0, s82, 81920\n\t" \
    "s_nop 0\n\t" \
    "global_load_lds_dwordx4 v200, s[42:43]\n\t" \
    "v_add_u32 v200, 131072, v200\n\t" \
    "s_add_u32 m0, s82, 86016\n\t" \
    "s_nop 0\n\t" \
    "global_load_lds_dwordx4 v200, s[42:43]\n\t" \
    "v_add_u32 v200, 131072, v200\n\t" \
    "s_add_u32 m0, s82, 90112\n\t" \
    "s_nop 0\n\t" \
    "global_load_lds_dwordx4 v200, s[42:43]\n\t" \
    "v_add_u32 v200, 131072, v200\n\t" \
    "s_add_u32 m0, s82, 94208\n\t" \
    "s_nop 0\n\t" \
    "global_load_lds_dwordx4 v200, s[42:43]\n\t" \
    "v_add_u32 v200, 131072, v200\n\t" \
    "ds_read_b64_tr_b16 a[152:153], v202 offset:53248\n\t" \
    "ds_read_b64_tr_b16 a[154:155], v202 offset:54272\n\t" \
    "ds_read_b64_tr_b16 a[156:157], v202 offset:61440\n\t" \
    "ds_read_b64_tr_b16 a[158:159], v202 offset:62464\n\t" \
    "ds_read_b64_tr_b16 a[224:225], v203 offset:53248\n\t" \
    "ds_read_b64_tr_b16 a[226:227], v203 offset:54272\n\t" \
    "ds_read_b64_tr_b16 a[228:229], v203 offset:61440\n\t" \
    "ds_read_b64_tr_b16 a[230:231], v203 offset:62464\n\t" \
    "s_mul_i32 s69, s67, 128\n\t" \
    "s_add_u32 s69, s69, 6127616\n\t" \
    "v_mov_b32 v14, s69\n\t" \
    "v_and_b32 v190, 57, v223\n\t" \
    "v_lshlrev_b32 v23, 4, v190\n\t" \
    "v_lshl_add_u32 v23, v184, 6, v23\n\t" \
    "v_bfe_u32 v191, v223, 2, 1\n\t" \
    "v_lshl_add_u32 v23, v191, 5, v23\n\t" \
    "v_add_u32 v15, 4096, v23\n\t" \
    "s_mul_i32 s69, s66, 512\n\t" \
    "s_add_u32 s69, s69, 8192\n\t" \
    "v_lshl_add_u32 v16, v187, 4, s69\n\t" \
    "v_lshl_add_u32 v16, v186, 2, v16\n\t" \
    "s_mov_b32 s69, 8192\n\t" \
    "v_and_b32 v192, 1, v223\n\t" \
    "v_lshl_add_u32 v17, v192, 3, s69\n\t" \
    "v_lshrrev_b32 v193, 2, v223\n\t" \
    "v_lshl_add_u32 v17, v193, 4, v17\n\t" \
    "s_mul_i32 s69, s67, 128\n\t" \
    "s_add_u32 s69, s69, 6160384\n\t" \
    "v_mov_b32 v24, s69\n\t" \
    "s_waitcnt vmcnt(12) lgkmcnt(0)\n\t" \
    "s_barrier\n\t" \
    "s_add_u32 m0, s82, 98304\n\t" \
    "s_nop 0\n\t" \
    "global_load_lds_dwordx4 v200, s[42:43]\n\t" \
    "v_add_u32 v200, 131072, v200\n\t" \
    "s_add_u32 m0, s82, 102400\n\t" \
    "s_nop 0\n\t" \
    "global_load_lds_dwordx4 v200, s[42:43]\n\t" \
    "v_add_u32 v200, 131072, v200\n\t" \
    "s_add_u32 m0, s82, 106496\n\t" \
    "s_nop 0\n\t" \
    "global_load_lds_dwordx4 v200, s[42:43]\n\t" \
    "v_add_u32 v200, 131072, v200\n\t" \
    "s_add_u32 m0, s82, 110592\n\t" \
    "s_nop 0\n\t" \
    "global_load_lds_dwordx4 v200, s[42:43]\n\t" \
    "v_add_u32 v200, 131072, v200\n\t" \
    "v_add_u32 v202, 69632, v202\n\t" \
    "v_add_u32 v203, 69632, v203\n\t" \
    "ds_read_b64_tr_b16 a[160:161], v202 offset:0\n\t" \
    "ds_read_b64_tr_b16 a[162:163], v202 offset:1024\n\t" \
    "ds_read_b64_tr_b16 a[164:165], v202 offset:8192\n\t" \
    "ds_read_b64_tr_b16 a[166:167], v202 offset:9216\n\t" \
    "ds_read_b64_tr_b16 a[232:233], v203 offset:0\n\t" \
    "ds_read_b64_tr_b16 a[234:235], v203 offset:1024\n\t" \
    "ds_read_b64_tr_b16 a[236:237], v203 offset:8192\n\t" \
    "ds_read_b64_tr_b16 a[238:239], v203 offset:9216\n\t" \
    "s_bfe_u32 s71, s66, 0x10000\n\t" \
    "s_mul_i32 s69, s71, 128\n\t" \
    "s_bfe_u32 s71, s67, 0x40000\n\t" \
    "s_mul_i32 s71, s71, 512\n\t" \
    "s_add_u32 s69, s69, s71\n\t" \
    "s_bfe_u32 s71, s67, 0x10004\n\t" \
    "s_mul_i32 s71, s71, 32768\n\t" \
    "s_add_u32 s69, s69, s71\n\t" \
    "v_lshl_add_u32 v18, v180, 2, s69\n\t" \
    "v_lshl_add_u32 v18, v191, 5, v18\n\t" \
    "v_bfe_u32 v194, v223, 3, 1\n\t" \
    "v_lshl_add_u32 v18, v194, 8, v18\n\t" \
    "v_lshl_add_u32 v18, v183, 4, v18\n\t" \
    "v_lshl_add_u32 v18, v181, 6, v18\n\t" \
    "s_bfe_u32 s71, s66, 0x10000\n\t" \
    "s_mul_i32 s69, s71, 128\n\t" \
    "s_bfe_u32 s71, s67, 0x10000\n\t" \
    "s_mul_i32 s71, s71, 8\n\t" \
    "s_add_u32 s69, s69, s71\n\t" \
    "s_bfe_u32 s71, s67, 0x40001\n\t" \
    "s_mul_i32 s71, s71, 256\n\t" \
    "s_add_u32 s69, s69, s71\n\t" \
    "s_add_u32 s69, s69, 98304\n\t" \
    "v_lshl_add_u32 v19, v184, 5, s69\n\t" \
    "v_and_b32 v195, 5, v193\n\t" \
    "v_lshl_add_u32 v19, v195, 2, v19\n\t" \
    "v_lshl_add_u32 v19, v181, 6, v19\n\t" \
    "v_add_u32 v3, 16384, v18\n\t" \
    "v_add_u32 v4, 16384, v19\n\t" \
    "s_waitcnt vmcnt(12) lgkmcnt(0)\n\t" \
    "s_barrier\n\t" \
    "s_add_u32 m0, s82, 0\n\t" \
    "s_nop 0\n\t" \
    "global_load_lds_dwordx4 v200, s[42:43]\n\t" \
    "v_add_u32 v200, 131072, v200\n\t" \
    "s_add_u32 m0, s82, 4096\n\t" \
    "s_nop 0\n\t" \
    "global_load_lds_dwordx4 v200, s[42:43]\n\t" \
    "v_add_u32 v200, 131072, v200\n\t" \
    "s_add_u32 m0, s82, 8192\n\t" \
    "s_nop 0\n\t" \
    "global_load_lds_dwordx4 v200, s[42:43]\n\t" \
    "v_add_u32 v200, 131072, v200\n\t" \
    "s_add_u32 m0, s82, 12288\n\t" \
    "s_nop 0\n\t" \
    "global_load_lds_dwordx4 v200, s[42:43]\n\t" \
    "v_add_u32 v200, 131072, v200\n\t" \
    "ds_read_b64_tr_b16 a[168:169], v202 offset:16384\n\t" \
    "ds_read_b64_tr_b16 a[170:171], v202 offset:17408\n\t" \
    "ds_read_b64_tr_b16 a[172:173], v202 offset:24576\n\t" \
    "ds_read_b64_tr_b16 a[174:175], v202 offset:25600\n\t" \
    "ds_read_b64_tr_b16 a[240:241], v203 offset:16384\n\t" \
    "ds_read_b64_tr_b16 a[242:243], v203 offset:17408\n\t" \
    "ds_read_b64_tr_b16 a[244:245], v203 offset:24576\n\t" \
    "ds_read_b64_tr_b16 a[246:247], v203 offset:25600\n\t" \
    "s_mov_b32 s77, 3019898880\n\t" \
    "s_mov_b32 s78, 3054198966\n\t" \
    "s_mov_b32 s71, 3\n\t" \
    "qk_E1_%=:\n\t" \
    "s_memtime s[54:55]\n\t" \
    "s_nop 0\n\t" \
    "s_memrealtime s[4:5]\n\t" \
    "s_nop 0\n\t" \
    "s_memrealtime s[6:7]\n\t" \
    "s_nop 0\n\t" \
    "s_memrealtime s[8:9]\n\t" \
    "s_nop 0\n\t" \
    "s_memrealtime s[10:11]\n\t" \
    "s_nop 0\n\t" \
    "s_memrealtime s[12:13]\n\t" \
    "s_nop 0\n\t" \
    "s_memrealtime s[14:15]\n\t" \
    "s_nop 0\n\t" \
    "s_memrealtime s[16:17]\n\t" \
    "s_nop 0\n\t" \
    "s_memrealtime s[18:19]\n\t" \
    "s_nop 0\n\t" \
    "s_memrealtime s[20:21]\n\t" \
    "s_nop 0\n\t" \
    "s_memrealtime s[22:23]\n\t" \
    "s_nop 0\n\t" \
    "s_memrealtime s[24:25]\n\t" \
    "s_nop 0\n\t" \
    "s_memrealtime s[26:27]\n\t" \
    "s_nop 0\n\t" \
    "s_memrealtime s[28:29]\n\t" \
    "s_nop 0\n\t" \
    "s_memtime s[56:57]\n\t" \
    "s_mov_b32 s70, 1\n\t" \
    "s_sub_u32 s71, s71, 1\n\t" \
    "s_cselect_b32 s69, 0, -1\n\t" \
    "s_waitcnt lgkmcnt(0)\n\t" \
    "s_sub_u32 s54, s56, s54\n\t" \
    "s_and_b32 s54, s54, s69\n\t" \
    "s_cmp_gt_u32 s54, 124\n\t" \
    "s_cbranch_scc1 qk_E1_%=\n\t" \
    "s_cmp_eq_u32 s4, s28\n\t" \
    "s_addc_u32 s70, s70, 0\n\t" \
    "s_cmp_eq_u32 s6, s28\n\t" \
    "s_addc_u32 s70, s70, 0\n\t" \
    "s_cmp_eq_u32 s8, s28\n\t" \
    "s_addc_u32 s70, s70, 0\n\t" \
    "s_cmp_eq_u32 s10, s28\n\t" \
    "s_addc_u32 s70, s70, 0\n\t" \
    "s_cmp_eq_u32 s12, s28\n\t" \
    "s_addc_u32 s70, s70, 0\n\t" \
    "s_cmp_eq_u32 s14, s28\n\t" \
    "s_addc_u32 s70, s70, 0\n\t" \
    "s_cmp_eq_u32 s16, s28\n\t" \
    "s_addc_u32 s70, s70, 0\n\t" \
    "s_cmp_eq_u32 s18, s28\n\t" \
    "s_addc_u32 s70, s70, 0\n\t" \
    "s_cmp_eq_u32 s20, s28\n\t" \
    "s_addc_u32 s70, s70, 0\n\t" \
    "s_cmp_eq_u32 s22, s28\n\t" \
    "s_addc_u32 s70, s70, 0\n\t" \
    "s_cmp_eq_u32 s24, s28\n\t" \
    "s_addc_u32 s70, s70, 0\n\t" \
    "s_cmp_eq_u32 s26, s28\n\t" \
    "s_addc_u32 s70, s70, 0\n\t" \
    "s_mov_b32 s75, s73\n\t" \
    "s_mov_b32 s76, s74\n\t" \
    "s_lshl_b32 s70, s70, 3\n\t" \
    "s_sub_u32 s73, s56, s70\n\t" \
    "s_lshl_b32 s74, s28, 4\n\t" \
    "s_waitcnt vmcnt(12) lgkmcnt(0)\n\t" \
    "s_barrier\n\t" \
    "s_add_u32 m0, s82, 16384\n\t" \
    "s_nop 0\n\t" \
    "global_load_lds_dwordx4 v200, s[42:43]\n\t" \
    "v_add_u32 v200, 131072, v200\n\t" \
    "s_add_u32 m0, s82, 20480\n\t" \
    "s_nop 0\n\t" \
    "global_load_lds_dwordx4 v200, s[42:43]\n\t" \
    "v_add_u32 v200, 131072, v200\n\t" \
    "s_add_u32 m0, s82, 24576\n\t" \
    "s_nop 0\n\t" \
    "global_load_lds_dwordx4 v200, s[42:43]\n\t" \
    "v_add_u32 v200, 131072, v200\n\t" \
    "s_add_u32 m0, s82, 28672\n\t" \
    "s_nop 0\n\t" \
    "global_load_lds_dwordx4 v200, s[42:43]\n\t" \
    "v_add_u32 v200, 131072, v200\n\t" \
    "ds_read_b64_tr_b16 a[176:177], v202 offset:32768\n\t" \
    "ds_read_b64_tr_b16 a[178:179], v202 offset:33792\n\t" \
    "ds_read_b64_tr_b16 a[180:181], v202 offset:40960\n\t" \
    "ds_read_b64_tr_b16 a[182:183], v202 offset:41984\n\t" \
    "ds_read_b64_tr_b16 a[248:249], v203 offset:32768\n\t" \
    "ds_read_b64_tr_b16 a[250:251], v203 offset:33792\n\t" \
    "ds_read_b64_tr_b16 a[252:253], v203 offset:40960\n\t" \
    "ds_read_b64_tr_b16 a[254:255], v203 offset:41984\n\t" \
    "v_mov_b32 v26, 0x3db504f3\n\t" \
    "v_mov_b32 v28, 0x3fb8aa3b\n\t" \
    "v_mov_b32 v27, 0x3db504f3\n\t" \
    "v_mov_b32 v29, 0x3fb8aa3b\n\t" \
    "v_mov_b32 v29, 0x3e0293ee\n\t" \
    "s_mov_b32 s48, 0xaaaaaaaa\n\t" \
    "s_mov_b32 s49, 0xaaaaaaaa\n\t" \
    "s_mov_b32 s58, 0x00ff00ff\n\t" \
    "s_mov_b32 s59, 0x00ff00ff\n\t" \
    "s_lshl_b32 s69, s66, 2\n\t" \
    "v_mov_b32 v30, s69\n\t" \
    "v_mov_b32 v31, 0\n\t" \
    "v_and_b32 v33, 12, v22\n\t" \
    "s_mov_b64 s[40:41], s[36:37]\n\t" \
    "s_waitcnt vmcnt(12) lgkmcnt(0)\n\t" \
    "s_barrier\n\t" \
    "v_subrev_u32 v200, 4718592, v200\n\t" \
    "s_add_u32 m0, s82, 32768\n\t" \
    "s_nop 0\n\t" \
    "global_load_lds_dwordx4 v200, s[44:45]\n\t" \
    "v_add_u32 v200, 131072, v200\n\t" \
    "s_add_u32 m0, s82, 36864\n\t" \
    "s_nop 0\n\t" \
    "global_load_lds_dwordx4 v200, s[44:45]\n\t" \
    "v_add_u32 v200, 131072, v200\n\t" \
    "s_add_u32 m0, s82, 40960\n\t" \
    "s_nop 0\n\t" \
    "global_load_lds_dwordx4 v200, s[44:45]\n\t" \
    "v_add_u32 v200, 131072, v200\n\t" \
    "s_add_u32 m0, s82, 45056\n\t" \
    "s_nop 0\n\t" \
    "global_load_lds_dwordx4 v200, s[44:45]\n\t" \
    "v_add_u32 v200, 131072, v200\n\t" \
    "ds_read_b64_tr_b16 a[184:185], v202 offset:49152\n\t" \
    "ds_read_b64_tr_b16 a[186:187], v202 offset:50176\n\t" \
    "ds_read_b64_tr_b16 a[188:189], v202 offset:57344\n\t" \
    "ds_read_b64_tr_b16 a[190:191], v202 offset:58368\n\t" \
    "ds_read_b64_tr_b16 v[224:225], v203 offset:49152\n\t" \
    "ds_read_b64_tr_b16 v[226:227], v203 offset:50176\n\t" \
    "ds_read_b64_tr_b16 v[228:229], v203 offset:57344\n\t" \
    "ds_read_b64_tr_b16 v[230:231], v203 offset:58368\n\t" \
    "s_waitcnt vmcnt(12) lgkmcnt(0)\n\t" \
    "s_barrier\n\t" \
    "s_add_u32 m0, s82, 49152\n\t" \
    "s_nop 0\n\t" \
    "global_load_lds_dwordx4 v200, s[44:45]\n\t" \
    "v_add_u32 v200, 131072, v200\n\t" \
    "s_add_u32 m0, s82, 53248\n\t" \
    "s_nop 0\n\t" \
    "global_load_lds_dwordx4 v200, s[44:45]\n\t" \
    "v_add_u32 v200, 131072, v200\n\t" \
    "s_add_u32 m0, s82, 57344\n\t" \
    "s_nop 0\n\t" \
    "global_load_lds_dwordx4 v200, s[44:45]\n\t" \
    "v_add_u32 v200, 131072, v200\n\t" \
    "s_add_u32 m0, s82, 61440\n\t" \
    "s_nop 0\n\t" \
    "global_load_lds_dwordx4 v200, s[44:45]\n\t" \
    "v_add_u32 v200, 131072, v200\n\t" \
    "v_add_u32 v202, 65536, v202\n\t" \
    "v_add_u32 v203, 65536, v203\n\t" \
    "ds_read_b64_tr_b16 a[192:193], v202 offset:0\n\t" \
    "ds_read_b64_tr_b16 a[194:195], v202 offset:1024\n\t" \
    "ds_read_b64_tr_b16 a[196:197], v202 offset:8192\n\t" \
    "ds_read_b64_tr_b16 a[198:199], v202 offset:9216\n\t" \
    "ds_read_b64_tr_b16 v[232:233], v203 offset:0\n\t" \
    "ds_read_b64_tr_b16 v[234:235], v203 offset:1024\n\t" \
    "ds_read_b64_tr_b16 v[236:237], v203 offset:8192\n\t" \
    "ds_read_b64_tr_b16 v[238:239], v203 offset:9216\n\t" \
    "s_waitcnt vmcnt(12) lgkmcnt(0)\n\t" \
    "s_barrier\n\t" \
    "s_add_u32 m0, s82, 65536\n\t" \
    "s_nop 0\n\t" \
    "global_load_lds_dwordx4 v200, s[44:45]\n\t" \
    "v_add_u32 v200, 131072, v200\n\t" \
    "s_add_u32 m0, s82, 69632\n\t" \
    "s_nop 0\n\t" \
    "global_load_lds_dwordx4 v200, s[44:45]\n\t" \
    "v_add_u32 v200, 131072, v200\n\t" \
    "s_add_u32 m0, s82, 73728\n\t" \
    "s_nop 0\n\t" \
    "global_load_lds_dwordx4 v200, s[44:45]\n\t" \
    "v_add_u32 v200, 131072, v200\n\t" \
    "s_add_u32 m0, s82, 77824\n\t" \
    "s_nop 0\n\t" \
    "global_load_lds_dwordx4 v200, s[44:45]\n\t" \
    "v_add_u32 v200, 131072, v200\n\t" \
    "v_add_u32 v202, -98304, v202\n\t" \
    "v_add_u32 v203, -98304, v203\n\t" \
    "ds_read_b64_tr_b16 a[200:201], v202 offset:0\n\t" \
    "ds_read_b64_tr_b16 a[202:203], v202 offset:1024\n\t" \
    "ds_read_b64_tr_b16 a[204:205], v202 offset:8192\n\t" \
    "ds_read_b64_tr_b16 a[206:207], v202 offset:9216\n\t" \
    "ds_read_b64_tr_b16 v[240:241], v203 offset:0\n\t" \
    "ds_read_b64_tr_b16 v[242:243], v203 offset:1024\n\t" \
    "ds_read_b64_tr_b16 v[244:245], v203 offset:8192\n\t" \
    "ds_read_b64_tr_b16 v[246:247], v203 offset:9216\n\t" \
    "s_waitcnt vmcnt(12) lgkmcnt(0)\n\t" \
    "s_barrier\n\t" \
    "s_add_u32 m0, s82, 81920\n\t" \
    "s_nop 0\n\t" \
    "global_load_lds_dwordx4 v200, s[44:45]\n\t" \
    "v_add_u32 v200, 131072, v200\n\t" \
    "s_add_u32 m0, s82, 86016\n\t" \
    "s_nop 0\n\t" \
    "global_load_lds_dwordx4 v200, s[44:45]\n\t" \
    "v_add_u32 v200, 131072, v200\n\t" \
    "s_add_u32 m0, s82, 90112\n\t" \
    "s_nop 0\n\t" \
    "global_load_lds_dwordx4 v200, s[44:45]\n\t" \
    "v_add_u32 v200, 131072, v200\n\t" \
    "s_add_u32 m0, s82, 94208\n\t" \
    "s_nop 0\n\t" \
    "global_load_lds_dwordx4 v200, s[44:45]\n\t" \
    "v_add_u32 v200, 131072, v200\n\t" \
    "ds_read_b64_tr_b16 a[208:209], v202 offset:16384\n\t" \
    "ds_read_b64_tr_b16 a[210:211], v202 offset:17408\n\t" \
    "ds_read_b64_tr_b16 a[212:213], v202 offset:24576\n\t" \
    "ds_read_b64_tr_b16 a[214:215], v202 offset:25600\n\t" \
    "ds_read_b64_tr_b16 v[248:249], v203 offset:16384\n\t" \
    "ds_read_b64_tr_b16 v[250:251], v203 offset:17408\n\t" \
    "ds_read_b64_tr_b16 v[252:253], v203 offset:24576\n\t" \
    "ds_read_b64_tr_b16 v[254:255], v203 offset:25600\n\t" \
    "s_waitcnt vmcnt(12) lgkmcnt(0)\n\t" \
    "s_barrier\n\t" \
    "s_add_u32 m0, s82, 98304\n\t" \
    "s_nop 0\n\t" \
    "global_load_lds_dwordx4 v200, s[44:45]\n\t" \
    "v_add_u32 v200, 131072, v200\n\t" \
    "s_add_u32 m0, s82, 102400\n\t" \
    "s_nop 0\n\t" \
    "global_load_lds_dwordx4 v200, s[44:45]\n\t" \
    "v_add_u32 v200, 131072, v200\n\t" \
    "s_add_u32 m0, s82, 106496\n\t" \
    "s_nop 0\n\t" \
    "global_load_lds_dwordx4 v200, s[44:45]\n\t" \
    "v_add_u32 v200, 131072, v200\n\t" \
    "s_add_u32 m0, s82, 110592\n\t" \
    "s_nop 0\n\t" \
    "global_load_lds_dwordx4 v200, s[44:45]\n\t" \
    "v_add_u32 v200, 131072, v200\n\t" \
    "ds_read_b64_tr_b16 a[0:1], v202 offset:32768\n\t" \
    "ds_read_b64_tr_b16 a[2:3], v202 offset:33792\n\t" \
    "ds_read_b64_tr_b16 a[4:5], v202 offset:40960\n\t" \
    "ds_read_b64_tr_b16 a[6:7], v202 offset:41984\n\t" \
    "ds_read_b64_tr_b16 a[72:73], v203 offset:32768\n\t" \
    "ds_read_b64_tr_b16 a[74:75], v203 offset:33792\n\t" \
    "ds_read_b64_tr_b16 a[76:77], v203 offset:40960\n\t" \
    "ds_read_b64_tr_b16 a[78:79], v203 offset:41984\n\t" \
    "s_waitcnt vmcnt(12) lgkmcnt(0)\n\t" \
    "s_barrier\n\t" \
    "s_add_u32 m0, s82, 0\n\t" \
    "s_nop 0\n\t" \
    "global_load_lds_dwordx4 v200, s[44:45]\n\t" \
    "v_add_u32 v200, 131072, v200\n\t" \
    "s_add_u32 m0, s82, 4096\n\t" \
    "s_nop 0\n\t" \
    "global_load_lds_dwordx4 v200, s[44:45]\n\t" \
    "v_add_u32 v200, 131072, v200\n\t" \
    "s_add_u32 m0, s82, 8192\n\t" \
    "s_nop 0\n\t" \
    "global_load_lds_dwordx4 v200, s[44:45]\n\t" \
    "v_add_u32 v200, 131072, v200\n\t" \
    "s_add_u32 m0, s82, 12288\n\t" \
    "s_nop 0\n\t" \
    "global_load_lds_dwordx4 v200, s[44:45]\n\t" \
    "v_add_u32 v200, 131072, v200\n\t" \
    "v_mbcnt_lo_u32_b32 v160, -1, 0\n\t" \
    "v_mbcnt_hi_u32_b32 v160, -1, v160\n\t" \
    "v_min_u32 v160, 59, v160\n\t" \
    "v_lshlrev_b32 v160, 6, v160\n\t" \
    "v_add_u32 v160, 6258688, v160\n\t" \
    "v_mov_b32 v161, 6266880\n\t" \
    "global_load_dword v161, v161, s[36:37] sc1\n\t" \
    "global_load_dwordx2 v[162:163], v160, s[36:37] sc1\n\t" \
    "ds_read_b64_tr_b16 a[8:9], v202 offset:49152\n\t" \
    "ds_read_b64_tr_b16 a[10:11], v202 offset:50176\n\t" \
    "ds_read_b64_tr_b16 a[12:13], v202 offset:57344\n\t" \
    "ds_read_b64_tr_b16 a[14:15], v202 offset:58368\n\t" \
    "ds_read_b64_tr_b16 a[80:81], v203 offset:49152\n\t" \
    "ds_read_b64_tr_b16 a[82:83], v203 offset:50176\n\t" \
    "ds_read_b64_tr_b16 a[84:85], v203 offset:57344\n\t" \
    "ds_read_b64_tr_b16 a[86:87], v203 offset:58368\n\t" \
    "s_waitcnt vmcnt(14) lgkmcnt(0)\n\t" \
    "s_barrier\n\t" \
    "s_add_u32 m0, s82, 16384\n\t" \
    "s_nop 0\n\t" \
    "global_load_lds_dwordx4 v200, s[44:45]\n\t" \
    "v_add_u32 v200, 131072, v200\n\t" \
    "s_add_u32 m0, s82, 20480\n\t" \
    "s_nop 0\n\t" \
    "global_load_lds_dwordx4 v200, s[44:45]\n\t" \
    "v_add_u32 v200, 131072, v200\n\t" \
    "s_add_u32 m0, s82, 24576\n\t" \
    "s_nop 0\n\t" \
    "global_load_lds_dwordx4 v200, s[44:45]\n\t" \
    "v_add_u32 v200, 131072, v200\n\t" \
    "s_add_u32 m0, s82, 28672\n\t" \
    "s_nop 0\n\t" \
    "global_load_lds_dwordx4 v200, s[44:45]\n\t" \
    "v_add_u32 v200, 131072, v200\n\t" \
    "v_add_u32 v202, 65536, v202\n\t" \
    "v_add_u32 v203, 65536, v203\n\t" \
    "ds_read_b64_tr_b16 a[16:17], v202 offset:0\n\t" \
    "ds_read_b64_tr_b16 a[18:19], v202 offset:1024\n\t" \
    "ds_read_b64_tr_b16 a[20:21], v202 offset:8192\n\t" \
    "ds_read_b64_tr_b16 a[22:23], v202 offset:9216\n\t" \
    "ds_read_b64_tr_b16 a[88:89], v203 offset:0\n\t" \
    "ds_read_b64_tr_b16 a[90:91], v203 offset:1024\n\t" \
    "ds_read_b64_tr_b16 a[92:93], v203 offset:8192\n\t" \
    "ds_read_b64_tr_b16 a[94:95], v203 offset:9216\n\t" \
    "s_waitcnt vmcnt(14) lgkmcnt(0)\n\t" \
    "s_barrier\n\t" \
    "s_add_u32 m0, s82, 32768\n\t" \
    "s_nop 0\n\t" \
    "global_load_lds_dwordx4 v200, s[44:45]\n\t" \
    "v_add_u32 v200, 131072, v200\n\t" \
    "s_add_u32 m0, s82, 36864\n\t" \
    "s_nop 0\n\t" \
    "global_load_lds_dwordx4 v200, s[44:45]\n\t" \
    "v_add_u32 v200, 131072, v200\n\t" \
    "s_add_u32 m0, s82, 40960\n\t" \
    "s_nop 0\n\t" \
    "global_load_lds_dwordx4 v200, s[44:45]\n\t" \
    "v_add_u32 v200, 131072, v200\n\t" \
    "s_add_u32 m0, s82, 45056\n\t" \
    "s_nop 0\n\t" \
    "global_load_lds_dwordx4 v200, s[44:45]\n\t" \
    "v_add_u32 v200, 131072, v200\n\t" \
    "ds_read_b64_tr_b16 a[24:25], v202 offset:16384\n\t" \
    "ds_read_b64_tr_b16 a[26:27], v202 offset:17408\n\t" \
    "ds_read_b64_tr_b16 a[28:29], v202 offset:24576\n\t" \
    "ds_read_b64_tr_b16 a[30:31], v202 offset:25600\n\t" \
    "ds_read_b64_tr_b16 a[96:97], v203 offset:16384\n\t" \
    "ds_read_b64_tr_b16 a[98:99], v203 offset:17408\n\t" \
    "ds_read_b64_tr_b16 a[100:101], v203 offset:24576\n\t" \
    "ds_read_b64_tr_b16 a[102:103], v203 offset:25600\n\t" \
    "s_waitcnt vmcnt(14) lgkmcnt(0)\n\t" \
    "s_barrier\n\t" \
    "s_add_u32 m0, s82, 49152\n\t" \
    "s_nop 0\n\t" \
    "global_load_lds_dwordx4 v200, s[44:45]\n\t" \
    "v_add_u32 v200, 131072, v200\n\t" \
    "s_add_u32 m0, s82, 53248\n\t" \
    "s_nop 0\n\t" \
    "global_load_lds_dwordx4 v200, s[44:45]\n\t" \
    "v_add_u32 v200, 131072, v200\n\t" \
    "s_add_u32 m0, s82, 57344\n\t" \
    "s_nop 0\n\t" \
    "global_load_lds_dwordx4 v200, s[44:45]\n\t" \
    "v_add_u32 v200, 131072, v200\n\t" \
    "s_add_u32 m0, s82, 61440\n\t" \
    "s_nop 0\n\t" \
    "global_load_lds_dwordx4 v200, s[44:45]\n\t" \
    "v_add_u32 v200, 131072, v200\n\t" \
    "ds_read_b64_tr_b16 a[32:33], v202 offset:32768\n\t" \
    "ds_read_b64_tr_b16 a[34:35], v202 offset:33792\n\t" \
    "ds_read_b64_tr_b16 a[36:37], v202 offset:40960\n\t" \
    "ds_read_b64_tr_b16 a[38:39], v202 offset:41984\n\t" \
    "ds_read_b64_tr_b16 a[104:105], v203 offset:32768\n\t" \
    "ds_read_b64_tr_b16 a[106:107], v203 offset:33792\n\t" \
    "ds_read_b64_tr_b16 a[108:109], v203 offset:40960\n\t" \
    "ds_read_b64_tr_b16 a[110:111], v203 offset:41984\n\t" \
    "s_waitcnt vmcnt(14) lgkmcnt(0)\n\t" \
    "s_barrier\n\t" \
    "v_add_u32 v202, -65536, v202\n\t" \
    "v_add_u32 v203, -65536, v203\n\t" \
    "ds_read_b64_tr_b16 a[40:41], v202 offset:0\n\t" \
    "ds_read_b64_tr_b16 a[42:43], v202 offset:1024\n\t" \
    "ds_read_b64_tr_b16 a[44:45], v202 offset:8192\n\t" \
    "ds_read_b64_tr_b16 a[46:47], v202 offset:9216\n\t" \
    "ds_read_b64_tr_b16 a[112:113], v203 offset:0\n\t" \
    "ds_read_b64_tr_b16 a[114:115], v203 offset:1024\n\t" \
    "ds_read_b64_tr_b16 a[116:117], v203 offset:8192\n\t" \
    "ds_read_b64_tr_b16 a[118:119], v203 offset:9216\n\t" \
    "s_waitcnt vmcnt(8) lgkmcnt(0)\n\t" \
    "s_barrier\n\t" \
    "ds_read_b64_tr_b16 a[48:49], v202 offset:16384\n\t" \
    "ds_read_b64_tr_b16 a[50:51], v202 offset:17408\n\t" \
    "ds_read_b64_tr_b16 a[52:53], v202 offset:24576\n\t" \
    "ds_read_b64_tr_b16 a[54:55], v202 offset:25600\n\t" \
    "ds_read_b64_tr_b16 a[120:121], v203 offset:16384\n\t" \
    "ds_read_b64_tr_b16 a[122:123], v203 offset:17408\n\t" \
    "ds_read_b64_tr_b16 a[124:125], v203 offset:24576\n\t" \
    "ds_read_b64_tr_b16 a[126:127], v203 offset:25600\n\t" \
    "v_sub_u32 v162, v162, v161\n\t" \
    "v_cmp_ne_u32 vcc, 1, v162\n\t" \
    "s_cbranch_vccz qk_Cok4_%=\n\t" \
    "qk_C3_%=:\n\t" \
    "global_load_dwordx2 v[162:163], v160, s[36:37] sc1\n\t" \
    "s_waitcnt vmcnt(0)\n\t" \
    "v_sub_u32 v162, v162, v161\n\t" \
    "v_cmp_ne_u32 vcc, 1, v162\n\t" \
    "s_cbranch_vccz qk_Cok4_%=\n\t" \
    "s_cmp_eq_u32 s68, 0\n\t" \
    "s_cbranch_scc1 qk_F5_%=\n\t" \
    "s_sub_u32 s68, s68, 1\n\t" \
    "s_sleep 4\n\t" \
    "s_branch qk_C3_%=\n\t" \
    "qk_F5_%=:\n\t" \
    "v_mov_b32 v164, 6095104\n\t" \
    "v_mov_b32 v165, 4\n\t" \
    "global_store_dword v164, v165, s[36:37] sc1\n\t" \
    "s_waitcnt vmcnt(0)\n\t" \
    "s_branch qk_Cok4_%=\n\t" \
    "qk_Cok4_%=:\n\t" \
    "s_cmp_lg_u32 s66, 0\n\t" \
    "s_cbranch_scc1 qk_Ca6_%=\n\t" \
    "v_mov_b32 v160, 6176768\n\t" \
    "s_mov_b64 exec, 1\n\t" \
    "global_atomic_add v160, v162, s[36:37] sc1\n\t" \
    "s_mov_b64 exec, -1\n\t" \
    "qk_Ca6_%=:\n\t" \
    "v_sub_u32_dpp v162, v163, v163 row_shl:6 row_mask:0xf bank_mask:0xf\n\t" \
    "v_min_u32_dpp v163, v163, v163 row_shl:6 row_mask:0xf bank_mask:0xf\n\t" \
    "v_min_i32 v162, 64, v162\n\t" \
    "v_max_i32 v162, -64, v162\n\t" \
    "s_mov_b32 s97, 0\n\t" \
    "v_readlane_b32 s69, v163, 0\n\t" \
    "v_readlane_b32 s70, v163, 1\n\t" \
    "v_readlane_b32 s54, v162, 0\n\t" \
    "v_readlane_b32 s55, v162, 1\n\t" \
    "s_nop 1\n\t" \
    "s_cmp_lt_u32 s70, s69\n\t" \
    "s_cselect_b32 s62, 8192, 0\n\t" \
    "s_cselect_b32 s54, s55, s54\n\t" \
    "s_cselect_b32 s69, s70, s69\n\t" \
    "s_mov_b32 s71, s69\n\t" \
    "s_add_u32 s97, s97, s54\n\t" \
    "v_readlane_b32 s69, v163, 2\n\t" \
    "v_readlane_b32 s70, v163, 3\n\t" \
    "v_readlane_b32 s54, v162, 2\n\t" \
    "v_readlane_b32 s55, v162, 3\n\t" \
    "s_nop 1\n\t" \
    "s_sub_u32 s55, 0, s55\n\t" \
    "s_cmp_lt_u32 s70, s69\n\t" \
    "s_cselect_b32 s63, 8192, 0\n\t" \
    "s_cselect_b32 s54, s55, s54\n\t" \
    "s_cselect_b32 s69, s70, s69\n\t" \
    "s_min_u32 s71, s69, s71\n\t" \
    "s_add_u32 s97, s97, s54\n\t" \
    "v_readlane_b32 s69, v163, 4\n\t" \
    "v_readlane_b32 s70, v163, 5\n\t" \
    "v_readlane_b32 s54, v162, 4\n\t" \
    "v_readlane_b32 s55, v162, 5\n\t" \
    "s_nop 1\n\t" \
    "s_sub_u32 s54, 0, s54\n\t" \
    "s_sub_u32 s55, 0, s55\n\t" \
    "s_cmp_lt_u32 s70, s69\n\t" \
    "s_cselect_b32 s64, 8192, 0\n\t" \
    "s_cselect_b32 s54, s55, s54\n\t" \
    "s_cselect_b32 s69, s70, s69\n\t" \
    "s_min_u32 s71, s69, s71\n\t" \
    "s_add_u32 s97, s97, s54\n\t" \
    "s_add_u32 s63, s63, 0x200000\n\t" \
    "s_xor_b32 s52, s64, 8192\n\t" \
    "s_cmp_gt_u32 s71, 625\n\t" \
    "s_cselect_b32 s69, 1, 0\n\t" \
    "s_mul_i32 s70, s69, 160\n\t" \
    "s_add_u32 s86, s86, s70\n\t" \
    "s_mul_i32 s70, s69, 536\n\t" \
    "s_add_u32 s87, s87, s70\n\t" \
    "s_mul_i32 s70, s69, 256\n\t" \
    "s_add_u32 s88, s88, s70\n\t" \
    "s_mul_i32 s70, s67, 7\n\t" \
    "s_add_u32 s69, s65, 0\n\t" \
    "s_xor_b32 s69, s69, s70\n\t" \
    "s_and_b32 s69, s69, 63\n\t" \
    "s_lshl_b32 s69, s69, 13\n\t" \
    "s_mul_i32 s70, s67, 524288\n\t" \
    "s_add_u32 s69, s69, s70\n\t" \
    "s_add_u32 s69, s69, 23068672\n\t" \
    "s_add_u32 s50, s36, s69\n\t" \
    "s_addc_u32 s51, s37, 0\n\t" \
    "v_add_u32 v14, s52, v14\n\t" \
    "v_add_u32 v24, s52, v24\n\t" \
    "s_cmp_gt_i32 s97, 0\n\t" \
    "s_cselect_b32 s70, 1, 0\n\t" \
    "s_lshr_b32 s69, s67, 3\n\t" \
    "s_xor_b32 s69, s69, s70\n\t" \
    "s_and_b32 s97, s69, 1\n\t" \
    "s_waitcnt vmcnt(4) lgkmcnt(0)\n\t" \
    "s_barrier\n\t" \
    "ds_read_b64_tr_b16 a[56:57], v202 offset:32768\n\t" \
    "ds_read_b64_tr_b16 a[58:59], v202 offset:33792\n\t" \
    "ds_read_b64_tr_b16 a[60:61], v202 offset:40960\n\t" \
    "ds_read_b64_tr_b16 a[62:63], v202 offset:41984\n\t" \
    "ds_read_b64_tr_b16 a[128:129], v203 offset:32768\n\t" \
    "ds_read_b64_tr_b16 a[130:131], v203 offset:33792\n\t" \
    "ds_read_b64_tr_b16 a[132:133], v203 offset:40960\n\t" \
    "ds_read_b64_tr_b16 a[134:135], v203 offset:41984\n\t" \
    "global_load_dword v25, v24, s[36:37] sc1\n\t" \
    "s_waitcnt lgkmcnt(0)\n\t" \
    "s_mov_b32 s71, 3\n\t" \
    "qk_E7_%=:\n\t" \
    "s_memtime s[54:55]\n\t" \
    "s_nop 0\n\t" \
    "s_memrealtime s[4:5]\n\t" \
    "s_nop 0\n\t" \
    "s_memrealtime s[6:7]\n\t" \
    "s_nop 0\n\t" \
    "s_memrealtime s[8:9]\n\t" \
    "s_nop 0\n\t" \
    "s_memrealtime s[10:11]\n\t" \
    "s_nop 0\n\t" \
    "s_memrealtime s[12:13]\n\t" \
    "s_nop 0\n\t" \
    "s_memrealtime s[14:15]\n\t" \
    "s_nop 0\n\t" \
    "s_memrealtime s[16:17]\n\t" \
    "s_nop 0\n\t" \
    "s_memrealtime s[18:19]\n\t" \
    "s_nop 0\n\t" \
    "s_memrealtime s[20:21]\n\t" \
    "s_nop 0\n\t" \
    "s_memrealtime s[22:23]\n\t" \
    "s_nop 0\n\t" \
    "s_memrealtime s[24:25]\n\t" \
    "s_nop 0\n\t" \
    "s_memrealtime s[26:27]\n\t" \
    "s_nop 0\n\t" \
    "s_memrealtime s[28:29]\n\t" \
    "s_nop 0\n\t" \
    "s_memtime s[56:57]\n\t" \
    "s_mov_b32 s70, 1\n\t" \
    "s_sub_u32 s71, s71, 1\n\t" \
    "s_cselect_b32 s69, 0, -1\n\t" \
    "s_waitcnt lgkmcnt(0)\n\t" \
    "s_sub_u32 s54, s56, s54\n\t" \
    "s_and_b32 s54, s54, s69\n\t" \
    "s_cmp_gt_u32 s54, 124\n\t" \
    "s_cbranch_scc1 qk_E7_%=\n\t" \
    "s_cmp_eq_u32 s4, s28\n\t" \
    "s_addc_u32 s70, s70, 0\n\t" \
    "s_cmp_eq_u32 s6, s28\n\t" \
    "s_addc_u32 s70, s70, 0\n\t" \
    "s_cmp_eq_u32 s8, s28\n\t" \
    "s_addc_u32 s70, s70, 0\n\t" \
    "s_cmp_eq_u32 s10, s28\n\t" \
    "s_addc_u32 s70, s70, 0\n\t" \
    "s_cmp_eq_u32 s12, s28\n\t" \
    "s_addc_u32 s70, s70, 0\n\t" \
    "s_cmp_eq_u32 s14, s28\n\t" \
    "s_addc_u32 s70, s70, 0\n\t" \
    "s_cmp_eq_u32 s16, s28\n\t" \
    "s_addc_u32 s70, s70, 0\n\t" \
    "s_cmp_eq_u32 s18, s28\n\t" \
    "s_addc_u32 s70, s70, 0\n\t" \
    "s_cmp_eq_u32 s20, s28\n\t" \
    "s_addc_u32 s70, s70, 0\n\t" \
    "s_cmp_eq_u32 s22, s28\n\t" \
    "s_addc_u32 s70, s70, 0\n\t" \
    "s_cmp_eq_u32 s24, s28\n\t" \
    "s_addc_u32 s70, s70, 0\n\t" \
    "s_cmp_eq_u32 s26, s28\n\t" \
    "s_addc_u32 s70, s70, 0\n\t" \
    "s_mov_b32 s75, s73\n\t" \
    "s_mov_b32 s76, s74\n\t" \
    "s_lshl_b32 s70, s70, 3\n\t" \
    "s_sub_u32 s73, s56, s70\n\t" \
    "s_lshl_b32 s74, s28, 4\n\t" \
    "s_sub_u32 s69, s73, s75\n\t" \
    "s_sub_u32 s70, s74, s76\n\t" \
    "s_sub_u32 s71, s70, 1600\n\t" \
    "s_cmp_lt_u32 s71, 400000\n\t" \
    "s_cbranch_scc0 qk_K9_%=\n\t" \
    "v_cvt_f32_u32 v160, s69\n\t" \
    "v_cvt_f32_u32 v161, s70\n\t" \
    "v_rcp_f32 v162, v160\n\t" \
    "v_rcp_f32 v163, v161\n\t" \
    "v_mul_f32 v162, v162, v161\n\t" \
    "v_mul_f32 v163, v163, v160\n\t" \
    "v_mul_f32 v162, 0x4f800000, v162\n\t" \
    "v_mul_f32 v163, 0x4f000000, v163\n\t" \
    "v_cvt_u32_f32 v162, v162\n\t" \
    "v_cvt_u32_f32 v163, v163\n\t" \
    "v_readfirstlane_b32 s69, v162\n\t" \
    "v_readfirstlane_b32 s70, v163\n\t" \
    "s_nop 1\n\t" \
    "s_mov_b32 s78, s69\n\t" \
    "s_mov_b32 s77, s70\n\t" \
    "qk_K9_%=:\n\t" \
    "s_waitcnt vmcnt(1) lgkmcnt(0)\n\t" \
    "s_barrier\n\t" \
    "ds_read_b64_tr_b16 a[64:65], v202 offset:49152\n\t" \
    "ds_read_b64_tr_b16 a[66:67], v202 offset:50176\n\t" \
    "ds_read_b64_tr_b16 a[68:69], v202 offset:57344\n\t" \
    "ds_read_b64_tr_b16 a[70:71], v202 offset:58368\n\t" \
    "ds_read_b64_tr_b16 a[136:137], v203 offset:49152\n\t" \
    "ds_read_b64_tr_b16 a[138:139], v203 offset:50176\n\t" \
    "ds_read_b64_tr_b16 a[140:141], v203 offset:57344\n\t" \
    "ds_read_b64_tr_b16 a[142:143], v203 offset:58368\n\t" \
    "s_waitcnt vmcnt(0)\n\t" \
    "ds_write_b32 v30, v25 offset:20496\n\t" \
    "s_waitcnt vmcnt(0) lgkmcnt(0)\n\t" \
    "s_barrier\n\t" \
    "ds_read_b32 v160, v11 offset:20496\n\t" \
    "s_waitcnt lgkmcnt(0)\n\t" \
    "v_readfirstlane_b32 s83, v160\n\t" \
    "s_nop 1\n\t" \
    "s_cmp_lt_i32 s83, 0\n\t" \
    "s_cselect_b64 s[44:45], 0, 0xfffff\n\t" \
    "s_cbranch_scc1 qk_nk10_%=\n\t" \
    "s_cmp_gt_u32 s66, 1\n\t" \
    "s_cbranch_scc1 qk_nk10_%=\n\t" \
    "global_load_dwordx4 v[100:103], v23, s[50:51] offset:0 sc1\n\t" \
    "global_load_dwordx4 v[104:107], v23, s[50:51] offset:2048 sc1\n\t" \
    "global_load_dwordx4 v[108:111], v15, s[50:51] offset:0 sc1\n\t" \
    "global_load_dwordx4 v[112:115], v15, s[50:51] offset:2048 sc1\n\t" \
    "global_load_dwordx4 v[116:119], v23, s[50:51] offset:1024 sc1\n\t" \
    "global_load_dwordx4 v[120:123], v23, s[50:51] offset:3072 sc1\n\t" \
    "global_load_dwordx4 v[124:127], v15, s[50:51] offset:1024 sc1\n\t" \
    "global_load_dwordx4 v[128:131], v15, s[50:51] offset:3072 sc1\n\t" \
    "qk_nk10_%=:\n\t" \
    "s_mov_b32 s79, s73\n\t" \
    "s_add_u32 s80, s74, 3200\n\t" \
    "s_cmp_eq_u32 s66, 1\n\t" \
    "s_cbranch_scc1 qk_loop12_%=\n\t" \
    "s_cmp_eq_u32 s66, 2\n\t" \
    "s_cbranch_scc1 qk_loop13_%=\n\t" \
    "s_cmp_eq_u32 s66, 3\n\t" \
    "s_cbranch_scc1 qk_loop14_%=\n\t" \
    "qk_loop11_%=:\n\t" \
    "v_mov_b32 v72, 0\n\t" \
    "v_mov_b32 v73, 0\n\t" \
    "v_mov_b32 v74, 0\n\t" \
    "v_mov_b32 v75, 0\n\t" \
    "v_mov_b32 v76, 0\n\t" \
    "v_mov_b32 v77, 0\n\t" \
    "v_mov_b32 v78, 0\n\t" \
    "v_mov_b32 v79, 0\n\t" \
    "v_mov_b32 v80, 0\n\t" \
    "v_mov_b32 v81, 0\n\t" \
    "v_mov_b32 v82, 0\n\t" \
    "v_mov_b32 v83, 0\n\t" \
    "v_mov_b32 v84, 0\n\t" \
    "v_mov_b32 v85, 0\n\t" \
    "v_mov_b32 v86, 0\n\t" \
    "v_mov_b32 v87, 0\n\t" \
    "v_mov_b32 v88, 0\n\t" \
    "v_mov_b32 v89, 0\n\t" \
    "v_mov_b32 v90, 0\n\t" \
    "v_mov_b32 v91, 0\n\t" \
    "v_mov_b32 v92, 0\n\t" \
    "v_mov_b32 v93, 0\n\t" \
    "v_mov_b32 v94, 0\n\t" \
    "v_mov_b32 v95, 0\n\t" \
    "v_mov_b32 v96, 0\n\t" \
    "v_mov_b32 v97, 0\n\t" \
    "v_mov_b32 v98, 0\n\t" \
    "v_mov_b32 v99, 0\n\t" \
    "v_add_u32 v31, 1, v31\n\t" \
    "s_add_u32 s69, s65, 0\n\t" \
    "s_bitcmp1_b32 s69, 0\n\t" \
    "s_cselect_b32 s70, s63, s62\n\t" \
    "s_bfe_u32 s69, s69, 0x20001\n\t" \
    "s_lshl_b32 s69, s69, 19\n\t" \
    "s_add_u32 s69, s69, s70\n\t" \
    "s_add_u32 s46, s36, s69\n\t" \
    "s_addc_u32 s47, s37, 0\n\t" \
    "s_mov_b64 s[42:43], s[46:47]\n\t" \
    "s_add_u32 s69, s65, 0\n\t" \
    "s_lshr_b32 s69, s69, 1\n\t" \
    "s_xor_b32 s69, s69, s97\n\t" \
    "s_and_b32 s69, s69, 1\n\t" \
    "s_lshl_b32 s69, s69, 14\n\t" \
    "s_or_b32 s46, s46, s69\n\t" \
    "s_add_u32 s69, s65, 3\n\t" \
    "s_cmp_ge_i32 s83, s69\n\t" \
    "s_cselect_b64 vcc, -1, 0\n\t" \
    "v_mov_b32 v9, 6233984\n\t" \
    "v_cndmask_b32 v9, v24, v9, vcc\n\t" \
    "s_mul_i32 s70, s67, 7\n\t" \
    "s_add_u32 s69, s65, 1\n\t" \
    "s_xor_b32 s69, s69, s70\n\t" \
    "s_and_b32 s69, s69, 63\n\t" \
    "s_lshl_b32 s69, s69, 13\n\t" \
    "s_mul_i32 s70, s67, 524288\n\t" \
    "s_add_u32 s69, s69, s70\n\t" \
    "s_add_u32 s69, s69, 23068672\n\t" \
    "s_add_u32 s38, s36, s69\n\t" \
    "s_addc_u32 s39, s37, 0\n\t" \
    "qk_go20_%=:\n\t" \
    "s_mov_b32 s70, s79\n\t" \
    "s_sub_u32 s70, s70, 100\n\t" \
    "s_getpc_b64 s[30:31]\n\t" \
    "qk_Wp22_%=:\n\t" \
    "s_add_u32 s30, s30, qk_We23_%=-qk_Wp22_%=\n\t" \
    "s_addc_u32 s31, s31, 0\n\t" \
    "qk_W21_%=:\n\t" \
    "s_memtime s[56:57]\n\t" \
    "s_waitcnt lgkmcnt(0)\n\t" \
    "s_sub_u32 s69, s70, s56\n\t" \
    "s_cmp_gt_i32 s69, 111\n\t" \
    "s_cbranch_scc1 qk_W21_%=\n\t" \
    "s_max_i32 s69, s69, 0\n\t" \
    "s_lshr_b32 s69, s69, 1\n\t" \
    "s_and_b32 s69, s69, 0x3c\n\t" \
    "s_sub_u32 s30, s30, s69\n\t" \
    "s_subb_u32 s31, s31, 0\n\t" \
    "s_setpc_b64 s[30:31]\n\t" \
    ".p2align 6\n\t" \
    "qk_al24_%=:\n\t" \
    "s_nop 0\n\t" \
    "s_nop 1\n\t" \
    "s_nop 1\n\t" \
    "s_nop 1\n\t" \
    "s_nop 1\n\t" \
    "s_nop 1\n\t" \
    "s_nop 1\n\t" \
    "s_nop 1\n\t" \
    "s_nop 1\n\t" \
    "s_nop 1\n\t" \
    "s_nop 1\n\t" \
    "s_nop 1\n\t" \
    "s_nop 1\n\t" \
    "s_nop 1\n\t" \
    "qk_We23_%=:\n\t" \
    "qk_G25_%=:\n\t" \
    "global_load_dwordx4 v[36:39], v1, s[46:47] sc1\n\t" \
    "s_nop 9\n\t" \
    "global_load_dwordx4 v[40:43], v1, s[46:47] offset:1024 sc1\n\t" \
    "s_nop 9\n\t" \
    "global_load_dwordx4 v[44:47], v2, s[46:47] sc1\n\t" \
    "s_waitcnt vmcnt(2)\n\t" \
    "v_smfmac_f32_16x16x64_bf16 v[72:75], v[36:39], a[0:7], v10\n\t" \
    "ds_write_b128 v0, v[36:39] offset:0\n\t" \
    "v_smfmac_f32_16x16x64_bf16 v[76:79], v[36:39], a[72:79], v10\n\t" \
    "v_max3_u32 v160, v36, v37, v38\n\t" \
    "v_smfmac_f32_16x16x64_bf16 v[80:83], v[36:39], a[144:151], v10\n\t" \
    "v_smfmac_f32_16x16x64_bf16 v[84:87], v[36:39], a[216:223], v10\n\t" \
    "s_waitcnt vmcnt(1)\n\t" \
    "ds_write_b128 v0, v[40:43] offset:1024\n\t" \
    "v_smfmac_f32_16x16x64_bf16 v[72:75], v[40:43], a[8:15], v10\n\t" \
    "v_max3_u32 v160, v160, v39, v40\n\t" \
    "v_max3_u32 v160, v160, v41, v42\n\t" \
    "v_smfmac_f32_16x16x64_bf16 v[76:79], v[40:43], a[80:87], v10\n\t" \
    "v_max_u32 v160, v160, v43\n\t" \
    "v_cmp_eq_u32 vcc, -1, v160\n\t" \
    "v_smfmac_f32_16x16x64_bf16 v[80:83], v[40:43], a[152:159], v10\n\t" \
    "v_smfmac_f32_16x16x64_bf16 v[84:87], v[40:43], a[224:231], v10\n\t" \
    "s_cbranch_vccnz qk_Gbad26_%=\n\t" \
    "qk_Gc28_%=:\n\t" \
    "s_waitcnt lgkmcnt(0)\n\t" \
    "s_barrier\n\t" \
    "ds_read_b128 v[48:51], v0 offset:2048\n\t" \
    "s_waitcnt vmcnt(0)\n\t" \
    "v_smfmac_f32_16x16x64_bf16 v[72:75], v[44:47], a[64:71], v10\n\t" \
    "v_max3_u32 v160, v44, v45, v46\n\t" \
    "v_smfmac_f32_16x16x64_bf16 v[76:79], v[44:47], a[136:143], v10\n\t" \
    "ds_read_b128 v[52:55], v0 offset:3072\n\t" \
    "v_max_u32 v160, v160, v47\n\t" \
    "v_smfmac_f32_16x16x64_bf16 v[80:83], v[44:47], a[208:215], v10\n\t" \
    "v_cmp_eq_u32 vcc, -1, v160\n\t" \
    "v_smfmac_f32_16x16x64_bf16 v[84:87], v[44:47], v[248:255], v10\n\t" \
    "ds_read_b128 v[56:59], v0 offset:4096\n\t" \
    "s_cbranch_vccnz qk_Gbad227_%=\n\t" \
    "qk_Gc229_%=:\n\t" \
    "s_waitcnt lgkmcnt(2)\n\t" \
    "v_smfmac_f32_16x16x64_bf16 v[72:75], v[48:51], a[16:23], v10\n\t" \
    "ds_read_b128 v[60:63], v0 offset:5120\n\t" \
    "v_smfmac_f32_16x16x64_bf16 v[76:79], v[48:51], a[88:95], v10\n\t" \
    "ds_read_b128 v[64:67], v0 offset:6144\n\t" \
    "v_smfmac_f32_16x16x64_bf16 v[80:83], v[48:51], a[160:167], v10\n\t" \
    "ds_read_b128 v[68:71], v0 offset:7168\n\t" \
    "global_load_dword v25, v9, s[36:37] sc1\n\t" \
    "v_smfmac_f32_16x16x64_bf16 v[84:87], v[48:51], a[232:239], v10\n\t" \
    "s_waitcnt lgkmcnt(4)\n\t" \
    "v_smfmac_f32_16x16x64_bf16 v[72:75], v[52:55], a[24:31], v10\n\t" \
    "s_waitcnt lgkmcnt(3)\n\t" \
    "v_smfmac_f32_16x16x64_bf16 v[72:75], v[56:59], a[32:39], v10\n\t" \
    "s_waitcnt lgkmcnt(2)\n\t" \
    "v_smfmac_f32_16x16x64_bf16 v[72:75], v[60:63], a[40:47], v10\n\t" \
    "s_waitcnt lgkmcnt(1)\n\t" \
    "v_smfmac_f32_16x16x64_bf16 v[72:75], v[64:67], a[48:55], v10\n\t" \
    "s_waitcnt lgkmcnt(0)\n\t" \
    "v_smfmac_f32_16x16x64_bf16 v[72:75], v[68:71], a[56:63], v10\n\t" \
    "v_smfmac_f32_16x16x64_bf16 v[76:79], v[52:55], a[96:103], v10\n\t" \
    "s_nop 1\n\t" \
    "s_nop 0\n\t" \
    "v_smfmac_f32_16x16x64_bf16 v[76:79], v[56:59], a[104:111], v10\n\t" \
    "s_nop 1\n\t" \
    "s_nop 0\n\t" \
    "v_smfmac_f32_16x16x64_bf16 v[76:79], v[60:63], a[112:119], v10\n\t" \
    "v_add_f32 v72, v72, v74\n\t" \
    "v_smfmac_f32_16x16x64_bf16 v[76:79], v[64:67], a[120:127], v10\n\t" \
    "v_add_f32 v73, v73, v75\n\t" \
    "v_smfmac_f32_16x16x64_bf16 v[76:79], v[68:71], a[128:135], v10\n\t" \
    "v_cvt_pk_bf16_f32 v156, v72, v73\n\t" \
    "v_smfmac_f32_16x16x64_bf16 v[80:83], v[52:55], a[168:175], v10\n\t" \
    "s_nop 1\n\t" \
    "ds_write_b32 v12, v156 offset:0\n\t" \
    "v_smfmac_f32_16x16x64_bf16 v[80:83], v[56:59], a[176:183], v10\n\t" \
    "s_nop 1\n\t" \
    "s_nop 0\n\t" \
    "v_smfmac_f32_16x16x64_bf16 v[80:83], v[60:63], a[184:191], v10\n\t" \
    "v_add_f32 v76, v76, v78\n\t" \
    "v_smfmac_f32_16x16x64_bf16 v[80:83], v[64:67], a[192:199], v10\n\t" \
    "v_add_f32 v77, v77, v79\n\t" \
    "v_smfmac_f32_16x16x64_bf16 v[80:83], v[68:71], a[200:207], v10\n\t" \
    "v_cvt_pk_bf16_f32 v157, v76, v77\n\t" \
    "v_smfmac_f32_16x16x64_bf16 v[84:87], v[52:55], a[240:247], v10\n\t" \
    "s_nop 1\n\t" \
    "ds_write_b32 v12, v157 offset:64\n\t" \
    "v_smfmac_f32_16x16x64_bf16 v[84:87], v[56:59], a[248:255], v10\n\t" \
    "s_nop 1\n\t" \
    "s_nop 0\n\t" \
    "v_smfmac_f32_16x16x64_bf16 v[84:87], v[60:63], v[224:231], v10\n\t" \
    "v_add_f32 v80, v80, v82\n\t" \
    "v_smfmac_f32_16x16x64_bf16 v[84:87], v[64:67], v[232:239], v10\n\t" \
    "v_add_f32 v81, v81, v83\n\t" \
    "v_smfmac_f32_16x16x64_bf16 v[84:87], v[68:71], v[240:247], v10\n\t" \
    "v_cvt_pk_bf16_f32 v158, v80, v81\n\t" \
    "ds_write_b32 v16, v158 offset:0\n\t" \
    "s_nop 5\n\t" \
    "v_pk_add_f32 v[84:85], v[84:85], v[86:87]\n\t" \
    "v_cvt_pk_bf16_f32 v159, v84, v85\n\t" \
    "ds_write_b32 v16, v159 offset:256\n\t" \
    "s_cmp_lt_i32 s83, 0\n\t" \
    "s_cbranch_scc1 qk_k018_%=\n\t" \
    "qk_k0b19_%=:\n\t" \
    "s_waitcnt lgkmcnt(0)\n\t" \
    "s_barrier\n\t" \
    "ds_read_b64_tr_b16 v[132:133], v13 offset:0\n\t" \
    "ds_read_b64_tr_b16 v[134:135], v13 offset:128\n\t" \
    "ds_read_b64_tr_b16 v[136:137], v13 offset:1024\n\t" \
    "ds_read_b64_tr_b16 v[138:139], v13 offset:1152\n\t" \
    "ds_read_b64_tr_b16 v[140:141], v17 offset:0\n\t" \
    "ds_read_b64_tr_b16 v[142:143], v17 offset:256\n\t" \
    "ds_read_b64_tr_b16 v[144:145], v17 offset:512\n\t" \
    "ds_read_b64_tr_b16 v[146:147], v17 offset:768\n\t" \
    "ds_read_b64_tr_b16 v[148:149], v17 offset:1024\n\t" \
    "ds_read_b64_tr_b16 v[150:151], v17 offset:1280\n\t" \
    "ds_read_b64_tr_b16 v[152:153], v17 offset:1536\n\t" \
    "ds_read_b64_tr_b16 v[154:155], v17 offset:1792\n\t" \
    "s_waitcnt lgkmcnt(10)\n\t" \
    "v_smfmac_f32_16x16x64_bf16 v[88:91], v[132:135], v[100:107], v10\n\t" \
    "v_smfmac_f32_16x16x64_bf16 v[92:95], v[132:135], v[116:123], v10\n\t" \
    "s_waitcnt lgkmcnt(8)\n\t" \
    "v_smfmac_f32_16x16x64_bf16 v[88:91], v[136:139], v[108:115], v10\n\t" \
    "v_smfmac_f32_16x16x64_bf16 v[92:95], v[136:139], v[124:131], v10\n\t" \
    "s_waitcnt lgkmcnt(4)\n\t" \
    "v_smfmac_f32_16x16x64_bf16 v[96:99], v[132:135], v[140:147], v10\n\t" \
    "s_waitcnt lgkmcnt(0)\n\t" \
    "s_nop 1\n\t" \
    "v_smfmac_f32_16x16x64_bf16 v[96:99], v[136:139], v[148:155], v10\n\t" \
    "s_nop 0\n\t" \
    "v_add_f32 v168, v88, v90\n\t" \
    "v_add_f32 v169, v92, v94\n\t" \
    "v_max_f32 v178, v168, v169\n\t" \
    "v_mul_f32 v168, v168, v29\n\t" \
    "v_mul_f32 v169, v169, v29\n\t" \
    "v_max_f32_dpp v178, v178, v178 row_mirror row_mask:0xf bank_mask:0xf\n\t" \
    "s_nop 0\n\t" \
    "v_add_f32 v172, v96, v98\n\t" \
    "v_max_f32 v173, v178, v172\n\t" \
    "v_mul_f32 v172, v172, v29\n\t" \
    "s_nop 0\n\t" \
    "v_max_f32_dpp v173, v173, v173 quad_perm:[1,0,3,2] row_mask:0xf bank_mask:0xf\n\t" \
    "s_nop 1\n\t" \
    "v_max_f32_dpp v173, v173, v173 quad_perm:[2,3,0,1] row_mask:0xf bank_mask:0xf\n\t" \
    "s_nop 1\n\t" \
    "v_max_f32_dpp v173, v173, v173 row_half_mirror row_mask:0xf bank_mask:0xf\n\t" \
    "v_fma_f32 v172, -v173, v29, v172\n\t" \
    "v_fma_f32 v168, -v173, v29, v168\n\t" \
    "v_fma_f32 v169, -v173, v29, v169\n\t" \
    "v_exp_f32 v172, v172\n\t" \
    "v_exp_f32 v168, v168\n\t" \
    "v_exp_f32 v169, v169\n\t" \
    "v_fma_f32 v173, v172, 0.5, v168\n\t" \
    "v_add_f32 v173, v173, v169\n\t" \
    "v_mov_b32_dpp v174, v172 quad_perm:[0,0,2,2] row_mask:0xf bank_mask:0xf\n\t" \
    "v_mov_b32_dpp v175, v172 quad_perm:[1,1,3,3] row_mask:0xf bank_mask:0xf\n\t" \
    "v_add_f32_dpp v173, v173, v173 quad_perm:[1,0,3,2] row_mask:0xf bank_mask:0xf\n\t" \
    "v_mov_b32_dpp v176, v168 quad_perm:[1,0,3,2] row_mask:0xf bank_mask:0xf\n\t" \
    "v_mov_b32_dpp v177, v169 quad_perm:[1,0,3,2] row_mask:0xf bank_mask:0xf\n\t" \
    "v_add_f32_dpp v173, v173, v173 quad_perm:[2,3,0,1] row_mask:0xf bank_mask:0xf\n\t" \
    "v_cndmask_b32 v168, v168, v177, s[48:49]\n\t" \
    "v_cndmask_b32 v169, v176, v169, s[48:49]\n\t" \
    "v_add_f32_dpp v173, v173, v173 row_half_mirror row_mask:0xf bank_mask:0xf\n\t" \
    "s_nop 1\n\t" \
    "v_add_f32_dpp v173, v173, v173 row_mirror row_mask:0xf bank_mask:0xf\n\t" \
    "v_rcp_f32 v173, v173\n\t" \
    "s_nop 0\n\t" \
    "v_pk_mul_f32 v[168:169], v[168:169], v[172:173] op_sel:[0,1] op_sel_hi:[1,1]\n\t" \
    "v_pk_mul_f32 v[174:175], v[174:175], v[172:173] op_sel:[0,1] op_sel_hi:[1,1]\n\t" \
    "v_cvt_pk_bf16_f32 v176, v168, v169\n\t" \
    "global_store_dword v18, v176, s[42:43] sc1\n\t" \
    "global_store_dword v3, v176, s[42:43] sc1\n\t" \
    "v_cvt_pk_bf16_f32 v177, v174, v175\n\t" \
    "global_store_dword v19, v177, s[42:43] sc1\n\t" \
    "global_store_dword v4, v177, s[42:43] sc1\n\t" \
    "s_cmp_eq_u32 s65, 63\n\t" \
    "s_cbranch_scc1 qk_last15_%=\n\t" \
    "s_memtime s[54:55]\n\t" \
    "s_waitcnt vmcnt(4) lgkmcnt(0)\n\t" \
    "s_add_u32 s79, s54, 728\n\t" \
    "v_readfirstlane_b32 s69, v25\n\t" \
    "s_add_u32 s71, s65, 1\n\t" \
    "s_max_i32 s83, s83, s69\n\t" \
    "qk_WF31_%=:\n\t" \
    "s_cmp_ge_i32 s83, s71\n\t" \
    "s_cbranch_scc1 qk_WFok32_%=\n\t" \
    "global_load_dword v160, v24, s[36:37] sc1\n\t" \
    "s_waitcnt vmcnt(0)\n\t" \
    "v_readfirstlane_b32 s83, v160\n\t" \
    "s_nop 1\n\t" \
    "s_cmp_ge_i32 s83, s71\n\t" \
    "s_cbranch_scc1 qk_WFok32_%=\n\t" \
    "s_cmp_eq_u32 s68, 0\n\t" \
    "s_cbranch_scc1 qk_F33_%=\n\t" \
    "s_sub_u32 s68, s68, 1\n\t" \
    "s_sleep 2\n\t" \
    "s_branch qk_WF31_%=\n\t" \
    "qk_F33_%=:\n\t" \
    "v_mov_b32 v160, 6095104\n\t" \
    "v_mov_b32 v161, 9\n\t" \
    "global_store_dword v160, v161, s[36:37] sc1\n\t" \
    "s_waitcnt vmcnt(0)\n\t" \
    "s_branch qk_WFok32_%=\n\t" \
    "qk_WFok32_%=:\n\t" \
    "v_mov_b32 v72, 0\n\t" \
    "v_mov_b32 v73, 0\n\t" \
    "v_mov_b32 v74, 0\n\t" \
    "v_mov_b32 v75, 0\n\t" \
    "v_mov_b32 v76, 0\n\t" \
    "v_mov_b32 v77, 0\n\t" \
    "v_mov_b32 v78, 0\n\t" \
    "v_mov_b32 v79, 0\n\t" \
    "v_mov_b32 v80, 0\n\t" \
    "v_mov_b32 v81, 0\n\t" \
    "v_mov_b32 v82, 0\n\t" \
    "v_mov_b32 v83, 0\n\t" \
    "v_mov_b32 v84, 0\n\t" \
    "v_mov_b32 v85, 0\n\t" \
    "v_mov_b32 v86, 0\n\t" \
    "v_mov_b32 v87, 0\n\t" \
    "v_mov_b32 v88, 0\n\t" \
    "v_mov_b32 v89, 0\n\t" \
    "v_mov_b32 v90, 0\n\t" \
    "v_mov_b32 v91, 0\n\t" \
    "v_mov_b32 v92, 0\n\t" \
    "v_mov_b32 v93, 0\n\t" \
    "v_mov_b32 v94, 0\n\t" \
    "v_mov_b32 v95, 0\n\t" \
    "v_mov_b32 v96, 0\n\t" \
    "v_mov_b32 v97, 0\n\t" \
    "v_mov_b32 v98, 0\n\t" \
    "v_mov_b32 v99, 0\n\t" \
    "s_mov_b32 s70, s79\n\t" \
    "qk_W34_%=:\n\t" \
    "s_memtime s[56:57]\n\t" \
    "s_waitcnt lgkmcnt(0)\n\t" \
    "s_sub_u32 s69, s70, s56\n\t" \
    "s_cmp_gt_i32 s69, 24\n\t" \
    "s_cbranch_scc1 qk_W34_%=\n\t" \
    "global_load_dwordx4 v[100:103], v23, s[38:39] offset:0 sc1\n\t" \
    "global_load_dwordx4 v[104:107], v23, s[38:39] offset:2048 sc1\n\t" \
    "global_load_dwordx4 v[108:111], v15, s[38:39] offset:0 sc1\n\t" \
    "global_load_dwordx4 v[112:115], v15, s[38:39] offset:2048 sc1\n\t" \
    "global_load_dwordx4 v[116:119], v23, s[38:39] offset:1024 sc1\n\t" \
    "global_load_dwordx4 v[120:123], v23, s[38:39] offset:3072 sc1\n\t" \
    "global_load_dwordx4 v[124:127], v15, s[38:39] offset:1024 sc1\n\t" \
    "global_load_dwordx4 v[128:131], v15, s[38:39] offset:3072 sc1\n\t" \
    "s_add_u32 s65, s65, 1\n\t" \
    "v_add_u32 v31, 1, v31\n\t" \
    "s_add_u32 s69, s65, 0\n\t" \
    "s_bitcmp1_b32 s69, 0\n\t" \
    "s_cselect_b32 s70, s63, s62\n\t" \
    "s_bfe_u32 s69, s69, 0x20001\n\t" \
    "s_lshl_b32 s69, s69, 19\n\t" \
    "s_add_u32 s69, s69, s70\n\t" \
    "s_add_u32 s46, s36, s69\n\t" \
    "s_addc_u32 s47, s37, 0\n\t" \
    "s_mov_b64 s[42:43], s[46:47]\n\t" \
    "s_add_u32 s69, s65, 0\n\t" \
    "s_lshr_b32 s69, s69, 1\n\t" \
    "s_xor_b32 s69, s69, s97\n\t" \
    "s_and_b32 s69, s69, 1\n\t" \
    "s_lshl_b32 s69, s69, 14\n\t" \
    "s_or_b32 s46, s46, s69\n\t" \
    "s_add_u32 s69, s65, 3\n\t" \
    "s_cmp_ge_i32 s83, s69\n\t" \
    "s_cselect_b64 vcc, -1, 0\n\t" \
    "v_mov_b32 v9, 6233984\n\t" \
    "v_cndmask_b32 v9, v24, v9, vcc\n\t" \
    "s_mul_i32 s70, s67, 7\n\t" \
    "s_add_u32 s69, s65, 1\n\t" \
    "s_xor_b32 s69, s69, s70\n\t" \
    "s_and_b32 s69, s69, 63\n\t" \
    "s_lshl_b32 s69, s69, 13\n\t" \
    "s_mul_i32 s70, s67, 524288\n\t" \
    "s_add_u32 s69, s69, s70\n\t" \
    "s_add_u32 s69, s69, 23068672\n\t" \
    "s_add_u32 s38, s36, s69\n\t" \
    "s_addc_u32 s39, s37, 0\n\t" \
    "s_waitcnt lgkmcnt(0)\n\t" \
    "s_barrier\n\t" \
    "ds_read_b32 v160, v11 offset:20480\n\t" \
    "s_waitcnt lgkmcnt(0)\n\t" \
    "v_readfirstlane_b32 s79, v160\n\t" \
    "s_nop 1\n\t" \
    "s_branch qk_go20_%=\n\t" \
    "qk_Gbad26_%=:\n\t" \
    "s_waitcnt vmcnt(0) lgkmcnt(0)\n\t" \
    "s_nop 7\n\t" \
    "v_mov_b32 v72, 0\n\t" \
    "v_mov_b32 v73, 0\n\t" \
    "v_mov_b32 v74, 0\n\t" \
    "v_mov_b32 v75, 0\n\t" \
    "v_mov_b32 v76, 0\n\t" \
    "v_mov_b32 v77, 0\n\t" \
    "v_mov_b32 v78, 0\n\t" \
    "v_mov_b32 v79, 0\n\t" \
    "v_mov_b32 v80, 0\n\t" \
    "v_mov_b32 v81, 0\n\t" \
    "v_mov_b32 v82, 0\n\t" \
    "v_mov_b32 v83, 0\n\t" \
    "v_mov_b32 v84, 0\n\t" \
    "v_mov_b32 v85, 0\n\t" \
    "v_mov_b32 v86, 0\n\t" \
    "v_mov_b32 v87, 0\n\t" \
    "s_cmp_eq_u32 s68, 0\n\t" \
    "s_cbranch_scc1 qk_F35_%=\n\t" \
    "s_sub_u32 s68, s68, 1\n\t" \
    "s_branch qk_G25_%=\n\t" \
    "qk_F35_%=:\n\t" \
    "v_mov_b32 v160, 6095104\n\t" \
    "v_mov_b32 v161, 1\n\t" \
    "global_store_dword v160, v161, s[36:37] sc1\n\t" \
    "s_waitcnt vmcnt(0)\n\t" \
    "s_branch qk_Gc28_%=\n\t" \
    "qk_Gbad227_%=:\n\t" \
    "global_load_dwordx4 v[44:47], v2, s[46:47] sc1\n\t" \
    "s_waitcnt vmcnt(0)\n\t" \
    "v_max3_u32 v160, v44, v45, v46\n\t" \
    "v_max_u32 v160, v160, v47\n\t" \
    "v_cmp_eq_u32 vcc, -1, v160\n\t" \
    "s_cbranch_vccz qk_Gre230_%=\n\t" \
    "s_cmp_eq_u32 s68, 0\n\t" \
    "s_cbranch_scc1 qk_F36_%=\n\t" \
    "s_sub_u32 s68, s68, 1\n\t" \
    "s_sleep 1\n\t" \
    "s_branch qk_Gbad227_%=\n\t" \
    "qk_F36_%=:\n\t" \
    "v_mov_b32 v160, 6095104\n\t" \
    "v_mov_b32 v161, 2\n\t" \
    "global_store_dword v160, v161, s[36:37] sc1\n\t" \
    "s_waitcnt vmcnt(0)\n\t" \
    "s_branch qk_Gre230_%=\n\t" \
    "qk_Gre230_%=:\n\t" \
    "v_mov_b32 v72, 0\n\t" \
    "v_mov_b32 v73, 0\n\t" \
    "v_mov_b32 v74, 0\n\t" \
    "v_mov_b32 v75, 0\n\t" \
    "v_mov_b32 v76, 0\n\t" \
    "v_mov_b32 v77, 0\n\t" \
    "v_mov_b32 v78, 0\n\t" \
    "v_mov_b32 v79, 0\n\t" \
    "v_mov_b32 v80, 0\n\t" \
    "v_mov_b32 v81, 0\n\t" \
    "v_mov_b32 v82, 0\n\t" \
    "v_mov_b32 v83, 0\n\t" \
    "v_mov_b32 v84, 0\n\t" \
    "v_mov_b32 v85, 0\n\t" \
    "v_mov_b32 v86, 0\n\t" \
    "v_mov_b32 v87, 0\n\t" \
    "v_smfmac_f32_16x16x64_bf16 v[72:75], v[36:39], a[0:7], v10\n\t" \
    "v_smfmac_f32_16x16x64_bf16 v[76:79], v[36:39], a[72:79], v10\n\t" \
    "v_smfmac_f32_16x16x64_bf16 v[80:83], v[36:39], a[144:151], v10\n\t" \
    "v_smfmac_f32_16x16x64_bf16 v[84:87], v[36:39], a[216:223], v10\n\t" \
    "v_smfmac_f32_16x16x64_bf16 v[72:75], v[40:43], a[8:15], v10\n\t" \
    "v_smfmac_f32_16x16x64_bf16 v[76:79], v[40:43], a[80:87], v10\n\t" \
    "v_smfmac_f32_16x16x64_bf16 v[80:83], v[40:43], a[152:159], v10\n\t" \
    "v_smfmac_f32_16x16x64_bf16 v[84:87], v[40:43], a[224:231], v10\n\t" \
    "v_smfmac_f32_16x16x64_bf16 v[72:75], v[44:47], a[64:71], v10\n\t" \
    "v_smfmac_f32_16x16x64_bf16 v[76:79], v[44:47], a[136:143], v10\n\t" \
    "v_smfmac_f32_16x16x64_bf16 v[80:83], v[44:47], a[208:215], v10\n\t" \
    "v_smfmac_f32_16x16x64_bf16 v[84:87], v[44:47], v[248:255], v10\n\t" \
    "s_branch qk_Gc229_%=\n\t" \
    "qk_k018_%=:\n\t" \
    "qk_WF37_%=:\n\t" \
    "s_cmp_ge_i32 s83, s65\n\t" \
    "s_cbranch_scc1 qk_WFok38_%=\n\t" \
    "global_load_dword v160, v24, s[36:37] sc1\n\t" \
    "s_waitcnt vmcnt(0)\n\t" \
    "v_readfirstlane_b32 s83, v160\n\t" \
    "s_nop 1\n\t" \
    "s_cmp_ge_i32 s83, s65\n\t" \
    "s_cbranch_scc1 qk_WFok38_%=\n\t" \
    "s_cmp_eq_u32 s68, 0\n\t" \
    "s_cbranch_scc1 qk_F39_%=\n\t" \
    "s_sub_u32 s68, s68, 1\n\t" \
    "s_sleep 2\n\t" \
    "s_branch qk_WF37_%=\n\t" \
    "qk_F39_%=:\n\t" \
    "v_mov_b32 v160, 6095104\n\t" \
    "v_mov_b32 v161, 8\n\t" \
    "global_store_dword v160, v161, s[36:37] sc1\n\t" \
    "s_waitcnt vmcnt(0)\n\t" \
    "s_branch qk_WFok38_%=\n\t" \
    "qk_WFok38_%=:\n\t" \
    "s_memrealtime s[54:55]\n\t" \
    "s_waitcnt lgkmcnt(0)\n\t" \
    "s_lshl_b32 s69, s54, 4\n\t" \
    "s_add_u32 s69, s69, 1280\n\t" \
    "v_mov_b32 v160, s69\n\t" \
    "global_load_dwordx4 v[100:103], v23, s[50:51] offset:0 sc1\n\t" \
    "global_load_dwordx4 v[104:107], v23, s[50:51] offset:2048 sc1\n\t" \
    "global_load_dwordx4 v[108:111], v15, s[50:51] offset:0 sc1\n\t" \
    "global_load_dwordx4 v[112:115], v15, s[50:51] offset:2048 sc1\n\t" \
    "global_load_dwordx4 v[116:119], v23, s[50:51] offset:1024 sc1\n\t" \
    "global_load_dwordx4 v[120:123], v23, s[50:51] offset:3072 sc1\n\t" \
    "global_load_dwordx4 v[124:127], v15, s[50:51] offset:1024 sc1\n\t" \
    "global_load_dwordx4 v[128:131], v15, s[50:51] offset:3072 sc1\n\t" \
    "s_mov_b64 exec, 0xff000\n\t" \
    "global_store_dword v21, v160, s[42:43] sc1\n\t" \
    "s_mov_b64 exec, -1\n\t" \
    "s_waitcnt vmcnt(1)\n\t" \
    "s_branch qk_k0b19_%=\n\t" \
    "qk_last15_%=:\n\t" \
    "s_endpgm\n\t" \
    "qk_loop12_%=:\n\t" \
    "v_mov_b32 v72, 0\n\t" \
    "v_mov_b32 v73, 0\n\t" \
    "v_mov_b32 v74, 0\n\t" \
    "v_mov_b32 v75, 0\n\t" \
    "v_mov_b32 v76, 0\n\t" \
    "v_mov_b32 v77, 0\n\t" \
    "v_mov_b32 v78, 0\n\t" \
    "v_mov_b32 v79, 0\n\t" \
    "v_mov_b32 v80, 0\n\t" \
    "v_mov_b32 v81, 0\n\t" \
    "v_mov_b32 v82, 0\n\t" \
    "v_mov_b32 v83, 0\n\t" \
    "v_mov_b32 v84, 0\n\t" \
    "v_mov_b32 v85, 0\n\t" \
    "v_mov_b32 v86, 0\n\t" \
    "v_mov_b32 v87, 0\n\t" \
    "v_mov_b32 v88, 0\n\t" \
    "v_mov_b32 v89, 0\n\t" \
    "v_mov_b32 v90, 0\n\t" \
    "v_mov_b32 v91, 0\n\t" \
    "v_mov_b32 v92, 0\n\t" \
    "v_mov_b32 v93, 0\n\t" \
    "v_mov_b32 v94, 0\n\t" \
    "v_mov_b32 v95, 0\n\t" \
    "v_mov_b32 v96, 0\n\t" \
    "v_mov_b32 v97, 0\n\t" \
    "v_mov_b32 v98, 0\n\t" \
    "v_mov_b32 v99, 0\n\t" \
    "v_add_u32 v31, 1, v31\n\t" \
    "s_add_u32 s69, s65, 0\n\t" \
    "s_bitcmp1_b32 s69, 0\n\t" \
    "s_cselect_b32 s70, s63, s62\n\t" \
    "s_bfe_u32 s69, s69, 0x20001\n\t" \
    "s_lshl_b32 s69, s69, 19\n\t" \
    "s_add_u32 s69, s69, s70\n\t" \
    "s_add_u32 s46, s36, s69\n\t" \
    "s_addc_u32 s47, s37, 0\n\t" \
    "s_mov_b64 s[42:43], s[46:47]\n\t" \
    "s_add_u32 s69, s65, 0\n\t" \
    "s_lshr_b32 s69, s69, 1\n\t" \
    "s_xor_b32 s69, s69, s97\n\t" \
    "s_and_b32 s69, s69, 1\n\t" \
    "s_lshl_b32 s69, s69, 14\n\t" \
    "s_or_b32 s46, s46, s69\n\t" \
    "s_add_u32 s69, s65, 3\n\t" \
    "s_cmp_ge_i32 s83, s69\n\t" \
    "s_cselect_b64 vcc, -1, 0\n\t" \
    "v_mov_b32 v9, 6233984\n\t" \
    "v_cndmask_b32 v9, v24, v9, vcc\n\t" \
    "s_mul_i32 s70, s67, 7\n\t" \
    "s_add_u32 s69, s65, 1\n\t" \
    "s_xor_b32 s69, s69, s70\n\t" \
    "s_and_b32 s69, s69, 63\n\t" \
    "s_lshl_b32 s69, s69, 13\n\t" \
    "s_mul_i32 s70, s67, 524288\n\t" \
    "s_add_u32 s69, s69, s70\n\t" \
    "s_add_u32 s69, s69, 23068672\n\t" \
    "s_add_u32 s38, s36, s69\n\t" \
    "s_addc_u32 s39, s37, 0\n\t" \
    "qk_go45_%=:\n\t" \
    "s_mov_b32 s70, s79\n\t" \
    "s_sub_u32 s70, s70, 100\n\t" \
    "s_getpc_b64 s[30:31]\n\t" \
    "qk_Wp47_%=:\n\t" \
    "s_add_u32 s30, s30, qk_We48_%=-qk_Wp47_%=\n\t" \
    "s_addc_u32 s31, s31, 0\n\t" \
    "qk_W46_%=:\n\t" \
    "s_memtime s[56:57]\n\t" \
    "s_waitcnt lgkmcnt(0)\n\t" \
    "s_sub_u32 s69, s70, s56\n\t" \
    "s_cmp_gt_i32 s69, 111\n\t" \
    "s_cbranch_scc1 qk_W46_%=\n\t" \
    "s_max_i32 s69, s69, 0\n\t" \
    "s_lshr_b32 s69, s69, 1\n\t" \
    "s_and_b32 s69, s69, 0x3c\n\t" \
    "s_sub_u32 s30, s30, s69\n\t" \
    "s_subb_u32 s31, s31, 0\n\t" \
    "s_setpc_b64 s[30:31]\n\t" \
    ".p2align 6\n\t" \
    "qk_al49_%=:\n\t" \
    "s_nop 0\n\t" \
    "s_nop 1\n\t" \
    "s_nop 1\n\t" \
    "s_nop 1\n\t" \
    "s_nop 1\n\t" \
    "s_nop 1\n\t" \
    "s_nop 1\n\t" \
    "s_nop 1\n\t" \
    "s_nop 1\n\t" \
    "s_nop 1\n\t" \
    "s_nop 1\n\t" \
    "s_nop 1\n\t" \
    "s_nop 1\n\t" \
    "s_nop 1\n\t" \
    "qk_We48_%=:\n\t" \
    "qk_G50_%=:\n\t" \
    "global_load_dwordx4 v[36:39], v1, s[46:47] sc1\n\t" \
    "s_nop 9\n\t" \
    "global_load_dwordx4 v[40:43], v1, s[46:47] offset:1024 sc1\n\t" \
    "s_nop 9\n\t" \
    "global_load_dwordx4 v[44:47], v2, s[46:47] sc1\n\t" \
    "s_waitcnt vmcnt(2)\n\t" \
    "v_smfmac_f32_16x16x64_bf16 v[72:75], v[36:39], a[16:23], v10\n\t" \
    "ds_write_b128 v0, v[36:39] offset:2048\n\t" \
    "v_smfmac_f32_16x16x64_bf16 v[76:79], v[36:39], a[88:95], v10\n\t" \
    "v_max3_u32 v160, v36, v37, v38\n\t" \
    "v_smfmac_f32_16x16x64_bf16 v[80:83], v[36:39], a[160:167], v10\n\t" \
    "v_smfmac_f32_16x16x64_bf16 v[84:87], v[36:39], a[232:239], v10\n\t" \
    "s_waitcnt vmcnt(1)\n\t" \
    "ds_write_b128 v0, v[40:43] offset:3072\n\t" \
    "v_smfmac_f32_16x16x64_bf16 v[72:75], v[40:43], a[24:31], v10\n\t" \
    "v_max3_u32 v160, v160, v39, v40\n\t" \
    "v_max3_u32 v160, v160, v41, v42\n\t" \
    "v_smfmac_f32_16x16x64_bf16 v[76:79], v[40:43], a[96:103], v10\n\t" \
    "v_max_u32 v160, v160, v43\n\t" \
    "v_cmp_eq_u32 vcc, -1, v160\n\t" \
    "v_smfmac_f32_16x16x64_bf16 v[80:83], v[40:43], a[168:175], v10\n\t" \
    "v_smfmac_f32_16x16x64_bf16 v[84:87], v[40:43], a[240:247], v10\n\t" \
    "s_cbranch_vccnz qk_Gbad51_%=\n\t" \
    "qk_Gc53_%=:\n\t" \
    "s_waitcnt lgkmcnt(0)\n\t" \
    "s_barrier\n\t" \
    "ds_read_b128 v[48:51], v0 offset:4096\n\t" \
    "s_waitcnt vmcnt(0)\n\t" \
    "v_smfmac_f32_16x16x64_bf16 v[72:75], v[44:47], a[64:71], v10\n\t" \
    "v_max3_u32 v160, v44, v45, v46\n\t" \
    "v_smfmac_f32_16x16x64_bf16 v[76:79], v[44:47], a[136:143], v10\n\t" \
    "ds_read_b128 v[52:55], v0 offset:5120\n\t" \
    "v_max_u32 v160, v160, v47\n\t" \
    "v_smfmac_f32_16x16x64_bf16 v[80:83], v[44:47], a[208:215], v10\n\t" \
    "v_cmp_eq_u32 vcc, -1, v160\n\t" \
    "v_smfmac_f32_16x16x64_bf16 v[84:87], v[44:47], v[248:255], v10\n\t" \
    "ds_read_b128 v[56:59], v0 offset:6144\n\t" \
    "s_cbranch_vccnz qk_Gbad252_%=\n\t" \
    "qk_Gc254_%=:\n\t" \
    "s_waitcnt lgkmcnt(2)\n\t" \
    "v_smfmac_f32_16x16x64_bf16 v[72:75], v[48:51], a[32:39], v10\n\t" \
    "ds_read_b128 v[60:63], v0 offset:7168\n\t" \
    "v_smfmac_f32_16x16x64_bf16 v[76:79], v[48:51], a[104:111], v10\n\t" \
    "ds_read_b128 v[64:67], v0 offset:0\n\t" \
    "v_smfmac_f32_16x16x64_bf16 v[80:83], v[48:51], a[176:183], v10\n\t" \
    "ds_read_b128 v[68:71], v0 offset:1024\n\t" \
    "global_load_dword v25, v9, s[36:37] sc1\n\t" \
    "v_smfmac_f32_16x16x64_bf16 v[84:87], v[48:51], a[248:255], v10\n\t" \
    "s_waitcnt lgkmcnt(4)\n\t" \
    "v_smfmac_f32_16x16x64_bf16 v[72:75], v[52:55], a[40:47], v10\n\t" \
    "s_waitcnt lgkmcnt(3)\n\t" \
    "v_smfmac_f32_16x16x64_bf16 v[72:75], v[56:59], a[48:55], v10\n\t" \
    "s_waitcnt lgkmcnt(2)\n\t" \
    "v_smfmac_f32_16x16x64_bf16 v[72:75], v[60:63], a[56:63], v10\n\t" \
    "s_waitcnt lgkmcnt(1)\n\t" \
    "v_smfmac_f32_16x16x64_bf16 v[72:75], v[64:67], a[0:7], v10\n\t" \
    "s_waitcnt lgkmcnt(0)\n\t" \
    "v_smfmac_f32_16x16x64_bf16 v[72:75], v[68:71], a[8:15], v10\n\t" \
    "v_smfmac_f32_16x16x64_bf16 v[76:79], v[52:55], a[112:119], v10\n\t" \
    "s_nop 1\n\t" \
    "s_nop 0\n\t" \
    "v_smfmac_f32_16x16x64_bf16 v[76:79], v[56:59], a[120:127], v10\n\t" \
    "s_nop 1\n\t" \
    "s_nop 0\n\t" \
    "v_smfmac_f32_16x16x64_bf16 v[76:79], v[60:63], a[128:135], v10\n\t" \
    "v_add_f32 v72, v72, v74\n\t" \
    "v_smfmac_f32_16x16x64_bf16 v[76:79], v[64:67], a[72:79], v10\n\t" \
    "v_add_f32 v73, v73, v75\n\t" \
    "v_smfmac_f32_16x16x64_bf16 v[76:79], v[68:71], a[80:87], v10\n\t" \
    "v_cvt_pk_bf16_f32 v156, v72, v73\n\t" \
    "v_smfmac_f32_16x16x64_bf16 v[80:83], v[52:55], a[184:191], v10\n\t" \
    "s_nop 1\n\t" \
    "ds_write_b32 v12, v156 offset:0\n\t" \
    "v_smfmac_f32_16x16x64_bf16 v[80:83], v[56:59], a[192:199], v10\n\t" \
    "s_nop 1\n\t" \
    "s_nop 0\n\t" \
    "v_smfmac_f32_16x16x64_bf16 v[80:83], v[60:63], a[200:207], v10\n\t" \
    "v_add_f32 v76, v76, v78\n\t" \
    "v_smfmac_f32_16x16x64_bf16 v[80:83], v[64:67], a[144:151], v10\n\t" \
    "v_add_f32 v77, v77, v79\n\t" \
    "v_smfmac_f32_16x16x64_bf16 v[80:83], v[68:71], a[152:159], v10\n\t" \
    "v_cvt_pk_bf16_f32 v157, v76, v77\n\t" \
    "v_smfmac_f32_16x16x64_bf16 v[84:87], v[52:55], v[224:231], v10\n\t" \
    "s_nop 1\n\t" \
    "ds_write_b32 v12, v157 offset:64\n\t" \
    "v_smfmac_f32_16x16x64_bf16 v[84:87], v[56:59], v[232:239], v10\n\t" \
    "s_nop 1\n\t" \
    "s_nop 0\n\t" \
    "v_smfmac_f32_16x16x64_bf16 v[84:87], v[60:63], v[240:247], v10\n\t" \
    "v_add_f32 v80, v80, v82\n\t" \
    "v_smfmac_f32_16x16x64_bf16 v[84:87], v[64:67], a[216:223], v10\n\t" \
    "v_add_f32 v81, v81, v83\n\t" \
    "v_smfmac_f32_16x16x64_bf16 v[84:87], v[68:71], a[224:231], v10\n\t" \
    "v_cvt_pk_bf16_f32 v158, v80, v81\n\t" \
    "ds_write_b32 v16, v158 offset:0\n\t" \
    "s_nop 5\n\t" \
    "v_pk_add_f32 v[84:85], v[84:85], v[86:87]\n\t" \
    "v_cvt_pk_bf16_f32 v159, v84, v85\n\t" \
    "ds_write_b32 v16, v159 offset:256\n\t" \
    "s_cmp_lt_i32 s83, 0\n\t" \
    "s_cbranch_scc1 qk_k043_%=\n\t" \
    "qk_k0b44_%=:\n\t" \
    "s_waitcnt lgkmcnt(0)\n\t" \
    "s_barrier\n\t" \
    "ds_read_b64_tr_b16 v[132:133], v13 offset:0\n\t" \
    "ds_read_b64_tr_b16 v[134:135], v13 offset:128\n\t" \
    "ds_read_b64_tr_b16 v[136:137], v13 offset:1024\n\t" \
    "ds_read_b64_tr_b16 v[138:139], v13 offset:1152\n\t" \
    "ds_read_b64_tr_b16 v[140:141], v17 offset:0\n\t" \
    "ds_read_b64_tr_b16 v[142:143], v17 offset:256\n\t" \
    "ds_read_b64_tr_b16 v[144:145], v17 offset:512\n\t" \
    "ds_read_b64_tr_b16 v[146:147], v17 offset:768\n\t" \
    "ds_read_b64_tr_b16 v[148:149], v17 offset:1024\n\t" \
    "ds_read_b64_tr_b16 v[150:151], v17 offset:1280\n\t" \
    "ds_read_b64_tr_b16 v[152:153], v17 offset:1536\n\t" \
    "ds_read_b64_tr_b16 v[154:155], v17 offset:1792\n\t" \
    "s_waitcnt lgkmcnt(10)\n\t" \
    "v_smfmac_f32_16x16x64_bf16 v[88:91], v[132:135], v[100:107], v10\n\t" \
    "v_smfmac_f32_16x16x64_bf16 v[92:95], v[132:135], v[116:123], v10\n\t" \
    "s_waitcnt lgkmcnt(8)\n\t" \
    "v_smfmac_f32_16x16x64_bf16 v[88:91], v[136:139], v[108:115], v10\n\t" \
    "v_smfmac_f32_16x16x64_bf16 v[92:95], v[136:139], v[124:131], v10\n\t" \
    "s_waitcnt lgkmcnt(4)\n\t" \
    "v_smfmac_f32_16x16x64_bf16 v[96:99], v[132:135], v[140:147], v10\n\t" \
    "s_waitcnt lgkmcnt(0)\n\t" \
    "s_nop 1\n\t" \
    "v_smfmac_f32_16x16x64_bf16 v[96:99], v[136:139], v[148:155], v10\n\t" \
    "s_nop 0\n\t" \
    "v_add_f32 v168, v89, v91\n\t" \
    "v_add_f32 v169, v93, v95\n\t" \
    "v_max_f32 v178, v168, v169\n\t" \
    "v_mul_f32 v168, v168, v29\n\t" \
    "v_mul_f32 v169, v169, v29\n\t" \
    "v_max_f32_dpp v178, v178, v178 row_mirror row_mask:0xf bank_mask:0xf\n\t" \
    "s_nop 0\n\t" \
    "v_add_f32 v172, v97, v99\n\t" \
    "v_max_f32 v173, v178, v172\n\t" \
    "v_mul_f32 v172, v172, v29\n\t" \
    "s_nop 0\n\t" \
    "v_max_f32_dpp v173, v173, v173 quad_perm:[1,0,3,2] row_mask:0xf bank_mask:0xf\n\t" \
    "s_nop 1\n\t" \
    "v_max_f32_dpp v173, v173, v173 quad_perm:[2,3,0,1] row_mask:0xf bank_mask:0xf\n\t" \
    "s_nop 1\n\t" \
    "v_max_f32_dpp v173, v173, v173 row_half_mirror row_mask:0xf bank_mask:0xf\n\t" \
    "v_fma_f32 v172, -v173, v29, v172\n\t" \
    "v_fma_f32 v168, -v173, v29, v168\n\t" \
    "v_fma_f32 v169, -v173, v29, v169\n\t" \
    "v_exp_f32 v172, v172\n\t" \
    "v_exp_f32 v168, v168\n\t" \
    "v_exp_f32 v169, v169\n\t" \
    "v_fma_f32 v173, v172, 0.5, v168\n\t" \
    "v_add_f32 v173, v173, v169\n\t" \
    "v_mov_b32_dpp v174, v172 quad_perm:[0,0,2,2] row_mask:0xf bank_mask:0xf\n\t" \
    "v_mov_b32_dpp v175, v172 quad_perm:[1,1,3,3] row_mask:0xf bank_mask:0xf\n\t" \
    "v_add_f32_dpp v173, v173, v173 quad_perm:[1,0,3,2] row_mask:0xf bank_mask:0xf\n\t" \
    "v_mov_b32_dpp v176, v168 quad_perm:[1,0,3,2] row_mask:0xf bank_mask:0xf\n\t" \
    "v_mov_b32_dpp v177, v169 quad_perm:[1,0,3,2] row_mask:0xf bank_mask:0xf\n\t" \
    "v_add_f32_dpp v173, v173, v173 quad_perm:[2,3,0,1] row_mask:0xf bank_mask:0xf\n\t" \
    "v_cndmask_b32 v168, v168, v177, s[48:49]\n\t" \
    "v_cndmask_b32 v169, v176, v169, s[48:49]\n\t" \
    "v_add_f32_dpp v173, v173, v173 row_half_mirror row_mask:0xf bank_mask:0xf\n\t" \
    "s_nop 1\n\t" \
    "v_add_f32_dpp v173, v173, v173 row_mirror row_mask:0xf bank_mask:0xf\n\t" \
    "v_rcp_f32 v173, v173\n\t" \
    "s_nop 0\n\t" \
    "v_pk_mul_f32 v[168:169], v[168:169], v[172:173] op_sel:[0,1] op_sel_hi:[1,1]\n\t" \
    "v_pk_mul_f32 v[174:175], v[174:175], v[172:173] op_sel:[0,1] op_sel_hi:[1,1]\n\t" \
    "v_cvt_pk_bf16_f32 v176, v168, v169\n\t" \
    "global_store_dword v18, v176, s[42:43] sc1\n\t" \
    "global_store_dword v3, v176, s[42:43] sc1\n\t" \
    "v_cvt_pk_bf16_f32 v177, v174, v175\n\t" \
    "global_store_dword v19, v177, s[42:43] sc1\n\t" \
    "global_store_dword v4, v177, s[42:43] sc1\n\t" \
    "s_cmp_eq_u32 s65, 63\n\t" \
    "s_cbranch_scc1 qk_last40_%=\n\t" \
    "s_memtime s[54:55]\n\t" \
    "s_waitcnt vmcnt(4) lgkmcnt(0)\n\t" \
    "s_add_u32 s79, s54, 728\n\t" \
    "v_readfirstlane_b32 s69, v25\n\t" \
    "s_add_u32 s71, s65, 1\n\t" \
    "s_max_i32 s83, s83, s69\n\t" \
    "qk_WF56_%=:\n\t" \
    "s_cmp_ge_i32 s83, s71\n\t" \
    "s_cbranch_scc1 qk_WFok57_%=\n\t" \
    "global_load_dword v160, v24, s[36:37] sc1\n\t" \
    "s_waitcnt vmcnt(0)\n\t" \
    "v_readfirstlane_b32 s83, v160\n\t" \
    "s_nop 1\n\t" \
    "s_cmp_ge_i32 s83, s71\n\t" \
    "s_cbranch_scc1 qk_WFok57_%=\n\t" \
    "s_cmp_eq_u32 s68, 0\n\t" \
    "s_cbranch_scc1 qk_F58_%=\n\t" \
    "s_sub_u32 s68, s68, 1\n\t" \
    "s_sleep 2\n\t" \
    "s_branch qk_WF56_%=\n\t" \
    "qk_F58_%=:\n\t" \
    "v_mov_b32 v160, 6095104\n\t" \
    "v_mov_b32 v161, 9\n\t" \
    "global_store_dword v160, v161, s[36:37] sc1\n\t" \
    "s_waitcnt vmcnt(0)\n\t" \
    "s_branch qk_WFok57_%=\n\t" \
    "qk_WFok57_%=:\n\t" \
    "v_mov_b32 v72, 0\n\t" \
    "v_mov_b32 v73, 0\n\t" \
    "v_mov_b32 v74, 0\n\t" \
    "v_mov_b32 v75, 0\n\t" \
    "v_mov_b32 v76, 0\n\t" \
    "v_mov_b32 v77, 0\n\t" \
    "v_mov_b32 v78, 0\n\t" \
    "v_mov_b32 v79, 0\n\t" \
    "v_mov_b32 v80, 0\n\t" \
    "v_mov_b32 v81, 0\n\t" \
    "v_mov_b32 v82, 0\n\t" \
    "v_mov_b32 v83, 0\n\t" \
    "v_mov_b32 v84, 0\n\t" \
    "v_mov_b32 v85, 0\n\t" \
    "v_mov_b32 v86, 0\n\t" \
    "v_mov_b32 v87, 0\n\t" \
    "v_mov_b32 v88, 0\n\t" \
    "v_mov_b32 v89, 0\n\t" \
    "v_mov_b32 v90, 0\n\t" \
    "v_mov_b32 v91, 0\n\t" \
    "v_mov_b32 v92, 0\n\t" \
    "v_mov_b32 v93, 0\n\t" \
    "v_mov_b32 v94, 0\n\t" \
    "v_mov_b32 v95, 0\n\t" \
    "v_mov_b32 v96, 0\n\t" \
    "v_mov_b32 v97, 0\n\t" \
    "v_mov_b32 v98, 0\n\t" \
    "v_mov_b32 v99, 0\n\t" \
    "s_mov_b32 s70, s79\n\t" \
    "qk_W59_%=:\n\t" \
    "s_memtime s[56:57]\n\t" \
    "s_waitcnt lgkmcnt(0)\n\t" \
    "s_sub_u32 s69, s70, s56\n\t" \
    "s_cmp_gt_i32 s69, 24\n\t" \
    "s_cbranch_scc1 qk_W59_%=\n\t" \
    "global_load_dwordx4 v[100:103], v23, s[38:39] offset:0 sc1\n\t" \
    "global_load_dwordx4 v[104:107], v23, s[38:39] offset:2048 sc1\n\t" \
    "global_load_dwordx4 v[108:111], v15, s[38:39] offset:0 sc1\n\t" \
    "global_load_dwordx4 v[112:115], v15, s[38:39] offset:2048 sc1\n\t" \
    "global_load_dwordx4 v[116:119], v23, s[38:39] offset:1024 sc1\n\t" \
    "global_load_dwordx4 v[120:123], v23, s[38:39] offset:3072 sc1\n\t" \
    "global_load_dwordx4 v[124:127], v15, s[38:39] offset:1024 sc1\n\t" \
    "global_load_dwordx4 v[128:131], v15, s[38:39] offset:3072 sc1\n\t" \
    "s_add_u32 s65, s65, 1\n\t" \
    "v_add_u32 v31, 1, v31\n\t" \
    "s_add_u32 s69, s65, 0\n\t" \
    "s_bitcmp1_b32 s69, 0\n\t" \
    "s_cselect_b32 s70, s63, s62\n\t" \
    "s_bfe_u32 s69, s69, 0x20001\n\t" \
    "s_lshl_b32 s69, s69, 19\n\t" \
    "s_add_u32 s69, s69, s70\n\t" \
    "s_add_u32 s46, s36, s69\n\t" \
    "s_addc_u32 s47, s37, 0\n\t" \
    "s_mov_b64 s[42:43], s[46:47]\n\t" \
    "s_add_u32 s69, s65, 0\n\t" \
    "s_lshr_b32 s69, s69, 1\n\t" \
    "s_xor_b32 s69, s69, s97\n\t" \
    "s_and_b32 s69, s69, 1\n\t" \
    "s_lshl_b32 s69, s69, 14\n\t" \
    "s_or_b32 s46, s46, s69\n\t" \
    "s_add_u32 s69, s65, 3\n\t" \
    "s_cmp_ge_i32 s83, s69\n\t" \
    "s_cselect_b64 vcc, -1, 0\n\t" \
    "v_mov_b32 v9, 6233984\n\t" \
    "v_cndmask_b32 v9, v24, v9, vcc\n\t" \
    "s_mul_i32 s70, s67, 7\n\t" \
    "s_add_u32 s69, s65, 1\n\t" \
    "s_xor_b32 s69, s69, s70\n\t" \
    "s_and_b32 s69, s69, 63\n\t" \
    "s_lshl_b32 s69, s69, 13\n\t" \
    "s_mul_i32 s70, s67, 524288\n\t" \
    "s_add_u32 s69, s69, s70\n\t" \
    "s_add_u32 s69, s69, 23068672\n\t" \
    "s_add_u32 s38, s36, s69\n\t" \
    "s_addc_u32 s39, s37, 0\n\t" \
    "s_waitcnt lgkmcnt(0)\n\t" \
    "s_barrier\n\t" \
    "ds_read_b32 v160, v11 offset:20480\n\t" \
    "s_waitcnt lgkmcnt(0)\n\t" \
    "v_readfirstlane_b32 s79, v160\n\t" \
    "s_nop 1\n\t" \
    "s_branch qk_go45_%=\n\t" \
    "qk_Gbad51_%=:\n\t" \
    "s_waitcnt vmcnt(0) lgkmcnt(0)\n\t" \
    "s_nop 7\n\t" \
    "v_mov_b32 v72, 0\n\t" \
    "v_mov_b32 v73, 0\n\t" \
    "v_mov_b32 v74, 0\n\t" \
    "v_mov_b32 v75, 0\n\t" \
    "v_mov_b32 v76, 0\n\t" \
    "v_mov_b32 v77, 0\n\t" \
    "v_mov_b32 v78, 0\n\t" \
    "v_mov_b32 v79, 0\n\t" \
    "v_mov_b32 v80, 0\n\t" \
    "v_mov_b32 v81, 0\n\t" \
    "v_mov_b32 v82, 0\n\t" \
    "v_mov_b32 v83, 0\n\t" \
    "v_mov_b32 v84, 0\n\t" \
    "v_mov_b32 v85, 0\n\t" \
    "v_mov_b32 v86, 0\n\t" \
    "v_mov_b32 v87, 0\n\t" \
    "s_cmp_eq_u32 s68, 0\n\t" \
    "s_cbranch_scc1 qk_F60_%=\n\t" \
    "s_sub_u32 s68, s68, 1\n\t" \
    "s_branch qk_G50_%=\n\t" \
    "qk_F60_%=:\n\t" \
    "v_mov_b32 v160, 6095104\n\t" \
    "v_mov_b32 v161, 1\n\t" \
    "global_store_dword v160, v161, s[36:37] sc1\n\t" \
    "s_waitcnt vmcnt(0)\n\t" \
    "s_branch qk_Gc53_%=\n\t" \
    "qk_Gbad252_%=:\n\t" \
    "global_load_dwordx4 v[44:47], v2, s[46:47] sc1\n\t" \
    "s_waitcnt vmcnt(0)\n\t" \
    "v_max3_u32 v160, v44, v45, v46\n\t" \
    "v_max_u32 v160, v160, v47\n\t" \
    "v_cmp_eq_u32 vcc, -1, v160\n\t" \
    "s_cbranch_vccz qk_Gre255_%=\n\t" \
    "s_cmp_eq_u32 s68, 0\n\t" \
    "s_cbranch_scc1 qk_F61_%=\n\t" \
    "s_sub_u32 s68, s68, 1\n\t" \
    "s_sleep 1\n\t" \
    "s_branch qk_Gbad252_%=\n\t" \
    "qk_F61_%=:\n\t" \
    "v_mov_b32 v160, 6095104\n\t" \
    "v_mov_b32 v161, 2\n\t" \
    "global_store_dword v160, v161, s[36:37] sc1\n\t" \
    "s_waitcnt vmcnt(0)\n\t" \
    "s_branch qk_Gre255_%=\n\t" \
    "qk_Gre255_%=:\n\t" \
    "v_mov_b32 v72, 0\n\t" \
    "v_mov_b32 v73, 0\n\t" \
    "v_mov_b32 v74, 0\n\t" \
    "v_mov_b32 v75, 0\n\t" \
    "v_mov_b32 v76, 0\n\t" \
    "v_mov_b32 v77, 0\n\t" \
    "v_mov_b32 v78, 0\n\t" \
    "v_mov_b32 v79, 0\n\t" \
    "v_mov_b32 v80, 0\n\t" \
    "v_mov_b32 v81, 0\n\t" \
    "v_mov_b32 v82, 0\n\t" \
    "v_mov_b32 v83, 0\n\t" \
    "v_mov_b32 v84, 0\n\t" \
    "v_mov_b32 v85, 0\n\t" \
    "v_mov_b32 v86, 0\n\t" \
    "v_mov_b32 v87, 0\n\t" \
    "v_smfmac_f32_16x16x64_bf16 v[72:75], v[36:39], a[16:23], v10\n\t" \
    "v_smfmac_f32_16x16x64_bf16 v[76:79], v[36:39], a[88:95], v10\n\t" \
    "v_smfmac_f32_16x16x64_bf16 v[80:83], v[36:39], a[160:167], v10\n\t" \
    "v_smfmac_f32_16x16x64_bf16 v[84:87], v[36:39], a[232:239], v10\n\t" \
    "v_smfmac_f32_16x16x64_bf16 v[72:75], v[40:43], a[24:31], v10\n\t" \
    "v_smfmac_f32_16x16x64_bf16 v[76:79], v[40:43], a[96:103], v10\n\t" \
    "v_smfmac_f32_16x16x64_bf16 v[80:83], v[40:43], a[168:175], v10\n\t" \
    "v_smfmac_f32_16x16x64_bf16 v[84:87], v[40:43], a[240:247], v10\n\t" \
    "v_smfmac_f32_16x16x64_bf16 v[72:75], v[44:47], a[64:71], v10\n\t" \
    "v_smfmac_f32_16x16x64_bf16 v[76:79], v[44:47], a[136:143], v10\n\t" \
    "v_smfmac_f32_16x16x64_bf16 v[80:83], v[44:47], a[208:215], v10\n\t" \
    "v_smfmac_f32_16x16x64_bf16 v[84:87], v[44:47], v[248:255], v10\n\t" \
    "s_branch qk_Gc254_%=\n\t" \
    "qk_k043_%=:\n\t" \
    "qk_WF62_%=:\n\t" \
    "s_cmp_ge_i32 s83, s65\n\t" \
    "s_cbranch_scc1 qk_WFok63_%=\n\t" \
    "global_load_dword v160, v24, s[36:37] sc1\n\t" \
    "s_waitcnt vmcnt(0)\n\t" \
    "v_readfirstlane_b32 s83, v160\n\t" \
    "s_nop 1\n\t" \
    "s_cmp_ge_i32 s83, s65\n\t" \
    "s_cbranch_scc1 qk_WFok63_%=\n\t" \
    "s_cmp_eq_u32 s68, 0\n\t" \
    "s_cbranch_scc1 qk_F64_%=\n\t" \
    "s_sub_u32 s68, s68, 1\n\t" \
    "s_sleep 2\n\t" \
    "s_branch qk_WF62_%=\n\t" \
    "qk_F64_%=:\n\t" \
    "v_mov_b32 v160, 6095104\n\t" \
    "v_mov_b32 v161, 8\n\t" \
    "global_store_dword v160, v161, s[36:37] sc1\n\t" \
    "s_waitcnt vmcnt(0)\n\t" \
    "s_branch qk_WFok63_%=\n\t" \
    "qk_WFok63_%=:\n\t" \
    "global_load_dwordx4 v[100:103], v23, s[50:51] offset:0 sc1\n\t" \
    "global_load_dwordx4 v[104:107], v23, s[50:51] offset:2048 sc1\n\t" \
    "global_load_dwordx4 v[108:111], v15, s[50:51] offset:0 sc1\n\t" \
    "global_load_dwordx4 v[112:115], v15, s[50:51] offset:2048 sc1\n\t" \
    "global_load_dwordx4 v[116:119], v23, s[50:51] offset:1024 sc1\n\t" \
    "global_load_dwordx4 v[120:123], v23, s[50:51] offset:3072 sc1\n\t" \
    "global_load_dwordx4 v[124:127], v15, s[50:51] offset:1024 sc1\n\t" \
    "global_load_dwordx4 v[128:131], v15, s[50:51] offset:3072 sc1\n\t" \
    "s_waitcnt vmcnt(0)\n\t" \
    "s_branch qk_k0b44_%=\n\t" \
    "qk_last40_%=:\n\t" \
    "s_endpgm\n\t" \
    "qk_loop13_%=:\n\t" \
    "v_mov_b32 v72, 0\n\t" \
    "v_mov_b32 v73, 0\n\t" \
    "v_mov_b32 v74, 0\n\t" \
    "v_mov_b32 v75, 0\n\t" \
    "v_mov_b32 v76, 0\n\t" \
    "v_mov_b32 v77, 0\n\t" \
    "v_mov_b32 v78, 0\n\t" \
    "v_mov_b32 v79, 0\n\t" \
    "v_mov_b32 v80, 0\n\t" \
    "v_mov_b32 v81, 0\n\t" \
    "v_mov_b32 v82, 0\n\t" \
    "v_mov_b32 v83, 0\n\t" \
    "v_mov_b32 v84, 0\n\t" \
    "v_mov_b32 v85, 0\n\t" \
    "v_mov_b32 v86, 0\n\t" \
    "v_mov_b32 v87, 0\n\t" \
    "v_add_u32 v31, 1, v31\n\t" \
    "s_add_u32 s69, s65, 0\n\t" \
    "s_bitcmp1_b32 s69, 0\n\t" \
    "s_cselect_b32 s70, s63, s62\n\t" \
    "s_bfe_u32 s69, s69, 0x20001\n\t" \
    "s_lshl_b32 s69, s69, 19\n\t" \
    "s_add_u32 s69, s69, s70\n\t" \
    "s_add_u32 s46, s36, s69\n\t" \
    "s_addc_u32 s47, s37, 0\n\t" \
    "s_mov_b64 s[42:43], s[46:47]\n\t" \
    "s_add_u32 s69, s65, 0\n\t" \
    "s_lshr_b32 s69, s69, 1\n\t" \
    "s_xor_b32 s69, s69, s97\n\t" \
    "s_and_b32 s69, s69, 1\n\t" \
    "s_lshl_b32 s69, s69, 14\n\t" \
    "s_or_b32 s46, s46, s69\n\t" \
    "s_mul_i32 s70, s67, 7\n\t" \
    "s_add_u32 s69, s65, 0\n\t" \
    "s_xor_b32 s69, s69, s70\n\t" \
    "s_and_b32 s69, s69, 63\n\t" \
    "s_lshl_b32 s69, s69, 12\n\t" \
    "s_mul_i32 s70, s67, 262144\n\t" \
    "s_add_u32 s69, s69, s70\n\t" \
    "s_add_u32 s69, s69, 39845888\n\t" \
    "s_add_u32 s40, s36, s69\n\t" \
    "s_addc_u32 s41, s37, 0\n\t" \
    "s_sub_u32 s69, s65, 1\n\t" \
    "v_mov_b32 v25, s69\n\t" \
    "qk_go70_%=:\n\t" \
    "s_mov_b32 s70, s79\n\t" \
    "s_sub_u32 s70, s70, 100\n\t" \
    "s_getpc_b64 s[30:31]\n\t" \
    "qk_Wp72_%=:\n\t" \
    "s_add_u32 s30, s30, qk_We73_%=-qk_Wp72_%=\n\t" \
    "s_addc_u32 s31, s31, 0\n\t" \
    "qk_W71_%=:\n\t" \
    "s_memtime s[56:57]\n\t" \
    "s_waitcnt lgkmcnt(0)\n\t" \
    "s_sub_u32 s69, s70, s56\n\t" \
    "s_cmp_gt_i32 s69, 111\n\t" \
    "s_cbranch_scc1 qk_W71_%=\n\t" \
    "s_max_i32 s69, s69, 0\n\t" \
    "s_lshr_b32 s69, s69, 1\n\t" \
    "s_and_b32 s69, s69, 0x3c\n\t" \
    "s_sub_u32 s30, s30, s69\n\t" \
    "s_subb_u32 s31, s31, 0\n\t" \
    "s_setpc_b64 s[30:31]\n\t" \
    ".p2align 6\n\t" \
    "qk_al74_%=:\n\t" \
    "s_nop 0\n\t" \
    "s_nop 1\n\t" \
    "s_nop 1\n\t" \
    "s_nop 1\n\t" \
    "s_nop 1\n\t" \
    "s_nop 1\n\t" \
    "s_nop 1\n\t" \
    "s_nop 1\n\t" \
    "s_nop 1\n\t" \
    "s_nop 1\n\t" \
    "s_nop 1\n\t" \
    "s_nop 1\n\t" \
    "s_nop 1\n\t" \
    "s_nop 1\n\t" \
    "qk_We73_%=:\n\t" \
    "qk_G75_%=:\n\t" \
    "global_load_dwordx4 v[36:39], v1, s[46:47] sc1\n\t" \
    "s_nop 9\n\t" \
    "global_load_dwordx4 v[40:43], v1, s[46:47] offset:1024 sc1\n\t" \
    "s_nop 9\n\t" \
    "global_load_dwordx4 v[44:47], v2, s[46:47] sc1\n\t" \
    "s_waitcnt vmcnt(2)\n\t" \
    "v_smfmac_f32_16x16x64_bf16 v[72:75], v[36:39], a[32:39], v10\n\t" \
    "ds_write_b128 v0, v[36:39] offset:4096\n\t" \
    "v_smfmac_f32_16x16x64_bf16 v[76:79], v[36:39], a[104:111], v10\n\t" \
    "v_max3_u32 v160, v36, v37, v38\n\t" \
    "v_smfmac_f32_16x16x64_bf16 v[80:83], v[36:39], a[176:183], v10\n\t" \
    "v_smfmac_f32_16x16x64_bf16 v[84:87], v[36:39], a[248:255], v10\n\t" \
    "s_waitcnt vmcnt(1)\n\t" \
    "ds_write_b128 v0, v[40:43] offset:5120\n\t" \
    "v_smfmac_f32_16x16x64_bf16 v[72:75], v[40:43], a[40:47], v10\n\t" \
    "v_max3_u32 v160, v160, v39, v40\n\t" \
    "v_max3_u32 v160, v160, v41, v42\n\t" \
    "v_smfmac_f32_16x16x64_bf16 v[76:79], v[40:43], a[112:119], v10\n\t" \
    "v_max_u32 v160, v160, v43\n\t" \
    "v_cmp_eq_u32 vcc, -1, v160\n\t" \
    "v_smfmac_f32_16x16x64_bf16 v[80:83], v[40:43], a[184:191], v10\n\t" \
    "v_smfmac_f32_16x16x64_bf16 v[84:87], v[40:43], v[224:231], v10\n\t" \
    "s_cbranch_vccnz qk_Gbad76_%=\n\t" \
    "qk_Gc78_%=:\n\t" \
    "s_waitcnt lgkmcnt(0)\n\t" \
    "s_barrier\n\t" \
    "ds_read_b128 v[48:51], v0 offset:6144\n\t" \
    "s_waitcnt vmcnt(0)\n\t" \
    "v_smfmac_f32_16x16x64_bf16 v[72:75], v[44:47], a[64:71], v10\n\t" \
    "v_max3_u32 v160, v44, v45, v46\n\t" \
    "v_smfmac_f32_16x16x64_bf16 v[76:79], v[44:47], a[136:143], v10\n\t" \
    "ds_read_b128 v[52:55], v0 offset:7168\n\t" \
    "v_max_u32 v160, v160, v47\n\t" \
    "v_smfmac_f32_16x16x64_bf16 v[80:83], v[44:47], a[208:215], v10\n\t" \
    "v_cmp_eq_u32 vcc, -1, v160\n\t" \
    "v_smfmac_f32_16x16x64_bf16 v[84:87], v[44:47], v[248:255], v10\n\t" \
    "ds_read_b128 v[56:59], v0 offset:0\n\t" \
    "s_cbranch_vccnz qk_Gbad277_%=\n\t" \
    "qk_Gc279_%=:\n\t" \
    "s_waitcnt lgkmcnt(2)\n\t" \
    "v_smfmac_f32_16x16x64_bf16 v[72:75], v[48:51], a[48:55], v10\n\t" \
    "ds_read_b128 v[60:63], v0 offset:1024\n\t" \
    "global_store_dword v14, v25, s[36:37] sc1\n\t" \
    "v_smfmac_f32_16x16x64_bf16 v[76:79], v[48:51], a[120:127], v10\n\t" \
    "ds_read_b128 v[64:67], v0 offset:2048\n\t" \
    "v_smfmac_f32_16x16x64_bf16 v[80:83], v[48:51], a[192:199], v10\n\t" \
    "ds_read_b128 v[68:71], v0 offset:3072\n\t" \
    "v_smfmac_f32_16x16x64_bf16 v[84:87], v[48:51], v[232:239], v10\n\t" \
    "s_waitcnt lgkmcnt(4)\n\t" \
    "v_smfmac_f32_16x16x64_bf16 v[72:75], v[52:55], a[56:63], v10\n\t" \
    "s_waitcnt lgkmcnt(3)\n\t" \
    "v_smfmac_f32_16x16x64_bf16 v[72:75], v[56:59], a[0:7], v10\n\t" \
    "s_waitcnt lgkmcnt(2)\n\t" \
    "v_smfmac_f32_16x16x64_bf16 v[72:75], v[60:63], a[8:15], v10\n\t" \
    "s_waitcnt lgkmcnt(1)\n\t" \
    "v_smfmac_f32_16x16x64_bf16 v[72:75], v[64:67], a[16:23], v10\n\t" \
    "s_waitcnt lgkmcnt(0)\n\t" \
    "v_smfmac_f32_16x16x64_bf16 v[72:75], v[68:71], a[24:31], v10\n\t" \
    "v_smfmac_f32_16x16x64_bf16 v[76:79], v[52:55], a[128:135], v10\n\t" \
    "s_nop 1\n\t" \
    "s_nop 0\n\t" \
    "v_smfmac_f32_16x16x64_bf16 v[76:79], v[56:59], a[72:79], v10\n\t" \
    "s_nop 1\n\t" \
    "s_nop 0\n\t" \
    "v_smfmac_f32_16x16x64_bf16 v[76:79], v[60:63], a[80:87], v10\n\t" \
    "v_add_f32 v72, v72, v74\n\t" \
    "v_smfmac_f32_16x16x64_bf16 v[76:79], v[64:67], a[88:95], v10\n\t" \
    "v_add_f32 v73, v73, v75\n\t" \
    "v_smfmac_f32_16x16x64_bf16 v[76:79], v[68:71], a[96:103], v10\n\t" \
    "v_cvt_pk_bf16_f32 v156, v72, v73\n\t" \
    "v_smfmac_f32_16x16x64_bf16 v[80:83], v[52:55], a[200:207], v10\n\t" \
    "s_nop 1\n\t" \
    "ds_write_b32 v12, v156 offset:0\n\t" \
    "v_smfmac_f32_16x16x64_bf16 v[80:83], v[56:59], a[144:151], v10\n\t" \
    "s_nop 1\n\t" \
    "s_nop 0\n\t" \
    "v_smfmac_f32_16x16x64_bf16 v[80:83], v[60:63], a[152:159], v10\n\t" \
    "v_add_f32 v76, v76, v78\n\t" \
    "v_smfmac_f32_16x16x64_bf16 v[80:83], v[64:67], a[160:167], v10\n\t" \
    "v_add_f32 v77, v77, v79\n\t" \
    "v_smfmac_f32_16x16x64_bf16 v[80:83], v[68:71], a[168:175], v10\n\t" \
    "v_cvt_pk_bf16_f32 v157, v76, v77\n\t" \
    "v_smfmac_f32_16x16x64_bf16 v[84:87], v[52:55], v[240:247], v10\n\t" \
    "s_nop 1\n\t" \
    "ds_write_b32 v12, v157 offset:64\n\t" \
    "v_smfmac_f32_16x16x64_bf16 v[84:87], v[56:59], a[216:223], v10\n\t" \
    "s_nop 1\n\t" \
    "s_nop 0\n\t" \
    "v_smfmac_f32_16x16x64_bf16 v[84:87], v[60:63], a[224:231], v10\n\t" \
    "v_add_f32 v80, v80, v82\n\t" \
    "v_smfmac_f32_16x16x64_bf16 v[84:87], v[64:67], a[232:239], v10\n\t" \
    "v_add_f32 v81, v81, v83\n\t" \
    "v_smfmac_f32_16x16x64_bf16 v[84:87], v[68:71], a[240:247], v10\n\t" \
    "v_cvt_pk_bf16_f32 v158, v80, v81\n\t" \
    "ds_write_b32 v16, v158 offset:0\n\t" \
    "s_nop 5\n\t" \
    "v_pk_add_f32 v[84:85], v[84:85], v[86:87]\n\t" \
    "v_cvt_pk_bf16_f32 v159, v84, v85\n\t" \
    "ds_write_b32 v16, v159 offset:256\n\t" \
    "s_waitcnt lgkmcnt(0)\n\t" \
    "s_barrier\n\t" \
    "s_sleep 1\n\t" \
    "ds_read_b64_tr_b16 v[140:141], v17 offset:0\n\t" \
    "ds_read_b64_tr_b16 v[142:143], v17 offset:256\n\t" \
    "ds_read_b64_tr_b16 v[144:145], v17 offset:512\n\t" \
    "ds_read_b64_tr_b16 v[146:147], v17 offset:768\n\t" \
    "ds_read_b64_tr_b16 v[148:149], v17 offset:1024\n\t" \
    "ds_read_b64_tr_b16 v[150:151], v17 offset:1280\n\t" \
    "ds_read_b64_tr_b16 v[152:153], v17 offset:1536\n\t" \
    "ds_read_b64_tr_b16 v[154:155], v17 offset:1792\n\t" \
    "s_waitcnt lgkmcnt(0)\n\t" \
    "s_cmp_eq_u32 s65, 63\n\t" \
    "s_cbranch_scc1 qk_last65_%=\n\t" \
    "s_memtime s[54:55]\n\t" \
    "s_waitcnt lgkmcnt(0)\n\t" \
    "s_cmp_ge_u32 s65, 58\n\t" \
    "s_cselect_b32 s69, 0, 0\n\t" \
    "s_sub_u32 s69, 940, s69\n\t" \
    "s_add_u32 s79, s54, s69\n\t" \
    "v_mov_b32 v72, 0\n\t" \
    "v_mov_b32 v73, 0\n\t" \
    "v_mov_b32 v74, 0\n\t" \
    "v_mov_b32 v75, 0\n\t" \
    "v_mov_b32 v76, 0\n\t" \
    "v_mov_b32 v77, 0\n\t" \
    "v_mov_b32 v78, 0\n\t" \
    "v_mov_b32 v79, 0\n\t" \
    "v_mov_b32 v80, 0\n\t" \
    "v_mov_b32 v81, 0\n\t" \
    "v_mov_b32 v82, 0\n\t" \
    "v_mov_b32 v83, 0\n\t" \
    "v_mov_b32 v84, 0\n\t" \
    "v_mov_b32 v85, 0\n\t" \
    "v_mov_b32 v86, 0\n\t" \
    "v_mov_b32 v87, 0\n\t" \
    "s_mov_b32 s70, s79\n\t" \
    "qk_W81_%=:\n\t" \
    "s_memtime s[56:57]\n\t" \
    "s_waitcnt lgkmcnt(0)\n\t" \
    "s_sub_u32 s69, s70, s56\n\t" \
    "s_cmp_gt_i32 s69, 24\n\t" \
    "s_cbranch_scc1 qk_W81_%=\n\t" \
    "s_mov_b64 exec, s[58:59]\n\t" \
    "global_store_dwordx4 v0, v[140:143], s[40:41] offset:0 sc1\n\t" \
    "global_store_dwordx4 v0, v[144:147], s[40:41] offset:1024 sc1\n\t" \
    "global_store_dwordx4 v0, v[148:151], s[40:41] offset:2048 sc1\n\t" \
    "global_store_dwordx4 v0, v[152:155], s[40:41] offset:3072 sc1\n\t" \
    "s_mov_b64 exec, -1\n\t" \
    "s_add_u32 s65, s65, 1\n\t" \
    "v_add_u32 v31, 1, v31\n\t" \
    "s_add_u32 s69, s65, 0\n\t" \
    "s_bitcmp1_b32 s69, 0\n\t" \
    "s_cselect_b32 s70, s63, s62\n\t" \
    "s_bfe_u32 s69, s69, 0x20001\n\t" \
    "s_lshl_b32 s69, s69, 19\n\t" \
    "s_add_u32 s69, s69, s70\n\t" \
    "s_add_u32 s46, s36, s69\n\t" \
    "s_addc_u32 s47, s37, 0\n\t" \
    "s_mov_b64 s[42:43], s[46:47]\n\t" \
    "s_add_u32 s69, s65, 0\n\t" \
    "s_lshr_b32 s69, s69, 1\n\t" \
    "s_xor_b32 s69, s69, s97\n\t" \
    "s_and_b32 s69, s69, 1\n\t" \
    "s_lshl_b32 s69, s69, 14\n\t" \
    "s_or_b32 s46, s46, s69\n\t" \
    "s_mul_i32 s70, s67, 7\n\t" \
    "s_add_u32 s69, s65, 0\n\t" \
    "s_xor_b32 s69, s69, s70\n\t" \
    "s_and_b32 s69, s69, 63\n\t" \
    "s_lshl_b32 s69, s69, 12\n\t" \
    "s_mul_i32 s70, s67, 262144\n\t" \
    "s_add_u32 s69, s69, s70\n\t" \
    "s_add_u32 s69, s69, 39845888\n\t" \
    "s_add_u32 s40, s36, s69\n\t" \
    "s_addc_u32 s41, s37, 0\n\t" \
    "s_sub_u32 s69, s65, 1\n\t" \
    "v_mov_b32 v25, s69\n\t" \
    "s_waitcnt lgkmcnt(0)\n\t" \
    "s_barrier\n\t" \
    "ds_read_b32 v160, v11 offset:20480\n\t" \
    "s_waitcnt lgkmcnt(0)\n\t" \
    "v_readfirstlane_b32 s79, v160\n\t" \
    "s_nop 1\n\t" \
    "s_branch qk_go70_%=\n\t" \
    "qk_Gbad76_%=:\n\t" \
    "s_waitcnt vmcnt(0) lgkmcnt(0)\n\t" \
    "s_nop 7\n\t" \
    "v_mov_b32 v72, 0\n\t" \
    "v_mov_b32 v73, 0\n\t" \
    "v_mov_b32 v74, 0\n\t" \
    "v_mov_b32 v75, 0\n\t" \
    "v_mov_b32 v76, 0\n\t" \
    "v_mov_b32 v77, 0\n\t" \
    "v_mov_b32 v78, 0\n\t" \
    "v_mov_b32 v79, 0\n\t" \
    "v_mov_b32 v80, 0\n\t" \
    "v_mov_b32 v81, 0\n\t" \
    "v_mov_b32 v82, 0\n\t" \
    "v_mov_b32 v83, 0\n\t" \
    "v_mov_b32 v84, 0\n\t" \
    "v_mov_b32 v85, 0\n\t" \
    "v_mov_b32 v86, 0\n\t" \
    "v_mov_b32 v87, 0\n\t" \
    "s_cmp_eq_u32 s68, 0\n\t" \
    "s_cbranch_scc1 qk_F82_%=\n\t" \
    "s_sub_u32 s68, s68, 1\n\t" \
    "s_branch qk_G75_%=\n\t" \
    "qk_F82_%=:\n\t" \
    "v_mov_b32 v160, 6095104\n\t" \
    "v_mov_b32 v161, 1\n\t" \
    "global_store_dword v160, v161, s[36:37] sc1\n\t" \
    "s_waitcnt vmcnt(0)\n\t" \
    "s_branch qk_Gc78_%=\n\t" \
    "qk_Gbad277_%=:\n\t" \
    "global_load_dwordx4 v[44:47], v2, s[46:47] sc1\n\t" \
    "s_waitcnt vmcnt(0)\n\t" \
    "v_max3_u32 v160, v44, v45, v46\n\t" \
    "v_max_u32 v160, v160, v47\n\t" \
    "v_cmp_eq_u32 vcc, -1, v160\n\t" \
    "s_cbranch_vccz qk_Gre280_%=\n\t" \
    "s_cmp_eq_u32 s68, 0\n\t" \
    "s_cbranch_scc1 qk_F83_%=\n\t" \
    "s_sub_u32 s68, s68, 1\n\t" \
    "s_sleep 1\n\t" \
    "s_branch qk_Gbad277_%=\n\t" \
    "qk_F83_%=:\n\t" \
    "v_mov_b32 v160, 6095104\n\t" \
    "v_mov_b32 v161, 2\n\t" \
    "global_store_dword v160, v161, s[36:37] sc1\n\t" \
    "s_waitcnt vmcnt(0)\n\t" \
    "s_branch qk_Gre280_%=\n\t" \
    "qk_Gre280_%=:\n\t" \
    "v_mov_b32 v72, 0\n\t" \
    "v_mov_b32 v73, 0\n\t" \
    "v_mov_b32 v74, 0\n\t" \
    "v_mov_b32 v75, 0\n\t" \
    "v_mov_b32 v76, 0\n\t" \
    "v_mov_b32 v77, 0\n\t" \
    "v_mov_b32 v78, 0\n\t" \
    "v_mov_b32 v79, 0\n\t" \
    "v_mov_b32 v80, 0\n\t" \
    "v_mov_b32 v81, 0\n\t" \
    "v_mov_b32 v82, 0\n\t" \
    "v_mov_b32 v83, 0\n\t" \
    "v_mov_b32 v84, 0\n\t" \
    "v_mov_b32 v85, 0\n\t" \
    "v_mov_b32 v86, 0\n\t" \
    "v_mov_b32 v87, 0\n\t" \
    "v_smfmac_f32_16x16x64_bf16 v[72:75], v[36:39], a[32:39], v10\n\t" \
    "v_smfmac_f32_16x16x64_bf16 v[76:79], v[36:39], a[104:111], v10\n\t" \
    "v_smfmac_f32_16x16x64_bf16 v[80:83], v[36:39], a[176:183], v10\n\t" \
    "v_smfmac_f32_16x16x64_bf16 v[84:87], v[36:39], a[248:255], v10\n\t" \
    "v_smfmac_f32_16x16x64_bf16 v[72:75], v[40:43], a[40:47], v10\n\t" \
    "v_smfmac_f32_16x16x64_bf16 v[76:79], v[40:43], a[112:119], v10\n\t" \
    "v_smfmac_f32_16x16x64_bf16 v[80:83], v[40:43], a[184:191], v10\n\t" \
    "v_smfmac_f32_16x16x64_bf16 v[84:87], v[40:43], v[224:231], v10\n\t" \
    "v_smfmac_f32_16x16x64_bf16 v[72:75], v[44:47], a[64:71], v10\n\t" \
    "v_smfmac_f32_16x16x64_bf16 v[76:79], v[44:47], a[136:143], v10\n\t" \
    "v_smfmac_f32_16x16x64_bf16 v[80:83], v[44:47], a[208:215], v10\n\t" \
    "v_smfmac_f32_16x16x64_bf16 v[84:87], v[44:47], v[248:255], v10\n\t" \
    "s_branch qk_Gc279_%=\n\t" \
    "qk_last65_%=:\n\t" \
    "s_mov_b64 exec, s[58:59]\n\t" \
    "global_store_dwordx4 v0, v[140:143], s[40:41] offset:0 sc1\n\t" \
    "global_store_dwordx4 v0, v[144:147], s[40:41] offset:1024 sc1\n\t" \
    "global_store_dwordx4 v0, v[148:151], s[40:41] offset:2048 sc1\n\t" \
    "global_store_dwordx4 v0, v[152:155], s[40:41] offset:3072 sc1\n\t" \
    "s_mov_b64 exec, -1\n\t" \
    "s_endpgm\n\t" \
    "qk_loop14_%=:\n\t" \
    "v_mov_b32 v72, 0\n\t" \
    "v_mov_b32 v73, 0\n\t" \
    "v_mov_b32 v74, 0\n\t" \
    "v_mov_b32 v75, 0\n\t" \
    "v_mov_b32 v76, 0\n\t" \
    "v_mov_b32 v77, 0\n\t" \
    "v_mov_b32 v78, 0\n\t" \
    "v_mov_b32 v79, 0\n\t" \
    "v_mov_b32 v80, 0\n\t" \
    "v_mov_b32 v81, 0\n\t" \
    "v_mov_b32 v82, 0\n\t" \
    "v_mov_b32 v83, 0\n\t" \
    "v_mov_b32 v84, 0\n\t" \
    "v_mov_b32 v85, 0\n\t" \
    "v_mov_b32 v86, 0\n\t" \
    "v_mov_b32 v87, 0\n\t" \
    "v_add_u32 v31, 1, v31\n\t" \
    "s_add_u32 s69, s65, 0\n\t" \
    "s_bitcmp1_b32 s69, 0\n\t" \
    "s_cselect_b32 s70, s63, s62\n\t" \
    "s_bfe_u32 s69, s69, 0x20001\n\t" \
    "s_lshl_b32 s69, s69, 19\n\t" \
    "s_add_u32 s69, s69, s70\n\t" \
    "s_add_u32 s46, s36, s69\n\t" \
    "s_addc_u32 s47, s37, 0\n\t" \
    "s_mov_b64 s[42:43], s[46:47]\n\t" \
    "s_add_u32 s69, s65, 0\n\t" \
    "s_lshr_b32 s69, s69, 1\n\t" \
    "s_xor_b32 s69, s69, s97\n\t" \
    "s_and_b32 s69, s69, 1\n\t" \
    "s_lshl_b32 s69, s69, 14\n\t" \
    "s_or_b32 s46, s46, s69\n\t" \
    "qk_go89_%=:\n\t" \
    "s_mov_b32 s70, s79\n\t" \
    "s_sub_u32 s70, s70, 100\n\t" \
    "s_getpc_b64 s[30:31]\n\t" \
    "qk_Wp91_%=:\n\t" \
    "s_add_u32 s30, s30, qk_We92_%=-qk_Wp91_%=\n\t" \
    "s_addc_u32 s31, s31, 0\n\t" \
    "qk_W90_%=:\n\t" \
    "s_memtime s[56:57]\n\t" \
    "s_waitcnt lgkmcnt(0)\n\t" \
    "s_sub_u32 s69, s70, s56\n\t" \
    "s_cmp_gt_i32 s69, 111\n\t" \
    "s_cbranch_scc1 qk_W90_%=\n\t" \
    "s_max_i32 s69, s69, 0\n\t" \
    "s_lshr_b32 s69, s69, 1\n\t" \
    "s_and_b32 s69, s69, 0x3c\n\t" \
    "s_sub_u32 s30, s30, s69\n\t" \
    "s_subb_u32 s31, s31, 0\n\t" \
    "s_setpc_b64 s[30:31]\n\t" \
    ".p2align 6\n\t" \
    "qk_al93_%=:\n\t" \
    "s_nop 0\n\t" \
    "s_nop 1\n\t" \
    "s_nop 1\n\t" \
    "s_nop 1\n\t" \
    "s_nop 1\n\t" \
    "s_nop 1\n\t" \
    "s_nop 1\n\t" \
    "s_nop 1\n\t" \
    "s_nop 1\n\t" \
    "s_nop 1\n\t" \
    "s_nop 1\n\t" \
    "s_nop 1\n\t" \
    "s_nop 1\n\t" \
    "s_nop 1\n\t" \
    "qk_We92_%=:\n\t" \
    "qk_G94_%=:\n\t" \
    "global_load_dwordx4 v[36:39], v1, s[46:47] sc1\n\t" \
    "s_nop 9\n\t" \
    "global_load_dwordx4 v[40:43], v1, s[46:47] offset:1024 sc1\n\t" \
    "s_nop 9\n\t" \
    "global_load_dwordx4 v[44:47], v2, s[46:47] sc1\n\t" \
    "s_mul_hi_u32 s69, s84, s78\n\t" \
    "s_add_u32 s33, s74, s69\n\t" \
    "s_lshr_b32 s69, s90, 16\n\t" \
    "s_add_u32 s34, s33, s69\n\t" \
    "s_and_b32 s69, s90, 0xffff\n\t" \
    "s_sub_u32 s35, s80, s74\n\t" \
    "s_sub_u32 s35, s35, s69\n\t" \
    "s_add_u32 s99, s35, 1280\n\t" \
    "s_add_u32 s98, s34, 600\n\t" \
    "s_waitcnt vmcnt(2)\n\t" \
    "v_smfmac_f32_16x16x64_bf16 v[72:75], v[36:39], a[48:55], v10\n\t" \
    "ds_write_b128 v0, v[36:39] offset:6144\n\t" \
    "v_smfmac_f32_16x16x64_bf16 v[76:79], v[36:39], a[120:127], v10\n\t" \
    "v_max3_u32 v160, v36, v37, v38\n\t" \
    "v_smfmac_f32_16x16x64_bf16 v[80:83], v[36:39], a[192:199], v10\n\t" \
    "v_smfmac_f32_16x16x64_bf16 v[84:87], v[36:39], v[232:239], v10\n\t" \
    "s_waitcnt vmcnt(1)\n\t" \
    "ds_write_b128 v0, v[40:43] offset:7168\n\t" \
    "v_smfmac_f32_16x16x64_bf16 v[72:75], v[40:43], a[56:63], v10\n\t" \
    "v_max3_u32 v160, v160, v39, v40\n\t" \
    "v_max3_u32 v160, v160, v41, v42\n\t" \
    "v_smfmac_f32_16x16x64_bf16 v[76:79], v[40:43], a[128:135], v10\n\t" \
    "v_max_u32 v160, v160, v43\n\t" \
    "v_cmp_eq_u32 vcc, -1, v160\n\t" \
    "v_smfmac_f32_16x16x64_bf16 v[80:83], v[40:43], a[200:207], v10\n\t" \
    "v_smfmac_f32_16x16x64_bf16 v[84:87], v[40:43], v[240:247], v10\n\t" \
    "s_cbranch_vccnz qk_Gbad95_%=\n\t" \
    "qk_Gc97_%=:\n\t" \
    "s_waitcnt lgkmcnt(0)\n\t" \
    "s_barrier\n\t" \
    "s_memtime s[56:57]\n\t" \
    "ds_read_b128 v[48:51], v0 offset:0\n\t" \
    "s_waitcnt vmcnt(0)\n\t" \
    "v_smfmac_f32_16x16x64_bf16 v[72:75], v[44:47], a[64:71], v10\n\t" \
    "v_max3_u32 v160, v44, v45, v46\n\t" \
    "v_smfmac_f32_16x16x64_bf16 v[76:79], v[44:47], a[136:143], v10\n\t" \
    "ds_read_b128 v[52:55], v0 offset:1024\n\t" \
    "v_max_u32 v160, v160, v47\n\t" \
    "v_smfmac_f32_16x16x64_bf16 v[80:83], v[44:47], a[208:215], v10\n\t" \
    "v_cmp_eq_u32 vcc, -1, v160\n\t" \
    "s_sub_u32 s69, s56, s73\n\t" \
    "v_smfmac_f32_16x16x64_bf16 v[84:87], v[44:47], v[248:255], v10\n\t" \
    "ds_read_b128 v[56:59], v0 offset:2048\n\t" \
    "s_mul_hi_u32 s69, s69, s78\n\t" \
    "s_cbranch_vccnz qk_Gbad296_%=\n\t" \
    "qk_Gc298_%=:\n\t" \
    "s_waitcnt lgkmcnt(2)\n\t" \
    "v_smfmac_f32_16x16x64_bf16 v[72:75], v[48:51], a[0:7], v10\n\t" \
    "ds_read_b128 v[60:63], v0 offset:3072\n\t" \
    "s_cmp_gt_i32 s69, s35\n\t" \
    "v_smfmac_f32_16x16x64_bf16 v[76:79], v[48:51], a[72:79], v10\n\t" \
    "ds_read_b128 v[64:67], v0 offset:4096\n\t" \
    "s_cselect_b32 s70, s34, s33\n\t" \
    "v_smfmac_f32_16x16x64_bf16 v[80:83], v[48:51], a[144:151], v10\n\t" \
    "ds_read_b128 v[68:71], v0 offset:5120\n\t" \
    "s_cmp_gt_i32 s69, s99\n\t" \
    "v_smfmac_f32_16x16x64_bf16 v[84:87], v[48:51], a[216:223], v10\n\t" \
    "s_cselect_b32 s70, s98, s70\n\t" \
    "s_waitcnt lgkmcnt(4)\n\t" \
    "v_smfmac_f32_16x16x64_bf16 v[72:75], v[52:55], a[8:15], v10\n\t" \
    "s_add_u32 s70, s69, s70\n\t" \
    "s_waitcnt lgkmcnt(3)\n\t" \
    "v_smfmac_f32_16x16x64_bf16 v[72:75], v[56:59], a[16:23], v10\n\t" \
    "v_and_b32_e64 v162, -2, s70\n\t" \
    "s_waitcnt lgkmcnt(2)\n\t" \
    "v_smfmac_f32_16x16x64_bf16 v[72:75], v[60:63], a[24:31], v10\n\t" \
    "s_mov_b64 exec, s[44:45]\n\t" \
    "global_store_dword v21, v162, s[42:43] sc1\n\t" \
    "s_mov_b64 exec, -1\n\t" \
    "s_waitcnt lgkmcnt(1)\n\t" \
    "v_smfmac_f32_16x16x64_bf16 v[72:75], v[64:67], a[32:39], v10\n\t" \
    "s_waitcnt lgkmcnt(0)\n\t" \
    "v_smfmac_f32_16x16x64_bf16 v[72:75], v[68:71], a[40:47], v10\n\t" \
    "v_smfmac_f32_16x16x64_bf16 v[76:79], v[52:55], a[80:87], v10\n\t" \
    "s_nop 1\n\t" \
    "s_nop 0\n\t" \
    "v_smfmac_f32_16x16x64_bf16 v[76:79], v[56:59], a[88:95], v10\n\t" \
    "s_nop 1\n\t" \
    "s_nop 0\n\t" \
    "v_smfmac_f32_16x16x64_bf16 v[76:79], v[60:63], a[96:103], v10\n\t" \
    "v_add_f32 v72, v72, v74\n\t" \
    "v_smfmac_f32_16x16x64_bf16 v[76:79], v[64:67], a[104:111], v10\n\t" \
    "v_add_f32 v73, v73, v75\n\t" \
    "v_smfmac_f32_16x16x64_bf16 v[76:79], v[68:71], a[112:119], v10\n\t" \
    "v_cvt_pk_bf16_f32 v156, v72, v73\n\t" \
    "v_smfmac_f32_16x16x64_bf16 v[80:83], v[52:55], a[152:159], v10\n\t" \
    "s_nop 1\n\t" \
    "ds_write_b32 v12, v156 offset:0\n\t" \
    "v_smfmac_f32_16x16x64_bf16 v[80:83], v[56:59], a[160:167], v10\n\t" \
    "s_nop 1\n\t" \
    "s_nop 0\n\t" \
    "v_smfmac_f32_16x16x64_bf16 v[80:83], v[60:63], a[168:175], v10\n\t" \
    "v_add_f32 v76, v76, v78\n\t" \
    "v_smfmac_f32_16x16x64_bf16 v[80:83], v[64:67], a[176:183], v10\n\t" \
    "v_add_f32 v77, v77, v79\n\t" \
    "v_smfmac_f32_16x16x64_bf16 v[80:83], v[68:71], a[184:191], v10\n\t" \
    "v_cvt_pk_bf16_f32 v157, v76, v77\n\t" \
    "v_smfmac_f32_16x16x64_bf16 v[84:87], v[52:55], a[224:231], v10\n\t" \
    "s_nop 1\n\t" \
    "ds_write_b32 v12, v157 offset:64\n\t" \
    "v_smfmac_f32_16x16x64_bf16 v[84:87], v[56:59], a[232:239], v10\n\t" \
    "s_nop 1\n\t" \
    "s_nop 0\n\t" \
    "v_smfmac_f32_16x16x64_bf16 v[84:87], v[60:63], a[240:247], v10\n\t" \
    "v_add_f32 v80, v80, v82\n\t" \
    "v_smfmac_f32_16x16x64_bf16 v[84:87], v[64:67], a[248:255], v10\n\t" \
    "v_add_f32 v81, v81, v83\n\t" \
    "v_smfmac_f32_16x16x64_bf16 v[84:87], v[68:71], v[224:231], v10\n\t" \
    "v_cvt_pk_bf16_f32 v158, v80, v81\n\t" \
    "ds_write_b32 v16, v158 offset:0\n\t" \
    "s_nop 5\n\t" \
    "v_pk_add_f32 v[84:85], v[84:85], v[86:87]\n\t" \
    "v_cvt_pk_bf16_f32 v159, v84, v85\n\t" \
    "ds_write_b32 v16, v159 offset:256\n\t" \
    "s_waitcnt lgkmcnt(0)\n\t" \
    "s_barrier\n\t" \
    "s_cmp_eq_u32 s65, 63\n\t" \
    "s_cbranch_scc1 qk_last84_%=\n\t" \
    "s_mov_b64 s[44:45], 0xfffff\n\t" \
    "s_cmp_eq_u32 s65, 0\n\t" \
    "s_cselect_b32 s80, s74, s80\n\t" \
    "s_sub_u32 s70, s80, s74\n\t" \
    "s_max_i32 s70, s70, 0\n\t" \
    "s_min_i32 s70, s70, 16000\n\t" \
    "s_lshl_b32 s70, s70, 1\n\t" \
    "s_mul_hi_u32 s70, s70, s77\n\t" \
    "s_add_u32 s70, s70, s73\n\t" \
    "qk_W100_%=:\n\t" \
    "s_memtime s[56:57]\n\t" \
    "s_waitcnt lgkmcnt(0)\n\t" \
    "s_sub_u32 s69, s70, s56\n\t" \
    "s_cmp_gt_i32 s69, 24\n\t" \
    "s_cbranch_scc1 qk_W100_%=\n\t" \
    "global_load_dword v160, v20, s[46:47] sc1\n\t" \
    "s_mov_b32 s71, 3\n\t" \
    "qk_E104_%=:\n\t" \
    "s_memtime s[54:55]\n\t" \
    "s_nop 0\n\t" \
    "s_memrealtime s[4:5]\n\t" \
    "s_nop 0\n\t" \
    "s_memrealtime s[6:7]\n\t" \
    "s_nop 0\n\t" \
    "s_memrealtime s[8:9]\n\t" \
    "s_nop 0\n\t" \
    "s_memrealtime s[10:11]\n\t" \
    "s_nop 0\n\t" \
    "s_memrealtime s[12:13]\n\t" \
    "s_nop 0\n\t" \
    "s_memrealtime s[14:15]\n\t" \
    "s_nop 0\n\t" \
    "s_memrealtime s[16:17]\n\t" \
    "s_nop 0\n\t" \
    "s_memrealtime s[18:19]\n\t" \
    "s_nop 0\n\t" \
    "s_memrealtime s[20:21]\n\t" \
    "s_nop 0\n\t" \
    "s_memrealtime s[22:23]\n\t" \
    "s_nop 0\n\t" \
    "s_memrealtime s[24:25]\n\t" \
    "s_nop 0\n\t" \
    "s_memrealtime s[26:27]\n\t" \
    "s_nop 0\n\t" \
    "s_memrealtime s[28:29]\n\t" \
    "s_nop 0\n\t" \
    "s_memtime s[56:57]\n\t" \
    "s_mov_b32 s70, 1\n\t" \
    "s_sub_u32 s71, s71, 1\n\t" \
    "s_cselect_b32 s69, 0, -1\n\t" \
    "s_waitcnt lgkmcnt(0)\n\t" \
    "s_sub_u32 s54, s56, s54\n\t" \
    "s_and_b32 s54, s54, s69\n\t" \
    "s_cmp_gt_u32 s54, 124\n\t" \
    "s_cbranch_scc1 qk_E104_%=\n\t" \
    "s_cmp_eq_u32 s4, s28\n\t" \
    "s_addc_u32 s70, s70, 0\n\t" \
    "s_cmp_eq_u32 s6, s28\n\t" \
    "s_addc_u32 s70, s70, 0\n\t" \
    "s_cmp_eq_u32 s8, s28\n\t" \
    "s_addc_u32 s70, s70, 0\n\t" \
    "s_cmp_eq_u32 s10, s28\n\t" \
    "s_addc_u32 s70, s70, 0\n\t" \
    "s_cmp_eq_u32 s12, s28\n\t" \
    "s_addc_u32 s70, s70, 0\n\t" \
    "s_cmp_eq_u32 s14, s28\n\t" \
    "s_addc_u32 s70, s70, 0\n\t" \
    "s_cmp_eq_u32 s16, s28\n\t" \
    "s_addc_u32 s70, s70, 0\n\t" \
    "s_cmp_eq_u32 s18, s28\n\t" \
    "s_addc_u32 s70, s70, 0\n\t" \
    "s_cmp_eq_u32 s20, s28\n\t" \
    "s_addc_u32 s70, s70, 0\n\t" \
    "s_cmp_eq_u32 s22, s28\n\t" \
    "s_addc_u32 s70, s70, 0\n\t" \
    "s_cmp_eq_u32 s24, s28\n\t" \
    "s_addc_u32 s70, s70, 0\n\t" \
    "s_cmp_eq_u32 s26, s28\n\t" \
    "s_addc_u32 s70, s70, 0\n\t" \
    "s_mov_b32 s75, s73\n\t" \
    "s_mov_b32 s76, s74\n\t" \
    "s_lshl_b32 s70, s70, 3\n\t" \
    "s_sub_u32 s73, s56, s70\n\t" \
    "s_lshl_b32 s74, s28, 4\n\t" \
    "s_sub_u32 s69, s73, s75\n\t" \
    "s_sub_u32 s70, s74, s76\n\t" \
    "s_sub_u32 s71, s70, 1600\n\t" \
    "s_cmp_lt_u32 s71, 400000\n\t" \
    "s_cbranch_scc0 qk_K106_%=\n\t" \
    "v_cvt_f32_u32 v161, s69\n\t" \
    "v_cvt_f32_u32 v162, s70\n\t" \
    "v_rcp_f32 v163, v161\n\t" \
    "v_rcp_f32 v164, v162\n\t" \
    "v_mul_f32 v163, v163, v162\n\t" \
    "v_mul_f32 v164, v164, v161\n\t" \
    "v_mul_f32 v163, 0x4f800000, v163\n\t" \
    "v_mul_f32 v164, 0x4f000000, v164\n\t" \
    "v_cvt_u32_f32 v163, v163\n\t" \
    "v_cvt_u32_f32 v164, v164\n\t" \
    "v_readfirstlane_b32 s69, v163\n\t" \
    "v_readfirstlane_b32 s70, v164\n\t" \
    "s_nop 1\n\t" \
    "s_sub_u32 s69, s69, s78\n\t" \
    "s_ashr_i32 s69, s69, 2\n\t" \
    "s_add_u32 s78, s78, s69\n\t" \
    "s_sub_u32 s70, s70, s77\n\t" \
    "s_ashr_i32 s70, s70, 2\n\t" \
    "s_add_u32 s77, s77, s70\n\t" \
    "qk_K106_%=:\n\t" \
    "s_waitcnt vmcnt(0)\n\t" \
    "v_cmp_eq_u32 vcc, -1, v160\n\t" \
    "s_cbranch_vccz qk_Hok102_%=\n\t" \
    "qk_H101_%=:\n\t" \
    "global_load_dword v160, v20, s[46:47] sc1\n\t" \
    "s_waitcnt vmcnt(0)\n\t" \
    "v_cmp_eq_u32 vcc, -1, v160\n\t" \
    "s_cbranch_vccz qk_Hc103_%=\n\t" \
    "s_cmp_eq_u32 s68, 0\n\t" \
    "s_cbranch_scc1 qk_F107_%=\n\t" \
    "s_sub_u32 s68, s68, 1\n\t" \
    "s_sleep 1\n\t" \
    "s_branch qk_H101_%=\n\t" \
    "qk_F107_%=:\n\t" \
    "v_mov_b32 v161, 6095104\n\t" \
    "v_mov_b32 v162, 5\n\t" \
    "global_store_dword v161, v162, s[36:37] sc1\n\t" \
    "s_waitcnt vmcnt(0)\n\t" \
    "s_branch qk_Hc103_%=\n\t" \
    "qk_Hc103_%=:\n\t" \
    "qk_Hok102_%=:\n\t" \
    "v_subrev_u32 v160, s80, v160\n\t" \
    "v_and_b32 v20, -5, v20\n\t" \
    "s_nop 0\n\t" \
    "v_max_i32_dpp v160, v160, v160 row_ror:1 row_mask:0xf bank_mask:0xf\n\t" \
    "s_nop 1\n\t" \
    "v_max_i32_dpp v160, v160, v160 row_ror:2 row_mask:0xf bank_mask:0xf\n\t" \
    "s_nop 1\n\t" \
    "v_max_i32_dpp v160, v160, v160 row_ror:4 row_mask:0xf bank_mask:0xf\n\t" \
    "s_nop 1\n\t" \
    "v_max_i32_dpp v160, v160, v160 row_ror:8 row_mask:0xf bank_mask:0xf\n\t" \
    "s_nop 0\n\t" \
    "v_readlane_b32 s69, v160, 0\n\t" \
    "v_readlane_b32 s71, v160, 16\n\t" \
    "v_readlane_b32 s70, v160, 32\n\t" \
    "v_readlane_b32 s81, v160, 48\n\t" \
    "s_max_i32 s69, s69, s71\n\t" \
    "s_nop 0\n\t" \
    "s_max_i32 s71, s70, s81\n\t" \
    "s_max_i32 s69, s69, s71\n\t" \
    "s_add_u32 s81, s69, s80\n\t" \
    "v_mov_b32 v72, 0\n\t" \
    "v_mov_b32 v73, 0\n\t" \
    "v_mov_b32 v74, 0\n\t" \
    "v_mov_b32 v75, 0\n\t" \
    "v_mov_b32 v76, 0\n\t" \
    "v_mov_b32 v77, 0\n\t" \
    "v_mov_b32 v78, 0\n\t" \
    "v_mov_b32 v79, 0\n\t" \
    "v_mov_b32 v80, 0\n\t" \
    "v_mov_b32 v81, 0\n\t" \
    "v_mov_b32 v82, 0\n\t" \
    "v_mov_b32 v83, 0\n\t" \
    "v_mov_b32 v84, 0\n\t" \
    "v_mov_b32 v85, 0\n\t" \
    "v_mov_b32 v86, 0\n\t" \
    "v_mov_b32 v87, 0\n\t" \
    "s_cmp_lg_u32 s65, 0\n\t" \
    "s_cbranch_scc1 qk_Oc110_%=\n\t" \
    "s_add_u32 s79, s81, 240\n\t" \
    "s_sub_u32 s70, s79, s74\n\t" \
    "s_max_i32 s70, s70, 0\n\t" \
    "s_min_i32 s70, s70, 16000\n\t" \
    "s_lshl_b32 s70, s70, 1\n\t" \
    "s_mul_hi_u32 s70, s70, s77\n\t" \
    "s_add_u32 s70, s70, s73\n\t" \
    "qk_W111_%=:\n\t" \
    "s_memtime s[56:57]\n\t" \
    "s_waitcnt lgkmcnt(0)\n\t" \
    "s_sub_u32 s69, s70, s56\n\t" \
    "s_cmp_gt_i32 s69, 24\n\t" \
    "s_cbranch_scc1 qk_W111_%=\n\t" \
    "global_load_dword v160, v35, s[46:47] sc1\n\t" \
    "s_waitcnt vmcnt(0)\n\t" \
    "v_cmp_eq_u32 vcc, -1, v160\n\t" \
    "s_cbranch_vccz qk_Ook109_%=\n\t" \
    "qk_O108_%=:\n\t" \
    "global_load_dword v160, v35, s[46:47] offset:1536 sc1\n\t" \
    "s_waitcnt vmcnt(0)\n\t" \
    "v_cmp_eq_u32 vcc, -1, v160\n\t" \
    "s_cbranch_vccz qk_Ook109_%=\n\t" \
    "s_cmp_eq_u32 s68, 0\n\t" \
    "s_cbranch_scc1 qk_F112_%=\n\t" \
    "s_sub_u32 s68, s68, 1\n\t" \
    "s_sleep 3\n\t" \
    "s_branch qk_O108_%=\n\t" \
    "qk_F112_%=:\n\t" \
    "v_mov_b32 v161, 6095104\n\t" \
    "v_mov_b32 v162, 16\n\t" \
    "global_store_dword v161, v162, s[36:37] sc1\n\t" \
    "s_waitcnt vmcnt(0)\n\t" \
    "s_branch qk_Ook109_%=\n\t" \
    "qk_Ook109_%=:\n\t" \
    "v_subrev_u32 v160, s80, v160\n\t" \
    "s_nop 1\n\t" \
    "v_max_i32_dpp v160, v160, v160 row_ror:1 row_mask:0xf bank_mask:0xf\n\t" \
    "s_nop 1\n\t" \
    "v_max_i32_dpp v160, v160, v160 row_ror:2 row_mask:0xf bank_mask:0xf\n\t" \
    "s_nop 1\n\t" \
    "v_max_i32_dpp v160, v160, v160 row_ror:4 row_mask:0xf bank_mask:0xf\n\t" \
    "s_nop 1\n\t" \
    "v_max_i32_dpp v160, v160, v160 row_ror:8 row_mask:0xf bank_mask:0xf\n\t" \
    "s_nop 0\n\t" \
    "v_readlane_b32 s69, v160, 0\n\t" \
    "v_readlane_b32 s71, v160, 16\n\t" \
    "v_readlane_b32 s70, v160, 32\n\t" \
    "v_readlane_b32 s81, v160, 48\n\t" \
    "s_max_i32 s69, s69, s71\n\t" \
    "s_nop 0\n\t" \
    "s_max_i32 s71, s70, s81\n\t" \
    "s_max_i32 s69, s69, s71\n\t" \
    "s_add_u32 s81, s69, s80\n\t" \
    "qk_Oc110_%=:\n\t" \
    "s_mul_hi_u32 s69, s85, s78\n\t" \
    "s_add_u32 s79, s81, s69\n\t" \
    "s_add_u32 s79, s79, s87\n\t" \
    "s_cmp_ge_u32 s65, 36\n\t" \
    "s_cselect_b32 s69, 28, 0\n\t" \
    "s_sub_u32 s79, s79, s69\n\t" \
    "s_cmp_lt_u32 s65, 8\n\t" \
    "s_cselect_b32 s69, 64, 0\n\t" \
    "s_add_u32 s79, s79, s69\n\t" \
    "s_cmp_ge_u32 s65, 57\n\t" \
    "s_cselect_b32 s69, 32, 0\n\t" \
    "s_add_u32 s79, s79, s69\n\t" \
    "s_add_u32 s80, s79, s88\n\t" \
    "s_sub_u32 s79, s79, s74\n\t" \
    "s_max_i32 s79, s79, 0\n\t" \
    "s_min_i32 s79, s79, 16000\n\t" \
    "s_lshl_b32 s79, s79, 1\n\t" \
    "s_mul_hi_u32 s79, s79, s77\n\t" \
    "s_add_u32 s79, s79, s73\n\t" \
    "v_mov_b32 v160, s79\n\t" \
    "ds_write_b32 v11, v160 offset:20480\n\t" \
    "s_add_u32 s65, s65, 1\n\t" \
    "v_add_u32 v31, 1, v31\n\t" \
    "s_add_u32 s69, s65, 0\n\t" \
    "s_bitcmp1_b32 s69, 0\n\t" \
    "s_cselect_b32 s70, s63, s62\n\t" \
    "s_bfe_u32 s69, s69, 0x20001\n\t" \
    "s_lshl_b32 s69, s69, 19\n\t" \
    "s_add_u32 s69, s69, s70\n\t" \
    "s_add_u32 s46, s36, s69\n\t" \
    "s_addc_u32 s47, s37, 0\n\t" \
    "s_mov_b64 s[42:43], s[46:47]\n\t" \
    "s_add_u32 s69, s65, 0\n\t" \
    "s_lshr_b32 s69, s69, 1\n\t" \
    "s_xor_b32 s69, s69, s97\n\t" \
    "s_and_b32 s69, s69, 1\n\t" \
    "s_lshl_b32 s69, s69, 14\n\t" \
    "s_or_b32 s46, s46, s69\n\t" \
    "s_waitcnt lgkmcnt(0)\n\t" \
    "s_barrier\n\t" \
    "s_branch qk_go89_%=\n\t" \
    "qk_Gbad95_%=:\n\t" \
    "s_waitcnt vmcnt(0) lgkmcnt(0)\n\t" \
    "s_nop 7\n\t" \
    "v_mov_b32 v72, 0\n\t" \
    "v_mov_b32 v73, 0\n\t" \
    "v_mov_b32 v74, 0\n\t" \
    "v_mov_b32 v75, 0\n\t" \
    "v_mov_b32 v76, 0\n\t" \
    "v_mov_b32 v77, 0\n\t" \
    "v_mov_b32 v78, 0\n\t" \
    "v_mov_b32 v79, 0\n\t" \
    "v_mov_b32 v80, 0\n\t" \
    "v_mov_b32 v81, 0\n\t" \
    "v_mov_b32 v82, 0\n\t" \
    "v_mov_b32 v83, 0\n\t" \
    "v_mov_b32 v84, 0\n\t" \
    "v_mov_b32 v85, 0\n\t" \
    "v_mov_b32 v86, 0\n\t" \
    "v_mov_b32 v87, 0\n\t" \
    "s_cmp_eq_u32 s68, 0\n\t" \
    "s_cbranch_scc1 qk_F113_%=\n\t" \
    "s_sub_u32 s68, s68, 1\n\t" \
    "s_branch qk_G94_%=\n\t" \
    "qk_F113_%=:\n\t" \
    "v_mov_b32 v160, 6095104\n\t" \
    "v_mov_b32 v161, 1\n\t" \
    "global_store_dword v160, v161, s[36:37] sc1\n\t" \
    "s_waitcnt vmcnt(0)\n\t" \
    "s_branch qk_Gc97_%=\n\t" \
    "qk_Gbad296_%=:\n\t" \
    "global_load_dwordx4 v[44:47], v2, s[46:47] sc1\n\t" \
    "s_waitcnt vmcnt(0)\n\t" \
    "v_max3_u32 v160, v44, v45, v46\n\t" \
    "v_max_u32 v160, v160, v47\n\t" \
    "v_cmp_eq_u32 vcc, -1, v160\n\t" \
    "s_cbranch_vccz qk_Gre299_%=\n\t" \
    "s_cmp_eq_u32 s68, 0\n\t" \
    "s_cbranch_scc1 qk_F114_%=\n\t" \
    "s_sub_u32 s68, s68, 1\n\t" \
    "s_sleep 1\n\t" \
    "s_branch qk_Gbad296_%=\n\t" \
    "qk_F114_%=:\n\t" \
    "v_mov_b32 v160, 6095104\n\t" \
    "v_mov_b32 v161, 2\n\t" \
    "global_store_dword v160, v161, s[36:37] sc1\n\t" \
    "s_waitcnt vmcnt(0)\n\t" \
    "s_branch qk_Gre299_%=\n\t" \
    "qk_Gre299_%=:\n\t" \
    "v_mov_b32 v72, 0\n\t" \
    "v_mov_b32 v73, 0\n\t" \
    "v_mov_b32 v74, 0\n\t" \
    "v_mov_b32 v75, 0\n\t" \
    "v_mov_b32 v76, 0\n\t" \
    "v_mov_b32 v77, 0\n\t" \
    "v_mov_b32 v78, 0\n\t" \
    "v_mov_b32 v79, 0\n\t" \
    "v_mov_b32 v80, 0\n\t" \
    "v_mov_b32 v81, 0\n\t" \
    "v_mov_b32 v82, 0\n\t" \
    "v_mov_b32 v83, 0\n\t" \
    "v_mov_b32 v84, 0\n\t" \
    "v_mov_b32 v85, 0\n\t" \
    "v_mov_b32 v86, 0\n\t" \
    "v_mov_b32 v87, 0\n\t" \
    "v_smfmac_f32_16x16x64_bf16 v[72:75], v[36:39], a[48:55], v10\n\t" \
    "v_smfmac_f32_16x16x64_bf16 v[76:79], v[36:39], a[120:127], v10\n\t" \
    "v_smfmac_f32_16x16x64_bf16 v[80:83], v[36:39], a[192:199], v10\n\t" \
    "v_smfmac_f32_16x16x64_bf16 v[84:87], v[36:39], v[232:239], v10\n\t" \
    "v_smfmac_f32_16x16x64_bf16 v[72:75], v[40:43], a[56:63], v10\n\t" \
    "v_smfmac_f32_16x16x64_bf16 v[76:79], v[40:43], a[128:135], v10\n\t" \
    "v_smfmac_f32_16x16x64_bf16 v[80:83], v[40:43], a[200:207], v10\n\t" \
    "v_smfmac_f32_16x16x64_bf16 v[84:87], v[40:43], v[240:247], v10\n\t" \
    "v_smfmac_f32_16x16x64_bf16 v[72:75], v[44:47], a[64:71], v10\n\t" \
    "v_smfmac_f32_16x16x64_bf16 v[76:79], v[44:47], a[136:143], v10\n\t" \
    "v_smfmac_f32_16x16x64_bf16 v[80:83], v[44:47], a[208:215], v10\n\t" \
    "v_smfmac_f32_16x16x64_bf16 v[84:87], v[44:47], v[248:255], v10\n\t" \
    "s_waitcnt lgkmcnt(0)\n\t" \
    "s_memtime s[56:57]\n\t" \
    "s_waitcnt lgkmcnt(0)\n\t" \
    "s_sub_u32 s69, s56, s73\n\t" \
    "s_mul_hi_u32 s69, s69, s78\n\t" \
    "s_branch qk_Gc298_%=\n\t" \
    "qk_last84_%=:\n\t" \
    "s_endpgm\n\t" \
    "vv_begin_%=:\n\t" \
    "v_mbcnt_lo_u32_b32 v223, -1, 0\n\t" \
    "v_mbcnt_hi_u32_b32 v223, -1, v223\n\t" \
    "s_mov_b32 s68, 262144\n\t" \
    "s_mov_b32 s72, 0\n\t" \
    "s_mov_b32 s83, -1\n\t" \
    "s_mov_b32 s65, 0\n\t" \
    "v_and_b32 v180, 3, v223\n\t" \
    "v_lshlrev_b32 v202, 3, v180\n\t" \
    "v_lshrrev_b32 v181, 5, v223\n\t" \
    "v_lshl_add_u32 v202, v181, 12, v202\n\t" \
    "v_bfe_u32 v182, v223, 2, 2\n\t" \
    "s_mov_b32 s70, 288\n\t" \
    "v_mad_u32_u24 v202, v182, s70, v202\n\t" \
    "v_bfe_u32 v183, v223, 4, 1\n\t" \
    "s_mov_b32 s70, 2176\n\t" \
    "v_mad_u32_u24 v202, v183, s70, v202\n\t" \
    "s_lshl_b32 s69, s66, 6\n\t" \
    "v_xor_b32 v202, s69, v202\n\t" \
    "v_xor_b32 v203, 32, v202\n\t" \
    "s_mul_i32 s69, s66, 9216\n\t" \
    "v_and_b32 v184, 1, v223\n\t" \
    "v_lshl_add_u32 v201, v184, 4, s69\n\t" \
    "v_lshrrev_b32 v185, 3, v223\n\t" \
    "s_mov_b32 s70, 1152\n\t" \
    "v_mad_u32_u24 v201, v185, s70, v201\n\t" \
    "v_bfe_u32 v186, v223, 1, 2\n\t" \
    "v_lshlrev_b32 v204, 5, v186\n\t" \
    "v_lshrrev_b32 v187, 4, v223\n\t" \
    "v_lshlrev_b32 v205, 5, v187\n\t" \
    "v_xor_b32 v204, v204, v205\n\t" \
    "v_add_u32 v201, v201, v204\n\t" \
    "s_mul_i32 s96, s66, 9216\n\t" \
    "v_lshlrev_b32 v207, 3, v180\n\t" \
    "v_bfe_u32 v188, v223, 2, 1\n\t" \
    "v_lshl_add_u32 v207, v188, 7, v207\n\t" \
    "v_bfe_u32 v189, v223, 3, 2\n\t" \
    "s_mov_b32 s70, 288\n\t" \
    "v_mad_u32_u24 v207, v189, s70, v207\n\t" \
    "s_mov_b32 s70, 9216\n\t" \
    "v_mad_u32_u24 v207, v181, s70, v207\n\t" \
    "s_mul_i32 s69, s66, 9\n\t" \
    "s_add_u32 s69, s69, 0\n\t" \
    "s_and_b32 s70, s69, 3\n\t" \
    "s_lshl_b32 s70, s70, 5\n\t" \
    "s_lshr_b32 s69, s69, 2\n\t" \
    "s_lshl_b32 s69, s69, 10\n\t" \
    "v_xor_b32 v204, s70, v207\n\t" \
    "v_add_u32 v204, s69, v204\n\t" \
    "s_mul_i32 s69, s66, 9\n\t" \
    "s_add_u32 s69, s69, 1\n\t" \
    "s_and_b32 s70, s69, 3\n\t" \
    "s_lshl_b32 s70, s70, 5\n\t" \
    "s_lshr_b32 s69, s69, 2\n\t" \
    "s_lshl_b32 s69, s69, 10\n\t" \
    "v_xor_b32 v205, s70, v207\n\t" \
    "v_add_u32 v205, s69, v205\n\t" \
    "s_mul_i32 s69, s66, 9\n\t" \
    "s_add_u32 s69, s69, 2\n\t" \
    "s_and_b32 s70, s69, 3\n\t" \
    "s_lshl_b32 s70, s70, 5\n\t" \
    "s_lshr_b32 s69, s69, 2\n\t" \
    "s_lshl_b32 s69, s69, 10\n\t" \
    "v_xor_b32 v206, s70, v207\n\t" \
    "v_add_u32 v206, s69, v206\n\t" \
    "s_mul_i32 s69, s66, 9\n\t" \
    "s_add_u32 s69, s69, 3\n\t" \
    "s_and_b32 s70, s69, 3\n\t" \
    "s_lshl_b32 s70, s70, 5\n\t" \
    "s_lshr_b32 s69, s69, 2\n\t" \
    "s_lshl_b32 s69, s69, 10\n\t" \
    "v_xor_b32 v207, s70, v207\n\t" \
    "v_add_u32 v207, s69, v207\n\t" \
    "s_waitcnt vmcnt(12) lgkmcnt(0)\n\t" \
    "s_barrier\n\t" \
    "s_add_u32 m0, s82, 65536\n\t" \
    "s_nop 0\n\t" \
    "global_load_lds_dwordx4 v200, s[42:43]\n\t" \
    "v_add_u32 v200, 131072, v200\n\t" \
    "s_add_u32 m0, s82, 69632\n\t" \
    "s_nop 0\n\t" \
    "global_load_lds_dwordx4 v200, s[42:43]\n\t" \
    "v_add_u32 v200, 131072, v200\n\t" \
    "s_add_u32 m0, s82, 73728\n\t" \
    "s_nop 0\n\t" \
    "global_load_lds_dwordx4 v200, s[42:43]\n\t" \
    "v_add_u32 v200, 131072, v200\n\t" \
    "s_add_u32 m0, s82, 77824\n\t" \
    "s_nop 0\n\t" \
    "global_load_lds_dwordx4 v200, s[42:43]\n\t" \
    "v_add_u32 v200, 131072, v200\n\t" \
    "ds_read_b64_tr_b16 a[0:1], v202 offset:36864\n\t" \
    "ds_read_b64_tr_b16 a[2:3], v202 offset:37888\n\t" \
    "ds_read_b64_tr_b16 a[4:5], v202 offset:45056\n\t" \
    "ds_read_b64_tr_b16 a[6:7], v202 offset:46080\n\t" \
    "ds_read_b64_tr_b16 a[72:73], v203 offset:36864\n\t" \
    "ds_read_b64_tr_b16 a[74:75], v203 offset:37888\n\t" \
    "ds_read_b64_tr_b16 a[76:77], v203 offset:45056\n\t" \
    "ds_read_b64_tr_b16 a[78:79], v203 offset:46080\n\t" \
    "v_lshlrev_b32 v0, 4, v223\n\t" \
    "s_mul_i32 s69, s66, 2048\n\t" \
    "s_bfe_u32 s71, s67, 0x10004\n\t" \
    "s_mul_i32 s71, s71, 131072\n\t" \
    "s_add_u32 s69, s69, s71\n\t" \
    "s_add_u32 s69, s69, 65536\n\t" \
    "v_lshl_add_u32 v1, v223, 4, s69\n\t" \
    "s_bfe_u32 s71, s67, 0x10004\n\t" \
    "s_mul_i32 s69, s71, 131072\n\t" \
    "s_add_u32 s69, s69, 102400\n\t" \
    "v_lshl_add_u32 v2, v223, 4, s69\n\t" \
    "v_xor_b32 v34, 131072, v1\n\t" \
    "s_mov_b32 s69, 17476\n\t" \
    "v_bfe_u32 v190, v223, 1, 1\n\t" \
    "v_mov_b32 v10, s69\n\t" \
    "s_mov_b32 s70, 43690\n\t" \
    "v_mad_u32_u24 v10, v190, s70, v10\n\t" \
    "v_mov_b32 v11, 0\n\t" \
    "s_waitcnt vmcnt(12) lgkmcnt(0)\n\t" \
    "s_barrier\n\t" \
    "s_add_u32 m0, s82, 81920\n\t" \
    "s_nop 0\n\t" \
    "global_load_lds_dwordx4 v200, s[42:43]\n\t" \
    "v_add_u32 v200, 131072, v200\n\t" \
    "s_add_u32 m0, s82, 86016\n\t" \
    "s_nop 0\n\t" \
    "global_load_lds_dwordx4 v200, s[42:43]\n\t" \
    "v_add_u32 v200, 131072, v200\n\t" \
    "s_add_u32 m0, s82, 90112\n\t" \
    "s_nop 0\n\t" \
    "global_load_lds_dwordx4 v200, s[42:43]\n\t" \
    "v_add_u32 v200, 131072, v200\n\t" \
    "s_add_u32 m0, s82, 94208\n\t" \
    "s_nop 0\n\t" \
    "global_load_lds_dwordx4 v200, s[42:43]\n\t" \
    "v_add_u32 v200, 131072, v200\n\t" \
    "ds_read_b64_tr_b16 a[8:9], v202 offset:53248\n\t" \
    "ds_read_b64_tr_b16 a[10:11], v202 offset:54272\n\t" \
    "ds_read_b64_tr_b16 a[12:13], v202 offset:61440\n\t" \
    "ds_read_b64_tr_b16 a[14:15], v202 offset:62464\n\t" \
    "ds_read_b64_tr_b16 a[80:81], v203 offset:53248\n\t" \
    "ds_read_b64_tr_b16 a[82:83], v203 offset:54272\n\t" \
    "ds_read_b64_tr_b16 a[84:85], v203 offset:61440\n\t" \
    "ds_read_b64_tr_b16 a[86:87], v203 offset:62464\n\t" \
    "s_mul_i32 s69, s66, 512\n\t" \
    "s_add_u32 s69, s69, 16384\n\t" \
    "v_and_b32 v191, 13, v223\n\t" \
    "v_lshl_add_u32 v12, v191, 5, s69\n\t" \
    "v_lshl_add_u32 v12, v190, 2, v12\n\t" \
    "v_lshl_add_u32 v12, v187, 3, v12\n\t" \
    "s_mov_b32 s69, 16384\n\t" \
    "v_and_b32 v192, 15, v223\n\t" \
    "v_lshl_add_u32 v13, v192, 3, s69\n\t" \
    "v_lshl_add_u32 v13, v187, 8, v13\n\t" \
    "s_bfe_u32 s71, s67, 0x10004\n\t" \
    "s_mul_i32 s69, s71, 32768\n\t" \
    "s_add_u32 s69, s69, 266240\n\t" \
    "v_and_b32 v193, 31, v223\n\t" \
    "v_lshl_add_u32 v20, v193, 5, s69\n\t" \
    "v_lshl_add_u32 v20, v181, 2, v20\n\t" \
    "s_mul_i32 s69, s67, 32\n\t" \
    "s_add_u32 s69, s69, 262144\n\t" \
    "v_lshl_add_u32 v21, v180, 14, s69\n\t" \
    "v_bfe_u32 v194, v223, 2, 3\n\t" \
    "v_lshlrev_b32 v199, 10, v194\n\t" \
    "v_min_u32 v199, 4096, v199\n\t" \
    "v_add_u32 v21, v199, v21\n\t" \
    "s_bfe_u32 s71, s67, 0x10004\n\t" \
    "s_mul_i32 s69, s71, 32768\n\t" \
    "s_add_u32 s69, s69, 267264\n\t" \
    "v_min_u32 v35, 35, v223\n\t" \
    "v_lshl_add_u32 v35, v35, 5, s69\n\t" \
    "v_lshlrev_b32 v22, 2, v223\n\t" \
    "s_waitcnt vmcnt(12) lgkmcnt(0)\n\t" \
    "s_barrier\n\t" \
    "s_add_u32 m0, s82, 98304\n\t" \
    "s_nop 0\n\t" \
    "global_load_lds_dwordx4 v200, s[42:43]\n\t" \
    "v_add_u32 v200, 131072, v200\n\t" \
    "s_add_u32 m0, s82, 102400\n\t" \
    "s_nop 0\n\t" \
    "global_load_lds_dwordx4 v200, s[42:43]\n\t" \
    "v_add_u32 v200, 131072, v200\n\t" \
    "s_add_u32 m0, s82, 106496\n\t" \
    "s_nop 0\n\t" \
    "global_load_lds_dwordx4 v200, s[42:43]\n\t" \
    "v_add_u32 v200, 131072, v200\n\t" \
    "s_add_u32 m0, s82, 110592\n\t" \
    "s_nop 0\n\t" \
    "global_load_lds_dwordx4 v200, s[42:43]\n\t" \
    "v_add_u32 v200, 131072, v200\n\t" \
    "v_add_u32 v202, 69632, v202\n\t" \
    "v_add_u32 v203, 69632, v203\n\t" \
    "ds_read_b64_tr_b16 a[16:17], v202 offset:0\n\t" \
    "ds_read_b64_tr_b16 a[18:19], v202 offset:1024\n\t" \
    "ds_read_b64_tr_b16 a[20:21], v202 offset:8192\n\t" \
    "ds_read_b64_tr_b16 a[22:23], v202 offset:9216\n\t" \
    "ds_read_b64_tr_b16 a[88:89], v203 offset:0\n\t" \
    "ds_read_b64_tr_b16 a[90:91], v203 offset:1024\n\t" \
    "ds_read_b64_tr_b16 a[92:93], v203 offset:8192\n\t" \
    "ds_read_b64_tr_b16 a[94:95], v203 offset:9216\n\t" \
    "s_mul_i32 s69, s66, 147456\n\t" \
    "s_mul_i32 s71, s67, 256\n\t" \
    "s_add_u32 s69, s69, s71\n\t" \
    "v_lshl_add_u32 v14, v192, 4, s69\n\t" \
    "v_lshl_add_u32 v14, v187, 14, v14\n\t" \
    "s_mul_i32 s69, s66, 147456\n\t" \
    "s_mul_i32 s71, s67, 256\n\t" \
    "s_add_u32 s69, s69, s71\n\t" \
    "s_add_u32 s69, s69, 65536\n\t" \
    "v_lshl_add_u32 v15, v192, 4, s69\n\t" \
    "v_lshl_add_u32 v15, v187, 14, v15\n\t" \
    "s_mul_i32 s69, s66, 147456\n\t" \
    "s_mul_i32 s71, s67, 256\n\t" \
    "s_add_u32 s69, s69, s71\n\t" \
    "s_add_u32 s69, s69, 131072\n\t" \
    "v_lshl_add_u32 v16, v192, 4, s69\n\t" \
    "v_lshl_add_u32 v16, v187, 2, v16\n\t" \
    "s_mul_i32 s97, s67, 171\n\t" \
    "s_lshr_b32 s97, s97, 9\n\t" \
    "s_mul_i32 s97, s97, 131072\n\t" \
    "s_mul_i32 s71, s66, 2304\n\t" \
    "s_add_u32 s69, s97, s71\n\t" \
    "s_mul_i32 s71, s67, 655360\n\t" \
    "s_add_u32 s69, s69, s71\n\t" \
    "v_lshl_add_u32 v17, v223, 4, s69\n\t" \
    "s_mul_i32 s71, s66, 2304\n\t" \
    "s_add_u32 s69, s97, s71\n\t" \
    "s_mul_i32 s71, s67, 655360\n\t" \
    "s_add_u32 s69, s69, s71\n\t" \
    "s_add_u32 s69, s69, 2048\n\t" \
    "v_lshl_add_u32 v18, v192, 4, s69\n\t" \
    "v_lshl_add_u32 v18, v187, 2, v18\n\t" \
    "s_waitcnt vmcnt(12) lgkmcnt(0)\n\t" \
    "s_barrier\n\t" \
    "s_add_u32 m0, s82, 0\n\t" \
    "s_nop 0\n\t" \
    "global_load_lds_dwordx4 v200, s[42:43]\n\t" \
    "v_add_u32 v200, 131072, v200\n\t" \
    "s_add_u32 m0, s82, 4096\n\t" \
    "s_nop 0\n\t" \
    "global_load_lds_dwordx4 v200, s[42:43]\n\t" \
    "v_add_u32 v200, 131072, v200\n\t" \
    "s_add_u32 m0, s82, 8192\n\t" \
    "s_nop 0\n\t" \
    "global_load_lds_dwordx4 v200, s[42:43]\n\t" \
    "v_add_u32 v200, 131072, v200\n\t" \
    "s_add_u32 m0, s82, 12288\n\t" \
    "s_nop 0\n\t" \
    "global_load_lds_dwordx4 v200, s[42:43]\n\t" \
    "v_add_u32 v200, 131072, v200\n\t" \
    "ds_read_b64_tr_b16 a[24:25], v202 offset:16384\n\t" \
    "ds_read_b64_tr_b16 a[26:27], v202 offset:17408\n\t" \
    "ds_read_b64_tr_b16 a[28:29], v202 offset:24576\n\t" \
    "ds_read_b64_tr_b16 a[30:31], v202 offset:25600\n\t" \
    "ds_read_b64_tr_b16 a[96:97], v203 offset:16384\n\t" \
    "ds_read_b64_tr_b16 a[98:99], v203 offset:17408\n\t" \
    "ds_read_b64_tr_b16 a[100:101], v203 offset:24576\n\t" \
    "ds_read_b64_tr_b16 a[102:103], v203 offset:25600\n\t" \
    "s_mov_b32 s77, 3019898880\n\t" \
    "s_mov_b32 s78, 3054198966\n\t" \
    "s_mov_b32 s71, 3\n\t" \
    "vv_E1_%=:\n\t" \
    "s_memtime s[54:55]\n\t" \
    "s_nop 0\n\t" \
    "s_memrealtime s[4:5]\n\t" \
    "s_nop 0\n\t" \
    "s_memrealtime s[6:7]\n\t" \
    "s_nop 0\n\t" \
    "s_memrealtime s[8:9]\n\t" \
    "s_nop 0\n\t" \
    "s_memrealtime s[10:11]\n\t" \
    "s_nop 0\n\t" \
    "s_memrealtime s[12:13]\n\t" \
    "s_nop 0\n\t" \
    "s_memrealtime s[14:15]\n\t" \
    "s_nop 0\n\t" \
    "s_memrealtime s[16:17]\n\t" \
    "s_nop 0\n\t" \
    "s_memrealtime s[18:19]\n\t" \
    "s_nop 0\n\t" \
    "s_memrealtime s[20:21]\n\t" \
    "s_nop 0\n\t" \
    "s_memrealtime s[22:23]\n\t" \
    "s_nop 0\n\t" \
    "s_memrealtime s[24:25]\n\t" \
    "s_nop 0\n\t" \
    "s_memrealtime s[26:27]\n\t" \
    "s_nop 0\n\t" \
    "s_memrealtime s[28:29]\n\t" \
    "s_nop 0\n\t" \
    "s_memtime s[56:57]\n\t" \
    "s_mov_b32 s70, 1\n\t" \
    "s_sub_u32 s71, s71, 1\n\t" \
    "s_cselect_b32 s69, 0, -1\n\t" \
    "s_waitcnt lgkmcnt(0)\n\t" \
    "s_sub_u32 s54, s56, s54\n\t" \
    "s_and_b32 s54, s54, s69\n\t" \
    "s_cmp_gt_u32 s54, 124\n\t" \
    "s_cbranch_scc1 vv_E1_%=\n\t" \
    "s_cmp_eq_u32 s4, s28\n\t" \
    "s_addc_u32 s70, s70, 0\n\t" \
    "s_cmp_eq_u32 s6, s28\n\t" \
    "s_addc_u32 s70, s70, 0\n\t" \
    "s_cmp_eq_u32 s8, s28\n\t" \
    "s_addc_u32 s70, s70, 0\n\t" \
    "s_cmp_eq_u32 s10, s28\n\t" \
    "s_addc_u32 s70, s70, 0\n\t" \
    "s_cmp_eq_u32 s12, s28\n\t" \
    "s_addc_u32 s70, s70, 0\n\t" \
    "s_cmp_eq_u32 s14, s28\n\t" \
    "s_addc_u32 s70, s70, 0\n\t" \
    "s_cmp_eq_u32 s16, s28\n\t" \
    "s_addc_u32 s70, s70, 0\n\t" \
    "s_cmp_eq_u32 s18, s28\n\t" \
    "s_addc_u32 s70, s70, 0\n\t" \
    "s_cmp_eq_u32 s20, s28\n\t" \
    "s_addc_u32 s70, s70, 0\n\t" \
    "s_cmp_eq_u32 s22, s28\n\t" \
    "s_addc_u32 s70, s70, 0\n\t" \
    "s_cmp_eq_u32 s24, s28\n\t" \
    "s_addc_u32 s70, s70, 0\n\t" \
    "s_cmp_eq_u32 s26, s28\n\t" \
    "s_addc_u32 s70, s70, 0\n\t" \
    "s_mov_b32 s75, s73\n\t" \
    "s_mov_b32 s76, s74\n\t" \
    "s_lshl_b32 s70, s70, 3\n\t" \
    "s_sub_u32 s73, s56, s70\n\t" \
    "s_lshl_b32 s74, s28, 4\n\t" \
    "s_waitcnt vmcnt(12) lgkmcnt(0)\n\t" \
    "s_barrier\n\t" \
    "s_add_u32 m0, s82, 16384\n\t" \
    "s_nop 0\n\t" \
    "global_load_lds_dwordx4 v200, s[42:43]\n\t" \
    "v_add_u32 v200, 131072, v200\n\t" \
    "s_add_u32 m0, s82, 20480\n\t" \
    "s_nop 0\n\t" \
    "global_load_lds_dwordx4 v200, s[42:43]\n\t" \
    "v_add_u32 v200, 131072, v200\n\t" \
    "s_add_u32 m0, s82, 24576\n\t" \
    "s_nop 0\n\t" \
    "global_load_lds_dwordx4 v200, s[42:43]\n\t" \
    "v_add_u32 v200, 131072, v200\n\t" \
    "s_add_u32 m0, s82, 28672\n\t" \
    "s_nop 0\n\t" \
    "global_load_lds_dwordx4 v200, s[42:43]\n\t" \
    "v_add_u32 v200, 131072, v200\n\t" \
    "ds_read_b64_tr_b16 a[32:33], v202 offset:32768\n\t" \
    "ds_read_b64_tr_b16 a[34:35], v202 offset:33792\n\t" \
    "ds_read_b64_tr_b16 a[36:37], v202 offset:40960\n\t" \
    "ds_read_b64_tr_b16 a[38:39], v202 offset:41984\n\t" \
    "ds_read_b64_tr_b16 a[104:105], v203 offset:32768\n\t" \
    "ds_read_b64_tr_b16 a[106:107], v203 offset:33792\n\t" \
    "ds_read_b64_tr_b16 a[108:109], v203 offset:40960\n\t" \
    "ds_read_b64_tr_b16 a[110:111], v203 offset:41984\n\t" \
    "s_waitcnt vmcnt(12) lgkmcnt(0)\n\t" \
    "s_barrier\n\t" \
    "s_add_u32 m0, s96, 0\n\t" \
    "s_nop 0\n\t" \
    "global_load_lds_dwordx4 v201, s[44:45] offset:0\n\t" \
    "s_add_u32 m0, s96, 896\n\t" \
    "s_nop 0\n\t" \
    "global_load_lds_dwordx4 v201, s[44:45] offset:128\n\t" \
    "s_add_u32 m0, s96, 1792\n\t" \
    "s_nop 0\n\t" \
    "global_load_lds_dwordx4 v201, s[44:45] offset:256\n\t" \
    "s_add_u32 m0, s96, 2688\n\t" \
    "s_nop 0\n\t" \
    "global_load_lds_dwordx4 v201, s[44:45] offset:384\n\t" \
    "s_add_u32 m0, s96, 3584\n\t" \
    "s_nop 0\n\t" \
    "global_load_lds_dwordx4 v201, s[44:45] offset:512\n\t" \
    "s_add_u32 m0, s96, 4480\n\t" \
    "s_nop 0\n\t" \
    "global_load_lds_dwordx4 v201, s[44:45] offset:640\n\t" \
    "s_add_u32 m0, s96, 5376\n\t" \
    "s_nop 0\n\t" \
    "global_load_lds_dwordx4 v201, s[44:45] offset:768\n\t" \
    "s_add_u32 m0, s96, 6272\n\t" \
    "s_nop 0\n\t" \
    "global_load_lds_dwordx4 v201, s[44:45] offset:896\n\t" \
    "s_add_u32 m0, s96, 7168\n\t" \
    "s_nop 0\n\t" \
    "global_load_lds_dwordx4 v201, s[44:45] offset:1024\n\t" \
    "v_add_u32 v201, 36864, v201\n\t" \
    "ds_read_b64_tr_b16 a[40:41], v202 offset:49152\n\t" \
    "ds_read_b64_tr_b16 a[42:43], v202 offset:50176\n\t" \
    "ds_read_b64_tr_b16 a[44:45], v202 offset:57344\n\t" \
    "ds_read_b64_tr_b16 a[46:47], v202 offset:58368\n\t" \
    "ds_read_b64_tr_b16 a[112:113], v203 offset:49152\n\t" \
    "ds_read_b64_tr_b16 a[114:115], v203 offset:50176\n\t" \
    "ds_read_b64_tr_b16 a[116:117], v203 offset:57344\n\t" \
    "ds_read_b64_tr_b16 a[118:119], v203 offset:58368\n\t" \
    "s_waitcnt vmcnt(17) lgkmcnt(0)\n\t" \
    "s_barrier\n\t" \
    "v_add_u32 v202, 65536, v202\n\t" \
    "v_add_u32 v203, 65536, v203\n\t" \
    "ds_read_b64_tr_b16 a[48:49], v202 offset:0\n\t" \
    "ds_read_b64_tr_b16 a[50:51], v202 offset:1024\n\t" \
    "ds_read_b64_tr_b16 a[52:53], v202 offset:8192\n\t" \
    "ds_read_b64_tr_b16 a[54:55], v202 offset:9216\n\t" \
    "ds_read_b64_tr_b16 a[120:121], v203 offset:0\n\t" \
    "ds_read_b64_tr_b16 a[122:123], v203 offset:1024\n\t" \
    "ds_read_b64_tr_b16 a[124:125], v203 offset:8192\n\t" \
    "ds_read_b64_tr_b16 a[126:127], v203 offset:9216\n\t" \
    "s_waitcnt vmcnt(13) lgkmcnt(0)\n\t" \
    "s_barrier\n\t" \
    "s_add_u32 m0, s96, 73728\n\t" \
    "s_nop 0\n\t" \
    "global_load_lds_dwordx4 v201, s[44:45] offset:0\n\t" \
    "s_add_u32 m0, s96, 74624\n\t" \
    "s_nop 0\n\t" \
    "global_load_lds_dwordx4 v201, s[44:45] offset:128\n\t" \
    "s_add_u32 m0, s96, 75520\n\t" \
    "s_nop 0\n\t" \
    "global_load_lds_dwordx4 v201, s[44:45] offset:256\n\t" \
    "s_add_u32 m0, s96, 76416\n\t" \
    "s_nop 0\n\t" \
    "global_load_lds_dwordx4 v201, s[44:45] offset:384\n\t" \
    "s_add_u32 m0, s96, 77312\n\t" \
    "s_nop 0\n\t" \
    "global_load_lds_dwordx4 v201, s[44:45] offset:512\n\t" \
    "s_add_u32 m0, s96, 78208\n\t" \
    "s_nop 0\n\t" \
    "global_load_lds_dwordx4 v201, s[44:45] offset:640\n\t" \
    "s_add_u32 m0, s96, 79104\n\t" \
    "s_nop 0\n\t" \
    "global_load_lds_dwordx4 v201, s[44:45] offset:768\n\t" \
    "s_add_u32 m0, s96, 80000\n\t" \
    "s_nop 0\n\t" \
    "global_load_lds_dwordx4 v201, s[44:45] offset:896\n\t" \
    "s_add_u32 m0, s96, 80896\n\t" \
    "s_nop 0\n\t" \
    "global_load_lds_dwordx4 v201, s[44:45] offset:1024\n\t" \
    "v_add_u32 v201, 36864, v201\n\t" \
    "v_add_u32 v202, -98304, v202\n\t" \
    "v_add_u32 v203, -98304, v203\n\t" \
    "ds_read_b64_tr_b16 a[56:57], v202 offset:0\n\t" \
    "ds_read_b64_tr_b16 a[58:59], v202 offset:1024\n\t" \
    "ds_read_b64_tr_b16 a[60:61], v202 offset:8192\n\t" \
    "ds_read_b64_tr_b16 a[62:63], v202 offset:9216\n\t" \
    "ds_read_b64_tr_b16 a[128:129], v203 offset:0\n\t" \
    "ds_read_b64_tr_b16 a[130:131], v203 offset:1024\n\t" \
    "ds_read_b64_tr_b16 a[132:133], v203 offset:8192\n\t" \
    "ds_read_b64_tr_b16 a[134:135], v203 offset:9216\n\t" \
    "s_waitcnt vmcnt(18) lgkmcnt(0)\n\t" \
    "s_barrier\n\t" \
    "s_add_u32 m0, s96, 110592\n\t" \
    "s_nop 0\n\t" \
    "global_load_lds_dwordx4 v201, s[44:45] offset:0\n\t" \
    "s_add_u32 m0, s96, 111488\n\t" \
    "s_nop 0\n\t" \
    "global_load_lds_dwordx4 v201, s[44:45] offset:128\n\t" \
    "s_add_u32 m0, s96, 112384\n\t" \
    "s_nop 0\n\t" \
    "global_load_lds_dwordx4 v201, s[44:45] offset:256\n\t" \
    "s_add_u32 m0, s96, 113280\n\t" \
    "s_nop 0\n\t" \
    "global_load_lds_dwordx4 v201, s[44:45] offset:384\n\t" \
    "s_add_u32 m0, s96, 114176\n\t" \
    "s_nop 0\n\t" \
    "global_load_lds_dwordx4 v201, s[44:45] offset:512\n\t" \
    "s_add_u32 m0, s96, 115072\n\t" \
    "s_nop 0\n\t" \
    "global_load_lds_dwordx4 v201, s[44:45] offset:640\n\t" \
    "s_add_u32 m0, s96, 115968\n\t" \
    "s_nop 0\n\t" \
    "global_load_lds_dwordx4 v201, s[44:45] offset:768\n\t" \
    "s_add_u32 m0, s96, 116864\n\t" \
    "s_nop 0\n\t" \
    "global_load_lds_dwordx4 v201, s[44:45] offset:896\n\t" \
    "s_add_u32 m0, s96, 117760\n\t" \
    "s_nop 0\n\t" \
    "global_load_lds_dwordx4 v201, s[44:45] offset:1024\n\t" \
    "v_add_u32 v201, 36864, v201\n\t" \
    "ds_read_b64_tr_b16 a[64:65], v202 offset:16384\n\t" \
    "ds_read_b64_tr_b16 a[66:67], v202 offset:17408\n\t" \
    "ds_read_b64_tr_b16 a[68:69], v202 offset:24576\n\t" \
    "ds_read_b64_tr_b16 a[70:71], v202 offset:25600\n\t" \
    "ds_read_b64_tr_b16 a[136:137], v203 offset:16384\n\t" \
    "ds_read_b64_tr_b16 a[138:139], v203 offset:17408\n\t" \
    "ds_read_b64_tr_b16 a[140:141], v203 offset:24576\n\t" \
    "ds_read_b64_tr_b16 a[142:143], v203 offset:25600\n\t" \
    "s_waitcnt vmcnt(18) lgkmcnt(0)\n\t" \
    "s_barrier\n\t" \
    "s_add_u32 m0, s96, 36864\n\t" \
    "s_nop 0\n\t" \
    "global_load_lds_dwordx4 v201, s[44:45] offset:0\n\t" \
    "s_add_u32 m0, s96, 37760\n\t" \
    "s_nop 0\n\t" \
    "global_load_lds_dwordx4 v201, s[44:45] offset:128\n\t" \
    "s_add_u32 m0, s96, 38656\n\t" \
    "s_nop 0\n\t" \
    "global_load_lds_dwordx4 v201, s[44:45] offset:256\n\t" \
    "s_add_u32 m0, s96, 39552\n\t" \
    "s_nop 0\n\t" \
    "global_load_lds_dwordx4 v201, s[44:45] offset:384\n\t" \
    "s_add_u32 m0, s96, 40448\n\t" \
    "s_nop 0\n\t" \
    "global_load_lds_dwordx4 v201, s[44:45] offset:512\n\t" \
    "s_add_u32 m0, s96, 41344\n\t" \
    "s_nop 0\n\t" \
    "global_load_lds_dwordx4 v201, s[44:45] offset:640\n\t" \
    "s_add_u32 m0, s96, 42240\n\t" \
    "s_nop 0\n\t" \
    "global_load_lds_dwordx4 v201, s[44:45] offset:768\n\t" \
    "s_add_u32 m0, s96, 43136\n\t" \
    "s_nop 0\n\t" \
    "global_load_lds_dwordx4 v201, s[44:45] offset:896\n\t" \
    "s_add_u32 m0, s96, 44032\n\t" \
    "s_nop 0\n\t" \
    "global_load_lds_dwordx4 v201, s[44:45] offset:1024\n\t" \
    "v_add_u32 v201, 36864, v201\n\t" \
    "ds_read_b64_tr_b16 a[144:145], v204 offset:0\n\t" \
    "ds_read_b64_tr_b16 a[146:147], v204 offset:18432\n\t" \
    "ds_read_b64_tr_b16 a[160:161], v205 offset:0\n\t" \
    "ds_read_b64_tr_b16 a[162:163], v205 offset:18432\n\t" \
    "ds_read_b64_tr_b16 a[176:177], v206 offset:0\n\t" \
    "ds_read_b64_tr_b16 a[178:179], v206 offset:18432\n\t" \
    "s_waitcnt lgkmcnt(6)\n\t" \
    "ds_read_b64_tr_b16 a[192:193], v207 offset:0\n\t" \
    "ds_read_b64_tr_b16 a[194:195], v207 offset:18432\n\t" \
    "ds_read_b64_tr_b16 a[208:209], v204 offset:1024\n\t" \
    "ds_read_b64_tr_b16 a[210:211], v204 offset:19456\n\t" \
    "ds_read_b64_tr_b16 a[224:225], v205 offset:1024\n\t" \
    "ds_read_b64_tr_b16 a[226:227], v205 offset:19456\n\t" \
    "s_waitcnt lgkmcnt(6)\n\t" \
    "ds_read_b64_tr_b16 a[240:241], v206 offset:1024\n\t" \
    "ds_read_b64_tr_b16 a[242:243], v206 offset:19456\n\t" \
    "ds_read_b64_tr_b16 v[224:225], v207 offset:1024\n\t" \
    "ds_read_b64_tr_b16 v[226:227], v207 offset:19456\n\t" \
    "ds_read_b64_tr_b16 v[240:241], v204 offset:2048\n\t" \
    "ds_read_b64_tr_b16 v[242:243], v204 offset:20480\n\t" \
    "s_waitcnt lgkmcnt(6)\n\t" \
    "s_waitcnt vmcnt(18) lgkmcnt(0)\n\t" \
    "s_barrier\n\t" \
    "v_mbcnt_lo_u32_b32 v140, -1, 0\n\t" \
    "v_mbcnt_hi_u32_b32 v140, -1, v140\n\t" \
    "v_min_u32 v140, 59, v140\n\t" \
    "v_lshlrev_b32 v140, 6, v140\n\t" \
    "v_add_u32 v140, 6258688, v140\n\t" \
    "v_mov_b32 v141, 6266880\n\t" \
    "global_load_dword v141, v141, s[36:37] sc1\n\t" \
    "global_load_dwordx2 v[142:143], v140, s[36:37] sc1\n\t" \
    "v_add_u32 v204, 73728, v204\n\t" \
    "v_add_u32 v205, 73728, v205\n\t" \
    "v_add_u32 v206, 73728, v206\n\t" \
    "v_add_u32 v207, 73728, v207\n\t" \
    "ds_read_b64_tr_b16 a[148:149], v204 offset:0\n\t" \
    "ds_read_b64_tr_b16 a[150:151], v204 offset:18432\n\t" \
    "ds_read_b64_tr_b16 a[164:165], v205 offset:0\n\t" \
    "ds_read_b64_tr_b16 a[166:167], v205 offset:18432\n\t" \
    "ds_read_b64_tr_b16 a[180:181], v206 offset:0\n\t" \
    "ds_read_b64_tr_b16 a[182:183], v206 offset:18432\n\t" \
    "s_waitcnt lgkmcnt(6)\n\t" \
    "ds_read_b64_tr_b16 a[196:197], v207 offset:0\n\t" \
    "ds_read_b64_tr_b16 a[198:199], v207 offset:18432\n\t" \
    "ds_read_b64_tr_b16 a[212:213], v204 offset:1024\n\t" \
    "ds_read_b64_tr_b16 a[214:215], v204 offset:19456\n\t" \
    "ds_read_b64_tr_b16 a[228:229], v205 offset:1024\n\t" \
    "ds_read_b64_tr_b16 a[230:231], v205 offset:19456\n\t" \
    "s_waitcnt lgkmcnt(6)\n\t" \
    "ds_read_b64_tr_b16 a[244:245], v206 offset:1024\n\t" \
    "ds_read_b64_tr_b16 a[246:247], v206 offset:19456\n\t" \
    "ds_read_b64_tr_b16 v[228:229], v207 offset:1024\n\t" \
    "ds_read_b64_tr_b16 v[230:231], v207 offset:19456\n\t" \
    "ds_read_b64_tr_b16 v[244:245], v204 offset:2048\n\t" \
    "ds_read_b64_tr_b16 v[246:247], v204 offset:20480\n\t" \
    "s_waitcnt lgkmcnt(6)\n\t" \
    "s_waitcnt vmcnt(11) lgkmcnt(0)\n\t" \
    "s_barrier\n\t" \
    "ds_read_b64_tr_b16 a[152:153], v204 offset:36864\n\t" \
    "ds_read_b64_tr_b16 a[154:155], v204 offset:55296\n\t" \
    "ds_read_b64_tr_b16 a[168:169], v205 offset:36864\n\t" \
    "ds_read_b64_tr_b16 a[170:171], v205 offset:55296\n\t" \
    "ds_read_b64_tr_b16 a[184:185], v206 offset:36864\n\t" \
    "ds_read_b64_tr_b16 a[186:187], v206 offset:55296\n\t" \
    "s_waitcnt lgkmcnt(6)\n\t" \
    "ds_read_b64_tr_b16 a[200:201], v207 offset:36864\n\t" \
    "ds_read_b64_tr_b16 a[202:203], v207 offset:55296\n\t" \
    "ds_read_b64_tr_b16 a[216:217], v204 offset:37888\n\t" \
    "ds_read_b64_tr_b16 a[218:219], v204 offset:56320\n\t" \
    "ds_read_b64_tr_b16 a[232:233], v205 offset:37888\n\t" \
    "ds_read_b64_tr_b16 a[234:235], v205 offset:56320\n\t" \
    "s_waitcnt lgkmcnt(6)\n\t" \
    "ds_read_b64_tr_b16 a[248:249], v206 offset:37888\n\t" \
    "ds_read_b64_tr_b16 a[250:251], v206 offset:56320\n\t" \
    "ds_read_b64_tr_b16 v[232:233], v207 offset:37888\n\t" \
    "ds_read_b64_tr_b16 v[234:235], v207 offset:56320\n\t" \
    "ds_read_b64_tr_b16 v[248:249], v204 offset:38912\n\t" \
    "ds_read_b64_tr_b16 v[250:251], v204 offset:57344\n\t" \
    "s_waitcnt lgkmcnt(6)\n\t" \
    "s_waitcnt vmcnt(2) lgkmcnt(0)\n\t" \
    "s_barrier\n\t" \
    "v_add_u32 v204, -36864, v204\n\t" \
    "v_add_u32 v205, -36864, v205\n\t" \
    "v_add_u32 v206, -36864, v206\n\t" \
    "v_add_u32 v207, -36864, v207\n\t" \
    "ds_read_b64_tr_b16 a[156:157], v204 offset:0\n\t" \
    "ds_read_b64_tr_b16 a[158:159], v204 offset:18432\n\t" \
    "ds_read_b64_tr_b16 a[172:173], v205 offset:0\n\t" \
    "ds_read_b64_tr_b16 a[174:175], v205 offset:18432\n\t" \
    "ds_read_b64_tr_b16 a[188:189], v206 offset:0\n\t" \
    "ds_read_b64_tr_b16 a[190:191], v206 offset:18432\n\t" \
    "s_waitcnt lgkmcnt(6)\n\t" \
    "ds_read_b64_tr_b16 a[204:205], v207 offset:0\n\t" \
    "ds_read_b64_tr_b16 a[206:207], v207 offset:18432\n\t" \
    "ds_read_b64_tr_b16 a[220:221], v204 offset:1024\n\t" \
    "ds_read_b64_tr_b16 a[222:223], v204 offset:19456\n\t" \
    "ds_read_b64_tr_b16 a[236:237], v205 offset:1024\n\t" \
    "ds_read_b64_tr_b16 a[238:239], v205 offset:19456\n\t" \
    "s_waitcnt lgkmcnt(6)\n\t" \
    "ds_read_b64_tr_b16 a[252:253], v206 offset:1024\n\t" \
    "ds_read_b64_tr_b16 a[254:255], v206 offset:19456\n\t" \
    "ds_read_b64_tr_b16 v[236:237], v207 offset:1024\n\t" \
    "ds_read_b64_tr_b16 v[238:239], v207 offset:19456\n\t" \
    "ds_read_b64_tr_b16 v[252:253], v204 offset:2048\n\t" \
    "ds_read_b64_tr_b16 v[254:255], v204 offset:20480\n\t" \
    "s_waitcnt lgkmcnt(6)\n\t" \
    "s_waitcnt vmcnt(0) lgkmcnt(0)\n\t" \
    "s_barrier\n\t" \
    "v_sub_u32 v142, v142, v141\n\t" \
    "v_cmp_ne_u32 vcc, 1, v142\n\t" \
    "s_cbranch_vccz vv_Cok4_%=\n\t" \
    "vv_C3_%=:\n\t" \
    "global_load_dwordx2 v[142:143], v140, s[36:37] sc1\n\t" \
    "s_waitcnt vmcnt(0)\n\t" \
    "v_sub_u32 v142, v142, v141\n\t" \
    "v_cmp_ne_u32 vcc, 1, v142\n\t" \
    "s_cbranch_vccz vv_Cok4_%=\n\t" \
    "s_cmp_eq_u32 s68, 0\n\t" \
    "s_cbranch_scc1 vv_F5_%=\n\t" \
    "s_sub_u32 s68, s68, 1\n\t" \
    "s_sleep 4\n\t" \
    "s_branch vv_C3_%=\n\t" \
    "vv_F5_%=:\n\t" \
    "v_mov_b32 v144, 6095104\n\t" \
    "v_mov_b32 v145, 4\n\t" \
    "global_store_dword v144, v145, s[36:37] sc1\n\t" \
    "s_waitcnt vmcnt(0)\n\t" \
    "s_branch vv_Cok4_%=\n\t" \
    "vv_Cok4_%=:\n\t" \
    "s_cmp_lg_u32 s66, 0\n\t" \
    "s_cbranch_scc1 vv_Ca6_%=\n\t" \
    "v_mov_b32 v140, 6176768\n\t" \
    "s_mov_b64 exec, 1\n\t" \
    "global_atomic_add v140, v142, s[36:37] sc1\n\t" \
    "s_mov_b64 exec, -1\n\t" \
    "vv_Ca6_%=:\n\t" \
    "v_sub_u32_dpp v142, v143, v143 row_shl:6 row_mask:0xf bank_mask:0xf\n\t" \
    "v_min_u32_dpp v143, v143, v143 row_shl:6 row_mask:0xf bank_mask:0xf\n\t" \
    "v_min_i32 v142, 64, v142\n\t" \
    "v_max_i32 v142, -64, v142\n\t" \
    "s_mov_b32 s97, 0\n\t" \
    "v_readlane_b32 s69, v143, 0\n\t" \
    "v_readlane_b32 s70, v143, 1\n\t" \
    "v_readlane_b32 s54, v142, 0\n\t" \
    "v_readlane_b32 s55, v142, 1\n\t" \
    "s_nop 1\n\t" \
    "s_cmp_lt_u32 s70, s69\n\t" \
    "s_cselect_b32 s62, 8192, 0\n\t" \
    "s_cselect_b32 s54, s55, s54\n\t" \
    "s_cselect_b32 s69, s70, s69\n\t" \
    "s_mov_b32 s71, s69\n\t" \
    "s_add_u32 s97, s97, s54\n\t" \
    "v_readlane_b32 s69, v143, 2\n\t" \
    "v_readlane_b32 s70, v143, 3\n\t" \
    "v_readlane_b32 s54, v142, 2\n\t" \
    "v_readlane_b32 s55, v142, 3\n\t" \
    "s_nop 1\n\t" \
    "s_sub_u32 s55, 0, s55\n\t" \
    "s_cmp_lt_u32 s70, s69\n\t" \
    "s_cselect_b32 s63, 8192, 0\n\t" \
    "s_cselect_b32 s54, s55, s54\n\t" \
    "s_cselect_b32 s69, s70, s69\n\t" \
    "s_min_u32 s71, s69, s71\n\t" \
    "s_add_u32 s97, s97, s54\n\t" \
    "v_readlane_b32 s69, v143, 4\n\t" \
    "v_readlane_b32 s70, v143, 5\n\t" \
    "v_readlane_b32 s54, v142, 4\n\t" \
    "v_readlane_b32 s55, v142, 5\n\t" \
    "s_nop 1\n\t" \
    "s_sub_u32 s54, 0, s54\n\t" \
    "s_sub_u32 s55, 0, s55\n\t" \
    "s_cmp_lt_u32 s70, s69\n\t" \
    "s_cselect_b32 s64, 8192, 0\n\t" \
    "s_cselect_b32 s54, s55, s54\n\t" \
    "s_cselect_b32 s69, s70, s69\n\t" \
    "s_min_u32 s71, s69, s71\n\t" \
    "s_add_u32 s97, s97, s54\n\t" \
    "s_add_u32 s63, s63, 0x200000\n\t" \
    "s_xor_b32 s52, s64, 8192\n\t" \
    "s_cmp_gt_u32 s71, 625\n\t" \
    "s_cselect_b32 s69, 1, 0\n\t" \
    "s_mul_i32 s70, s69, 160\n\t" \
    "s_add_u32 s86, s86, s70\n\t" \
    "s_mul_i32 s70, s69, 536\n\t" \
    "s_add_u32 s87, s87, s70\n\t" \
    "s_mul_i32 s70, s69, 256\n\t" \
    "s_add_u32 s88, s88, s70\n\t" \
    "s_cmp_gt_i32 s97, 0\n\t" \
    "s_cselect_b32 s70, 1, 0\n\t" \
    "s_lshr_b32 s69, s67, 3\n\t" \
    "s_xor_b32 s69, s69, s70\n\t" \
    "s_and_b32 s97, s69, 1\n\t" \
    "v_mov_b32 v148, -1\n\t" \
    "v_mov_b32 v149, -1\n\t" \
    "v_mov_b32 v150, -1\n\t" \
    "v_mov_b32 v151, -1\n\t" \
    "s_mov_b32 s71, 3\n\t" \
    "vv_E7_%=:\n\t" \
    "s_memtime s[54:55]\n\t" \
    "s_nop 0\n\t" \
    "s_memrealtime s[4:5]\n\t" \
    "s_nop 0\n\t" \
    "s_memrealtime s[6:7]\n\t" \
    "s_nop 0\n\t" \
    "s_memrealtime s[8:9]\n\t" \
    "s_nop 0\n\t" \
    "s_memrealtime s[10:11]\n\t" \
    "s_nop 0\n\t" \
    "s_memrealtime s[12:13]\n\t" \
    "s_nop 0\n\t" \
    "s_memrealtime s[14:15]\n\t" \
    "s_nop 0\n\t" \
    "s_memrealtime s[16:17]\n\t" \
    "s_nop 0\n\t" \
    "s_memrealtime s[18:19]\n\t" \
    "s_nop 0\n\t" \
    "s_memrealtime s[20:21]\n\t" \
    "s_nop 0\n\t" \
    "s_memrealtime s[22:23]\n\t" \
    "s_nop 0\n\t" \
    "s_memrealtime s[24:25]\n\t" \
    "s_nop 0\n\t" \
    "s_memrealtime s[26:27]\n\t" \
    "s_nop 0\n\t" \
    "s_memrealtime s[28:29]\n\t" \
    "s_nop 0\n\t" \
    "s_memtime s[56:57]\n\t" \
    "s_mov_b32 s70, 1\n\t" \
    "s_sub_u32 s71, s71, 1\n\t" \
    "s_cselect_b32 s69, 0, -1\n\t" \
    "s_waitcnt lgkmcnt(0)\n\t" \
    "s_sub_u32 s54, s56, s54\n\t" \
    "s_and_b32 s54, s54, s69\n\t" \
    "s_cmp_gt_u32 s54, 124\n\t" \
    "s_cbranch_scc1 vv_E7_%=\n\t" \
    "s_cmp_eq_u32 s4, s28\n\t" \
    "s_addc_u32 s70, s70, 0\n\t" \
    "s_cmp_eq_u32 s6, s28\n\t" \
    "s_addc_u32 s70, s70, 0\n\t" \
    "s_cmp_eq_u32 s8, s28\n\t" \
    "s_addc_u32 s70, s70, 0\n\t" \
    "s_cmp_eq_u32 s10, s28\n\t" \
    "s_addc_u32 s70, s70, 0\n\t" \
    "s_cmp_eq_u32 s12, s28\n\t" \
    "s_addc_u32 s70, s70, 0\n\t" \
    "s_cmp_eq_u32 s14, s28\n\t" \
    "s_addc_u32 s70, s70, 0\n\t" \
    "s_cmp_eq_u32 s16, s28\n\t" \
    "s_addc_u32 s70, s70, 0\n\t" \
    "s_cmp_eq_u32 s18, s28\n\t" \
    "s_addc_u32 s70, s70, 0\n\t" \
    "s_cmp_eq_u32 s20, s28\n\t" \
    "s_addc_u32 s70, s70, 0\n\t" \
    "s_cmp_eq_u32 s22, s28\n\t" \
    "s_addc_u32 s70, s70, 0\n\t" \
    "s_cmp_eq_u32 s24, s28\n\t" \
    "s_addc_u32 s70, s70, 0\n\t" \
    "s_cmp_eq_u32 s26, s28\n\t" \
    "s_addc_u32 s70, s70, 0\n\t" \
    "s_mov_b32 s75, s73\n\t" \
    "s_mov_b32 s76, s74\n\t" \
    "s_lshl_b32 s70, s70, 3\n\t" \
    "s_sub_u32 s73, s56, s70\n\t" \
    "s_lshl_b32 s74, s28, 4\n\t" \
    "s_sub_u32 s69, s73, s75\n\t" \
    "s_sub_u32 s70, s74, s76\n\t" \
    "s_sub_u32 s71, s70, 1600\n\t" \
    "s_cmp_lt_u32 s71, 400000\n\t" \
    "s_cbranch_scc0 vv_K9_%=\n\t" \
    "v_cvt_f32_u32 v140, s69\n\t" \
    "v_cvt_f32_u32 v141, s70\n\t" \
    "v_rcp_f32 v142, v140\n\t" \
    "v_rcp_f32 v143, v141\n\t" \
    "v_mul_f32 v142, v142, v141\n\t" \
    "v_mul_f32 v143, v143, v140\n\t" \
    "v_mul_f32 v142, 0x4f800000, v142\n\t" \
    "v_mul_f32 v143, 0x4f000000, v143\n\t" \
    "v_cvt_u32_f32 v142, v142\n\t" \
    "v_cvt_u32_f32 v143, v143\n\t" \
    "v_readfirstlane_b32 s69, v142\n\t" \
    "v_readfirstlane_b32 s70, v143\n\t" \
    "s_nop 1\n\t" \
    "s_mov_b32 s78, s69\n\t" \
    "s_mov_b32 s77, s70\n\t" \
    "vv_K9_%=:\n\t" \
    "s_mov_b32 s79, s74\n\t" \
    "s_sub_u32 s80, s74, 32000\n\t" \
    "s_cmp_eq_u32 s66, 1\n\t" \
    "s_cbranch_scc1 vv_loop11_%=\n\t" \
    "s_cmp_eq_u32 s66, 2\n\t" \
    "s_cbranch_scc1 vv_loop12_%=\n\t" \
    "s_cmp_eq_u32 s66, 3\n\t" \
    "s_cbranch_scc1 vv_loop13_%=\n\t" \
    "vv_loop10_%=:\n\t" \
    "v_mov_b32 v72, 0\n\t" \
    "v_mov_b32 v73, 0\n\t" \
    "v_mov_b32 v74, 0\n\t" \
    "v_mov_b32 v75, 0\n\t" \
    "v_mov_b32 v76, 0\n\t" \
    "v_mov_b32 v77, 0\n\t" \
    "v_mov_b32 v78, 0\n\t" \
    "v_mov_b32 v79, 0\n\t" \
    "v_mov_b32 v92, 0\n\t" \
    "v_mov_b32 v93, 0\n\t" \
    "v_mov_b32 v94, 0\n\t" \
    "v_mov_b32 v95, 0\n\t" \
    "v_mov_b32 v96, 0\n\t" \
    "v_mov_b32 v97, 0\n\t" \
    "v_mov_b32 v98, 0\n\t" \
    "v_mov_b32 v99, 0\n\t" \
    "v_mov_b32 v100, 0\n\t" \
    "v_mov_b32 v101, 0\n\t" \
    "v_mov_b32 v102, 0\n\t" \
    "v_mov_b32 v103, 0\n\t" \
    "v_mov_b32 v104, 0\n\t" \
    "v_mov_b32 v105, 0\n\t" \
    "v_mov_b32 v106, 0\n\t" \
    "v_mov_b32 v107, 0\n\t" \
    "v_mov_b32 v108, 0\n\t" \
    "v_mov_b32 v109, 0\n\t" \
    "v_mov_b32 v110, 0\n\t" \
    "v_mov_b32 v111, 0\n\t" \
    "v_mov_b32 v112, 0\n\t" \
    "v_mov_b32 v113, 0\n\t" \
    "v_mov_b32 v114, 0\n\t" \
    "v_mov_b32 v115, 0\n\t" \
    "v_mov_b32 v116, 0\n\t" \
    "v_mov_b32 v117, 0\n\t" \
    "v_mov_b32 v118, 0\n\t" \
    "v_mov_b32 v119, 0\n\t" \
    "v_mov_b32 v120, 0\n\t" \
    "v_mov_b32 v121, 0\n\t" \
    "v_mov_b32 v122, 0\n\t" \
    "v_mov_b32 v123, 0\n\t" \
    "v_mov_b32 v124, 0\n\t" \
    "v_mov_b32 v125, 0\n\t" \
    "v_mov_b32 v126, 0\n\t" \
    "v_mov_b32 v127, 0\n\t" \
    "vv_go15_%=:\n\t" \
    "s_add_u32 s69, s65, 0\n\t" \
    "s_bitcmp1_b32 s69, 0\n\t" \
    "s_cselect_b32 s70, s63, s62\n\t" \
    "s_bfe_u32 s69, s69, 0x20001\n\t" \
    "s_lshl_b32 s69, s69, 19\n\t" \
    "s_add_u32 s69, s69, s70\n\t" \
    "s_add_u32 s46, s36, s69\n\t" \
    "s_addc_u32 s47, s37, 0\n\t" \
    "s_add_u32 s69, s65, 0\n\t" \
    "s_lshr_b32 s69, s69, 1\n\t" \
    "s_xor_b32 s69, s69, s97\n\t" \
    "s_and_b32 s69, s69, 1\n\t" \
    "s_lshl_b32 s69, s69, 14\n\t" \
    "s_or_b32 s46, s46, s69\n\t" \
    "s_add_u32 s69, s65, 0\n\t" \
    "s_mul_i32 s70, s69, 171\n\t" \
    "s_lshr_b32 s70, s70, 9\n\t" \
    "s_mul_i32 s70, s70, 3\n\t" \
    "s_sub_u32 s69, s69, s70\n\t" \
    "s_mul_i32 s69, s69, 36\n\t" \
    "s_lshl_b32 s69, s69, 14\n\t" \
    "s_add_u32 s69, s69, s64\n\t" \
    "s_add_u32 s69, s69, 4194304\n\t" \
    "s_add_u32 s38, s36, s69\n\t" \
    "s_addc_u32 s39, s37, 0\n\t" \
    "s_mul_i32 s69, s65, 9216\n\t" \
    "s_add_u32 s69, s69, 123731968\n\t" \
    "s_add_u32 s40, s36, s69\n\t" \
    "s_addc_u32 s41, s37, 0\n\t" \
    "s_sub_u32 s70, s79, s74\n\t" \
    "s_max_i32 s70, s70, 0\n\t" \
    "s_min_i32 s70, s70, 16000\n\t" \
    "s_lshl_b32 s70, s70, 1\n\t" \
    "s_mul_hi_u32 s70, s70, s77\n\t" \
    "s_add_u32 s70, s70, s73\n\t" \
    "s_sub_u32 s70, s70, 100\n\t" \
    "s_getpc_b64 s[30:31]\n\t" \
    "vv_Wp17_%=:\n\t" \
    "s_add_u32 s30, s30, vv_We18_%=-vv_Wp17_%=\n\t" \
    "s_addc_u32 s31, s31, 0\n\t" \
    "vv_W16_%=:\n\t" \
    "s_memtime s[56:57]\n\t" \
    "s_waitcnt lgkmcnt(0)\n\t" \
    "s_sub_u32 s69, s70, s56\n\t" \
    "s_cmp_gt_i32 s69, 111\n\t" \
    "s_cbranch_scc1 vv_W16_%=\n\t" \
    "s_max_i32 s69, s69, 0\n\t" \
    "s_lshr_b32 s69, s69, 1\n\t" \
    "s_and_b32 s69, s69, 0x3c\n\t" \
    "s_sub_u32 s30, s30, s69\n\t" \
    "s_subb_u32 s31, s31, 0\n\t" \
    "s_setpc_b64 s[30:31]\n\t" \
    ".p2align 6\n\t" \
    "vv_al19_%=:\n\t" \
    "s_nop 0\n\t" \
    "s_nop 1\n\t" \
    "s_nop 1\n\t" \
    "s_nop 1\n\t" \
    "s_nop 1\n\t" \
    "s_nop 1\n\t" \
    "s_nop 1\n\t" \
    "s_nop 1\n\t" \
    "s_nop 1\n\t" \
    "s_nop 1\n\t" \
    "s_nop 1\n\t" \
    "s_nop 1\n\t" \
    "s_nop 1\n\t" \
    "s_nop 1\n\t" \
    "vv_We18_%=:\n\t" \
    "vv_G20_%=:\n\t" \
    "global_load_dwordx4 v[36:39], v1, s[46:47] sc1\n\t" \
    "s_nop 9\n\t" \
    "global_load_dwordx4 v[40:43], v1, s[46:47] offset:1024 sc1\n\t" \
    "s_nop 9\n\t" \
    "global_load_dwordx4 v[44:47], v2, s[46:47] sc1\n\t" \
    "s_waitcnt vmcnt(2)\n\t" \
    "v_smfmac_f32_16x16x64_bf16 v[72:75], v[36:39], a[0:7], v10\n\t" \
    "ds_write_b128 v0, v[36:39] offset:0\n\t" \
    "v_smfmac_f32_16x16x64_bf16 v[76:79], v[36:39], a[72:79], v10\n\t" \
    "v_max3_u32 v140, v36, v37, v38\n\t" \
    "s_waitcnt vmcnt(1)\n\t" \
    "ds_write_b128 v0, v[40:43] offset:1024\n\t" \
    "v_smfmac_f32_16x16x64_bf16 v[72:75], v[40:43], a[8:15], v10\n\t" \
    "v_max3_u32 v140, v140, v39, v40\n\t" \
    "v_max3_u32 v140, v140, v41, v42\n\t" \
    "v_max_u32 v140, v140, v43\n\t" \
    "v_cmp_eq_u32 vcc, -1, v140\n\t" \
    "v_smfmac_f32_16x16x64_bf16 v[76:79], v[40:43], a[80:87], v10\n\t" \
    "s_cbranch_vccnz vv_Gbad21_%=\n\t" \
    "vv_Gc23_%=:\n\t" \
    "s_waitcnt lgkmcnt(0)\n\t" \
    "s_barrier\n\t" \
    "ds_read_b128 v[48:51], v0 offset:2048\n\t" \
    "ds_read_b128 v[52:55], v0 offset:3072\n\t" \
    "s_waitcnt vmcnt(0)\n\t" \
    "v_smfmac_f32_16x16x64_bf16 v[72:75], v[44:47], a[64:71], v10\n\t" \
    "ds_read_b128 v[56:59], v0 offset:4096\n\t" \
    "ds_read_b128 v[60:63], v0 offset:5120\n\t" \
    "v_max3_u32 v140, v44, v45, v46\n\t" \
    "v_max_u32 v140, v140, v47\n\t" \
    "v_smfmac_f32_16x16x64_bf16 v[76:79], v[44:47], a[136:143], v10\n\t" \
    "ds_read_b128 v[64:67], v0 offset:6144\n\t" \
    "ds_read_b128 v[68:71], v0 offset:7168\n\t" \
    "v_cmp_eq_u32 vcc, -1, v140\n\t" \
    "s_cbranch_vccnz vv_Gbad222_%=\n\t" \
    "vv_Gc224_%=:\n\t" \
    "s_waitcnt lgkmcnt(5)\n\t" \
    "v_smfmac_f32_16x16x64_bf16 v[72:75], v[48:51], a[16:23], v10\n\t" \
    "s_waitcnt lgkmcnt(4)\n\t" \
    "v_smfmac_f32_16x16x64_bf16 v[72:75], v[52:55], a[24:31], v10\n\t" \
    "s_waitcnt lgkmcnt(3)\n\t" \
    "v_smfmac_f32_16x16x64_bf16 v[72:75], v[56:59], a[32:39], v10\n\t" \
    "s_waitcnt lgkmcnt(2)\n\t" \
    "v_smfmac_f32_16x16x64_bf16 v[72:75], v[60:63], a[40:47], v10\n\t" \
    "s_waitcnt lgkmcnt(1)\n\t" \
    "v_smfmac_f32_16x16x64_bf16 v[72:75], v[64:67], a[48:55], v10\n\t" \
    "s_waitcnt lgkmcnt(0)\n\t" \
    "v_smfmac_f32_16x16x64_bf16 v[72:75], v[68:71], a[56:63], v10\n\t" \
    "v_smfmac_f32_16x16x64_bf16 v[76:79], v[48:51], a[88:95], v10\n\t" \
    "s_nop 1\n\t" \
    "s_nop 0\n\t" \
    "v_smfmac_f32_16x16x64_bf16 v[76:79], v[52:55], a[96:103], v10\n\t" \
    "s_nop 1\n\t" \
    "s_nop 0\n\t" \
    "v_smfmac_f32_16x16x64_bf16 v[76:79], v[56:59], a[104:111], v10\n\t" \
    "v_add_f32 v72, v72, v74\n\t" \
    "v_smfmac_f32_16x16x64_bf16 v[76:79], v[60:63], a[112:119], v10\n\t" \
    "v_add_f32 v73, v73, v75\n\t" \
    "v_smfmac_f32_16x16x64_bf16 v[76:79], v[64:67], a[120:127], v10\n\t" \
    "v_cvt_pk_bf16_f32 v80, v72, v73\n\t" \
    "v_smfmac_f32_16x16x64_bf16 v[76:79], v[68:71], a[128:135], v10\n\t" \
    "ds_write_b32 v12, v80 offset:0\n\t" \
    "s_nop 6\n\t" \
    "v_pk_add_f32 v[76:77], v[76:77], v[78:79]\n\t" \
    "v_cvt_pk_bf16_f32 v81, v76, v77\n\t" \
    "ds_write_b32 v12, v81 offset:64\n\t" \
    "s_waitcnt lgkmcnt(0)\n\t" \
    "s_barrier\n\t" \
    "ds_read_b64_tr_b16 v[84:85], v13 offset:0\n\t" \
    "ds_read_b64_tr_b16 v[86:87], v13 offset:128\n\t" \
    "ds_read_b64_tr_b16 v[88:89], v13 offset:1024\n\t" \
    "ds_read_b64_tr_b16 v[90:91], v13 offset:1152\n\t" \
    "s_waitcnt lgkmcnt(2)\n\t" \
    "v_smfmac_f32_16x16x64_bf16 v[92:95], v[84:87], a[144:151], v10\n\t" \
    "s_nop 2\n\t" \
    "s_waitcnt lgkmcnt(0)\n\t" \
    "v_smfmac_f32_16x16x64_bf16 v[92:95], v[88:91], a[152:159], v10\n\t" \
    "s_nop 2\n\t" \
    "v_smfmac_f32_16x16x64_bf16 v[96:99], v[84:87], a[160:167], v10\n\t" \
    "s_nop 2\n\t" \
    "v_smfmac_f32_16x16x64_bf16 v[96:99], v[88:91], a[168:175], v10\n\t" \
    "s_nop 2\n\t" \
    "v_smfmac_f32_16x16x64_bf16 v[100:103], v[84:87], a[176:183], v10\n\t" \
    "s_nop 0\n\t" \
    "v_add_f32 v92, v92, v94\n\t" \
    "v_add_f32 v93, v93, v95\n\t" \
    "v_smfmac_f32_16x16x64_bf16 v[100:103], v[88:91], a[184:191], v10\n\t" \
    "s_nop 1\n\t" \
    "v_cvt_pk_bf16_f32 v128, v92, v93\n\t" \
    "v_smfmac_f32_16x16x64_bf16 v[104:107], v[84:87], a[192:199], v10\n\t" \
    "s_nop 0\n\t" \
    "v_add_f32 v96, v96, v98\n\t" \
    "v_add_f32 v97, v97, v99\n\t" \
    "v_smfmac_f32_16x16x64_bf16 v[104:107], v[88:91], a[200:207], v10\n\t" \
    "s_nop 1\n\t" \
    "v_cvt_pk_bf16_f32 v129, v96, v97\n\t" \
    "v_smfmac_f32_16x16x64_bf16 v[108:111], v[84:87], a[208:215], v10\n\t" \
    "s_nop 0\n\t" \
    "v_add_f32 v100, v100, v102\n\t" \
    "v_add_f32 v101, v101, v103\n\t" \
    "v_smfmac_f32_16x16x64_bf16 v[108:111], v[88:91], a[216:223], v10\n\t" \
    "s_nop 1\n\t" \
    "global_load_dword v140, v20, s[46:47] sc1\n\t" \
    "v_cvt_pk_bf16_f32 v130, v100, v101\n\t" \
    "v_smfmac_f32_16x16x64_bf16 v[112:115], v[84:87], a[224:231], v10\n\t" \
    "s_nop 0\n\t" \
    "v_add_f32 v104, v104, v106\n\t" \
    "v_add_f32 v105, v105, v107\n\t" \
    "v_smfmac_f32_16x16x64_bf16 v[112:115], v[88:91], a[232:239], v10\n\t" \
    "s_nop 0\n\t" \
    "v_cvt_pk_bf16_f32 v131, v104, v105\n\t" \
    "v_permlane32_swap_b32 v128, v130\n\t" \
    "v_smfmac_f32_16x16x64_bf16 v[116:119], v[84:87], a[240:247], v10\n\t" \
    "s_nop 0\n\t" \
    "v_permlane32_swap_b32 v129, v131\n\t" \
    "s_nop 1\n\t" \
    "v_permlane16_swap_b32 v128, v129\n\t" \
    "v_smfmac_f32_16x16x64_bf16 v[116:119], v[88:91], a[248:255], v10\n\t" \
    "v_permlane16_swap_b32 v130, v131\n\t" \
    "global_store_dwordx4 v14, v[128:131], s[38:39] sc1\n\t" \
    "v_add_f32 v108, v108, v110\n\t" \
    "v_smfmac_f32_16x16x64_bf16 v[120:123], v[84:87], v[224:231], v10\n\t" \
    "s_nop 0\n\t" \
    "v_add_f32 v109, v109, v111\n\t" \
    "v_cvt_pk_bf16_f32 v132, v108, v109\n\t" \
    "v_smfmac_f32_16x16x64_bf16 v[120:123], v[88:91], v[232:239], v10\n\t" \
    "s_nop 0\n\t" \
    "v_add_f32 v112, v112, v114\n\t" \
    "v_add_f32 v113, v113, v115\n\t" \
    "v_smfmac_f32_16x16x64_bf16 v[124:127], v[84:87], v[240:247], v10\n\t" \
    "s_nop 0\n\t" \
    "v_cvt_pk_bf16_f32 v133, v112, v113\n\t" \
    "v_add_f32 v116, v116, v118\n\t" \
    "v_smfmac_f32_16x16x64_bf16 v[124:127], v[88:91], v[248:255], v10\n\t" \
    "v_add_f32 v117, v117, v119\n\t" \
    "v_cvt_pk_bf16_f32 v134, v116, v117\n\t" \
    "v_add_f32 v120, v120, v122\n\t" \
    "v_add_f32 v121, v121, v123\n\t" \
    "v_cvt_pk_bf16_f32 v135, v120, v121\n\t" \
    "v_permlane32_swap_b32 v132, v134\n\t" \
    "s_nop 0\n\t" \
    "v_permlane32_swap_b32 v133, v135\n\t" \
    "s_nop 1\n\t" \
    "v_permlane16_swap_b32 v132, v133\n\t" \
    "v_permlane16_swap_b32 v134, v135\n\t" \
    "global_store_dwordx4 v15, v[132:135], s[38:39] sc1\n\t" \
    "v_pk_add_f32 v[124:125], v[124:125], v[126:127]\n\t" \
    "v_cvt_pk_bf16_f32 v136, v124, v125\n\t" \
    "global_store_dword v16, v136, s[38:39] sc1\n\t" \
    "s_nop 1\n\t" \
    "global_store_dwordx4 v17, v[128:131], s[40:41] sc1\n\t" \
    "global_store_dwordx4 v17, v[132:135], s[40:41] offset:1024 sc1\n\t" \
    "global_store_dword v18, v136, s[40:41] sc1\n\t" \
    "s_cmp_eq_u32 s65, 63\n\t" \
    "s_cbranch_scc1 vv_end14_%=\n\t" \
    "s_mov_b32 s71, 3\n\t" \
    "vv_E26_%=:\n\t" \
    "s_memtime s[54:55]\n\t" \
    "s_nop 0\n\t" \
    "s_memrealtime s[4:5]\n\t" \
    "s_nop 0\n\t" \
    "s_memrealtime s[6:7]\n\t" \
    "s_nop 0\n\t" \
    "s_memrealtime s[8:9]\n\t" \
    "s_nop 0\n\t" \
    "s_memrealtime s[10:11]\n\t" \
    "s_nop 0\n\t" \
    "s_memrealtime s[12:13]\n\t" \
    "s_nop 0\n\t" \
    "s_memrealtime s[14:15]\n\t" \
    "s_nop 0\n\t" \
    "s_memrealtime s[16:17]\n\t" \
    "s_nop 0\n\t" \
    "s_memrealtime s[18:19]\n\t" \
    "s_nop 0\n\t" \
    "s_memrealtime s[20:21]\n\t" \
    "s_nop 0\n\t" \
    "s_memrealtime s[22:23]\n\t" \
    "s_nop 0\n\t" \
    "s_memrealtime s[24:25]\n\t" \
    "s_nop 0\n\t" \
    "s_memrealtime s[26:27]\n\t" \
    "s_nop 0\n\t" \
    "s_memrealtime s[28:29]\n\t" \
    "s_nop 0\n\t" \
    "s_memtime s[56:57]\n\t" \
    "s_mov_b32 s70, 1\n\t" \
    "s_sub_u32 s71, s71, 1\n\t" \
    "s_cselect_b32 s69, 0, -1\n\t" \
    "s_waitcnt lgkmcnt(0)\n\t" \
    "s_sub_u32 s54, s56, s54\n\t" \
    "s_and_b32 s54, s54, s69\n\t" \
    "s_cmp_gt_u32 s54, 124\n\t" \
    "s_cbranch_scc1 vv_E26_%=\n\t" \
    "s_cmp_eq_u32 s4, s28\n\t" \
    "s_addc_u32 s70, s70, 0\n\t" \
    "s_cmp_eq_u32 s6, s28\n\t" \
    "s_addc_u32 s70, s70, 0\n\t" \
    "s_cmp_eq_u32 s8, s28\n\t" \
    "s_addc_u32 s70, s70, 0\n\t" \
    "s_cmp_eq_u32 s10, s28\n\t" \
    "s_addc_u32 s70, s70, 0\n\t" \
    "s_cmp_eq_u32 s12, s28\n\t" \
    "s_addc_u32 s70, s70, 0\n\t" \
    "s_cmp_eq_u32 s14, s28\n\t" \
    "s_addc_u32 s70, s70, 0\n\t" \
    "s_cmp_eq_u32 s16, s28\n\t" \
    "s_addc_u32 s70, s70, 0\n\t" \
    "s_cmp_eq_u32 s18, s28\n\t" \
    "s_addc_u32 s70, s70, 0\n\t" \
    "s_cmp_eq_u32 s20, s28\n\t" \
    "s_addc_u32 s70, s70, 0\n\t" \
    "s_cmp_eq_u32 s22, s28\n\t" \
    "s_addc_u32 s70, s70, 0\n\t" \
    "s_cmp_eq_u32 s24, s28\n\t" \
    "s_addc_u32 s70, s70, 0\n\t" \
    "s_cmp_eq_u32 s26, s28\n\t" \
    "s_addc_u32 s70, s70, 0\n\t" \
    "s_mov_b32 s75, s73\n\t" \
    "s_mov_b32 s76, s74\n\t" \
    "s_lshl_b32 s70, s70, 3\n\t" \
    "s_sub_u32 s73, s56, s70\n\t" \
    "s_lshl_b32 s74, s28, 4\n\t" \
    "s_sub_u32 s69, s73, s75\n\t" \
    "s_sub_u32 s70, s74, s76\n\t" \
    "s_sub_u32 s71, s70, 1600\n\t" \
    "s_cmp_lt_u32 s71, 400000\n\t" \
    "s_cbranch_scc0 vv_K28_%=\n\t" \
    "v_cvt_f32_u32 v141, s69\n\t" \
    "v_cvt_f32_u32 v142, s70\n\t" \
    "v_rcp_f32 v143, v141\n\t" \
    "v_rcp_f32 v144, v142\n\t" \
    "v_mul_f32 v143, v143, v142\n\t" \
    "v_mul_f32 v144, v144, v141\n\t" \
    "v_mul_f32 v143, 0x4f800000, v143\n\t" \
    "v_mul_f32 v144, 0x4f000000, v144\n\t" \
    "v_cvt_u32_f32 v143, v143\n\t" \
    "v_cvt_u32_f32 v144, v144\n\t" \
    "v_readfirstlane_b32 s69, v143\n\t" \
    "v_readfirstlane_b32 s70, v144\n\t" \
    "s_nop 1\n\t" \
    "s_sub_u32 s69, s69, s78\n\t" \
    "s_ashr_i32 s69, s69, 2\n\t" \
    "s_add_u32 s78, s78, s69\n\t" \
    "s_sub_u32 s70, s70, s77\n\t" \
    "s_ashr_i32 s70, s70, 2\n\t" \
    "s_add_u32 s77, s77, s70\n\t" \
    "vv_K28_%=:\n\t" \
    "s_waitcnt vmcnt(6)\n\t" \
    "v_cmp_eq_u32 vcc, -1, v140\n\t" \
    "s_cbranch_vccz vv_Hok30_%=\n\t" \
    "vv_H29_%=:\n\t" \
    "global_load_dword v140, v20, s[46:47] sc1\n\t" \
    "s_waitcnt vmcnt(0)\n\t" \
    "v_cmp_eq_u32 vcc, -1, v140\n\t" \
    "s_cbranch_vccz vv_Hc31_%=\n\t" \
    "s_cmp_eq_u32 s68, 0\n\t" \
    "s_cbranch_scc1 vv_F32_%=\n\t" \
    "s_sub_u32 s68, s68, 1\n\t" \
    "s_sleep 5\n\t" \
    "s_branch vv_H29_%=\n\t" \
    "vv_F32_%=:\n\t" \
    "v_mov_b32 v141, 6095104\n\t" \
    "v_mov_b32 v142, 6\n\t" \
    "global_store_dword v141, v142, s[36:37] sc1\n\t" \
    "s_waitcnt vmcnt(0)\n\t" \
    "s_branch vv_Hc31_%=\n\t" \
    "vv_Hc31_%=:\n\t" \
    "vv_Hok30_%=:\n\t" \
    "v_subrev_u32 v140, s80, v140\n\t" \
    "v_and_b32 v20, -5, v20\n\t" \
    "s_nop 0\n\t" \
    "v_max_i32_dpp v140, v140, v140 row_ror:1 row_mask:0xf bank_mask:0xf\n\t" \
    "s_nop 1\n\t" \
    "v_max_i32_dpp v140, v140, v140 row_ror:2 row_mask:0xf bank_mask:0xf\n\t" \
    "s_nop 1\n\t" \
    "v_max_i32_dpp v140, v140, v140 row_ror:4 row_mask:0xf bank_mask:0xf\n\t" \
    "s_nop 1\n\t" \
    "v_max_i32_dpp v140, v140, v140 row_ror:8 row_mask:0xf bank_mask:0xf\n\t" \
    "s_nop 0\n\t" \
    "v_readlane_b32 s69, v140, 0\n\t" \
    "v_readlane_b32 s71, v140, 16\n\t" \
    "v_readlane_b32 s70, v140, 32\n\t" \
    "v_readlane_b32 s81, v140, 48\n\t" \
    "s_max_i32 s69, s69, s71\n\t" \
    "s_nop 0\n\t" \
    "s_max_i32 s71, s70, s81\n\t" \
    "s_max_i32 s69, s69, s71\n\t" \
    "s_add_u32 s81, s69, s80\n\t" \
    "s_cmp_lg_u32 s66, 0\n\t" \
    "s_cbranch_scc1 vv_SF33_%=\n\t" \
    "s_lshl_b32 s69, s67, 7\n\t" \
    "s_add_u32 s69, s69, 6131712\n\t" \
    "s_add_u32 s69, s69, s52\n\t" \
    "s_add_u32 s70, s65, -1\n\t" \
    "v_mov_b32 v140, s69\n\t" \
    "v_mov_b32 v141, s70\n\t" \
    "s_mov_b64 exec, 1\n\t" \
    "global_store_dword v140, v141, s[36:37] sc1\n\t" \
    "s_mov_b64 exec, -1\n\t" \
    "vv_SF33_%=:\n\t" \
    "v_mov_b32 v72, 0\n\t" \
    "v_mov_b32 v73, 0\n\t" \
    "v_mov_b32 v74, 0\n\t" \
    "v_mov_b32 v75, 0\n\t" \
    "v_mov_b32 v76, 0\n\t" \
    "v_mov_b32 v77, 0\n\t" \
    "v_mov_b32 v78, 0\n\t" \
    "v_mov_b32 v79, 0\n\t" \
    "v_mov_b32 v92, 0\n\t" \
    "v_mov_b32 v93, 0\n\t" \
    "v_mov_b32 v94, 0\n\t" \
    "v_mov_b32 v95, 0\n\t" \
    "v_mov_b32 v96, 0\n\t" \
    "v_mov_b32 v97, 0\n\t" \
    "v_mov_b32 v98, 0\n\t" \
    "v_mov_b32 v99, 0\n\t" \
    "v_mov_b32 v100, 0\n\t" \
    "v_mov_b32 v101, 0\n\t" \
    "v_mov_b32 v102, 0\n\t" \
    "v_mov_b32 v103, 0\n\t" \
    "v_mov_b32 v104, 0\n\t" \
    "v_mov_b32 v105, 0\n\t" \
    "v_mov_b32 v106, 0\n\t" \
    "v_mov_b32 v107, 0\n\t" \
    "v_mov_b32 v108, 0\n\t" \
    "v_mov_b32 v109, 0\n\t" \
    "v_mov_b32 v110, 0\n\t" \
    "v_mov_b32 v111, 0\n\t" \
    "v_mov_b32 v112, 0\n\t" \
    "v_mov_b32 v113, 0\n\t" \
    "v_mov_b32 v114, 0\n\t" \
    "v_mov_b32 v115, 0\n\t" \
    "v_mov_b32 v116, 0\n\t" \
    "v_mov_b32 v117, 0\n\t" \
    "v_mov_b32 v118, 0\n\t" \
    "v_mov_b32 v119, 0\n\t" \
    "v_mov_b32 v120, 0\n\t" \
    "v_mov_b32 v121, 0\n\t" \
    "v_mov_b32 v122, 0\n\t" \
    "v_mov_b32 v123, 0\n\t" \
    "v_mov_b32 v124, 0\n\t" \
    "v_mov_b32 v125, 0\n\t" \
    "v_mov_b32 v126, 0\n\t" \
    "v_mov_b32 v127, 0\n\t" \
    "s_cmp_lg_u32 s65, 0\n\t" \
    "s_cbranch_scc1 vv_Oc36_%=\n\t" \
    "s_add_u32 s79, s81, 240\n\t" \
    "s_sub_u32 s70, s79, s74\n\t" \
    "s_max_i32 s70, s70, 0\n\t" \
    "s_min_i32 s70, s70, 16000\n\t" \
    "s_lshl_b32 s70, s70, 1\n\t" \
    "s_mul_hi_u32 s70, s70, s77\n\t" \
    "s_add_u32 s70, s70, s73\n\t" \
    "vv_W37_%=:\n\t" \
    "s_memtime s[56:57]\n\t" \
    "s_waitcnt lgkmcnt(0)\n\t" \
    "s_sub_u32 s69, s70, s56\n\t" \
    "s_cmp_gt_i32 s69, 24\n\t" \
    "s_cbranch_scc1 vv_W37_%=\n\t" \
    "global_load_dword v140, v35, s[46:47] sc1\n\t" \
    "s_waitcnt vmcnt(0)\n\t" \
    "v_cmp_eq_u32 vcc, -1, v140\n\t" \
    "s_cbranch_vccz vv_Ook35_%=\n\t" \
    "vv_O34_%=:\n\t" \
    "global_load_dword v140, v35, s[46:47] offset:1536 sc1\n\t" \
    "s_waitcnt vmcnt(0)\n\t" \
    "v_cmp_eq_u32 vcc, -1, v140\n\t" \
    "s_cbranch_vccz vv_Ook35_%=\n\t" \
    "s_cmp_eq_u32 s68, 0\n\t" \
    "s_cbranch_scc1 vv_F38_%=\n\t" \
    "s_sub_u32 s68, s68, 1\n\t" \
    "s_sleep 3\n\t" \
    "s_branch vv_O34_%=\n\t" \
    "vv_F38_%=:\n\t" \
    "v_mov_b32 v141, 6095104\n\t" \
    "v_mov_b32 v142, 17\n\t" \
    "global_store_dword v141, v142, s[36:37] sc1\n\t" \
    "s_waitcnt vmcnt(0)\n\t" \
    "s_branch vv_Ook35_%=\n\t" \
    "vv_Ook35_%=:\n\t" \
    "v_subrev_u32 v140, s80, v140\n\t" \
    "s_nop 1\n\t" \
    "v_max_i32_dpp v140, v140, v140 row_ror:1 row_mask:0xf bank_mask:0xf\n\t" \
    "s_nop 1\n\t" \
    "v_max_i32_dpp v140, v140, v140 row_ror:2 row_mask:0xf bank_mask:0xf\n\t" \
    "s_nop 1\n\t" \
    "v_max_i32_dpp v140, v140, v140 row_ror:4 row_mask:0xf bank_mask:0xf\n\t" \
    "s_nop 1\n\t" \
    "v_max_i32_dpp v140, v140, v140 row_ror:8 row_mask:0xf bank_mask:0xf\n\t" \
    "s_nop 0\n\t" \
    "v_readlane_b32 s69, v140, 0\n\t" \
    "v_readlane_b32 s71, v140, 16\n\t" \
    "v_readlane_b32 s70, v140, 32\n\t" \
    "v_readlane_b32 s81, v140, 48\n\t" \
    "s_max_i32 s69, s69, s71\n\t" \
    "s_nop 0\n\t" \
    "s_max_i32 s71, s70, s81\n\t" \
    "s_max_i32 s69, s69, s71\n\t" \
    "s_add_u32 s81, s69, s80\n\t" \
    "vv_Oc36_%=:\n\t" \
    "s_mul_hi_u32 s69, s85, s78\n\t" \
    "s_add_u32 s79, s81, s69\n\t" \
    "s_add_u32 s79, s79, s87\n\t" \
    "s_cmp_ge_u32 s65, 36\n\t" \
    "s_cselect_b32 s69, 28, 0\n\t" \
    "s_sub_u32 s79, s79, s69\n\t" \
    "s_cmp_lt_u32 s65, 8\n\t" \
    "s_cselect_b32 s69, 64, 0\n\t" \
    "s_add_u32 s79, s79, s69\n\t" \
    "s_cmp_ge_u32 s65, 57\n\t" \
    "s_cselect_b32 s69, 32, 0\n\t" \
    "s_add_u32 s79, s79, s69\n\t" \
    "s_add_u32 s80, s79, s88\n\t" \
    "s_add_u32 s65, s65, 1\n\t" \
    "s_branch vv_go15_%=\n\t" \
    "vv_Gbad21_%=:\n\t" \
    "s_waitcnt vmcnt(0) lgkmcnt(0)\n\t" \
    "s_nop 7\n\t" \
    "v_mov_b32 v72, 0\n\t" \
    "v_mov_b32 v73, 0\n\t" \
    "v_mov_b32 v74, 0\n\t" \
    "v_mov_b32 v75, 0\n\t" \
    "v_mov_b32 v76, 0\n\t" \
    "v_mov_b32 v77, 0\n\t" \
    "v_mov_b32 v78, 0\n\t" \
    "v_mov_b32 v79, 0\n\t" \
    "s_cmp_eq_u32 s68, 0\n\t" \
    "s_cbranch_scc1 vv_F39_%=\n\t" \
    "s_sub_u32 s68, s68, 1\n\t" \
    "s_branch vv_G20_%=\n\t" \
    "vv_F39_%=:\n\t" \
    "v_mov_b32 v140, 6095104\n\t" \
    "v_mov_b32 v141, 1\n\t" \
    "global_store_dword v140, v141, s[36:37] sc1\n\t" \
    "s_waitcnt vmcnt(0)\n\t" \
    "s_branch vv_Gc23_%=\n\t" \
    "vv_Gbad222_%=:\n\t" \
    "global_load_dwordx4 v[44:47], v2, s[46:47] sc1\n\t" \
    "s_waitcnt vmcnt(0)\n\t" \
    "v_max3_u32 v140, v44, v45, v46\n\t" \
    "v_max_u32 v140, v140, v47\n\t" \
    "v_cmp_eq_u32 vcc, -1, v140\n\t" \
    "s_cbranch_vccz vv_Gre225_%=\n\t" \
    "s_cmp_eq_u32 s68, 0\n\t" \
    "s_cbranch_scc1 vv_F40_%=\n\t" \
    "s_sub_u32 s68, s68, 1\n\t" \
    "s_sleep 1\n\t" \
    "s_branch vv_Gbad222_%=\n\t" \
    "vv_F40_%=:\n\t" \
    "v_mov_b32 v140, 6095104\n\t" \
    "v_mov_b32 v141, 2\n\t" \
    "global_store_dword v140, v141, s[36:37] sc1\n\t" \
    "s_waitcnt vmcnt(0)\n\t" \
    "s_branch vv_Gre225_%=\n\t" \
    "vv_Gre225_%=:\n\t" \
    "v_mov_b32 v72, 0\n\t" \
    "v_mov_b32 v73, 0\n\t" \
    "v_mov_b32 v74, 0\n\t" \
    "v_mov_b32 v75, 0\n\t" \
    "v_mov_b32 v76, 0\n\t" \
    "v_mov_b32 v77, 0\n\t" \
    "v_mov_b32 v78, 0\n\t" \
    "v_mov_b32 v79, 0\n\t" \
    "v_smfmac_f32_16x16x64_bf16 v[72:75], v[36:39], a[0:7], v10\n\t" \
    "s_nop 0\n\t" \
    "v_smfmac_f32_16x16x64_bf16 v[76:79], v[36:39], a[72:79], v10\n\t" \
    "v_smfmac_f32_16x16x64_bf16 v[72:75], v[40:43], a[8:15], v10\n\t" \
    "v_smfmac_f32_16x16x64_bf16 v[76:79], v[40:43], a[80:87], v10\n\t" \
    "v_smfmac_f32_16x16x64_bf16 v[72:75], v[44:47], a[64:71], v10\n\t" \
    "v_smfmac_f32_16x16x64_bf16 v[76:79], v[44:47], a[136:143], v10\n\t" \
    "s_branch vv_Gc224_%=\n\t" \
    "vv_end14_%=:\n\t" \
    "s_endpgm\n\t" \
    "vv_loop11_%=:\n\t" \
    "v_mov_b32 v72, 0\n\t" \
    "v_mov_b32 v73, 0\n\t" \
    "v_mov_b32 v74, 0\n\t" \
    "v_mov_b32 v75, 0\n\t" \
    "v_mov_b32 v76, 0\n\t" \
    "v_mov_b32 v77, 0\n\t" \
    "v_mov_b32 v78, 0\n\t" \
    "v_mov_b32 v79, 0\n\t" \
    "v_mov_b32 v92, 0\n\t" \
    "v_mov_b32 v93, 0\n\t" \
    "v_mov_b32 v94, 0\n\t" \
    "v_mov_b32 v95, 0\n\t" \
    "v_mov_b32 v96, 0\n\t" \
    "v_mov_b32 v97, 0\n\t" \
    "v_mov_b32 v98, 0\n\t" \
    "v_mov_b32 v99, 0\n\t" \
    "v_mov_b32 v100, 0\n\t" \
    "v_mov_b32 v101, 0\n\t" \
    "v_mov_b32 v102, 0\n\t" \
    "v_mov_b32 v103, 0\n\t" \
    "v_mov_b32 v104, 0\n\t" \
    "v_mov_b32 v105, 0\n\t" \
    "v_mov_b32 v106, 0\n\t" \
    "v_mov_b32 v107, 0\n\t" \
    "v_mov_b32 v108, 0\n\t" \
    "v_mov_b32 v109, 0\n\t" \
    "v_mov_b32 v110, 0\n\t" \
    "v_mov_b32 v111, 0\n\t" \
    "v_mov_b32 v112, 0\n\t" \
    "v_mov_b32 v113, 0\n\t" \
    "v_mov_b32 v114, 0\n\t" \
    "v_mov_b32 v115, 0\n\t" \
    "v_mov_b32 v116, 0\n\t" \
    "v_mov_b32 v117, 0\n\t" \
    "v_mov_b32 v118, 0\n\t" \
    "v_mov_b32 v119, 0\n\t" \
    "v_mov_b32 v120, 0\n\t" \
    "v_mov_b32 v121, 0\n\t" \
    "v_mov_b32 v122, 0\n\t" \
    "v_mov_b32 v123, 0\n\t" \
    "v_mov_b32 v124, 0\n\t" \
    "v_mov_b32 v125, 0\n\t" \
    "v_mov_b32 v126, 0\n\t" \
    "v_mov_b32 v127, 0\n\t" \
    "vv_go42_%=:\n\t" \
    "s_add_u32 s69, s65, 0\n\t" \
    "s_bitcmp1_b32 s69, 0\n\t" \
    "s_cselect_b32 s70, s63, s62\n\t" \
    "s_bfe_u32 s69, s69, 0x20001\n\t" \
    "s_lshl_b32 s69, s69, 19\n\t" \
    "s_add_u32 s69, s69, s70\n\t" \
    "s_add_u32 s46, s36, s69\n\t" \
    "s_addc_u32 s47, s37, 0\n\t" \
    "s_add_u32 s69, s65, 0\n\t" \
    "s_lshr_b32 s69, s69, 1\n\t" \
    "s_xor_b32 s69, s69, s97\n\t" \
    "s_and_b32 s69, s69, 1\n\t" \
    "s_lshl_b32 s69, s69, 14\n\t" \
    "s_or_b32 s46, s46, s69\n\t" \
    "s_add_u32 s69, s65, 0\n\t" \
    "s_mul_i32 s70, s69, 171\n\t" \
    "s_lshr_b32 s70, s70, 9\n\t" \
    "s_mul_i32 s70, s70, 3\n\t" \
    "s_sub_u32 s69, s69, s70\n\t" \
    "s_mul_i32 s69, s69, 36\n\t" \
    "s_lshl_b32 s69, s69, 14\n\t" \
    "s_add_u32 s69, s69, s64\n\t" \
    "s_add_u32 s69, s69, 4194304\n\t" \
    "s_add_u32 s38, s36, s69\n\t" \
    "s_addc_u32 s39, s37, 0\n\t" \
    "s_mul_i32 s69, s65, 9216\n\t" \
    "s_add_u32 s69, s69, 123731968\n\t" \
    "s_add_u32 s40, s36, s69\n\t" \
    "s_addc_u32 s41, s37, 0\n\t" \
    "s_sub_u32 s70, s79, s74\n\t" \
    "s_max_i32 s70, s70, 0\n\t" \
    "s_min_i32 s70, s70, 16000\n\t" \
    "s_lshl_b32 s70, s70, 1\n\t" \
    "s_mul_hi_u32 s70, s70, s77\n\t" \
    "s_add_u32 s70, s70, s73\n\t" \
    "s_sub_u32 s70, s70, 100\n\t" \
    "s_getpc_b64 s[30:31]\n\t" \
    "vv_Wp44_%=:\n\t" \
    "s_add_u32 s30, s30, vv_We45_%=-vv_Wp44_%=\n\t" \
    "s_addc_u32 s31, s31, 0\n\t" \
    "vv_W43_%=:\n\t" \
    "s_memtime s[56:57]\n\t" \
    "s_waitcnt lgkmcnt(0)\n\t" \
    "s_sub_u32 s69, s70, s56\n\t" \
    "s_cmp_gt_i32 s69, 111\n\t" \
    "s_cbranch_scc1 vv_W43_%=\n\t" \
    "s_max_i32 s69, s69, 0\n\t" \
    "s_lshr_b32 s69, s69, 1\n\t" \
    "s_and_b32 s69, s69, 0x3c\n\t" \
    "s_sub_u32 s30, s30, s69\n\t" \
    "s_subb_u32 s31, s31, 0\n\t" \
    "s_setpc_b64 s[30:31]\n\t" \
    ".p2align 6\n\t" \
    "vv_al46_%=:\n\t" \
    "s_nop 0\n\t" \
    "s_nop 1\n\t" \
    "s_nop 1\n\t" \
    "s_nop 1\n\t" \
    "s_nop 1\n\t" \
    "s_nop 1\n\t" \
    "s_nop 1\n\t" \
    "s_nop 1\n\t" \
    "s_nop 1\n\t" \
    "s_nop 1\n\t" \
    "s_nop 1\n\t" \
    "s_nop 1\n\t" \
    "s_nop 1\n\t" \
    "s_nop 1\n\t" \
    "vv_We45_%=:\n\t" \
    "vv_G47_%=:\n\t" \
    "global_load_dwordx4 v[36:39], v1, s[46:47] sc1\n\t" \
    "s_nop 9\n\t" \
    "global_load_dwordx4 v[40:43], v1, s[46:47] offset:1024 sc1\n\t" \
    "s_nop 9\n\t" \
    "global_load_dwordx4 v[44:47], v2, s[46:47] sc1\n\t" \
    "s_waitcnt vmcnt(2)\n\t" \
    "v_smfmac_f32_16x16x64_bf16 v[72:75], v[36:39], a[16:23], v10\n\t" \
    "ds_write_b128 v0, v[36:39] offset:2048\n\t" \
    "v_smfmac_f32_16x16x64_bf16 v[76:79], v[36:39], a[88:95], v10\n\t" \
    "v_max3_u32 v140, v36, v37, v38\n\t" \
    "s_waitcnt vmcnt(1)\n\t" \
    "ds_write_b128 v0, v[40:43] offset:3072\n\t" \
    "v_smfmac_f32_16x16x64_bf16 v[72:75], v[40:43], a[24:31], v10\n\t" \
    "v_max3_u32 v140, v140, v39, v40\n\t" \
    "v_max3_u32 v140, v140, v41, v42\n\t" \
    "v_max_u32 v140, v140, v43\n\t" \
    "v_cmp_eq_u32 vcc, -1, v140\n\t" \
    "v_smfmac_f32_16x16x64_bf16 v[76:79], v[40:43], a[96:103], v10\n\t" \
    "s_cbranch_vccnz vv_Gbad48_%=\n\t" \
    "vv_Gc50_%=:\n\t" \
    "s_waitcnt lgkmcnt(0)\n\t" \
    "s_barrier\n\t" \
    "ds_read_b128 v[48:51], v0 offset:4096\n\t" \
    "ds_read_b128 v[52:55], v0 offset:5120\n\t" \
    "s_waitcnt vmcnt(0)\n\t" \
    "v_smfmac_f32_16x16x64_bf16 v[72:75], v[44:47], a[64:71], v10\n\t" \
    "ds_read_b128 v[56:59], v0 offset:6144\n\t" \
    "ds_read_b128 v[60:63], v0 offset:7168\n\t" \
    "v_max3_u32 v140, v44, v45, v46\n\t" \
    "v_max_u32 v140, v140, v47\n\t" \
    "v_smfmac_f32_16x16x64_bf16 v[76:79], v[44:47], a[136:143], v10\n\t" \
    "ds_read_b128 v[64:67], v0 offset:0\n\t" \
    "ds_read_b128 v[68:71], v0 offset:1024\n\t" \
    "v_cmp_eq_u32 vcc, -1, v140\n\t" \
    "s_cbranch_vccnz vv_Gbad249_%=\n\t" \
    "vv_Gc251_%=:\n\t" \
    "s_waitcnt lgkmcnt(5)\n\t" \
    "v_smfmac_f32_16x16x64_bf16 v[72:75], v[48:51], a[32:39], v10\n\t" \
    "s_waitcnt lgkmcnt(4)\n\t" \
    "v_smfmac_f32_16x16x64_bf16 v[72:75], v[52:55], a[40:47], v10\n\t" \
    "s_waitcnt lgkmcnt(3)\n\t" \
    "v_smfmac_f32_16x16x64_bf16 v[72:75], v[56:59], a[48:55], v10\n\t" \
    "s_waitcnt lgkmcnt(2)\n\t" \
    "v_smfmac_f32_16x16x64_bf16 v[72:75], v[60:63], a[56:63], v10\n\t" \
    "s_waitcnt lgkmcnt(1)\n\t" \
    "v_smfmac_f32_16x16x64_bf16 v[72:75], v[64:67], a[0:7], v10\n\t" \
    "s_waitcnt lgkmcnt(0)\n\t" \
    "v_smfmac_f32_16x16x64_bf16 v[72:75], v[68:71], a[8:15], v10\n\t" \
    "v_smfmac_f32_16x16x64_bf16 v[76:79], v[48:51], a[104:111], v10\n\t" \
    "s_nop 1\n\t" \
    "s_nop 0\n\t" \
    "v_smfmac_f32_16x16x64_bf16 v[76:79], v[52:55], a[112:119], v10\n\t" \
    "s_nop 1\n\t" \
    "s_nop 0\n\t" \
    "v_smfmac_f32_16x16x64_bf16 v[76:79], v[56:59], a[120:127], v10\n\t" \
    "v_add_f32 v72, v72, v74\n\t" \
    "v_smfmac_f32_16x16x64_bf16 v[76:79], v[60:63], a[128:135], v10\n\t" \
    "v_add_f32 v73, v73, v75\n\t" \
    "v_smfmac_f32_16x16x64_bf16 v[76:79], v[64:67], a[72:79], v10\n\t" \
    "v_cvt_pk_bf16_f32 v80, v72, v73\n\t" \
    "v_smfmac_f32_16x16x64_bf16 v[76:79], v[68:71], a[80:87], v10\n\t" \
    "ds_write_b32 v12, v80 offset:0\n\t" \
    "s_nop 6\n\t" \
    "v_pk_add_f32 v[76:77], v[76:77], v[78:79]\n\t" \
    "v_cvt_pk_bf16_f32 v81, v76, v77\n\t" \
    "ds_write_b32 v12, v81 offset:64\n\t" \
    "s_waitcnt lgkmcnt(0)\n\t" \
    "s_barrier\n\t" \
    "ds_read_b64_tr_b16 v[84:85], v13 offset:0\n\t" \
    "ds_read_b64_tr_b16 v[86:87], v13 offset:128\n\t" \
    "ds_read_b64_tr_b16 v[88:89], v13 offset:1024\n\t" \
    "ds_read_b64_tr_b16 v[90:91], v13 offset:1152\n\t" \
    "s_waitcnt lgkmcnt(2)\n\t" \
    "v_smfmac_f32_16x16x64_bf16 v[92:95], v[84:87], a[144:151], v10\n\t" \
    "s_nop 2\n\t" \
    "s_waitcnt lgkmcnt(0)\n\t" \
    "v_smfmac_f32_16x16x64_bf16 v[92:95], v[88:91], a[152:159], v10\n\t" \
    "s_nop 2\n\t" \
    "v_smfmac_f32_16x16x64_bf16 v[96:99], v[84:87], a[160:167], v10\n\t" \
    "s_nop 2\n\t" \
    "v_smfmac_f32_16x16x64_bf16 v[96:99], v[88:91], a[168:175], v10\n\t" \
    "s_nop 2\n\t" \
    "v_smfmac_f32_16x16x64_bf16 v[100:103], v[84:87], a[176:183], v10\n\t" \
    "s_nop 0\n\t" \
    "v_add_f32 v92, v92, v94\n\t" \
    "v_add_f32 v93, v93, v95\n\t" \
    "v_smfmac_f32_16x16x64_bf16 v[100:103], v[88:91], a[184:191], v10\n\t" \
    "s_nop 1\n\t" \
    "v_cvt_pk_bf16_f32 v128, v92, v93\n\t" \
    "v_smfmac_f32_16x16x64_bf16 v[104:107], v[84:87], a[192:199], v10\n\t" \
    "s_nop 0\n\t" \
    "v_add_f32 v96, v96, v98\n\t" \
    "v_add_f32 v97, v97, v99\n\t" \
    "v_smfmac_f32_16x16x64_bf16 v[104:107], v[88:91], a[200:207], v10\n\t" \
    "s_nop 1\n\t" \
    "v_cvt_pk_bf16_f32 v129, v96, v97\n\t" \
    "v_smfmac_f32_16x16x64_bf16 v[108:111], v[84:87], a[208:215], v10\n\t" \
    "s_nop 0\n\t" \
    "v_add_f32 v100, v100, v102\n\t" \
    "v_add_f32 v101, v101, v103\n\t" \
    "v_smfmac_f32_16x16x64_bf16 v[108:111], v[88:91], a[216:223], v10\n\t" \
    "s_nop 1\n\t" \
    "global_load_dword v140, v20, s[46:47] sc1\n\t" \
    "v_cvt_pk_bf16_f32 v130, v100, v101\n\t" \
    "v_smfmac_f32_16x16x64_bf16 v[112:115], v[84:87], a[224:231], v10\n\t" \
    "s_nop 0\n\t" \
    "v_add_f32 v104, v104, v106\n\t" \
    "v_add_f32 v105, v105, v107\n\t" \
    "v_smfmac_f32_16x16x64_bf16 v[112:115], v[88:91], a[232:239], v10\n\t" \
    "s_nop 0\n\t" \
    "v_cvt_pk_bf16_f32 v131, v104, v105\n\t" \
    "v_permlane32_swap_b32 v128, v130\n\t" \
    "v_smfmac_f32_16x16x64_bf16 v[116:119], v[84:87], a[240:247], v10\n\t" \
    "s_nop 0\n\t" \
    "v_permlane32_swap_b32 v129, v131\n\t" \
    "s_nop 1\n\t" \
    "v_permlane16_swap_b32 v128, v129\n\t" \
    "v_smfmac_f32_16x16x64_bf16 v[116:119], v[88:91], a[248:255], v10\n\t" \
    "v_permlane16_swap_b32 v130, v131\n\t" \
    "global_store_dwordx4 v14, v[128:131], s[38:39] sc1\n\t" \
    "v_add_f32 v108, v108, v110\n\t" \
    "v_smfmac_f32_16x16x64_bf16 v[120:123], v[84:87], v[224:231], v10\n\t" \
    "s_nop 0\n\t" \
    "v_add_f32 v109, v109, v111\n\t" \
    "v_cvt_pk_bf16_f32 v132, v108, v109\n\t" \
    "v_smfmac_f32_16x16x64_bf16 v[120:123], v[88:91], v[232:239], v10\n\t" \
    "s_nop 0\n\t" \
    "v_add_f32 v112, v112, v114\n\t" \
    "v_add_f32 v113, v113, v115\n\t" \
    "v_smfmac_f32_16x16x64_bf16 v[124:127], v[84:87], v[240:247], v10\n\t" \
    "s_nop 0\n\t" \
    "v_cvt_pk_bf16_f32 v133, v112, v113\n\t" \
    "v_add_f32 v116, v116, v118\n\t" \
    "v_smfmac_f32_16x16x64_bf16 v[124:127], v[88:91], v[248:255], v10\n\t" \
    "v_add_f32 v117, v117, v119\n\t" \
    "v_cvt_pk_bf16_f32 v134, v116, v117\n\t" \
    "v_add_f32 v120, v120, v122\n\t" \
    "v_add_f32 v121, v121, v123\n\t" \
    "v_cvt_pk_bf16_f32 v135, v120, v121\n\t" \
    "v_permlane32_swap_b32 v132, v134\n\t" \
    "s_nop 0\n\t" \
    "v_permlane32_swap_b32 v133, v135\n\t" \
    "s_nop 1\n\t" \
    "v_permlane16_swap_b32 v132, v133\n\t" \
    "v_permlane16_swap_b32 v134, v135\n\t" \
    "global_store_dwordx4 v15, v[132:135], s[38:39] sc1\n\t" \
    "v_pk_add_f32 v[124:125], v[124:125], v[126:127]\n\t" \
    "v_cvt_pk_bf16_f32 v136, v124, v125\n\t" \
    "global_store_dword v16, v136, s[38:39] sc1\n\t" \
    "s_nop 1\n\t" \
    "global_store_dwordx4 v17, v[128:131], s[40:41] sc1\n\t" \
    "global_store_dwordx4 v17, v[132:135], s[40:41] offset:1024 sc1\n\t" \
    "global_store_dword v18, v136, s[40:41] sc1\n\t" \
    "s_cmp_eq_u32 s65, 63\n\t" \
    "s_cbranch_scc1 vv_end41_%=\n\t" \
    "s_mov_b32 s71, 3\n\t" \
    "vv_E53_%=:\n\t" \
    "s_memtime s[54:55]\n\t" \
    "s_nop 0\n\t" \
    "s_memrealtime s[4:5]\n\t" \
    "s_nop 0\n\t" \
    "s_memrealtime s[6:7]\n\t" \
    "s_nop 0\n\t" \
    "s_memrealtime s[8:9]\n\t" \
    "s_nop 0\n\t" \
    "s_memrealtime s[10:11]\n\t" \
    "s_nop 0\n\t" \
    "s_memrealtime s[12:13]\n\t" \
    "s_nop 0\n\t" \
    "s_memrealtime s[14:15]\n\t" \
    "s_nop 0\n\t" \
    "s_memrealtime s[16:17]\n\t" \
    "s_nop 0\n\t" \
    "s_memrealtime s[18:19]\n\t" \
    "s_nop 0\n\t" \
    "s_memrealtime s[20:21]\n\t" \
    "s_nop 0\n\t" \
    "s_memrealtime s[22:23]\n\t" \
    "s_nop 0\n\t" \
    "s_memrealtime s[24:25]\n\t" \
    "s_nop 0\n\t" \
    "s_memrealtime s[26:27]\n\t" \
    "s_nop 0\n\t" \
    "s_memrealtime s[28:29]\n\t" \
    "s_nop 0\n\t" \
    "s_memtime s[56:57]\n\t" \
    "s_mov_b32 s70, 1\n\t" \
    "s_sub_u32 s71, s71, 1\n\t" \
    "s_cselect_b32 s69, 0, -1\n\t" \
    "s_waitcnt lgkmcnt(0)\n\t" \
    "s_sub_u32 s54, s56, s54\n\t" \
    "s_and_b32 s54, s54, s69\n\t" \
    "s_cmp_gt_u32 s54, 124\n\t" \
    "s_cbranch_scc1 vv_E53_%=\n\t" \
    "s_cmp_eq_u32 s4, s28\n\t" \
    "s_addc_u32 s70, s70, 0\n\t" \
    "s_cmp_eq_u32 s6, s28\n\t" \
    "s_addc_u32 s70, s70, 0\n\t" \
    "s_cmp_eq_u32 s8, s28\n\t" \
    "s_addc_u32 s70, s70, 0\n\t" \
    "s_cmp_eq_u32 s10, s28\n\t" \
    "s_addc_u32 s70, s70, 0\n\t" \
    "s_cmp_eq_u32 s12, s28\n\t" \
    "s_addc_u32 s70, s70, 0\n\t" \
    "s_cmp_eq_u32 s14, s28\n\t" \
    "s_addc_u32 s70, s70, 0\n\t" \
    "s_cmp_eq_u32 s16, s28\n\t" \
    "s_addc_u32 s70, s70, 0\n\t" \
    "s_cmp_eq_u32 s18, s28\n\t" \
    "s_addc_u32 s70, s70, 0\n\t" \
    "s_cmp_eq_u32 s20, s28\n\t" \
    "s_addc_u32 s70, s70, 0\n\t" \
    "s_cmp_eq_u32 s22, s28\n\t" \
    "s_addc_u32 s70, s70, 0\n\t" \
    "s_cmp_eq_u32 s24, s28\n\t" \
    "s_addc_u32 s70, s70, 0\n\t" \
    "s_cmp_eq_u32 s26, s28\n\t" \
    "s_addc_u32 s70, s70, 0\n\t" \
    "s_mov_b32 s75, s73\n\t" \
    "s_mov_b32 s76, s74\n\t" \
    "s_lshl_b32 s70, s70, 3\n\t" \
    "s_sub_u32 s73, s56, s70\n\t" \
    "s_lshl_b32 s74, s28, 4\n\t" \
    "s_sub_u32 s69, s73, s75\n\t" \
    "s_sub_u32 s70, s74, s76\n\t" \
    "s_sub_u32 s71, s70, 1600\n\t" \
    "s_cmp_lt_u32 s71, 400000\n\t" \
    "s_cbranch_scc0 vv_K55_%=\n\t" \
    "v_cvt_f32_u32 v141, s69\n\t" \
    "v_cvt_f32_u32 v142, s70\n\t" \
    "v_rcp_f32 v143, v141\n\t" \
    "v_rcp_f32 v144, v142\n\t" \
    "v_mul_f32 v143, v143, v142\n\t" \
    "v_mul_f32 v144, v144, v141\n\t" \
    "v_mul_f32 v143, 0x4f800000, v143\n\t" \
    "v_mul_f32 v144, 0x4f000000, v144\n\t" \
    "v_cvt_u32_f32 v143, v143\n\t" \
    "v_cvt_u32_f32 v144, v144\n\t" \
    "v_readfirstlane_b32 s69, v143\n\t" \
    "v_readfirstlane_b32 s70, v144\n\t" \
    "s_nop 1\n\t" \
    "s_sub_u32 s69, s69, s78\n\t" \
    "s_ashr_i32 s69, s69, 2\n\t" \
    "s_add_u32 s78, s78, s69\n\t" \
    "s_sub_u32 s70, s70, s77\n\t" \
    "s_ashr_i32 s70, s70, 2\n\t" \
    "s_add_u32 s77, s77, s70\n\t" \
    "vv_K55_%=:\n\t" \
    "s_waitcnt vmcnt(6)\n\t" \
    "v_cmp_eq_u32 vcc, -1, v140\n\t" \
    "s_cbranch_vccz vv_Hok57_%=\n\t" \
    "vv_H56_%=:\n\t" \
    "global_load_dword v140, v20, s[46:47] sc1\n\t" \
    "s_waitcnt vmcnt(0)\n\t" \
    "v_cmp_eq_u32 vcc, -1, v140\n\t" \
    "s_cbranch_vccz vv_Hc58_%=\n\t" \
    "s_cmp_eq_u32 s68, 0\n\t" \
    "s_cbranch_scc1 vv_F59_%=\n\t" \
    "s_sub_u32 s68, s68, 1\n\t" \
    "s_sleep 5\n\t" \
    "s_branch vv_H56_%=\n\t" \
    "vv_F59_%=:\n\t" \
    "v_mov_b32 v141, 6095104\n\t" \
    "v_mov_b32 v142, 6\n\t" \
    "global_store_dword v141, v142, s[36:37] sc1\n\t" \
    "s_waitcnt vmcnt(0)\n\t" \
    "s_branch vv_Hc58_%=\n\t" \
    "vv_Hc58_%=:\n\t" \
    "vv_Hok57_%=:\n\t" \
    "v_subrev_u32 v140, s80, v140\n\t" \
    "v_and_b32 v20, -5, v20\n\t" \
    "s_nop 0\n\t" \
    "v_max_i32_dpp v140, v140, v140 row_ror:1 row_mask:0xf bank_mask:0xf\n\t" \
    "s_nop 1\n\t" \
    "v_max_i32_dpp v140, v140, v140 row_ror:2 row_mask:0xf bank_mask:0xf\n\t" \
    "s_nop 1\n\t" \
    "v_max_i32_dpp v140, v140, v140 row_ror:4 row_mask:0xf bank_mask:0xf\n\t" \
    "s_nop 1\n\t" \
    "v_max_i32_dpp v140, v140, v140 row_ror:8 row_mask:0xf bank_mask:0xf\n\t" \
    "s_nop 0\n\t" \
    "v_readlane_b32 s69, v140, 0\n\t" \
    "v_readlane_b32 s71, v140, 16\n\t" \
    "v_readlane_b32 s70, v140, 32\n\t" \
    "v_readlane_b32 s81, v140, 48\n\t" \
    "s_max_i32 s69, s69, s71\n\t" \
    "s_nop 0\n\t" \
    "s_max_i32 s71, s70, s81\n\t" \
    "s_max_i32 s69, s69, s71\n\t" \
    "s_add_u32 s81, s69, s80\n\t" \
    "v_mov_b32 v72, 0\n\t" \
    "v_mov_b32 v73, 0\n\t" \
    "v_mov_b32 v74, 0\n\t" \
    "v_mov_b32 v75, 0\n\t" \
    "v_mov_b32 v76, 0\n\t" \
    "v_mov_b32 v77, 0\n\t" \
    "v_mov_b32 v78, 0\n\t" \
    "v_mov_b32 v79, 0\n\t" \
    "v_mov_b32 v92, 0\n\t" \
    "v_mov_b32 v93, 0\n\t" \
    "v_mov_b32 v94, 0\n\t" \
    "v_mov_b32 v95, 0\n\t" \
    "v_mov_b32 v96, 0\n\t" \
    "v_mov_b32 v97, 0\n\t" \
    "v_mov_b32 v98, 0\n\t" \
    "v_mov_b32 v99, 0\n\t" \
    "v_mov_b32 v100, 0\n\t" \
    "v_mov_b32 v101, 0\n\t" \
    "v_mov_b32 v102, 0\n\t" \
    "v_mov_b32 v103, 0\n\t" \
    "v_mov_b32 v104, 0\n\t" \
    "v_mov_b32 v105, 0\n\t" \
    "v_mov_b32 v106, 0\n\t" \
    "v_mov_b32 v107, 0\n\t" \
    "v_mov_b32 v108, 0\n\t" \
    "v_mov_b32 v109, 0\n\t" \
    "v_mov_b32 v110, 0\n\t" \
    "v_mov_b32 v111, 0\n\t" \
    "v_mov_b32 v112, 0\n\t" \
    "v_mov_b32 v113, 0\n\t" \
    "v_mov_b32 v114, 0\n\t" \
    "v_mov_b32 v115, 0\n\t" \
    "v_mov_b32 v116, 0\n\t" \
    "v_mov_b32 v117, 0\n\t" \
    "v_mov_b32 v118, 0\n\t" \
    "v_mov_b32 v119, 0\n\t" \
    "v_mov_b32 v120, 0\n\t" \
    "v_mov_b32 v121, 0\n\t" \
    "v_mov_b32 v122, 0\n\t" \
    "v_mov_b32 v123, 0\n\t" \
    "v_mov_b32 v124, 0\n\t" \
    "v_mov_b32 v125, 0\n\t" \
    "v_mov_b32 v126, 0\n\t" \
    "v_mov_b32 v127, 0\n\t" \
    "s_cmp_lg_u32 s65, 0\n\t" \
    "s_cbranch_scc1 vv_Oc62_%=\n\t" \
    "s_add_u32 s79, s81, 240\n\t" \
    "s_sub_u32 s70, s79, s74\n\t" \
    "s_max_i32 s70, s70, 0\n\t" \
    "s_min_i32 s70, s70, 16000\n\t" \
    "s_lshl_b32 s70, s70, 1\n\t" \
    "s_mul_hi_u32 s70, s70, s77\n\t" \
    "s_add_u32 s70, s70, s73\n\t" \
    "vv_W63_%=:\n\t" \
    "s_memtime s[56:57]\n\t" \
    "s_waitcnt lgkmcnt(0)\n\t" \
    "s_sub_u32 s69, s70, s56\n\t" \
    "s_cmp_gt_i32 s69, 24\n\t" \
    "s_cbranch_scc1 vv_W63_%=\n\t" \
    "global_load_dword v140, v35, s[46:47] sc1\n\t" \
    "s_waitcnt vmcnt(0)\n\t" \
    "v_cmp_eq_u32 vcc, -1, v140\n\t" \
    "s_cbranch_vccz vv_Ook61_%=\n\t" \
    "vv_O60_%=:\n\t" \
    "global_load_dword v140, v35, s[46:47] offset:1536 sc1\n\t" \
    "s_waitcnt vmcnt(0)\n\t" \
    "v_cmp_eq_u32 vcc, -1, v140\n\t" \
    "s_cbranch_vccz vv_Ook61_%=\n\t" \
    "s_cmp_eq_u32 s68, 0\n\t" \
    "s_cbranch_scc1 vv_F64_%=\n\t" \
    "s_sub_u32 s68, s68, 1\n\t" \
    "s_sleep 3\n\t" \
    "s_branch vv_O60_%=\n\t" \
    "vv_F64_%=:\n\t" \
    "v_mov_b32 v141, 6095104\n\t" \
    "v_mov_b32 v142, 17\n\t" \
    "global_store_dword v141, v142, s[36:37] sc1\n\t" \
    "s_waitcnt vmcnt(0)\n\t" \
    "s_branch vv_Ook61_%=\n\t" \
    "vv_Ook61_%=:\n\t" \
    "v_subrev_u32 v140, s80, v140\n\t" \
    "s_nop 1\n\t" \
    "v_max_i32_dpp v140, v140, v140 row_ror:1 row_mask:0xf bank_mask:0xf\n\t" \
    "s_nop 1\n\t" \
    "v_max_i32_dpp v140, v140, v140 row_ror:2 row_mask:0xf bank_mask:0xf\n\t" \
    "s_nop 1\n\t" \
    "v_max_i32_dpp v140, v140, v140 row_ror:4 row_mask:0xf bank_mask:0xf\n\t" \
    "s_nop 1\n\t" \
    "v_max_i32_dpp v140, v140, v140 row_ror:8 row_mask:0xf bank_mask:0xf\n\t" \
    "s_nop 0\n\t" \
    "v_readlane_b32 s69, v140, 0\n\t" \
    "v_readlane_b32 s71, v140, 16\n\t" \
    "v_readlane_b32 s70, v140, 32\n\t" \
    "v_readlane_b32 s81, v140, 48\n\t" \
    "s_max_i32 s69, s69, s71\n\t" \
    "s_nop 0\n\t" \
    "s_max_i32 s71, s70, s81\n\t" \
    "s_max_i32 s69, s69, s71\n\t" \
    "s_add_u32 s81, s69, s80\n\t" \
    "vv_Oc62_%=:\n\t" \
    "s_mul_hi_u32 s69, s85, s78\n\t" \
    "s_add_u32 s79, s81, s69\n\t" \
    "s_add_u32 s79, s79, s87\n\t" \
    "s_cmp_ge_u32 s65, 36\n\t" \
    "s_cselect_b32 s69, 28, 0\n\t" \
    "s_sub_u32 s79, s79, s69\n\t" \
    "s_cmp_lt_u32 s65, 8\n\t" \
    "s_cselect_b32 s69, 64, 0\n\t" \
    "s_add_u32 s79, s79, s69\n\t" \
    "s_cmp_ge_u32 s65, 57\n\t" \
    "s_cselect_b32 s69, 32, 0\n\t" \
    "s_add_u32 s79, s79, s69\n\t" \
    "s_add_u32 s80, s79, s88\n\t" \
    "s_add_u32 s65, s65, 1\n\t" \
    "s_branch vv_go42_%=\n\t" \
    "vv_Gbad48_%=:\n\t" \
    "s_waitcnt vmcnt(0) lgkmcnt(0)\n\t" \
    "s_nop 7\n\t" \
    "v_mov_b32 v72, 0\n\t" \
    "v_mov_b32 v73, 0\n\t" \
    "v_mov_b32 v74, 0\n\t" \
    "v_mov_b32 v75, 0\n\t" \
    "v_mov_b32 v76, 0\n\t" \
    "v_mov_b32 v77, 0\n\t" \
    "v_mov_b32 v78, 0\n\t" \
    "v_mov_b32 v79, 0\n\t" \
    "s_cmp_eq_u32 s68, 0\n\t" \
    "s_cbranch_scc1 vv_F65_%=\n\t" \
    "s_sub_u32 s68, s68, 1\n\t" \
    "s_branch vv_G47_%=\n\t" \
    "vv_F65_%=:\n\t" \
    "v_mov_b32 v140, 6095104\n\t" \
    "v_mov_b32 v141, 1\n\t" \
    "global_store_dword v140, v141, s[36:37] sc1\n\t" \
    "s_waitcnt vmcnt(0)\n\t" \
    "s_branch vv_Gc50_%=\n\t" \
    "vv_Gbad249_%=:\n\t" \
    "global_load_dwordx4 v[44:47], v2, s[46:47] sc1\n\t" \
    "s_waitcnt vmcnt(0)\n\t" \
    "v_max3_u32 v140, v44, v45, v46\n\t" \
    "v_max_u32 v140, v140, v47\n\t" \
    "v_cmp_eq_u32 vcc, -1, v140\n\t" \
    "s_cbranch_vccz vv_Gre252_%=\n\t" \
    "s_cmp_eq_u32 s68, 0\n\t" \
    "s_cbranch_scc1 vv_F66_%=\n\t" \
    "s_sub_u32 s68, s68, 1\n\t" \
    "s_sleep 1\n\t" \
    "s_branch vv_Gbad249_%=\n\t" \
    "vv_F66_%=:\n\t" \
    "v_mov_b32 v140, 6095104\n\t" \
    "v_mov_b32 v141, 2\n\t" \
    "global_store_dword v140, v141, s[36:37] sc1\n\t" \
    "s_waitcnt vmcnt(0)\n\t" \
    "s_branch vv_Gre252_%=\n\t" \
    "vv_Gre252_%=:\n\t" \
    "v_mov_b32 v72, 0\n\t" \
    "v_mov_b32 v73, 0\n\t" \
    "v_mov_b32 v74, 0\n\t" \
    "v_mov_b32 v75, 0\n\t" \
    "v_mov_b32 v76, 0\n\t" \
    "v_mov_b32 v77, 0\n\t" \
    "v_mov_b32 v78, 0\n\t" \
    "v_mov_b32 v79, 0\n\t" \
    "v_smfmac_f32_16x16x64_bf16 v[72:75], v[36:39], a[16:23], v10\n\t" \
    "s_nop 0\n\t" \
    "v_smfmac_f32_16x16x64_bf16 v[76:79], v[36:39], a[88:95], v10\n\t" \
    "v_smfmac_f32_16x16x64_bf16 v[72:75], v[40:43], a[24:31], v10\n\t" \
    "v_smfmac_f32_16x16x64_bf16 v[76:79], v[40:43], a[96:103], v10\n\t" \
    "v_smfmac_f32_16x16x64_bf16 v[72:75], v[44:47], a[64:71], v10\n\t" \
    "v_smfmac_f32_16x16x64_bf16 v[76:79], v[44:47], a[136:143], v10\n\t" \
    "s_branch vv_Gc251_%=\n\t" \
    "vv_end41_%=:\n\t" \
    "s_endpgm\n\t" \
    "vv_loop12_%=:\n\t" \
    "v_mov_b32 v72, 0\n\t" \
    "v_mov_b32 v73, 0\n\t" \
    "v_mov_b32 v74, 0\n\t" \
    "v_mov_b32 v75, 0\n\t" \
    "v_mov_b32 v76, 0\n\t" \
    "v_mov_b32 v77, 0\n\t" \
    "v_mov_b32 v78, 0\n\t" \
    "v_mov_b32 v79, 0\n\t" \
    "v_mov_b32 v92, 0\n\t" \
    "v_mov_b32 v93, 0\n\t" \
    "v_mov_b32 v94, 0\n\t" \
    "v_mov_b32 v95, 0\n\t" \
    "v_mov_b32 v96, 0\n\t" \
    "v_mov_b32 v97, 0\n\t" \
    "v_mov_b32 v98, 0\n\t" \
    "v_mov_b32 v99, 0\n\t" \
    "v_mov_b32 v100, 0\n\t" \
    "v_mov_b32 v101, 0\n\t" \
    "v_mov_b32 v102, 0\n\t" \
    "v_mov_b32 v103, 0\n\t" \
    "v_mov_b32 v104, 0\n\t" \
    "v_mov_b32 v105, 0\n\t" \
    "v_mov_b32 v106, 0\n\t" \
    "v_mov_b32 v107, 0\n\t" \
    "v_mov_b32 v108, 0\n\t" \
    "v_mov_b32 v109, 0\n\t" \
    "v_mov_b32 v110, 0\n\t" \
    "v_mov_b32 v111, 0\n\t" \
    "v_mov_b32 v112, 0\n\t" \
    "v_mov_b32 v113, 0\n\t" \
    "v_mov_b32 v114, 0\n\t" \
    "v_mov_b32 v115, 0\n\t" \
    "v_mov_b32 v116, 0\n\t" \
    "v_mov_b32 v117, 0\n\t" \
    "v_mov_b32 v118, 0\n\t" \
    "v_mov_b32 v119, 0\n\t" \
    "v_mov_b32 v120, 0\n\t" \
    "v_mov_b32 v121, 0\n\t" \
    "v_mov_b32 v122, 0\n\t" \
    "v_mov_b32 v123, 0\n\t" \
    "v_mov_b32 v124, 0\n\t" \
    "v_mov_b32 v125, 0\n\t" \
    "v_mov_b32 v126, 0\n\t" \
    "v_mov_b32 v127, 0\n\t" \
    "vv_go68_%=:\n\t" \
    "s_add_u32 s69, s65, 0\n\t" \
    "s_bitcmp1_b32 s69, 0\n\t" \
    "s_cselect_b32 s70, s63, s62\n\t" \
    "s_bfe_u32 s69, s69, 0x20001\n\t" \
    "s_lshl_b32 s69, s69, 19\n\t" \
    "s_add_u32 s69, s69, s70\n\t" \
    "s_add_u32 s46, s36, s69\n\t" \
    "s_addc_u32 s47, s37, 0\n\t" \
    "s_add_u32 s69, s65, 0\n\t" \
    "s_lshr_b32 s69, s69, 1\n\t" \
    "s_xor_b32 s69, s69, s97\n\t" \
    "s_and_b32 s69, s69, 1\n\t" \
    "s_lshl_b32 s69, s69, 14\n\t" \
    "s_or_b32 s46, s46, s69\n\t" \
    "s_add_u32 s69, s65, 0\n\t" \
    "s_mul_i32 s70, s69, 171\n\t" \
    "s_lshr_b32 s70, s70, 9\n\t" \
    "s_mul_i32 s70, s70, 3\n\t" \
    "s_sub_u32 s69, s69, s70\n\t" \
    "s_mul_i32 s69, s69, 36\n\t" \
    "s_lshl_b32 s69, s69, 14\n\t" \
    "s_add_u32 s69, s69, s64\n\t" \
    "s_add_u32 s69, s69, 4194304\n\t" \
    "s_add_u32 s38, s36, s69\n\t" \
    "s_addc_u32 s39, s37, 0\n\t" \
    "s_mul_i32 s69, s65, 9216\n\t" \
    "s_add_u32 s69, s69, 123731968\n\t" \
    "s_add_u32 s40, s36, s69\n\t" \
    "s_addc_u32 s41, s37, 0\n\t" \
    "s_sub_u32 s70, s79, s74\n\t" \
    "s_max_i32 s70, s70, 0\n\t" \
    "s_min_i32 s70, s70, 16000\n\t" \
    "s_lshl_b32 s70, s70, 1\n\t" \
    "s_mul_hi_u32 s70, s70, s77\n\t" \
    "s_add_u32 s70, s70, s73\n\t" \
    "s_sub_u32 s70, s70, 100\n\t" \
    "s_getpc_b64 s[30:31]\n\t" \
    "vv_Wp70_%=:\n\t" \
    "s_add_u32 s30, s30, vv_We71_%=-vv_Wp70_%=\n\t" \
    "s_addc_u32 s31, s31, 0\n\t" \
    "vv_W69_%=:\n\t" \
    "s_memtime s[56:57]\n\t" \
    "s_waitcnt lgkmcnt(0)\n\t" \
    "s_sub_u32 s69, s70, s56\n\t" \
    "s_cmp_gt_i32 s69, 111\n\t" \
    "s_cbranch_scc1 vv_W69_%=\n\t" \
    "s_max_i32 s69, s69, 0\n\t" \
    "s_lshr_b32 s69, s69, 1\n\t" \
    "s_and_b32 s69, s69, 0x3c\n\t" \
    "s_sub_u32 s30, s30, s69\n\t" \
    "s_subb_u32 s31, s31, 0\n\t" \
    "s_setpc_b64 s[30:31]\n\t" \
    ".p2align 6\n\t" \
    "vv_al72_%=:\n\t" \
    "s_nop 0\n\t" \
    "s_nop 1\n\t" \
    "s_nop 1\n\t" \
    "s_nop 1\n\t" \
    "s_nop 1\n\t" \
    "s_nop 1\n\t" \
    "s_nop 1\n\t" \
    "s_nop 1\n\t" \
    "s_nop 1\n\t" \
    "s_nop 1\n\t" \
    "s_nop 1\n\t" \
    "s_nop 1\n\t" \
    "s_nop 1\n\t" \
    "s_nop 1\n\t" \
    "vv_We71_%=:\n\t" \
    "vv_G73_%=:\n\t" \
    "global_load_dwordx4 v[36:39], v1, s[46:47] sc1\n\t" \
    "s_nop 9\n\t" \
    "global_load_dwordx4 v[40:43], v1, s[46:47] offset:1024 sc1\n\t" \
    "s_nop 9\n\t" \
    "global_load_dwordx4 v[44:47], v2, s[46:47] sc1\n\t" \
    "s_waitcnt vmcnt(2)\n\t" \
    "v_smfmac_f32_16x16x64_bf16 v[72:75], v[36:39], a[32:39], v10\n\t" \
    "ds_write_b128 v0, v[36:39] offset:4096\n\t" \
    "v_smfmac_f32_16x16x64_bf16 v[76:79], v[36:39], a[104:111], v10\n\t" \
    "v_max3_u32 v140, v36, v37, v38\n\t" \
    "s_waitcnt vmcnt(1)\n\t" \
    "ds_write_b128 v0, v[40:43] offset:5120\n\t" \
    "v_smfmac_f32_16x16x64_bf16 v[72:75], v[40:43], a[40:47], v10\n\t" \
    "v_max3_u32 v140, v140, v39, v40\n\t" \
    "v_max3_u32 v140, v140, v41, v42\n\t" \
    "v_max_u32 v140, v140, v43\n\t" \
    "v_cmp_eq_u32 vcc, -1, v140\n\t" \
    "v_smfmac_f32_16x16x64_bf16 v[76:79], v[40:43], a[112:119], v10\n\t" \
    "s_cbranch_vccnz vv_Gbad74_%=\n\t" \
    "vv_Gc76_%=:\n\t" \
    "s_waitcnt lgkmcnt(0)\n\t" \
    "s_barrier\n\t" \
    "ds_read_b128 v[48:51], v0 offset:6144\n\t" \
    "ds_read_b128 v[52:55], v0 offset:7168\n\t" \
    "s_waitcnt vmcnt(0)\n\t" \
    "v_smfmac_f32_16x16x64_bf16 v[72:75], v[44:47], a[64:71], v10\n\t" \
    "ds_read_b128 v[56:59], v0 offset:0\n\t" \
    "ds_read_b128 v[60:63], v0 offset:1024\n\t" \
    "v_max3_u32 v140, v44, v45, v46\n\t" \
    "v_max_u32 v140, v140, v47\n\t" \
    "v_smfmac_f32_16x16x64_bf16 v[76:79], v[44:47], a[136:143], v10\n\t" \
    "ds_read_b128 v[64:67], v0 offset:2048\n\t" \
    "ds_read_b128 v[68:71], v0 offset:3072\n\t" \
    "v_cmp_eq_u32 vcc, -1, v140\n\t" \
    "s_cbranch_vccnz vv_Gbad275_%=\n\t" \
    "vv_Gc277_%=:\n\t" \
    "s_waitcnt lgkmcnt(5)\n\t" \
    "v_smfmac_f32_16x16x64_bf16 v[72:75], v[48:51], a[48:55], v10\n\t" \
    "s_waitcnt lgkmcnt(4)\n\t" \
    "v_smfmac_f32_16x16x64_bf16 v[72:75], v[52:55], a[56:63], v10\n\t" \
    "s_waitcnt lgkmcnt(3)\n\t" \
    "v_smfmac_f32_16x16x64_bf16 v[72:75], v[56:59], a[0:7], v10\n\t" \
    "s_waitcnt lgkmcnt(2)\n\t" \
    "v_smfmac_f32_16x16x64_bf16 v[72:75], v[60:63], a[8:15], v10\n\t" \
    "s_waitcnt lgkmcnt(1)\n\t" \
    "v_smfmac_f32_16x16x64_bf16 v[72:75], v[64:67], a[16:23], v10\n\t" \
    "s_waitcnt lgkmcnt(0)\n\t" \
    "v_smfmac_f32_16x16x64_bf16 v[72:75], v[68:71], a[24:31], v10\n\t" \
    "v_smfmac_f32_16x16x64_bf16 v[76:79], v[48:51], a[120:127], v10\n\t" \
    "s_nop 1\n\t" \
    "s_nop 0\n\t" \
    "v_smfmac_f32_16x16x64_bf16 v[76:79], v[52:55], a[128:135], v10\n\t" \
    "s_nop 1\n\t" \
    "s_nop 0\n\t" \
    "v_smfmac_f32_16x16x64_bf16 v[76:79], v[56:59], a[72:79], v10\n\t" \
    "v_add_f32 v72, v72, v74\n\t" \
    "v_smfmac_f32_16x16x64_bf16 v[76:79], v[60:63], a[80:87], v10\n\t" \
    "v_add_f32 v73, v73, v75\n\t" \
    "v_smfmac_f32_16x16x64_bf16 v[76:79], v[64:67], a[88:95], v10\n\t" \
    "v_cvt_pk_bf16_f32 v80, v72, v73\n\t" \
    "v_smfmac_f32_16x16x64_bf16 v[76:79], v[68:71], a[96:103], v10\n\t" \
    "ds_write_b32 v12, v80 offset:0\n\t" \
    "s_nop 6\n\t" \
    "v_pk_add_f32 v[76:77], v[76:77], v[78:79]\n\t" \
    "v_cvt_pk_bf16_f32 v81, v76, v77\n\t" \
    "ds_write_b32 v12, v81 offset:64\n\t" \
    "s_waitcnt lgkmcnt(0)\n\t" \
    "s_barrier\n\t" \
    "ds_read_b64_tr_b16 v[84:85], v13 offset:0\n\t" \
    "ds_read_b64_tr_b16 v[86:87], v13 offset:128\n\t" \
    "ds_read_b64_tr_b16 v[88:89], v13 offset:1024\n\t" \
    "ds_read_b64_tr_b16 v[90:91], v13 offset:1152\n\t" \
    "s_waitcnt lgkmcnt(2)\n\t" \
    "v_smfmac_f32_16x16x64_bf16 v[92:95], v[84:87], a[144:151], v10\n\t" \
    "s_nop 2\n\t" \
    "s_waitcnt lgkmcnt(0)\n\t" \
    "v_smfmac_f32_16x16x64_bf16 v[92:95], v[88:91], a[152:159], v10\n\t" \
    "s_nop 2\n\t" \
    "v_smfmac_f32_16x16x64_bf16 v[96:99], v[84:87], a[160:167], v10\n\t" \
    "s_nop 2\n\t" \
    "v_smfmac_f32_16x16x64_bf16 v[96:99], v[88:91], a[168:175], v10\n\t" \
    "s_nop 2\n\t" \
    "v_smfmac_f32_16x16x64_bf16 v[100:103], v[84:87], a[176:183], v10\n\t" \
    "s_nop 0\n\t" \
    "v_add_f32 v92, v92, v94\n\t" \
    "v_add_f32 v93, v93, v95\n\t" \
    "v_smfmac_f32_16x16x64_bf16 v[100:103], v[88:91], a[184:191], v10\n\t" \
    "s_nop 1\n\t" \
    "v_cvt_pk_bf16_f32 v128, v92, v93\n\t" \
    "v_smfmac_f32_16x16x64_bf16 v[104:107], v[84:87], a[192:199], v10\n\t" \
    "s_nop 0\n\t" \
    "v_add_f32 v96, v96, v98\n\t" \
    "v_add_f32 v97, v97, v99\n\t" \
    "v_smfmac_f32_16x16x64_bf16 v[104:107], v[88:91], a[200:207], v10\n\t" \
    "s_nop 1\n\t" \
    "v_cvt_pk_bf16_f32 v129, v96, v97\n\t" \
    "v_smfmac_f32_16x16x64_bf16 v[108:111], v[84:87], a[208:215], v10\n\t" \
    "s_nop 0\n\t" \
    "v_add_f32 v100, v100, v102\n\t" \
    "v_add_f32 v101, v101, v103\n\t" \
    "v_smfmac_f32_16x16x64_bf16 v[108:111], v[88:91], a[216:223], v10\n\t" \
    "s_nop 1\n\t" \
    "global_load_dword v140, v20, s[46:47] sc1\n\t" \
    "v_cvt_pk_bf16_f32 v130, v100, v101\n\t" \
    "v_smfmac_f32_16x16x64_bf16 v[112:115], v[84:87], a[224:231], v10\n\t" \
    "s_nop 0\n\t" \
    "v_add_f32 v104, v104, v106\n\t" \
    "v_add_f32 v105, v105, v107\n\t" \
    "v_smfmac_f32_16x16x64_bf16 v[112:115], v[88:91], a[232:239], v10\n\t" \
    "s_nop 0\n\t" \
    "v_cvt_pk_bf16_f32 v131, v104, v105\n\t" \
    "v_permlane32_swap_b32 v128, v130\n\t" \
    "v_smfmac_f32_16x16x64_bf16 v[116:119], v[84:87], a[240:247], v10\n\t" \
    "s_nop 0\n\t" \
    "v_permlane32_swap_b32 v129, v131\n\t" \
    "s_nop 1\n\t" \
    "v_permlane16_swap_b32 v128, v129\n\t" \
    "v_smfmac_f32_16x16x64_bf16 v[116:119], v[88:91], a[248:255], v10\n\t" \
    "v_permlane16_swap_b32 v130, v131\n\t" \
    "global_store_dwordx4 v14, v[128:131], s[38:39] sc1\n\t" \
    "v_add_f32 v108, v108, v110\n\t" \
    "v_smfmac_f32_16x16x64_bf16 v[120:123], v[84:87], v[224:231], v10\n\t" \
    "s_nop 0\n\t" \
    "v_add_f32 v109, v109, v111\n\t" \
    "v_cvt_pk_bf16_f32 v132, v108, v109\n\t" \
    "v_smfmac_f32_16x16x64_bf16 v[120:123], v[88:91], v[232:239], v10\n\t" \
    "s_nop 0\n\t" \
    "v_add_f32 v112, v112, v114\n\t" \
    "v_add_f32 v113, v113, v115\n\t" \
    "v_smfmac_f32_16x16x64_bf16 v[124:127], v[84:87], v[240:247], v10\n\t" \
    "s_nop 0\n\t" \
    "v_cvt_pk_bf16_f32 v133, v112, v113\n\t" \
    "v_add_f32 v116, v116, v118\n\t" \
    "v_smfmac_f32_16x16x64_bf16 v[124:127], v[88:91], v[248:255], v10\n\t" \
    "v_add_f32 v117, v117, v119\n\t" \
    "v_cvt_pk_bf16_f32 v134, v116, v117\n\t" \
    "v_add_f32 v120, v120, v122\n\t" \
    "v_add_f32 v121, v121, v123\n\t" \
    "v_cvt_pk_bf16_f32 v135, v120, v121\n\t" \
    "v_permlane32_swap_b32 v132, v134\n\t" \
    "s_nop 0\n\t" \
    "v_permlane32_swap_b32 v133, v135\n\t" \
    "s_nop 1\n\t" \
    "v_permlane16_swap_b32 v132, v133\n\t" \
    "v_permlane16_swap_b32 v134, v135\n\t" \
    "global_store_dwordx4 v15, v[132:135], s[38:39] sc1\n\t" \
    "v_pk_add_f32 v[124:125], v[124:125], v[126:127]\n\t" \
    "v_cvt_pk_bf16_f32 v136, v124, v125\n\t" \
    "global_store_dword v16, v136, s[38:39] sc1\n\t" \
    "s_nop 1\n\t" \
    "global_store_dwordx4 v17, v[128:131], s[40:41] sc1\n\t" \
    "global_store_dwordx4 v17, v[132:135], s[40:41] offset:1024 sc1\n\t" \
    "global_store_dword v18, v136, s[40:41] sc1\n\t" \
    "s_cmp_eq_u32 s65, 63\n\t" \
    "s_cbranch_scc1 vv_end67_%=\n\t" \
    "s_mov_b32 s71, 3\n\t" \
    "vv_E79_%=:\n\t" \
    "s_memtime s[54:55]\n\t" \
    "s_nop 0\n\t" \
    "s_memrealtime s[4:5]\n\t" \
    "s_nop 0\n\t" \
    "s_memrealtime s[6:7]\n\t" \
    "s_nop 0\n\t" \
    "s_memrealtime s[8:9]\n\t" \
    "s_nop 0\n\t" \
    "s_memrealtime s[10:11]\n\t" \
    "s_nop 0\n\t" \
    "s_memrealtime s[12:13]\n\t" \
    "s_nop 0\n\t" \
    "s_memrealtime s[14:15]\n\t" \
    "s_nop 0\n\t" \
    "s_memrealtime s[16:17]\n\t" \
    "s_nop 0\n\t" \
    "s_memrealtime s[18:19]\n\t" \
    "s_nop 0\n\t" \
    "s_memrealtime s[20:21]\n\t" \
    "s_nop 0\n\t" \
    "s_memrealtime s[22:23]\n\t" \
    "s_nop 0\n\t" \
    "s_memrealtime s[24:25]\n\t" \
    "s_nop 0\n\t" \
    "s_memrealtime s[26:27]\n\t" \
    "s_nop 0\n\t" \
    "s_memrealtime s[28:29]\n\t" \
    "s_nop 0\n\t" \
    "s_memtime s[56:57]\n\t" \
    "s_mov_b32 s70, 1\n\t" \
    "s_sub_u32 s71, s71, 1\n\t" \
    "s_cselect_b32 s69, 0, -1\n\t" \
    "s_waitcnt lgkmcnt(0)\n\t" \
    "s_sub_u32 s54, s56, s54\n\t" \
    "s_and_b32 s54, s54, s69\n\t" \
    "s_cmp_gt_u32 s54, 124\n\t" \
    "s_cbranch_scc1 vv_E79_%=\n\t" \
    "s_cmp_eq_u32 s4, s28\n\t" \
    "s_addc_u32 s70, s70, 0\n\t" \
    "s_cmp_eq_u32 s6, s28\n\t" \
    "s_addc_u32 s70, s70, 0\n\t" \
    "s_cmp_eq_u32 s8, s28\n\t" \
    "s_addc_u32 s70, s70, 0\n\t" \
    "s_cmp_eq_u32 s10, s28\n\t" \
    "s_addc_u32 s70, s70, 0\n\t" \
    "s_cmp_eq_u32 s12, s28\n\t" \
    "s_addc_u32 s70, s70, 0\n\t" \
    "s_cmp_eq_u32 s14, s28\n\t" \
    "s_addc_u32 s70, s70, 0\n\t" \
    "s_cmp_eq_u32 s16, s28\n\t" \
    "s_addc_u32 s70, s70, 0\n\t" \
    "s_cmp_eq_u32 s18, s28\n\t" \
    "s_addc_u32 s70, s70, 0\n\t" \
    "s_cmp_eq_u32 s20, s28\n\t" \
    "s_addc_u32 s70, s70, 0\n\t" \
    "s_cmp_eq_u32 s22, s28\n\t" \
    "s_addc_u32 s70, s70, 0\n\t" \
    "s_cmp_eq_u32 s24, s28\n\t" \
    "s_addc_u32 s70, s70, 0\n\t" \
    "s_cmp_eq_u32 s26, s28\n\t" \
    "s_addc_u32 s70, s70, 0\n\t" \
    "s_mov_b32 s75, s73\n\t" \
    "s_mov_b32 s76, s74\n\t" \
    "s_lshl_b32 s70, s70, 3\n\t" \
    "s_sub_u32 s73, s56, s70\n\t" \
    "s_lshl_b32 s74, s28, 4\n\t" \
    "s_sub_u32 s69, s73, s75\n\t" \
    "s_sub_u32 s70, s74, s76\n\t" \
    "s_sub_u32 s71, s70, 1600\n\t" \
    "s_cmp_lt_u32 s71, 400000\n\t" \
    "s_cbranch_scc0 vv_K81_%=\n\t" \
    "v_cvt_f32_u32 v141, s69\n\t" \
    "v_cvt_f32_u32 v142, s70\n\t" \
    "v_rcp_f32 v143, v141\n\t" \
    "v_rcp_f32 v144, v142\n\t" \
    "v_mul_f32 v143, v143, v142\n\t" \
    "v_mul_f32 v144, v144, v141\n\t" \
    "v_mul_f32 v143, 0x4f800000, v143\n\t" \
    "v_mul_f32 v144, 0x4f000000, v144\n\t" \
    "v_cvt_u32_f32 v143, v143\n\t" \
    "v_cvt_u32_f32 v144, v144\n\t" \
    "v_readfirstlane_b32 s69, v143\n\t" \
    "v_readfirstlane_b32 s70, v144\n\t" \
    "s_nop 1\n\t" \
    "s_sub_u32 s69, s69, s78\n\t" \
    "s_ashr_i32 s69, s69, 2\n\t" \
    "s_add_u32 s78, s78, s69\n\t" \
    "s_sub_u32 s70, s70, s77\n\t" \
    "s_ashr_i32 s70, s70, 2\n\t" \
    "s_add_u32 s77, s77, s70\n\t" \
    "vv_K81_%=:\n\t" \
    "s_waitcnt vmcnt(6)\n\t" \
    "v_cmp_eq_u32 vcc, -1, v140\n\t" \
    "s_cbranch_vccz vv_Hok83_%=\n\t" \
    "vv_H82_%=:\n\t" \
    "global_load_dword v140, v20, s[46:47] sc1\n\t" \
    "s_waitcnt vmcnt(0)\n\t" \
    "v_cmp_eq_u32 vcc, -1, v140\n\t" \
    "s_cbranch_vccz vv_Hc84_%=\n\t" \
    "s_cmp_eq_u32 s68, 0\n\t" \
    "s_cbranch_scc1 vv_F85_%=\n\t" \
    "s_sub_u32 s68, s68, 1\n\t" \
    "s_sleep 5\n\t" \
    "s_branch vv_H82_%=\n\t" \
    "vv_F85_%=:\n\t" \
    "v_mov_b32 v141, 6095104\n\t" \
    "v_mov_b32 v142, 6\n\t" \
    "global_store_dword v141, v142, s[36:37] sc1\n\t" \
    "s_waitcnt vmcnt(0)\n\t" \
    "s_branch vv_Hc84_%=\n\t" \
    "vv_Hc84_%=:\n\t" \
    "vv_Hok83_%=:\n\t" \
    "v_subrev_u32 v140, s80, v140\n\t" \
    "v_and_b32 v20, -5, v20\n\t" \
    "s_nop 0\n\t" \
    "v_max_i32_dpp v140, v140, v140 row_ror:1 row_mask:0xf bank_mask:0xf\n\t" \
    "s_nop 1\n\t" \
    "v_max_i32_dpp v140, v140, v140 row_ror:2 row_mask:0xf bank_mask:0xf\n\t" \
    "s_nop 1\n\t" \
    "v_max_i32_dpp v140, v140, v140 row_ror:4 row_mask:0xf bank_mask:0xf\n\t" \
    "s_nop 1\n\t" \
    "v_max_i32_dpp v140, v140, v140 row_ror:8 row_mask:0xf bank_mask:0xf\n\t" \
    "s_nop 0\n\t" \
    "v_readlane_b32 s69, v140, 0\n\t" \
    "v_readlane_b32 s71, v140, 16\n\t" \
    "v_readlane_b32 s70, v140, 32\n\t" \
    "v_readlane_b32 s81, v140, 48\n\t" \
    "s_max_i32 s69, s69, s71\n\t" \
    "s_nop 0\n\t" \
    "s_max_i32 s71, s70, s81\n\t" \
    "s_max_i32 s69, s69, s71\n\t" \
    "s_add_u32 s81, s69, s80\n\t" \
    "v_mov_b32 v72, 0\n\t" \
    "v_mov_b32 v73, 0\n\t" \
    "v_mov_b32 v74, 0\n\t" \
    "v_mov_b32 v75, 0\n\t" \
    "v_mov_b32 v76, 0\n\t" \
    "v_mov_b32 v77, 0\n\t" \
    "v_mov_b32 v78, 0\n\t" \
    "v_mov_b32 v79, 0\n\t" \
    "v_mov_b32 v92, 0\n\t" \
    "v_mov_b32 v93, 0\n\t" \
    "v_mov_b32 v94, 0\n\t" \
    "v_mov_b32 v95, 0\n\t" \
    "v_mov_b32 v96, 0\n\t" \
    "v_mov_b32 v97, 0\n\t" \
    "v_mov_b32 v98, 0\n\t" \
    "v_mov_b32 v99, 0\n\t" \
    "v_mov_b32 v100, 0\n\t" \
    "v_mov_b32 v101, 0\n\t" \
    "v_mov_b32 v102, 0\n\t" \
    "v_mov_b32 v103, 0\n\t" \
    "v_mov_b32 v104, 0\n\t" \
    "v_mov_b32 v105, 0\n\t" \
    "v_mov_b32 v106, 0\n\t" \
    "v_mov_b32 v107, 0\n\t" \
    "v_mov_b32 v108, 0\n\t" \
    "v_mov_b32 v109, 0\n\t" \
    "v_mov_b32 v110, 0\n\t" \
    "v_mov_b32 v111, 0\n\t" \
    "v_mov_b32 v112, 0\n\t" \
    "v_mov_b32 v113, 0\n\t" \
    "v_mov_b32 v114, 0\n\t" \
    "v_mov_b32 v115, 0\n\t" \
    "v_mov_b32 v116, 0\n\t" \
    "v_mov_b32 v117, 0\n\t" \
    "v_mov_b32 v118, 0\n\t" \
    "v_mov_b32 v119, 0\n\t" \
    "v_mov_b32 v120, 0\n\t" \
    "v_mov_b32 v121, 0\n\t" \
    "v_mov_b32 v122, 0\n\t" \
    "v_mov_b32 v123, 0\n\t" \
    "v_mov_b32 v124, 0\n\t" \
    "v_mov_b32 v125, 0\n\t" \
    "v_mov_b32 v126, 0\n\t" \
    "v_mov_b32 v127, 0\n\t" \
    "s_cmp_lg_u32 s65, 0\n\t" \
    "s_cbranch_scc1 vv_Oc88_%=\n\t" \
    "s_add_u32 s79, s81, 240\n\t" \
    "s_sub_u32 s70, s79, s74\n\t" \
    "s_max_i32 s70, s70, 0\n\t" \
    "s_min_i32 s70, s70, 16000\n\t" \
    "s_lshl_b32 s70, s70, 1\n\t" \
    "s_mul_hi_u32 s70, s70, s77\n\t" \
    "s_add_u32 s70, s70, s73\n\t" \
    "vv_W89_%=:\n\t" \
    "s_memtime s[56:57]\n\t" \
    "s_waitcnt lgkmcnt(0)\n\t" \
    "s_sub_u32 s69, s70, s56\n\t" \
    "s_cmp_gt_i32 s69, 24\n\t" \
    "s_cbranch_scc1 vv_W89_%=\n\t" \
    "global_load_dword v140, v35, s[46:47] sc1\n\t" \
    "s_waitcnt vmcnt(0)\n\t" \
    "v_cmp_eq_u32 vcc, -1, v140\n\t" \
    "s_cbranch_vccz vv_Ook87_%=\n\t" \
    "vv_O86_%=:\n\t" \
    "global_load_dword v140, v35, s[46:47] offset:1536 sc1\n\t" \
    "s_waitcnt vmcnt(0)\n\t" \
    "v_cmp_eq_u32 vcc, -1, v140\n\t" \
    "s_cbranch_vccz vv_Ook87_%=\n\t" \
    "s_cmp_eq_u32 s68, 0\n\t" \
    "s_cbranch_scc1 vv_F90_%=\n\t" \
    "s_sub_u32 s68, s68, 1\n\t" \
    "s_sleep 3\n\t" \
    "s_branch vv_O86_%=\n\t" \
    "vv_F90_%=:\n\t" \
    "v_mov_b32 v141, 6095104\n\t" \
    "v_mov_b32 v142, 17\n\t" \
    "global_store_dword v141, v142, s[36:37] sc1\n\t" \
    "s_waitcnt vmcnt(0)\n\t" \
    "s_branch vv_Ook87_%=\n\t" \
    "vv_Ook87_%=:\n\t" \
    "v_subrev_u32 v140, s80, v140\n\t" \
    "s_nop 1\n\t" \
    "v_max_i32_dpp v140, v140, v140 row_ror:1 row_mask:0xf bank_mask:0xf\n\t" \
    "s_nop 1\n\t" \
    "v_max_i32_dpp v140, v140, v140 row_ror:2 row_mask:0xf bank_mask:0xf\n\t" \
    "s_nop 1\n\t" \
    "v_max_i32_dpp v140, v140, v140 row_ror:4 row_mask:0xf bank_mask:0xf\n\t" \
    "s_nop 1\n\t" \
    "v_max_i32_dpp v140, v140, v140 row_ror:8 row_mask:0xf bank_mask:0xf\n\t" \
    "s_nop 0\n\t" \
    "v_readlane_b32 s69, v140, 0\n\t" \
    "v_readlane_b32 s71, v140, 16\n\t" \
    "v_readlane_b32 s70, v140, 32\n\t" \
    "v_readlane_b32 s81, v140, 48\n\t" \
    "s_max_i32 s69, s69, s71\n\t" \
    "s_nop 0\n\t" \
    "s_max_i32 s71, s70, s81\n\t" \
    "s_max_i32 s69, s69, s71\n\t" \
    "s_add_u32 s81, s69, s80\n\t" \
    "vv_Oc88_%=:\n\t" \
    "s_mul_hi_u32 s69, s85, s78\n\t" \
    "s_add_u32 s79, s81, s69\n\t" \
    "s_add_u32 s79, s79, s87\n\t" \
    "s_cmp_ge_u32 s65, 36\n\t" \
    "s_cselect_b32 s69, 28, 0\n\t" \
    "s_sub_u32 s79, s79, s69\n\t" \
    "s_cmp_lt_u32 s65, 8\n\t" \
    "s_cselect_b32 s69, 64, 0\n\t" \
    "s_add_u32 s79, s79, s69\n\t" \
    "s_cmp_ge_u32 s65, 57\n\t" \
    "s_cselect_b32 s69, 32, 0\n\t" \
    "s_add_u32 s79, s79, s69\n\t" \
    "s_add_u32 s80, s79, s88\n\t" \
    "s_add_u32 s65, s65, 1\n\t" \
    "s_branch vv_go68_%=\n\t" \
    "vv_Gbad74_%=:\n\t" \
    "s_waitcnt vmcnt(0) lgkmcnt(0)\n\t" \
    "s_nop 7\n\t" \
    "v_mov_b32 v72, 0\n\t" \
    "v_mov_b32 v73, 0\n\t" \
    "v_mov_b32 v74, 0\n\t" \
    "v_mov_b32 v75, 0\n\t" \
    "v_mov_b32 v76, 0\n\t" \
    "v_mov_b32 v77, 0\n\t" \
    "v_mov_b32 v78, 0\n\t" \
    "v_mov_b32 v79, 0\n\t" \
    "s_cmp_eq_u32 s68, 0\n\t" \
    "s_cbranch_scc1 vv_F91_%=\n\t" \
    "s_sub_u32 s68, s68, 1\n\t" \
    "s_branch vv_G73_%=\n\t" \
    "vv_F91_%=:\n\t" \
    "v_mov_b32 v140, 6095104\n\t" \
    "v_mov_b32 v141, 1\n\t" \
    "global_store_dword v140, v141, s[36:37] sc1\n\t" \
    "s_waitcnt vmcnt(0)\n\t" \
    "s_branch vv_Gc76_%=\n\t" \
    "vv_Gbad275_%=:\n\t" \
    "global_load_dwordx4 v[44:47], v2, s[46:47] sc1\n\t" \
    "s_waitcnt vmcnt(0)\n\t" \
    "v_max3_u32 v140, v44, v45, v46\n\t" \
    "v_max_u32 v140, v140, v47\n\t" \
    "v_cmp_eq_u32 vcc, -1, v140\n\t" \
    "s_cbranch_vccz vv_Gre278_%=\n\t" \
    "s_cmp_eq_u32 s68, 0\n\t" \
    "s_cbranch_scc1 vv_F92_%=\n\t" \
    "s_sub_u32 s68, s68, 1\n\t" \
    "s_sleep 1\n\t" \
    "s_branch vv_Gbad275_%=\n\t" \
    "vv_F92_%=:\n\t" \
    "v_mov_b32 v140, 6095104\n\t" \
    "v_mov_b32 v141, 2\n\t" \
    "global_store_dword v140, v141, s[36:37] sc1\n\t" \
    "s_waitcnt vmcnt(0)\n\t" \
    "s_branch vv_Gre278_%=\n\t" \
    "vv_Gre278_%=:\n\t" \
    "v_mov_b32 v72, 0\n\t" \
    "v_mov_b32 v73, 0\n\t" \
    "v_mov_b32 v74, 0\n\t" \
    "v_mov_b32 v75, 0\n\t" \
    "v_mov_b32 v76, 0\n\t" \
    "v_mov_b32 v77, 0\n\t" \
    "v_mov_b32 v78, 0\n\t" \
    "v_mov_b32 v79, 0\n\t" \
    "v_smfmac_f32_16x16x64_bf16 v[72:75], v[36:39], a[32:39], v10\n\t" \
    "s_nop 0\n\t" \
    "v_smfmac_f32_16x16x64_bf16 v[76:79], v[36:39], a[104:111], v10\n\t" \
    "v_smfmac_f32_16x16x64_bf16 v[72:75], v[40:43], a[40:47], v10\n\t" \
    "v_smfmac_f32_16x16x64_bf16 v[76:79], v[40:43], a[112:119], v10\n\t" \
    "v_smfmac_f32_16x16x64_bf16 v[72:75], v[44:47], a[64:71], v10\n\t" \
    "v_smfmac_f32_16x16x64_bf16 v[76:79], v[44:47], a[136:143], v10\n\t" \
    "s_branch vv_Gc277_%=\n\t" \
    "vv_end67_%=:\n\t" \
    "s_endpgm\n\t" \
    "vv_loop13_%=:\n\t" \
    "v_mov_b32 v72, 0\n\t" \
    "v_mov_b32 v73, 0\n\t" \
    "v_mov_b32 v74, 0\n\t" \
    "v_mov_b32 v75, 0\n\t" \
    "v_mov_b32 v76, 0\n\t" \
    "v_mov_b32 v77, 0\n\t" \
    "v_mov_b32 v78, 0\n\t" \
    "v_mov_b32 v79, 0\n\t" \
    "v_mov_b32 v92, 0\n\t" \
    "v_mov_b32 v93, 0\n\t" \
    "v_mov_b32 v94, 0\n\t" \
    "v_mov_b32 v95, 0\n\t" \
    "v_mov_b32 v96, 0\n\t" \
    "v_mov_b32 v97, 0\n\t" \
    "v_mov_b32 v98, 0\n\t" \
    "v_mov_b32 v99, 0\n\t" \
    "v_mov_b32 v100, 0\n\t" \
    "v_mov_b32 v101, 0\n\t" \
    "v_mov_b32 v102, 0\n\t" \
    "v_mov_b32 v103, 0\n\t" \
    "v_mov_b32 v104, 0\n\t" \
    "v_mov_b32 v105, 0\n\t" \
    "v_mov_b32 v106, 0\n\t" \
    "v_mov_b32 v107, 0\n\t" \
    "v_mov_b32 v108, 0\n\t" \
    "v_mov_b32 v109, 0\n\t" \
    "v_mov_b32 v110, 0\n\t" \
    "v_mov_b32 v111, 0\n\t" \
    "v_mov_b32 v112, 0\n\t" \
    "v_mov_b32 v113, 0\n\t" \
    "v_mov_b32 v114, 0\n\t" \
    "v_mov_b32 v115, 0\n\t" \
    "v_mov_b32 v116, 0\n\t" \
    "v_mov_b32 v117, 0\n\t" \
    "v_mov_b32 v118, 0\n\t" \
    "v_mov_b32 v119, 0\n\t" \
    "v_mov_b32 v120, 0\n\t" \
    "v_mov_b32 v121, 0\n\t" \
    "v_mov_b32 v122, 0\n\t" \
    "v_mov_b32 v123, 0\n\t" \
    "v_mov_b32 v124, 0\n\t" \
    "v_mov_b32 v125, 0\n\t" \
    "v_mov_b32 v126, 0\n\t" \
    "v_mov_b32 v127, 0\n\t" \
    "vv_go94_%=:\n\t" \
    "s_add_u32 s69, s65, 0\n\t" \
    "s_bitcmp1_b32 s69, 0\n\t" \
    "s_cselect_b32 s70, s63, s62\n\t" \
    "s_bfe_u32 s69, s69, 0x20001\n\t" \
    "s_lshl_b32 s69, s69, 19\n\t" \
    "s_add_u32 s69, s69, s70\n\t" \
    "s_add_u32 s46, s36, s69\n\t" \
    "s_addc_u32 s47, s37, 0\n\t" \
    "s_add_u32 s69, s65, 0\n\t" \
    "s_lshr_b32 s69, s69, 1\n\t" \
    "s_xor_b32 s69, s69, s97\n\t" \
    "s_and_b32 s69, s69, 1\n\t" \
    "s_lshl_b32 s69, s69, 14\n\t" \
    "s_or_b32 s46, s46, s69\n\t" \
    "s_add_u32 s69, s65, 0\n\t" \
    "s_mul_i32 s70, s69, 171\n\t" \
    "s_lshr_b32 s70, s70, 9\n\t" \
    "s_mul_i32 s70, s70, 3\n\t" \
    "s_sub_u32 s69, s69, s70\n\t" \
    "s_mul_i32 s69, s69, 36\n\t" \
    "s_lshl_b32 s69, s69, 14\n\t" \
    "s_add_u32 s69, s69, s64\n\t" \
    "s_add_u32 s69, s69, 4194304\n\t" \
    "s_add_u32 s38, s36, s69\n\t" \
    "s_addc_u32 s39, s37, 0\n\t" \
    "s_mul_i32 s69, s65, 9216\n\t" \
    "s_add_u32 s69, s69, 123731968\n\t" \
    "s_add_u32 s40, s36, s69\n\t" \
    "s_addc_u32 s41, s37, 0\n\t" \
    "s_sub_u32 s70, s79, s74\n\t" \
    "s_max_i32 s70, s70, 0\n\t" \
    "s_min_i32 s70, s70, 16000\n\t" \
    "s_lshl_b32 s70, s70, 1\n\t" \
    "s_mul_hi_u32 s70, s70, s77\n\t" \
    "s_add_u32 s70, s70, s73\n\t" \
    "s_sub_u32 s70, s70, 100\n\t" \
    "s_getpc_b64 s[30:31]\n\t" \
    "vv_Wp96_%=:\n\t" \
    "s_add_u32 s30, s30, vv_We97_%=-vv_Wp96_%=\n\t" \
    "s_addc_u32 s31, s31, 0\n\t" \
    "vv_W95_%=:\n\t" \
    "s_memtime s[56:57]\n\t" \
    "s_waitcnt lgkmcnt(0)\n\t" \
    "s_sub_u32 s69, s70, s56\n\t" \
    "s_cmp_gt_i32 s69, 111\n\t" \
    "s_cbranch_scc1 vv_W95_%=\n\t" \
    "s_max_i32 s69, s69, 0\n\t" \
    "s_lshr_b32 s69, s69, 1\n\t" \
    "s_and_b32 s69, s69, 0x3c\n\t" \
    "s_sub_u32 s30, s30, s69\n\t" \
    "s_subb_u32 s31, s31, 0\n\t" \
    "s_setpc_b64 s[30:31]\n\t" \
    ".p2align 6\n\t" \
    "vv_al98_%=:\n\t" \
    "s_nop 0\n\t" \
    "s_nop 1\n\t" \
    "s_nop 1\n\t" \
    "s_nop 1\n\t" \
    "s_nop 1\n\t" \
    "s_nop 1\n\t" \
    "s_nop 1\n\t" \
    "s_nop 1\n\t" \
    "s_nop 1\n\t" \
    "s_nop 1\n\t" \
    "s_nop 1\n\t" \
    "s_nop 1\n\t" \
    "s_nop 1\n\t" \
    "s_nop 1\n\t" \
    "vv_We97_%=:\n\t" \
    "vv_G99_%=:\n\t" \
    "global_load_dwordx4 v[36:39], v1, s[46:47] sc1\n\t" \
    "s_nop 9\n\t" \
    "global_load_dwordx4 v[40:43], v1, s[46:47] offset:1024 sc1\n\t" \
    "s_nop 9\n\t" \
    "global_load_dwordx4 v[44:47], v2, s[46:47] sc1\n\t" \
    "s_waitcnt vmcnt(2)\n\t" \
    "v_smfmac_f32_16x16x64_bf16 v[72:75], v[36:39], a[48:55], v10\n\t" \
    "ds_write_b128 v0, v[36:39] offset:6144\n\t" \
    "v_smfmac_f32_16x16x64_bf16 v[76:79], v[36:39], a[120:127], v10\n\t" \
    "v_max3_u32 v140, v36, v37, v38\n\t" \
    "s_waitcnt vmcnt(1)\n\t" \
    "ds_write_b128 v0, v[40:43] offset:7168\n\t" \
    "v_smfmac_f32_16x16x64_bf16 v[72:75], v[40:43], a[56:63], v10\n\t" \
    "v_max3_u32 v140, v140, v39, v40\n\t" \
    "v_max3_u32 v140, v140, v41, v42\n\t" \
    "v_max_u32 v140, v140, v43\n\t" \
    "v_cmp_eq_u32 vcc, -1, v140\n\t" \
    "v_smfmac_f32_16x16x64_bf16 v[76:79], v[40:43], a[128:135], v10\n\t" \
    "s_cbranch_vccnz vv_Gbad100_%=\n\t" \
    "vv_Gc102_%=:\n\t" \
    "s_waitcnt lgkmcnt(0)\n\t" \
    "s_barrier\n\t" \
    "ds_read_b128 v[48:51], v0 offset:0\n\t" \
    "ds_read_b128 v[52:55], v0 offset:1024\n\t" \
    "s_waitcnt vmcnt(0)\n\t" \
    "v_smfmac_f32_16x16x64_bf16 v[72:75], v[44:47], a[64:71], v10\n\t" \
    "ds_read_b128 v[56:59], v0 offset:2048\n\t" \
    "ds_read_b128 v[60:63], v0 offset:3072\n\t" \
    "v_max3_u32 v140, v44, v45, v46\n\t" \
    "v_max_u32 v140, v140, v47\n\t" \
    "v_smfmac_f32_16x16x64_bf16 v[76:79], v[44:47], a[136:143], v10\n\t" \
    "ds_read_b128 v[64:67], v0 offset:4096\n\t" \
    "ds_read_b128 v[68:71], v0 offset:5120\n\t" \
    "v_cmp_eq_u32 vcc, -1, v140\n\t" \
    "s_cbranch_vccnz vv_Gbad2101_%=\n\t" \
    "vv_Gc2103_%=:\n\t" \
    "s_waitcnt lgkmcnt(5)\n\t" \
    "v_smfmac_f32_16x16x64_bf16 v[72:75], v[48:51], a[0:7], v10\n\t" \
    "s_waitcnt lgkmcnt(4)\n\t" \
    "v_smfmac_f32_16x16x64_bf16 v[72:75], v[52:55], a[8:15], v10\n\t" \
    "s_waitcnt lgkmcnt(3)\n\t" \
    "v_smfmac_f32_16x16x64_bf16 v[72:75], v[56:59], a[16:23], v10\n\t" \
    "s_waitcnt lgkmcnt(2)\n\t" \
    "v_smfmac_f32_16x16x64_bf16 v[72:75], v[60:63], a[24:31], v10\n\t" \
    "s_waitcnt lgkmcnt(1)\n\t" \
    "v_smfmac_f32_16x16x64_bf16 v[72:75], v[64:67], a[32:39], v10\n\t" \
    "s_waitcnt lgkmcnt(0)\n\t" \
    "v_smfmac_f32_16x16x64_bf16 v[72:75], v[68:71], a[40:47], v10\n\t" \
    "v_smfmac_f32_16x16x64_bf16 v[76:79], v[48:51], a[72:79], v10\n\t" \
    "s_nop 1\n\t" \
    "s_nop 0\n\t" \
    "v_smfmac_f32_16x16x64_bf16 v[76:79], v[52:55], a[80:87], v10\n\t" \
    "s_nop 1\n\t" \
    "s_nop 0\n\t" \
    "v_smfmac_f32_16x16x64_bf16 v[76:79], v[56:59], a[88:95], v10\n\t" \
    "v_add_f32 v72, v72, v74\n\t" \
    "v_smfmac_f32_16x16x64_bf16 v[76:79], v[60:63], a[96:103], v10\n\t" \
    "v_add_f32 v73, v73, v75\n\t" \
    "v_smfmac_f32_16x16x64_bf16 v[76:79], v[64:67], a[104:111], v10\n\t" \
    "v_cvt_pk_bf16_f32 v80, v72, v73\n\t" \
    "v_smfmac_f32_16x16x64_bf16 v[76:79], v[68:71], a[112:119], v10\n\t" \
    "ds_write_b32 v12, v80 offset:0\n\t" \
    "s_nop 6\n\t" \
    "v_pk_add_f32 v[76:77], v[76:77], v[78:79]\n\t" \
    "v_cvt_pk_bf16_f32 v81, v76, v77\n\t" \
    "ds_write_b32 v12, v81 offset:64\n\t" \
    "s_waitcnt lgkmcnt(0)\n\t" \
    "s_barrier\n\t" \
    "ds_read_b64_tr_b16 v[84:85], v13 offset:0\n\t" \
    "ds_read_b64_tr_b16 v[86:87], v13 offset:128\n\t" \
    "ds_read_b64_tr_b16 v[88:89], v13 offset:1024\n\t" \
    "ds_read_b64_tr_b16 v[90:91], v13 offset:1152\n\t" \
    "s_waitcnt lgkmcnt(2)\n\t" \
    "v_smfmac_f32_16x16x64_bf16 v[92:95], v[84:87], a[144:151], v10\n\t" \
    "s_nop 2\n\t" \
    "s_waitcnt lgkmcnt(0)\n\t" \
    "v_smfmac_f32_16x16x64_bf16 v[92:95], v[88:91], a[152:159], v10\n\t" \
    "s_nop 2\n\t" \
    "v_smfmac_f32_16x16x64_bf16 v[96:99], v[84:87], a[160:167], v10\n\t" \
    "s_nop 2\n\t" \
    "v_smfmac_f32_16x16x64_bf16 v[96:99], v[88:91], a[168:175], v10\n\t" \
    "s_nop 2\n\t" \
    "v_smfmac_f32_16x16x64_bf16 v[100:103], v[84:87], a[176:183], v10\n\t" \
    "s_nop 0\n\t" \
    "v_add_f32 v92, v92, v94\n\t" \
    "v_add_f32 v93, v93, v95\n\t" \
    "v_smfmac_f32_16x16x64_bf16 v[100:103], v[88:91], a[184:191], v10\n\t" \
    "s_nop 1\n\t" \
    "v_cvt_pk_bf16_f32 v128, v92, v93\n\t" \
    "v_smfmac_f32_16x16x64_bf16 v[104:107], v[84:87], a[192:199], v10\n\t" \
    "s_nop 0\n\t" \
    "v_add_f32 v96, v96, v98\n\t" \
    "v_add_f32 v97, v97, v99\n\t" \
    "v_smfmac_f32_16x16x64_bf16 v[104:107], v[88:91], a[200:207], v10\n\t" \
    "s_nop 1\n\t" \
    "v_cvt_pk_bf16_f32 v129, v96, v97\n\t" \
    "v_smfmac_f32_16x16x64_bf16 v[108:111], v[84:87], a[208:215], v10\n\t" \
    "s_nop 0\n\t" \
    "v_add_f32 v100, v100, v102\n\t" \
    "v_add_f32 v101, v101, v103\n\t" \
    "v_smfmac_f32_16x16x64_bf16 v[108:111], v[88:91], a[216:223], v10\n\t" \
    "s_nop 1\n\t" \
    "global_load_dword v140, v20, s[46:47] sc1\n\t" \
    "v_cvt_pk_bf16_f32 v130, v100, v101\n\t" \
    "v_smfmac_f32_16x16x64_bf16 v[112:115], v[84:87], a[224:231], v10\n\t" \
    "s_nop 0\n\t" \
    "v_add_f32 v104, v104, v106\n\t" \
    "v_add_f32 v105, v105, v107\n\t" \
    "v_smfmac_f32_16x16x64_bf16 v[112:115], v[88:91], a[232:239], v10\n\t" \
    "s_nop 0\n\t" \
    "v_cvt_pk_bf16_f32 v131, v104, v105\n\t" \
    "v_permlane32_swap_b32 v128, v130\n\t" \
    "v_smfmac_f32_16x16x64_bf16 v[116:119], v[84:87], a[240:247], v10\n\t" \
    "s_nop 0\n\t" \
    "v_permlane32_swap_b32 v129, v131\n\t" \
    "s_nop 1\n\t" \
    "v_permlane16_swap_b32 v128, v129\n\t" \
    "v_smfmac_f32_16x16x64_bf16 v[116:119], v[88:91], a[248:255], v10\n\t" \
    "v_permlane16_swap_b32 v130, v131\n\t" \
    "global_store_dwordx4 v14, v[128:131], s[38:39] sc1\n\t" \
    "v_add_f32 v108, v108, v110\n\t" \
    "v_smfmac_f32_16x16x64_bf16 v[120:123], v[84:87], v[224:231], v10\n\t" \
    "s_nop 0\n\t" \
    "v_add_f32 v109, v109, v111\n\t" \
    "v_cvt_pk_bf16_f32 v132, v108, v109\n\t" \
    "v_smfmac_f32_16x16x64_bf16 v[120:123], v[88:91], v[232:239], v10\n\t" \
    "s_nop 0\n\t" \
    "v_add_f32 v112, v112, v114\n\t" \
    "v_add_f32 v113, v113, v115\n\t" \
    "v_smfmac_f32_16x16x64_bf16 v[124:127], v[84:87], v[240:247], v10\n\t" \
    "s_nop 0\n\t" \
    "v_cvt_pk_bf16_f32 v133, v112, v113\n\t" \
    "v_add_f32 v116, v116, v118\n\t" \
    "v_smfmac_f32_16x16x64_bf16 v[124:127], v[88:91], v[248:255], v10\n\t" \
    "v_add_f32 v117, v117, v119\n\t" \
    "v_cvt_pk_bf16_f32 v134, v116, v117\n\t" \
    "v_add_f32 v120, v120, v122\n\t" \
    "v_add_f32 v121, v121, v123\n\t" \
    "v_cvt_pk_bf16_f32 v135, v120, v121\n\t" \
    "v_permlane32_swap_b32 v132, v134\n\t" \
    "s_nop 0\n\t" \
    "v_permlane32_swap_b32 v133, v135\n\t" \
    "s_nop 1\n\t" \
    "v_permlane16_swap_b32 v132, v133\n\t" \
    "v_permlane16_swap_b32 v134, v135\n\t" \
    "global_store_dwordx4 v15, v[132:135], s[38:39] sc1\n\t" \
    "v_pk_add_f32 v[124:125], v[124:125], v[126:127]\n\t" \
    "v_cvt_pk_bf16_f32 v136, v124, v125\n\t" \
    "global_store_dword v16, v136, s[38:39] sc1\n\t" \
    "s_nop 1\n\t" \
    "global_store_dwordx4 v17, v[128:131], s[40:41] sc1\n\t" \
    "global_store_dwordx4 v17, v[132:135], s[40:41] offset:1024 sc1\n\t" \
    "global_store_dword v18, v136, s[40:41] sc1\n\t" \
    "s_cmp_eq_u32 s65, 63\n\t" \
    "s_cbranch_scc1 vv_end93_%=\n\t" \
    "s_mov_b32 s71, 3\n\t" \
    "vv_E105_%=:\n\t" \
    "s_memtime s[54:55]\n\t" \
    "s_nop 0\n\t" \
    "s_memrealtime s[4:5]\n\t" \
    "s_nop 0\n\t" \
    "s_memrealtime s[6:7]\n\t" \
    "s_nop 0\n\t" \
    "s_memrealtime s[8:9]\n\t" \
    "s_nop 0\n\t" \
    "s_memrealtime s[10:11]\n\t" \
    "s_nop 0\n\t" \
    "s_memrealtime s[12:13]\n\t" \
    "s_nop 0\n\t" \
    "s_memrealtime s[14:15]\n\t" \
    "s_nop 0\n\t" \
    "s_memrealtime s[16:17]\n\t" \
    "s_nop 0\n\t" \
    "s_memrealtime s[18:19]\n\t" \
    "s_nop 0\n\t" \
    "s_memrealtime s[20:21]\n\t" \
    "s_nop 0\n\t" \
    "s_memrealtime s[22:23]\n\t" \
    "s_nop 0\n\t" \
    "s_memrealtime s[24:25]\n\t" \
    "s_nop 0\n\t" \
    "s_memrealtime s[26:27]\n\t" \
    "s_nop 0\n\t" \
    "s_memrealtime s[28:29]\n\t" \
    "s_nop 0\n\t" \
    "s_memtime s[56:57]\n\t" \
    "s_mov_b32 s70, 1\n\t" \
    "s_sub_u32 s71, s71, 1\n\t" \
    "s_cselect_b32 s69, 0, -1\n\t" \
    "s_waitcnt lgkmcnt(0)\n\t" \
    "s_sub_u32 s54, s56, s54\n\t" \
    "s_and_b32 s54, s54, s69\n\t" \
    "s_cmp_gt_u32 s54, 124\n\t" \
    "s_cbranch_scc1 vv_E105_%=\n\t" \
    "s_cmp_eq_u32 s4, s28\n\t" \
    "s_addc_u32 s70, s70, 0\n\t" \
    "s_cmp_eq_u32 s6, s28\n\t" \
    "s_addc_u32 s70, s70, 0\n\t" \
    "s_cmp_eq_u32 s8, s28\n\t" \
    "s_addc_u32 s70, s70, 0\n\t" \
    "s_cmp_eq_u32 s10, s28\n\t" \
    "s_addc_u32 s70, s70, 0\n\t" \
    "s_cmp_eq_u32 s12, s28\n\t" \
    "s_addc_u32 s70, s70, 0\n\t" \
    "s_cmp_eq_u32 s14, s28\n\t" \
    "s_addc_u32 s70, s70, 0\n\t" \
    "s_cmp_eq_u32 s16, s28\n\t" \
    "s_addc_u32 s70, s70, 0\n\t" \
    "s_cmp_eq_u32 s18, s28\n\t" \
    "s_addc_u32 s70, s70, 0\n\t" \
    "s_cmp_eq_u32 s20, s28\n\t" \
    "s_addc_u32 s70, s70, 0\n\t" \
    "s_cmp_eq_u32 s22, s28\n\t" \
    "s_addc_u32 s70, s70, 0\n\t" \
    "s_cmp_eq_u32 s24, s28\n\t" \
    "s_addc_u32 s70, s70, 0\n\t" \
    "s_cmp_eq_u32 s26, s28\n\t" \
    "s_addc_u32 s70, s70, 0\n\t" \
    "s_mov_b32 s75, s73\n\t" \
    "s_mov_b32 s76, s74\n\t" \
    "s_lshl_b32 s70, s70, 3\n\t" \
    "s_sub_u32 s73, s56, s70\n\t" \
    "s_lshl_b32 s74, s28, 4\n\t" \
    "s_sub_u32 s69, s73, s75\n\t" \
    "s_sub_u32 s70, s74, s76\n\t" \
    "s_sub_u32 s71, s70, 1600\n\t" \
    "s_cmp_lt_u32 s71, 400000\n\t" \
    "s_cbranch_scc0 vv_K107_%=\n\t" \
    "v_cvt_f32_u32 v141, s69\n\t" \
    "v_cvt_f32_u32 v142, s70\n\t" \
    "v_rcp_f32 v143, v141\n\t" \
    "v_rcp_f32 v144, v142\n\t" \
    "v_mul_f32 v143, v143, v142\n\t" \
    "v_mul_f32 v144, v144, v141\n\t" \
    "v_mul_f32 v143, 0x4f800000, v143\n\t" \
    "v_mul_f32 v144, 0x4f000000, v144\n\t" \
    "v_cvt_u32_f32 v143, v143\n\t" \
    "v_cvt_u32_f32 v144, v144\n\t" \
    "v_readfirstlane_b32 s69, v143\n\t" \
    "v_readfirstlane_b32 s70, v144\n\t" \
    "s_nop 1\n\t" \
    "s_sub_u32 s69, s69, s78\n\t" \
    "s_ashr_i32 s69, s69, 2\n\t" \
    "s_add_u32 s78, s78, s69\n\t" \
    "s_sub_u32 s70, s70, s77\n\t" \
    "s_ashr_i32 s70, s70, 2\n\t" \
    "s_add_u32 s77, s77, s70\n\t" \
    "vv_K107_%=:\n\t" \
    "s_waitcnt vmcnt(6)\n\t" \
    "v_cmp_eq_u32 vcc, -1, v140\n\t" \
    "s_cbranch_vccz vv_Hok109_%=\n\t" \
    "vv_H108_%=:\n\t" \
    "global_load_dword v140, v20, s[46:47] sc1\n\t" \
    "s_waitcnt vmcnt(0)\n\t" \
    "v_cmp_eq_u32 vcc, -1, v140\n\t" \
    "s_cbranch_vccz vv_Hc110_%=\n\t" \
    "s_cmp_eq_u32 s68, 0\n\t" \
    "s_cbranch_scc1 vv_F111_%=\n\t" \
    "s_sub_u32 s68, s68, 1\n\t" \
    "s_sleep 5\n\t" \
    "s_branch vv_H108_%=\n\t" \
    "vv_F111_%=:\n\t" \
    "v_mov_b32 v141, 6095104\n\t" \
    "v_mov_b32 v142, 6\n\t" \
    "global_store_dword v141, v142, s[36:37] sc1\n\t" \
    "s_waitcnt vmcnt(0)\n\t" \
    "s_branch vv_Hc110_%=\n\t" \
    "vv_Hc110_%=:\n\t" \
    "vv_Hok109_%=:\n\t" \
    "v_subrev_u32 v140, s80, v140\n\t" \
    "v_and_b32 v20, -5, v20\n\t" \
    "s_nop 0\n\t" \
    "v_max_i32_dpp v140, v140, v140 row_ror:1 row_mask:0xf bank_mask:0xf\n\t" \
    "s_nop 1\n\t" \
    "v_max_i32_dpp v140, v140, v140 row_ror:2 row_mask:0xf bank_mask:0xf\n\t" \
    "s_nop 1\n\t" \
    "v_max_i32_dpp v140, v140, v140 row_ror:4 row_mask:0xf bank_mask:0xf\n\t" \
    "s_nop 1\n\t" \
    "v_max_i32_dpp v140, v140, v140 row_ror:8 row_mask:0xf bank_mask:0xf\n\t" \
    "s_nop 0\n\t" \
    "v_readlane_b32 s69, v140, 0\n\t" \
    "v_readlane_b32 s71, v140, 16\n\t" \
    "v_readlane_b32 s70, v140, 32\n\t" \
    "v_readlane_b32 s81, v140, 48\n\t" \
    "s_max_i32 s69, s69, s71\n\t" \
    "s_nop 0\n\t" \
    "s_max_i32 s71, s70, s81\n\t" \
    "s_max_i32 s69, s69, s71\n\t" \
    "s_add_u32 s81, s69, s80\n\t" \
    "v_mov_b32 v72, 0\n\t" \
    "v_mov_b32 v73, 0\n\t" \
    "v_mov_b32 v74, 0\n\t" \
    "v_mov_b32 v75, 0\n\t" \
    "v_mov_b32 v76, 0\n\t" \
    "v_mov_b32 v77, 0\n\t" \
    "v_mov_b32 v78, 0\n\t" \
    "v_mov_b32 v79, 0\n\t" \
    "v_mov_b32 v92, 0\n\t" \
    "v_mov_b32 v93, 0\n\t" \
    "v_mov_b32 v94, 0\n\t" \
    "v_mov_b32 v95, 0\n\t" \
    "v_mov_b32 v96, 0\n\t" \
    "v_mov_b32 v97, 0\n\t" \
    "v_mov_b32 v98, 0\n\t" \
    "v_mov_b32 v99, 0\n\t" \
    "v_mov_b32 v100, 0\n\t" \
    "v_mov_b32 v101, 0\n\t" \
    "v_mov_b32 v102, 0\n\t" \
    "v_mov_b32 v103, 0\n\t" \
    "v_mov_b32 v104, 0\n\t" \
    "v_mov_b32 v105, 0\n\t" \
    "v_mov_b32 v106, 0\n\t" \
    "v_mov_b32 v107, 0\n\t" \
    "v_mov_b32 v108, 0\n\t" \
    "v_mov_b32 v109, 0\n\t" \
    "v_mov_b32 v110, 0\n\t" \
    "v_mov_b32 v111, 0\n\t" \
    "v_mov_b32 v112, 0\n\t" \
    "v_mov_b32 v113, 0\n\t" \
    "v_mov_b32 v114, 0\n\t" \
    "v_mov_b32 v115, 0\n\t" \
    "v_mov_b32 v116, 0\n\t" \
    "v_mov_b32 v117, 0\n\t" \
    "v_mov_b32 v118, 0\n\t" \
    "v_mov_b32 v119, 0\n\t" \
    "v_mov_b32 v120, 0\n\t" \
    "v_mov_b32 v121, 0\n\t" \
    "v_mov_b32 v122, 0\n\t" \
    "v_mov_b32 v123, 0\n\t" \
    "v_mov_b32 v124, 0\n\t" \
    "v_mov_b32 v125, 0\n\t" \
    "v_mov_b32 v126, 0\n\t" \
    "v_mov_b32 v127, 0\n\t" \
    "s_cmp_lg_u32 s65, 0\n\t" \
    "s_cbranch_scc1 vv_Oc114_%=\n\t" \
    "s_add_u32 s79, s81, 240\n\t" \
    "s_sub_u32 s70, s79, s74\n\t" \
    "s_max_i32 s70, s70, 0\n\t" \
    "s_min_i32 s70, s70, 16000\n\t" \
    "s_lshl_b32 s70, s70, 1\n\t" \
    "s_mul_hi_u32 s70, s70, s77\n\t" \
    "s_add_u32 s70, s70, s73\n\t" \
    "vv_W115_%=:\n\t" \
    "s_memtime s[56:57]\n\t" \
    "s_waitcnt lgkmcnt(0)\n\t" \
    "s_sub_u32 s69, s70, s56\n\t" \
    "s_cmp_gt_i32 s69, 24\n\t" \
    "s_cbranch_scc1 vv_W115_%=\n\t" \
    "global_load_dword v140, v35, s[46:47] sc1\n\t" \
    "s_waitcnt vmcnt(0)\n\t" \
    "v_cmp_eq_u32 vcc, -1, v140\n\t" \
    "s_cbranch_vccz vv_Ook113_%=\n\t" \
    "vv_O112_%=:\n\t" \
    "global_load_dword v140, v35, s[46:47] offset:1536 sc1\n\t" \
    "s_waitcnt vmcnt(0)\n\t" \
    "v_cmp_eq_u32 vcc, -1, v140\n\t" \
    "s_cbranch_vccz vv_Ook113_%=\n\t" \
    "s_cmp_eq_u32 s68, 0\n\t" \
    "s_cbranch_scc1 vv_F116_%=\n\t" \
    "s_sub_u32 s68, s68, 1\n\t" \
    "s_sleep 3\n\t" \
    "s_branch vv_O112_%=\n\t" \
    "vv_F116_%=:\n\t" \
    "v_mov_b32 v141, 6095104\n\t" \
    "v_mov_b32 v142, 17\n\t" \
    "global_store_dword v141, v142, s[36:37] sc1\n\t" \
    "s_waitcnt vmcnt(0)\n\t" \
    "s_branch vv_Ook113_%=\n\t" \
    "vv_Ook113_%=:\n\t" \
    "v_subrev_u32 v140, s80, v140\n\t" \
    "s_nop 1\n\t" \
    "v_max_i32_dpp v140, v140, v140 row_ror:1 row_mask:0xf bank_mask:0xf\n\t" \
    "s_nop 1\n\t" \
    "v_max_i32_dpp v140, v140, v140 row_ror:2 row_mask:0xf bank_mask:0xf\n\t" \
    "s_nop 1\n\t" \
    "v_max_i32_dpp v140, v140, v140 row_ror:4 row_mask:0xf bank_mask:0xf\n\t" \
    "s_nop 1\n\t" \
    "v_max_i32_dpp v140, v140, v140 row_ror:8 row_mask:0xf bank_mask:0xf\n\t" \
    "s_nop 0\n\t" \
    "v_readlane_b32 s69, v140, 0\n\t" \
    "v_readlane_b32 s71, v140, 16\n\t" \
    "v_readlane_b32 s70, v140, 32\n\t" \
    "v_readlane_b32 s81, v140, 48\n\t" \
    "s_max_i32 s69, s69, s71\n\t" \
    "s_nop 0\n\t" \
    "s_max_i32 s71, s70, s81\n\t" \
    "s_max_i32 s69, s69, s71\n\t" \
    "s_add_u32 s81, s69, s80\n\t" \
    "vv_Oc114_%=:\n\t" \
    "s_mul_hi_u32 s69, s85, s78\n\t" \
    "s_add_u32 s79, s81, s69\n\t" \
    "s_add_u32 s79, s79, s87\n\t" \
    "s_cmp_ge_u32 s65, 36\n\t" \
    "s_cselect_b32 s69, 28, 0\n\t" \
    "s_sub_u32 s79, s79, s69\n\t" \
    "s_cmp_lt_u32 s65, 8\n\t" \
    "s_cselect_b32 s69, 64, 0\n\t" \
    "s_add_u32 s79, s79, s69\n\t" \
    "s_cmp_ge_u32 s65, 57\n\t" \
    "s_cselect_b32 s69, 32, 0\n\t" \
    "s_add_u32 s79, s79, s69\n\t" \
    "s_add_u32 s80, s79, s88\n\t" \
    "s_add_u32 s65, s65, 1\n\t" \
    "s_branch vv_go94_%=\n\t" \
    "vv_Gbad100_%=:\n\t" \
    "s_waitcnt vmcnt(0) lgkmcnt(0)\n\t" \
    "s_nop 7\n\t" \
    "v_mov_b32 v72, 0\n\t" \
    "v_mov_b32 v73, 0\n\t" \
    "v_mov_b32 v74, 0\n\t" \
    "v_mov_b32 v75, 0\n\t" \
    "v_mov_b32 v76, 0\n\t" \
    "v_mov_b32 v77, 0\n\t" \
    "v_mov_b32 v78, 0\n\t" \
    "v_mov_b32 v79, 0\n\t" \
    "s_cmp_eq_u32 s68, 0\n\t" \
    "s_cbranch_scc1 vv_F117_%=\n\t" \
    "s_sub_u32 s68, s68, 1\n\t" \
    "s_branch vv_G99_%=\n\t" \
    "vv_F117_%=:\n\t" \
    "v_mov_b32 v140, 6095104\n\t" \
    "v_mov_b32 v141, 1\n\t" \
    "global_store_dword v140, v141, s[36:37] sc1\n\t" \
    "s_waitcnt vmcnt(0)\n\t" \
    "s_branch vv_Gc102_%=\n\t" \
    "vv_Gbad2101_%=:\n\t" \
    "global_load_dwordx4 v[44:47], v2, s[46:47] sc1\n\t" \
    "s_waitcnt vmcnt(0)\n\t" \
    "v_max3_u32 v140, v44, v45, v46\n\t" \
    "v_max_u32 v140, v140, v47\n\t" \
    "v_cmp_eq_u32 vcc, -1, v140\n\t" \
    "s_cbranch_vccz vv_Gre2104_%=\n\t" \
    "s_cmp_eq_u32 s68, 0\n\t" \
    "s_cbranch_scc1 vv_F118_%=\n\t" \
    "s_sub_u32 s68, s68, 1\n\t" \
    "s_sleep 1\n\t" \
    "s_branch vv_Gbad2101_%=\n\t" \
    "vv_F118_%=:\n\t" \
    "v_mov_b32 v140, 6095104\n\t" \
    "v_mov_b32 v141, 2\n\t" \
    "global_store_dword v140, v141, s[36:37] sc1\n\t" \
    "s_waitcnt vmcnt(0)\n\t" \
    "s_branch vv_Gre2104_%=\n\t" \
    "vv_Gre2104_%=:\n\t" \
    "v_mov_b32 v72, 0\n\t" \
    "v_mov_b32 v73, 0\n\t" \
    "v_mov_b32 v74, 0\n\t" \
    "v_mov_b32 v75, 0\n\t" \
    "v_mov_b32 v76, 0\n\t" \
    "v_mov_b32 v77, 0\n\t" \
    "v_mov_b32 v78, 0\n\t" \
    "v_mov_b32 v79, 0\n\t" \
    "v_smfmac_f32_16x16x64_bf16 v[72:75], v[36:39], a[48:55], v10\n\t" \
    "s_nop 0\n\t" \
    "v_smfmac_f32_16x16x64_bf16 v[76:79], v[36:39], a[120:127], v10\n\t" \
    "v_smfmac_f32_16x16x64_bf16 v[72:75], v[40:43], a[56:63], v10\n\t" \
    "v_smfmac_f32_16x16x64_bf16 v[76:79], v[40:43], a[128:135], v10\n\t" \
    "v_smfmac_f32_16x16x64_bf16 v[72:75], v[44:47], a[64:71], v10\n\t" \
    "v_smfmac_f32_16x16x64_bf16 v[76:79], v[44:47], a[136:143], v10\n\t" \
    "s_branch vv_Gc2103_%=\n\t" \
    "vv_end93_%=:\n\t" \
    "s_endpgm\n\t" \
    "gx_begin_%=:\n\t" \
    "v_mbcnt_lo_u32_b32 v223, -1, 0\n\t" \
    "v_mbcnt_hi_u32_b32 v223, -1, v223\n\t" \
    "s_mov_b32 s68, 262144\n\t" \
    "s_mov_b32 s72, 0\n\t" \
    "s_mov_b32 s83, -1\n\t" \
    "s_mov_b32 s65, 0\n\t" \
    "v_and_b32 v180, 3, v223\n\t" \
    "v_lshlrev_b32 v202, 3, v180\n\t" \
    "v_lshrrev_b32 v181, 5, v223\n\t" \
    "v_lshl_add_u32 v202, v181, 12, v202\n\t" \
    "v_bfe_u32 v182, v223, 2, 2\n\t" \
    "s_mov_b32 s70, 288\n\t" \
    "v_mad_u32_u24 v202, v182, s70, v202\n\t" \
    "v_bfe_u32 v183, v223, 4, 1\n\t" \
    "s_mov_b32 s70, 2176\n\t" \
    "v_mad_u32_u24 v202, v183, s70, v202\n\t" \
    "s_lshl_b32 s69, s66, 6\n\t" \
    "v_xor_b32 v202, s69, v202\n\t" \
    "v_xor_b32 v203, 32, v202\n\t" \
    "v_lshlrev_b32 v3, 4, v180\n\t" \
    "v_lshlrev_b32 v2, 5, v181\n\t" \
    "v_xor_b32 v3, v3, v2\n\t" \
    "s_bfe_u32 s71, s66, 0x10000\n\t" \
    "s_mul_i32 s69, s71, 576\n\t" \
    "s_bfe_u32 s71, s66, 0x10001\n\t" \
    "s_mul_i32 s71, s71, 18432\n\t" \
    "s_add_u32 s69, s69, s71\n\t" \
    "v_lshrrev_b32 v184, 2, v223\n\t" \
    "v_mov_b32 v1, s69\n\t" \
    "s_mov_b32 s70, 1152\n\t" \
    "v_mad_u32_u24 v1, v184, s70, v1\n\t" \
    "v_add_u32 v1, v1, v3\n\t" \
    "v_and_b32 v185, 15, v223\n\t" \
    "v_lshlrev_b32 v3, 6, v185\n\t" \
    "v_lshrrev_b32 v186, 4, v223\n\t" \
    "v_lshl_add_u32 v3, v186, 4, v3\n\t" \
    "v_bfe_u32 v187, v223, 3, 1\n\t" \
    "v_lshlrev_b32 v2, 5, v187\n\t" \
    "v_xor_b32 v3, v3, v2\n\t" \
    "v_mov_b32 v2, v3\n\t" \
    "s_mul_i32 s89, s66, 9216\n\t" \
    "s_mov_b32 s90, 0\n\t" \
    "s_waitcnt vmcnt(12) lgkmcnt(0)\n\t" \
    "s_barrier\n\t" \
    "s_add_u32 m0, s82, 65536\n\t" \
    "s_nop 0\n\t" \
    "global_load_lds_dwordx4 v200, s[42:43]\n\t" \
    "v_add_u32 v200, 131072, v200\n\t" \
    "s_add_u32 m0, s82, 69632\n\t" \
    "s_nop 0\n\t" \
    "global_load_lds_dwordx4 v200, s[42:43]\n\t" \
    "v_add_u32 v200, 131072, v200\n\t" \
    "s_add_u32 m0, s82, 73728\n\t" \
    "s_nop 0\n\t" \
    "global_load_lds_dwordx4 v200, s[42:43]\n\t" \
    "v_add_u32 v200, 131072, v200\n\t" \
    "s_add_u32 m0, s82, 77824\n\t" \
    "s_nop 0\n\t" \
    "global_load_lds_dwordx4 v200, s[42:43]\n\t" \
    "v_add_u32 v200, 131072, v200\n\t" \
    "v_add_u32 v202, 73728, v202\n\t" \
    "v_add_u32 v203, 73728, v203\n\t" \
    "ds_read_b64_tr_b16 a[144:145], v202 offset:0\n\t" \
    "ds_read_b64_tr_b16 a[146:147], v202 offset:1024\n\t" \
    "ds_read_b64_tr_b16 a[148:149], v202 offset:8192\n\t" \
    "ds_read_b64_tr_b16 a[150:151], v202 offset:9216\n\t" \
    "ds_read_b64_tr_b16 a[216:217], v203 offset:0\n\t" \
    "ds_read_b64_tr_b16 a[218:219], v203 offset:1024\n\t" \
    "ds_read_b64_tr_b16 a[220:221], v203 offset:8192\n\t" \
    "ds_read_b64_tr_b16 a[222:223], v203 offset:9216\n\t" \
    "v_lshlrev_b32 v0, 4, v223\n\t" \
    "s_mul_i32 s69, s66, 2048\n\t" \
    "s_add_u32 s69, s69, 147456\n\t" \
    "v_lshl_add_u32 v4, v223, 4, s69\n\t" \
    "s_mov_b32 s69, 147456\n\t" \
    "v_lshl_add_u32 v5, v223, 4, s69\n\t" \
    "v_lshlrev_b32 v6, 2, v223\n\t" \
    "v_mov_b32 v7, 0\n\t" \
    "s_mul_i32 s97, s67, 7\n\t" \
    "s_and_b32 s97, s97, 63\n\t" \
    "s_lshl_b32 s97, s97, 13\n\t" \
    "s_mul_i32 s71, s66, 2048\n\t" \
    "s_add_u32 s69, s97, s71\n\t" \
    "s_mul_i32 s71, s67, 524288\n\t" \
    "s_add_u32 s69, s69, s71\n\t" \
    "v_lshl_add_u32 v8, v223, 4, s69\n\t" \
    "s_add_u32 s38, s36, 6291456\n\t" \
    "s_addc_u32 s39, s37, 0\n\t" \
    "s_add_u32 s40, s36, 23068672\n\t" \
    "s_addc_u32 s41, s37, 0\n\t" \
    "s_mul_i32 s69, s67, 128\n\t" \
    "s_add_u32 s69, s69, 6098944\n\t" \
    "v_and_b32 v188, 1, v223\n\t" \
    "v_mov_b32 v10, s69\n\t" \
    "s_mov_b32 s70, 61440\n\t" \
    "v_mad_u32_u24 v10, v188, s70, v10\n\t" \
    "v_lshlrev_b32 v11, 0, v188\n\t" \
    "s_cmp_eq_u32 s66, 0\n\t" \
    "s_cselect_b32 s58, 3, 0\n\t" \
    "s_mov_b32 s59, 0\n\t" \
    "s_waitcnt vmcnt(12) lgkmcnt(0)\n\t" \
    "s_barrier\n\t" \
    "s_add_u32 m0, s82, 0\n\t" \
    "s_nop 0\n\t" \
    "global_load_lds_dwordx4 v200, s[42:43]\n\t" \
    "v_add_u32 v200, 131072, v200\n\t" \
    "s_add_u32 m0, s82, 4096\n\t" \
    "s_nop 0\n\t" \
    "global_load_lds_dwordx4 v200, s[42:43]\n\t" \
    "v_add_u32 v200, 131072, v200\n\t" \
    "s_add_u32 m0, s82, 8192\n\t" \
    "s_nop 0\n\t" \
    "global_load_lds_dwordx4 v200, s[42:43]\n\t" \
    "v_add_u32 v200, 131072, v200\n\t" \
    "s_add_u32 m0, s82, 12288\n\t" \
    "s_nop 0\n\t" \
    "global_load_lds_dwordx4 v200, s[42:43]\n\t" \
    "v_add_u32 v200, 131072, v200\n\t" \
    "ds_read_b64_tr_b16 a[152:153], v202 offset:16384\n\t" \
    "ds_read_b64_tr_b16 a[154:155], v202 offset:17408\n\t" \
    "ds_read_b64_tr_b16 a[156:157], v202 offset:24576\n\t" \
    "ds_read_b64_tr_b16 a[158:159], v202 offset:25600\n\t" \
    "ds_read_b64_tr_b16 a[224:225], v203 offset:16384\n\t" \
    "ds_read_b64_tr_b16 a[226:227], v203 offset:17408\n\t" \
    "ds_read_b64_tr_b16 a[228:229], v203 offset:24576\n\t" \
    "ds_read_b64_tr_b16 a[230:231], v203 offset:25600\n\t" \
    "s_waitcnt vmcnt(12) lgkmcnt(0)\n\t" \
    "s_barrier\n\t" \
    "s_add_u32 m0, s82, 16384\n\t" \
    "s_nop 0\n\t" \
    "global_load_lds_dwordx4 v200, s[42:43]\n\t" \
    "v_add_u32 v200, 131072, v200\n\t" \
    "s_add_u32 m0, s82, 20480\n\t" \
    "s_nop 0\n\t" \
    "global_load_lds_dwordx4 v200, s[42:43]\n\t" \
    "v_add_u32 v200, 131072, v200\n\t" \
    "s_add_u32 m0, s82, 24576\n\t" \
    "s_nop 0\n\t" \
    "global_load_lds_dwordx4 v200, s[42:43]\n\t" \
    "v_add_u32 v200, 131072, v200\n\t" \
    "s_add_u32 m0, s82, 28672\n\t" \
    "s_nop 0\n\t" \
    "global_load_lds_dwordx4 v200, s[42:43]\n\t" \
    "v_add_u32 v200, 131072, v200\n\t" \
    "s_add_u32 m0, s89, 0\n\t" \
    "s_nop 0\n\t" \
    "global_load_lds_dwordx4 v1, s[50:51] offset:0\n\t" \
    "s_add_u32 m0, s89, 960\n\t" \
    "s_nop 0\n\t" \
    "global_load_lds_dwordx4 v1, s[50:51] offset:64\n\t" \
    "s_add_u32 m0, s89, 1920\n\t" \
    "s_nop 0\n\t" \
    "global_load_lds_dwordx4 v1, s[50:51] offset:128\n\t" \
    "s_add_u32 m0, s89, 2880\n\t" \
    "s_nop 0\n\t" \
    "global_load_lds_dwordx4 v1, s[50:51] offset:192\n\t" \
    "s_add_u32 m0, s89, 3840\n\t" \
    "s_nop 0\n\t" \
    "global_load_lds_dwordx4 v1, s[50:51] offset:256\n\t" \
    "s_add_u32 m0, s89, 4800\n\t" \
    "s_nop 0\n\t" \
    "global_load_lds_dwordx4 v1, s[50:51] offset:320\n\t" \
    "s_add_u32 m0, s89, 5760\n\t" \
    "s_nop 0\n\t" \
    "global_load_lds_dwordx4 v1, s[50:51] offset:384\n\t" \
    "s_add_u32 m0, s89, 6720\n\t" \
    "s_nop 0\n\t" \
    "global_load_lds_dwordx4 v1, s[50:51] offset:448\n\t" \
    "s_add_u32 m0, s89, 7680\n\t" \
    "s_nop 0\n\t" \
    "global_load_lds_dwordx4 v1, s[50:51] offset:512\n\t" \
    "ds_read_b64_tr_b16 a[160:161], v202 offset:32768\n\t" \
    "ds_read_b64_tr_b16 a[162:163], v202 offset:33792\n\t" \
    "ds_read_b64_tr_b16 a[164:165], v202 offset:40960\n\t" \
    "ds_read_b64_tr_b16 a[166:167], v202 offset:41984\n\t" \
    "ds_read_b64_tr_b16 a[232:233], v203 offset:32768\n\t" \
    "ds_read_b64_tr_b16 a[234:235], v203 offset:33792\n\t" \
    "ds_read_b64_tr_b16 a[236:237], v203 offset:40960\n\t" \
    "ds_read_b64_tr_b16 a[238:239], v203 offset:41984\n\t" \
    "s_waitcnt vmcnt(21) lgkmcnt(0)\n\t" \
    "s_barrier\n\t" \
    "s_add_u32 m0, s82, 32768\n\t" \
    "s_nop 0\n\t" \
    "global_load_lds_dwordx4 v200, s[42:43]\n\t" \
    "v_add_u32 v200, 131072, v200\n\t" \
    "s_add_u32 m0, s82, 36864\n\t" \
    "s_nop 0\n\t" \
    "global_load_lds_dwordx4 v200, s[42:43]\n\t" \
    "v_add_u32 v200, 131072, v200\n\t" \
    "s_add_u32 m0, s82, 40960\n\t" \
    "s_nop 0\n\t" \
    "global_load_lds_dwordx4 v200, s[42:43]\n\t" \
    "v_add_u32 v200, 131072, v200\n\t" \
    "s_add_u32 m0, s82, 45056\n\t" \
    "s_nop 0\n\t" \
    "global_load_lds_dwordx4 v200, s[42:43]\n\t" \
    "v_add_u32 v200, 131072, v200\n\t" \
    "s_add_u32 s50, s50, 36864\n\t" \
    "s_addc_u32 s51, s51, 0\n\t" \
    "s_add_u32 m0, s89, 36864\n\t" \
    "s_nop 0\n\t" \
    "global_load_lds_dwordx4 v1, s[50:51] offset:0\n\t" \
    "s_add_u32 m0, s89, 37824\n\t" \
    "s_nop 0\n\t" \
    "global_load_lds_dwordx4 v1, s[50:51] offset:64\n\t" \
    "s_add_u32 m0, s89, 38784\n\t" \
    "s_nop 0\n\t" \
    "global_load_lds_dwordx4 v1, s[50:51] offset:128\n\t" \
    "s_add_u32 m0, s89, 39744\n\t" \
    "s_nop 0\n\t" \
    "global_load_lds_dwordx4 v1, s[50:51] offset:192\n\t" \
    "s_add_u32 m0, s89, 40704\n\t" \
    "s_nop 0\n\t" \
    "global_load_lds_dwordx4 v1, s[50:51] offset:256\n\t" \
    "s_add_u32 m0, s89, 41664\n\t" \
    "s_nop 0\n\t" \
    "global_load_lds_dwordx4 v1, s[50:51] offset:320\n\t" \
    "s_add_u32 m0, s89, 42624\n\t" \
    "s_nop 0\n\t" \
    "global_load_lds_dwordx4 v1, s[50:51] offset:384\n\t" \
    "s_add_u32 m0, s89, 43584\n\t" \
    "s_nop 0\n\t" \
    "global_load_lds_dwordx4 v1, s[50:51] offset:448\n\t" \
    "s_add_u32 m0, s89, 44544\n\t" \
    "s_nop 0\n\t" \
    "global_load_lds_dwordx4 v1, s[50:51] offset:512\n\t" \
    "ds_read_b64_tr_b16 a[168:169], v202 offset:49152\n\t" \
    "ds_read_b64_tr_b16 a[170:171], v202 offset:50176\n\t" \
    "ds_read_b64_tr_b16 a[172:173], v202 offset:57344\n\t" \
    "ds_read_b64_tr_b16 a[174:175], v202 offset:58368\n\t" \
    "ds_read_b64_tr_b16 a[240:241], v203 offset:49152\n\t" \
    "ds_read_b64_tr_b16 a[242:243], v203 offset:50176\n\t" \
    "ds_read_b64_tr_b16 a[244:245], v203 offset:57344\n\t" \
    "ds_read_b64_tr_b16 a[246:247], v203 offset:58368\n\t" \
    "s_waitcnt vmcnt(30) lgkmcnt(0)\n\t" \
    "s_barrier\n\t" \
    "s_add_u32 m0, s82, 49152\n\t" \
    "s_nop 0\n\t" \
    "global_load_lds_dwordx4 v200, s[42:43]\n\t" \
    "v_add_u32 v200, 131072, v200\n\t" \
    "s_add_u32 m0, s82, 53248\n\t" \
    "s_nop 0\n\t" \
    "global_load_lds_dwordx4 v200, s[42:43]\n\t" \
    "v_add_u32 v200, 131072, v200\n\t" \
    "s_add_u32 m0, s82, 57344\n\t" \
    "s_nop 0\n\t" \
    "global_load_lds_dwordx4 v200, s[42:43]\n\t" \
    "v_add_u32 v200, 131072, v200\n\t" \
    "s_add_u32 m0, s82, 61440\n\t" \
    "s_nop 0\n\t" \
    "global_load_lds_dwordx4 v200, s[42:43]\n\t" \
    "v_add_u32 v200, 131072, v200\n\t" \
    "v_add_u32 v202, 65536, v202\n\t" \
    "v_add_u32 v203, 65536, v203\n\t" \
    "ds_read_b64_tr_b16 a[176:177], v202 offset:0\n\t" \
    "ds_read_b64_tr_b16 a[178:179], v202 offset:1024\n\t" \
    "ds_read_b64_tr_b16 a[180:181], v202 offset:8192\n\t" \
    "ds_read_b64_tr_b16 a[182:183], v202 offset:9216\n\t" \
    "ds_read_b64_tr_b16 a[248:249], v203 offset:0\n\t" \
    "ds_read_b64_tr_b16 a[250:251], v203 offset:1024\n\t" \
    "ds_read_b64_tr_b16 a[252:253], v203 offset:8192\n\t" \
    "ds_read_b64_tr_b16 a[254:255], v203 offset:9216\n\t" \
    "s_waitcnt vmcnt(30) lgkmcnt(0)\n\t" \
    "s_barrier\n\t" \
    "v_subrev_u32 v200, 4718592, v200\n\t" \
    "s_add_u32 m0, s82, 65536\n\t" \
    "s_nop 0\n\t" \
    "global_load_lds_dwordx4 v200, s[44:45]\n\t" \
    "v_add_u32 v200, 131072, v200\n\t" \
    "s_add_u32 m0, s82, 69632\n\t" \
    "s_nop 0\n\t" \
    "global_load_lds_dwordx4 v200, s[44:45]\n\t" \
    "v_add_u32 v200, 131072, v200\n\t" \
    "s_add_u32 m0, s82, 73728\n\t" \
    "s_nop 0\n\t" \
    "global_load_lds_dwordx4 v200, s[44:45]\n\t" \
    "v_add_u32 v200, 131072, v200\n\t" \
    "s_add_u32 m0, s82, 77824\n\t" \
    "s_nop 0\n\t" \
    "global_load_lds_dwordx4 v200, s[44:45]\n\t" \
    "v_add_u32 v200, 131072, v200\n\t" \
    "v_add_u32 v202, -65536, v202\n\t" \
    "v_add_u32 v203, -65536, v203\n\t" \
    "ds_read_b64_tr_b16 a[184:185], v202 offset:0\n\t" \
    "ds_read_b64_tr_b16 a[186:187], v202 offset:1024\n\t" \
    "ds_read_b64_tr_b16 a[188:189], v202 offset:8192\n\t" \
    "ds_read_b64_tr_b16 a[190:191], v202 offset:9216\n\t" \
    "ds_read_b64_tr_b16 v[224:225], v203 offset:0\n\t" \
    "ds_read_b64_tr_b16 v[226:227], v203 offset:1024\n\t" \
    "ds_read_b64_tr_b16 v[228:229], v203 offset:8192\n\t" \
    "ds_read_b64_tr_b16 v[230:231], v203 offset:9216\n\t" \
    "s_waitcnt vmcnt(30) lgkmcnt(0)\n\t" \
    "s_barrier\n\t" \
    "s_add_u32 m0, s82, 0\n\t" \
    "s_nop 0\n\t" \
    "global_load_lds_dwordx4 v200, s[44:45]\n\t" \
    "v_add_u32 v200, 131072, v200\n\t" \
    "s_add_u32 m0, s82, 4096\n\t" \
    "s_nop 0\n\t" \
    "global_load_lds_dwordx4 v200, s[44:45]\n\t" \
    "v_add_u32 v200, 131072, v200\n\t" \
    "s_add_u32 m0, s82, 8192\n\t" \
    "s_nop 0\n\t" \
    "global_load_lds_dwordx4 v200, s[44:45]\n\t" \
    "v_add_u32 v200, 131072, v200\n\t" \
    "s_add_u32 m0, s82, 12288\n\t" \
    "s_nop 0\n\t" \
    "global_load_lds_dwordx4 v200, s[44:45]\n\t" \
    "v_add_u32 v200, 131072, v200\n\t" \
    "ds_read_b64_tr_b16 a[192:193], v202 offset:16384\n\t" \
    "ds_read_b64_tr_b16 a[194:195], v202 offset:17408\n\t" \
    "ds_read_b64_tr_b16 a[196:197], v202 offset:24576\n\t" \
    "ds_read_b64_tr_b16 a[198:199], v202 offset:25600\n\t" \
    "ds_read_b64_tr_b16 v[232:233], v203 offset:16384\n\t" \
    "ds_read_b64_tr_b16 v[234:235], v203 offset:17408\n\t" \
    "ds_read_b64_tr_b16 v[236:237], v203 offset:24576\n\t" \
    "ds_read_b64_tr_b16 v[238:239], v203 offset:25600\n\t" \
    "s_waitcnt vmcnt(21) lgkmcnt(0)\n\t" \
    "s_barrier\n\t" \
    "s_add_u32 m0, s82, 16384\n\t" \
    "s_nop 0\n\t" \
    "global_load_lds_dwordx4 v200, s[44:45]\n\t" \
    "v_add_u32 v200, 131072, v200\n\t" \
    "s_add_u32 m0, s82, 20480\n\t" \
    "s_nop 0\n\t" \
    "global_load_lds_dwordx4 v200, s[44:45]\n\t" \
    "v_add_u32 v200, 131072, v200\n\t" \
    "s_add_u32 m0, s82, 24576\n\t" \
    "s_nop 0\n\t" \
    "global_load_lds_dwordx4 v200, s[44:45]\n\t" \
    "v_add_u32 v200, 131072, v200\n\t" \
    "s_add_u32 m0, s82, 28672\n\t" \
    "s_nop 0\n\t" \
    "global_load_lds_dwordx4 v200, s[44:45]\n\t" \
    "v_add_u32 v200, 131072, v200\n\t" \
    "ds_read_b64_tr_b16 a[200:201], v202 offset:32768\n\t" \
    "ds_read_b64_tr_b16 a[202:203], v202 offset:33792\n\t" \
    "ds_read_b64_tr_b16 a[204:205], v202 offset:40960\n\t" \
    "ds_read_b64_tr_b16 a[206:207], v202 offset:41984\n\t" \
    "ds_read_b64_tr_b16 v[240:241], v203 offset:32768\n\t" \
    "ds_read_b64_tr_b16 v[242:243], v203 offset:33792\n\t" \
    "ds_read_b64_tr_b16 v[244:245], v203 offset:40960\n\t" \
    "ds_read_b64_tr_b16 v[246:247], v203 offset:41984\n\t" \
    "s_waitcnt vmcnt(12) lgkmcnt(0)\n\t" \
    "s_barrier\n\t" \
    "s_add_u32 m0, s82, 32768\n\t" \
    "s_nop 0\n\t" \
    "global_load_lds_dwordx4 v200, s[44:45]\n\t" \
    "v_add_u32 v200, 131072, v200\n\t" \
    "s_add_u32 m0, s82, 36864\n\t" \
    "s_nop 0\n\t" \
    "global_load_lds_dwordx4 v200, s[44:45]\n\t" \
    "v_add_u32 v200, 131072, v200\n\t" \
    "s_add_u32 m0, s82, 40960\n\t" \
    "s_nop 0\n\t" \
    "global_load_lds_dwordx4 v200, s[44:45]\n\t" \
    "v_add_u32 v200, 131072, v200\n\t" \
    "s_add_u32 m0, s82, 45056\n\t" \
    "s_nop 0\n\t" \
    "global_load_lds_dwordx4 v200, s[44:45]\n\t" \
    "v_add_u32 v200, 131072, v200\n\t" \
    "v_mbcnt_lo_u32_b32 v164, -1, 0\n\t" \
    "v_mbcnt_hi_u32_b32 v164, -1, v164\n\t" \
    "s_cmp_eq_u32 s66, 0\n\t" \
    "s_cselect_b32 s69, 59, 11\n\t" \
    "v_min_u32 v164, s69, v164\n\t" \
    "v_lshlrev_b32 v164, 6, v164\n\t" \
    "v_add_u32 v164, 6258688, v164\n\t" \
    "v_mov_b32 v165, 6266880\n\t" \
    "global_load_dword v165, v165, s[36:37] sc1\n\t" \
    "global_load_dwordx2 v[166:167], v164, s[36:37] sc1\n\t" \
    "ds_read_b64_tr_b16 a[208:209], v202 offset:49152\n\t" \
    "ds_read_b64_tr_b16 a[210:211], v202 offset:50176\n\t" \
    "ds_read_b64_tr_b16 a[212:213], v202 offset:57344\n\t" \
    "ds_read_b64_tr_b16 a[214:215], v202 offset:58368\n\t" \
    "ds_read_b64_tr_b16 v[248:249], v203 offset:49152\n\t" \
    "ds_read_b64_tr_b16 v[250:251], v203 offset:50176\n\t" \
    "ds_read_b64_tr_b16 v[252:253], v203 offset:57344\n\t" \
    "ds_read_b64_tr_b16 v[254:255], v203 offset:58368\n\t" \
    "s_waitcnt lgkmcnt(0)\n\t" \
    "s_waitcnt lgkmcnt(6)\n\t" \
    "v_add_u32 v2, 0, v3\n\t" \
    "ds_read_b128 v[24:27], v2 offset:0\n\t" \
    "ds_read_b128 v[28:31], v2 offset:18432\n\t" \
    "ds_read_b128 v[32:35], v2 offset:1024\n\t" \
    "ds_read_b128 v[36:39], v2 offset:19456\n\t" \
    "s_waitcnt lgkmcnt(3)\n\t" \
    "v_mfma_f32_16x16x32_bf16 v[88:91], a[144:147], v[24:27], 0\n\t" \
    "ds_read_b128 v[40:43], v2 offset:2048\n\t" \
    "s_waitcnt lgkmcnt(3)\n\t" \
    "v_mfma_f32_16x16x32_bf16 v[92:95], a[144:147], v[28:31], 0\n\t" \
    "ds_read_b128 v[44:47], v2 offset:20480\n\t" \
    "v_mfma_f32_16x16x32_bf16 v[96:99], a[216:219], v[24:27], 0\n\t" \
    "v_mfma_f32_16x16x32_bf16 v[100:103], a[216:219], v[28:31], 0\n\t" \
    "s_waitcnt lgkmcnt(3)\n\t" \
    "v_mfma_f32_16x16x32_bf16 v[88:91], a[148:151], v[32:35], v[88:91]\n\t" \
    "ds_read_b128 v[48:51], v2 offset:3072\n\t" \
    "s_waitcnt lgkmcnt(3)\n\t" \
    "v_mfma_f32_16x16x32_bf16 v[92:95], a[148:151], v[36:39], v[92:95]\n\t" \
    "ds_read_b128 v[52:55], v2 offset:21504\n\t" \
    "v_mfma_f32_16x16x32_bf16 v[96:99], a[220:223], v[32:35], v[96:99]\n\t" \
    "v_mfma_f32_16x16x32_bf16 v[100:103], a[220:223], v[36:39], v[100:103]\n\t" \
    "s_waitcnt lgkmcnt(3)\n\t" \
    "v_mfma_f32_16x16x32_bf16 v[88:91], a[152:155], v[40:43], v[88:91]\n\t" \
    "ds_read_b128 v[56:59], v2 offset:4096\n\t" \
    "s_waitcnt lgkmcnt(3)\n\t" \
    "v_mfma_f32_16x16x32_bf16 v[92:95], a[152:155], v[44:47], v[92:95]\n\t" \
    "ds_read_b128 v[60:63], v2 offset:22528\n\t" \
    "v_mfma_f32_16x16x32_bf16 v[96:99], a[224:227], v[40:43], v[96:99]\n\t" \
    "v_mfma_f32_16x16x32_bf16 v[100:103], a[224:227], v[44:47], v[100:103]\n\t" \
    "s_waitcnt lgkmcnt(3)\n\t" \
    "v_mfma_f32_16x16x32_bf16 v[88:91], a[156:159], v[48:51], v[88:91]\n\t" \
    "ds_read_b128 v[64:67], v2 offset:5120\n\t" \
    "s_waitcnt lgkmcnt(3)\n\t" \
    "v_mfma_f32_16x16x32_bf16 v[92:95], a[156:159], v[52:55], v[92:95]\n\t" \
    "ds_read_b128 v[68:71], v2 offset:23552\n\t" \
    "v_mfma_f32_16x16x32_bf16 v[96:99], a[228:231], v[48:51], v[96:99]\n\t" \
    "v_mfma_f32_16x16x32_bf16 v[100:103], a[228:231], v[52:55], v[100:103]\n\t" \
    "s_waitcnt lgkmcnt(3)\n\t" \
    "v_mfma_f32_16x16x32_bf16 v[88:91], a[160:163], v[56:59], v[88:91]\n\t" \
    "ds_read_b128 v[24:27], v2 offset:6144\n\t" \
    "s_waitcnt lgkmcnt(3)\n\t" \
    "v_mfma_f32_16x16x32_bf16 v[92:95], a[160:163], v[60:63], v[92:95]\n\t" \
    "ds_read_b128 v[28:31], v2 offset:24576\n\t" \
    "v_mfma_f32_16x16x32_bf16 v[96:99], a[232:235], v[56:59], v[96:99]\n\t" \
    "v_mfma_f32_16x16x32_bf16 v[100:103], a[232:235], v[60:63], v[100:103]\n\t" \
    "s_waitcnt lgkmcnt(3)\n\t" \
    "v_mfma_f32_16x16x32_bf16 v[88:91], a[164:167], v[64:67], v[88:91]\n\t" \
    "ds_read_b128 v[32:35], v2 offset:7168\n\t" \
    "s_waitcnt lgkmcnt(3)\n\t" \
    "v_mfma_f32_16x16x32_bf16 v[92:95], a[164:167], v[68:71], v[92:95]\n\t" \
    "ds_read_b128 v[36:39], v2 offset:25600\n\t" \
    "v_mfma_f32_16x16x32_bf16 v[96:99], a[236:239], v[64:67], v[96:99]\n\t" \
    "v_mfma_f32_16x16x32_bf16 v[100:103], a[236:239], v[68:71], v[100:103]\n\t" \
    "s_waitcnt lgkmcnt(3)\n\t" \
    "v_mfma_f32_16x16x32_bf16 v[88:91], a[168:171], v[24:27], v[88:91]\n\t" \
    "ds_read_b128 v[40:43], v2 offset:8192\n\t" \
    "s_waitcnt lgkmcnt(3)\n\t" \
    "v_mfma_f32_16x16x32_bf16 v[92:95], a[168:171], v[28:31], v[92:95]\n\t" \
    "ds_read_b128 v[44:47], v2 offset:26624\n\t" \
    "v_mfma_f32_16x16x32_bf16 v[96:99], a[240:243], v[24:27], v[96:99]\n\t" \
    "v_mfma_f32_16x16x32_bf16 v[100:103], a[240:243], v[28:31], v[100:103]\n\t" \
    "s_waitcnt lgkmcnt(3)\n\t" \
    "v_mfma_f32_16x16x32_bf16 v[88:91], a[172:175], v[32:35], v[88:91]\n\t" \
    "ds_read_b128 v[48:51], v2 offset:9216\n\t" \
    "s_waitcnt lgkmcnt(3)\n\t" \
    "v_mfma_f32_16x16x32_bf16 v[92:95], a[172:175], v[36:39], v[92:95]\n\t" \
    "ds_read_b128 v[52:55], v2 offset:27648\n\t" \
    "v_mfma_f32_16x16x32_bf16 v[96:99], a[244:247], v[32:35], v[96:99]\n\t" \
    "v_mfma_f32_16x16x32_bf16 v[100:103], a[244:247], v[36:39], v[100:103]\n\t" \
    "s_waitcnt lgkmcnt(3)\n\t" \
    "v_mfma_f32_16x16x32_bf16 v[88:91], a[176:179], v[40:43], v[88:91]\n\t" \
    "ds_read_b128 v[56:59], v2 offset:10240\n\t" \
    "s_waitcnt lgkmcnt(3)\n\t" \
    "v_mfma_f32_16x16x32_bf16 v[92:95], a[176:179], v[44:47], v[92:95]\n\t" \
    "ds_read_b128 v[60:63], v2 offset:28672\n\t" \
    "v_mfma_f32_16x16x32_bf16 v[96:99], a[248:251], v[40:43], v[96:99]\n\t" \
    "v_mfma_f32_16x16x32_bf16 v[100:103], a[248:251], v[44:47], v[100:103]\n\t" \
    "s_waitcnt lgkmcnt(3)\n\t" \
    "v_mfma_f32_16x16x32_bf16 v[88:91], a[180:183], v[48:51], v[88:91]\n\t" \
    "ds_read_b128 v[64:67], v2 offset:11264\n\t" \
    "s_waitcnt lgkmcnt(3)\n\t" \
    "v_mfma_f32_16x16x32_bf16 v[92:95], a[180:183], v[52:55], v[92:95]\n\t" \
    "ds_read_b128 v[68:71], v2 offset:29696\n\t" \
    "v_mfma_f32_16x16x32_bf16 v[96:99], a[252:255], v[48:51], v[96:99]\n\t" \
    "v_mfma_f32_16x16x32_bf16 v[100:103], a[252:255], v[52:55], v[100:103]\n\t" \
    "s_waitcnt lgkmcnt(3)\n\t" \
    "v_mfma_f32_16x16x32_bf16 v[88:91], a[184:187], v[56:59], v[88:91]\n\t" \
    "ds_read_b128 v[24:27], v2 offset:12288\n\t" \
    "s_waitcnt lgkmcnt(3)\n\t" \
    "v_mfma_f32_16x16x32_bf16 v[92:95], a[184:187], v[60:63], v[92:95]\n\t" \
    "ds_read_b128 v[28:31], v2 offset:30720\n\t" \
    "v_mfma_f32_16x16x32_bf16 v[96:99], v[224:227], v[56:59], v[96:99]\n\t" \
    "v_mfma_f32_16x16x32_bf16 v[100:103], v[224:227], v[60:63], v[100:103]\n\t" \
    "s_waitcnt lgkmcnt(3)\n\t" \
    "v_mfma_f32_16x16x32_bf16 v[88:91], a[188:191], v[64:67], v[88:91]\n\t" \
    "ds_read_b128 v[32:35], v2 offset:13312\n\t" \
    "s_waitcnt lgkmcnt(3)\n\t" \
    "v_mfma_f32_16x16x32_bf16 v[92:95], a[188:191], v[68:71], v[92:95]\n\t" \
    "ds_read_b128 v[36:39], v2 offset:31744\n\t" \
    "v_mfma_f32_16x16x32_bf16 v[96:99], v[228:231], v[64:67], v[96:99]\n\t" \
    "v_mfma_f32_16x16x32_bf16 v[100:103], v[228:231], v[68:71], v[100:103]\n\t" \
    "s_waitcnt lgkmcnt(3)\n\t" \
    "v_mfma_f32_16x16x32_bf16 v[88:91], a[192:195], v[24:27], v[88:91]\n\t" \
    "ds_read_b128 v[40:43], v2 offset:14336\n\t" \
    "s_waitcnt lgkmcnt(3)\n\t" \
    "v_mfma_f32_16x16x32_bf16 v[92:95], a[192:195], v[28:31], v[92:95]\n\t" \
    "ds_read_b128 v[44:47], v2 offset:32768\n\t" \
    "v_mfma_f32_16x16x32_bf16 v[96:99], v[232:235], v[24:27], v[96:99]\n\t" \
    "v_mfma_f32_16x16x32_bf16 v[100:103], v[232:235], v[28:31], v[100:103]\n\t" \
    "s_waitcnt lgkmcnt(3)\n\t" \
    "v_mfma_f32_16x16x32_bf16 v[88:91], a[196:199], v[32:35], v[88:91]\n\t" \
    "ds_read_b128 v[48:51], v2 offset:15360\n\t" \
    "s_waitcnt lgkmcnt(3)\n\t" \
    "v_mfma_f32_16x16x32_bf16 v[92:95], a[196:199], v[36:39], v[92:95]\n\t" \
    "ds_read_b128 v[52:55], v2 offset:33792\n\t" \
    "v_mfma_f32_16x16x32_bf16 v[96:99], v[236:239], v[32:35], v[96:99]\n\t" \
    "v_mfma_f32_16x16x32_bf16 v[100:103], v[236:239], v[36:39], v[100:103]\n\t" \
    "s_waitcnt lgkmcnt(3)\n\t" \
    "v_mfma_f32_16x16x32_bf16 v[88:91], a[200:203], v[40:43], v[88:91]\n\t" \
    "ds_read_b128 v[56:59], v2 offset:16384\n\t" \
    "s_waitcnt lgkmcnt(3)\n\t" \
    "v_mfma_f32_16x16x32_bf16 v[92:95], a[200:203], v[44:47], v[92:95]\n\t" \
    "ds_read_b128 v[60:63], v2 offset:34816\n\t" \
    "v_mfma_f32_16x16x32_bf16 v[96:99], v[240:243], v[40:43], v[96:99]\n\t" \
    "v_mfma_f32_16x16x32_bf16 v[100:103], v[240:243], v[44:47], v[100:103]\n\t" \
    "s_waitcnt lgkmcnt(3)\n\t" \
    "v_mfma_f32_16x16x32_bf16 v[88:91], a[204:207], v[48:51], v[88:91]\n\t" \
    "ds_read_b128 v[64:67], v2 offset:17408\n\t" \
    "s_waitcnt lgkmcnt(3)\n\t" \
    "v_mfma_f32_16x16x32_bf16 v[92:95], a[204:207], v[52:55], v[92:95]\n\t" \
    "ds_read_b128 v[68:71], v2 offset:35840\n\t" \
    "v_mfma_f32_16x16x32_bf16 v[96:99], v[244:247], v[48:51], v[96:99]\n\t" \
    "v_mfma_f32_16x16x32_bf16 v[100:103], v[244:247], v[52:55], v[100:103]\n\t" \
    "s_waitcnt lgkmcnt(3)\n\t" \
    "v_mfma_f32_16x16x32_bf16 v[88:91], a[208:211], v[56:59], v[88:91]\n\t" \
    "s_waitcnt lgkmcnt(2)\n\t" \
    "v_mfma_f32_16x16x32_bf16 v[92:95], a[208:211], v[60:63], v[92:95]\n\t" \
    "v_mfma_f32_16x16x32_bf16 v[96:99], v[248:251], v[56:59], v[96:99]\n\t" \
    "v_mfma_f32_16x16x32_bf16 v[100:103], v[248:251], v[60:63], v[100:103]\n\t" \
    "s_waitcnt lgkmcnt(1)\n\t" \
    "v_mfma_f32_16x16x32_bf16 v[88:91], a[212:215], v[64:67], v[88:91]\n\t" \
    "s_waitcnt lgkmcnt(0)\n\t" \
    "v_mfma_f32_16x16x32_bf16 v[92:95], a[212:215], v[68:71], v[92:95]\n\t" \
    "v_mfma_f32_16x16x32_bf16 v[96:99], v[252:255], v[64:67], v[96:99]\n\t" \
    "v_mfma_f32_16x16x32_bf16 v[100:103], v[252:255], v[68:71], v[100:103]\n\t" \
    "v_xor_b32 v9, 0, v8\n\t" \
    "s_nop 2\n\t" \
    "v_cvt_pk_bf16_f32 v112, v88, v89\n\t" \
    "v_cvt_pk_bf16_f32 v113, v90, v91\n\t" \
    "s_nop 0\n\t" \
    "v_cvt_pk_bf16_f32 v114, v96, v97\n\t" \
    "v_cvt_pk_bf16_f32 v115, v98, v99\n\t" \
    "global_store_dwordx4 v9, v[112:115], s[40:41] offset:0 sc1\n\t" \
    "v_cvt_pk_bf16_f32 v116, v92, v93\n\t" \
    "v_cvt_pk_bf16_f32 v117, v94, v95\n\t" \
    "v_cvt_pk_bf16_f32 v118, v100, v101\n\t" \
    "v_cvt_pk_bf16_f32 v119, v102, v103\n\t" \
    "global_store_dwordx4 v9, v[116:119], s[40:41] offset:1024 sc1\n\t" \
    "s_waitcnt lgkmcnt(6)\n\t" \
    "v_add_u32 v2, 36864, v3\n\t" \
    "ds_read_b128 v[24:27], v2 offset:0\n\t" \
    "ds_read_b128 v[28:31], v2 offset:18432\n\t" \
    "ds_read_b128 v[32:35], v2 offset:1024\n\t" \
    "ds_read_b128 v[36:39], v2 offset:19456\n\t" \
    "s_waitcnt lgkmcnt(3)\n\t" \
    "v_mfma_f32_16x16x32_bf16 v[88:91], a[144:147], v[24:27], 0\n\t" \
    "ds_read_b128 v[40:43], v2 offset:2048\n\t" \
    "s_waitcnt lgkmcnt(3)\n\t" \
    "v_mfma_f32_16x16x32_bf16 v[92:95], a[144:147], v[28:31], 0\n\t" \
    "ds_read_b128 v[44:47], v2 offset:20480\n\t" \
    "v_mfma_f32_16x16x32_bf16 v[96:99], a[216:219], v[24:27], 0\n\t" \
    "v_mfma_f32_16x16x32_bf16 v[100:103], a[216:219], v[28:31], 0\n\t" \
    "s_waitcnt lgkmcnt(3)\n\t" \
    "v_mfma_f32_16x16x32_bf16 v[88:91], a[148:151], v[32:35], v[88:91]\n\t" \
    "ds_read_b128 v[48:51], v2 offset:3072\n\t" \
    "s_waitcnt lgkmcnt(3)\n\t" \
    "v_mfma_f32_16x16x32_bf16 v[92:95], a[148:151], v[36:39], v[92:95]\n\t" \
    "ds_read_b128 v[52:55], v2 offset:21504\n\t" \
    "v_mfma_f32_16x16x32_bf16 v[96:99], a[220:223], v[32:35], v[96:99]\n\t" \
    "v_mfma_f32_16x16x32_bf16 v[100:103], a[220:223], v[36:39], v[100:103]\n\t" \
    "s_waitcnt lgkmcnt(3)\n\t" \
    "v_mfma_f32_16x16x32_bf16 v[88:91], a[152:155], v[40:43], v[88:91]\n\t" \
    "ds_read_b128 v[56:59], v2 offset:4096\n\t" \
    "s_waitcnt lgkmcnt(3)\n\t" \
    "v_mfma_f32_16x16x32_bf16 v[92:95], a[152:155], v[44:47], v[92:95]\n\t" \
    "ds_read_b128 v[60:63], v2 offset:22528\n\t" \
    "v_mfma_f32_16x16x32_bf16 v[96:99], a[224:227], v[40:43], v[96:99]\n\t" \
    "v_mfma_f32_16x16x32_bf16 v[100:103], a[224:227], v[44:47], v[100:103]\n\t" \
    "s_waitcnt lgkmcnt(3)\n\t" \
    "v_mfma_f32_16x16x32_bf16 v[88:91], a[156:159], v[48:51], v[88:91]\n\t" \
    "ds_read_b128 v[64:67], v2 offset:5120\n\t" \
    "s_waitcnt lgkmcnt(3)\n\t" \
    "v_mfma_f32_16x16x32_bf16 v[92:95], a[156:159], v[52:55], v[92:95]\n\t" \
    "ds_read_b128 v[68:71], v2 offset:23552\n\t" \
    "v_mfma_f32_16x16x32_bf16 v[96:99], a[228:231], v[48:51], v[96:99]\n\t" \
    "v_mfma_f32_16x16x32_bf16 v[100:103], a[228:231], v[52:55], v[100:103]\n\t" \
    "s_waitcnt lgkmcnt(3)\n\t" \
    "v_mfma_f32_16x16x32_bf16 v[88:91], a[160:163], v[56:59], v[88:91]\n\t" \
    "ds_read_b128 v[24:27], v2 offset:6144\n\t" \
    "s_waitcnt lgkmcnt(3)\n\t" \
    "v_mfma_f32_16x16x32_bf16 v[92:95], a[160:163], v[60:63], v[92:95]\n\t" \
    "ds_read_b128 v[28:31], v2 offset:24576\n\t" \
    "v_mfma_f32_16x16x32_bf16 v[96:99], a[232:235], v[56:59], v[96:99]\n\t" \
    "v_mfma_f32_16x16x32_bf16 v[100:103], a[232:235], v[60:63], v[100:103]\n\t" \
    "s_waitcnt lgkmcnt(3)\n\t" \
    "v_mfma_f32_16x16x32_bf16 v[88:91], a[164:167], v[64:67], v[88:91]\n\t" \
    "ds_read_b128 v[32:35], v2 offset:7168\n\t" \
    "s_waitcnt lgkmcnt(3)\n\t" \
    "v_mfma_f32_16x16x32_bf16 v[92:95], a[164:167], v[68:71], v[92:95]\n\t" \
    "ds_read_b128 v[36:39], v2 offset:25600\n\t" \
    "v_mfma_f32_16x16x32_bf16 v[96:99], a[236:239], v[64:67], v[96:99]\n\t" \
    "v_mfma_f32_16x16x32_bf16 v[100:103], a[236:239], v[68:71], v[100:103]\n\t" \
    "s_waitcnt lgkmcnt(3)\n\t" \
    "v_mfma_f32_16x16x32_bf16 v[88:91], a[168:171], v[24:27], v[88:91]\n\t" \
    "ds_read_b128 v[40:43], v2 offset:8192\n\t" \
    "s_waitcnt lgkmcnt(3)\n\t" \
    "v_mfma_f32_16x16x32_bf16 v[92:95], a[168:171], v[28:31], v[92:95]\n\t" \
    "ds_read_b128 v[44:47], v2 offset:26624\n\t" \
    "v_mfma_f32_16x16x32_bf16 v[96:99], a[240:243], v[24:27], v[96:99]\n\t" \
    "v_mfma_f32_16x16x32_bf16 v[100:103], a[240:243], v[28:31], v[100:103]\n\t" \
    "s_waitcnt lgkmcnt(3)\n\t" \
    "v_mfma_f32_16x16x32_bf16 v[88:91], a[172:175], v[32:35], v[88:91]\n\t" \
    "ds_read_b128 v[48:51], v2 offset:9216\n\t" \
    "s_waitcnt lgkmcnt(3)\n\t" \
    "v_mfma_f32_16x16x32_bf16 v[92:95], a[172:175], v[36:39], v[92:95]\n\t" \
    "ds_read_b128 v[52:55], v2 offset:27648\n\t" \
    "v_mfma_f32_16x16x32_bf16 v[96:99], a[244:247], v[32:35], v[96:99]\n\t" \
    "v_mfma_f32_16x16x32_bf16 v[100:103], a[244:247], v[36:39], v[100:103]\n\t" \
    "s_waitcnt lgkmcnt(3)\n\t" \
    "v_mfma_f32_16x16x32_bf16 v[88:91], a[176:179], v[40:43], v[88:91]\n\t" \
    "ds_read_b128 v[56:59], v2 offset:10240\n\t" \
    "s_waitcnt lgkmcnt(3)\n\t" \
    "v_mfma_f32_16x16x32_bf16 v[92:95], a[176:179], v[44:47], v[92:95]\n\t" \
    "ds_read_b128 v[60:63], v2 offset:28672\n\t" \
    "v_mfma_f32_16x16x32_bf16 v[96:99], a[248:251], v[40:43], v[96:99]\n\t" \
    "v_mfma_f32_16x16x32_bf16 v[100:103], a[248:251], v[44:47], v[100:103]\n\t" \
    "s_waitcnt lgkmcnt(3)\n\t" \
    "v_mfma_f32_16x16x32_bf16 v[88:91], a[180:183], v[48:51], v[88:91]\n\t" \
    "ds_read_b128 v[64:67], v2 offset:11264\n\t" \
    "s_waitcnt lgkmcnt(3)\n\t" \
    "v_mfma_f32_16x16x32_bf16 v[92:95], a[180:183], v[52:55], v[92:95]\n\t" \
    "ds_read_b128 v[68:71], v2 offset:29696\n\t" \
    "v_mfma_f32_16x16x32_bf16 v[96:99], a[252:255], v[48:51], v[96:99]\n\t" \
    "v_mfma_f32_16x16x32_bf16 v[100:103], a[252:255], v[52:55], v[100:103]\n\t" \
    "s_waitcnt lgkmcnt(3)\n\t" \
    "v_mfma_f32_16x16x32_bf16 v[88:91], a[184:187], v[56:59], v[88:91]\n\t" \
    "ds_read_b128 v[24:27], v2 offset:12288\n\t" \
    "s_waitcnt lgkmcnt(3)\n\t" \
    "v_mfma_f32_16x16x32_bf16 v[92:95], a[184:187], v[60:63], v[92:95]\n\t" \
    "ds_read_b128 v[28:31], v2 offset:30720\n\t" \
    "v_mfma_f32_16x16x32_bf16 v[96:99], v[224:227], v[56:59], v[96:99]\n\t" \
    "v_mfma_f32_16x16x32_bf16 v[100:103], v[224:227], v[60:63], v[100:103]\n\t" \
    "s_waitcnt lgkmcnt(3)\n\t" \
    "v_mfma_f32_16x16x32_bf16 v[88:91], a[188:191], v[64:67], v[88:91]\n\t" \
    "ds_read_b128 v[32:35], v2 offset:13312\n\t" \
    "s_waitcnt lgkmcnt(3)\n\t" \
    "v_mfma_f32_16x16x32_bf16 v[92:95], a[188:191], v[68:71], v[92:95]\n\t" \
    "ds_read_b128 v[36:39], v2 offset:31744\n\t" \
    "v_mfma_f32_16x16x32_bf16 v[96:99], v[228:231], v[64:67], v[96:99]\n\t" \
    "v_mfma_f32_16x16x32_bf16 v[100:103], v[228:231], v[68:71], v[100:103]\n\t" \
    "s_waitcnt lgkmcnt(3)\n\t" \
    "v_mfma_f32_16x16x32_bf16 v[88:91], a[192:195], v[24:27], v[88:91]\n\t" \
    "ds_read_b128 v[40:43], v2 offset:14336\n\t" \
    "s_waitcnt lgkmcnt(3)\n\t" \
    "v_mfma_f32_16x16x32_bf16 v[92:95], a[192:195], v[28:31], v[92:95]\n\t" \
    "ds_read_b128 v[44:47], v2 offset:32768\n\t" \
    "v_mfma_f32_16x16x32_bf16 v[96:99], v[232:235], v[24:27], v[96:99]\n\t" \
    "v_mfma_f32_16x16x32_bf16 v[100:103], v[232:235], v[28:31], v[100:103]\n\t" \
    "s_waitcnt lgkmcnt(3)\n\t" \
    "v_mfma_f32_16x16x32_bf16 v[88:91], a[196:199], v[32:35], v[88:91]\n\t" \
    "ds_read_b128 v[48:51], v2 offset:15360\n\t" \
    "s_waitcnt lgkmcnt(3)\n\t" \
    "v_mfma_f32_16x16x32_bf16 v[92:95], a[196:199], v[36:39], v[92:95]\n\t" \
    "ds_read_b128 v[52:55], v2 offset:33792\n\t" \
    "v_mfma_f32_16x16x32_bf16 v[96:99], v[236:239], v[32:35], v[96:99]\n\t" \
    "v_mfma_f32_16x16x32_bf16 v[100:103], v[236:239], v[36:39], v[100:103]\n\t" \
    "s_waitcnt lgkmcnt(3)\n\t" \
    "v_mfma_f32_16x16x32_bf16 v[88:91], a[200:203], v[40:43], v[88:91]\n\t" \
    "ds_read_b128 v[56:59], v2 offset:16384\n\t" \
    "s_waitcnt lgkmcnt(3)\n\t" \
    "v_mfma_f32_16x16x32_bf16 v[92:95], a[200:203], v[44:47], v[92:95]\n\t" \
    "ds_read_b128 v[60:63], v2 offset:34816\n\t" \
    "v_mfma_f32_16x16x32_bf16 v[96:99], v[240:243], v[40:43], v[96:99]\n\t" \
    "v_mfma_f32_16x16x32_bf16 v[100:103], v[240:243], v[44:47], v[100:103]\n\t" \
    "s_waitcnt lgkmcnt(3)\n\t" \
    "v_mfma_f32_16x16x32_bf16 v[88:91], a[204:207], v[48:51], v[88:91]\n\t" \
    "ds_read_b128 v[64:67], v2 offset:17408\n\t" \
    "s_waitcnt lgkmcnt(3)\n\t" \
    "v_mfma_f32_16x16x32_bf16 v[92:95], a[204:207], v[52:55], v[92:95]\n\t" \
    "ds_read_b128 v[68:71], v2 offset:35840\n\t" \
    "v_mfma_f32_16x16x32_bf16 v[96:99], v[244:247], v[48:51], v[96:99]\n\t" \
    "v_mfma_f32_16x16x32_bf16 v[100:103], v[244:247], v[52:55], v[100:103]\n\t" \
    "s_waitcnt lgkmcnt(3)\n\t" \
    "v_mfma_f32_16x16x32_bf16 v[88:91], a[208:211], v[56:59], v[88:91]\n\t" \
    "s_waitcnt lgkmcnt(2)\n\t" \
    "v_mfma_f32_16x16x32_bf16 v[92:95], a[208:211], v[60:63], v[92:95]\n\t" \
    "v_mfma_f32_16x16x32_bf16 v[96:99], v[248:251], v[56:59], v[96:99]\n\t" \
    "v_mfma_f32_16x16x32_bf16 v[100:103], v[248:251], v[60:63], v[100:103]\n\t" \
    "s_waitcnt lgkmcnt(1)\n\t" \
    "v_mfma_f32_16x16x32_bf16 v[88:91], a[212:215], v[64:67], v[88:91]\n\t" \
    "s_waitcnt lgkmcnt(0)\n\t" \
    "v_mfma_f32_16x16x32_bf16 v[92:95], a[212:215], v[68:71], v[92:95]\n\t" \
    "v_mfma_f32_16x16x32_bf16 v[96:99], v[252:255], v[64:67], v[96:99]\n\t" \
    "v_mfma_f32_16x16x32_bf16 v[100:103], v[252:255], v[68:71], v[100:103]\n\t" \
    "v_xor_b32 v9, 8192, v8\n\t" \
    "s_nop 2\n\t" \
    "v_cvt_pk_bf16_f32 v112, v88, v89\n\t" \
    "v_cvt_pk_bf16_f32 v113, v90, v91\n\t" \
    "s_nop 0\n\t" \
    "v_cvt_pk_bf16_f32 v114, v96, v97\n\t" \
    "v_cvt_pk_bf16_f32 v115, v98, v99\n\t" \
    "global_store_dwordx4 v9, v[112:115], s[40:41] offset:0 sc1\n\t" \
    "v_cvt_pk_bf16_f32 v116, v92, v93\n\t" \
    "v_cvt_pk_bf16_f32 v117, v94, v95\n\t" \
    "v_cvt_pk_bf16_f32 v118, v100, v101\n\t" \
    "v_cvt_pk_bf16_f32 v119, v102, v103\n\t" \
    "global_store_dwordx4 v9, v[116:119], s[40:41] offset:1024 sc1\n\t" \
    "s_waitcnt vmcnt(18) lgkmcnt(0)\n\t" \
    "s_barrier\n\t" \
    "s_add_u32 m0, s82, 49152\n\t" \
    "s_nop 0\n\t" \
    "global_load_lds_dwordx4 v200, s[44:45]\n\t" \
    "v_add_u32 v200, 131072, v200\n\t" \
    "s_add_u32 m0, s82, 53248\n\t" \
    "s_nop 0\n\t" \
    "global_load_lds_dwordx4 v200, s[44:45]\n\t" \
    "v_add_u32 v200, 131072, v200\n\t" \
    "s_add_u32 m0, s82, 57344\n\t" \
    "s_nop 0\n\t" \
    "global_load_lds_dwordx4 v200, s[44:45]\n\t" \
    "v_add_u32 v200, 131072, v200\n\t" \
    "s_add_u32 m0, s82, 61440\n\t" \
    "s_nop 0\n\t" \
    "global_load_lds_dwordx4 v200, s[44:45]\n\t" \
    "v_add_u32 v200, 131072, v200\n\t" \
    "v_add_u32 v202, 65536, v202\n\t" \
    "v_add_u32 v203, 65536, v203\n\t" \
    "ds_read_b64_tr_b16 a[0:1], v202 offset:0\n\t" \
    "ds_read_b64_tr_b16 a[2:3], v202 offset:1024\n\t" \
    "ds_read_b64_tr_b16 a[4:5], v202 offset:8192\n\t" \
    "ds_read_b64_tr_b16 a[6:7], v202 offset:9216\n\t" \
    "ds_read_b64_tr_b16 a[72:73], v203 offset:0\n\t" \
    "ds_read_b64_tr_b16 a[74:75], v203 offset:1024\n\t" \
    "ds_read_b64_tr_b16 a[76:77], v203 offset:8192\n\t" \
    "ds_read_b64_tr_b16 a[78:79], v203 offset:9216\n\t" \
    "s_waitcnt vmcnt(18) lgkmcnt(0)\n\t" \
    "s_barrier\n\t" \
    "s_add_u32 m0, s82, 65536\n\t" \
    "s_nop 0\n\t" \
    "global_load_lds_dwordx4 v200, s[44:45]\n\t" \
    "v_add_u32 v200, 131072, v200\n\t" \
    "s_add_u32 m0, s82, 69632\n\t" \
    "s_nop 0\n\t" \
    "global_load_lds_dwordx4 v200, s[44:45]\n\t" \
    "v_add_u32 v200, 131072, v200\n\t" \
    "s_add_u32 m0, s82, 73728\n\t" \
    "s_nop 0\n\t" \
    "global_load_lds_dwordx4 v200, s[44:45]\n\t" \
    "v_add_u32 v200, 131072, v200\n\t" \
    "s_add_u32 m0, s82, 77824\n\t" \
    "s_nop 0\n\t" \
    "global_load_lds_dwordx4 v200, s[44:45]\n\t" \
    "v_add_u32 v200, 131072, v200\n\t" \
    "v_add_u32 v202, -65536, v202\n\t" \
    "v_add_u32 v203, -65536, v203\n\t" \
    "ds_read_b64_tr_b16 a[8:9], v202 offset:0\n\t" \
    "ds_read_b64_tr_b16 a[10:11], v202 offset:1024\n\t" \
    "ds_read_b64_tr_b16 a[12:13], v202 offset:8192\n\t" \
    "ds_read_b64_tr_b16 a[14:15], v202 offset:9216\n\t" \
    "ds_read_b64_tr_b16 a[80:81], v203 offset:0\n\t" \
    "ds_read_b64_tr_b16 a[82:83], v203 offset:1024\n\t" \
    "ds_read_b64_tr_b16 a[84:85], v203 offset:8192\n\t" \
    "ds_read_b64_tr_b16 a[86:87], v203 offset:9216\n\t" \
    "s_waitcnt vmcnt(18) lgkmcnt(0)\n\t" \
    "s_barrier\n\t" \
    "s_add_u32 m0, s82, 0\n\t" \
    "s_nop 0\n\t" \
    "global_load_lds_dwordx4 v200, s[44:45]\n\t" \
    "v_add_u32 v200, 131072, v200\n\t" \
    "s_add_u32 m0, s82, 4096\n\t" \
    "s_nop 0\n\t" \
    "global_load_lds_dwordx4 v200, s[44:45]\n\t" \
    "v_add_u32 v200, 131072, v200\n\t" \
    "s_add_u32 m0, s82, 8192\n\t" \
    "s_nop 0\n\t" \
    "global_load_lds_dwordx4 v200, s[44:45]\n\t" \
    "v_add_u32 v200, 131072, v200\n\t" \
    "s_add_u32 m0, s82, 12288\n\t" \
    "s_nop 0\n\t" \
    "global_load_lds_dwordx4 v200, s[44:45]\n\t" \
    "v_add_u32 v200, 131072, v200\n\t" \
    "ds_read_b64_tr_b16 a[16:17], v202 offset:16384\n\t" \
    "ds_read_b64_tr_b16 a[18:19], v202 offset:17408\n\t" \
    "ds_read_b64_tr_b16 a[20:21], v202 offset:24576\n\t" \
    "ds_read_b64_tr_b16 a[22:23], v202 offset:25600\n\t" \
    "ds_read_b64_tr_b16 a[88:89], v203 offset:16384\n\t" \
    "ds_read_b64_tr_b16 a[90:91], v203 offset:17408\n\t" \
    "ds_read_b64_tr_b16 a[92:93], v203 offset:24576\n\t" \
    "ds_read_b64_tr_b16 a[94:95], v203 offset:25600\n\t" \
    "s_waitcnt vmcnt(18) lgkmcnt(0)\n\t" \
    "s_barrier\n\t" \
    "s_add_u32 m0, s82, 16384\n\t" \
    "s_nop 0\n\t" \
    "global_load_lds_dwordx4 v200, s[44:45]\n\t" \
    "v_add_u32 v200, 131072, v200\n\t" \
    "s_add_u32 m0, s82, 20480\n\t" \
    "s_nop 0\n\t" \
    "global_load_lds_dwordx4 v200, s[44:45]\n\t" \
    "v_add_u32 v200, 131072, v200\n\t" \
    "s_add_u32 m0, s82, 24576\n\t" \
    "s_nop 0\n\t" \
    "global_load_lds_dwordx4 v200, s[44:45]\n\t" \
    "v_add_u32 v200, 131072, v200\n\t" \
    "s_add_u32 m0, s82, 28672\n\t" \
    "s_nop 0\n\t" \
    "global_load_lds_dwordx4 v200, s[44:45]\n\t" \
    "v_add_u32 v200, 131072, v200\n\t" \
    "ds_read_b64_tr_b16 a[24:25], v202 offset:32768\n\t" \
    "ds_read_b64_tr_b16 a[26:27], v202 offset:33792\n\t" \
    "ds_read_b64_tr_b16 a[28:29], v202 offset:40960\n\t" \
    "ds_read_b64_tr_b16 a[30:31], v202 offset:41984\n\t" \
    "ds_read_b64_tr_b16 a[96:97], v203 offset:32768\n\t" \
    "ds_read_b64_tr_b16 a[98:99], v203 offset:33792\n\t" \
    "ds_read_b64_tr_b16 a[100:101], v203 offset:40960\n\t" \
    "ds_read_b64_tr_b16 a[102:103], v203 offset:41984\n\t" \
    "s_waitcnt vmcnt(12) lgkmcnt(0)\n\t" \
    "s_barrier\n\t" \
    "s_add_u32 m0, s82, 32768\n\t" \
    "s_nop 0\n\t" \
    "global_load_lds_dwordx4 v200, s[44:45]\n\t" \
    "v_add_u32 v200, 131072, v200\n\t" \
    "s_add_u32 m0, s82, 36864\n\t" \
    "s_nop 0\n\t" \
    "global_load_lds_dwordx4 v200, s[44:45]\n\t" \
    "v_add_u32 v200, 131072, v200\n\t" \
    "s_add_u32 m0, s82, 40960\n\t" \
    "s_nop 0\n\t" \
    "global_load_lds_dwordx4 v200, s[44:45]\n\t" \
    "v_add_u32 v200, 131072, v200\n\t" \
    "s_add_u32 m0, s82, 45056\n\t" \
    "s_nop 0\n\t" \
    "global_load_lds_dwordx4 v200, s[44:45]\n\t" \
    "v_add_u32 v200, 131072, v200\n\t" \
    "v_sub_u32 v166, v166, v165\n\t" \
    "v_cmp_ne_u32 vcc, 1, v166\n\t" \
    "s_cbranch_vccz gx_Cok2_%=\n\t" \
    "gx_C1_%=:\n\t" \
    "global_load_dwordx2 v[166:167], v164, s[36:37] sc1\n\t" \
    "s_waitcnt vmcnt(0)\n\t" \
    "v_sub_u32 v166, v166, v165\n\t" \
    "v_cmp_ne_u32 vcc, 1, v166\n\t" \
    "s_cbranch_vccz gx_Cok2_%=\n\t" \
    "s_cmp_eq_u32 s68, 0\n\t" \
    "s_cbranch_scc1 gx_F3_%=\n\t" \
    "s_sub_u32 s68, s68, 1\n\t" \
    "s_sleep 4\n\t" \
    "s_branch gx_C1_%=\n\t" \
    "gx_F3_%=:\n\t" \
    "v_mov_b32 v168, 6095104\n\t" \
    "v_mov_b32 v169, 4\n\t" \
    "global_store_dword v168, v169, s[36:37] sc1\n\t" \
    "s_waitcnt vmcnt(0)\n\t" \
    "s_branch gx_Cok2_%=\n\t" \
    "gx_Cok2_%=:\n\t" \
    "s_cmp_lg_u32 s66, 0\n\t" \
    "s_cbranch_scc1 gx_Ca4_%=\n\t" \
    "v_mov_b32 v164, 6176768\n\t" \
    "s_mov_b64 exec, 1\n\t" \
    "global_atomic_add v164, v166, s[36:37] sc1\n\t" \
    "s_mov_b64 exec, -1\n\t" \
    "gx_Ca4_%=:\n\t" \
    "v_min_u32_dpp v167, v167, v167 row_shl:6 row_mask:0xf bank_mask:0xf\n\t" \
    "s_nop 0\n\t" \
    "v_readlane_b32 s69, v167, 4\n\t" \
    "v_readlane_b32 s70, v167, 5\n\t" \
    "s_nop 1\n\t" \
    "s_cmp_lt_u32 s70, s69\n\t" \
    "s_cselect_b32 s52, 8192, 0\n\t" \
    "s_xor_b32 s52, s52, 8192\n\t" \
    "s_cmp_lg_u32 s66, 0\n\t" \
    "s_cbranch_scc1 gx_SF5_%=\n\t" \
    "s_lshl_b32 s69, s67, 7\n\t" \
    "s_add_u32 s69, s69, 6160384\n\t" \
    "s_add_u32 s69, s69, s52\n\t" \
    "s_add_u32 s70, s65, 1\n\t" \
    "v_mov_b32 v156, s69\n\t" \
    "v_mov_b32 v157, s70\n\t" \
    "s_mov_b64 exec, 1\n\t" \
    "global_store_dword v156, v157, s[36:37] sc1\n\t" \
    "s_mov_b64 exec, -1\n\t" \
    "gx_SF5_%=:\n\t" \
    "v_add_u32 v10, s52, v10\n\t" \
    "ds_read_b64_tr_b16 a[32:33], v202 offset:49152\n\t" \
    "ds_read_b64_tr_b16 a[34:35], v202 offset:50176\n\t" \
    "ds_read_b64_tr_b16 a[36:37], v202 offset:57344\n\t" \
    "ds_read_b64_tr_b16 a[38:39], v202 offset:58368\n\t" \
    "ds_read_b64_tr_b16 a[104:105], v203 offset:49152\n\t" \
    "ds_read_b64_tr_b16 a[106:107], v203 offset:50176\n\t" \
    "ds_read_b64_tr_b16 a[108:109], v203 offset:57344\n\t" \
    "ds_read_b64_tr_b16 a[110:111], v203 offset:58368\n\t" \
    "s_waitcnt vmcnt(12) lgkmcnt(0)\n\t" \
    "s_barrier\n\t" \
    "v_add_u32 v202, 65536, v202\n\t" \
    "v_add_u32 v203, 65536, v203\n\t" \
    "ds_read_b64_tr_b16 a[40:41], v202 offset:0\n\t" \
    "ds_read_b64_tr_b16 a[42:43], v202 offset:1024\n\t" \
    "ds_read_b64_tr_b16 a[44:45], v202 offset:8192\n\t" \
    "ds_read_b64_tr_b16 a[46:47], v202 offset:9216\n\t" \
    "ds_read_b64_tr_b16 a[112:113], v203 offset:0\n\t" \
    "ds_read_b64_tr_b16 a[114:115], v203 offset:1024\n\t" \
    "ds_read_b64_tr_b16 a[116:117], v203 offset:8192\n\t" \
    "ds_read_b64_tr_b16 a[118:119], v203 offset:9216\n\t" \
    "s_waitcnt vmcnt(8) lgkmcnt(0)\n\t" \
    "s_barrier\n\t" \
    "v_add_u32 v202, -65536, v202\n\t" \
    "v_add_u32 v203, -65536, v203\n\t" \
    "ds_read_b64_tr_b16 a[48:49], v202 offset:0\n\t" \
    "ds_read_b64_tr_b16 a[50:51], v202 offset:1024\n\t" \
    "ds_read_b64_tr_b16 a[52:53], v202 offset:8192\n\t" \
    "ds_read_b64_tr_b16 a[54:55], v202 offset:9216\n\t" \
    "ds_read_b64_tr_b16 a[120:121], v203 offset:0\n\t" \
    "ds_read_b64_tr_b16 a[122:123], v203 offset:1024\n\t" \
    "ds_read_b64_tr_b16 a[124:125], v203 offset:8192\n\t" \
    "ds_read_b64_tr_b16 a[126:127], v203 offset:9216\n\t" \
    "s_waitcnt vmcnt(4) lgkmcnt(0)\n\t" \
    "s_barrier\n\t" \
    "ds_read_b64_tr_b16 a[56:57], v202 offset:16384\n\t" \
    "ds_read_b64_tr_b16 a[58:59], v202 offset:17408\n\t" \
    "ds_read_b64_tr_b16 a[60:61], v202 offset:24576\n\t" \
    "ds_read_b64_tr_b16 a[62:63], v202 offset:25600\n\t" \
    "ds_read_b64_tr_b16 a[128:129], v203 offset:16384\n\t" \
    "ds_read_b64_tr_b16 a[130:131], v203 offset:17408\n\t" \
    "ds_read_b64_tr_b16 a[132:133], v203 offset:24576\n\t" \
    "ds_read_b64_tr_b16 a[134:135], v203 offset:25600\n\t" \
    "s_waitcnt vmcnt(0) lgkmcnt(0)\n\t" \
    "s_barrier\n\t" \
    "ds_read_b64_tr_b16 a[64:65], v202 offset:32768\n\t" \
    "ds_read_b64_tr_b16 a[66:67], v202 offset:33792\n\t" \
    "ds_read_b64_tr_b16 a[68:69], v202 offset:40960\n\t" \
    "ds_read_b64_tr_b16 a[70:71], v202 offset:41984\n\t" \
    "ds_read_b64_tr_b16 a[136:137], v203 offset:32768\n\t" \
    "ds_read_b64_tr_b16 a[138:139], v203 offset:33792\n\t" \
    "ds_read_b64_tr_b16 a[140:141], v203 offset:40960\n\t" \
    "ds_read_b64_tr_b16 a[142:143], v203 offset:41984\n\t" \
    "s_waitcnt lgkmcnt(0)\n\t" \
    "s_barrier\n\t" \
    "v_mov_b32 v13, v2\n\t" \
    "v_mov_b32 v2, v3\n\t" \
    "ds_read_b128 v[24:27], v2 offset:0\n\t" \
    "ds_read_b128 v[28:31], v2 offset:18432\n\t" \
    "ds_read_b128 v[32:35], v2 offset:1024\n\t" \
    "ds_read_b128 v[36:39], v2 offset:19456\n\t" \
    "ds_read_b128 v[40:43], v2 offset:2048\n\t" \
    "ds_read_b128 v[44:47], v2 offset:20480\n\t" \
    "s_add_u32 s50, s50, 36864\n\t" \
    "s_addc_u32 s51, s51, 0\n\t" \
    "s_add_u32 m0, s89, 73728\n\t" \
    "s_nop 0\n\t" \
    "global_load_lds_dwordx4 v1, s[50:51] offset:0\n\t" \
    "s_add_u32 m0, s89, 74688\n\t" \
    "s_nop 0\n\t" \
    "global_load_lds_dwordx4 v1, s[50:51] offset:64\n\t" \
    "s_add_u32 m0, s89, 75648\n\t" \
    "s_nop 0\n\t" \
    "global_load_lds_dwordx4 v1, s[50:51] offset:128\n\t" \
    "s_add_u32 m0, s89, 76608\n\t" \
    "s_nop 0\n\t" \
    "global_load_lds_dwordx4 v1, s[50:51] offset:192\n\t" \
    "s_add_u32 m0, s89, 77568\n\t" \
    "s_nop 0\n\t" \
    "global_load_lds_dwordx4 v1, s[50:51] offset:256\n\t" \
    "s_add_u32 m0, s89, 78528\n\t" \
    "s_nop 0\n\t" \
    "global_load_lds_dwordx4 v1, s[50:51] offset:320\n\t" \
    "s_add_u32 m0, s89, 79488\n\t" \
    "s_nop 0\n\t" \
    "global_load_lds_dwordx4 v1, s[50:51] offset:384\n\t" \
    "s_add_u32 m0, s89, 80448\n\t" \
    "s_nop 0\n\t" \
    "global_load_lds_dwordx4 v1, s[50:51] offset:448\n\t" \
    "s_add_u32 m0, s89, 81408\n\t" \
    "s_nop 0\n\t" \
    "global_load_lds_dwordx4 v1, s[50:51] offset:512\n\t" \
    "s_mov_b32 s90, 36864\n\t" \
    "s_cmp_lt_u32 s65, 61\n\t" \
    "s_cselect_b32 s69, 36864, 0\n\t" \
    "s_add_u32 s50, s50, s69\n\t" \
    "s_addc_u32 s51, s51, 0\n\t" \
    "s_add_u32 s90, s90, 36864\n\t" \
    "s_cmp_eq_u32 s90, 147456\n\t" \
    "s_cselect_b32 s90, 0, s90\n\t" \
    "s_add_u32 s91, s90, 36864\n\t" \
    "s_cmp_eq_u32 s91, 147456\n\t" \
    "s_cselect_b32 s91, 0, s91\n\t" \
    "s_add_u32 s91, s91, s89\n\t" \
    "v_add_u32 v17, s90, v3\n\t" \
    "s_branch gx_mid8_%=\n\t" \
    ".p2align 6\n\t" \
    "gx_al9_%=:\n\t" \
    "s_nop 0\n\t" \
    "s_nop 0\n\t" \
    "gx_loop6_%=:\n\t" \
    "s_waitcnt lgkmcnt(5)\n\t" \
    "v_mfma_f32_16x16x32_bf16 v[88:91], a[144:147], v[24:27], 0\n\t" \
    "ds_read_b128 v[48:51], v13 offset:3072\n\t" \
    "s_waitcnt lgkmcnt(5)\n\t" \
    "v_mfma_f32_16x16x32_bf16 v[92:95], a[144:147], v[28:31], 0\n\t" \
    "ds_read_b128 v[52:55], v13 offset:21504\n\t" \
    "v_mfma_f32_16x16x32_bf16 v[96:99], a[216:219], v[24:27], 0\n\t" \
    "s_sub_u32 s70, s65, 1\n\t" \
    "v_add_u32 v157, s70, v11\n\t" \
    "s_cmp_lt_u32 s65, 61\n\t" \
    "v_mfma_f32_16x16x32_bf16 v[100:103], a[216:219], v[28:31], 0\n\t" \
    "s_cselect_b32 s69, 36864, 0\n\t" \
    "s_add_u32 s50, s50, s69\n\t" \
    "s_addc_u32 s51, s51, 0\n\t" \
    "s_waitcnt lgkmcnt(5)\n\t" \
    "v_mfma_f32_16x16x32_bf16 v[88:91], a[148:151], v[32:35], v[88:91]\n\t" \
    "ds_read_b128 v[56:59], v13 offset:4096\n\t" \
    "s_waitcnt lgkmcnt(5)\n\t" \
    "v_mfma_f32_16x16x32_bf16 v[92:95], a[148:151], v[36:39], v[92:95]\n\t" \
    "ds_read_b128 v[60:63], v13 offset:22528\n\t" \
    "v_mfma_f32_16x16x32_bf16 v[96:99], a[220:223], v[32:35], v[96:99]\n\t" \
    "s_add_u32 s90, s90, 36864\n\t" \
    "s_cmp_eq_u32 s90, 147456\n\t" \
    "s_cselect_b32 s90, 0, s90\n\t" \
    "v_mfma_f32_16x16x32_bf16 v[100:103], a[220:223], v[36:39], v[100:103]\n\t" \
    "s_add_u32 s91, s90, 36864\n\t" \
    "s_cmp_eq_u32 s91, 147456\n\t" \
    "s_cselect_b32 s91, 0, s91\n\t" \
    "s_waitcnt lgkmcnt(5)\n\t" \
    "v_mfma_f32_16x16x32_bf16 v[88:91], a[152:155], v[40:43], v[88:91]\n\t" \
    "ds_read_b128 v[64:67], v13 offset:5120\n\t" \
    "s_waitcnt lgkmcnt(5)\n\t" \
    "v_mfma_f32_16x16x32_bf16 v[92:95], a[152:155], v[44:47], v[92:95]\n\t" \
    "ds_read_b128 v[68:71], v13 offset:23552\n\t" \
    "v_mfma_f32_16x16x32_bf16 v[96:99], a[224:227], v[40:43], v[96:99]\n\t" \
    "s_add_u32 s91, s91, s89\n\t" \
    "v_add_u32 v17, s90, v3\n\t" \
    "v_mfma_f32_16x16x32_bf16 v[100:103], a[224:227], v[44:47], v[100:103]\n\t" \
    "s_waitcnt lgkmcnt(5)\n\t" \
    "v_mfma_f32_16x16x32_bf16 v[88:91], a[156:159], v[48:51], v[88:91]\n\t" \
    "ds_read_b128 v[24:27], v13 offset:6144\n\t" \
    "s_waitcnt lgkmcnt(5)\n\t" \
    "v_mfma_f32_16x16x32_bf16 v[92:95], a[156:159], v[52:55], v[92:95]\n\t" \
    "ds_read_b128 v[28:31], v13 offset:24576\n\t" \
    "v_mfma_f32_16x16x32_bf16 v[96:99], a[228:231], v[48:51], v[96:99]\n\t" \
    "s_waitcnt vmcnt(0)\n\t" \
    "s_barrier\n\t" \
    "v_mfma_f32_16x16x32_bf16 v[100:103], a[228:231], v[52:55], v[100:103]\n\t" \
    "s_mov_b64 exec, s[58:59]\n\t" \
    "global_store_dword v10, v157, s[36:37] sc1\n\t" \
    "s_mov_b64 exec, -1\n\t" \
    "s_waitcnt lgkmcnt(5)\n\t" \
    "v_mfma_f32_16x16x32_bf16 v[88:91], a[160:163], v[56:59], v[88:91]\n\t" \
    "ds_read_b128 v[32:35], v13 offset:7168\n\t" \
    "s_waitcnt lgkmcnt(5)\n\t" \
    "v_mfma_f32_16x16x32_bf16 v[92:95], a[160:163], v[60:63], v[92:95]\n\t" \
    "ds_read_b128 v[36:39], v13 offset:25600\n\t" \
    "v_mfma_f32_16x16x32_bf16 v[96:99], a[232:235], v[56:59], v[96:99]\n\t" \
    "v_cvt_pk_bf16_f32 v104, v72, v73\n\t" \
    "v_mfma_f32_16x16x32_bf16 v[100:103], a[232:235], v[60:63], v[100:103]\n\t" \
    "v_cvt_pk_bf16_f32 v105, v74, v75\n\t" \
    "s_waitcnt lgkmcnt(5)\n\t" \
    "v_mfma_f32_16x16x32_bf16 v[88:91], a[164:167], v[64:67], v[88:91]\n\t" \
    "ds_read_b128 v[40:43], v13 offset:8192\n\t" \
    "s_waitcnt lgkmcnt(5)\n\t" \
    "v_mfma_f32_16x16x32_bf16 v[92:95], a[164:167], v[68:71], v[92:95]\n\t" \
    "ds_read_b128 v[44:47], v13 offset:26624\n\t" \
    "v_mfma_f32_16x16x32_bf16 v[96:99], a[236:239], v[64:67], v[96:99]\n\t" \
    "v_cvt_pk_bf16_f32 v106, v80, v81\n\t" \
    "v_mfma_f32_16x16x32_bf16 v[100:103], a[236:239], v[68:71], v[100:103]\n\t" \
    "v_cvt_pk_bf16_f32 v107, v82, v83\n\t" \
    "s_waitcnt lgkmcnt(5)\n\t" \
    "v_mfma_f32_16x16x32_bf16 v[88:91], a[168:171], v[24:27], v[88:91]\n\t" \
    "ds_read_b128 v[48:51], v13 offset:9216\n\t" \
    "s_waitcnt lgkmcnt(5)\n\t" \
    "v_mfma_f32_16x16x32_bf16 v[92:95], a[168:171], v[28:31], v[92:95]\n\t" \
    "ds_read_b128 v[52:55], v13 offset:27648\n\t" \
    "v_mfma_f32_16x16x32_bf16 v[96:99], a[240:243], v[24:27], v[96:99]\n\t" \
    "global_store_dwordx4 v12, v[104:107], s[38:39] offset:0 sc1\n\t" \
    "v_mfma_f32_16x16x32_bf16 v[100:103], a[240:243], v[28:31], v[100:103]\n\t" \
    "v_cvt_pk_bf16_f32 v108, v76, v77\n\t" \
    "s_waitcnt lgkmcnt(5)\n\t" \
    "v_mfma_f32_16x16x32_bf16 v[88:91], a[172:175], v[32:35], v[88:91]\n\t" \
    "ds_read_b128 v[56:59], v13 offset:10240\n\t" \
    "s_waitcnt lgkmcnt(5)\n\t" \
    "v_mfma_f32_16x16x32_bf16 v[92:95], a[172:175], v[36:39], v[92:95]\n\t" \
    "ds_read_b128 v[60:63], v13 offset:28672\n\t" \
    "v_mfma_f32_16x16x32_bf16 v[96:99], a[244:247], v[32:35], v[96:99]\n\t" \
    "v_cvt_pk_bf16_f32 v109, v78, v79\n\t" \
    "v_mfma_f32_16x16x32_bf16 v[100:103], a[244:247], v[36:39], v[100:103]\n\t" \
    "v_cvt_pk_bf16_f32 v110, v84, v85\n\t" \
    "s_waitcnt lgkmcnt(5)\n\t" \
    "v_mfma_f32_16x16x32_bf16 v[88:91], a[176:179], v[40:43], v[88:91]\n\t" \
    "ds_read_b128 v[64:67], v13 offset:11264\n\t" \
    "s_waitcnt lgkmcnt(5)\n\t" \
    "v_mfma_f32_16x16x32_bf16 v[92:95], a[176:179], v[44:47], v[92:95]\n\t" \
    "ds_read_b128 v[68:71], v13 offset:29696\n\t" \
    "v_mfma_f32_16x16x32_bf16 v[96:99], a[248:251], v[40:43], v[96:99]\n\t" \
    "v_cvt_pk_bf16_f32 v111, v86, v87\n\t" \
    "v_mfma_f32_16x16x32_bf16 v[100:103], a[248:251], v[44:47], v[100:103]\n\t" \
    "global_store_dwordx4 v12, v[108:111], s[38:39] offset:1024 sc1\n\t" \
    "s_waitcnt lgkmcnt(5)\n\t" \
    "v_mfma_f32_16x16x32_bf16 v[88:91], a[180:183], v[48:51], v[88:91]\n\t" \
    "ds_read_b128 v[24:27], v13 offset:12288\n\t" \
    "s_waitcnt lgkmcnt(5)\n\t" \
    "v_mfma_f32_16x16x32_bf16 v[92:95], a[180:183], v[52:55], v[92:95]\n\t" \
    "ds_read_b128 v[28:31], v13 offset:30720\n\t" \
    "v_mfma_f32_16x16x32_bf16 v[96:99], a[252:255], v[48:51], v[96:99]\n\t" \
    "s_add_u32 m0, s91, 0\n\t" \
    "v_mfma_f32_16x16x32_bf16 v[100:103], a[252:255], v[52:55], v[100:103]\n\t" \
    "global_load_lds_dwordx4 v1, s[50:51] offset:0\n\t" \
    "s_waitcnt lgkmcnt(5)\n\t" \
    "v_mfma_f32_16x16x32_bf16 v[88:91], a[184:187], v[56:59], v[88:91]\n\t" \
    "ds_read_b128 v[32:35], v13 offset:13312\n\t" \
    "s_waitcnt lgkmcnt(5)\n\t" \
    "v_mfma_f32_16x16x32_bf16 v[92:95], a[184:187], v[60:63], v[92:95]\n\t" \
    "ds_read_b128 v[36:39], v13 offset:31744\n\t" \
    "v_mfma_f32_16x16x32_bf16 v[96:99], v[224:227], v[56:59], v[96:99]\n\t" \
    "s_add_u32 m0, s91, 960\n\t" \
    "v_mfma_f32_16x16x32_bf16 v[100:103], v[224:227], v[60:63], v[100:103]\n\t" \
    "global_load_lds_dwordx4 v1, s[50:51] offset:64\n\t" \
    "s_waitcnt lgkmcnt(5)\n\t" \
    "v_mfma_f32_16x16x32_bf16 v[88:91], a[188:191], v[64:67], v[88:91]\n\t" \
    "ds_read_b128 v[40:43], v13 offset:14336\n\t" \
    "s_waitcnt lgkmcnt(5)\n\t" \
    "v_mfma_f32_16x16x32_bf16 v[92:95], a[188:191], v[68:71], v[92:95]\n\t" \
    "ds_read_b128 v[44:47], v13 offset:32768\n\t" \
    "v_mfma_f32_16x16x32_bf16 v[96:99], v[228:231], v[64:67], v[96:99]\n\t" \
    "s_add_u32 m0, s91, 1920\n\t" \
    "v_mfma_f32_16x16x32_bf16 v[100:103], v[228:231], v[68:71], v[100:103]\n\t" \
    "global_load_lds_dwordx4 v1, s[50:51] offset:128\n\t" \
    "s_waitcnt lgkmcnt(5)\n\t" \
    "v_mfma_f32_16x16x32_bf16 v[88:91], a[192:195], v[24:27], v[88:91]\n\t" \
    "ds_read_b128 v[48:51], v13 offset:15360\n\t" \
    "s_waitcnt lgkmcnt(5)\n\t" \
    "v_mfma_f32_16x16x32_bf16 v[92:95], a[192:195], v[28:31], v[92:95]\n\t" \
    "ds_read_b128 v[52:55], v13 offset:33792\n\t" \
    "v_mfma_f32_16x16x32_bf16 v[96:99], v[232:235], v[24:27], v[96:99]\n\t" \
    "s_add_u32 m0, s91, 2880\n\t" \
    "v_mfma_f32_16x16x32_bf16 v[100:103], v[232:235], v[28:31], v[100:103]\n\t" \
    "global_load_lds_dwordx4 v1, s[50:51] offset:192\n\t" \
    "s_waitcnt lgkmcnt(5)\n\t" \
    "v_mfma_f32_16x16x32_bf16 v[88:91], a[196:199], v[32:35], v[88:91]\n\t" \
    "ds_read_b128 v[56:59], v13 offset:16384\n\t" \
    "s_waitcnt lgkmcnt(5)\n\t" \
    "v_mfma_f32_16x16x32_bf16 v[92:95], a[196:199], v[36:39], v[92:95]\n\t" \
    "ds_read_b128 v[60:63], v13 offset:34816\n\t" \
    "v_mfma_f32_16x16x32_bf16 v[96:99], v[236:239], v[32:35], v[96:99]\n\t" \
    "s_add_u32 m0, s91, 3840\n\t" \
    "v_mfma_f32_16x16x32_bf16 v[100:103], v[236:239], v[36:39], v[100:103]\n\t" \
    "global_load_lds_dwordx4 v1, s[50:51] offset:256\n\t" \
    "s_waitcnt lgkmcnt(5)\n\t" \
    "v_mfma_f32_16x16x32_bf16 v[88:91], a[200:203], v[40:43], v[88:91]\n\t" \
    "ds_read_b128 v[64:67], v13 offset:17408\n\t" \
    "s_waitcnt lgkmcnt(5)\n\t" \
    "v_mfma_f32_16x16x32_bf16 v[92:95], a[200:203], v[44:47], v[92:95]\n\t" \
    "ds_read_b128 v[68:71], v13 offset:35840\n\t" \
    "v_mfma_f32_16x16x32_bf16 v[96:99], v[240:243], v[40:43], v[96:99]\n\t" \
    "s_add_u32 m0, s91, 4800\n\t" \
    "v_mfma_f32_16x16x32_bf16 v[100:103], v[240:243], v[44:47], v[100:103]\n\t" \
    "global_load_lds_dwordx4 v1, s[50:51] offset:320\n\t" \
    "s_waitcnt lgkmcnt(5)\n\t" \
    "v_mfma_f32_16x16x32_bf16 v[88:91], a[204:207], v[48:51], v[88:91]\n\t" \
    "ds_read_b128 v[24:27], v2 offset:0\n\t" \
    "s_waitcnt lgkmcnt(5)\n\t" \
    "v_mfma_f32_16x16x32_bf16 v[92:95], a[204:207], v[52:55], v[92:95]\n\t" \
    "ds_read_b128 v[28:31], v2 offset:18432\n\t" \
    "v_mfma_f32_16x16x32_bf16 v[96:99], v[244:247], v[48:51], v[96:99]\n\t" \
    "s_add_u32 m0, s91, 5760\n\t" \
    "v_mfma_f32_16x16x32_bf16 v[100:103], v[244:247], v[52:55], v[100:103]\n\t" \
    "global_load_lds_dwordx4 v1, s[50:51] offset:384\n\t" \
    "s_waitcnt lgkmcnt(5)\n\t" \
    "v_mfma_f32_16x16x32_bf16 v[88:91], a[208:211], v[56:59], v[88:91]\n\t" \
    "ds_read_b128 v[32:35], v2 offset:1024\n\t" \
    "s_waitcnt lgkmcnt(5)\n\t" \
    "v_mfma_f32_16x16x32_bf16 v[92:95], a[208:211], v[60:63], v[92:95]\n\t" \
    "ds_read_b128 v[36:39], v2 offset:19456\n\t" \
    "v_mfma_f32_16x16x32_bf16 v[96:99], v[248:251], v[56:59], v[96:99]\n\t" \
    "s_add_u32 m0, s91, 6720\n\t" \
    "v_mfma_f32_16x16x32_bf16 v[100:103], v[248:251], v[60:63], v[100:103]\n\t" \
    "global_load_lds_dwordx4 v1, s[50:51] offset:448\n\t" \
    "s_waitcnt lgkmcnt(5)\n\t" \
    "v_mfma_f32_16x16x32_bf16 v[88:91], a[212:215], v[64:67], v[88:91]\n\t" \
    "ds_read_b128 v[40:43], v2 offset:2048\n\t" \
    "s_waitcnt lgkmcnt(5)\n\t" \
    "v_mfma_f32_16x16x32_bf16 v[92:95], a[212:215], v[68:71], v[92:95]\n\t" \
    "ds_read_b128 v[44:47], v2 offset:20480\n\t" \
    "v_mfma_f32_16x16x32_bf16 v[96:99], v[252:255], v[64:67], v[96:99]\n\t" \
    "s_add_u32 m0, s91, 7680\n\t" \
    "v_mfma_f32_16x16x32_bf16 v[100:103], v[252:255], v[68:71], v[100:103]\n\t" \
    "global_load_lds_dwordx4 v1, s[50:51] offset:512\n\t" \
    "gx_mid8_%=:\n\t" \
    "s_waitcnt lgkmcnt(5)\n\t" \
    "v_mfma_f32_16x16x32_bf16 v[72:75], a[0:3], v[24:27], 0\n\t" \
    "ds_read_b128 v[48:51], v2 offset:3072\n\t" \
    "s_waitcnt lgkmcnt(5)\n\t" \
    "v_mfma_f32_16x16x32_bf16 v[76:79], a[0:3], v[28:31], 0\n\t" \
    "ds_read_b128 v[52:55], v2 offset:21504\n\t" \
    "v_mfma_f32_16x16x32_bf16 v[80:83], a[72:75], v[24:27], 0\n\t" \
    "s_lshl_b32 s69, s65, 13\n\t" \
    "v_xor_b32 v12, s69, v8\n\t" \
    "s_add_u32 s69, s69, 8192\n\t" \
    "v_mfma_f32_16x16x32_bf16 v[84:87], a[72:75], v[28:31], 0\n\t" \
    "s_min_u32 s69, s69, 516096\n\t" \
    "v_xor_b32 v9, s69, v8\n\t" \
    "s_waitcnt lgkmcnt(5)\n\t" \
    "v_mfma_f32_16x16x32_bf16 v[72:75], a[4:7], v[32:35], v[72:75]\n\t" \
    "ds_read_b128 v[56:59], v2 offset:4096\n\t" \
    "s_waitcnt lgkmcnt(5)\n\t" \
    "v_mfma_f32_16x16x32_bf16 v[76:79], a[4:7], v[36:39], v[76:79]\n\t" \
    "ds_read_b128 v[60:63], v2 offset:22528\n\t" \
    "v_mfma_f32_16x16x32_bf16 v[80:83], a[76:79], v[32:35], v[80:83]\n\t" \
    "v_mfma_f32_16x16x32_bf16 v[84:87], a[76:79], v[36:39], v[84:87]\n\t" \
    "s_waitcnt lgkmcnt(5)\n\t" \
    "v_mfma_f32_16x16x32_bf16 v[72:75], a[8:11], v[40:43], v[72:75]\n\t" \
    "ds_read_b128 v[64:67], v2 offset:5120\n\t" \
    "s_waitcnt lgkmcnt(5)\n\t" \
    "v_mfma_f32_16x16x32_bf16 v[76:79], a[8:11], v[44:47], v[76:79]\n\t" \
    "ds_read_b128 v[68:71], v2 offset:23552\n\t" \
    "v_mfma_f32_16x16x32_bf16 v[80:83], a[80:83], v[40:43], v[80:83]\n\t" \
    "v_cvt_pk_bf16_f32 v112, v88, v89\n\t" \
    "v_mfma_f32_16x16x32_bf16 v[84:87], a[80:83], v[44:47], v[84:87]\n\t" \
    "v_cvt_pk_bf16_f32 v113, v90, v91\n\t" \
    "s_waitcnt lgkmcnt(5)\n\t" \
    "v_mfma_f32_16x16x32_bf16 v[72:75], a[12:15], v[48:51], v[72:75]\n\t" \
    "ds_read_b128 v[24:27], v2 offset:6144\n\t" \
    "s_waitcnt lgkmcnt(5)\n\t" \
    "v_mfma_f32_16x16x32_bf16 v[76:79], a[12:15], v[52:55], v[76:79]\n\t" \
    "ds_read_b128 v[28:31], v2 offset:24576\n\t" \
    "v_mfma_f32_16x16x32_bf16 v[80:83], a[84:87], v[48:51], v[80:83]\n\t" \
    "v_cvt_pk_bf16_f32 v114, v96, v97\n\t" \
    "v_mfma_f32_16x16x32_bf16 v[84:87], a[84:87], v[52:55], v[84:87]\n\t" \
    "v_cvt_pk_bf16_f32 v115, v98, v99\n\t" \
    "s_waitcnt lgkmcnt(5)\n\t" \
    "v_mfma_f32_16x16x32_bf16 v[72:75], a[16:19], v[56:59], v[72:75]\n\t" \
    "ds_read_b128 v[32:35], v2 offset:7168\n\t" \
    "s_waitcnt lgkmcnt(5)\n\t" \
    "v_mfma_f32_16x16x32_bf16 v[76:79], a[16:19], v[60:63], v[76:79]\n\t" \
    "ds_read_b128 v[36:39], v2 offset:25600\n\t" \
    "v_mfma_f32_16x16x32_bf16 v[80:83], a[88:91], v[56:59], v[80:83]\n\t" \
    "global_store_dwordx4 v9, v[112:115], s[40:41] offset:0 sc1\n\t" \
    "v_mfma_f32_16x16x32_bf16 v[84:87], a[88:91], v[60:63], v[84:87]\n\t" \
    "v_cvt_pk_bf16_f32 v116, v92, v93\n\t" \
    "s_waitcnt lgkmcnt(5)\n\t" \
    "v_mfma_f32_16x16x32_bf16 v[72:75], a[20:23], v[64:67], v[72:75]\n\t" \
    "ds_read_b128 v[40:43], v2 offset:8192\n\t" \
    "s_waitcnt lgkmcnt(5)\n\t" \
    "v_mfma_f32_16x16x32_bf16 v[76:79], a[20:23], v[68:71], v[76:79]\n\t" \
    "ds_read_b128 v[44:47], v2 offset:26624\n\t" \
    "v_mfma_f32_16x16x32_bf16 v[80:83], a[92:95], v[64:67], v[80:83]\n\t" \
    "v_cvt_pk_bf16_f32 v117, v94, v95\n\t" \
    "v_mfma_f32_16x16x32_bf16 v[84:87], a[92:95], v[68:71], v[84:87]\n\t" \
    "v_cvt_pk_bf16_f32 v118, v100, v101\n\t" \
    "s_waitcnt lgkmcnt(5)\n\t" \
    "v_mfma_f32_16x16x32_bf16 v[72:75], a[24:27], v[24:27], v[72:75]\n\t" \
    "ds_read_b128 v[48:51], v2 offset:9216\n\t" \
    "s_waitcnt lgkmcnt(5)\n\t" \
    "v_mfma_f32_16x16x32_bf16 v[76:79], a[24:27], v[28:31], v[76:79]\n\t" \
    "ds_read_b128 v[52:55], v2 offset:27648\n\t" \
    "v_mfma_f32_16x16x32_bf16 v[80:83], a[96:99], v[24:27], v[80:83]\n\t" \
    "v_cvt_pk_bf16_f32 v119, v102, v103\n\t" \
    "v_mfma_f32_16x16x32_bf16 v[84:87], a[96:99], v[28:31], v[84:87]\n\t" \
    "global_store_dwordx4 v9, v[116:119], s[40:41] offset:1024 sc1\n\t" \
    "s_waitcnt lgkmcnt(5)\n\t" \
    "v_mfma_f32_16x16x32_bf16 v[72:75], a[28:31], v[32:35], v[72:75]\n\t" \
    "ds_read_b128 v[56:59], v2 offset:10240\n\t" \
    "s_waitcnt lgkmcnt(5)\n\t" \
    "v_mfma_f32_16x16x32_bf16 v[76:79], a[28:31], v[36:39], v[76:79]\n\t" \
    "ds_read_b128 v[60:63], v2 offset:28672\n\t" \
    "v_mfma_f32_16x16x32_bf16 v[80:83], a[100:103], v[32:35], v[80:83]\n\t" \
    "v_mfma_f32_16x16x32_bf16 v[84:87], a[100:103], v[36:39], v[84:87]\n\t" \
    "s_waitcnt lgkmcnt(5)\n\t" \
    "v_mfma_f32_16x16x32_bf16 v[72:75], a[32:35], v[40:43], v[72:75]\n\t" \
    "ds_read_b128 v[64:67], v2 offset:11264\n\t" \
    "s_waitcnt lgkmcnt(5)\n\t" \
    "v_mfma_f32_16x16x32_bf16 v[76:79], a[32:35], v[44:47], v[76:79]\n\t" \
    "ds_read_b128 v[68:71], v2 offset:29696\n\t" \
    "v_mfma_f32_16x16x32_bf16 v[80:83], a[104:107], v[40:43], v[80:83]\n\t" \
    "v_mfma_f32_16x16x32_bf16 v[84:87], a[104:107], v[44:47], v[84:87]\n\t" \
    "s_waitcnt lgkmcnt(5)\n\t" \
    "v_mfma_f32_16x16x32_bf16 v[72:75], a[36:39], v[48:51], v[72:75]\n\t" \
    "ds_read_b128 v[24:27], v2 offset:12288\n\t" \
    "s_waitcnt lgkmcnt(5)\n\t" \
    "v_mfma_f32_16x16x32_bf16 v[76:79], a[36:39], v[52:55], v[76:79]\n\t" \
    "ds_read_b128 v[28:31], v2 offset:30720\n\t" \
    "v_mfma_f32_16x16x32_bf16 v[80:83], a[108:111], v[48:51], v[80:83]\n\t" \
    "v_mfma_f32_16x16x32_bf16 v[84:87], a[108:111], v[52:55], v[84:87]\n\t" \
    "s_waitcnt lgkmcnt(5)\n\t" \
    "v_mfma_f32_16x16x32_bf16 v[72:75], a[40:43], v[56:59], v[72:75]\n\t" \
    "ds_read_b128 v[32:35], v2 offset:13312\n\t" \
    "s_waitcnt lgkmcnt(5)\n\t" \
    "v_mfma_f32_16x16x32_bf16 v[76:79], a[40:43], v[60:63], v[76:79]\n\t" \
    "ds_read_b128 v[36:39], v2 offset:31744\n\t" \
    "v_mfma_f32_16x16x32_bf16 v[80:83], a[112:115], v[56:59], v[80:83]\n\t" \
    "v_mfma_f32_16x16x32_bf16 v[84:87], a[112:115], v[60:63], v[84:87]\n\t" \
    "s_waitcnt lgkmcnt(5)\n\t" \
    "v_mfma_f32_16x16x32_bf16 v[72:75], a[44:47], v[64:67], v[72:75]\n\t" \
    "ds_read_b128 v[40:43], v2 offset:14336\n\t" \
    "s_waitcnt lgkmcnt(5)\n\t" \
    "v_mfma_f32_16x16x32_bf16 v[76:79], a[44:47], v[68:71], v[76:79]\n\t" \
    "ds_read_b128 v[44:47], v2 offset:32768\n\t" \
    "v_mfma_f32_16x16x32_bf16 v[80:83], a[116:119], v[64:67], v[80:83]\n\t" \
    "v_mfma_f32_16x16x32_bf16 v[84:87], a[116:119], v[68:71], v[84:87]\n\t" \
    "s_waitcnt lgkmcnt(5)\n\t" \
    "v_mfma_f32_16x16x32_bf16 v[72:75], a[48:51], v[24:27], v[72:75]\n\t" \
    "ds_read_b128 v[48:51], v2 offset:15360\n\t" \
    "s_waitcnt lgkmcnt(5)\n\t" \
    "v_mfma_f32_16x16x32_bf16 v[76:79], a[48:51], v[28:31], v[76:79]\n\t" \
    "ds_read_b128 v[52:55], v2 offset:33792\n\t" \
    "v_mfma_f32_16x16x32_bf16 v[80:83], a[120:123], v[24:27], v[80:83]\n\t" \
    "v_mfma_f32_16x16x32_bf16 v[84:87], a[120:123], v[28:31], v[84:87]\n\t" \
    "s_waitcnt lgkmcnt(5)\n\t" \
    "v_mfma_f32_16x16x32_bf16 v[72:75], a[52:55], v[32:35], v[72:75]\n\t" \
    "ds_read_b128 v[56:59], v2 offset:16384\n\t" \
    "s_waitcnt lgkmcnt(5)\n\t" \
    "v_mfma_f32_16x16x32_bf16 v[76:79], a[52:55], v[36:39], v[76:79]\n\t" \
    "ds_read_b128 v[60:63], v2 offset:34816\n\t" \
    "v_mfma_f32_16x16x32_bf16 v[80:83], a[124:127], v[32:35], v[80:83]\n\t" \
    "v_mfma_f32_16x16x32_bf16 v[84:87], a[124:127], v[36:39], v[84:87]\n\t" \
    "s_cmp_eq_u32 s65, 0\n\t" \
    "s_cbranch_scc1 gx_g110_%=\n\t" \
    "gx_g1b11_%=:\n\t" \
    "s_waitcnt lgkmcnt(5)\n\t" \
    "v_mfma_f32_16x16x32_bf16 v[72:75], a[56:59], v[40:43], v[72:75]\n\t" \
    "ds_read_b128 v[64:67], v2 offset:17408\n\t" \
    "s_waitcnt lgkmcnt(5)\n\t" \
    "v_mfma_f32_16x16x32_bf16 v[76:79], a[56:59], v[44:47], v[76:79]\n\t" \
    "ds_read_b128 v[68:71], v2 offset:35840\n\t" \
    "v_mfma_f32_16x16x32_bf16 v[80:83], a[128:131], v[40:43], v[80:83]\n\t" \
    "v_mfma_f32_16x16x32_bf16 v[84:87], a[128:131], v[44:47], v[84:87]\n\t" \
    "s_waitcnt lgkmcnt(5)\n\t" \
    "v_mfma_f32_16x16x32_bf16 v[72:75], a[60:63], v[48:51], v[72:75]\n\t" \
    "ds_read_b128 v[24:27], v17 offset:0\n\t" \
    "s_waitcnt lgkmcnt(5)\n\t" \
    "v_mfma_f32_16x16x32_bf16 v[76:79], a[60:63], v[52:55], v[76:79]\n\t" \
    "ds_read_b128 v[28:31], v17 offset:18432\n\t" \
    "v_mfma_f32_16x16x32_bf16 v[80:83], a[132:135], v[48:51], v[80:83]\n\t" \
    "v_mov_b32 v2, v13\n\t" \
    "v_mfma_f32_16x16x32_bf16 v[84:87], a[132:135], v[52:55], v[84:87]\n\t" \
    "v_mov_b32 v13, v17\n\t" \
    "s_waitcnt lgkmcnt(5)\n\t" \
    "v_mfma_f32_16x16x32_bf16 v[72:75], a[64:67], v[56:59], v[72:75]\n\t" \
    "ds_read_b128 v[32:35], v17 offset:1024\n\t" \
    "s_waitcnt lgkmcnt(5)\n\t" \
    "v_mfma_f32_16x16x32_bf16 v[76:79], a[64:67], v[60:63], v[76:79]\n\t" \
    "ds_read_b128 v[36:39], v17 offset:19456\n\t" \
    "v_mfma_f32_16x16x32_bf16 v[80:83], a[136:139], v[56:59], v[80:83]\n\t" \
    "v_mfma_f32_16x16x32_bf16 v[84:87], a[136:139], v[60:63], v[84:87]\n\t" \
    "s_waitcnt lgkmcnt(5)\n\t" \
    "v_mfma_f32_16x16x32_bf16 v[72:75], a[68:71], v[64:67], v[72:75]\n\t" \
    "ds_read_b128 v[40:43], v17 offset:2048\n\t" \
    "s_waitcnt lgkmcnt(5)\n\t" \
    "v_mfma_f32_16x16x32_bf16 v[76:79], a[68:71], v[68:71], v[76:79]\n\t" \
    "ds_read_b128 v[44:47], v17 offset:20480\n\t" \
    "v_mfma_f32_16x16x32_bf16 v[80:83], a[140:143], v[64:67], v[80:83]\n\t" \
    "s_add_u32 s65, s65, 1\n\t" \
    "s_cmp_lt_u32 s65, 64\n\t" \
    "v_mfma_f32_16x16x32_bf16 v[84:87], a[140:143], v[68:71], v[84:87]\n\t" \
    "s_cbranch_scc1 gx_loop6_%=\n\t" \
    "s_nop 7\n\t" \
    "v_cvt_pk_bf16_f32 v104, v72, v73\n\t" \
    "v_cvt_pk_bf16_f32 v105, v74, v75\n\t" \
    "v_cvt_pk_bf16_f32 v106, v80, v81\n\t" \
    "v_cvt_pk_bf16_f32 v107, v82, v83\n\t" \
    "global_store_dwordx4 v12, v[104:107], s[38:39] offset:0 sc1\n\t" \
    "v_cvt_pk_bf16_f32 v108, v76, v77\n\t" \
    "v_cvt_pk_bf16_f32 v109, v78, v79\n\t" \
    "v_cvt_pk_bf16_f32 v110, v84, v85\n\t" \
    "v_cvt_pk_bf16_f32 v111, v86, v87\n\t" \
    "global_store_dwordx4 v12, v[108:111], s[38:39] offset:1024 sc1\n\t" \
    "s_waitcnt vmcnt(0) lgkmcnt(0)\n\t" \
    "s_barrier\n\t" \
    "s_cmp_lg_u32 s66, 0\n\t" \
    "s_cbranch_scc1 gx_SF12_%=\n\t" \
    "s_lshl_b32 s69, s67, 7\n\t" \
    "s_add_u32 s69, s69, 6098944\n\t" \
    "s_add_u32 s69, s69, s52\n\t" \
    "s_add_u32 s70, s65, 0\n\t" \
    "v_mov_b32 v156, s69\n\t" \
    "v_mov_b32 v157, s70\n\t" \
    "s_add_u32 s69, s69, 61440\n\t" \
    "s_max_i32 s70, s70, 1\n\t" \
    "v_writelane_b32 v156, s69, 1\n\t" \
    "v_writelane_b32 v157, s70, 1\n\t" \
    "s_mov_b64 exec, 3\n\t" \
    "global_store_dword v156, v157, s[36:37] sc1\n\t" \
    "s_mov_b64 exec, -1\n\t" \
    "gx_SF12_%=:\n\t" \
    "s_waitcnt vmcnt(0)\n\t" \
    "s_branch ea_done_%=\n\t" \
    "gx_g110_%=:\n\t" \
    "s_waitcnt vmcnt(2)\n\t" \
    "s_barrier\n\t" \
    "s_add_u32 m0, s91, 0\n\t" \
    "s_nop 0\n\t" \
    "global_load_lds_dwordx4 v1, s[50:51] offset:0\n\t" \
    "s_add_u32 m0, s91, 960\n\t" \
    "s_nop 0\n\t" \
    "global_load_lds_dwordx4 v1, s[50:51] offset:64\n\t" \
    "s_add_u32 m0, s91, 1920\n\t" \
    "s_nop 0\n\t" \
    "global_load_lds_dwordx4 v1, s[50:51] offset:128\n\t" \
    "s_add_u32 m0, s91, 2880\n\t" \
    "s_nop 0\n\t" \
    "global_load_lds_dwordx4 v1, s[50:51] offset:192\n\t" \
    "s_add_u32 m0, s91, 3840\n\t" \
    "s_nop 0\n\t" \
    "global_load_lds_dwordx4 v1, s[50:51] offset:256\n\t" \
    "s_add_u32 m0, s91, 4800\n\t" \
    "s_nop 0\n\t" \
    "global_load_lds_dwordx4 v1, s[50:51] offset:320\n\t" \
    "s_add_u32 m0, s91, 5760\n\t" \
    "s_nop 0\n\t" \
    "global_load_lds_dwordx4 v1, s[50:51] offset:384\n\t" \
    "s_add_u32 m0, s91, 6720\n\t" \
    "s_nop 0\n\t" \
    "global_load_lds_dwordx4 v1, s[50:51] offset:448\n\t" \
    "s_add_u32 m0, s91, 7680\n\t" \
    "s_nop 0\n\t" \
    "global_load_lds_dwordx4 v1, s[50:51] offset:512\n\t" \
    "s_branch gx_g1b11_%=\n\t" \
    "gv_begin_%=:\n\t" \
    "v_mbcnt_lo_u32_b32 v223, -1, 0\n\t" \
    "v_mbcnt_hi_u32_b32 v223, -1, v223\n\t" \
    "s_mov_b32 s68, 262144\n\t" \
    "s_mov_b32 s72, 0\n\t" \
    "s_mov_b32 s83, -1\n\t" \
    "s_mov_b32 s65, 0\n\t" \
    "v_and_b32 v180, 3, v223\n\t" \
    "v_lshlrev_b32 v202, 3, v180\n\t" \
    "v_lshrrev_b32 v181, 5, v223\n\t" \
    "v_lshl_add_u32 v202, v181, 12, v202\n\t" \
    "v_bfe_u32 v182, v223, 2, 2\n\t" \
    "s_mov_b32 s70, 288\n\t" \
    "v_mad_u32_u24 v202, v182, s70, v202\n\t" \
    "v_bfe_u32 v183, v223, 4, 1\n\t" \
    "s_mov_b32 s70, 2176\n\t" \
    "v_mad_u32_u24 v202, v183, s70, v202\n\t" \
    "s_lshl_b32 s69, s66, 6\n\t" \
    "v_xor_b32 v202, s69, v202\n\t" \
    "v_xor_b32 v203, 32, v202\n\t" \
    "s_mul_i32 s69, s66, 9216\n\t" \
    "v_and_b32 v184, 1, v223\n\t" \
    "v_lshl_add_u32 v201, v184, 4, s69\n\t" \
    "v_lshrrev_b32 v185, 3, v223\n\t" \
    "s_mov_b32 s70, 1152\n\t" \
    "v_mad_u32_u24 v201, v185, s70, v201\n\t" \
    "v_bfe_u32 v186, v223, 1, 2\n\t" \
    "v_lshlrev_b32 v204, 5, v186\n\t" \
    "v_lshrrev_b32 v187, 4, v223\n\t" \
    "v_lshlrev_b32 v205, 5, v187\n\t" \
    "v_xor_b32 v204, v204, v205\n\t" \
    "v_add_u32 v201, v201, v204\n\t" \
    "s_mul_i32 s96, s66, 9216\n\t" \
    "v_lshlrev_b32 v207, 3, v180\n\t" \
    "v_bfe_u32 v188, v223, 2, 1\n\t" \
    "v_lshl_add_u32 v207, v188, 7, v207\n\t" \
    "v_bfe_u32 v189, v223, 3, 2\n\t" \
    "s_mov_b32 s70, 288\n\t" \
    "v_mad_u32_u24 v207, v189, s70, v207\n\t" \
    "s_mov_b32 s70, 9216\n\t" \
    "v_mad_u32_u24 v207, v181, s70, v207\n\t" \
    "s_mul_i32 s69, s66, 9\n\t" \
    "s_add_u32 s69, s69, 0\n\t" \
    "s_and_b32 s70, s69, 3\n\t" \
    "s_lshl_b32 s70, s70, 5\n\t" \
    "s_lshr_b32 s69, s69, 2\n\t" \
    "s_lshl_b32 s69, s69, 10\n\t" \
    "v_xor_b32 v204, s70, v207\n\t" \
    "v_add_u32 v204, s69, v204\n\t" \
    "s_mul_i32 s69, s66, 9\n\t" \
    "s_add_u32 s69, s69, 1\n\t" \
    "s_and_b32 s70, s69, 3\n\t" \
    "s_lshl_b32 s70, s70, 5\n\t" \
    "s_lshr_b32 s69, s69, 2\n\t" \
    "s_lshl_b32 s69, s69, 10\n\t" \
    "v_xor_b32 v205, s70, v207\n\t" \
    "v_add_u32 v205, s69, v205\n\t" \
    "s_mul_i32 s69, s66, 9\n\t" \
    "s_add_u32 s69, s69, 2\n\t" \
    "s_and_b32 s70, s69, 3\n\t" \
    "s_lshl_b32 s70, s70, 5\n\t" \
    "s_lshr_b32 s69, s69, 2\n\t" \
    "s_lshl_b32 s69, s69, 10\n\t" \
    "v_xor_b32 v206, s70, v207\n\t" \
    "v_add_u32 v206, s69, v206\n\t" \
    "s_mul_i32 s69, s66, 9\n\t" \
    "s_add_u32 s69, s69, 3\n\t" \
    "s_and_b32 s70, s69, 3\n\t" \
    "s_lshl_b32 s70, s70, 5\n\t" \
    "s_lshr_b32 s69, s69, 2\n\t" \
    "s_lshl_b32 s69, s69, 10\n\t" \
    "v_xor_b32 v207, s70, v207\n\t" \
    "v_add_u32 v207, s69, v207\n\t" \
    "s_waitcnt vmcnt(12) lgkmcnt(0)\n\t" \
    "s_barrier\n\t" \
    "s_add_u32 m0, s82, 65536\n\t" \
    "s_nop 0\n\t" \
    "global_load_lds_dwordx4 v200, s[42:43]\n\t" \
    "v_add_u32 v200, 131072, v200\n\t" \
    "s_add_u32 m0, s82, 69632\n\t" \
    "s_nop 0\n\t" \
    "global_load_lds_dwordx4 v200, s[42:43]\n\t" \
    "v_add_u32 v200, 131072, v200\n\t" \
    "s_add_u32 m0, s82, 73728\n\t" \
    "s_nop 0\n\t" \
    "global_load_lds_dwordx4 v200, s[42:43]\n\t" \
    "v_add_u32 v200, 131072, v200\n\t" \
    "s_add_u32 m0, s82, 77824\n\t" \
    "s_nop 0\n\t" \
    "global_load_lds_dwordx4 v200, s[42:43]\n\t" \
    "v_add_u32 v200, 131072, v200\n\t" \
    "ds_read_b64_tr_b16 a[0:1], v202 offset:36864\n\t" \
    "ds_read_b64_tr_b16 a[2:3], v202 offset:37888\n\t" \
    "ds_read_b64_tr_b16 a[4:5], v202 offset:45056\n\t" \
    "ds_read_b64_tr_b16 a[6:7], v202 offset:46080\n\t" \
    "ds_read_b64_tr_b16 a[72:73], v203 offset:36864\n\t" \
    "ds_read_b64_tr_b16 a[74:75], v203 offset:37888\n\t" \
    "ds_read_b64_tr_b16 a[76:77], v203 offset:45056\n\t" \
    "ds_read_b64_tr_b16 a[78:79], v203 offset:46080\n\t" \
    "v_lshlrev_b32 v3, 4, v180\n\t" \
    "v_lshlrev_b32 v2, 5, v181\n\t" \
    "v_xor_b32 v3, v3, v2\n\t" \
    "s_bfe_u32 s71, s66, 0x10000\n\t" \
    "s_mul_i32 s69, s71, 576\n\t" \
    "s_bfe_u32 s71, s66, 0x10001\n\t" \
    "s_mul_i32 s71, s71, 18432\n\t" \
    "s_add_u32 s69, s69, s71\n\t" \
    "v_lshrrev_b32 v190, 2, v223\n\t" \
    "v_mov_b32 v1, s69\n\t" \
    "s_mov_b32 s70, 1152\n\t" \
    "v_mad_u32_u24 v1, v190, s70, v1\n\t" \
    "v_add_u32 v1, v1, v3\n\t" \
    "v_and_b32 v191, 15, v223\n\t" \
    "v_lshlrev_b32 v3, 6, v191\n\t" \
    "v_lshl_add_u32 v3, v187, 4, v3\n\t" \
    "v_bfe_u32 v192, v223, 3, 1\n\t" \
    "v_lshlrev_b32 v2, 5, v192\n\t" \
    "v_xor_b32 v3, v3, v2\n\t" \
    "v_mov_b32 v2, v3\n\t" \
    "s_lshr_b32 s65, s67, 5\n\t" \
    "s_mul_i32 s69, s65, 36864\n\t" \
    "s_add_u32 s50, s50, s69\n\t" \
    "s_addc_u32 s51, s51, 0\n\t" \
    "s_add_u32 s38, s36, 48234496\n\t" \
    "s_addc_u32 s39, s37, 0\n\t" \
    "s_mul_i32 s89, s66, 9216\n\t" \
    "s_mov_b32 s90, 0\n\t" \
    "s_waitcnt vmcnt(12) lgkmcnt(0)\n\t" \
    "s_barrier\n\t" \
    "s_add_u32 m0, s82, 81920\n\t" \
    "s_nop 0\n\t" \
    "global_load_lds_dwordx4 v200, s[42:43]\n\t" \
    "v_add_u32 v200, 131072, v200\n\t" \
    "s_add_u32 m0, s82, 86016\n\t" \
    "s_nop 0\n\t" \
    "global_load_lds_dwordx4 v200, s[42:43]\n\t" \
    "v_add_u32 v200, 131072, v200\n\t" \
    "s_add_u32 m0, s82, 90112\n\t" \
    "s_nop 0\n\t" \
    "global_load_lds_dwordx4 v200, s[42:43]\n\t" \
    "v_add_u32 v200, 131072, v200\n\t" \
    "s_add_u32 m0, s82, 94208\n\t" \
    "s_nop 0\n\t" \
    "global_load_lds_dwordx4 v200, s[42:43]\n\t" \
    "v_add_u32 v200, 131072, v200\n\t" \
    "ds_read_b64_tr_b16 a[8:9], v202 offset:53248\n\t" \
    "ds_read_b64_tr_b16 a[10:11], v202 offset:54272\n\t" \
    "ds_read_b64_tr_b16 a[12:13], v202 offset:61440\n\t" \
    "ds_read_b64_tr_b16 a[14:15], v202 offset:62464\n\t" \
    "ds_read_b64_tr_b16 a[80:81], v203 offset:53248\n\t" \
    "ds_read_b64_tr_b16 a[82:83], v203 offset:54272\n\t" \
    "ds_read_b64_tr_b16 a[84:85], v203 offset:61440\n\t" \
    "ds_read_b64_tr_b16 a[86:87], v203 offset:62464\n\t" \
    "v_lshlrev_b32 v0, 4, v223\n\t" \
    "s_mul_i32 s69, s66, 2048\n\t" \
    "s_add_u32 s69, s69, 147456\n\t" \
    "v_lshl_add_u32 v4, v223, 4, s69\n\t" \
    "s_mov_b32 s69, 147456\n\t" \
    "v_lshl_add_u32 v5, v223, 4, s69\n\t" \
    "v_lshlrev_b32 v6, 2, v223\n\t" \
    "v_mov_b32 v7, 0\n\t" \
    "s_cmp_eq_u32 s66, 0\n\t" \
    "s_cselect_b32 s58, 31, 0\n\t" \
    "s_mov_b32 s59, 0\n\t" \
    "s_waitcnt vmcnt(12) lgkmcnt(0)\n\t" \
    "s_barrier\n\t" \
    "s_add_u32 m0, s82, 98304\n\t" \
    "s_nop 0\n\t" \
    "global_load_lds_dwordx4 v200, s[42:43]\n\t" \
    "v_add_u32 v200, 131072, v200\n\t" \
    "s_add_u32 m0, s82, 102400\n\t" \
    "s_nop 0\n\t" \
    "global_load_lds_dwordx4 v200, s[42:43]\n\t" \
    "v_add_u32 v200, 131072, v200\n\t" \
    "s_add_u32 m0, s82, 106496\n\t" \
    "s_nop 0\n\t" \
    "global_load_lds_dwordx4 v200, s[42:43]\n\t" \
    "v_add_u32 v200, 131072, v200\n\t" \
    "s_add_u32 m0, s82, 110592\n\t" \
    "s_nop 0\n\t" \
    "global_load_lds_dwordx4 v200, s[42:43]\n\t" \
    "v_add_u32 v200, 131072, v200\n\t" \
    "v_add_u32 v202, 69632, v202\n\t" \
    "v_add_u32 v203, 69632, v203\n\t" \
    "ds_read_b64_tr_b16 a[16:17], v202 offset:0\n\t" \
    "ds_read_b64_tr_b16 a[18:19], v202 offset:1024\n\t" \
    "ds_read_b64_tr_b16 a[20:21], v202 offset:8192\n\t" \
    "ds_read_b64_tr_b16 a[22:23], v202 offset:9216\n\t" \
    "ds_read_b64_tr_b16 a[88:89], v203 offset:0\n\t" \
    "ds_read_b64_tr_b16 a[90:91], v203 offset:1024\n\t" \
    "ds_read_b64_tr_b16 a[92:93], v203 offset:8192\n\t" \
    "ds_read_b64_tr_b16 a[94:95], v203 offset:9216\n\t" \
    "s_bfe_u32 s71, s67, 0x50000\n\t" \
    "s_mul_i32 s69, s71, 32\n\t" \
    "s_add_u32 s69, s69, 262148\n\t" \
    "v_lshl_add_u32 v19, v180, 14, s69\n\t" \
    "v_lshl_add_u32 v19, v181, 13, v19\n\t" \
    "v_bfe_u32 v193, v223, 2, 3\n\t" \
    "v_lshlrev_b32 v199, 10, v193\n\t" \
    "v_min_u32 v199, 4096, v199\n\t" \
    "v_add_u32 v19, v199, v19\n\t" \
    "s_mul_i32 s69, s67, 32\n\t" \
    "s_add_u32 s69, s69, 6193152\n\t" \
    "v_lshl_add_u32 v20, v180, 11, s69\n\t" \
    "s_lshl_b32 s69, s67, 7\n\t" \
    "s_add_u32 s69, s69, 6111232\n\t" \
    "v_writelane_b32 v20, s69, 4\n\t" \
    "s_bfe_u32 s71, s67, 0x50000\n\t" \
    "s_mul_i32 s69, s71, 1024\n\t" \
    "v_lshl_add_u32 v17, v223, 4, s69\n\t" \
    "s_mul_i32 s69, s66, 9\n\t" \
    "s_add_u32 s69, s69, 0\n\t" \
    "s_mul_i32 s70, s69, 7\n\t" \
    "s_and_b32 s70, s70, 63\n\t" \
    "s_lshl_b32 s70, s70, 15\n\t" \
    "s_mul_i32 s69, s69, 2097152\n\t" \
    "s_add_u32 s69, s69, s70\n\t" \
    "v_add_u32 v8, s69, v17\n\t" \
    "s_mul_i32 s69, s66, 9\n\t" \
    "s_add_u32 s69, s69, 1\n\t" \
    "s_mul_i32 s70, s69, 7\n\t" \
    "s_and_b32 s70, s70, 63\n\t" \
    "s_lshl_b32 s70, s70, 15\n\t" \
    "s_mul_i32 s69, s69, 2097152\n\t" \
    "s_add_u32 s69, s69, s70\n\t" \
    "v_add_u32 v9, s69, v17\n\t" \
    "s_mul_i32 s69, s66, 9\n\t" \
    "s_add_u32 s69, s69, 2\n\t" \
    "s_mul_i32 s70, s69, 7\n\t" \
    "s_and_b32 s70, s70, 63\n\t" \
    "s_lshl_b32 s70, s70, 15\n\t" \
    "s_mul_i32 s69, s69, 2097152\n\t" \
    "s_add_u32 s69, s69, s70\n\t" \
    "v_add_u32 v10, s69, v17\n\t" \
    "s_mul_i32 s69, s66, 9\n\t" \
    "s_add_u32 s69, s69, 3\n\t" \
    "s_mul_i32 s70, s69, 7\n\t" \
    "s_and_b32 s70, s70, 63\n\t" \
    "s_lshl_b32 s70, s70, 15\n\t" \
    "s_mul_i32 s69, s69, 2097152\n\t" \
    "s_add_u32 s69, s69, s70\n\t" \
    "v_add_u32 v11, s69, v17\n\t" \
    "s_mul_i32 s69, s66, 9\n\t" \
    "s_add_u32 s69, s69, 4\n\t" \
    "s_mul_i32 s70, s69, 7\n\t" \
    "s_and_b32 s70, s70, 63\n\t" \
    "s_lshl_b32 s70, s70, 15\n\t" \
    "s_mul_i32 s69, s69, 2097152\n\t" \
    "s_add_u32 s69, s69, s70\n\t" \
    "v_add_u32 v12, s69, v17\n\t" \
    "s_mul_i32 s69, s66, 9\n\t" \
    "s_add_u32 s69, s69, 5\n\t" \
    "s_mul_i32 s70, s69, 7\n\t" \
    "s_and_b32 s70, s70, 63\n\t" \
    "s_lshl_b32 s70, s70, 15\n\t" \
    "s_mul_i32 s69, s69, 2097152\n\t" \
    "s_add_u32 s69, s69, s70\n\t" \
    "v_add_u32 v13, s69, v17\n\t" \
    "s_mul_i32 s69, s66, 9\n\t" \
    "s_add_u32 s69, s69, 6\n\t" \
    "s_mul_i32 s70, s69, 7\n\t" \
    "s_and_b32 s70, s70, 63\n\t" \
    "s_lshl_b32 s70, s70, 15\n\t" \
    "s_mul_i32 s69, s69, 2097152\n\t" \
    "s_add_u32 s69, s69, s70\n\t" \
    "v_add_u32 v14, s69, v17\n\t" \
    "s_mul_i32 s69, s66, 9\n\t" \
    "s_add_u32 s69, s69, 7\n\t" \
    "s_mul_i32 s70, s69, 7\n\t" \
    "s_and_b32 s70, s70, 63\n\t" \
    "s_lshl_b32 s70, s70, 15\n\t" \
    "s_mul_i32 s69, s69, 2097152\n\t" \
    "s_add_u32 s69, s69, s70\n\t" \
    "v_add_u32 v15, s69, v17\n\t" \
    "s_mul_i32 s69, s66, 9\n\t" \
    "s_add_u32 s69, s69, 8\n\t" \
    "s_mul_i32 s70, s69, 7\n\t" \
    "s_and_b32 s70, s70, 63\n\t" \
    "s_lshl_b32 s70, s70, 15\n\t" \
    "s_mul_i32 s69, s69, 2097152\n\t" \
    "s_add_u32 s69, s69, s70\n\t" \
    "v_add_u32 v16, s69, v17\n\t" \
    "s_waitcnt vmcnt(12) lgkmcnt(0)\n\t" \
    "s_barrier\n\t" \
    "s_add_u32 m0, s82, 0\n\t" \
    "s_nop 0\n\t" \
    "global_load_lds_dwordx4 v200, s[42:43]\n\t" \
    "v_add_u32 v200, 131072, v200\n\t" \
    "s_add_u32 m0, s82, 4096\n\t" \
    "s_nop 0\n\t" \
    "global_load_lds_dwordx4 v200, s[42:43]\n\t" \
    "v_add_u32 v200, 131072, v200\n\t" \
    "s_add_u32 m0, s82, 8192\n\t" \
    "s_nop 0\n\t" \
    "global_load_lds_dwordx4 v200, s[42:43]\n\t" \
    "v_add_u32 v200, 131072, v200\n\t" \
    "s_add_u32 m0, s82, 12288\n\t" \
    "s_nop 0\n\t" \
    "global_load_lds_dwordx4 v200, s[42:43]\n\t" \
    "v_add_u32 v200, 131072, v200\n\t" \
    "ds_read_b64_tr_b16 a[24:25], v202 offset:16384\n\t" \
    "ds_read_b64_tr_b16 a[26:27], v202 offset:17408\n\t" \
    "ds_read_b64_tr_b16 a[28:29], v202 offset:24576\n\t" \
    "ds_read_b64_tr_b16 a[30:31], v202 offset:25600\n\t" \
    "ds_read_b64_tr_b16 a[96:97], v203 offset:16384\n\t" \
    "ds_read_b64_tr_b16 a[98:99], v203 offset:17408\n\t" \
    "ds_read_b64_tr_b16 a[100:101], v203 offset:24576\n\t" \
    "ds_read_b64_tr_b16 a[102:103], v203 offset:25600\n\t" \
    "s_waitcnt vmcnt(12) lgkmcnt(0)\n\t" \
    "s_barrier\n\t" \
    "s_add_u32 m0, s82, 16384\n\t" \
    "s_nop 0\n\t" \
    "global_load_lds_dwordx4 v200, s[42:43]\n\t" \
    "v_add_u32 v200, 131072, v200\n\t" \
    "s_add_u32 m0, s82, 20480\n\t" \
    "s_nop 0\n\t" \
    "global_load_lds_dwordx4 v200, s[42:43]\n\t" \
    "v_add_u32 v200, 131072, v200\n\t" \
    "s_add_u32 m0, s82, 24576\n\t" \
    "s_nop 0\n\t" \
    "global_load_lds_dwordx4 v200, s[42:43]\n\t" \
    "v_add_u32 v200, 131072, v200\n\t" \
    "s_add_u32 m0, s82, 28672\n\t" \
    "s_nop 0\n\t" \
    "global_load_lds_dwordx4 v200, s[42:43]\n\t" \
    "v_add_u32 v200, 131072, v200\n\t" \
    "s_add_u32 m0, s89, 0\n\t" \
    "s_nop 0\n\t" \
    "global_load_lds_dwordx4 v1, s[50:51] offset:0\n\t" \
    "s_add_u32 m0, s89, 960\n\t" \
    "s_nop 0\n\t" \
    "global_load_lds_dwordx4 v1, s[50:51] offset:64\n\t" \
    "s_add_u32 m0, s89, 1920\n\t" \
    "s_nop 0\n\t" \
    "global_load_lds_dwordx4 v1, s[50:51] offset:128\n\t" \
    "s_add_u32 m0, s89, 2880\n\t" \
    "s_nop 0\n\t" \
    "global_load_lds_dwordx4 v1, s[50:51] offset:192\n\t" \
    "s_add_u32 m0, s89, 3840\n\t" \
    "s_nop 0\n\t" \
    "global_load_lds_dwordx4 v1, s[50:51] offset:256\n\t" \
    "s_add_u32 m0, s89, 4800\n\t" \
    "s_nop 0\n\t" \
    "global_load_lds_dwordx4 v1, s[50:51] offset:320\n\t" \
    "s_add_u32 m0, s89, 5760\n\t" \
    "s_nop 0\n\t" \
    "global_load_lds_dwordx4 v1, s[50:51] offset:384\n\t" \
    "s_add_u32 m0, s89, 6720\n\t" \
    "s_nop 0\n\t" \
    "global_load_lds_dwordx4 v1, s[50:51] offset:448\n\t" \
    "s_add_u32 m0, s89, 7680\n\t" \
    "s_nop 0\n\t" \
    "global_load_lds_dwordx4 v1, s[50:51] offset:512\n\t" \
    "ds_read_b64_tr_b16 a[32:33], v202 offset:32768\n\t" \
    "ds_read_b64_tr_b16 a[34:35], v202 offset:33792\n\t" \
    "ds_read_b64_tr_b16 a[36:37], v202 offset:40960\n\t" \
    "ds_read_b64_tr_b16 a[38:39], v202 offset:41984\n\t" \
    "ds_read_b64_tr_b16 a[104:105], v203 offset:32768\n\t" \
    "ds_read_b64_tr_b16 a[106:107], v203 offset:33792\n\t" \
    "ds_read_b64_tr_b16 a[108:109], v203 offset:40960\n\t" \
    "ds_read_b64_tr_b16 a[110:111], v203 offset:41984\n\t" \
    "s_waitcnt vmcnt(21) lgkmcnt(0)\n\t" \
    "s_barrier\n\t" \
    "s_add_u32 m0, s96, 73728\n\t" \
    "s_nop 0\n\t" \
    "global_load_lds_dwordx4 v201, s[44:45] offset:0\n\t" \
    "s_add_u32 m0, s96, 74624\n\t" \
    "s_nop 0\n\t" \
    "global_load_lds_dwordx4 v201, s[44:45] offset:128\n\t" \
    "s_add_u32 m0, s96, 75520\n\t" \
    "s_nop 0\n\t" \
    "global_load_lds_dwordx4 v201, s[44:45] offset:256\n\t" \
    "s_add_u32 m0, s96, 76416\n\t" \
    "s_nop 0\n\t" \
    "global_load_lds_dwordx4 v201, s[44:45] offset:384\n\t" \
    "s_add_u32 m0, s96, 77312\n\t" \
    "s_nop 0\n\t" \
    "global_load_lds_dwordx4 v201, s[44:45] offset:512\n\t" \
    "s_add_u32 m0, s96, 78208\n\t" \
    "s_nop 0\n\t" \
    "global_load_lds_dwordx4 v201, s[44:45] offset:640\n\t" \
    "s_add_u32 m0, s96, 79104\n\t" \
    "s_nop 0\n\t" \
    "global_load_lds_dwordx4 v201, s[44:45] offset:768\n\t" \
    "s_add_u32 m0, s96, 80000\n\t" \
    "s_nop 0\n\t" \
    "global_load_lds_dwordx4 v201, s[44:45] offset:896\n\t" \
    "s_add_u32 m0, s96, 80896\n\t" \
    "s_nop 0\n\t" \
    "global_load_lds_dwordx4 v201, s[44:45] offset:1024\n\t" \
    "v_add_u32 v201, 36864, v201\n\t" \
    "ds_read_b64_tr_b16 a[40:41], v202 offset:49152\n\t" \
    "ds_read_b64_tr_b16 a[42:43], v202 offset:50176\n\t" \
    "ds_read_b64_tr_b16 a[44:45], v202 offset:57344\n\t" \
    "ds_read_b64_tr_b16 a[46:47], v202 offset:58368\n\t" \
    "ds_read_b64_tr_b16 a[112:113], v203 offset:49152\n\t" \
    "ds_read_b64_tr_b16 a[114:115], v203 offset:50176\n\t" \
    "ds_read_b64_tr_b16 a[116:117], v203 offset:57344\n\t" \
    "ds_read_b64_tr_b16 a[118:119], v203 offset:58368\n\t" \
    "s_waitcnt vmcnt(26) lgkmcnt(0)\n\t" \
    "s_barrier\n\t" \
    "v_add_u32 v202, 65536, v202\n\t" \
    "v_add_u32 v203, 65536, v203\n\t" \
    "ds_read_b64_tr_b16 a[48:49], v202 offset:0\n\t" \
    "ds_read_b64_tr_b16 a[50:51], v202 offset:1024\n\t" \
    "ds_read_b64_tr_b16 a[52:53], v202 offset:8192\n\t" \
    "ds_read_b64_tr_b16 a[54:55], v202 offset:9216\n\t" \
    "ds_read_b64_tr_b16 a[120:121], v203 offset:0\n\t" \
    "ds_read_b64_tr_b16 a[122:123], v203 offset:1024\n\t" \
    "ds_read_b64_tr_b16 a[124:125], v203 offset:8192\n\t" \
    "ds_read_b64_tr_b16 a[126:127], v203 offset:9216\n\t" \
    "s_waitcnt vmcnt(22) lgkmcnt(0)\n\t" \
    "s_barrier\n\t" \
    "s_add_u32 m0, s96, 110592\n\t" \
    "s_nop 0\n\t" \
    "global_load_lds_dwordx4 v201, s[44:45] offset:0\n\t" \
    "s_add_u32 m0, s96, 111488\n\t" \
    "s_nop 0\n\t" \
    "global_load_lds_dwordx4 v201, s[44:45] offset:128\n\t" \
    "s_add_u32 m0, s96, 112384\n\t" \
    "s_nop 0\n\t" \
    "global_load_lds_dwordx4 v201, s[44:45] offset:256\n\t" \
    "s_add_u32 m0, s96, 113280\n\t" \
    "s_nop 0\n\t" \
    "global_load_lds_dwordx4 v201, s[44:45] offset:384\n\t" \
    "s_add_u32 m0, s96, 114176\n\t" \
    "s_nop 0\n\t" \
    "global_load_lds_dwordx4 v201, s[44:45] offset:512\n\t" \
    "s_add_u32 m0, s96, 115072\n\t" \
    "s_nop 0\n\t" \
    "global_load_lds_dwordx4 v201, s[44:45] offset:640\n\t" \
    "s_add_u32 m0, s96, 115968\n\t" \
    "s_nop 0\n\t" \
    "global_load_lds_dwordx4 v201, s[44:45] offset:768\n\t" \
    "s_add_u32 m0, s96, 116864\n\t" \
    "s_nop 0\n\t" \
    "global_load_lds_dwordx4 v201, s[44:45] offset:896\n\t" \
    "s_add_u32 m0, s96, 117760\n\t" \
    "s_nop 0\n\t" \
    "global_load_lds_dwordx4 v201, s[44:45] offset:1024\n\t" \
    "v_add_u32 v201, 36864, v201\n\t" \
    "v_add_u32 v202, -98304, v202\n\t" \
    "v_add_u32 v203, -98304, v203\n\t" \
    "ds_read_b64_tr_b16 a[56:57], v202 offset:0\n\t" \
    "ds_read_b64_tr_b16 a[58:59], v202 offset:1024\n\t" \
    "ds_read_b64_tr_b16 a[60:61], v202 offset:8192\n\t" \
    "ds_read_b64_tr_b16 a[62:63], v202 offset:9216\n\t" \
    "ds_read_b64_tr_b16 a[128:129], v203 offset:0\n\t" \
    "ds_read_b64_tr_b16 a[130:131], v203 offset:1024\n\t" \
    "ds_read_b64_tr_b16 a[132:133], v203 offset:8192\n\t" \
    "ds_read_b64_tr_b16 a[134:135], v203 offset:9216\n\t" \
    "s_waitcnt vmcnt(27) lgkmcnt(0)\n\t" \
    "s_barrier\n\t" \
    "ds_read_b64_tr_b16 a[64:65], v202 offset:16384\n\t" \
    "ds_read_b64_tr_b16 a[66:67], v202 offset:17408\n\t" \
    "ds_read_b64_tr_b16 a[68:69], v202 offset:24576\n\t" \
    "ds_read_b64_tr_b16 a[70:71], v202 offset:25600\n\t" \
    "ds_read_b64_tr_b16 a[136:137], v203 offset:16384\n\t" \
    "ds_read_b64_tr_b16 a[138:139], v203 offset:17408\n\t" \
    "ds_read_b64_tr_b16 a[140:141], v203 offset:24576\n\t" \
    "ds_read_b64_tr_b16 a[142:143], v203 offset:25600\n\t" \
    "s_waitcnt vmcnt(9) lgkmcnt(0)\n\t" \
    "s_barrier\n\t" \
    "s_add_u32 m0, s96, 36864\n\t" \
    "s_nop 0\n\t" \
    "global_load_lds_dwordx4 v201, s[44:45] offset:0\n\t" \
    "s_add_u32 m0, s96, 37760\n\t" \
    "s_nop 0\n\t" \
    "global_load_lds_dwordx4 v201, s[44:45] offset:128\n\t" \
    "s_add_u32 m0, s96, 38656\n\t" \
    "s_nop 0\n\t" \
    "global_load_lds_dwordx4 v201, s[44:45] offset:256\n\t" \
    "s_add_u32 m0, s96, 39552\n\t" \
    "s_nop 0\n\t" \
    "global_load_lds_dwordx4 v201, s[44:45] offset:384\n\t" \
    "s_add_u32 m0, s96, 40448\n\t" \
    "s_nop 0\n\t" \
    "global_load_lds_dwordx4 v201, s[44:45] offset:512\n\t" \
    "s_add_u32 m0, s96, 41344\n\t" \
    "s_nop 0\n\t" \
    "global_load_lds_dwordx4 v201, s[44:45] offset:640\n\t" \
    "s_add_u32 m0, s96, 42240\n\t" \
    "s_nop 0\n\t" \
    "global_load_lds_dwordx4 v201, s[44:45] offset:768\n\t" \
    "s_add_u32 m0, s96, 43136\n\t" \
    "s_nop 0\n\t" \
    "global_load_lds_dwordx4 v201, s[44:45] offset:896\n\t" \
    "s_add_u32 m0, s96, 44032\n\t" \
    "s_nop 0\n\t" \
    "global_load_lds_dwordx4 v201, s[44:45] offset:1024\n\t" \
    "v_add_u32 v201, 36864, v201\n\t" \
    "v_add_u32 v204, 73728, v204\n\t" \
    "v_add_u32 v205, 73728, v205\n\t" \
    "v_add_u32 v206, 73728, v206\n\t" \
    "v_add_u32 v207, 73728, v207\n\t" \
    "ds_read_b64_tr_b16 a[144:145], v204 offset:0\n\t" \
    "ds_read_b64_tr_b16 a[146:147], v204 offset:18432\n\t" \
    "ds_read_b64_tr_b16 a[160:161], v205 offset:0\n\t" \
    "ds_read_b64_tr_b16 a[162:163], v205 offset:18432\n\t" \
    "ds_read_b64_tr_b16 a[176:177], v206 offset:0\n\t" \
    "ds_read_b64_tr_b16 a[178:179], v206 offset:18432\n\t" \
    "s_waitcnt lgkmcnt(6)\n\t" \
    "ds_read_b64_tr_b16 a[192:193], v207 offset:0\n\t" \
    "ds_read_b64_tr_b16 a[194:195], v207 offset:18432\n\t" \
    "ds_read_b64_tr_b16 a[208:209], v204 offset:1024\n\t" \
    "ds_read_b64_tr_b16 a[210:211], v204 offset:19456\n\t" \
    "ds_read_b64_tr_b16 a[224:225], v205 offset:1024\n\t" \
    "ds_read_b64_tr_b16 a[226:227], v205 offset:19456\n\t" \
    "s_waitcnt lgkmcnt(6)\n\t" \
    "ds_read_b64_tr_b16 a[240:241], v206 offset:1024\n\t" \
    "ds_read_b64_tr_b16 a[242:243], v206 offset:19456\n\t" \
    "ds_read_b64_tr_b16 v[224:225], v207 offset:1024\n\t" \
    "ds_read_b64_tr_b16 v[226:227], v207 offset:19456\n\t" \
    "ds_read_b64_tr_b16 v[240:241], v204 offset:2048\n\t" \
    "ds_read_b64_tr_b16 v[242:243], v204 offset:20480\n\t" \
    "s_waitcnt lgkmcnt(6)\n\t" \
    "s_waitcnt lgkmcnt(6)\n\t" \
    "ds_read_b128 v[24:27], v2 offset:0\n\t" \
    "ds_read_b128 v[28:31], v2 offset:18432\n\t" \
    "ds_read_b128 v[32:35], v2 offset:1024\n\t" \
    "ds_read_b128 v[36:39], v2 offset:19456\n\t" \
    "s_waitcnt lgkmcnt(3)\n\t" \
    "v_mfma_f32_16x16x32_bf16 v[72:75], a[0:3], v[24:27], 0\n\t" \
    "ds_read_b128 v[40:43], v2 offset:2048\n\t" \
    "s_waitcnt lgkmcnt(3)\n\t" \
    "v_mfma_f32_16x16x32_bf16 v[76:79], a[0:3], v[28:31], 0\n\t" \
    "ds_read_b128 v[44:47], v2 offset:20480\n\t" \
    "v_mfma_f32_16x16x32_bf16 v[80:83], a[72:75], v[24:27], 0\n\t" \
    "v_mfma_f32_16x16x32_bf16 v[84:87], a[72:75], v[28:31], 0\n\t" \
    "s_waitcnt lgkmcnt(3)\n\t" \
    "v_mfma_f32_16x16x32_bf16 v[72:75], a[4:7], v[32:35], v[72:75]\n\t" \
    "ds_read_b128 v[48:51], v2 offset:3072\n\t" \
    "s_waitcnt lgkmcnt(3)\n\t" \
    "v_mfma_f32_16x16x32_bf16 v[76:79], a[4:7], v[36:39], v[76:79]\n\t" \
    "ds_read_b128 v[52:55], v2 offset:21504\n\t" \
    "v_mfma_f32_16x16x32_bf16 v[80:83], a[76:79], v[32:35], v[80:83]\n\t" \
    "v_mfma_f32_16x16x32_bf16 v[84:87], a[76:79], v[36:39], v[84:87]\n\t" \
    "s_waitcnt lgkmcnt(3)\n\t" \
    "v_mfma_f32_16x16x32_bf16 v[72:75], a[8:11], v[40:43], v[72:75]\n\t" \
    "ds_read_b128 v[56:59], v2 offset:4096\n\t" \
    "s_waitcnt lgkmcnt(3)\n\t" \
    "v_mfma_f32_16x16x32_bf16 v[76:79], a[8:11], v[44:47], v[76:79]\n\t" \
    "ds_read_b128 v[60:63], v2 offset:22528\n\t" \
    "v_mfma_f32_16x16x32_bf16 v[80:83], a[80:83], v[40:43], v[80:83]\n\t" \
    "v_mfma_f32_16x16x32_bf16 v[84:87], a[80:83], v[44:47], v[84:87]\n\t" \
    "s_waitcnt lgkmcnt(3)\n\t" \
    "v_mfma_f32_16x16x32_bf16 v[72:75], a[12:15], v[48:51], v[72:75]\n\t" \
    "ds_read_b128 v[64:67], v2 offset:5120\n\t" \
    "s_waitcnt lgkmcnt(3)\n\t" \
    "v_mfma_f32_16x16x32_bf16 v[76:79], a[12:15], v[52:55], v[76:79]\n\t" \
    "ds_read_b128 v[68:71], v2 offset:23552\n\t" \
    "v_mfma_f32_16x16x32_bf16 v[80:83], a[84:87], v[48:51], v[80:83]\n\t" \
    "v_mfma_f32_16x16x32_bf16 v[84:87], a[84:87], v[52:55], v[84:87]\n\t" \
    "s_waitcnt lgkmcnt(3)\n\t" \
    "v_mfma_f32_16x16x32_bf16 v[72:75], a[16:19], v[56:59], v[72:75]\n\t" \
    "s_waitcnt lgkmcnt(2)\n\t" \
    "v_mfma_f32_16x16x32_bf16 v[76:79], a[16:19], v[60:63], v[76:79]\n\t" \
    "v_mfma_f32_16x16x32_bf16 v[80:83], a[88:91], v[56:59], v[80:83]\n\t" \
    "v_mfma_f32_16x16x32_bf16 v[84:87], a[88:91], v[60:63], v[84:87]\n\t" \
    "s_waitcnt lgkmcnt(1)\n\t" \
    "v_mfma_f32_16x16x32_bf16 v[72:75], a[20:23], v[64:67], v[72:75]\n\t" \
    "s_waitcnt lgkmcnt(0)\n\t" \
    "v_mfma_f32_16x16x32_bf16 v[76:79], a[20:23], v[68:71], v[76:79]\n\t" \
    "v_mfma_f32_16x16x32_bf16 v[80:83], a[92:95], v[64:67], v[80:83]\n\t" \
    "v_mfma_f32_16x16x32_bf16 v[84:87], a[92:95], v[68:71], v[84:87]\n\t" \
    "s_waitcnt vmcnt(9) lgkmcnt(0)\n\t" \
    "s_barrier\n\t" \
    "s_add_u32 m0, s96, 73728\n\t" \
    "s_nop 0\n\t" \
    "global_load_lds_dwordx4 v201, s[44:45] offset:0\n\t" \
    "s_add_u32 m0, s96, 74624\n\t" \
    "s_nop 0\n\t" \
    "global_load_lds_dwordx4 v201, s[44:45] offset:128\n\t" \
    "s_add_u32 m0, s96, 75520\n\t" \
    "s_nop 0\n\t" \
    "global_load_lds_dwordx4 v201, s[44:45] offset:256\n\t" \
    "s_add_u32 m0, s96, 76416\n\t" \
    "s_nop 0\n\t" \
    "global_load_lds_dwordx4 v201, s[44:45] offset:384\n\t" \
    "s_add_u32 m0, s96, 77312\n\t" \
    "s_nop 0\n\t" \
    "global_load_lds_dwordx4 v201, s[44:45] offset:512\n\t" \
    "s_add_u32 m0, s96, 78208\n\t" \
    "s_nop 0\n\t" \
    "global_load_lds_dwordx4 v201, s[44:45] offset:640\n\t" \
    "s_add_u32 m0, s96, 79104\n\t" \
    "s_nop 0\n\t" \
    "global_load_lds_dwordx4 v201, s[44:45] offset:768\n\t" \
    "s_add_u32 m0, s96, 80000\n\t" \
    "s_nop 0\n\t" \
    "global_load_lds_dwordx4 v201, s[44:45] offset:896\n\t" \
    "s_add_u32 m0, s96, 80896\n\t" \
    "s_nop 0\n\t" \
    "global_load_lds_dwordx4 v201, s[44:45] offset:1024\n\t" \
    "v_add_u32 v201, 36864, v201\n\t" \
    "v_mbcnt_lo_u32_b32 v164, -1, 0\n\t" \
    "v_mbcnt_hi_u32_b32 v164, -1, v164\n\t" \
    "s_cmp_eq_u32 s66, 0\n\t" \
    "s_cselect_b32 s69, 59, 11\n\t" \
    "v_min_u32 v164, s69, v164\n\t" \
    "v_lshlrev_b32 v164, 6, v164\n\t" \
    "v_add_u32 v164, 6258688, v164\n\t" \
    "v_mov_b32 v165, 6266880\n\t" \
    "global_load_dword v165, v165, s[36:37] sc1\n\t" \
    "global_load_dwordx2 v[166:167], v164, s[36:37] sc1\n\t" \
    "ds_read_b64_tr_b16 a[148:149], v204 offset:36864\n\t" \
    "ds_read_b64_tr_b16 a[150:151], v204 offset:55296\n\t" \
    "ds_read_b64_tr_b16 a[164:165], v205 offset:36864\n\t" \
    "ds_read_b64_tr_b16 a[166:167], v205 offset:55296\n\t" \
    "ds_read_b64_tr_b16 a[180:181], v206 offset:36864\n\t" \
    "ds_read_b64_tr_b16 a[182:183], v206 offset:55296\n\t" \
    "s_waitcnt lgkmcnt(6)\n\t" \
    "ds_read_b64_tr_b16 a[196:197], v207 offset:36864\n\t" \
    "ds_read_b64_tr_b16 a[198:199], v207 offset:55296\n\t" \
    "ds_read_b64_tr_b16 a[212:213], v204 offset:37888\n\t" \
    "ds_read_b64_tr_b16 a[214:215], v204 offset:56320\n\t" \
    "ds_read_b64_tr_b16 a[228:229], v205 offset:37888\n\t" \
    "ds_read_b64_tr_b16 a[230:231], v205 offset:56320\n\t" \
    "s_waitcnt lgkmcnt(6)\n\t" \
    "ds_read_b64_tr_b16 a[244:245], v206 offset:37888\n\t" \
    "ds_read_b64_tr_b16 a[246:247], v206 offset:56320\n\t" \
    "ds_read_b64_tr_b16 v[228:229], v207 offset:37888\n\t" \
    "ds_read_b64_tr_b16 v[230:231], v207 offset:56320\n\t" \
    "ds_read_b64_tr_b16 v[244:245], v204 offset:38912\n\t" \
    "ds_read_b64_tr_b16 v[246:247], v204 offset:57344\n\t" \
    "s_waitcnt lgkmcnt(6)\n\t" \
    "s_waitcnt lgkmcnt(6)\n\t" \
    "ds_read_b128 v[24:27], v2 offset:6144\n\t" \
    "ds_read_b128 v[28:31], v2 offset:24576\n\t" \
    "ds_read_b128 v[32:35], v2 offset:7168\n\t" \
    "ds_read_b128 v[36:39], v2 offset:25600\n\t" \
    "s_waitcnt lgkmcnt(3)\n\t" \
    "v_mfma_f32_16x16x32_bf16 v[72:75], a[24:27], v[24:27], v[72:75]\n\t" \
    "ds_read_b128 v[40:43], v2 offset:8192\n\t" \
    "s_waitcnt lgkmcnt(3)\n\t" \
    "v_mfma_f32_16x16x32_bf16 v[76:79], a[24:27], v[28:31], v[76:79]\n\t" \
    "ds_read_b128 v[44:47], v2 offset:26624\n\t" \
    "v_mfma_f32_16x16x32_bf16 v[80:83], a[96:99], v[24:27], v[80:83]\n\t" \
    "v_mfma_f32_16x16x32_bf16 v[84:87], a[96:99], v[28:31], v[84:87]\n\t" \
    "s_waitcnt lgkmcnt(3)\n\t" \
    "v_mfma_f32_16x16x32_bf16 v[72:75], a[28:31], v[32:35], v[72:75]\n\t" \
    "ds_read_b128 v[48:51], v2 offset:9216\n\t" \
    "s_waitcnt lgkmcnt(3)\n\t" \
    "v_mfma_f32_16x16x32_bf16 v[76:79], a[28:31], v[36:39], v[76:79]\n\t" \
    "ds_read_b128 v[52:55], v2 offset:27648\n\t" \
    "v_mfma_f32_16x16x32_bf16 v[80:83], a[100:103], v[32:35], v[80:83]\n\t" \
    "v_mfma_f32_16x16x32_bf16 v[84:87], a[100:103], v[36:39], v[84:87]\n\t" \
    "s_waitcnt lgkmcnt(3)\n\t" \
    "v_mfma_f32_16x16x32_bf16 v[72:75], a[32:35], v[40:43], v[72:75]\n\t" \
    "ds_read_b128 v[56:59], v2 offset:10240\n\t" \
    "s_waitcnt lgkmcnt(3)\n\t" \
    "v_mfma_f32_16x16x32_bf16 v[76:79], a[32:35], v[44:47], v[76:79]\n\t" \
    "ds_read_b128 v[60:63], v2 offset:28672\n\t" \
    "v_mfma_f32_16x16x32_bf16 v[80:83], a[104:107], v[40:43], v[80:83]\n\t" \
    "v_mfma_f32_16x16x32_bf16 v[84:87], a[104:107], v[44:47], v[84:87]\n\t" \
    "s_waitcnt lgkmcnt(3)\n\t" \
    "v_mfma_f32_16x16x32_bf16 v[72:75], a[36:39], v[48:51], v[72:75]\n\t" \
    "ds_read_b128 v[64:67], v2 offset:11264\n\t" \
    "s_waitcnt lgkmcnt(3)\n\t" \
    "v_mfma_f32_16x16x32_bf16 v[76:79], a[36:39], v[52:55], v[76:79]\n\t" \
    "ds_read_b128 v[68:71], v2 offset:29696\n\t" \
    "v_mfma_f32_16x16x32_bf16 v[80:83], a[108:111], v[48:51], v[80:83]\n\t" \
    "v_mfma_f32_16x16x32_bf16 v[84:87], a[108:111], v[52:55], v[84:87]\n\t" \
    "s_waitcnt lgkmcnt(3)\n\t" \
    "v_mfma_f32_16x16x32_bf16 v[72:75], a[40:43], v[56:59], v[72:75]\n\t" \
    "s_waitcnt lgkmcnt(2)\n\t" \
    "v_mfma_f32_16x16x32_bf16 v[76:79], a[40:43], v[60:63], v[76:79]\n\t" \
    "v_mfma_f32_16x16x32_bf16 v[80:83], a[112:115], v[56:59], v[80:83]\n\t" \
    "v_mfma_f32_16x16x32_bf16 v[84:87], a[112:115], v[60:63], v[84:87]\n\t" \
    "s_waitcnt lgkmcnt(1)\n\t" \
    "v_mfma_f32_16x16x32_bf16 v[72:75], a[44:47], v[64:67], v[72:75]\n\t" \
    "s_waitcnt lgkmcnt(0)\n\t" \
    "v_mfma_f32_16x16x32_bf16 v[76:79], a[44:47], v[68:71], v[76:79]\n\t" \
    "v_mfma_f32_16x16x32_bf16 v[80:83], a[116:119], v[64:67], v[80:83]\n\t" \
    "v_mfma_f32_16x16x32_bf16 v[84:87], a[116:119], v[68:71], v[84:87]\n\t" \
    "s_waitcnt vmcnt(11) lgkmcnt(0)\n\t" \
    "s_barrier\n\t" \
    "s_add_u32 s50, s50, 73728\n\t" \
    "s_addc_u32 s51, s51, 0\n\t" \
    "s_add_u32 m0, s89, 110592\n\t" \
    "s_nop 0\n\t" \
    "global_load_lds_dwordx4 v1, s[50:51] offset:0\n\t" \
    "s_add_u32 m0, s89, 111552\n\t" \
    "s_nop 0\n\t" \
    "global_load_lds_dwordx4 v1, s[50:51] offset:64\n\t" \
    "s_add_u32 m0, s89, 112512\n\t" \
    "s_nop 0\n\t" \
    "global_load_lds_dwordx4 v1, s[50:51] offset:128\n\t" \
    "s_add_u32 m0, s89, 113472\n\t" \
    "s_nop 0\n\t" \
    "global_load_lds_dwordx4 v1, s[50:51] offset:192\n\t" \
    "s_add_u32 m0, s89, 114432\n\t" \
    "s_nop 0\n\t" \
    "global_load_lds_dwordx4 v1, s[50:51] offset:256\n\t" \
    "s_add_u32 m0, s89, 115392\n\t" \
    "s_nop 0\n\t" \
    "global_load_lds_dwordx4 v1, s[50:51] offset:320\n\t" \
    "s_add_u32 m0, s89, 116352\n\t" \
    "s_nop 0\n\t" \
    "global_load_lds_dwordx4 v1, s[50:51] offset:384\n\t" \
    "s_add_u32 m0, s89, 117312\n\t" \
    "s_nop 0\n\t" \
    "global_load_lds_dwordx4 v1, s[50:51] offset:448\n\t" \
    "s_add_u32 m0, s89, 118272\n\t" \
    "s_nop 0\n\t" \
    "global_load_lds_dwordx4 v1, s[50:51] offset:512\n\t" \
    "v_add_u32 v204, -36864, v204\n\t" \
    "v_add_u32 v205, -36864, v205\n\t" \
    "v_add_u32 v206, -36864, v206\n\t" \
    "v_add_u32 v207, -36864, v207\n\t" \
    "ds_read_b64_tr_b16 a[152:153], v204 offset:0\n\t" \
    "ds_read_b64_tr_b16 a[154:155], v204 offset:18432\n\t" \
    "ds_read_b64_tr_b16 a[168:169], v205 offset:0\n\t" \
    "ds_read_b64_tr_b16 a[170:171], v205 offset:18432\n\t" \
    "ds_read_b64_tr_b16 a[184:185], v206 offset:0\n\t" \
    "ds_read_b64_tr_b16 a[186:187], v206 offset:18432\n\t" \
    "s_waitcnt lgkmcnt(6)\n\t" \
    "ds_read_b64_tr_b16 a[200:201], v207 offset:0\n\t" \
    "ds_read_b64_tr_b16 a[202:203], v207 offset:18432\n\t" \
    "ds_read_b64_tr_b16 a[216:217], v204 offset:1024\n\t" \
    "ds_read_b64_tr_b16 a[218:219], v204 offset:19456\n\t" \
    "ds_read_b64_tr_b16 a[232:233], v205 offset:1024\n\t" \
    "ds_read_b64_tr_b16 a[234:235], v205 offset:19456\n\t" \
    "s_waitcnt lgkmcnt(6)\n\t" \
    "ds_read_b64_tr_b16 a[248:249], v206 offset:1024\n\t" \
    "ds_read_b64_tr_b16 a[250:251], v206 offset:19456\n\t" \
    "ds_read_b64_tr_b16 v[232:233], v207 offset:1024\n\t" \
    "ds_read_b64_tr_b16 v[234:235], v207 offset:19456\n\t" \
    "ds_read_b64_tr_b16 v[248:249], v204 offset:2048\n\t" \
    "ds_read_b64_tr_b16 v[250:251], v204 offset:20480\n\t" \
    "s_waitcnt lgkmcnt(6)\n\t" \
    "s_waitcnt lgkmcnt(6)\n\t" \
    "ds_read_b128 v[24:27], v2 offset:12288\n\t" \
    "ds_read_b128 v[28:31], v2 offset:30720\n\t" \
    "ds_read_b128 v[32:35], v2 offset:13312\n\t" \
    "ds_read_b128 v[36:39], v2 offset:31744\n\t" \
    "s_waitcnt lgkmcnt(3)\n\t" \
    "v_mfma_f32_16x16x32_bf16 v[72:75], a[48:51], v[24:27], v[72:75]\n\t" \
    "ds_read_b128 v[40:43], v2 offset:14336\n\t" \
    "s_waitcnt lgkmcnt(3)\n\t" \
    "v_mfma_f32_16x16x32_bf16 v[76:79], a[48:51], v[28:31], v[76:79]\n\t" \
    "ds_read_b128 v[44:47], v2 offset:32768\n\t" \
    "v_mfma_f32_16x16x32_bf16 v[80:83], a[120:123], v[24:27], v[80:83]\n\t" \
    "v_mfma_f32_16x16x32_bf16 v[84:87], a[120:123], v[28:31], v[84:87]\n\t" \
    "s_waitcnt lgkmcnt(3)\n\t" \
    "v_mfma_f32_16x16x32_bf16 v[72:75], a[52:55], v[32:35], v[72:75]\n\t" \
    "ds_read_b128 v[48:51], v2 offset:15360\n\t" \
    "s_waitcnt lgkmcnt(3)\n\t" \
    "v_mfma_f32_16x16x32_bf16 v[76:79], a[52:55], v[36:39], v[76:79]\n\t" \
    "ds_read_b128 v[52:55], v2 offset:33792\n\t" \
    "v_mfma_f32_16x16x32_bf16 v[80:83], a[124:127], v[32:35], v[80:83]\n\t" \
    "v_mfma_f32_16x16x32_bf16 v[84:87], a[124:127], v[36:39], v[84:87]\n\t" \
    "s_waitcnt lgkmcnt(3)\n\t" \
    "v_mfma_f32_16x16x32_bf16 v[72:75], a[56:59], v[40:43], v[72:75]\n\t" \
    "ds_read_b128 v[56:59], v2 offset:16384\n\t" \
    "s_waitcnt lgkmcnt(3)\n\t" \
    "v_mfma_f32_16x16x32_bf16 v[76:79], a[56:59], v[44:47], v[76:79]\n\t" \
    "ds_read_b128 v[60:63], v2 offset:34816\n\t" \
    "v_mfma_f32_16x16x32_bf16 v[80:83], a[128:131], v[40:43], v[80:83]\n\t" \
    "v_mfma_f32_16x16x32_bf16 v[84:87], a[128:131], v[44:47], v[84:87]\n\t" \
    "s_waitcnt lgkmcnt(3)\n\t" \
    "v_mfma_f32_16x16x32_bf16 v[72:75], a[60:63], v[48:51], v[72:75]\n\t" \
    "ds_read_b128 v[64:67], v2 offset:17408\n\t" \
    "s_waitcnt lgkmcnt(3)\n\t" \
    "v_mfma_f32_16x16x32_bf16 v[76:79], a[60:63], v[52:55], v[76:79]\n\t" \
    "ds_read_b128 v[68:71], v2 offset:35840\n\t" \
    "v_mfma_f32_16x16x32_bf16 v[80:83], a[132:135], v[48:51], v[80:83]\n\t" \
    "v_mfma_f32_16x16x32_bf16 v[84:87], a[132:135], v[52:55], v[84:87]\n\t" \
    "s_waitcnt lgkmcnt(3)\n\t" \
    "v_mfma_f32_16x16x32_bf16 v[72:75], a[64:67], v[56:59], v[72:75]\n\t" \
    "s_waitcnt lgkmcnt(2)\n\t" \
    "v_mfma_f32_16x16x32_bf16 v[76:79], a[64:67], v[60:63], v[76:79]\n\t" \
    "v_mfma_f32_16x16x32_bf16 v[80:83], a[136:139], v[56:59], v[80:83]\n\t" \
    "v_mfma_f32_16x16x32_bf16 v[84:87], a[136:139], v[60:63], v[84:87]\n\t" \
    "s_waitcnt lgkmcnt(1)\n\t" \
    "v_mfma_f32_16x16x32_bf16 v[72:75], a[68:71], v[64:67], v[72:75]\n\t" \
    "s_waitcnt lgkmcnt(0)\n\t" \
    "v_mfma_f32_16x16x32_bf16 v[76:79], a[68:71], v[68:71], v[76:79]\n\t" \
    "v_mfma_f32_16x16x32_bf16 v[80:83], a[140:143], v[64:67], v[80:83]\n\t" \
    "v_mfma_f32_16x16x32_bf16 v[84:87], a[140:143], v[68:71], v[84:87]\n\t" \
    "s_waitcnt vmcnt(11) lgkmcnt(0)\n\t" \
    "s_barrier\n\t" \
    "ds_read_b64_tr_b16 a[156:157], v204 offset:36864\n\t" \
    "ds_read_b64_tr_b16 a[158:159], v204 offset:55296\n\t" \
    "ds_read_b64_tr_b16 a[172:173], v205 offset:36864\n\t" \
    "ds_read_b64_tr_b16 a[174:175], v205 offset:55296\n\t" \
    "ds_read_b64_tr_b16 a[188:189], v206 offset:36864\n\t" \
    "ds_read_b64_tr_b16 a[190:191], v206 offset:55296\n\t" \
    "s_waitcnt lgkmcnt(6)\n\t" \
    "ds_read_b64_tr_b16 a[204:205], v207 offset:36864\n\t" \
    "ds_read_b64_tr_b16 a[206:207], v207 offset:55296\n\t" \
    "ds_read_b64_tr_b16 a[220:221], v204 offset:37888\n\t" \
    "ds_read_b64_tr_b16 a[222:223], v204 offset:56320\n\t" \
    "ds_read_b64_tr_b16 a[236:237], v205 offset:37888\n\t" \
    "ds_read_b64_tr_b16 a[238:239], v205 offset:56320\n\t" \
    "s_waitcnt lgkmcnt(6)\n\t" \
    "ds_read_b64_tr_b16 a[252:253], v206 offset:37888\n\t" \
    "ds_read_b64_tr_b16 a[254:255], v206 offset:56320\n\t" \
    "ds_read_b64_tr_b16 v[236:237], v207 offset:37888\n\t" \
    "ds_read_b64_tr_b16 v[238:239], v207 offset:56320\n\t" \
    "ds_read_b64_tr_b16 v[252:253], v204 offset:38912\n\t" \
    "ds_read_b64_tr_b16 v[254:255], v204 offset:57344\n\t" \
    "s_waitcnt lgkmcnt(6)\n\t" \
    "s_waitcnt vmcnt(9) lgkmcnt(0)\n\t" \
    "s_barrier\n\t" \
    "v_sub_u32 v166, v166, v165\n\t" \
    "v_cmp_ne_u32 vcc, 1, v166\n\t" \
    "s_cbranch_vccz gv_Cok2_%=\n\t" \
    "gv_C1_%=:\n\t" \
    "global_load_dwordx2 v[166:167], v164, s[36:37] sc1\n\t" \
    "s_waitcnt vmcnt(0)\n\t" \
    "v_sub_u32 v166, v166, v165\n\t" \
    "v_cmp_ne_u32 vcc, 1, v166\n\t" \
    "s_cbranch_vccz gv_Cok2_%=\n\t" \
    "s_cmp_eq_u32 s68, 0\n\t" \
    "s_cbranch_scc1 gv_F3_%=\n\t" \
    "s_sub_u32 s68, s68, 1\n\t" \
    "s_sleep 4\n\t" \
    "s_branch gv_C1_%=\n\t" \
    "gv_F3_%=:\n\t" \
    "v_mov_b32 v168, 6095104\n\t" \
    "v_mov_b32 v169, 4\n\t" \
    "global_store_dword v168, v169, s[36:37] sc1\n\t" \
    "s_waitcnt vmcnt(0)\n\t" \
    "s_branch gv_Cok2_%=\n\t" \
    "gv_Cok2_%=:\n\t" \
    "s_cmp_lg_u32 s66, 0\n\t" \
    "s_cbranch_scc1 gv_Ca4_%=\n\t" \
    "v_mov_b32 v164, 6176768\n\t" \
    "s_mov_b64 exec, 1\n\t" \
    "global_atomic_add v164, v166, s[36:37] sc1\n\t" \
    "s_mov_b64 exec, -1\n\t" \
    "gv_Ca4_%=:\n\t" \
    "v_min_u32_dpp v167, v167, v167 row_shl:6 row_mask:0xf bank_mask:0xf\n\t" \
    "s_nop 0\n\t" \
    "v_readlane_b32 s69, v167, 4\n\t" \
    "v_readlane_b32 s70, v167, 5\n\t" \
    "s_nop 1\n\t" \
    "s_cmp_lt_u32 s70, s69\n\t" \
    "s_cselect_b32 s52, 8192, 0\n\t" \
    "s_xor_b32 s52, s52, 8192\n\t" \
    "v_xor_b32 v20, s52, v20\n\t" \
    "s_lshr_b32 s69, s67, 5\n\t" \
    "s_or_b32 s69, s69, s66\n\t" \
    "s_cbranch_scc1 gv_npr5_%=\n\t" \
    "s_memrealtime s[54:55]\n\t" \
    "s_waitcnt lgkmcnt(0)\n\t" \
    "s_lshl_b32 s70, s54, 4\n\t" \
    "s_add_u32 s70, s70, 3680\n\t" \
    "v_mov_b32 v157, s70\n\t" \
    "s_mov_b32 exec_lo, 0xf00f0\n\t" \
    "s_mov_b32 exec_hi, 0xf00f0\n\t" \
    "global_store_dword v19, v157, s[36:37] sc1\n\t" \
    "s_mov_b64 exec, -1\n\t" \
    "gv_npr5_%=:\n\t" \
    "s_cmp_lt_u32 s65, 60\n\t" \
    "s_cselect_b32 s69, 73728, 0\n\t" \
    "s_add_u32 s50, s50, s69\n\t" \
    "s_addc_u32 s51, s51, 0\n\t" \
    "s_sub_u32 s90, s90, 36864\n\t" \
    "s_cselect_b32 s90, 110592, s90\n\t" \
    "s_sub_u32 s91, s90, 36864\n\t" \
    "s_cselect_b32 s91, 110592, s91\n\t" \
    "s_add_u32 s91, s91, s89\n\t" \
    "v_add_u32 v17, s90, v3\n\t" \
    "s_lshl_b32 s40, s65, 15\n\t" \
    "v_mov_b32 v2, v17\n\t" \
    "s_mov_b64 s[46:47], 0\n\t" \
    "s_branch gv_mid8_%=\n\t" \
    ".p2align 6\n\t" \
    "gv_al9_%=:\n\t" \
    "s_nop 0\n\t" \
    "s_nop 0\n\t" \
    "gv_loop6_%=:\n\t" \
    "s_waitcnt lgkmcnt(5)\n\t" \
    "v_mfma_f32_16x16x32_bf16 v[72:75], a[0:3], v[24:27], 0\n\t" \
    "ds_read_b128 v[48:51], v2 offset:3072\n\t" \
    "s_waitcnt lgkmcnt(5)\n\t" \
    "v_mfma_f32_16x16x32_bf16 v[76:79], a[0:3], v[28:31], 0\n\t" \
    "ds_read_b128 v[52:55], v2 offset:21504\n\t" \
    "v_mfma_f32_16x16x32_bf16 v[80:83], a[72:75], v[24:27], 0\n\t" \
    "s_cmp_lt_u32 s65, 60\n\t" \
    "s_cselect_b32 s69, 73728, 0\n\t" \
    "s_add_u32 s50, s50, s69\n\t" \
    "v_mfma_f32_16x16x32_bf16 v[84:87], a[72:75], v[28:31], 0\n\t" \
    "s_addc_u32 s51, s51, 0\n\t" \
    "s_sub_u32 s90, s90, 36864\n\t" \
    "s_cselect_b32 s90, 110592, s90\n\t" \
    "s_waitcnt lgkmcnt(5)\n\t" \
    "v_mfma_f32_16x16x32_bf16 v[72:75], a[4:7], v[32:35], v[72:75]\n\t" \
    "ds_read_b128 v[56:59], v2 offset:4096\n\t" \
    "s_waitcnt lgkmcnt(5)\n\t" \
    "v_mfma_f32_16x16x32_bf16 v[76:79], a[4:7], v[36:39], v[76:79]\n\t" \
    "ds_read_b128 v[60:63], v2 offset:22528\n\t" \
    "v_mfma_f32_16x16x32_bf16 v[80:83], a[76:79], v[32:35], v[80:83]\n\t" \
    "s_sub_u32 s91, s90, 36864\n\t" \
    "s_cselect_b32 s91, 110592, s91\n\t" \
    "s_add_u32 s91, s91, s89\n\t" \
    "v_mfma_f32_16x16x32_bf16 v[84:87], a[76:79], v[36:39], v[84:87]\n\t" \
    "v_add_u32 v17, s90, v3\n\t" \
    "s_lshl_b32 s40, s65, 15\n\t" \
    "s_sub_u32 s70, s65, 4\n\t" \
    "s_waitcnt lgkmcnt(5)\n\t" \
    "v_mfma_f32_16x16x32_bf16 v[72:75], a[8:11], v[40:43], v[72:75]\n\t" \
    "ds_read_b128 v[64:67], v2 offset:5120\n\t" \
    "s_waitcnt lgkmcnt(5)\n\t" \
    "v_mfma_f32_16x16x32_bf16 v[76:79], a[8:11], v[44:47], v[76:79]\n\t" \
    "ds_read_b128 v[68:71], v2 offset:23552\n\t" \
    "v_mfma_f32_16x16x32_bf16 v[80:83], a[80:83], v[40:43], v[80:83]\n\t" \
    "v_mov_b32 v157, s70\n\t" \
    "s_cmp_lt_u32 s65, 14\n\t" \
    "s_cselect_b64 s[46:47], 0, s[58:59]\n\t" \
    "v_mfma_f32_16x16x32_bf16 v[84:87], a[80:83], v[44:47], v[84:87]\n\t" \
    "s_waitcnt lgkmcnt(5)\n\t" \
    "v_mfma_f32_16x16x32_bf16 v[72:75], a[12:15], v[48:51], v[72:75]\n\t" \
    "ds_read_b128 v[24:27], v2 offset:6144\n\t" \
    "s_waitcnt lgkmcnt(5)\n\t" \
    "v_mfma_f32_16x16x32_bf16 v[76:79], a[12:15], v[52:55], v[76:79]\n\t" \
    "ds_read_b128 v[28:31], v2 offset:24576\n\t" \
    "v_mfma_f32_16x16x32_bf16 v[80:83], a[84:87], v[48:51], v[80:83]\n\t" \
    "s_add_u32 m0, s91, 0\n\t" \
    "v_mfma_f32_16x16x32_bf16 v[84:87], a[84:87], v[52:55], v[84:87]\n\t" \
    "global_load_lds_dwordx4 v1, s[50:51] offset:0\n\t" \
    "s_waitcnt lgkmcnt(5)\n\t" \
    "v_mfma_f32_16x16x32_bf16 v[72:75], a[16:19], v[56:59], v[72:75]\n\t" \
    "ds_read_b128 v[32:35], v2 offset:7168\n\t" \
    "s_waitcnt lgkmcnt(5)\n\t" \
    "v_mfma_f32_16x16x32_bf16 v[76:79], a[16:19], v[60:63], v[76:79]\n\t" \
    "ds_read_b128 v[36:39], v2 offset:25600\n\t" \
    "v_mfma_f32_16x16x32_bf16 v[80:83], a[88:91], v[56:59], v[80:83]\n\t" \
    "s_add_u32 m0, s91, 960\n\t" \
    "v_mfma_f32_16x16x32_bf16 v[84:87], a[88:91], v[60:63], v[84:87]\n\t" \
    "global_load_lds_dwordx4 v1, s[50:51] offset:64\n\t" \
    "s_waitcnt lgkmcnt(5)\n\t" \
    "v_mfma_f32_16x16x32_bf16 v[72:75], a[20:23], v[64:67], v[72:75]\n\t" \
    "ds_read_b128 v[40:43], v2 offset:8192\n\t" \
    "s_waitcnt lgkmcnt(5)\n\t" \
    "v_mfma_f32_16x16x32_bf16 v[76:79], a[20:23], v[68:71], v[76:79]\n\t" \
    "ds_read_b128 v[44:47], v2 offset:26624\n\t" \
    "v_mfma_f32_16x16x32_bf16 v[80:83], a[92:95], v[64:67], v[80:83]\n\t" \
    "s_add_u32 m0, s91, 1920\n\t" \
    "v_mfma_f32_16x16x32_bf16 v[84:87], a[92:95], v[68:71], v[84:87]\n\t" \
    "global_load_lds_dwordx4 v1, s[50:51] offset:128\n\t" \
    "s_waitcnt lgkmcnt(5)\n\t" \
    "v_mfma_f32_16x16x32_bf16 v[72:75], a[24:27], v[24:27], v[72:75]\n\t" \
    "ds_read_b128 v[48:51], v2 offset:9216\n\t" \
    "s_waitcnt lgkmcnt(5)\n\t" \
    "v_mfma_f32_16x16x32_bf16 v[76:79], a[24:27], v[28:31], v[76:79]\n\t" \
    "ds_read_b128 v[52:55], v2 offset:27648\n\t" \
    "v_mfma_f32_16x16x32_bf16 v[80:83], a[96:99], v[24:27], v[80:83]\n\t" \
    "s_add_u32 m0, s91, 2880\n\t" \
    "v_mfma_f32_16x16x32_bf16 v[84:87], a[96:99], v[28:31], v[84:87]\n\t" \
    "global_load_lds_dwordx4 v1, s[50:51] offset:192\n\t" \
    "s_waitcnt lgkmcnt(5)\n\t" \
    "v_mfma_f32_16x16x32_bf16 v[72:75], a[28:31], v[32:35], v[72:75]\n\t" \
    "ds_read_b128 v[56:59], v2 offset:10240\n\t" \
    "s_waitcnt lgkmcnt(5)\n\t" \
    "v_mfma_f32_16x16x32_bf16 v[76:79], a[28:31], v[36:39], v[76:79]\n\t" \
    "ds_read_b128 v[60:63], v2 offset:28672\n\t" \
    "v_mfma_f32_16x16x32_bf16 v[80:83], a[100:103], v[32:35], v[80:83]\n\t" \
    "s_add_u32 m0, s91, 3840\n\t" \
    "v_mfma_f32_16x16x32_bf16 v[84:87], a[100:103], v[36:39], v[84:87]\n\t" \
    "global_load_lds_dwordx4 v1, s[50:51] offset:256\n\t" \
    "s_waitcnt lgkmcnt(5)\n\t" \
    "v_mfma_f32_16x16x32_bf16 v[72:75], a[32:35], v[40:43], v[72:75]\n\t" \
    "ds_read_b128 v[64:67], v2 offset:11264\n\t" \
    "s_waitcnt lgkmcnt(5)\n\t" \
    "v_mfma_f32_16x16x32_bf16 v[76:79], a[32:35], v[44:47], v[76:79]\n\t" \
    "ds_read_b128 v[68:71], v2 offset:29696\n\t" \
    "v_mfma_f32_16x16x32_bf16 v[80:83], a[104:107], v[40:43], v[80:83]\n\t" \
    "s_add_u32 m0, s91, 4800\n\t" \
    "v_mfma_f32_16x16x32_bf16 v[84:87], a[104:107], v[44:47], v[84:87]\n\t" \
    "global_load_lds_dwordx4 v1, s[50:51] offset:320\n\t" \
    "s_waitcnt lgkmcnt(5)\n\t" \
    "v_mfma_f32_16x16x32_bf16 v[72:75], a[36:39], v[48:51], v[72:75]\n\t" \
    "ds_read_b128 v[24:27], v2 offset:12288\n\t" \
    "s_waitcnt lgkmcnt(5)\n\t" \
    "v_mfma_f32_16x16x32_bf16 v[76:79], a[36:39], v[52:55], v[76:79]\n\t" \
    "ds_read_b128 v[28:31], v2 offset:30720\n\t" \
    "v_mfma_f32_16x16x32_bf16 v[80:83], a[108:111], v[48:51], v[80:83]\n\t" \
    "s_add_u32 m0, s91, 5760\n\t" \
    "v_mfma_f32_16x16x32_bf16 v[84:87], a[108:111], v[52:55], v[84:87]\n\t" \
    "global_load_lds_dwordx4 v1, s[50:51] offset:384\n\t" \
    "s_waitcnt lgkmcnt(5)\n\t" \
    "v_mfma_f32_16x16x32_bf16 v[72:75], a[40:43], v[56:59], v[72:75]\n\t" \
    "ds_read_b128 v[32:35], v2 offset:13312\n\t" \
    "s_waitcnt lgkmcnt(5)\n\t" \
    "v_mfma_f32_16x16x32_bf16 v[76:79], a[40:43], v[60:63], v[76:79]\n\t" \
    "ds_read_b128 v[36:39], v2 offset:31744\n\t" \
    "v_mfma_f32_16x16x32_bf16 v[80:83], a[112:115], v[56:59], v[80:83]\n\t" \
    "s_add_u32 m0, s91, 6720\n\t" \
    "v_mfma_f32_16x16x32_bf16 v[84:87], a[112:115], v[60:63], v[84:87]\n\t" \
    "global_load_lds_dwordx4 v1, s[50:51] offset:448\n\t" \
    "s_waitcnt lgkmcnt(5)\n\t" \
    "v_mfma_f32_16x16x32_bf16 v[72:75], a[44:47], v[64:67], v[72:75]\n\t" \
    "ds_read_b128 v[40:43], v2 offset:14336\n\t" \
    "s_waitcnt lgkmcnt(5)\n\t" \
    "v_mfma_f32_16x16x32_bf16 v[76:79], a[44:47], v[68:71], v[76:79]\n\t" \
    "ds_read_b128 v[44:47], v2 offset:32768\n\t" \
    "v_mfma_f32_16x16x32_bf16 v[80:83], a[116:119], v[64:67], v[80:83]\n\t" \
    "s_add_u32 m0, s91, 7680\n\t" \
    "v_mfma_f32_16x16x32_bf16 v[84:87], a[116:119], v[68:71], v[84:87]\n\t" \
    "global_load_lds_dwordx4 v1, s[50:51] offset:512\n\t" \
    "s_waitcnt lgkmcnt(5)\n\t" \
    "v_mfma_f32_16x16x32_bf16 v[72:75], a[48:51], v[24:27], v[72:75]\n\t" \
    "ds_read_b128 v[48:51], v2 offset:15360\n\t" \
    "s_waitcnt lgkmcnt(5)\n\t" \
    "v_mfma_f32_16x16x32_bf16 v[76:79], a[48:51], v[28:31], v[76:79]\n\t" \
    "ds_read_b128 v[52:55], v2 offset:33792\n\t" \
    "v_mfma_f32_16x16x32_bf16 v[80:83], a[120:123], v[24:27], v[80:83]\n\t" \
    "v_mfma_f32_16x16x32_bf16 v[84:87], a[120:123], v[28:31], v[84:87]\n\t" \
    "s_waitcnt lgkmcnt(5)\n\t" \
    "v_mfma_f32_16x16x32_bf16 v[72:75], a[52:55], v[32:35], v[72:75]\n\t" \
    "ds_read_b128 v[56:59], v2 offset:16384\n\t" \
    "s_waitcnt lgkmcnt(5)\n\t" \
    "v_mfma_f32_16x16x32_bf16 v[76:79], a[52:55], v[36:39], v[76:79]\n\t" \
    "ds_read_b128 v[60:63], v2 offset:34816\n\t" \
    "v_mfma_f32_16x16x32_bf16 v[80:83], a[124:127], v[32:35], v[80:83]\n\t" \
    "v_mfma_f32_16x16x32_bf16 v[84:87], a[124:127], v[36:39], v[84:87]\n\t" \
    "s_waitcnt lgkmcnt(5)\n\t" \
    "v_mfma_f32_16x16x32_bf16 v[72:75], a[56:59], v[40:43], v[72:75]\n\t" \
    "ds_read_b128 v[64:67], v2 offset:17408\n\t" \
    "s_waitcnt lgkmcnt(5)\n\t" \
    "v_mfma_f32_16x16x32_bf16 v[76:79], a[56:59], v[44:47], v[76:79]\n\t" \
    "ds_read_b128 v[68:71], v2 offset:35840\n\t" \
    "v_mfma_f32_16x16x32_bf16 v[80:83], a[128:131], v[40:43], v[80:83]\n\t" \
    "v_mfma_f32_16x16x32_bf16 v[84:87], a[128:131], v[44:47], v[84:87]\n\t" \
    "s_waitcnt lgkmcnt(5)\n\t" \
    "v_mfma_f32_16x16x32_bf16 v[72:75], a[60:63], v[48:51], v[72:75]\n\t" \
    "s_waitcnt lgkmcnt(4)\n\t" \
    "v_mfma_f32_16x16x32_bf16 v[76:79], a[60:63], v[52:55], v[76:79]\n\t" \
    "v_mfma_f32_16x16x32_bf16 v[80:83], a[132:135], v[48:51], v[80:83]\n\t" \
    "v_mfma_f32_16x16x32_bf16 v[84:87], a[132:135], v[52:55], v[84:87]\n\t" \
    "s_waitcnt lgkmcnt(3)\n\t" \
    "v_mfma_f32_16x16x32_bf16 v[72:75], a[64:67], v[56:59], v[72:75]\n\t" \
    "s_waitcnt lgkmcnt(2)\n\t" \
    "v_mfma_f32_16x16x32_bf16 v[76:79], a[64:67], v[60:63], v[76:79]\n\t" \
    "v_mfma_f32_16x16x32_bf16 v[80:83], a[136:139], v[56:59], v[80:83]\n\t" \
    "v_mov_b32 v2, v17\n\t" \
    "v_mfma_f32_16x16x32_bf16 v[84:87], a[136:139], v[60:63], v[84:87]\n\t" \
    "s_waitcnt lgkmcnt(1)\n\t" \
    "v_mfma_f32_16x16x32_bf16 v[72:75], a[68:71], v[64:67], v[72:75]\n\t" \
    "s_waitcnt lgkmcnt(0)\n\t" \
    "v_mfma_f32_16x16x32_bf16 v[76:79], a[68:71], v[68:71], v[76:79]\n\t" \
    "v_mfma_f32_16x16x32_bf16 v[80:83], a[140:143], v[64:67], v[80:83]\n\t" \
    "v_mfma_f32_16x16x32_bf16 v[84:87], a[140:143], v[68:71], v[84:87]\n\t" \
    "gv_mid8_%=:\n\t" \
    "s_nop 3\n\t" \
    "v_cvt_pk_bf16_f32 v104, v72, v73\n\t" \
    "v_cvt_pk_bf16_f32 v105, v74, v75\n\t" \
    "s_nop 0\n\t" \
    "v_cvt_pk_bf16_f32 v106, v80, v81\n\t" \
    "v_cvt_pk_bf16_f32 v107, v82, v83\n\t" \
    "ds_write_b128 v4, v[104:107] offset:0\n\t" \
    "v_cvt_pk_bf16_f32 v108, v76, v77\n\t" \
    "v_cvt_pk_bf16_f32 v109, v78, v79\n\t" \
    "v_cvt_pk_bf16_f32 v110, v84, v85\n\t" \
    "v_cvt_pk_bf16_f32 v111, v86, v87\n\t" \
    "ds_write_b128 v4, v[108:111] offset:1024\n\t" \
    "s_waitcnt vmcnt(18) lgkmcnt(0)\n\t" \
    "s_barrier\n\t" \
    "ds_read_b128 v[164:167], v5 offset:0\n\t" \
    "ds_read_b128 v[168:171], v5 offset:1024\n\t" \
    "ds_read_b128 v[172:175], v5 offset:2048\n\t" \
    "ds_read_b128 v[176:179], v5 offset:3072\n\t" \
    "ds_read_b128 v[180:183], v5 offset:4096\n\t" \
    "ds_read_b128 v[184:187], v5 offset:5120\n\t" \
    "ds_read_b128 v[188:191], v5 offset:6144\n\t" \
    "ds_read_b128 v[192:195], v5 offset:7168\n\t" \
    "s_mov_b64 exec, s[46:47]\n\t" \
    "global_store_dword v20, v157, s[36:37] sc1\n\t" \
    "s_mov_b64 exec, -1\n\t" \
    "v_xor_b32 v4, 0x2000, v4\n\t" \
    "v_xor_b32 v5, 0x2000, v5\n\t" \
    "s_waitcnt lgkmcnt(7)\n\t" \
    "v_mfma_f32_16x16x32_bf16 v[72:75], v[164:167], a[144:147], 0\n\t" \
    "s_waitcnt lgkmcnt(6)\n\t" \
    "v_mfma_f32_16x16x32_bf16 v[76:79], v[168:171], a[144:147], 0\n\t" \
    "s_waitcnt lgkmcnt(5)\n\t" \
    "v_mfma_f32_16x16x32_bf16 v[72:75], v[172:175], a[148:151], v[72:75]\n\t" \
    "s_waitcnt lgkmcnt(4)\n\t" \
    "v_mfma_f32_16x16x32_bf16 v[76:79], v[176:179], a[148:151], v[76:79]\n\t" \
    "s_waitcnt lgkmcnt(3)\n\t" \
    "v_mfma_f32_16x16x32_bf16 v[72:75], v[180:183], a[152:155], v[72:75]\n\t" \
    "s_waitcnt lgkmcnt(2)\n\t" \
    "v_mfma_f32_16x16x32_bf16 v[76:79], v[184:187], a[152:155], v[76:79]\n\t" \
    "s_waitcnt lgkmcnt(1)\n\t" \
    "v_mfma_f32_16x16x32_bf16 v[72:75], v[188:191], a[156:159], v[72:75]\n\t" \
    "s_waitcnt lgkmcnt(0)\n\t" \
    "v_mfma_f32_16x16x32_bf16 v[76:79], v[192:195], a[156:159], v[76:79]\n\t" \
    "v_mfma_f32_16x16x32_bf16 v[80:83], v[164:167], a[160:163], 0\n\t" \
    "v_mfma_f32_16x16x32_bf16 v[84:87], v[168:171], a[160:163], 0\n\t" \
    "v_mfma_f32_16x16x32_bf16 v[80:83], v[172:175], a[164:167], v[80:83]\n\t" \
    "v_mfma_f32_16x16x32_bf16 v[84:87], v[176:179], a[164:167], v[84:87]\n\t" \
    "v_mfma_f32_16x16x32_bf16 v[80:83], v[180:183], a[168:171], v[80:83]\n\t" \
    "v_mfma_f32_16x16x32_bf16 v[84:87], v[184:187], a[168:171], v[84:87]\n\t" \
    "v_cvt_pk_bf16_f32 v104, v72, v73\n\t" \
    "v_mfma_f32_16x16x32_bf16 v[80:83], v[188:191], a[172:175], v[80:83]\n\t" \
    "v_cvt_pk_bf16_f32 v105, v74, v75\n\t" \
    "v_mfma_f32_16x16x32_bf16 v[84:87], v[192:195], a[172:175], v[84:87]\n\t" \
    "v_cvt_pk_bf16_f32 v106, v76, v77\n\t" \
    "v_mfma_f32_16x16x32_bf16 v[88:91], v[164:167], a[176:179], 0\n\t" \
    "v_cvt_pk_bf16_f32 v107, v78, v79\n\t" \
    "v_mfma_f32_16x16x32_bf16 v[92:95], v[168:171], a[176:179], 0\n\t" \
    "v_xor_b32 v18, s40, v8\n\t" \
    "v_mfma_f32_16x16x32_bf16 v[88:91], v[172:175], a[180:183], v[88:91]\n\t" \
    "global_store_dwordx4 v18, v[104:107], s[38:39] sc1\n\t" \
    "v_mfma_f32_16x16x32_bf16 v[92:95], v[176:179], a[180:183], v[92:95]\n\t" \
    "v_mfma_f32_16x16x32_bf16 v[88:91], v[180:183], a[184:187], v[88:91]\n\t" \
    "v_mfma_f32_16x16x32_bf16 v[92:95], v[184:187], a[184:187], v[92:95]\n\t" \
    "v_cvt_pk_bf16_f32 v108, v80, v81\n\t" \
    "v_mfma_f32_16x16x32_bf16 v[88:91], v[188:191], a[188:191], v[88:91]\n\t" \
    "v_cvt_pk_bf16_f32 v109, v82, v83\n\t" \
    "v_mfma_f32_16x16x32_bf16 v[92:95], v[192:195], a[188:191], v[92:95]\n\t" \
    "v_cvt_pk_bf16_f32 v110, v84, v85\n\t" \
    "v_mfma_f32_16x16x32_bf16 v[72:75], v[164:167], a[192:195], 0\n\t" \
    "v_cvt_pk_bf16_f32 v111, v86, v87\n\t" \
    "v_mfma_f32_16x16x32_bf16 v[76:79], v[168:171], a[192:195], 0\n\t" \
    "v_xor_b32 v18, s40, v9\n\t" \
    "v_mfma_f32_16x16x32_bf16 v[72:75], v[172:175], a[196:199], v[72:75]\n\t" \
    "global_store_dwordx4 v18, v[108:111], s[38:39] sc1\n\t" \
    "v_mfma_f32_16x16x32_bf16 v[76:79], v[176:179], a[196:199], v[76:79]\n\t" \
    "v_mfma_f32_16x16x32_bf16 v[72:75], v[180:183], a[200:203], v[72:75]\n\t" \
    "v_mfma_f32_16x16x32_bf16 v[76:79], v[184:187], a[200:203], v[76:79]\n\t" \
    "v_cvt_pk_bf16_f32 v104, v88, v89\n\t" \
    "v_mfma_f32_16x16x32_bf16 v[72:75], v[188:191], a[204:207], v[72:75]\n\t" \
    "v_cvt_pk_bf16_f32 v105, v90, v91\n\t" \
    "v_mfma_f32_16x16x32_bf16 v[76:79], v[192:195], a[204:207], v[76:79]\n\t" \
    "v_cvt_pk_bf16_f32 v106, v92, v93\n\t" \
    "v_mfma_f32_16x16x32_bf16 v[80:83], v[164:167], a[208:211], 0\n\t" \
    "v_cvt_pk_bf16_f32 v107, v94, v95\n\t" \
    "v_mfma_f32_16x16x32_bf16 v[84:87], v[168:171], a[208:211], 0\n\t" \
    "v_xor_b32 v18, s40, v10\n\t" \
    "v_mfma_f32_16x16x32_bf16 v[80:83], v[172:175], a[212:215], v[80:83]\n\t" \
    "global_store_dwordx4 v18, v[104:107], s[38:39] sc1\n\t" \
    "v_mfma_f32_16x16x32_bf16 v[84:87], v[176:179], a[212:215], v[84:87]\n\t" \
    "v_mfma_f32_16x16x32_bf16 v[80:83], v[180:183], a[216:219], v[80:83]\n\t" \
    "v_mfma_f32_16x16x32_bf16 v[84:87], v[184:187], a[216:219], v[84:87]\n\t" \
    "v_cvt_pk_bf16_f32 v108, v72, v73\n\t" \
    "v_mfma_f32_16x16x32_bf16 v[80:83], v[188:191], a[220:223], v[80:83]\n\t" \
    "v_cvt_pk_bf16_f32 v109, v74, v75\n\t" \
    "v_mfma_f32_16x16x32_bf16 v[84:87], v[192:195], a[220:223], v[84:87]\n\t" \
    "v_cvt_pk_bf16_f32 v110, v76, v77\n\t" \
    "v_mfma_f32_16x16x32_bf16 v[88:91], v[164:167], a[224:227], 0\n\t" \
    "v_cvt_pk_bf16_f32 v111, v78, v79\n\t" \
    "v_mfma_f32_16x16x32_bf16 v[92:95], v[168:171], a[224:227], 0\n\t" \
    "v_xor_b32 v18, s40, v11\n\t" \
    "v_mfma_f32_16x16x32_bf16 v[88:91], v[172:175], a[228:231], v[88:91]\n\t" \
    "global_store_dwordx4 v18, v[108:111], s[38:39] sc1\n\t" \
    "v_mfma_f32_16x16x32_bf16 v[92:95], v[176:179], a[228:231], v[92:95]\n\t" \
    "v_mfma_f32_16x16x32_bf16 v[88:91], v[180:183], a[232:235], v[88:91]\n\t" \
    "v_mfma_f32_16x16x32_bf16 v[92:95], v[184:187], a[232:235], v[92:95]\n\t" \
    "v_cvt_pk_bf16_f32 v104, v80, v81\n\t" \
    "v_mfma_f32_16x16x32_bf16 v[88:91], v[188:191], a[236:239], v[88:91]\n\t" \
    "v_cvt_pk_bf16_f32 v105, v82, v83\n\t" \
    "v_mfma_f32_16x16x32_bf16 v[92:95], v[192:195], a[236:239], v[92:95]\n\t" \
    "v_cvt_pk_bf16_f32 v106, v84, v85\n\t" \
    "v_mfma_f32_16x16x32_bf16 v[72:75], v[164:167], a[240:243], 0\n\t" \
    "v_cvt_pk_bf16_f32 v107, v86, v87\n\t" \
    "v_mfma_f32_16x16x32_bf16 v[76:79], v[168:171], a[240:243], 0\n\t" \
    "v_xor_b32 v18, s40, v12\n\t" \
    "v_mfma_f32_16x16x32_bf16 v[72:75], v[172:175], a[244:247], v[72:75]\n\t" \
    "global_store_dwordx4 v18, v[104:107], s[38:39] sc1\n\t" \
    "v_mfma_f32_16x16x32_bf16 v[76:79], v[176:179], a[244:247], v[76:79]\n\t" \
    "v_mfma_f32_16x16x32_bf16 v[72:75], v[180:183], a[248:251], v[72:75]\n\t" \
    "v_mfma_f32_16x16x32_bf16 v[76:79], v[184:187], a[248:251], v[76:79]\n\t" \
    "v_cvt_pk_bf16_f32 v108, v88, v89\n\t" \
    "v_mfma_f32_16x16x32_bf16 v[72:75], v[188:191], a[252:255], v[72:75]\n\t" \
    "v_cvt_pk_bf16_f32 v109, v90, v91\n\t" \
    "v_mfma_f32_16x16x32_bf16 v[76:79], v[192:195], a[252:255], v[76:79]\n\t" \
    "v_cvt_pk_bf16_f32 v110, v92, v93\n\t" \
    "v_mfma_f32_16x16x32_bf16 v[80:83], v[164:167], v[224:227], 0\n\t" \
    "v_cvt_pk_bf16_f32 v111, v94, v95\n\t" \
    "v_mfma_f32_16x16x32_bf16 v[84:87], v[168:171], v[224:227], 0\n\t" \
    "v_xor_b32 v18, s40, v13\n\t" \
    "v_mfma_f32_16x16x32_bf16 v[80:83], v[172:175], v[228:231], v[80:83]\n\t" \
    "global_store_dwordx4 v18, v[108:111], s[38:39] sc1\n\t" \
    "v_mfma_f32_16x16x32_bf16 v[84:87], v[176:179], v[228:231], v[84:87]\n\t" \
    "v_mfma_f32_16x16x32_bf16 v[80:83], v[180:183], v[232:235], v[80:83]\n\t" \
    "v_mfma_f32_16x16x32_bf16 v[84:87], v[184:187], v[232:235], v[84:87]\n\t" \
    "v_cvt_pk_bf16_f32 v104, v72, v73\n\t" \
    "v_mfma_f32_16x16x32_bf16 v[80:83], v[188:191], v[236:239], v[80:83]\n\t" \
    "v_cvt_pk_bf16_f32 v105, v74, v75\n\t" \
    "v_mfma_f32_16x16x32_bf16 v[84:87], v[192:195], v[236:239], v[84:87]\n\t" \
    "v_cvt_pk_bf16_f32 v106, v76, v77\n\t" \
    "s_cmp_lt_u32 s65, 4\n\t" \
    "s_cbranch_scc1 gv_f1st10_%=\n\t" \
    "gv_f1b11_%=:\n\t" \
    "v_mfma_f32_16x16x32_bf16 v[88:91], v[164:167], v[240:243], 0\n\t" \
    "v_cvt_pk_bf16_f32 v107, v78, v79\n\t" \
    "ds_read_b128 v[24:27], v17 offset:0\n\t" \
    "v_mfma_f32_16x16x32_bf16 v[92:95], v[168:171], v[240:243], 0\n\t" \
    "v_xor_b32 v18, s40, v14\n\t" \
    "ds_read_b128 v[28:31], v17 offset:18432\n\t" \
    "v_mfma_f32_16x16x32_bf16 v[88:91], v[172:175], v[244:247], v[88:91]\n\t" \
    "global_store_dwordx4 v18, v[104:107], s[38:39] sc1\n\t" \
    "ds_read_b128 v[32:35], v17 offset:1024\n\t" \
    "v_mfma_f32_16x16x32_bf16 v[92:95], v[176:179], v[244:247], v[92:95]\n\t" \
    "ds_read_b128 v[36:39], v17 offset:19456\n\t" \
    "v_mfma_f32_16x16x32_bf16 v[88:91], v[180:183], v[248:251], v[88:91]\n\t" \
    "ds_read_b128 v[40:43], v17 offset:2048\n\t" \
    "v_mfma_f32_16x16x32_bf16 v[92:95], v[184:187], v[248:251], v[92:95]\n\t" \
    "v_cvt_pk_bf16_f32 v108, v80, v81\n\t" \
    "ds_read_b128 v[44:47], v17 offset:20480\n\t" \
    "v_mfma_f32_16x16x32_bf16 v[88:91], v[188:191], v[252:255], v[88:91]\n\t" \
    "v_cvt_pk_bf16_f32 v109, v82, v83\n\t" \
    "v_mfma_f32_16x16x32_bf16 v[92:95], v[192:195], v[252:255], v[92:95]\n\t" \
    "v_cvt_pk_bf16_f32 v110, v84, v85\n\t" \
    "v_cvt_pk_bf16_f32 v111, v86, v87\n\t" \
    "v_xor_b32 v18, s40, v15\n\t" \
    "global_store_dwordx4 v18, v[108:111], s[38:39] sc1\n\t" \
    "s_nop 1\n\t" \
    "v_cvt_pk_bf16_f32 v104, v88, v89\n\t" \
    "v_cvt_pk_bf16_f32 v105, v90, v91\n\t" \
    "v_cvt_pk_bf16_f32 v106, v92, v93\n\t" \
    "v_cvt_pk_bf16_f32 v107, v94, v95\n\t" \
    "v_xor_b32 v18, s40, v16\n\t" \
    "global_store_dwordx4 v18, v[104:107], s[38:39] sc1\n\t" \
    "s_sub_u32 s69, s65, 10\n\t" \
    "s_cmp_ge_u32 s69, 52\n\t" \
    "s_cbranch_scc1 gv_fast12_%=\n\t" \
    "gv_nofast7_%=:\n\t" \
    "s_add_u32 s65, s65, 2\n\t" \
    "s_cmp_lt_u32 s65, 64\n\t" \
    "s_cbranch_scc1 gv_loop6_%=\n\t" \
    "s_waitcnt vmcnt(0) lgkmcnt(0)\n\t" \
    "s_barrier\n\t" \
    "s_branch ea_done_%=\n\t" \
    "gv_f1st10_%=:\n\t" \
    "s_waitcnt vmcnt(6)\n\t" \
    "s_barrier\n\t" \
    "s_branch gv_f1b11_%=\n\t" \
    "gv_fast12_%=:\n\t" \
    "s_waitcnt vmcnt(0)\n\t" \
    "s_barrier\n\t" \
    "s_cmp_lg_u32 s66, 0\n\t" \
    "s_cbranch_scc1 gv_SF13_%=\n\t" \
    "s_add_u32 s70, s65, 0\n\t" \
    "v_mov_b32 v157, s70\n\t" \
    "s_mov_b64 exec, 31\n\t" \
    "global_store_dword v20, v157, s[36:37] sc1\n\t" \
    "s_mov_b64 exec, -1\n\t" \
    "gv_SF13_%=:\n\t" \
    "s_cmp_ge_u32 s65, 2\n\t" \
    "s_cbranch_scc1 gv_nofast7_%=\n\t" \
    "s_add_u32 m0, s91, 0\n\t" \
    "s_nop 0\n\t" \
    "global_load_lds_dwordx4 v1, s[50:51] offset:0\n\t" \
    "s_add_u32 m0, s91, 960\n\t" \
    "s_nop 0\n\t" \
    "global_load_lds_dwordx4 v1, s[50:51] offset:64\n\t" \
    "s_add_u32 m0, s91, 1920\n\t" \
    "s_nop 0\n\t" \
    "global_load_lds_dwordx4 v1, s[50:51] offset:128\n\t" \
    "s_add_u32 m0, s91, 2880\n\t" \
    "s_nop 0\n\t" \
    "global_load_lds_dwordx4 v1, s[50:51] offset:192\n\t" \
    "s_add_u32 m0, s91, 3840\n\t" \
    "s_nop 0\n\t" \
    "global_load_lds_dwordx4 v1, s[50:51] offset:256\n\t" \
    "s_add_u32 m0, s91, 4800\n\t" \
    "s_nop 0\n\t" \
    "global_load_lds_dwordx4 v1, s[50:51] offset:320\n\t" \
    "s_add_u32 m0, s91, 5760\n\t" \
    "s_nop 0\n\t" \
    "global_load_lds_dwordx4 v1, s[50:51] offset:384\n\t" \
    "s_add_u32 m0, s91, 6720\n\t" \
    "s_nop 0\n\t" \
    "global_load_lds_dwordx4 v1, s[50:51] offset:448\n\t" \
    "s_add_u32 m0, s91, 7680\n\t" \
    "s_nop 0\n\t" \
    "global_load_lds_dwordx4 v1, s[50:51] offset:512\n\t" \
    "s_branch gv_nofast7_%=\n\t" \
    "ea_done_%=:\n\t" \
    "s_waitcnt lgkmcnt(0)\n\t" \
    "ea_last_%=:\n\t" \
    ""
#define ASM_R \
    "s_mov_b64 s[36:37], %0\n\t" \
    "s_mov_b64 s[42:43], %1\n\t" \
    "s_mov_b64 s[44:45], %2\n\t" \
    "s_mov_b32 s66, %3\n\t" \
    "s_mov_b32 s67, %4\n\t" \
    "s_mov_b64 s[50:51], %5\n\t" \
    "s_mov_b32 s82, %7\n\t" \
    "s_load_dwordx8 s[84:91], %6, 0\n\t" \
    "v_mbcnt_lo_u32_b32 v223, -1, 0\n\t" \
    "v_mbcnt_hi_u32_b32 v223, -1, v223\n\t" \
    "s_getpc_b64 s[46:47]\n\t" \
    "rr_pc_%=:\n\t" \
    "s_lshl_b32 s69, s66, 13\n\t" \
    "v_lshl_add_u32 v222, v223, 7, s69\n\t" \
    "v_min_u32 v222, rr_last_%=-rr_pc_%=-4, v222\n\t" \
    "global_load_dword v222, v222, s[46:47]\n\t" \
    "v_lshlrev_b32 v223, 2, v223\n\t" \
    "v_add_u32 v223, s82, v223\n\t" \
    "ds_read_b32 v0, v223 offset:0\n\t" \
    "ds_read_b32 v1, v223 offset:256\n\t" \
    "ds_read_b32 v2, v223 offset:512\n\t" \
    "ds_read_b32 v3, v223 offset:768\n\t" \
    "ds_read_b32 v4, v223 offset:1024\n\t" \
    "ds_read_b32 v5, v223 offset:1280\n\t" \
    "ds_read_b32 v6, v223 offset:1536\n\t" \
    "ds_read_b32 v7, v223 offset:1792\n\t" \
    "ds_read_b32 v8, v223 offset:2048\n\t" \
    "ds_read_b32 v9, v223 offset:2304\n\t" \
    "ds_read_b32 v10, v223 offset:2560\n\t" \
    "ds_read_b32 v11, v223 offset:2816\n\t" \
    "s_waitcnt lgkmcnt(0)\n\t" \
    "ds_read_b32 v12, v223 offset:3072\n\t" \
    "ds_read_b32 v13, v223 offset:3328\n\t" \
    "ds_read_b32 v14, v223 offset:3584\n\t" \
    "ds_read_b32 v15, v223 offset:3840\n\t" \
    "ds_read_b32 v16, v223 offset:4096\n\t" \
    "ds_read_b32 v17, v223 offset:4352\n\t" \
    "ds_read_b32 v18, v223 offset:4608\n\t" \
    "ds_read_b32 v19, v223 offset:4864\n\t" \
    "ds_read_b32 v20, v223 offset:5120\n\t" \
    "ds_read_b32 v21, v223 offset:5376\n\t" \
    "ds_read_b32 v22, v223 offset:5632\n\t" \
    "ds_read_b32 v23, v223 offset:5888\n\t" \
    "s_waitcnt lgkmcnt(0)\n\t" \
    "ds_read_b32 v24, v223 offset:6144\n\t" \
    "ds_read_b32 v25, v223 offset:6400\n\t" \
    "ds_read_b32 v26, v223 offset:6656\n\t" \
    "ds_read_b32 v27, v223 offset:6912\n\t" \
    "ds_read_b32 v28, v223 offset:7168\n\t" \
    "ds_read_b32 v29, v223 offset:7424\n\t" \
    "ds_read_b32 v30, v223 offset:7680\n\t" \
    "ds_read_b32 v31, v223 offset:7936\n\t" \
    "ds_read_b32 v32, v223 offset:8192\n\t" \
    "ds_read_b32 v33, v223 offset:8448\n\t" \
    "ds_read_b32 v34, v223 offset:8704\n\t" \
    "ds_read_b32 v35, v223 offset:8960\n\t" \
    "s_waitcnt lgkmcnt(0)\n\t" \
    "ds_read_b32 v36, v223 offset:9216\n\t" \
    "ds_read_b32 v37, v223 offset:9472\n\t" \
    "ds_read_b32 v38, v223 offset:9728\n\t" \
    "ds_read_b32 v39, v223 offset:9984\n\t" \
    "ds_read_b32 v40, v223 offset:10240\n\t" \
    "ds_read_b32 v41, v223 offset:10496\n\t" \
    "ds_read_b32 v42, v223 offset:10752\n\t" \
    "ds_read_b32 v43, v223 offset:11008\n\t" \
    "ds_read_b32 v44, v223 offset:11264\n\t" \
    "ds_read_b32 v45, v223 offset:11520\n\t" \
    "ds_read_b32 v46, v223 offset:11776\n\t" \
    "ds_read_b32 v164, v223 offset:12032\n\t" \
    "s_waitcnt lgkmcnt(0)\n\t" \
    "ds_read_b32 v165, v223 offset:12288\n\t" \
    "ds_read_b32 v166, v223 offset:12544\n\t" \
    "ds_read_b32 v167, v223 offset:12800\n\t" \
    "ds_read_b32 v196, v223 offset:13312\n\t" \
    "ds_read_b32 v197, v223 offset:13568\n\t" \
    "ds_read_b32 v198, v223 offset:13824\n\t" \
    "ds_read_b32 v199, v223 offset:14080\n\t" \
    "ds_read_b32 v200, v223 offset:14336\n\t" \
    "s_waitcnt lgkmcnt(0)\n\t" \
    "s_mov_b32 s68, 262144\n\t" \
    "s_mov_b32 s72, 0\n\t" \
    "s_mov_b32 s83, -1\n\t" \
    "s_mov_b32 s65, 0\n\t" \
    "v_add_u32 v181, 16384, v2\n\t" \
    "v_add_u32 v182, 131072, v2\n\t" \
    "v_add_u32 v183, 147456, v2\n\t" \
    "s_mov_b32 s77, 3019898880\n\t" \
    "s_mov_b32 s78, 3054198966\n\t" \
    "s_mov_b32 s71, 3\n\t" \
    "rr_E1_%=:\n\t" \
    "s_memtime s[54:55]\n\t" \
    "s_nop 0\n\t" \
    "s_memrealtime s[4:5]\n\t" \
    "s_nop 0\n\t" \
    "s_memrealtime s[6:7]\n\t" \
    "s_nop 0\n\t" \
    "s_memrealtime s[8:9]\n\t" \
    "s_nop 0\n\t" \
    "s_memrealtime s[10:11]\n\t" \
    "s_nop 0\n\t" \
    "s_memrealtime s[12:13]\n\t" \
    "s_nop 0\n\t" \
    "s_memrealtime s[14:15]\n\t" \
    "s_nop 0\n\t" \
    "s_memrealtime s[16:17]\n\t" \
    "s_nop 0\n\t" \
    "s_memrealtime s[18:19]\n\t" \
    "s_nop 0\n\t" \
    "s_memrealtime s[20:21]\n\t" \
    "s_nop 0\n\t" \
    "s_memrealtime s[22:23]\n\t" \
    "s_nop 0\n\t" \
    "s_memrealtime s[24:25]\n\t" \
    "s_nop 0\n\t" \
    "s_memrealtime s[26:27]\n\t" \
    "s_nop 0\n\t" \
    "s_memrealtime s[28:29]\n\t" \
    "s_nop 0\n\t" \
    "s_memtime s[56:57]\n\t" \
    "s_mov_b32 s70, 1\n\t" \
    "s_sub_u32 s71, s71, 1\n\t" \
    "s_cselect_b32 s69, 0, -1\n\t" \
    "s_waitcnt lgkmcnt(0)\n\t" \
    "s_sub_u32 s54, s56, s54\n\t" \
    "s_and_b32 s54, s54, s69\n\t" \
    "s_cmp_gt_u32 s54, 124\n\t" \
    "s_cbranch_scc1 rr_E1_%=\n\t" \
    "s_cmp_eq_u32 s4, s28\n\t" \
    "s_addc_u32 s70, s70, 0\n\t" \
    "s_cmp_eq_u32 s6, s28\n\t" \
    "s_addc_u32 s70, s70, 0\n\t" \
    "s_cmp_eq_u32 s8, s28\n\t" \
    "s_addc_u32 s70, s70, 0\n\t" \
    "s_cmp_eq_u32 s10, s28\n\t" \
    "s_addc_u32 s70, s70, 0\n\t" \
    "s_cmp_eq_u32 s12, s28\n\t" \
    "s_addc_u32 s70, s70, 0\n\t" \
    "s_cmp_eq_u32 s14, s28\n\t" \
    "s_addc_u32 s70, s70, 0\n\t" \
    "s_cmp_eq_u32 s16, s28\n\t" \
    "s_addc_u32 s70, s70, 0\n\t" \
    "s_cmp_eq_u32 s18, s28\n\t" \
    "s_addc_u32 s70, s70, 0\n\t" \
    "s_cmp_eq_u32 s20, s28\n\t" \
    "s_addc_u32 s70, s70, 0\n\t" \
    "s_cmp_eq_u32 s22, s28\n\t" \
    "s_addc_u32 s70, s70, 0\n\t" \
    "s_cmp_eq_u32 s24, s28\n\t" \
    "s_addc_u32 s70, s70, 0\n\t" \
    "s_cmp_eq_u32 s26, s28\n\t" \
    "s_addc_u32 s70, s70, 0\n\t" \
    "s_mov_b32 s75, s73\n\t" \
    "s_mov_b32 s76, s74\n\t" \
    "s_lshl_b32 s70, s70, 3\n\t" \
    "s_sub_u32 s73, s56, s70\n\t" \
    "s_lshl_b32 s74, s28, 4\n\t" \
    "v_mov_b32 v172, 0x1f0000\n\t" \
    "v_mov_b32 v173, 0x3f0000\n\t" \
    "global_load_dword v172, v172, s[36:37]\n\t" \
    "global_load_dword v173, v173, s[36:37]\n\t" \
    "s_mov_b32 s65, 63\n\t" \
    "s_mul_i32 s70, s67, 7\n\t" \
    "s_add_u32 s69, s65, 0\n\t" \
    "s_xor_b32 s69, s69, s70\n\t" \
    "s_and_b32 s69, s69, 63\n\t" \
    "s_lshl_b32 s69, s69, 15\n\t" \
    "s_mul_i32 s70, s67, 2097152\n\t" \
    "s_add_u32 s69, s69, s70\n\t" \
    "s_add_u32 s69, s69, 48234496\n\t" \
    "s_add_u32 s38, s36, s69\n\t" \
    "s_addc_u32 s39, s37, 0\n\t" \
    "v_mov_b32 v174, 0\n\t" \
    "global_load_dword v174, v174, s[38:39]\n\t" \
    "v_mbcnt_lo_u32_b32 v184, -1, 0\n\t" \
    "v_mbcnt_hi_u32_b32 v184, -1, v184\n\t" \
    "v_min_u32 v184, 59, v184\n\t" \
    "v_lshlrev_b32 v184, 6, v184\n\t" \
    "v_add_u32 v184, 6258688, v184\n\t" \
    "v_mov_b32 v185, 6266880\n\t" \
    "global_load_dword v185, v185, s[36:37] sc1\n\t" \
    "global_load_dwordx2 v[186:187], v184, s[36:37] sc1\n\t" \
    "s_waitcnt vmcnt(0)\n\t" \
    "v_sub_u32 v186, v186, v185\n\t" \
    "v_cmp_ne_u32 vcc, 1, v186\n\t" \
    "s_cbranch_vccz rr_Cok4_%=\n\t" \
    "rr_C3_%=:\n\t" \
    "global_load_dwordx2 v[186:187], v184, s[36:37] sc1\n\t" \
    "s_waitcnt vmcnt(0)\n\t" \
    "v_sub_u32 v186, v186, v185\n\t" \
    "v_cmp_ne_u32 vcc, 1, v186\n\t" \
    "s_cbranch_vccz rr_Cok4_%=\n\t" \
    "s_cmp_eq_u32 s68, 0\n\t" \
    "s_cbranch_scc1 rr_F5_%=\n\t" \
    "s_sub_u32 s68, s68, 1\n\t" \
    "s_sleep 4\n\t" \
    "s_branch rr_C3_%=\n\t" \
    "rr_F5_%=:\n\t" \
    "v_mov_b32 v188, 6095104\n\t" \
    "v_mov_b32 v189, 4\n\t" \
    "global_store_dword v188, v189, s[36:37] sc1\n\t" \
    "s_waitcnt vmcnt(0)\n\t" \
    "s_branch rr_Cok4_%=\n\t" \
    "rr_Cok4_%=:\n\t" \
    "v_sub_u32_dpp v186, v187, v187 row_shl:6 row_mask:0xf bank_mask:0xf\n\t" \
    "v_min_u32_dpp v187, v187, v187 row_shl:6 row_mask:0xf bank_mask:0xf\n\t" \
    "v_min_i32 v186, 64, v186\n\t" \
    "v_max_i32 v186, -64, v186\n\t" \
    "s_mov_b32 s33, 0\n\t" \
    "v_readlane_b32 s69, v187, 0\n\t" \
    "v_readlane_b32 s70, v187, 1\n\t" \
    "v_readlane_b32 s54, v186, 0\n\t" \
    "v_readlane_b32 s55, v186, 1\n\t" \
    "s_nop 1\n\t" \
    "s_cmp_lt_u32 s70, s69\n\t" \
    "s_cselect_b32 s62, 8192, 0\n\t" \
    "s_cselect_b32 s54, s55, s54\n\t" \
    "s_cselect_b32 s69, s70, s69\n\t" \
    "s_mov_b32 s71, s69\n\t" \
    "s_add_u32 s33, s33, s54\n\t" \
    "v_readlane_b32 s69, v187, 2\n\t" \
    "v_readlane_b32 s70, v187, 3\n\t" \
    "v_readlane_b32 s54, v186, 2\n\t" \
    "v_readlane_b32 s55, v186, 3\n\t" \
    "s_nop 1\n\t" \
    "s_sub_u32 s55, 0, s55\n\t" \
    "s_cmp_lt_u32 s70, s69\n\t" \
    "s_cselect_b32 s63, 8192, 0\n\t" \
    "s_cselect_b32 s54, s55, s54\n\t" \
    "s_cselect_b32 s69, s70, s69\n\t" \
    "s_min_u32 s71, s69, s71\n\t" \
    "s_add_u32 s33, s33, s54\n\t" \
    "v_readlane_b32 s69, v187, 4\n\t" \
    "v_readlane_b32 s70, v187, 5\n\t" \
    "v_readlane_b32 s54, v186, 4\n\t" \
    "v_readlane_b32 s55, v186, 5\n\t" \
    "s_nop 1\n\t" \
    "s_sub_u32 s54, 0, s54\n\t" \
    "s_sub_u32 s55, 0, s55\n\t" \
    "s_cmp_lt_u32 s70, s69\n\t" \
    "s_cselect_b32 s64, 8192, 0\n\t" \
    "s_cselect_b32 s54, s55, s54\n\t" \
    "s_cselect_b32 s69, s70, s69\n\t" \
    "s_min_u32 s71, s69, s71\n\t" \
    "s_add_u32 s33, s33, s54\n\t" \
    "s_add_u32 s63, s63, 0x200000\n\t" \
    "s_xor_b32 s52, s64, 8192\n\t" \
    "s_cmp_gt_u32 s71, 625\n\t" \
    "s_cselect_b32 s69, 1, 0\n\t" \
    "s_mul_i32 s70, s69, 160\n\t" \
    "s_add_u32 s86, s86, s70\n\t" \
    "s_mul_i32 s70, s69, 536\n\t" \
    "s_add_u32 s87, s87, s70\n\t" \
    "s_mul_i32 s70, s69, 256\n\t" \
    "s_add_u32 s88, s88, s70\n\t" \
    "v_xor_b32 v6, s52, v6\n\t" \
    "v_lshlrev_b32 v178, 3, v0\n\t" \
    "v_and_b32 v178, 0xf80, v178\n\t" \
    "s_add_u32 s69, s52, 6148096\n\t" \
    "v_add_u32 v178, s69, v178\n\t" \
    "v_mov_b32 v172, -1\n\t" \
    "v_mov_b32 v173, -1\n\t" \
    "v_mov_b32 v174, -1\n\t" \
    "v_mov_b32 v175, -1\n\t" \
    "v_mov_b32 v170, 0\n\t" \
    "s_mov_b32 s65, s66\n\t" \
    "s_sub_u32 s80, s74, 32000\n\t" \
    "v_mov_b32 v177, -1\n\t" \
    "s_cmp_lg_u32 s51, 0\n\t" \
    "s_cselect_b32 s42, 0, 0\n\t" \
    "s_cselect_b32 s43, 32, 0\n\t" \
    "s_cmp_le_u32 s51, s65\n\t" \
    "s_cbranch_scc1 rr_n316_%=\n\t" \
    "s_add_u32 s69, s65, 2\n\t" \
    "s_mul_i32 s70, s69, 171\n\t" \
    "s_lshr_b32 s70, s70, 9\n\t" \
    "s_mul_i32 s70, s70, 3\n\t" \
    "s_sub_u32 s69, s69, s70\n\t" \
    "s_mul_i32 s69, s69, 36\n\t" \
    "s_lshl_b32 s69, s69, 14\n\t" \
    "s_add_u32 s69, s69, s64\n\t" \
    "s_add_u32 s69, s69, 4194304\n\t" \
    "s_add_u32 s40, s36, s69\n\t" \
    "s_addc_u32 s41, s37, 0\n\t" \
    "global_store_dwordx4 v196, v[172:175], s[40:41] sc1\n\t" \
    "global_store_dwordx4 v197, v[172:175], s[40:41] sc1\n\t" \
    "global_store_dwordx4 v198, v[172:175], s[40:41] sc1\n\t" \
    "s_add_u32 s69, s65, 2\n\t" \
    "s_mul_i32 s70, s69, 171\n\t" \
    "s_lshr_b32 s70, s70, 9\n\t" \
    "s_mul_i32 s70, s70, 3\n\t" \
    "s_sub_u32 s69, s69, s70\n\t" \
    "s_mul_i32 s69, s69, 36\n\t" \
    "s_add_u32 s69, s69, s67\n\t" \
    "s_lshl_b32 s69, s69, 14\n\t" \
    "s_add_u32 s69, s69, s64\n\t" \
    "s_add_u32 s69, s69, 4194304\n\t" \
    "s_add_u32 s40, s36, s69\n\t" \
    "s_addc_u32 s41, s37, 0\n\t" \
    "global_store_dwordx4 v0, v[172:175], s[40:41] offset:0 sc1\n\t" \
    "global_store_dwordx4 v0, v[172:175], s[40:41] offset:1024 sc1\n\t" \
    "global_store_dwordx4 v0, v[172:175], s[40:41] offset:2048 sc1\n\t" \
    "global_store_dwordx4 v0, v[172:175], s[40:41] offset:3072 sc1\n\t" \
    "global_store_dwordx4 v1, v[172:175], s[40:41] offset:0 sc1\n\t" \
    "global_store_dwordx4 v1, v[172:175], s[40:41] offset:1024 sc1\n\t" \
    "global_store_dwordx4 v1, v[172:175], s[40:41] offset:2048 sc1\n\t" \
    "global_store_dwordx4 v1, v[172:175], s[40:41] offset:3072 sc1\n\t" \
    "rr_n316_%=:\n\t" \
    "s_cmp_eq_u32 s51, 0\n\t" \
    "s_cbranch_scc1 rr_arm17_%=\n\t" \
    "s_cmp_gt_u32 s65, 61\n\t" \
    "s_cbranch_scc1 rr_arm17_%=\n\t" \
    "s_add_u32 s69, s65, 2\n\t" \
    "s_bitcmp1_b32 s69, 0\n\t" \
    "s_cselect_b32 s70, s63, s62\n\t" \
    "s_bfe_u32 s69, s69, 0x20001\n\t" \
    "s_lshl_b32 s69, s69, 19\n\t" \
    "s_add_u32 s69, s69, s70\n\t" \
    "s_add_u32 s46, s36, s69\n\t" \
    "s_addc_u32 s47, s37, 0\n\t" \
    "global_store_dwordx4 v164, v[172:175], s[46:47] sc1\n\t" \
    "global_store_dwordx4 v165, v[172:175], s[46:47] sc1\n\t" \
    "global_store_dwordx4 v166, v[172:175], s[46:47] sc1\n\t" \
    "global_store_dwordx4 v167, v[172:175], s[46:47] sc1\n\t" \
    "rr_arm17_%=:\n\t" \
    "s_waitcnt vmcnt(0)\n\t" \
    "s_cmp_gt_i32 s33, 0\n\t" \
    "s_cselect_b32 s70, 1, 0\n\t" \
    "s_mul_i32 s69, s67, 57\n\t" \
    "s_lshr_b32 s69, s69, 9\n\t" \
    "s_xor_b32 s69, s69, s70\n\t" \
    "s_and_b32 s33, s69, 1\n\t" \
    "s_and_b32 s69, s33, 1\n\t" \
    "s_lshl_b32 s69, s69, 14\n\t" \
    "s_add_u32 s69, s69, s62\n\t" \
    "s_add_u32 s52, s36, s69\n\t" \
    "s_addc_u32 s53, s37, 0\n\t" \
    "v_lshrrev_b32 v187, 9, v0\n\t" \
    "v_mul_i32_i24 v187, -2044, v187\n\t" \
    "v_add_u32 v187, v187, v7\n\t" \
    "v_add_u32 v187, 3072, v187\n\t" \
    "v_mov_b32 v189, -1\n\t" \
    "s_sleep 8\n\t" \
    "s_mov_b32 s71, 3\n\t" \
    "rr_E18_%=:\n\t" \
    "s_memtime s[54:55]\n\t" \
    "s_nop 0\n\t" \
    "s_memrealtime s[4:5]\n\t" \
    "s_nop 0\n\t" \
    "s_memrealtime s[6:7]\n\t" \
    "s_nop 0\n\t" \
    "s_memrealtime s[8:9]\n\t" \
    "s_nop 0\n\t" \
    "s_memrealtime s[10:11]\n\t" \
    "s_nop 0\n\t" \
    "s_memrealtime s[12:13]\n\t" \
    "s_nop 0\n\t" \
    "s_memrealtime s[14:15]\n\t" \
    "s_nop 0\n\t" \
    "s_memrealtime s[16:17]\n\t" \
    "s_nop 0\n\t" \
    "s_memrealtime s[18:19]\n\t" \
    "s_nop 0\n\t" \
    "s_memrealtime s[20:21]\n\t" \
    "s_nop 0\n\t" \
    "s_memrealtime s[22:23]\n\t" \
    "s_nop 0\n\t" \
    "s_memrealtime s[24:25]\n\t" \
    "s_nop 0\n\t" \
    "s_memrealtime s[26:27]\n\t" \
    "s_nop 0\n\t" \
    "s_memrealtime s[28:29]\n\t" \
    "s_nop 0\n\t" \
    "s_memtime s[56:57]\n\t" \
    "s_mov_b32 s70, 1\n\t" \
    "s_sub_u32 s71, s71, 1\n\t" \
    "s_cselect_b32 s69, 0, -1\n\t" \
    "s_waitcnt lgkmcnt(0)\n\t" \
    "s_sub_u32 s54, s56, s54\n\t" \
    "s_and_b32 s54, s54, s69\n\t" \
    "s_cmp_gt_u32 s54, 124\n\t" \
    "s_cbranch_scc1 rr_E18_%=\n\t" \
    "s_cmp_eq_u32 s4, s28\n\t" \
    "s_addc_u32 s70, s70, 0\n\t" \
    "s_cmp_eq_u32 s6, s28\n\t" \
    "s_addc_u32 s70, s70, 0\n\t" \
    "s_cmp_eq_u32 s8, s28\n\t" \
    "s_addc_u32 s70, s70, 0\n\t" \
    "s_cmp_eq_u32 s10, s28\n\t" \
    "s_addc_u32 s70, s70, 0\n\t" \
    "s_cmp_eq_u32 s12, s28\n\t" \
    "s_addc_u32 s70, s70, 0\n\t" \
    "s_cmp_eq_u32 s14, s28\n\t" \
    "s_addc_u32 s70, s70, 0\n\t" \
    "s_cmp_eq_u32 s16, s28\n\t" \
    "s_addc_u32 s70, s70, 0\n\t" \
    "s_cmp_eq_u32 s18, s28\n\t" \
    "s_addc_u32 s70, s70, 0\n\t" \
    "s_cmp_eq_u32 s20, s28\n\t" \
    "s_addc_u32 s70, s70, 0\n\t" \
    "s_cmp_eq_u32 s22, s28\n\t" \
    "s_addc_u32 s70, s70, 0\n\t" \
    "s_cmp_eq_u32 s24, s28\n\t" \
    "s_addc_u32 s70, s70, 0\n\t" \
    "s_cmp_eq_u32 s26, s28\n\t" \
    "s_addc_u32 s70, s70, 0\n\t" \
    "s_mov_b32 s75, s73\n\t" \
    "s_mov_b32 s76, s74\n\t" \
    "s_lshl_b32 s70, s70, 3\n\t" \
    "s_sub_u32 s73, s56, s70\n\t" \
    "s_lshl_b32 s74, s28, 4\n\t" \
    "s_sub_u32 s69, s73, s75\n\t" \
    "s_sub_u32 s70, s74, s76\n\t" \
    "s_sub_u32 s71, s70, 1600\n\t" \
    "s_cmp_lt_u32 s71, 400000\n\t" \
    "s_cbranch_scc0 rr_K20_%=\n\t" \
    "v_cvt_f32_u32 v190, s69\n\t" \
    "v_cvt_f32_u32 v191, s70\n\t" \
    "v_rcp_f32 v192, v190\n\t" \
    "v_rcp_f32 v193, v191\n\t" \
    "v_mul_f32 v192, v192, v191\n\t" \
    "v_mul_f32 v193, v193, v190\n\t" \
    "v_mul_f32 v192, 0x4f800000, v192\n\t" \
    "v_mul_f32 v193, 0x4f000000, v193\n\t" \
    "v_cvt_u32_f32 v192, v192\n\t" \
    "v_cvt_u32_f32 v193, v193\n\t" \
    "v_readfirstlane_b32 s69, v192\n\t" \
    "v_readfirstlane_b32 s70, v193\n\t" \
    "s_nop 1\n\t" \
    "s_mov_b32 s78, s69\n\t" \
    "s_mov_b32 s77, s70\n\t" \
    "rr_K20_%=:\n\t" \
    "s_add_u32 s69, s65, 0\n\t" \
    "s_bitcmp1_b32 s69, 0\n\t" \
    "s_cselect_b32 s70, s63, s62\n\t" \
    "s_bfe_u32 s69, s69, 0x20001\n\t" \
    "s_lshl_b32 s69, s69, 19\n\t" \
    "s_add_u32 s69, s69, s70\n\t" \
    "s_add_u32 s46, s36, s69\n\t" \
    "s_addc_u32 s47, s37, 0\n\t" \
    "s_add_u32 s69, s65, 0\n\t" \
    "s_lshr_b32 s69, s69, 1\n\t" \
    "s_xor_b32 s69, s69, s33\n\t" \
    "s_and_b32 s69, s69, 1\n\t" \
    "s_lshl_b32 s69, s69, 14\n\t" \
    "s_or_b32 s46, s46, s69\n\t" \
    "s_add_u32 s69, s65, 1\n\t" \
    "s_bitcmp1_b32 s69, 0\n\t" \
    "s_cselect_b32 s70, s63, s62\n\t" \
    "s_bfe_u32 s69, s69, 0x20001\n\t" \
    "s_lshl_b32 s69, s69, 19\n\t" \
    "s_add_u32 s69, s69, s70\n\t" \
    "s_add_u32 s48, s36, s69\n\t" \
    "s_addc_u32 s49, s37, 0\n\t" \
    "s_mul_i32 s70, s67, 7\n\t" \
    "s_add_u32 s69, s65, 0\n\t" \
    "s_xor_b32 s69, s69, s70\n\t" \
    "s_and_b32 s69, s69, 63\n\t" \
    "s_lshl_b32 s69, s69, 15\n\t" \
    "s_mul_i32 s70, s67, 2097152\n\t" \
    "s_add_u32 s69, s69, s70\n\t" \
    "s_add_u32 s69, s69, 48234496\n\t" \
    "s_add_u32 s38, s36, s69\n\t" \
    "s_addc_u32 s39, s37, 0\n\t" \
    "s_add_u32 s69, s65, 0\n\t" \
    "s_mul_i32 s70, s69, 171\n\t" \
    "s_lshr_b32 s70, s70, 9\n\t" \
    "s_mul_i32 s70, s70, 3\n\t" \
    "s_sub_u32 s69, s69, s70\n\t" \
    "s_mul_i32 s69, s69, 36\n\t" \
    "s_add_u32 s69, s69, s67\n\t" \
    "s_lshl_b32 s69, s69, 14\n\t" \
    "s_add_u32 s69, s69, s64\n\t" \
    "s_add_u32 s69, s69, 4194304\n\t" \
    "s_add_u32 s40, s36, s69\n\t" \
    "s_addc_u32 s41, s37, 0\n\t" \
    "s_mov_b32 s91, 0\n\t" \
    "s_mov_b32 s96, 0x55555555\n\t" \
    "s_mov_b32 s58, 0\n\t" \
    "s_mov_b32 s59, -1\n\t" \
    "v_mov_b32 v160, 0\n\t" \
    "v_mov_b32 v161, 0\n\t" \
    "v_mov_b32 v162, 0\n\t" \
    "v_mov_b32 v163, 0\n\t" \
    "v_mov_b32 v185, 0\n\t" \
    "s_mov_b32 s91, 3\n\t" \
    "s_mov_b64 exec, 0\n\t" \
    "s_branch rr_h025_%=\n\t" \
    "rr_dry28_%=:\n\t" \
    "s_mov_b64 exec, -1\n\t" \
    "s_mov_b32 s91, 0\n\t" \
    "s_waitcnt vmcnt(0) lgkmcnt(0)\n\t" \
    "s_nop 7\n\t" \
    "v_mov_b32 v160, 0\n\t" \
    "v_mov_b32 v161, 0\n\t" \
    "v_mov_b32 v162, 0\n\t" \
    "v_mov_b32 v163, 0\n\t" \
    "rr_p129_%=:\n\t" \
    "s_mov_b32 exec_lo, 0\n\t" \
    "global_load_dword v189, v187, s[52:53] sc1\n\t" \
    "s_mov_b64 exec, -1\n\t" \
    "s_waitcnt vmcnt(0)\n\t" \
    "v_cmp_eq_u32 vcc, -1, v189\n\t" \
    "s_nop 1\n\t" \
    "s_cmp_eq_u32 vcc_hi, 0\n\t" \
    "s_cbranch_scc1 rr_p1x30_%=\n\t" \
    "s_cmp_eq_u32 s68, 0\n\t" \
    "s_cbranch_scc1 rr_F31_%=\n\t" \
    "s_sub_u32 s68, s68, 1\n\t" \
    "s_sleep 10\n\t" \
    "s_branch rr_p129_%=\n\t" \
    "rr_F31_%=:\n\t" \
    "v_mov_b32 v184, 6095104\n\t" \
    "v_mov_b32 v185, 13\n\t" \
    "global_store_dword v184, v185, s[36:37] sc1\n\t" \
    "s_waitcnt vmcnt(0)\n\t" \
    "s_branch rr_p1x30_%=\n\t" \
    "rr_p1x30_%=:\n\t" \
    "s_mov_b32 s58, 1\n\t" \
    "rr_pg21_%=:\n\t" \
    "global_load_dword v177, v6, s[36:37] sc1\n\t" \
    "s_waitcnt vmcnt(0)\n\t" \
    "v_cmp_le_i32 vcc, s65, v177\n\t" \
    "s_nop 1\n\t" \
    "s_lshr_b32 s69, vcc_hi, 1\n\t" \
    "s_and_b32 s69, s69, vcc_hi\n\t" \
    "s_and_b32 s54, s69, s96\n\t" \
    "s_andn2_b32 s96, s96, s54\n\t" \
    "s_mov_b32 s55, s54\n\t" \
    "s_lshr_b64 s[54:55], s[54:55], s50\n\t" \
    "s_and_b32 s69, s54, 85\n\t" \
    "s_cbranch_scc0 rr_pq32_%=\n\t" \
    "s_bitcmp1_b32 s54, 0\n\t" \
    "s_cbranch_scc0 rr_pk33_%=\n\t" \
    "global_load_dwordx4 a[0:3], v26, s[38:39] offset:0 sc1\n\t" \
    "global_load_dwordx4 a[4:7], v26, s[38:39] offset:1024 sc1\n\t" \
    "rr_pk33_%=:\n\t" \
    "s_bitcmp1_b32 s54, 2\n\t" \
    "s_cbranch_scc0 rr_pk34_%=\n\t" \
    "global_load_dwordx4 a[8:11], v27, s[38:39] offset:0 sc1\n\t" \
    "global_load_dwordx4 a[12:15], v27, s[38:39] offset:1024 sc1\n\t" \
    "rr_pk34_%=:\n\t" \
    "s_bitcmp1_b32 s54, 4\n\t" \
    "s_cbranch_scc0 rr_pk35_%=\n\t" \
    "global_load_dwordx4 a[16:19], v28, s[38:39] offset:0 sc1\n\t" \
    "global_load_dwordx4 a[20:23], v28, s[38:39] offset:1024 sc1\n\t" \
    "rr_pk35_%=:\n\t" \
    "s_bitcmp1_b32 s54, 6\n\t" \
    "s_cbranch_scc0 rr_pk36_%=\n\t" \
    "global_load_dwordx4 a[24:27], v29, s[38:39] offset:0 sc1\n\t" \
    "global_load_dwordx4 a[28:31], v29, s[38:39] offset:1024 sc1\n\t" \
    "rr_pk36_%=:\n\t" \
    "rr_pq32_%=:\n\t" \
    "s_and_b32 s69, s54, 21760\n\t" \
    "s_cbranch_scc0 rr_pq37_%=\n\t" \
    "s_bitcmp1_b32 s54, 8\n\t" \
    "s_cbranch_scc0 rr_pk38_%=\n\t" \
    "global_load_dwordx4 a[32:35], v30, s[38:39] offset:0 sc1\n\t" \
    "global_load_dwordx4 a[36:39], v30, s[38:39] offset:1024 sc1\n\t" \
    "rr_pk38_%=:\n\t" \
    "s_bitcmp1_b32 s54, 10\n\t" \
    "s_cbranch_scc0 rr_pk39_%=\n\t" \
    "global_load_dwordx4 a[40:43], v31, s[38:39] offset:0 sc1\n\t" \
    "global_load_dwordx4 a[44:47], v31, s[38:39] offset:1024 sc1\n\t" \
    "rr_pk39_%=:\n\t" \
    "s_bitcmp1_b32 s54, 12\n\t" \
    "s_cbranch_scc0 rr_pk40_%=\n\t" \
    "global_load_dwordx4 a[48:51], v32, s[38:39] offset:0 sc1\n\t" \
    "global_load_dwordx4 a[52:55], v32, s[38:39] offset:1024 sc1\n\t" \
    "rr_pk40_%=:\n\t" \
    "s_bitcmp1_b32 s54, 14\n\t" \
    "s_cbranch_scc0 rr_pk41_%=\n\t" \
    "global_load_dwordx4 a[56:59], v33, s[38:39] offset:0 sc1\n\t" \
    "global_load_dwordx4 a[60:63], v33, s[38:39] offset:1024 sc1\n\t" \
    "rr_pk41_%=:\n\t" \
    "rr_pq37_%=:\n\t" \
    "s_and_b32 s69, s54, 5570560\n\t" \
    "s_cbranch_scc0 rr_pq42_%=\n\t" \
    "s_bitcmp1_b32 s54, 16\n\t" \
    "s_cbranch_scc0 rr_pk43_%=\n\t" \
    "global_load_dwordx4 a[64:67], v34, s[38:39] offset:0 sc1\n\t" \
    "global_load_dwordx4 a[68:71], v34, s[38:39] offset:1024 sc1\n\t" \
    "rr_pk43_%=:\n\t" \
    "s_bitcmp1_b32 s54, 18\n\t" \
    "s_cbranch_scc0 rr_pk44_%=\n\t" \
    "global_load_dwordx4 a[72:75], v35, s[38:39] offset:0 sc1\n\t" \
    "global_load_dwordx4 a[76:79], v35, s[38:39] offset:1024 sc1\n\t" \
    "rr_pk44_%=:\n\t" \
    "s_bitcmp1_b32 s54, 20\n\t" \
    "s_cbranch_scc0 rr_pk45_%=\n\t" \
    "global_load_dwordx4 a[80:83], v36, s[38:39] offset:0 sc1\n\t" \
    "global_load_dwordx4 a[84:87], v36, s[38:39] offset:1024 sc1\n\t" \
    "rr_pk45_%=:\n\t" \
    "s_bitcmp1_b32 s54, 22\n\t" \
    "s_cbranch_scc0 rr_pk46_%=\n\t" \
    "global_load_dwordx4 a[88:91], v37, s[38:39] offset:0 sc1\n\t" \
    "global_load_dwordx4 a[92:95], v37, s[38:39] offset:1024 sc1\n\t" \
    "rr_pk46_%=:\n\t" \
    "rr_pq42_%=:\n\t" \
    "s_and_b32 s69, s54, 1426063360\n\t" \
    "s_cbranch_scc0 rr_pq47_%=\n\t" \
    "s_bitcmp1_b32 s54, 24\n\t" \
    "s_cbranch_scc0 rr_pk48_%=\n\t" \
    "global_load_dwordx4 a[96:99], v38, s[38:39] offset:0 sc1\n\t" \
    "global_load_dwordx4 a[100:103], v38, s[38:39] offset:1024 sc1\n\t" \
    "rr_pk48_%=:\n\t" \
    "s_bitcmp1_b32 s54, 26\n\t" \
    "s_cbranch_scc0 rr_pk49_%=\n\t" \
    "global_load_dwordx4 a[104:107], v39, s[38:39] offset:0 sc1\n\t" \
    "global_load_dwordx4 a[108:111], v39, s[38:39] offset:1024 sc1\n\t" \
    "rr_pk49_%=:\n\t" \
    "s_bitcmp1_b32 s54, 28\n\t" \
    "s_cbranch_scc0 rr_pk50_%=\n\t" \
    "global_load_dwordx4 a[112:115], v40, s[38:39] offset:0 sc1\n\t" \
    "global_load_dwordx4 a[116:119], v40, s[38:39] offset:1024 sc1\n\t" \
    "rr_pk50_%=:\n\t" \
    "s_bitcmp1_b32 s54, 30\n\t" \
    "s_cbranch_scc0 rr_pk51_%=\n\t" \
    "global_load_dwordx4 a[120:123], v41, s[38:39] offset:0 sc1\n\t" \
    "global_load_dwordx4 a[124:127], v41, s[38:39] offset:1024 sc1\n\t" \
    "rr_pk51_%=:\n\t" \
    "rr_pq47_%=:\n\t" \
    "s_cmp_eq_u32 s58, 0\n\t" \
    "s_cbranch_scc1 rr_pn22_%=\n\t" \
    "s_mov_b32 s71, 3\n\t" \
    "rr_E52_%=:\n\t" \
    "s_memtime s[54:55]\n\t" \
    "s_nop 0\n\t" \
    "s_memrealtime s[4:5]\n\t" \
    "s_nop 0\n\t" \
    "s_memrealtime s[6:7]\n\t" \
    "s_nop 0\n\t" \
    "s_memrealtime s[8:9]\n\t" \
    "s_nop 0\n\t" \
    "s_memrealtime s[10:11]\n\t" \
    "s_nop 0\n\t" \
    "s_memrealtime s[12:13]\n\t" \
    "s_nop 0\n\t" \
    "s_memrealtime s[14:15]\n\t" \
    "s_nop 0\n\t" \
    "s_memrealtime s[16:17]\n\t" \
    "s_nop 0\n\t" \
    "s_memrealtime s[18:19]\n\t" \
    "s_nop 0\n\t" \
    "s_memrealtime s[20:21]\n\t" \
    "s_nop 0\n\t" \
    "s_memrealtime s[22:23]\n\t" \
    "s_nop 0\n\t" \
    "s_memrealtime s[24:25]\n\t" \
    "s_nop 0\n\t" \
    "s_memrealtime s[26:27]\n\t" \
    "s_nop 0\n\t" \
    "s_memrealtime s[28:29]\n\t" \
    "s_nop 0\n\t" \
    "s_memtime s[56:57]\n\t" \
    "s_mov_b32 s70, 1\n\t" \
    "s_sub_u32 s71, s71, 1\n\t" \
    "s_cselect_b32 s69, 0, -1\n\t" \
    "s_waitcnt lgkmcnt(0)\n\t" \
    "s_sub_u32 s54, s56, s54\n\t" \
    "s_and_b32 s54, s54, s69\n\t" \
    "s_cmp_gt_u32 s54, 124\n\t" \
    "s_cbranch_scc1 rr_E52_%=\n\t" \
    "s_cmp_eq_u32 s4, s28\n\t" \
    "s_addc_u32 s70, s70, 0\n\t" \
    "s_cmp_eq_u32 s6, s28\n\t" \
    "s_addc_u32 s70, s70, 0\n\t" \
    "s_cmp_eq_u32 s8, s28\n\t" \
    "s_addc_u32 s70, s70, 0\n\t" \
    "s_cmp_eq_u32 s10, s28\n\t" \
    "s_addc_u32 s70, s70, 0\n\t" \
    "s_cmp_eq_u32 s12, s28\n\t" \
    "s_addc_u32 s70, s70, 0\n\t" \
    "s_cmp_eq_u32 s14, s28\n\t" \
    "s_addc_u32 s70, s70, 0\n\t" \
    "s_cmp_eq_u32 s16, s28\n\t" \
    "s_addc_u32 s70, s70, 0\n\t" \
    "s_cmp_eq_u32 s18, s28\n\t" \
    "s_addc_u32 s70, s70, 0\n\t" \
    "s_cmp_eq_u32 s20, s28\n\t" \
    "s_addc_u32 s70, s70, 0\n\t" \
    "s_cmp_eq_u32 s22, s28\n\t" \
    "s_addc_u32 s70, s70, 0\n\t" \
    "s_cmp_eq_u32 s24, s28\n\t" \
    "s_addc_u32 s70, s70, 0\n\t" \
    "s_cmp_eq_u32 s26, s28\n\t" \
    "s_addc_u32 s70, s70, 0\n\t" \
    "s_mov_b32 s75, s73\n\t" \
    "s_mov_b32 s76, s74\n\t" \
    "s_lshl_b32 s70, s70, 3\n\t" \
    "s_sub_u32 s73, s56, s70\n\t" \
    "s_lshl_b32 s74, s28, 4\n\t" \
    "s_sub_u32 s69, s73, s75\n\t" \
    "s_sub_u32 s70, s74, s76\n\t" \
    "s_sub_u32 s71, s70, 1600\n\t" \
    "s_cmp_lt_u32 s71, 400000\n\t" \
    "s_cbranch_scc0 rr_K54_%=\n\t" \
    "v_cvt_f32_u32 v190, s69\n\t" \
    "v_cvt_f32_u32 v191, s70\n\t" \
    "v_rcp_f32 v192, v190\n\t" \
    "v_rcp_f32 v193, v191\n\t" \
    "v_mul_f32 v192, v192, v191\n\t" \
    "v_mul_f32 v193, v193, v190\n\t" \
    "v_mul_f32 v192, 0x4f800000, v192\n\t" \
    "v_mul_f32 v193, 0x4f000000, v193\n\t" \
    "v_cvt_u32_f32 v192, v192\n\t" \
    "v_cvt_u32_f32 v193, v193\n\t" \
    "v_readfirstlane_b32 s69, v192\n\t" \
    "v_readfirstlane_b32 s70, v193\n\t" \
    "s_nop 1\n\t" \
    "s_sub_u32 s69, s69, s78\n\t" \
    "s_ashr_i32 s69, s69, 2\n\t" \
    "s_add_u32 s78, s78, s69\n\t" \
    "s_sub_u32 s70, s70, s77\n\t" \
    "s_ashr_i32 s70, s70, 2\n\t" \
    "s_add_u32 s77, s77, s70\n\t" \
    "rr_K54_%=:\n\t" \
    "v_mov_b32 v184, v189\n\t" \
    "v_subrev_u32 v184, s80, v184\n\t" \
    "s_nop 1\n\t" \
    "v_max_i32_dpp v184, v184, v184 row_ror:1 row_mask:0xf bank_mask:0xf\n\t" \
    "s_nop 1\n\t" \
    "v_max_i32_dpp v184, v184, v184 row_ror:2 row_mask:0xf bank_mask:0xf\n\t" \
    "s_nop 1\n\t" \
    "v_max_i32_dpp v184, v184, v184 row_ror:4 row_mask:0xf bank_mask:0xf\n\t" \
    "s_nop 1\n\t" \
    "v_max_i32_dpp v184, v184, v184 row_ror:8 row_mask:0xf bank_mask:0xf\n\t" \
    "s_nop 0\n\t" \
    "v_readlane_b32 s69, v184, 0\n\t" \
    "v_readlane_b32 s71, v184, 16\n\t" \
    "v_readlane_b32 s70, v184, 32\n\t" \
    "v_readlane_b32 s81, v184, 48\n\t" \
    "s_max_i32 s69, s69, s71\n\t" \
    "s_nop 0\n\t" \
    "s_max_i32 s71, s70, s81\n\t" \
    "s_max_i32 s69, s69, s71\n\t" \
    "s_add_u32 s81, s69, s80\n\t" \
    "s_add_u32 s81, s71, s80\n\t" \
    "s_mov_b32 s58, 0\n\t" \
    "s_cmp_eq_u32 s96, 0\n\t" \
    "s_cbranch_scc1 rr_pe23_%=\n\t" \
    "s_sub_u32 s79, s81, 832\n\t" \
    "s_sub_u32 s70, s79, s74\n\t" \
    "s_max_i32 s70, s70, 0\n\t" \
    "s_min_i32 s70, s70, 16000\n\t" \
    "s_lshl_b32 s70, s70, 1\n\t" \
    "s_mul_hi_u32 s70, s70, s77\n\t" \
    "s_add_u32 s70, s70, s73\n\t" \
    "rr_W55_%=:\n\t" \
    "s_memtime s[56:57]\n\t" \
    "s_waitcnt lgkmcnt(0)\n\t" \
    "s_sub_u32 s69, s70, s56\n\t" \
    "s_cmp_gt_i32 s69, 24\n\t" \
    "s_cbranch_scc1 rr_W55_%=\n\t" \
    "s_mov_b32 exec_hi, 0\n\t" \
    "global_load_dword v189, v187, s[52:53] sc1\n\t" \
    "s_mov_b64 exec, -1\n\t" \
    "s_branch rr_pg21_%=\n\t" \
    "rr_pn22_%=:\n\t" \
    "s_cmp_eq_u32 s96, 0\n\t" \
    "s_cbranch_scc1 rr_pe23_%=\n\t" \
    "s_cmp_eq_u32 s68, 0\n\t" \
    "s_cbranch_scc1 rr_F56_%=\n\t" \
    "s_sub_u32 s68, s68, 1\n\t" \
    "s_sleep 2\n\t" \
    "s_branch rr_pg21_%=\n\t" \
    "rr_F56_%=:\n\t" \
    "v_mov_b32 v184, 6095104\n\t" \
    "v_mov_b32 v185, 10\n\t" \
    "global_store_dword v184, v185, s[36:37] sc1\n\t" \
    "s_waitcnt vmcnt(0)\n\t" \
    "s_branch rr_pe23_%=\n\t" \
    "rr_pe23_%=:\n\t" \
    "s_mov_b32 s83, s65\n\t" \
    "s_cmp_lg_u32 s65, 0\n\t" \
    "s_cbranch_scc1 rr_u227_%=\n\t" \
    "v_cmp_eq_u32 vcc, -1, v189\n\t" \
    "s_cbranch_vccz rr_p2x57_%=\n\t" \
    "rr_p258_%=:\n\t" \
    "s_mov_b32 exec_hi, 0\n\t" \
    "global_load_dword v189, v187, s[52:53] sc1\n\t" \
    "s_mov_b64 exec, -1\n\t" \
    "s_waitcnt vmcnt(0)\n\t" \
    "v_cmp_eq_u32 vcc, -1, v189\n\t" \
    "s_cbranch_vccz rr_p2x57_%=\n\t" \
    "s_cmp_eq_u32 s68, 0\n\t" \
    "s_cbranch_scc1 rr_F59_%=\n\t" \
    "s_sub_u32 s68, s68, 1\n\t" \
    "s_sleep 3\n\t" \
    "s_branch rr_p258_%=\n\t" \
    "rr_F59_%=:\n\t" \
    "v_mov_b32 v184, 6095104\n\t" \
    "v_mov_b32 v185, 13\n\t" \
    "global_store_dword v184, v185, s[36:37] sc1\n\t" \
    "s_waitcnt vmcnt(0)\n\t" \
    "s_branch rr_p2x57_%=\n\t" \
    "rr_p2x57_%=:\n\t" \
    "s_memtime s[56:57]\n\t" \
    "v_mov_b32 v184, v189\n\t" \
    "v_subrev_u32 v184, s80, v184\n\t" \
    "s_nop 1\n\t" \
    "v_max_i32_dpp v184, v184, v184 row_ror:1 row_mask:0xf bank_mask:0xf\n\t" \
    "s_nop 1\n\t" \
    "v_max_i32_dpp v184, v184, v184 row_ror:2 row_mask:0xf bank_mask:0xf\n\t" \
    "s_nop 1\n\t" \
    "v_max_i32_dpp v184, v184, v184 row_ror:4 row_mask:0xf bank_mask:0xf\n\t" \
    "s_nop 1\n\t" \
    "v_max_i32_dpp v184, v184, v184 row_ror:8 row_mask:0xf bank_mask:0xf\n\t" \
    "s_nop 0\n\t" \
    "v_readlane_b32 s69, v184, 0\n\t" \
    "v_readlane_b32 s71, v184, 16\n\t" \
    "s_nop 1\n\t" \
    "s_max_i32 s69, s69, s71\n\t" \
    "s_add_u32 s81, s69, s80\n\t" \
    "s_waitcnt lgkmcnt(0)\n\t" \
    "s_sub_u32 s69, s56, s73\n\t" \
    "s_mul_hi_u32 s69, s69, s78\n\t" \
    "s_add_u32 s69, s69, s74\n\t" \
    "s_add_u32 s69, s69, 208\n\t" \
    "s_sub_u32 s69, s69, s81\n\t" \
    "s_max_i32 s69, s69, 0\n\t" \
    "s_add_u32 s69, s69, s81\n\t" \
    "v_and_b32_e64 v184, -2, s69\n\t" \
    "s_add_u32 s69, s69, 160\n\t" \
    "v_and_b32_e64 v185, -2, s69\n\t" \
    "s_add_u32 s69, s65, 0\n\t" \
    "s_bitcmp1_b32 s69, 0\n\t" \
    "s_cselect_b32 s70, s63, s62\n\t" \
    "s_bfe_u32 s69, s69, 0x20001\n\t" \
    "s_lshl_b32 s69, s69, 19\n\t" \
    "s_add_u32 s69, s69, s70\n\t" \
    "s_add_u32 s52, s36, s69\n\t" \
    "s_addc_u32 s53, s37, 0\n\t" \
    "global_store_dword v3, v185, s[52:53] sc1\n\t" \
    "s_branch rr_h126_%=\n\t" \
    "rr_pf13_%=:\n\t" \
    "s_cmp_gt_u32 s65, 62\n\t" \
    "s_cbranch_scc1 rr_end8_%=\n\t" \
    "v_min_i32_dpp v177, v177, v177 row_ror:1 row_mask:0xf bank_mask:0xf\n\t" \
    "s_nop 1\n\t" \
    "v_min_i32_dpp v177, v177, v177 row_ror:2 row_mask:0xf bank_mask:0xf\n\t" \
    "s_nop 1\n\t" \
    "v_min_i32_dpp v177, v177, v177 row_ror:4 row_mask:0xf bank_mask:0xf\n\t" \
    "s_nop 1\n\t" \
    "v_min_i32_dpp v177, v177, v177 row_ror:8 row_mask:0xf bank_mask:0xf\n\t" \
    "s_nop 0\n\t" \
    "v_readlane_b32 s69, v177, 0\n\t" \
    "v_readlane_b32 s70, v177, 16\n\t" \
    "s_nop 1\n\t" \
    "s_min_i32 s69, s69, s70\n\t" \
    "s_max_i32 s83, s83, s69\n\t" \
    "rr_WF60_%=:\n\t" \
    "s_cmp_ge_i32 s83, s65\n\t" \
    "s_cbranch_scc1 rr_WFok61_%=\n\t" \
    "global_load_dword v184, v6, s[36:37] sc1\n\t" \
    "s_waitcnt vmcnt(0)\n\t" \
    "v_min_i32_dpp v184, v184, v184 row_ror:1 row_mask:0xf bank_mask:0xf\n\t" \
    "s_nop 1\n\t" \
    "v_min_i32_dpp v184, v184, v184 row_ror:2 row_mask:0xf bank_mask:0xf\n\t" \
    "s_nop 1\n\t" \
    "v_min_i32_dpp v184, v184, v184 row_ror:4 row_mask:0xf bank_mask:0xf\n\t" \
    "s_nop 1\n\t" \
    "v_min_i32_dpp v184, v184, v184 row_ror:8 row_mask:0xf bank_mask:0xf\n\t" \
    "s_nop 0\n\t" \
    "v_readlane_b32 s69, v184, 0\n\t" \
    "v_readlane_b32 s70, v184, 16\n\t" \
    "s_nop 1\n\t" \
    "s_min_i32 s83, s69, s70\n\t" \
    "s_cmp_ge_i32 s83, s65\n\t" \
    "s_cbranch_scc1 rr_WFok61_%=\n\t" \
    "s_cmp_eq_u32 s68, 0\n\t" \
    "s_cbranch_scc1 rr_F62_%=\n\t" \
    "s_sub_u32 s68, s68, 1\n\t" \
    "s_sleep 2\n\t" \
    "s_branch rr_WF60_%=\n\t" \
    "rr_F62_%=:\n\t" \
    "v_mov_b32 v184, 6095104\n\t" \
    "v_mov_b32 v185, 10\n\t" \
    "global_store_dword v184, v185, s[36:37] sc1\n\t" \
    "s_waitcnt vmcnt(0)\n\t" \
    "s_branch rr_WFok61_%=\n\t" \
    "rr_WFok61_%=:\n\t" \
    "s_mul_i32 s70, s67, 7\n\t" \
    "s_add_u32 s69, s65, 0\n\t" \
    "s_xor_b32 s69, s69, s70\n\t" \
    "s_and_b32 s69, s69, 63\n\t" \
    "s_lshl_b32 s69, s69, 15\n\t" \
    "s_mul_i32 s70, s67, 2097152\n\t" \
    "s_add_u32 s69, s69, s70\n\t" \
    "s_add_u32 s69, s69, 48234496\n\t" \
    "s_add_u32 s38, s36, s69\n\t" \
    "s_addc_u32 s39, s37, 0\n\t" \
    "s_mul_hi_u32 s69, s85, s78\n\t" \
    "s_add_u32 s79, s81, s69\n\t" \
    "s_add_u32 s79, s79, s87\n\t" \
    "s_cmp_ge_u32 s65, 38\n\t" \
    "s_cselect_b32 s69, 28, 0\n\t" \
    "s_sub_u32 s79, s79, s69\n\t" \
    "s_cmp_lt_u32 s65, 10\n\t" \
    "s_cselect_b32 s69, 64, 0\n\t" \
    "s_add_u32 s79, s79, s69\n\t" \
    "s_cmp_ge_u32 s65, 59\n\t" \
    "s_cselect_b32 s69, 32, 0\n\t" \
    "s_add_u32 s79, s79, s69\n\t" \
    "s_add_u32 s80, s79, s88\n\t" \
    "s_add_u32 s79, s79, -96\n\t" \
    "s_add_u32 s79, s79, s42\n\t" \
    "s_sub_u32 s70, s79, s74\n\t" \
    "s_max_i32 s70, s70, 0\n\t" \
    "s_min_i32 s70, s70, 16000\n\t" \
    "s_lshl_b32 s70, s70, 1\n\t" \
    "s_mul_hi_u32 s70, s70, s77\n\t" \
    "s_add_u32 s70, s70, s73\n\t" \
    "rr_W63_%=:\n\t" \
    "s_memtime s[56:57]\n\t" \
    "s_waitcnt lgkmcnt(0)\n\t" \
    "s_sub_u32 s69, s70, s56\n\t" \
    "s_cmp_gt_i32 s69, 24\n\t" \
    "s_cbranch_scc1 rr_W63_%=\n\t" \
    "s_add_u32 s69, s65, 4\n\t" \
    "s_min_i32 s69, s69, 62\n\t" \
    "s_cmp_ge_i32 s83, s69\n\t" \
    "s_cbranch_scc1 rr_nfl64_%=\n\t" \
    "global_load_dword v177, v6, s[36:37] sc1\n\t" \
    "rr_nfl64_%=:\n\t" \
    "global_load_dwordx4 a[0:3], v26, s[38:39] offset:0 sc1\n\t" \
    "global_load_dwordx4 a[4:7], v26, s[38:39] offset:1024 sc1\n\t" \
    "global_load_dwordx4 a[8:11], v27, s[38:39] offset:0 sc1\n\t" \
    "global_load_dwordx4 a[12:15], v27, s[38:39] offset:1024 sc1\n\t" \
    "global_load_dwordx4 a[16:19], v28, s[38:39] offset:0 sc1\n\t" \
    "global_load_dwordx4 a[20:23], v28, s[38:39] offset:1024 sc1\n\t" \
    "global_load_dwordx4 a[24:27], v29, s[38:39] offset:0 sc1\n\t" \
    "global_load_dwordx4 a[28:31], v29, s[38:39] offset:1024 sc1\n\t" \
    "global_load_dwordx4 a[32:35], v30, s[38:39] offset:0 sc1\n\t" \
    "global_load_dwordx4 a[36:39], v30, s[38:39] offset:1024 sc1\n\t" \
    "global_load_dwordx4 a[40:43], v31, s[38:39] offset:0 sc1\n\t" \
    "global_load_dwordx4 a[44:47], v31, s[38:39] offset:1024 sc1\n\t" \
    "global_load_dwordx4 a[48:51], v32, s[38:39] offset:0 sc1\n\t" \
    "global_load_dwordx4 a[52:55], v32, s[38:39] offset:1024 sc1\n\t" \
    "global_load_dwordx4 a[56:59], v33, s[38:39] offset:0 sc1\n\t" \
    "global_load_dwordx4 a[60:63], v33, s[38:39] offset:1024 sc1\n\t" \
    "global_load_dwordx4 a[64:67], v34, s[38:39] offset:0 sc1\n\t" \
    "global_load_dwordx4 a[68:71], v34, s[38:39] offset:1024 sc1\n\t" \
    "global_load_dwordx4 a[72:75], v35, s[38:39] offset:0 sc1\n\t" \
    "global_load_dwordx4 a[76:79], v35, s[38:39] offset:1024 sc1\n\t" \
    "global_load_dwordx4 a[80:83], v36, s[38:39] offset:0 sc1\n\t" \
    "global_load_dwordx4 a[84:87], v36, s[38:39] offset:1024 sc1\n\t" \
    "global_load_dwordx4 a[88:91], v37, s[38:39] offset:0 sc1\n\t" \
    "global_load_dwordx4 a[92:95], v37, s[38:39] offset:1024 sc1\n\t" \
    "global_load_dwordx4 a[96:99], v38, s[38:39] offset:0 sc1\n\t" \
    "global_load_dwordx4 a[100:103], v38, s[38:39] offset:1024 sc1\n\t" \
    "global_load_dwordx4 a[104:107], v39, s[38:39] offset:0 sc1\n\t" \
    "global_load_dwordx4 a[108:111], v39, s[38:39] offset:1024 sc1\n\t" \
    "global_load_dwordx4 a[112:115], v40, s[38:39] offset:0 sc1\n\t" \
    "global_load_dwordx4 a[116:119], v40, s[38:39] offset:1024 sc1\n\t" \
    "global_load_dwordx4 a[120:123], v41, s[38:39] offset:0 sc1\n\t" \
    "global_load_dwordx4 a[124:127], v41, s[38:39] offset:1024 sc1\n\t" \
    "s_cmp_eq_u32 s51, 0\n\t" \
    "s_cbranch_scc1 rr_nb65_%=\n\t" \
    "s_add_u32 s69, s65, 1\n\t" \
    "s_lshl_b32 s69, s69, 15\n\t" \
    "v_xor_b32 v201, s69, v199\n\t" \
    "v_xor_b32 v202, s69, v200\n\t" \
    "global_load_dword v201, v201, s[36:37] sc1\n\t" \
    "global_load_dword v202, v202, s[36:37] sc1\n\t" \
    "rr_nb65_%=:\n\t" \
    "s_mul_hi_u32 s69, s85, s78\n\t" \
    "s_add_u32 s79, s81, s69\n\t" \
    "s_add_u32 s79, s79, s87\n\t" \
    "s_cmp_ge_u32 s65, 38\n\t" \
    "s_cselect_b32 s69, 28, 0\n\t" \
    "s_sub_u32 s79, s79, s69\n\t" \
    "s_cmp_lt_u32 s65, 10\n\t" \
    "s_cselect_b32 s69, 64, 0\n\t" \
    "s_add_u32 s79, s79, s69\n\t" \
    "s_cmp_ge_u32 s65, 59\n\t" \
    "s_cselect_b32 s69, 32, 0\n\t" \
    "s_add_u32 s79, s79, s69\n\t" \
    "s_add_u32 s80, s79, s88\n\t" \
    "s_add_u32 s79, s79, 320\n\t" \
    "s_add_u32 s79, s79, s43\n\t" \
    "s_sub_u32 s70, s79, s74\n\t" \
    "s_max_i32 s70, s70, 0\n\t" \
    "s_min_i32 s70, s70, 16000\n\t" \
    "s_lshl_b32 s70, s70, 1\n\t" \
    "s_mul_hi_u32 s70, s70, s77\n\t" \
    "s_add_u32 s70, s70, s73\n\t" \
    "rr_W66_%=:\n\t" \
    "s_memtime s[56:57]\n\t" \
    "s_waitcnt lgkmcnt(0)\n\t" \
    "s_sub_u32 s69, s70, s56\n\t" \
    "s_cmp_gt_i32 s69, 24\n\t" \
    "s_cbranch_scc1 rr_W66_%=\n\t" \
    "s_cmp_eq_u32 s51, 0\n\t" \
    "s_cbranch_scc1 rr_arm67_%=\n\t" \
    "global_store_dwordx4 v0, v[172:175], s[40:41] offset:0 sc1\n\t" \
    "global_store_dwordx4 v0, v[172:175], s[40:41] offset:1024 sc1\n\t" \
    "global_store_dwordx4 v0, v[172:175], s[40:41] offset:2048 sc1\n\t" \
    "global_store_dwordx4 v0, v[172:175], s[40:41] offset:3072 sc1\n\t" \
    "global_store_dwordx4 v1, v[172:175], s[40:41] offset:0 sc1\n\t" \
    "global_store_dwordx4 v1, v[172:175], s[40:41] offset:1024 sc1\n\t" \
    "global_store_dwordx4 v1, v[172:175], s[40:41] offset:2048 sc1\n\t" \
    "global_store_dwordx4 v1, v[172:175], s[40:41] offset:3072 sc1\n\t" \
    "s_cmp_gt_u32 s65, 61\n\t" \
    "s_cbranch_scc1 rr_arm67_%=\n\t" \
    "s_add_u32 s69, s65, 2\n\t" \
    "s_bitcmp1_b32 s69, 0\n\t" \
    "s_cselect_b32 s70, s63, s62\n\t" \
    "s_bfe_u32 s69, s69, 0x20001\n\t" \
    "s_lshl_b32 s69, s69, 19\n\t" \
    "s_add_u32 s69, s69, s70\n\t" \
    "s_add_u32 s46, s36, s69\n\t" \
    "s_addc_u32 s47, s37, 0\n\t" \
    "global_store_dwordx4 v164, v[172:175], s[46:47] sc1\n\t" \
    "global_store_dwordx4 v165, v[172:175], s[46:47] sc1\n\t" \
    "global_store_dwordx4 v166, v[172:175], s[46:47] sc1\n\t" \
    "global_store_dwordx4 v167, v[172:175], s[46:47] sc1\n\t" \
    "rr_arm67_%=:\n\t" \
    "s_waitcnt vmcnt(0)\n\t" \
    "v_min_i32_dpp v177, v177, v177 row_ror:1 row_mask:0xf bank_mask:0xf\n\t" \
    "s_nop 1\n\t" \
    "v_min_i32_dpp v177, v177, v177 row_ror:2 row_mask:0xf bank_mask:0xf\n\t" \
    "s_nop 1\n\t" \
    "v_min_i32_dpp v177, v177, v177 row_ror:4 row_mask:0xf bank_mask:0xf\n\t" \
    "s_nop 1\n\t" \
    "v_min_i32_dpp v177, v177, v177 row_ror:8 row_mask:0xf bank_mask:0xf\n\t" \
    "s_nop 0\n\t" \
    "v_readlane_b32 s69, v177, 0\n\t" \
    "v_readlane_b32 s70, v177, 16\n\t" \
    "s_nop 1\n\t" \
    "s_min_i32 s69, s69, s70\n\t" \
    "s_max_i32 s83, s83, s69\n\t" \
    "s_cmp_lg_u32 s65, 40\n\t" \
    "s_cbranch_scc1 rr_f268_%=\n\t" \
    "s_cmp_lg_u32 s67, 35\n\t" \
    "s_cbranch_scc1 rr_f268_%=\n\t" \
    "rr_f2l69_%=:\n\t" \
    "global_load_dword v186, v178, s[36:37] sc1\n\t" \
    "s_waitcnt vmcnt(0)\n\t" \
    "v_cmp_ne_u32 vcc, 1, v186\n\t" \
    "s_cbranch_vccz rr_f268_%=\n\t" \
    "s_cmp_eq_u32 s68, 0\n\t" \
    "s_cbranch_scc1 rr_F70_%=\n\t" \
    "s_sub_u32 s68, s68, 1\n\t" \
    "s_sleep 2\n\t" \
    "s_branch rr_f2l69_%=\n\t" \
    "rr_F70_%=:\n\t" \
    "v_mov_b32 v187, 6095104\n\t" \
    "v_mov_b32 v188, 11\n\t" \
    "global_store_dword v187, v188, s[36:37] sc1\n\t" \
    "s_waitcnt vmcnt(0)\n\t" \
    "s_branch rr_f268_%=\n\t" \
    "rr_f268_%=:\n\t" \
    "s_cmp_eq_u32 s51, 0\n\t" \
    "s_cbranch_scc1 rr_sl24_%=\n\t" \
    "s_add_u32 s69, s65, -1\n\t" \
    "s_bitcmp1_b32 s69, 0\n\t" \
    "s_cselect_b32 s70, s63, s62\n\t" \
    "s_bfe_u32 s69, s69, 0x20001\n\t" \
    "s_lshl_b32 s69, s69, 19\n\t" \
    "s_add_u32 s69, s69, s70\n\t" \
    "s_add_u32 s46, s36, s69\n\t" \
    "s_addc_u32 s47, s37, 0\n\t" \
    "s_add_u32 s69, s65, -1\n\t" \
    "s_lshr_b32 s69, s69, 1\n\t" \
    "s_xor_b32 s69, s69, s33\n\t" \
    "s_and_b32 s69, s69, 1\n\t" \
    "s_lshl_b32 s69, s69, 14\n\t" \
    "s_or_b32 s46, s46, s69\n\t" \
    "s_add_u32 s79, s80, 480\n\t" \
    "s_sub_u32 s70, s79, s74\n\t" \
    "s_max_i32 s70, s70, 0\n\t" \
    "s_min_i32 s70, s70, 16000\n\t" \
    "s_lshl_b32 s70, s70, 1\n\t" \
    "s_mul_hi_u32 s70, s70, s77\n\t" \
    "s_add_u32 s70, s70, s73\n\t" \
    "rr_W71_%=:\n\t" \
    "s_memtime s[56:57]\n\t" \
    "s_waitcnt lgkmcnt(0)\n\t" \
    "s_sub_u32 s69, s70, s56\n\t" \
    "s_cmp_gt_i32 s69, 24\n\t" \
    "s_cbranch_scc1 rr_W71_%=\n\t" \
    "v_add_u32 v187, 4096, v7\n\t" \
    "global_load_dword v184, v187, s[46:47] sc1\n\t" \
    "s_waitcnt vmcnt(0)\n\t" \
    "v_cmp_eq_u32 vcc, -1, v184\n\t" \
    "s_cbranch_vccz rr_Hok73_%=\n\t" \
    "rr_H72_%=:\n\t" \
    "global_load_dword v184, v187, s[46:47] sc1\n\t" \
    "s_waitcnt vmcnt(0)\n\t" \
    "v_cmp_eq_u32 vcc, -1, v184\n\t" \
    "s_cbranch_vccz rr_Hc74_%=\n\t" \
    "s_cmp_eq_u32 s68, 0\n\t" \
    "s_cbranch_scc1 rr_F75_%=\n\t" \
    "s_sub_u32 s68, s68, 1\n\t" \
    "s_sleep 4\n\t" \
    "s_branch rr_H72_%=\n\t" \
    "rr_F75_%=:\n\t" \
    "v_mov_b32 v185, 6095104\n\t" \
    "v_mov_b32 v186, 19\n\t" \
    "global_store_dword v185, v186, s[36:37] sc1\n\t" \
    "s_waitcnt vmcnt(0)\n\t" \
    "s_branch rr_Hc74_%=\n\t" \
    "rr_Hc74_%=:\n\t" \
    "rr_Hok73_%=:\n\t" \
    "v_subrev_u32 v184, s80, v184\n\t" \
    "s_nop 1\n\t" \
    "v_max_i32_dpp v184, v184, v184 row_ror:1 row_mask:0xf bank_mask:0xf\n\t" \
    "s_nop 1\n\t" \
    "v_max_i32_dpp v184, v184, v184 row_ror:2 row_mask:0xf bank_mask:0xf\n\t" \
    "s_nop 1\n\t" \
    "v_max_i32_dpp v184, v184, v184 row_ror:4 row_mask:0xf bank_mask:0xf\n\t" \
    "s_nop 1\n\t" \
    "v_max_i32_dpp v184, v184, v184 row_ror:8 row_mask:0xf bank_mask:0xf\n\t" \
    "s_nop 0\n\t" \
    "v_readlane_b32 s69, v184, 0\n\t" \
    "v_readlane_b32 s71, v184, 16\n\t" \
    "s_nop 1\n\t" \
    "s_max_i32 s69, s69, s71\n\t" \
    "s_add_u32 s81, s69, s80\n\t" \
    "s_mul_hi_u32 s69, s85, s78\n\t" \
    "s_add_u32 s79, s81, s69\n\t" \
    "s_add_u32 s79, s79, s87\n\t" \
    "s_cmp_ge_u32 s65, 37\n\t" \
    "s_cselect_b32 s69, 28, 0\n\t" \
    "s_sub_u32 s79, s79, s69\n\t" \
    "s_cmp_lt_u32 s65, 9\n\t" \
    "s_cselect_b32 s69, 64, 0\n\t" \
    "s_add_u32 s79, s79, s69\n\t" \
    "s_cmp_ge_u32 s65, 58\n\t" \
    "s_cselect_b32 s69, 32, 0\n\t" \
    "s_add_u32 s79, s79, s69\n\t" \
    "s_add_u32 s80, s79, s88\n\t" \
    "s_add_u32 s79, s79, 96\n\t" \
    "s_add_u32 s69, s65, 1\n\t" \
    "s_mul_i32 s70, s69, 171\n\t" \
    "s_lshr_b32 s70, s70, 9\n\t" \
    "s_mul_i32 s70, s70, 3\n\t" \
    "s_sub_u32 s69, s69, s70\n\t" \
    "s_mul_i32 s69, s69, 36\n\t" \
    "s_lshl_b32 s69, s69, 14\n\t" \
    "s_add_u32 s69, s69, s64\n\t" \
    "s_add_u32 s69, s69, 4194304\n\t" \
    "s_add_u32 s40, s36, s69\n\t" \
    "s_addc_u32 s41, s37, 0\n\t" \
    "s_sub_u32 s70, s79, s74\n\t" \
    "s_max_i32 s70, s70, 0\n\t" \
    "s_min_i32 s70, s70, 16000\n\t" \
    "s_lshl_b32 s70, s70, 1\n\t" \
    "s_mul_hi_u32 s70, s70, s77\n\t" \
    "s_add_u32 s70, s70, s73\n\t" \
    "rr_W76_%=:\n\t" \
    "s_memtime s[56:57]\n\t" \
    "s_waitcnt lgkmcnt(0)\n\t" \
    "s_sub_u32 s69, s70, s56\n\t" \
    "s_cmp_gt_i32 s69, 24\n\t" \
    "s_cbranch_scc1 rr_W76_%=\n\t" \
    "global_store_dwordx4 v196, v[172:175], s[40:41] sc1\n\t" \
    "global_store_dwordx4 v197, v[172:175], s[40:41] sc1\n\t" \
    "global_store_dwordx4 v198, v[172:175], s[40:41] sc1\n\t" \
    "s_branch rr_u227_%=\n\t" \
    "rr_sl24_%=:\n\t" \
    "ds_read_b64 v[184:185], v9\n\t" \
    "s_sub_u32 s70, s65, 1\n\t" \
    "s_waitcnt lgkmcnt(0)\n\t" \
    "v_readfirstlane_b32 s69, v185\n\t" \
    "v_readfirstlane_b32 s81, v184\n\t" \
    "s_nop 0\n\t" \
    "s_cmp_eq_u32 s69, s70\n\t" \
    "s_cbranch_scc1 rr_u227_%=\n\t" \
    "s_cmp_eq_u32 s68, 0\n\t" \
    "s_cbranch_scc1 rr_F77_%=\n\t" \
    "s_sub_u32 s68, s68, 1\n\t" \
    "s_sleep 2\n\t" \
    "s_branch rr_sl24_%=\n\t" \
    "rr_F77_%=:\n\t" \
    "v_mov_b32 v184, 6095104\n\t" \
    "v_mov_b32 v185, 14\n\t" \
    "global_store_dword v184, v185, s[36:37] sc1\n\t" \
    "s_waitcnt vmcnt(0)\n\t" \
    "s_branch rr_u227_%=\n\t" \
    "rr_u227_%=:\n\t" \
    "s_add_u32 s69, s65, -1\n\t" \
    "s_bitcmp1_b32 s69, 0\n\t" \
    "s_cselect_b32 s70, s63, s62\n\t" \
    "s_bfe_u32 s69, s69, 0x20001\n\t" \
    "s_lshl_b32 s69, s69, 19\n\t" \
    "s_add_u32 s69, s69, s70\n\t" \
    "s_add_u32 s46, s36, s69\n\t" \
    "s_addc_u32 s47, s37, 0\n\t" \
    "s_add_u32 s69, s65, -1\n\t" \
    "s_lshr_b32 s69, s69, 1\n\t" \
    "s_xor_b32 s69, s69, s33\n\t" \
    "s_and_b32 s69, s69, 1\n\t" \
    "s_lshl_b32 s69, s69, 14\n\t" \
    "s_or_b32 s46, s46, s69\n\t" \
    "s_cmp_lg_u32 s65, 1\n\t" \
    "s_cbranch_scc1 rr_Oc80_%=\n\t" \
    "s_add_u32 s79, s81, 480\n\t" \
    "s_sub_u32 s70, s79, s74\n\t" \
    "s_max_i32 s70, s70, 0\n\t" \
    "s_min_i32 s70, s70, 16000\n\t" \
    "s_lshl_b32 s70, s70, 1\n\t" \
    "s_mul_hi_u32 s70, s70, s77\n\t" \
    "s_add_u32 s70, s70, s73\n\t" \
    "rr_W81_%=:\n\t" \
    "s_memtime s[56:57]\n\t" \
    "s_waitcnt lgkmcnt(0)\n\t" \
    "s_sub_u32 s69, s70, s56\n\t" \
    "s_cmp_gt_i32 s69, 24\n\t" \
    "s_cbranch_scc1 rr_W81_%=\n\t" \
    "global_load_dword v184, v46, s[46:47] sc1\n\t" \
    "s_waitcnt vmcnt(0)\n\t" \
    "v_cmp_eq_u32 vcc, -1, v184\n\t" \
    "s_cbranch_vccz rr_Ook79_%=\n\t" \
    "rr_O78_%=:\n\t" \
    "global_load_dword v184, v46, s[46:47] offset:1536 sc1\n\t" \
    "s_waitcnt vmcnt(0)\n\t" \
    "v_cmp_eq_u32 vcc, -1, v184\n\t" \
    "s_cbranch_vccz rr_Ook79_%=\n\t" \
    "s_cmp_eq_u32 s68, 0\n\t" \
    "s_cbranch_scc1 rr_F82_%=\n\t" \
    "s_sub_u32 s68, s68, 1\n\t" \
    "s_sleep 3\n\t" \
    "s_branch rr_O78_%=\n\t" \
    "rr_F82_%=:\n\t" \
    "v_mov_b32 v185, 6095104\n\t" \
    "v_mov_b32 v186, 18\n\t" \
    "global_store_dword v185, v186, s[36:37] sc1\n\t" \
    "s_waitcnt vmcnt(0)\n\t" \
    "s_branch rr_Ook79_%=\n\t" \
    "rr_Ook79_%=:\n\t" \
    "v_subrev_u32 v184, s80, v184\n\t" \
    "s_nop 1\n\t" \
    "v_max_i32_dpp v184, v184, v184 row_ror:1 row_mask:0xf bank_mask:0xf\n\t" \
    "s_nop 1\n\t" \
    "v_max_i32_dpp v184, v184, v184 row_ror:2 row_mask:0xf bank_mask:0xf\n\t" \
    "s_nop 1\n\t" \
    "v_max_i32_dpp v184, v184, v184 row_ror:4 row_mask:0xf bank_mask:0xf\n\t" \
    "s_nop 1\n\t" \
    "v_max_i32_dpp v184, v184, v184 row_ror:8 row_mask:0xf bank_mask:0xf\n\t" \
    "s_nop 0\n\t" \
    "v_readlane_b32 s69, v184, 0\n\t" \
    "v_readlane_b32 s71, v184, 16\n\t" \
    "v_readlane_b32 s70, v184, 32\n\t" \
    "v_readlane_b32 s81, v184, 48\n\t" \
    "s_max_i32 s69, s69, s71\n\t" \
    "s_nop 0\n\t" \
    "s_max_i32 s71, s70, s81\n\t" \
    "s_max_i32 s69, s69, s71\n\t" \
    "s_add_u32 s81, s69, s80\n\t" \
    "rr_Oc80_%=:\n\t" \
    "s_mul_hi_u32 s69, s85, s78\n\t" \
    "s_add_u32 s79, s81, s69\n\t" \
    "s_add_u32 s79, s79, s87\n\t" \
    "s_cmp_ge_u32 s65, 37\n\t" \
    "s_cselect_b32 s69, 28, 0\n\t" \
    "s_sub_u32 s79, s79, s69\n\t" \
    "s_cmp_lt_u32 s65, 9\n\t" \
    "s_cselect_b32 s69, 64, 0\n\t" \
    "s_add_u32 s79, s79, s69\n\t" \
    "s_cmp_ge_u32 s65, 58\n\t" \
    "s_cselect_b32 s69, 32, 0\n\t" \
    "s_add_u32 s79, s79, s69\n\t" \
    "s_add_u32 s80, s79, s88\n\t" \
    "s_sub_u32 s70, s79, s74\n\t" \
    "s_max_i32 s70, s70, 0\n\t" \
    "s_min_i32 s70, s70, 16000\n\t" \
    "s_lshl_b32 s70, s70, 1\n\t" \
    "s_mul_hi_u32 s70, s70, s77\n\t" \
    "s_add_u32 s70, s70, s73\n\t" \
    "rr_W83_%=:\n\t" \
    "s_memtime s[56:57]\n\t" \
    "s_waitcnt lgkmcnt(0)\n\t" \
    "s_sub_u32 s69, s70, s56\n\t" \
    "s_cmp_gt_i32 s69, 24\n\t" \
    "s_cbranch_scc1 rr_W83_%=\n\t" \
    "rr_act7_%=:\n\t" \
    "s_add_u32 s69, s65, 0\n\t" \
    "s_bitcmp1_b32 s69, 0\n\t" \
    "s_cselect_b32 s70, s63, s62\n\t" \
    "s_bfe_u32 s69, s69, 0x20001\n\t" \
    "s_lshl_b32 s69, s69, 19\n\t" \
    "s_add_u32 s69, s69, s70\n\t" \
    "s_add_u32 s46, s36, s69\n\t" \
    "s_addc_u32 s47, s37, 0\n\t" \
    "s_add_u32 s69, s65, 0\n\t" \
    "s_lshr_b32 s69, s69, 1\n\t" \
    "s_xor_b32 s69, s69, s33\n\t" \
    "s_and_b32 s69, s69, 1\n\t" \
    "s_lshl_b32 s69, s69, 14\n\t" \
    "s_or_b32 s46, s46, s69\n\t" \
    "s_add_u32 s69, s65, 1\n\t" \
    "s_bitcmp1_b32 s69, 0\n\t" \
    "s_cselect_b32 s70, s63, s62\n\t" \
    "s_bfe_u32 s69, s69, 0x20001\n\t" \
    "s_lshl_b32 s69, s69, 19\n\t" \
    "s_add_u32 s69, s69, s70\n\t" \
    "s_add_u32 s48, s36, s69\n\t" \
    "s_addc_u32 s49, s37, 0\n\t" \
    "s_add_u32 s69, s65, 0\n\t" \
    "s_mul_i32 s70, s69, 171\n\t" \
    "s_lshr_b32 s70, s70, 9\n\t" \
    "s_mul_i32 s70, s70, 3\n\t" \
    "s_sub_u32 s69, s69, s70\n\t" \
    "s_mul_i32 s69, s69, 36\n\t" \
    "s_add_u32 s69, s69, s67\n\t" \
    "s_lshl_b32 s69, s69, 14\n\t" \
    "s_add_u32 s69, s69, s64\n\t" \
    "s_add_u32 s69, s69, 4194304\n\t" \
    "s_add_u32 s40, s36, s69\n\t" \
    "s_addc_u32 s41, s37, 0\n\t" \
    "s_add_u32 s69, s65, 2\n\t" \
    "s_cmp_ge_i32 s83, s69\n\t" \
    "s_cbranch_scc1 rr_nf84_%=\n\t" \
    "global_load_dword v177, v6, s[36:37] sc1\n\t" \
    "rr_nf84_%=:\n\t" \
    "s_mov_b32 s71, 3\n\t" \
    "rr_E85_%=:\n\t" \
    "s_memtime s[54:55]\n\t" \
    "s_nop 0\n\t" \
    "s_memrealtime s[4:5]\n\t" \
    "s_nop 0\n\t" \
    "s_memrealtime s[6:7]\n\t" \
    "s_nop 0\n\t" \
    "s_memrealtime s[8:9]\n\t" \
    "s_nop 0\n\t" \
    "s_memrealtime s[10:11]\n\t" \
    "s_nop 0\n\t" \
    "s_memrealtime s[12:13]\n\t" \
    "s_nop 0\n\t" \
    "s_memrealtime s[14:15]\n\t" \
    "s_nop 0\n\t" \
    "s_memrealtime s[16:17]\n\t" \
    "s_nop 0\n\t" \
    "s_memrealtime s[18:19]\n\t" \
    "s_nop 0\n\t" \
    "s_memrealtime s[20:21]\n\t" \
    "s_nop 0\n\t" \
    "s_memrealtime s[22:23]\n\t" \
    "s_nop 0\n\t" \
    "s_memrealtime s[24:25]\n\t" \
    "s_nop 0\n\t" \
    "s_memrealtime s[26:27]\n\t" \
    "s_nop 0\n\t" \
    "s_memrealtime s[28:29]\n\t" \
    "s_nop 0\n\t" \
    "s_memtime s[56:57]\n\t" \
    "s_mov_b32 s70, 1\n\t" \
    "s_sub_u32 s71, s71, 1\n\t" \
    "s_cselect_b32 s69, 0, -1\n\t" \
    "s_waitcnt lgkmcnt(0)\n\t" \
    "s_sub_u32 s54, s56, s54\n\t" \
    "s_and_b32 s54, s54, s69\n\t" \
    "s_cmp_gt_u32 s54, 124\n\t" \
    "s_cbranch_scc1 rr_E85_%=\n\t" \
    "s_cmp_eq_u32 s4, s28\n\t" \
    "s_addc_u32 s70, s70, 0\n\t" \
    "s_cmp_eq_u32 s6, s28\n\t" \
    "s_addc_u32 s70, s70, 0\n\t" \
    "s_cmp_eq_u32 s8, s28\n\t" \
    "s_addc_u32 s70, s70, 0\n\t" \
    "s_cmp_eq_u32 s10, s28\n\t" \
    "s_addc_u32 s70, s70, 0\n\t" \
    "s_cmp_eq_u32 s12, s28\n\t" \
    "s_addc_u32 s70, s70, 0\n\t" \
    "s_cmp_eq_u32 s14, s28\n\t" \
    "s_addc_u32 s70, s70, 0\n\t" \
    "s_cmp_eq_u32 s16, s28\n\t" \
    "s_addc_u32 s70, s70, 0\n\t" \
    "s_cmp_eq_u32 s18, s28\n\t" \
    "s_addc_u32 s70, s70, 0\n\t" \
    "s_cmp_eq_u32 s20, s28\n\t" \
    "s_addc_u32 s70, s70, 0\n\t" \
    "s_cmp_eq_u32 s22, s28\n\t" \
    "s_addc_u32 s70, s70, 0\n\t" \
    "s_cmp_eq_u32 s24, s28\n\t" \
    "s_addc_u32 s70, s70, 0\n\t" \
    "s_cmp_eq_u32 s26, s28\n\t" \
    "s_addc_u32 s70, s70, 0\n\t" \
    "s_mov_b32 s75, s73\n\t" \
    "s_mov_b32 s76, s74\n\t" \
    "s_lshl_b32 s70, s70, 3\n\t" \
    "s_sub_u32 s73, s56, s70\n\t" \
    "s_lshl_b32 s74, s28, 4\n\t" \
    "s_sub_u32 s69, s73, s75\n\t" \
    "s_sub_u32 s70, s74, s76\n\t" \
    "s_sub_u32 s71, s70, 1600\n\t" \
    "s_cmp_lt_u32 s71, 400000\n\t" \
    "s_cbranch_scc0 rr_K87_%=\n\t" \
    "v_cvt_f32_u32 v184, s69\n\t" \
    "v_cvt_f32_u32 v185, s70\n\t" \
    "v_rcp_f32 v186, v184\n\t" \
    "v_rcp_f32 v187, v185\n\t" \
    "v_mul_f32 v186, v186, v185\n\t" \
    "v_mul_f32 v187, v187, v184\n\t" \
    "v_mul_f32 v186, 0x4f800000, v186\n\t" \
    "v_mul_f32 v187, 0x4f000000, v187\n\t" \
    "v_cvt_u32_f32 v186, v186\n\t" \
    "v_cvt_u32_f32 v187, v187\n\t" \
    "v_readfirstlane_b32 s69, v186\n\t" \
    "v_readfirstlane_b32 s70, v187\n\t" \
    "s_nop 1\n\t" \
    "s_sub_u32 s69, s69, s78\n\t" \
    "s_ashr_i32 s69, s69, 2\n\t" \
    "s_add_u32 s78, s78, s69\n\t" \
    "s_sub_u32 s70, s70, s77\n\t" \
    "s_ashr_i32 s70, s70, 2\n\t" \
    "s_add_u32 s77, s77, s70\n\t" \
    "rr_K87_%=:\n\t" \
    "s_mov_b32 s91, 0\n\t" \
    "v_mov_b32 v160, 0\n\t" \
    "v_mov_b32 v161, 0\n\t" \
    "v_mov_b32 v162, 0\n\t" \
    "v_mov_b32 v163, 0\n\t" \
    "s_sub_u32 s70, s80, s74\n\t" \
    "s_max_i32 s70, s70, 0\n\t" \
    "s_min_i32 s70, s70, 16000\n\t" \
    "s_lshl_b32 s70, s70, 1\n\t" \
    "s_mul_hi_u32 s70, s70, s77\n\t" \
    "s_add_u32 s70, s70, s73\n\t" \
    "s_sub_u32 s70, s70, 100\n\t" \
    "s_getpc_b64 s[30:31]\n\t" \
    "rr_Wp94_%=:\n\t" \
    "s_add_u32 s30, s30, rr_We95_%=-rr_Wp94_%=\n\t" \
    "s_addc_u32 s31, s31, 0\n\t" \
    "rr_W93_%=:\n\t" \
    "s_memtime s[56:57]\n\t" \
    "s_waitcnt lgkmcnt(0)\n\t" \
    "s_sub_u32 s69, s70, s56\n\t" \
    "s_cmp_gt_i32 s69, 111\n\t" \
    "s_cbranch_scc1 rr_W93_%=\n\t" \
    "s_max_i32 s69, s69, 0\n\t" \
    "s_lshr_b32 s69, s69, 1\n\t" \
    "s_and_b32 s69, s69, 0x3c\n\t" \
    "s_sub_u32 s30, s30, s69\n\t" \
    "s_subb_u32 s31, s31, 0\n\t" \
    "s_setpc_b64 s[30:31]\n\t" \
    ".p2align 6\n\t" \
    "rr_al96_%=:\n\t" \
    "s_nop 0\n\t" \
    "s_nop 0\n\t" \
    "s_nop 1\n\t" \
    "s_nop 1\n\t" \
    "s_nop 1\n\t" \
    "s_nop 1\n\t" \
    "s_nop 1\n\t" \
    "s_nop 1\n\t" \
    "s_nop 1\n\t" \
    "s_nop 1\n\t" \
    "s_nop 1\n\t" \
    "s_nop 1\n\t" \
    "s_nop 1\n\t" \
    "s_nop 1\n\t" \
    "s_nop 1\n\t" \
    "rr_We95_%=:\n\t" \
    "global_load_dword v184, v7, s[46:47] sc1\n\t" \
    "s_sub_u32 s97, s74, s86\n\t" \
    "s_sub_u32 s71, s73, 190\n\t" \
    "s_getpc_b64 s[30:31]\n\t" \
    "rr_wp97_%=:\n\t" \
    "s_add_u32 s30, s30, rr_again9_%=-rr_wp97_%=\n\t" \
    "s_addc_u32 s31, s31, (rr_again9_%=-rr_wp97_%=)>>32\n\t" \
    "s_waitcnt vmcnt(0) lgkmcnt(0)\n\t" \
    "rr_hok88_%=:\n\t" \
    "s_memtime s[56:57]\n\t" \
    "v_cmp_eq_u32 vcc, -1, v184\n\t" \
    "v_subrev_u32 v185, s97, v184\n\t" \
    "s_nop 1\n\t" \
    "v_max_i32_dpp v185, v185, v185 row_ror:1 row_mask:0xf bank_mask:0xf\n\t" \
    "s_cbranch_vccnz rr_hlate89_%=\n\t" \
    "s_nop 0\n\t" \
    "v_max_i32_dpp v185, v185, v185 row_ror:2 row_mask:0xf bank_mask:0xf\n\t" \
    "s_nop 1\n\t" \
    "v_max_i32_dpp v185, v185, v185 row_ror:4 row_mask:0xf bank_mask:0xf\n\t" \
    "s_nop 1\n\t" \
    "v_max_i32_dpp v185, v185, v185 row_ror:8 row_mask:0xf bank_mask:0xf\n\t" \
    "s_nop 0\n\t" \
    "v_readlane_b32 s69, v185, 0\n\t" \
    "v_readlane_b32 s70, v185, 16\n\t" \
    "s_waitcnt lgkmcnt(0)\n\t" \
    "s_sub_u32 s71, s71, s56\n\t" \
    "s_max_i32 s81, s69, s70\n\t" \
    "s_max_i32 s69, s81, 0\n\t" \
    "s_lshl_b32 s69, s69, 1\n\t" \
    "s_mul_hi_u32 s69, s69, s77\n\t" \
    "s_add_u32 s69, s69, s71\n\t" \
    "s_cmp_gt_i32 s69, 111\n\t" \
    "s_cbranch_scc1 rr_wl90_%=\n\t" \
    "rr_wf92_%=:\n\t" \
    "s_max_i32 s69, s69, 0\n\t" \
    "s_lshr_b32 s69, s69, 1\n\t" \
    "s_and_b32 s69, s69, 0x3c\n\t" \
    "s_sub_u32 s30, s30, s69\n\t" \
    "s_subb_u32 s31, s31, 0\n\t" \
    "s_setpc_b64 s[30:31]\n\t" \
    "rr_wl90_%=:\n\t" \
    "s_min_i32 s69, s69, 24000\n\t" \
    "s_add_u32 s70, s69, s56\n\t" \
    "s_add_u32 s70, s70, 62\n\t" \
    "rr_wp91_%=:\n\t" \
    "s_memtime s[56:57]\n\t" \
    "s_waitcnt lgkmcnt(0)\n\t" \
    "s_sub_u32 s69, s70, s56\n\t" \
    "s_cmp_gt_i32 s69, 111\n\t" \
    "s_cbranch_scc1 rr_wp91_%=\n\t" \
    "s_branch rr_wf92_%=\n\t" \
    ".p2align 6\n\t" \
    "rr_al98_%=:\n\t" \
    "s_nop 0\n\t" \
    "s_nop 0\n\t" \
    "s_nop 1\n\t" \
    "s_nop 1\n\t" \
    "s_nop 1\n\t" \
    "s_nop 1\n\t" \
    "s_nop 1\n\t" \
    "s_nop 1\n\t" \
    "s_nop 1\n\t" \
    "s_nop 1\n\t" \
    "s_nop 1\n\t" \
    "s_nop 1\n\t" \
    "s_nop 1\n\t" \
    "s_nop 1\n\t" \
    "s_nop 1\n\t" \
    "rr_again9_%=:\n\t" \
    "global_load_dwordx4 v[48:51], v10, s[46:47] sc1\n\t" \
    "global_load_dwordx4 v[52:55], v11, s[46:47] sc1\n\t" \
    "global_load_dwordx4 v[56:59], v12, s[46:47] sc1\n\t" \
    "global_load_dwordx4 v[60:63], v13, s[46:47] sc1\n\t" \
    "global_load_dwordx4 v[64:67], v14, s[46:47] sc1\n\t" \
    "global_load_dwordx4 v[68:71], v15, s[46:47] sc1\n\t" \
    "global_load_dwordx4 v[72:75], v16, s[46:47] sc1\n\t" \
    "global_load_dwordx4 v[76:79], v17, s[46:47] sc1\n\t" \
    "global_load_dwordx4 v[80:83], v18, s[46:47] sc1\n\t" \
    "global_load_dwordx4 v[84:87], v19, s[46:47] sc1\n\t" \
    "global_load_dwordx4 v[88:91], v20, s[46:47] sc1\n\t" \
    "global_load_dwordx4 v[92:95], v21, s[46:47] sc1\n\t" \
    "global_load_dwordx4 v[96:99], v22, s[46:47] sc1\n\t" \
    "global_load_dwordx4 v[100:103], v23, s[46:47] sc1\n\t" \
    "global_load_dwordx4 v[104:107], v24, s[46:47] sc1\n\t" \
    "global_load_dwordx4 v[108:111], v25, s[46:47] sc1\n\t" \
    "global_load_dwordx4 v[112:115], v42, s[46:47] sc1\n\t" \
    "global_load_dwordx4 v[116:119], v43, s[46:47] sc1\n\t" \
    "global_load_dwordx4 v[120:123], v44, s[46:47] sc1\n\t" \
    "global_load_dwordx4 v[124:127], v45, s[46:47] sc1\n\t" \
    "global_load_dwordx4 v[128:131], v0, s[40:41] offset:0 sc1\n\t" \
    "global_load_dwordx4 v[132:135], v0, s[40:41] offset:1024 sc1\n\t" \
    "global_load_dwordx4 v[136:139], v0, s[40:41] offset:2048 sc1\n\t" \
    "global_load_dwordx4 v[140:143], v0, s[40:41] offset:3072 sc1\n\t" \
    "global_load_dwordx4 v[144:147], v1, s[40:41] offset:0 sc1\n\t" \
    "global_load_dwordx4 v[148:151], v1, s[40:41] offset:1024 sc1\n\t" \
    "global_load_dwordx4 v[152:155], v1, s[40:41] offset:2048 sc1\n\t" \
    "global_load_dwordx4 v[156:159], v1, s[40:41] offset:3072 sc1\n\t" \
    "s_cmp_lg_u32 s91, 0\n\t" \
    "s_cbranch_scc1 rr_nob12_%=\n\t" \
    "s_add_u32 s81, s81, s97\n\t" \
    "v_mov_b32 v184, s81\n\t" \
    "v_mov_b32 v185, s65\n\t" \
    "ds_write_b64 v9, v[184:185]\n\t" \
    "rr_nob12_%=:\n\t" \
    "s_waitcnt vmcnt(27)\n\t" \
    "v_smfmac_f32_16x16x64_bf16 v[160:163], v[48:51], a[0:7], v4\n\t" \
    "s_waitcnt vmcnt(26)\n\t" \
    "v_smfmac_f32_16x16x64_bf16 v[160:163], v[52:55], a[8:15], v4\n\t" \
    "s_waitcnt vmcnt(25)\n\t" \
    "v_smfmac_f32_16x16x64_bf16 v[160:163], v[56:59], a[16:23], v4\n\t" \
    "s_waitcnt vmcnt(24)\n\t" \
    "v_smfmac_f32_16x16x64_bf16 v[160:163], v[60:63], a[24:31], v4\n\t" \
    "s_waitcnt vmcnt(23)\n\t" \
    "v_smfmac_f32_16x16x64_bf16 v[160:163], v[64:67], a[32:39], v4\n\t" \
    "s_waitcnt vmcnt(22)\n\t" \
    "v_smfmac_f32_16x16x64_bf16 v[160:163], v[68:71], a[40:47], v4\n\t" \
    "s_waitcnt vmcnt(21)\n\t" \
    "v_smfmac_f32_16x16x64_bf16 v[160:163], v[72:75], a[48:55], v4\n\t" \
    "s_waitcnt vmcnt(20)\n\t" \
    "v_smfmac_f32_16x16x64_bf16 v[160:163], v[76:79], a[56:63], v4\n\t" \
    "s_waitcnt vmcnt(19)\n\t" \
    "v_smfmac_f32_16x16x64_bf16 v[160:163], v[80:83], a[64:71], v4\n\t" \
    "s_waitcnt vmcnt(18)\n\t" \
    "v_smfmac_f32_16x16x64_bf16 v[160:163], v[84:87], a[72:79], v4\n\t" \
    "s_waitcnt vmcnt(17)\n\t" \
    "v_smfmac_f32_16x16x64_bf16 v[160:163], v[88:91], a[80:87], v4\n\t" \
    "s_waitcnt vmcnt(16)\n\t" \
    "v_smfmac_f32_16x16x64_bf16 v[160:163], v[92:95], a[88:95], v4\n\t" \
    "s_waitcnt vmcnt(15)\n\t" \
    "v_smfmac_f32_16x16x64_bf16 v[160:163], v[96:99], a[96:103], v4\n\t" \
    "s_waitcnt vmcnt(14)\n\t" \
    "v_smfmac_f32_16x16x64_bf16 v[160:163], v[100:103], a[104:111], v4\n\t" \
    "s_waitcnt vmcnt(13)\n\t" \
    "v_smfmac_f32_16x16x64_bf16 v[160:163], v[104:107], a[112:119], v4\n\t" \
    "s_waitcnt vmcnt(12)\n\t" \
    "v_smfmac_f32_16x16x64_bf16 v[160:163], v[108:111], a[120:127], v4\n\t" \
    "s_waitcnt vmcnt(6)\n\t" \
    "v_smfmac_f32_16x16x64_bf16 v[160:163], v[112:115], v[128:135], v4\n\t" \
    "s_waitcnt vmcnt(4)\n\t" \
    "v_smfmac_f32_16x16x64_bf16 v[160:163], v[116:119], v[136:143], v4\n\t" \
    "s_waitcnt vmcnt(2)\n\t" \
    "v_smfmac_f32_16x16x64_bf16 v[160:163], v[120:123], v[144:151], v4\n\t" \
    "s_waitcnt vmcnt(0)\n\t" \
    "v_smfmac_f32_16x16x64_bf16 v[160:163], v[124:127], v[152:159], v4\n\t" \
    "s_nop 7\n\t" \
    "v_pk_add_f32 v[160:161], v[160:161], v[162:163]\n\t" \
    "v_cvt_pk_bf16_f32 v184, v160, v161\n\t" \
    "v_cmp_u_f32 vcc, v160, v161\n\t" \
    "s_nop 0\n\t" \
    "v_mov_b32_dpp v185, v184 quad_perm:[1,0,3,2] row_mask:0xf bank_mask:0xf\n\t" \
    "v_perm_b32 v184, v185, v184, v5\n\t" \
    "s_cbranch_vccnz rr_bad10_%=\n\t" \
    "rr_cont11_%=:\n\t" \
    "global_store_dword v2, v184, s[48:49] sc1\n\t" \
    "global_store_dword v181, v184, s[48:49] sc1\n\t" \
    "global_store_dword v182, v184, s[48:49] sc1\n\t" \
    "global_store_dword v183, v184, s[48:49] sc1\n\t" \
    "s_cmp_eq_u32 s91, 3\n\t" \
    "s_cbranch_scc1 rr_dry28_%=\n\t" \
    "s_cmp_eq_u32 s65, 62\n\t" \
    "s_cbranch_scc1 rr_end8_%=\n\t" \
    "s_add_u32 s65, s65, 2\n\t" \
    "s_branch rr_pf13_%=\n\t" \
    "rr_bad10_%=:\n\t" \
    "s_waitcnt lgkmcnt(0)\n\t" \
    "v_max_u32 v185, v48, v49\n\t" \
    "v_max3_u32 v185, v185, v50, v51\n\t" \
    "v_max3_u32 v185, v185, v52, v53\n\t" \
    "v_max3_u32 v185, v185, v54, v55\n\t" \
    "v_max3_u32 v185, v185, v56, v57\n\t" \
    "v_max3_u32 v185, v185, v58, v59\n\t" \
    "v_max3_u32 v185, v185, v60, v61\n\t" \
    "v_max3_u32 v185, v185, v62, v63\n\t" \
    "v_max3_u32 v185, v185, v64, v65\n\t" \
    "v_max3_u32 v185, v185, v66, v67\n\t" \
    "v_max3_u32 v185, v185, v68, v69\n\t" \
    "v_max3_u32 v185, v185, v70, v71\n\t" \
    "v_max3_u32 v185, v185, v72, v73\n\t" \
    "v_max3_u32 v185, v185, v74, v75\n\t" \
    "v_max3_u32 v185, v185, v76, v77\n\t" \
    "v_max3_u32 v185, v185, v78, v79\n\t" \
    "v_max3_u32 v185, v185, v80, v81\n\t" \
    "v_max3_u32 v185, v185, v82, v83\n\t" \
    "v_max3_u32 v185, v185, v84, v85\n\t" \
    "v_max3_u32 v185, v185, v86, v87\n\t" \
    "v_max3_u32 v185, v185, v88, v89\n\t" \
    "v_max3_u32 v185, v185, v90, v91\n\t" \
    "v_max3_u32 v185, v185, v92, v93\n\t" \
    "v_max3_u32 v185, v185, v94, v95\n\t" \
    "v_max3_u32 v185, v185, v96, v97\n\t" \
    "v_max3_u32 v185, v185, v98, v99\n\t" \
    "v_max3_u32 v185, v185, v100, v101\n\t" \
    "v_max3_u32 v185, v185, v102, v103\n\t" \
    "v_max3_u32 v185, v185, v104, v105\n\t" \
    "v_max3_u32 v185, v185, v106, v107\n\t" \
    "v_max3_u32 v185, v185, v108, v109\n\t" \
    "v_max3_u32 v185, v185, v110, v111\n\t" \
    "v_max3_u32 v185, v185, v112, v113\n\t" \
    "v_max3_u32 v185, v185, v114, v115\n\t" \
    "v_max3_u32 v185, v185, v116, v117\n\t" \
    "v_max3_u32 v185, v185, v118, v119\n\t" \
    "v_max3_u32 v185, v185, v120, v121\n\t" \
    "v_max3_u32 v185, v185, v122, v123\n\t" \
    "v_max3_u32 v185, v185, v124, v125\n\t" \
    "v_max3_u32 v185, v185, v126, v127\n\t" \
    "v_max_u32 v186, v128, v129\n\t" \
    "v_max3_u32 v186, v186, v130, v131\n\t" \
    "v_max3_u32 v186, v186, v132, v133\n\t" \
    "v_max3_u32 v186, v186, v134, v135\n\t" \
    "v_max3_u32 v186, v186, v136, v137\n\t" \
    "v_max3_u32 v186, v186, v138, v139\n\t" \
    "v_max3_u32 v186, v186, v140, v141\n\t" \
    "v_max3_u32 v186, v186, v142, v143\n\t" \
    "v_max3_u32 v186, v186, v144, v145\n\t" \
    "v_max3_u32 v186, v186, v146, v147\n\t" \
    "v_max3_u32 v186, v186, v148, v149\n\t" \
    "v_max3_u32 v186, v186, v150, v151\n\t" \
    "v_max3_u32 v186, v186, v152, v153\n\t" \
    "v_max3_u32 v186, v186, v154, v155\n\t" \
    "v_max3_u32 v186, v186, v156, v157\n\t" \
    "v_max3_u32 v186, v186, v158, v159\n\t" \
    "v_max_u32 v185, v185, v186\n\t" \
    "v_cmp_eq_u32 vcc, -1, v185\n\t" \
    "s_cbranch_vccz rr_cont11_%=\n\t" \
    "s_mov_b32 s91, 1\n\t" \
    "v_mov_b32 v160, 0\n\t" \
    "v_mov_b32 v161, 0\n\t" \
    "v_mov_b32 v162, 0\n\t" \
    "v_mov_b32 v163, 0\n\t" \
    "s_cmp_eq_u32 s68, 0\n\t" \
    "s_cbranch_scc1 rr_F99_%=\n\t" \
    "s_sub_u32 s68, s68, 1\n\t" \
    "s_branch rr_again9_%=\n\t" \
    "rr_F99_%=:\n\t" \
    "v_mov_b32 v186, 6095104\n\t" \
    "v_mov_b32 v187, 3\n\t" \
    "global_store_dword v186, v187, s[36:37] sc1\n\t" \
    "s_waitcnt vmcnt(0)\n\t" \
    "s_branch rr_cont11_%=\n\t" \
    "rr_hlate89_%=:\n\t" \
    "s_waitcnt lgkmcnt(0)\n\t" \
    "v_add_u32 v189, 1024, v7\n\t" \
    "global_load_dword v186, v189, s[46:47] sc1\n\t" \
    "s_sleep 4\n\t" \
    "v_add_u32 v189, 2048, v7\n\t" \
    "global_load_dword v187, v189, s[46:47] sc1\n\t" \
    "s_waitcnt vmcnt(1)\n\t" \
    "v_cmp_eq_u32 vcc, -1, v186\n\t" \
    "s_cbranch_vccz rr_hg100_%=\n\t" \
    "v_add_u32 v189, 3072, v7\n\t" \
    "global_load_dword v186, v189, s[46:47] sc1\n\t" \
    "s_waitcnt vmcnt(1)\n\t" \
    "v_cmp_eq_u32 vcc, -1, v187\n\t" \
    "s_cbranch_vccz rr_hg101_%=\n\t" \
    "s_waitcnt vmcnt(0)\n\t" \
    "v_cmp_eq_u32 vcc, -1, v186\n\t" \
    "s_cbranch_vccz rr_hg100_%=\n\t" \
    "rr_hp102_%=:\n\t" \
    "global_load_dword v184, v189, s[46:47] sc1\n\t" \
    "s_waitcnt vmcnt(0)\n\t" \
    "v_cmp_eq_u32 vcc, -1, v184\n\t" \
    "s_cbranch_vccz rr_hpd103_%=\n\t" \
    "s_cmp_eq_u32 s68, 0\n\t" \
    "s_cbranch_scc1 rr_F104_%=\n\t" \
    "s_sub_u32 s68, s68, 1\n\t" \
    "s_sleep 1\n\t" \
    "s_branch rr_hp102_%=\n\t" \
    "rr_F104_%=:\n\t" \
    "v_mov_b32 v186, 6095104\n\t" \
    "v_mov_b32 v187, 7\n\t" \
    "global_store_dword v186, v187, s[36:37] sc1\n\t" \
    "s_waitcnt vmcnt(0)\n\t" \
    "s_branch rr_hpd103_%=\n\t" \
    "rr_hpd103_%=:\n\t" \
    "rr_h025_%=:\n\t" \
    "v_and_b32 v184, -2, v184\n\t" \
    "s_mov_b32 s71, 3\n\t" \
    "rr_E105_%=:\n\t" \
    "s_memtime s[54:55]\n\t" \
    "s_nop 0\n\t" \
    "s_memrealtime s[4:5]\n\t" \
    "s_nop 0\n\t" \
    "s_memrealtime s[6:7]\n\t" \
    "s_nop 0\n\t" \
    "s_memrealtime s[8:9]\n\t" \
    "s_nop 0\n\t" \
    "s_memrealtime s[10:11]\n\t" \
    "s_nop 0\n\t" \
    "s_memrealtime s[12:13]\n\t" \
    "s_nop 0\n\t" \
    "s_memrealtime s[14:15]\n\t" \
    "s_nop 0\n\t" \
    "s_memrealtime s[16:17]\n\t" \
    "s_nop 0\n\t" \
    "s_memrealtime s[18:19]\n\t" \
    "s_nop 0\n\t" \
    "s_memrealtime s[20:21]\n\t" \
    "s_nop 0\n\t" \
    "s_memrealtime s[22:23]\n\t" \
    "s_nop 0\n\t" \
    "s_memrealtime s[24:25]\n\t" \
    "s_nop 0\n\t" \
    "s_memrealtime s[26:27]\n\t" \
    "s_nop 0\n\t" \
    "s_memrealtime s[28:29]\n\t" \
    "s_nop 0\n\t" \
    "s_memtime s[56:57]\n\t" \
    "s_mov_b32 s70, 1\n\t" \
    "s_sub_u32 s71, s71, 1\n\t" \
    "s_cselect_b32 s69, 0, -1\n\t" \
    "s_waitcnt lgkmcnt(0)\n\t" \
    "s_sub_u32 s54, s56, s54\n\t" \
    "s_and_b32 s54, s54, s69\n\t" \
    "s_cmp_gt_u32 s54, 124\n\t" \
    "s_cbranch_scc1 rr_E105_%=\n\t" \
    "s_cmp_eq_u32 s4, s28\n\t" \
    "s_addc_u32 s70, s70, 0\n\t" \
    "s_cmp_eq_u32 s6, s28\n\t" \
    "s_addc_u32 s70, s70, 0\n\t" \
    "s_cmp_eq_u32 s8, s28\n\t" \
    "s_addc_u32 s70, s70, 0\n\t" \
    "s_cmp_eq_u32 s10, s28\n\t" \
    "s_addc_u32 s70, s70, 0\n\t" \
    "s_cmp_eq_u32 s12, s28\n\t" \
    "s_addc_u32 s70, s70, 0\n\t" \
    "s_cmp_eq_u32 s14, s28\n\t" \
    "s_addc_u32 s70, s70, 0\n\t" \
    "s_cmp_eq_u32 s16, s28\n\t" \
    "s_addc_u32 s70, s70, 0\n\t" \
    "s_cmp_eq_u32 s18, s28\n\t" \
    "s_addc_u32 s70, s70, 0\n\t" \
    "s_cmp_eq_u32 s20, s28\n\t" \
    "s_addc_u32 s70, s70, 0\n\t" \
    "s_cmp_eq_u32 s22, s28\n\t" \
    "s_addc_u32 s70, s70, 0\n\t" \
    "s_cmp_eq_u32 s24, s28\n\t" \
    "s_addc_u32 s70, s70, 0\n\t" \
    "s_cmp_eq_u32 s26, s28\n\t" \
    "s_addc_u32 s70, s70, 0\n\t" \
    "s_mov_b32 s75, s73\n\t" \
    "s_mov_b32 s76, s74\n\t" \
    "s_lshl_b32 s70, s70, 3\n\t" \
    "s_sub_u32 s73, s56, s70\n\t" \
    "s_lshl_b32 s74, s28, 4\n\t" \
    "rr_h126_%=:\n\t" \
    "s_sub_u32 s97, s74, s86\n\t" \
    "s_sub_u32 s71, s73, 190\n\t" \
    "s_getpc_b64 s[30:31]\n\t" \
    "rr_wp107_%=:\n\t" \
    "s_add_u32 s30, s30, rr_again9_%=-rr_wp107_%=\n\t" \
    "s_addc_u32 s31, s31, (rr_again9_%=-rr_wp107_%=)>>32\n\t" \
    "s_branch rr_hok88_%=\n\t" \
    "rr_hg100_%=:\n\t" \
    "v_mov_b32 v184, v186\n\t" \
    "s_branch rr_hok88_%=\n\t" \
    "rr_hg101_%=:\n\t" \
    "v_mov_b32 v184, v187\n\t" \
    "s_branch rr_hok88_%=\n\t" \
    "rr_end8_%=:\n\t" \
    "s_waitcnt vmcnt(0) lgkmcnt(0)\n\t" \
    "s_endpgm\n\t" \
    "rr_last_%=:\n\t" \
    ""
#define ASM_O \
    "s_mov_b64 s[36:37], %0\n\t" \
    "s_mov_b64 s[42:43], %1\n\t" \
    "s_mov_b64 s[44:45], %2\n\t" \
    "s_mov_b32 s66, %3\n\t" \
    "s_mov_b32 s67, %4\n\t" \
    "s_mov_b64 s[50:51], %5\n\t" \
    "s_mov_b32 s82, %7\n\t" \
    "v_mbcnt_lo_u32_b32 v223, -1, 0\n\t" \
    "v_mbcnt_hi_u32_b32 v223, -1, v223\n\t" \
    "s_getpc_b64 s[46:47]\n\t" \
    "oo_pc_%=:\n\t" \
    "s_lshl_b32 s69, s66, 13\n\t" \
    "v_lshl_add_u32 v222, v223, 7, s69\n\t" \
    "v_min_u32 v222, oo_last_%=-oo_pc_%=-4, v222\n\t" \
    "global_load_dword v222, v222, s[46:47]\n\t" \
    "v_lshlrev_b32 v223, 2, v223\n\t" \
    "v_add_u32 v223, s82, v223\n\t" \
    "ds_read_b32 v0, v223 offset:0\n\t" \
    "ds_read_b32 v1, v223 offset:256\n\t" \
    "ds_read_b32 v2, v223 offset:512\n\t" \
    "ds_read_b32 v3, v223 offset:768\n\t" \
    "ds_read_b32 v4, v223 offset:1024\n\t" \
    "ds_read_b32 v5, v223 offset:1280\n\t" \
    "ds_read_b32 v6, v223 offset:1536\n\t" \
    "ds_read_b32 v7, v223 offset:1792\n\t" \
    "ds_read_b32 v8, v223 offset:2048\n\t" \
    "ds_read_b32 v9, v223 offset:2304\n\t" \
    "ds_read_b32 v10, v223 offset:2560\n\t" \
    "ds_read_b32 v11, v223 offset:2816\n\t" \
    "s_waitcnt lgkmcnt(0)\n\t" \
    "ds_read_b32 v12, v223 offset:3072\n\t" \
    "ds_read_b32 v13, v223 offset:3328\n\t" \
    "ds_read_b32 v14, v223 offset:3584\n\t" \
    "ds_read_b32 v15, v223 offset:3840\n\t" \
    "ds_read_b32 v16, v223 offset:4096\n\t" \
    "ds_read_b32 v17, v223 offset:4352\n\t" \
    "s_waitcnt lgkmcnt(0)\n\t" \
    "s_mov_b32 s68, 262144\n\t" \
    "s_mov_b32 s72, 0\n\t" \
    "s_mov_b32 s83, -1\n\t" \
    "s_mov_b32 s65, 0\n\t" \
    "s_barrier\n\t" \
    "s_mov_b32 s65, s67\n\t" \
    "s_mov_b32 s96, s67\n\t" \
    "s_add_u32 s38, s36, 48234496\n\t" \
    "s_addc_u32 s39, s37, 0\n\t" \
    "oo_poll1_%=:\n\t" \
    "s_cmp_gt_u32 s96, 34\n\t" \
    "s_cselect_b64 vcc, -1, 0\n\t" \
    "v_cndmask_b32 v25, v12, v13, vcc\n\t" \
    "global_load_dword v24, v25, s[36:37] sc1\n\t" \
    "s_waitcnt vmcnt(0)\n\t" \
    "v_cmp_gt_i32 vcc, s96, v24\n\t" \
    "s_cbranch_vccz oo_go2_%=\n\t" \
    "s_cmp_eq_u32 s68, 0\n\t" \
    "s_cbranch_scc1 oo_F7_%=\n\t" \
    "s_sub_u32 s68, s68, 1\n\t" \
    "s_sleep 10\n\t" \
    "s_branch oo_poll1_%=\n\t" \
    "oo_F7_%=:\n\t" \
    "v_mov_b32 v25, 6095104\n\t" \
    "v_mov_b32 v26, 40\n\t" \
    "global_store_dword v25, v26, s[36:37] sc1\n\t" \
    "s_waitcnt vmcnt(0)\n\t" \
    "s_branch oo_go2_%=\n\t" \
    "oo_go2_%=:\n\t" \
    "s_mul_i32 s69, s96, 65536\n\t" \
    "s_add_u32 s69, s69, 146800640\n\t" \
    "s_add_u32 s40, s36, s69\n\t" \
    "s_addc_u32 s41, s37, 0\n\t" \
    "s_lshl_b32 s69, s96, 15\n\t" \
    "v_xor_b32 v18, s69, v0\n\t" \
    "v_xor_b32 v19, s69, v1\n\t" \
    "v_xor_b32 v20, s69, v2\n\t" \
    "v_xor_b32 v21, s69, v3\n\t" \
    "v_xor_b32 v22, s69, v4\n\t" \
    "v_xor_b32 v23, s69, v5\n\t" \
    "global_load_dwordx4 v[32:35], v6, s[40:41] offset:-4096 sc1\n\t" \
    "global_load_dwordx4 v[36:39], v6, s[40:41] offset:-3072 sc1\n\t" \
    "global_load_dwordx4 a[0:3], v18, s[38:39] offset:-4096 sc1\n\t" \
    "global_load_dwordx4 a[4:7], v19, s[38:39] offset:-4096 sc1\n\t" \
    "global_load_dwordx4 a[8:11], v20, s[38:39] offset:-4096 sc1\n\t" \
    "global_load_dwordx4 a[12:15], v21, s[38:39] offset:-4096 sc1\n\t" \
    "global_load_dwordx4 a[16:19], v22, s[38:39] offset:-4096 sc1\n\t" \
    "global_load_dwordx4 a[20:23], v23, s[38:39] offset:-4096 sc1\n\t" \
    "global_load_dwordx4 v[40:43], v6, s[40:41] offset:-2048 sc1\n\t" \
    "global_load_dwordx4 v[44:47], v6, s[40:41] offset:-1024 sc1\n\t" \
    "global_load_dwordx4 a[24:27], v18, s[38:39] offset:-3072 sc1\n\t" \
    "global_load_dwordx4 a[28:31], v19, s[38:39] offset:-3072 sc1\n\t" \
    "global_load_dwordx4 a[32:35], v20, s[38:39] offset:-3072 sc1\n\t" \
    "global_load_dwordx4 a[36:39], v21, s[38:39] offset:-3072 sc1\n\t" \
    "global_load_dwordx4 a[40:43], v22, s[38:39] offset:-3072 sc1\n\t" \
    "global_load_dwordx4 a[44:47], v23, s[38:39] offset:-3072 sc1\n\t" \
    "global_load_dwordx4 v[48:51], v6, s[40:41] offset:0 sc1\n\t" \
    "global_load_dwordx4 v[52:55], v6, s[40:41] offset:1024 sc1\n\t" \
    "global_load_dwordx4 a[48:51], v18, s[38:39] offset:-2048 sc1\n\t" \
    "global_load_dwordx4 a[52:55], v19, s[38:39] offset:-2048 sc1\n\t" \
    "global_load_dwordx4 a[56:59], v20, s[38:39] offset:-2048 sc1\n\t" \
    "global_load_dwordx4 a[60:63], v21, s[38:39] offset:-2048 sc1\n\t" \
    "global_load_dwordx4 a[64:67], v22, s[38:39] offset:-2048 sc1\n\t" \
    "global_load_dwordx4 a[68:71], v23, s[38:39] offset:-2048 sc1\n\t" \
    "global_load_dwordx4 v[56:59], v6, s[40:41] offset:2048 sc1\n\t" \
    "global_load_dwordx4 v[60:63], v6, s[40:41] offset:3072 sc1\n\t" \
    "global_load_dwordx4 a[72:75], v18, s[38:39] offset:-1024 sc1\n\t" \
    "global_load_dwordx4 a[76:79], v19, s[38:39] offset:-1024 sc1\n\t" \
    "global_load_dwordx4 a[80:83], v20, s[38:39] offset:-1024 sc1\n\t" \
    "global_load_dwordx4 a[84:87], v21, s[38:39] offset:-1024 sc1\n\t" \
    "global_load_dwordx4 a[88:91], v22, s[38:39] offset:-1024 sc1\n\t" \
    "global_load_dwordx4 a[92:95], v23, s[38:39] offset:-1024 sc1\n\t" \
    "oo_loop3_%=:\n\t" \
    "s_mul_i32 s69, s96, 16384\n\t" \
    "s_add_u32 s69, s69, 150994944\n\t" \
    "s_add_u32 s46, s36, s69\n\t" \
    "s_addc_u32 s47, s37, 0\n\t" \
    "s_mul_i32 s69, s96, 9216\n\t" \
    "s_add_u32 s69, s69, 0\n\t" \
    "s_add_u32 s48, s36, s69\n\t" \
    "s_addc_u32 s49, s37, 0\n\t" \
    "s_waitcnt vmcnt(29)\n\t" \
    "v_mfma_f32_16x16x32_bf16 v[72:75], a[0:3], v[32:35], 0\n\t" \
    "global_load_dwordx4 v[64:67], v7, s[40:41] offset:-4096 sc1\n\t" \
    "v_mfma_f32_16x16x32_bf16 v[76:79], a[0:3], v[36:39], 0\n\t" \
    "global_load_dwordx4 v[68:71], v7, s[40:41] offset:-3072 sc1\n\t" \
    "s_waitcnt vmcnt(30)\n\t" \
    "v_mfma_f32_16x16x32_bf16 v[80:83], a[4:7], v[32:35], 0\n\t" \
    "global_load_dwordx4 a[96:99], v18, s[38:39] offset:0 sc1\n\t" \
    "v_mfma_f32_16x16x32_bf16 v[84:87], a[4:7], v[36:39], 0\n\t" \
    "global_load_dwordx4 a[100:103], v19, s[38:39] offset:0 sc1\n\t" \
    "s_waitcnt vmcnt(31)\n\t" \
    "v_mfma_f32_16x16x32_bf16 v[88:91], a[8:11], v[32:35], 0\n\t" \
    "global_load_dwordx4 a[104:107], v20, s[38:39] offset:0 sc1\n\t" \
    "v_mfma_f32_16x16x32_bf16 v[92:95], a[8:11], v[36:39], 0\n\t" \
    "global_load_dwordx4 a[108:111], v21, s[38:39] offset:0 sc1\n\t" \
    "s_waitcnt vmcnt(32)\n\t" \
    "v_mfma_f32_16x16x32_bf16 v[96:99], a[12:15], v[32:35], 0\n\t" \
    "global_load_dwordx4 a[112:115], v22, s[38:39] offset:0 sc1\n\t" \
    "v_mfma_f32_16x16x32_bf16 v[100:103], a[12:15], v[36:39], 0\n\t" \
    "global_load_dwordx4 a[116:119], v23, s[38:39] offset:0 sc1\n\t" \
    "s_waitcnt vmcnt(33)\n\t" \
    "v_mfma_f32_16x16x32_bf16 v[104:107], a[16:19], v[32:35], 0\n\t" \
    "v_mfma_f32_16x16x32_bf16 v[108:111], a[16:19], v[36:39], 0\n\t" \
    "s_waitcnt vmcnt(32)\n\t" \
    "v_mfma_f32_16x16x32_bf16 v[112:115], a[20:23], v[32:35], 0\n\t" \
    "v_mfma_f32_16x16x32_bf16 v[116:119], a[20:23], v[36:39], 0\n\t" \
    "s_add_u32 s96, s96, 4\n\t" \
    "s_cmp_gt_u32 s96, 34\n\t" \
    "s_cselect_b64 vcc, -1, 0\n\t" \
    "v_cndmask_b32 v25, v12, v13, vcc\n\t" \
    "global_load_dword v24, v25, s[36:37] sc1\n\t" \
    "s_waitcnt vmcnt(30)\n\t" \
    "v_mfma_f32_16x16x32_bf16 v[72:75], a[24:27], v[40:43], v[72:75]\n\t" \
    "global_load_dwordx4 v[32:35], v7, s[40:41] offset:-2048 sc1\n\t" \
    "v_mfma_f32_16x16x32_bf16 v[76:79], a[24:27], v[44:47], v[76:79]\n\t" \
    "global_load_dwordx4 v[36:39], v7, s[40:41] offset:-1024 sc1\n\t" \
    "s_waitcnt vmcnt(31)\n\t" \
    "v_mfma_f32_16x16x32_bf16 v[80:83], a[28:31], v[40:43], v[80:83]\n\t" \
    "global_load_dwordx4 a[0:3], v18, s[38:39] offset:1024 sc1\n\t" \
    "v_mfma_f32_16x16x32_bf16 v[84:87], a[28:31], v[44:47], v[84:87]\n\t" \
    "global_load_dwordx4 a[4:7], v19, s[38:39] offset:1024 sc1\n\t" \
    "s_waitcnt vmcnt(32)\n\t" \
    "v_mfma_f32_16x16x32_bf16 v[88:91], a[32:35], v[40:43], v[88:91]\n\t" \
    "global_load_dwordx4 a[8:11], v20, s[38:39] offset:1024 sc1\n\t" \
    "v_mfma_f32_16x16x32_bf16 v[92:95], a[32:35], v[44:47], v[92:95]\n\t" \
    "global_load_dwordx4 a[12:15], v21, s[38:39] offset:1024 sc1\n\t" \
    "s_waitcnt vmcnt(33)\n\t" \
    "v_mfma_f32_16x16x32_bf16 v[96:99], a[36:39], v[40:43], v[96:99]\n\t" \
    "global_load_dwordx4 a[16:19], v22, s[38:39] offset:1024 sc1\n\t" \
    "v_mfma_f32_16x16x32_bf16 v[100:103], a[36:39], v[44:47], v[100:103]\n\t" \
    "global_load_dwordx4 a[20:23], v23, s[38:39] offset:1024 sc1\n\t" \
    "s_waitcnt vmcnt(34)\n\t" \
    "v_mfma_f32_16x16x32_bf16 v[104:107], a[40:43], v[40:43], v[104:107]\n\t" \
    "v_mfma_f32_16x16x32_bf16 v[108:111], a[40:43], v[44:47], v[108:111]\n\t" \
    "s_waitcnt vmcnt(33)\n\t" \
    "v_mfma_f32_16x16x32_bf16 v[112:115], a[44:47], v[40:43], v[112:115]\n\t" \
    "v_mfma_f32_16x16x32_bf16 v[116:119], a[44:47], v[44:47], v[116:119]\n\t" \
    "s_waitcnt vmcnt(30)\n\t" \
    "v_mfma_f32_16x16x32_bf16 v[72:75], a[48:51], v[48:51], v[72:75]\n\t" \
    "global_load_dwordx4 v[40:43], v7, s[40:41] offset:0 sc1\n\t" \
    "v_mfma_f32_16x16x32_bf16 v[76:79], a[48:51], v[52:55], v[76:79]\n\t" \
    "global_load_dwordx4 v[44:47], v7, s[40:41] offset:1024 sc1\n\t" \
    "s_waitcnt vmcnt(31)\n\t" \
    "v_mfma_f32_16x16x32_bf16 v[80:83], a[52:55], v[48:51], v[80:83]\n\t" \
    "global_load_dwordx4 a[24:27], v18, s[38:39] offset:2048 sc1\n\t" \
    "v_mfma_f32_16x16x32_bf16 v[84:87], a[52:55], v[52:55], v[84:87]\n\t" \
    "global_load_dwordx4 a[28:31], v19, s[38:39] offset:2048 sc1\n\t" \
    "s_waitcnt vmcnt(32)\n\t" \
    "v_mfma_f32_16x16x32_bf16 v[88:91], a[56:59], v[48:51], v[88:91]\n\t" \
    "global_load_dwordx4 a[32:35], v20, s[38:39] offset:2048 sc1\n\t" \
    "v_mfma_f32_16x16x32_bf16 v[92:95], a[56:59], v[52:55], v[92:95]\n\t" \
    "global_load_dwordx4 a[36:39], v21, s[38:39] offset:2048 sc1\n\t" \
    "s_waitcnt vmcnt(33)\n\t" \
    "v_mfma_f32_16x16x32_bf16 v[96:99], a[60:63], v[48:51], v[96:99]\n\t" \
    "global_load_dwordx4 a[40:43], v22, s[38:39] offset:2048 sc1\n\t" \
    "v_mfma_f32_16x16x32_bf16 v[100:103], a[60:63], v[52:55], v[100:103]\n\t" \
    "global_load_dwordx4 a[44:47], v23, s[38:39] offset:2048 sc1\n\t" \
    "s_waitcnt vmcnt(34)\n\t" \
    "v_mfma_f32_16x16x32_bf16 v[104:107], a[64:67], v[48:51], v[104:107]\n\t" \
    "v_mfma_f32_16x16x32_bf16 v[108:111], a[64:67], v[52:55], v[108:111]\n\t" \
    "s_waitcnt vmcnt(33)\n\t" \
    "v_mfma_f32_16x16x32_bf16 v[112:115], a[68:71], v[48:51], v[112:115]\n\t" \
    "v_mfma_f32_16x16x32_bf16 v[116:119], a[68:71], v[52:55], v[116:119]\n\t" \
    "s_waitcnt vmcnt(30)\n\t" \
    "v_mfma_f32_16x16x32_bf16 v[72:75], a[72:75], v[56:59], v[72:75]\n\t" \
    "global_load_dwordx4 v[48:51], v7, s[40:41] offset:2048 sc1\n\t" \
    "v_mfma_f32_16x16x32_bf16 v[76:79], a[72:75], v[60:63], v[76:79]\n\t" \
    "global_load_dwordx4 v[52:55], v7, s[40:41] offset:3072 sc1\n\t" \
    "s_waitcnt vmcnt(31)\n\t" \
    "v_mfma_f32_16x16x32_bf16 v[80:83], a[76:79], v[56:59], v[80:83]\n\t" \
    "global_load_dwordx4 a[48:51], v18, s[38:39] offset:3072 sc1\n\t" \
    "v_mfma_f32_16x16x32_bf16 v[84:87], a[76:79], v[60:63], v[84:87]\n\t" \
    "global_load_dwordx4 a[52:55], v19, s[38:39] offset:3072 sc1\n\t" \
    "s_waitcnt vmcnt(32)\n\t" \
    "v_mfma_f32_16x16x32_bf16 v[88:91], a[80:83], v[56:59], v[88:91]\n\t" \
    "global_load_dwordx4 a[56:59], v20, s[38:39] offset:3072 sc1\n\t" \
    "v_mfma_f32_16x16x32_bf16 v[92:95], a[80:83], v[60:63], v[92:95]\n\t" \
    "global_load_dwordx4 a[60:63], v21, s[38:39] offset:3072 sc1\n\t" \
    "s_waitcnt vmcnt(33)\n\t" \
    "v_mfma_f32_16x16x32_bf16 v[96:99], a[84:87], v[56:59], v[96:99]\n\t" \
    "global_load_dwordx4 a[64:67], v22, s[38:39] offset:3072 sc1\n\t" \
    "v_mfma_f32_16x16x32_bf16 v[100:103], a[84:87], v[60:63], v[100:103]\n\t" \
    "global_load_dwordx4 a[68:71], v23, s[38:39] offset:3072 sc1\n\t" \
    "s_waitcnt vmcnt(34)\n\t" \
    "v_mfma_f32_16x16x32_bf16 v[104:107], a[88:91], v[56:59], v[104:107]\n\t" \
    "v_mfma_f32_16x16x32_bf16 v[108:111], a[88:91], v[60:63], v[108:111]\n\t" \
    "s_waitcnt vmcnt(33)\n\t" \
    "v_mfma_f32_16x16x32_bf16 v[112:115], a[92:95], v[56:59], v[112:115]\n\t" \
    "v_mfma_f32_16x16x32_bf16 v[116:119], a[92:95], v[60:63], v[116:119]\n\t" \
    "s_waitcnt vmcnt(30)\n\t" \
    "v_mfma_f32_16x16x32_bf16 v[72:75], a[96:99], v[64:67], v[72:75]\n\t" \
    "global_load_dwordx4 v[56:59], v8, s[46:47] offset:0 sc1\n\t" \
    "v_mfma_f32_16x16x32_bf16 v[76:79], a[96:99], v[68:71], v[76:79]\n\t" \
    "global_load_dwordx4 v[60:63], v9, s[46:47] offset:0 sc1\n\t" \
    "s_waitcnt vmcnt(31)\n\t" \
    "v_mfma_f32_16x16x32_bf16 v[80:83], a[100:103], v[64:67], v[80:83]\n\t" \
    "global_load_dwordx4 a[72:75], v10, s[48:49] offset:0 sc1\n\t" \
    "v_mfma_f32_16x16x32_bf16 v[84:87], a[100:103], v[68:71], v[84:87]\n\t" \
    "global_load_dwordx4 a[76:79], v10, s[48:49] offset:256 sc1\n\t" \
    "s_waitcnt vmcnt(32)\n\t" \
    "v_mfma_f32_16x16x32_bf16 v[88:91], a[104:107], v[64:67], v[88:91]\n\t" \
    "global_load_dwordx4 a[80:83], v10, s[48:49] offset:512 sc1\n\t" \
    "v_mfma_f32_16x16x32_bf16 v[92:95], a[104:107], v[68:71], v[92:95]\n\t" \
    "global_load_dwordx4 a[84:87], v10, s[48:49] offset:768 sc1\n\t" \
    "s_waitcnt vmcnt(33)\n\t" \
    "v_mfma_f32_16x16x32_bf16 v[96:99], a[108:111], v[64:67], v[96:99]\n\t" \
    "global_load_dwordx4 a[88:91], v10, s[48:49] offset:1024 sc1\n\t" \
    "v_mfma_f32_16x16x32_bf16 v[100:103], a[108:111], v[68:71], v[100:103]\n\t" \
    "global_load_dwordx4 a[92:95], v10, s[48:49] offset:1280 sc1\n\t" \
    "s_waitcnt vmcnt(34)\n\t" \
    "v_mfma_f32_16x16x32_bf16 v[104:107], a[112:115], v[64:67], v[104:107]\n\t" \
    "v_mfma_f32_16x16x32_bf16 v[108:111], a[112:115], v[68:71], v[108:111]\n\t" \
    "s_waitcnt vmcnt(33)\n\t" \
    "v_mfma_f32_16x16x32_bf16 v[112:115], a[116:119], v[64:67], v[112:115]\n\t" \
    "v_mfma_f32_16x16x32_bf16 v[116:119], a[116:119], v[68:71], v[116:119]\n\t" \
    "s_waitcnt vmcnt(29)\n\t" \
    "v_mfma_f32_16x16x32_bf16 v[72:75], a[0:3], v[32:35], v[72:75]\n\t" \
    "global_load_dwordx4 v[64:67], v8, s[46:47] offset:1024 sc1\n\t" \
    "v_mfma_f32_16x16x32_bf16 v[76:79], a[0:3], v[36:39], v[76:79]\n\t" \
    "global_load_dwordx4 v[68:71], v9, s[46:47] offset:1024 sc1\n\t" \
    "s_waitcnt vmcnt(30)\n\t" \
    "v_mfma_f32_16x16x32_bf16 v[80:83], a[4:7], v[32:35], v[80:83]\n\t" \
    "global_load_dwordx4 a[96:99], v11, s[48:49] offset:0 sc1\n\t" \
    "v_mfma_f32_16x16x32_bf16 v[84:87], a[4:7], v[36:39], v[84:87]\n\t" \
    "global_load_dwordx4 a[100:103], v11, s[48:49] offset:256 sc1\n\t" \
    "s_waitcnt vmcnt(31)\n\t" \
    "v_mfma_f32_16x16x32_bf16 v[88:91], a[8:11], v[32:35], v[88:91]\n\t" \
    "global_load_dwordx4 a[104:107], v11, s[48:49] offset:512 sc1\n\t" \
    "v_mfma_f32_16x16x32_bf16 v[92:95], a[8:11], v[36:39], v[92:95]\n\t" \
    "global_load_dwordx4 a[108:111], v11, s[48:49] offset:768 sc1\n\t" \
    "s_waitcnt vmcnt(32)\n\t" \
    "v_mfma_f32_16x16x32_bf16 v[96:99], a[12:15], v[32:35], v[96:99]\n\t" \
    "global_load_dwordx4 a[112:115], v11, s[48:49] offset:1024 sc1\n\t" \
    "v_mfma_f32_16x16x32_bf16 v[100:103], a[12:15], v[36:39], v[100:103]\n\t" \
    "global_load_dwordx4 a[116:119], v11, s[48:49] offset:1280 sc1\n\t" \
    "s_waitcnt vmcnt(33)\n\t" \
    "v_mfma_f32_16x16x32_bf16 v[104:107], a[16:19], v[32:35], v[104:107]\n\t" \
    "v_mfma_f32_16x16x32_bf16 v[108:111], a[16:19], v[36:39], v[108:111]\n\t" \
    "s_waitcnt vmcnt(32)\n\t" \
    "v_mfma_f32_16x16x32_bf16 v[112:115], a[20:23], v[32:35], v[112:115]\n\t" \
    "v_mfma_f32_16x16x32_bf16 v[116:119], a[20:23], v[36:39], v[116:119]\n\t" \
    "v_cmp_gt_i32 vcc, s96, v24\n\t" \
    "s_cmp_ge_u32 s96, 58\n\t" \
    "s_cbranch_scc1 oo_stop4_%=\n\t" \
    "s_cbranch_vccnz oo_stop4_%=\n\t" \
    "s_mul_i32 s69, s96, 65536\n\t" \
    "s_add_u32 s69, s69, 146800640\n\t" \
    "s_add_u32 s40, s36, s69\n\t" \
    "s_addc_u32 s41, s37, 0\n\t" \
    "s_lshl_b32 s69, s96, 15\n\t" \
    "v_xor_b32 v18, s69, v0\n\t" \
    "v_xor_b32 v19, s69, v1\n\t" \
    "v_xor_b32 v20, s69, v2\n\t" \
    "v_xor_b32 v21, s69, v3\n\t" \
    "v_xor_b32 v22, s69, v4\n\t" \
    "v_xor_b32 v23, s69, v5\n\t" \
    "s_waitcnt vmcnt(29)\n\t" \
    "v_mfma_f32_16x16x32_bf16 v[72:75], a[24:27], v[40:43], v[72:75]\n\t" \
    "global_load_dwordx4 v[32:35], v6, s[40:41] offset:-4096 sc1\n\t" \
    "v_mfma_f32_16x16x32_bf16 v[76:79], a[24:27], v[44:47], v[76:79]\n\t" \
    "global_load_dwordx4 v[36:39], v6, s[40:41] offset:-3072 sc1\n\t" \
    "s_waitcnt vmcnt(30)\n\t" \
    "v_mfma_f32_16x16x32_bf16 v[80:83], a[28:31], v[40:43], v[80:83]\n\t" \
    "global_load_dwordx4 a[0:3], v18, s[38:39] offset:-4096 sc1\n\t" \
    "v_mfma_f32_16x16x32_bf16 v[84:87], a[28:31], v[44:47], v[84:87]\n\t" \
    "global_load_dwordx4 a[4:7], v19, s[38:39] offset:-4096 sc1\n\t" \
    "s_waitcnt vmcnt(31)\n\t" \
    "v_mfma_f32_16x16x32_bf16 v[88:91], a[32:35], v[40:43], v[88:91]\n\t" \
    "global_load_dwordx4 a[8:11], v20, s[38:39] offset:-4096 sc1\n\t" \
    "v_mfma_f32_16x16x32_bf16 v[92:95], a[32:35], v[44:47], v[92:95]\n\t" \
    "global_load_dwordx4 a[12:15], v21, s[38:39] offset:-4096 sc1\n\t" \
    "s_waitcnt vmcnt(32)\n\t" \
    "v_mfma_f32_16x16x32_bf16 v[96:99], a[36:39], v[40:43], v[96:99]\n\t" \
    "global_load_dwordx4 a[16:19], v22, s[38:39] offset:-4096 sc1\n\t" \
    "v_mfma_f32_16x16x32_bf16 v[100:103], a[36:39], v[44:47], v[100:103]\n\t" \
    "global_load_dwordx4 a[20:23], v23, s[38:39] offset:-4096 sc1\n\t" \
    "s_waitcnt vmcnt(33)\n\t" \
    "v_mfma_f32_16x16x32_bf16 v[104:107], a[40:43], v[40:43], v[104:107]\n\t" \
    "v_mfma_f32_16x16x32_bf16 v[108:111], a[40:43], v[44:47], v[108:111]\n\t" \
    "s_waitcnt vmcnt(32)\n\t" \
    "v_mfma_f32_16x16x32_bf16 v[112:115], a[44:47], v[40:43], v[112:115]\n\t" \
    "v_mfma_f32_16x16x32_bf16 v[116:119], a[44:47], v[44:47], v[116:119]\n\t" \
    "s_waitcnt vmcnt(29)\n\t" \
    "v_mfma_f32_16x16x32_bf16 v[72:75], a[48:51], v[48:51], v[72:75]\n\t" \
    "global_load_dwordx4 v[40:43], v6, s[40:41] offset:-2048 sc1\n\t" \
    "v_mfma_f32_16x16x32_bf16 v[76:79], a[48:51], v[52:55], v[76:79]\n\t" \
    "global_load_dwordx4 v[44:47], v6, s[40:41] offset:-1024 sc1\n\t" \
    "s_waitcnt vmcnt(30)\n\t" \
    "v_mfma_f32_16x16x32_bf16 v[80:83], a[52:55], v[48:51], v[80:83]\n\t" \
    "global_load_dwordx4 a[24:27], v18, s[38:39] offset:-3072 sc1\n\t" \
    "v_mfma_f32_16x16x32_bf16 v[84:87], a[52:55], v[52:55], v[84:87]\n\t" \
    "global_load_dwordx4 a[28:31], v19, s[38:39] offset:-3072 sc1\n\t" \
    "s_waitcnt vmcnt(31)\n\t" \
    "v_mfma_f32_16x16x32_bf16 v[88:91], a[56:59], v[48:51], v[88:91]\n\t" \
    "global_load_dwordx4 a[32:35], v20, s[38:39] offset:-3072 sc1\n\t" \
    "v_mfma_f32_16x16x32_bf16 v[92:95], a[56:59], v[52:55], v[92:95]\n\t" \
    "global_load_dwordx4 a[36:39], v21, s[38:39] offset:-3072 sc1\n\t" \
    "s_waitcnt vmcnt(32)\n\t" \
    "v_mfma_f32_16x16x32_bf16 v[96:99], a[60:63], v[48:51], v[96:99]\n\t" \
    "global_load_dwordx4 a[40:43], v22, s[38:39] offset:-3072 sc1\n\t" \
    "v_mfma_f32_16x16x32_bf16 v[100:103], a[60:63], v[52:55], v[100:103]\n\t" \
    "global_load_dwordx4 a[44:47], v23, s[38:39] offset:-3072 sc1\n\t" \
    "s_waitcnt vmcnt(33)\n\t" \
    "v_mfma_f32_16x16x32_bf16 v[104:107], a[64:67], v[48:51], v[104:107]\n\t" \
    "v_mfma_f32_16x16x32_bf16 v[108:111], a[64:67], v[52:55], v[108:111]\n\t" \
    "s_waitcnt vmcnt(32)\n\t" \
    "v_mfma_f32_16x16x32_bf16 v[112:115], a[68:71], v[48:51], v[112:115]\n\t" \
    "v_mfma_f32_16x16x32_bf16 v[116:119], a[68:71], v[52:55], v[116:119]\n\t" \
    "s_waitcnt vmcnt(29)\n\t" \
    "v_mfma_f32_16x16x32_bf16 v[72:75], a[72:75], v[56:59], v[72:75]\n\t" \
    "global_load_dwordx4 v[48:51], v6, s[40:41] offset:0 sc1\n\t" \
    "v_mfma_f32_16x16x32_bf16 v[76:79], a[72:75], v[60:63], v[76:79]\n\t" \
    "global_load_dwordx4 v[52:55], v6, s[40:41] offset:1024 sc1\n\t" \
    "s_waitcnt vmcnt(30)\n\t" \
    "v_mfma_f32_16x16x32_bf16 v[80:83], a[76:79], v[56:59], v[80:83]\n\t" \
    "global_load_dwordx4 a[48:51], v18, s[38:39] offset:-2048 sc1\n\t" \
    "v_mfma_f32_16x16x32_bf16 v[84:87], a[76:79], v[60:63], v[84:87]\n\t" \
    "global_load_dwordx4 a[52:55], v19, s[38:39] offset:-2048 sc1\n\t" \
    "s_waitcnt vmcnt(31)\n\t" \
    "v_mfma_f32_16x16x32_bf16 v[88:91], a[80:83], v[56:59], v[88:91]\n\t" \
    "global_load_dwordx4 a[56:59], v20, s[38:39] offset:-2048 sc1\n\t" \
    "v_mfma_f32_16x16x32_bf16 v[92:95], a[80:83], v[60:63], v[92:95]\n\t" \
    "global_load_dwordx4 a[60:63], v21, s[38:39] offset:-2048 sc1\n\t" \
    "s_waitcnt vmcnt(32)\n\t" \
    "v_mfma_f32_16x16x32_bf16 v[96:99], a[84:87], v[56:59], v[96:99]\n\t" \
    "global_load_dwordx4 a[64:67], v22, s[38:39] offset:-2048 sc1\n\t" \
    "v_mfma_f32_16x16x32_bf16 v[100:103], a[84:87], v[60:63], v[100:103]\n\t" \
    "global_load_dwordx4 a[68:71], v23, s[38:39] offset:-2048 sc1\n\t" \
    "s_waitcnt vmcnt(33)\n\t" \
    "v_mfma_f32_16x16x32_bf16 v[104:107], a[88:91], v[56:59], v[104:107]\n\t" \
    "v_mfma_f32_16x16x32_bf16 v[108:111], a[88:91], v[60:63], v[108:111]\n\t" \
    "s_waitcnt vmcnt(32)\n\t" \
    "v_mfma_f32_16x16x32_bf16 v[112:115], a[92:95], v[56:59], v[112:115]\n\t" \
    "v_mfma_f32_16x16x32_bf16 v[116:119], a[92:95], v[60:63], v[116:119]\n\t" \
    "s_waitcnt vmcnt(29)\n\t" \
    "v_mfma_f32_16x16x32_bf16 v[72:75], a[96:99], v[64:67], v[72:75]\n\t" \
    "global_load_dwordx4 v[56:59], v6, s[40:41] offset:2048 sc1\n\t" \
    "v_mfma_f32_16x16x32_bf16 v[76:79], a[96:99], v[68:71], v[76:79]\n\t" \
    "global_load_dwordx4 v[60:63], v6, s[40:41] offset:3072 sc1\n\t" \
    "s_waitcnt vmcnt(30)\n\t" \
    "v_mfma_f32_16x16x32_bf16 v[80:83], a[100:103], v[64:67], v[80:83]\n\t" \
    "global_load_dwordx4 a[72:75], v18, s[38:39] offset:-1024 sc1\n\t" \
    "v_mfma_f32_16x16x32_bf16 v[84:87], a[100:103], v[68:71], v[84:87]\n\t" \
    "global_load_dwordx4 a[76:79], v19, s[38:39] offset:-1024 sc1\n\t" \
    "s_waitcnt vmcnt(31)\n\t" \
    "v_mfma_f32_16x16x32_bf16 v[88:91], a[104:107], v[64:67], v[88:91]\n\t" \
    "global_load_dwordx4 a[80:83], v20, s[38:39] offset:-1024 sc1\n\t" \
    "v_mfma_f32_16x16x32_bf16 v[92:95], a[104:107], v[68:71], v[92:95]\n\t" \
    "global_load_dwordx4 a[84:87], v21, s[38:39] offset:-1024 sc1\n\t" \
    "s_waitcnt vmcnt(32)\n\t" \
    "v_mfma_f32_16x16x32_bf16 v[96:99], a[108:111], v[64:67], v[96:99]\n\t" \
    "global_load_dwordx4 a[88:91], v22, s[38:39] offset:-1024 sc1\n\t" \
    "v_mfma_f32_16x16x32_bf16 v[100:103], a[108:111], v[68:71], v[100:103]\n\t" \
    "global_load_dwordx4 a[92:95], v23, s[38:39] offset:-1024 sc1\n\t" \
    "s_waitcnt vmcnt(33)\n\t" \
    "v_mfma_f32_16x16x32_bf16 v[104:107], a[112:115], v[64:67], v[104:107]\n\t" \
    "v_mfma_f32_16x16x32_bf16 v[108:111], a[112:115], v[68:71], v[108:111]\n\t" \
    "s_waitcnt vmcnt(32)\n\t" \
    "v_mfma_f32_16x16x32_bf16 v[112:115], a[116:119], v[64:67], v[112:115]\n\t" \
    "v_mfma_f32_16x16x32_bf16 v[116:119], a[116:119], v[68:71], v[116:119]\n\t" \
    "s_mov_b32 s97, 1\n\t" \
    "s_branch oo_epi5_%=\n\t" \
    "oo_stop4_%=:\n\t" \
    "s_waitcnt vmcnt(29)\n\t" \
    "v_mfma_f32_16x16x32_bf16 v[72:75], a[24:27], v[40:43], v[72:75]\n\t" \
    "v_mfma_f32_16x16x32_bf16 v[76:79], a[24:27], v[44:47], v[76:79]\n\t" \
    "s_waitcnt vmcnt(28)\n\t" \
    "v_mfma_f32_16x16x32_bf16 v[80:83], a[28:31], v[40:43], v[80:83]\n\t" \
    "v_mfma_f32_16x16x32_bf16 v[84:87], a[28:31], v[44:47], v[84:87]\n\t" \
    "s_waitcnt vmcnt(27)\n\t" \
    "v_mfma_f32_16x16x32_bf16 v[88:91], a[32:35], v[40:43], v[88:91]\n\t" \
    "v_mfma_f32_16x16x32_bf16 v[92:95], a[32:35], v[44:47], v[92:95]\n\t" \
    "s_waitcnt vmcnt(26)\n\t" \
    "v_mfma_f32_16x16x32_bf16 v[96:99], a[36:39], v[40:43], v[96:99]\n\t" \
    "v_mfma_f32_16x16x32_bf16 v[100:103], a[36:39], v[44:47], v[100:103]\n\t" \
    "s_waitcnt vmcnt(25)\n\t" \
    "v_mfma_f32_16x16x32_bf16 v[104:107], a[40:43], v[40:43], v[104:107]\n\t" \
    "v_mfma_f32_16x16x32_bf16 v[108:111], a[40:43], v[44:47], v[108:111]\n\t" \
    "s_waitcnt vmcnt(24)\n\t" \
    "v_mfma_f32_16x16x32_bf16 v[112:115], a[44:47], v[40:43], v[112:115]\n\t" \
    "v_mfma_f32_16x16x32_bf16 v[116:119], a[44:47], v[44:47], v[116:119]\n\t" \
    "s_waitcnt vmcnt(21)\n\t" \
    "v_mfma_f32_16x16x32_bf16 v[72:75], a[48:51], v[48:51], v[72:75]\n\t" \
    "v_mfma_f32_16x16x32_bf16 v[76:79], a[48:51], v[52:55], v[76:79]\n\t" \
    "s_waitcnt vmcnt(20)\n\t" \
    "v_mfma_f32_16x16x32_bf16 v[80:83], a[52:55], v[48:51], v[80:83]\n\t" \
    "v_mfma_f32_16x16x32_bf16 v[84:87], a[52:55], v[52:55], v[84:87]\n\t" \
    "s_waitcnt vmcnt(19)\n\t" \
    "v_mfma_f32_16x16x32_bf16 v[88:91], a[56:59], v[48:51], v[88:91]\n\t" \
    "v_mfma_f32_16x16x32_bf16 v[92:95], a[56:59], v[52:55], v[92:95]\n\t" \
    "s_waitcnt vmcnt(18)\n\t" \
    "v_mfma_f32_16x16x32_bf16 v[96:99], a[60:63], v[48:51], v[96:99]\n\t" \
    "v_mfma_f32_16x16x32_bf16 v[100:103], a[60:63], v[52:55], v[100:103]\n\t" \
    "s_waitcnt vmcnt(17)\n\t" \
    "v_mfma_f32_16x16x32_bf16 v[104:107], a[64:67], v[48:51], v[104:107]\n\t" \
    "v_mfma_f32_16x16x32_bf16 v[108:111], a[64:67], v[52:55], v[108:111]\n\t" \
    "s_waitcnt vmcnt(16)\n\t" \
    "v_mfma_f32_16x16x32_bf16 v[112:115], a[68:71], v[48:51], v[112:115]\n\t" \
    "v_mfma_f32_16x16x32_bf16 v[116:119], a[68:71], v[52:55], v[116:119]\n\t" \
    "s_waitcnt vmcnt(13)\n\t" \
    "v_mfma_f32_16x16x32_bf16 v[72:75], a[72:75], v[56:59], v[72:75]\n\t" \
    "v_mfma_f32_16x16x32_bf16 v[76:79], a[72:75], v[60:63], v[76:79]\n\t" \
    "s_waitcnt vmcnt(12)\n\t" \
    "v_mfma_f32_16x16x32_bf16 v[80:83], a[76:79], v[56:59], v[80:83]\n\t" \
    "v_mfma_f32_16x16x32_bf16 v[84:87], a[76:79], v[60:63], v[84:87]\n\t" \
    "s_waitcnt vmcnt(11)\n\t" \
    "v_mfma_f32_16x16x32_bf16 v[88:91], a[80:83], v[56:59], v[88:91]\n\t" \
    "v_mfma_f32_16x16x32_bf16 v[92:95], a[80:83], v[60:63], v[92:95]\n\t" \
    "s_waitcnt vmcnt(10)\n\t" \
    "v_mfma_f32_16x16x32_bf16 v[96:99], a[84:87], v[56:59], v[96:99]\n\t" \
    "v_mfma_f32_16x16x32_bf16 v[100:103], a[84:87], v[60:63], v[100:103]\n\t" \
    "s_waitcnt vmcnt(9)\n\t" \
    "v_mfma_f32_16x16x32_bf16 v[104:107], a[88:91], v[56:59], v[104:107]\n\t" \
    "v_mfma_f32_16x16x32_bf16 v[108:111], a[88:91], v[60:63], v[108:111]\n\t" \
    "s_waitcnt vmcnt(8)\n\t" \
    "v_mfma_f32_16x16x32_bf16 v[112:115], a[92:95], v[56:59], v[112:115]\n\t" \
    "v_mfma_f32_16x16x32_bf16 v[116:119], a[92:95], v[60:63], v[116:119]\n\t" \
    "s_waitcnt vmcnt(5)\n\t" \
    "v_mfma_f32_16x16x32_bf16 v[72:75], a[96:99], v[64:67], v[72:75]\n\t" \
    "v_mfma_f32_16x16x32_bf16 v[76:79], a[96:99], v[68:71], v[76:79]\n\t" \
    "s_waitcnt vmcnt(4)\n\t" \
    "v_mfma_f32_16x16x32_bf16 v[80:83], a[100:103], v[64:67], v[80:83]\n\t" \
    "v_mfma_f32_16x16x32_bf16 v[84:87], a[100:103], v[68:71], v[84:87]\n\t" \
    "s_waitcnt vmcnt(3)\n\t" \
    "v_mfma_f32_16x16x32_bf16 v[88:91], a[104:107], v[64:67], v[88:91]\n\t" \
    "v_mfma_f32_16x16x32_bf16 v[92:95], a[104:107], v[68:71], v[92:95]\n\t" \
    "s_waitcnt vmcnt(2)\n\t" \
    "v_mfma_f32_16x16x32_bf16 v[96:99], a[108:111], v[64:67], v[96:99]\n\t" \
    "v_mfma_f32_16x16x32_bf16 v[100:103], a[108:111], v[68:71], v[100:103]\n\t" \
    "s_waitcnt vmcnt(1)\n\t" \
    "v_mfma_f32_16x16x32_bf16 v[104:107], a[112:115], v[64:67], v[104:107]\n\t" \
    "v_mfma_f32_16x16x32_bf16 v[108:111], a[112:115], v[68:71], v[108:111]\n\t" \
    "s_waitcnt vmcnt(0)\n\t" \
    "v_mfma_f32_16x16x32_bf16 v[112:115], a[116:119], v[64:67], v[112:115]\n\t" \
    "v_mfma_f32_16x16x32_bf16 v[116:119], a[116:119], v[68:71], v[116:119]\n\t" \
    "s_mov_b32 s97, 0\n\t" \
    "oo_epi5_%=:\n\t" \
    "s_mul_i32 s69, s65, 73728\n\t" \
    "s_add_u32 s44, s42, s69\n\t" \
    "s_addc_u32 s45, s43, 0\n\t" \
    "s_nop 7\n\t" \
    "ds_write_b128 v14, v[72:75] offset:0\n\t" \
    "ds_write_b128 v14, v[76:79] offset:1024\n\t" \
    "ds_write_b128 v14, v[80:83] offset:2048\n\t" \
    "ds_write_b128 v14, v[84:87] offset:3072\n\t" \
    "ds_write_b128 v14, v[88:91] offset:4096\n\t" \
    "ds_write_b128 v14, v[92:95] offset:5120\n\t" \
    "ds_write_b128 v14, v[96:99] offset:6144\n\t" \
    "ds_write_b128 v14, v[100:103] offset:7168\n\t" \
    "ds_write_b128 v14, v[104:107] offset:8192\n\t" \
    "ds_write_b128 v14, v[108:111] offset:9216\n\t" \
    "ds_write_b128 v14, v[112:115] offset:10240\n\t" \
    "ds_write_b128 v14, v[116:119] offset:11264\n\t" \
    "s_waitcnt lgkmcnt(0)\n\t" \
    "s_barrier\n\t" \
    "ds_read_b128 v[120:123], v15 offset:0\n\t" \
    "ds_read_b128 v[124:127], v15 offset:12288\n\t" \
    "ds_read_b128 v[128:131], v15 offset:24576\n\t" \
    "ds_read_b128 v[132:135], v15 offset:36864\n\t" \
    "ds_read_b128 v[136:139], v15 offset:4096\n\t" \
    "ds_read_b128 v[140:143], v15 offset:16384\n\t" \
    "ds_read_b128 v[144:147], v15 offset:28672\n\t" \
    "ds_read_b128 v[148:151], v15 offset:40960\n\t" \
    "ds_read_b128 v[152:155], v15 offset:8192\n\t" \
    "ds_read_b128 v[156:159], v15 offset:20480\n\t" \
    "ds_read_b128 v[160:163], v15 offset:32768\n\t" \
    "ds_read_b128 v[164:167], v15 offset:45056\n\t" \
    "v_xor_b32 v14, 0x10000, v14\n\t" \
    "v_xor_b32 v15, 0x10000, v15\n\t" \
    "s_waitcnt lgkmcnt(10)\n\t" \
    "v_pk_add_f32 v[120:121], v[120:121], v[124:125]\n\t" \
    "v_pk_add_f32 v[122:123], v[122:123], v[126:127]\n\t" \
    "s_waitcnt lgkmcnt(8)\n\t" \
    "v_pk_add_f32 v[128:129], v[128:129], v[132:133]\n\t" \
    "v_pk_add_f32 v[130:131], v[130:131], v[134:135]\n\t" \
    "v_pk_add_f32 v[120:121], v[120:121], v[128:129]\n\t" \
    "v_pk_add_f32 v[122:123], v[122:123], v[130:131]\n\t" \
    "v_cvt_pk_bf16_f32 v120, v120, v121\n\t" \
    "v_cvt_pk_bf16_f32 v121, v122, v123\n\t" \
    "global_store_dwordx2 v16, v[120:121], s[44:45] offset:0\n\t" \
    "s_waitcnt lgkmcnt(6)\n\t" \
    "v_pk_add_f32 v[136:137], v[136:137], v[140:141]\n\t" \
    "v_pk_add_f32 v[138:139], v[138:139], v[142:143]\n\t" \
    "s_waitcnt lgkmcnt(4)\n\t" \
    "v_pk_add_f32 v[144:145], v[144:145], v[148:149]\n\t" \
    "v_pk_add_f32 v[146:147], v[146:147], v[150:151]\n\t" \
    "v_pk_add_f32 v[136:137], v[136:137], v[144:145]\n\t" \
    "v_pk_add_f32 v[138:139], v[138:139], v[146:147]\n\t" \
    "v_cvt_pk_bf16_f32 v136, v136, v137\n\t" \
    "v_cvt_pk_bf16_f32 v137, v138, v139\n\t" \
    "global_store_dwordx2 v16, v[136:137], s[44:45] offset:64\n\t" \
    "s_waitcnt lgkmcnt(2)\n\t" \
    "v_pk_add_f32 v[152:153], v[152:153], v[156:157]\n\t" \
    "v_pk_add_f32 v[154:155], v[154:155], v[158:159]\n\t" \
    "s_waitcnt lgkmcnt(0)\n\t" \
    "v_pk_add_f32 v[160:161], v[160:161], v[164:165]\n\t" \
    "v_pk_add_f32 v[162:163], v[162:163], v[166:167]\n\t" \
    "v_pk_add_f32 v[152:153], v[152:153], v[160:161]\n\t" \
    "v_pk_add_f32 v[154:155], v[154:155], v[162:163]\n\t" \
    "v_cvt_pk_bf16_f32 v152, v152, v153\n\t" \
    "v_cvt_pk_bf16_f32 v153, v154, v155\n\t" \
    "global_store_dwordx2 v16, v[152:153], s[44:45] offset:128\n\t" \
    "s_mov_b32 s65, s96\n\t" \
    "s_cmp_lg_u32 s97, 0\n\t" \
    "s_cbranch_scc1 oo_loop3_%=\n\t" \
    "s_cmp_lt_u32 s65, 58\n\t" \
    "s_cbranch_scc1 oo_poll1_%=\n\t" \
    "s_waitcnt vmcnt(0) lgkmcnt(0)\n\t" \
    "s_barrier\n\t" \
    "oo_last_%=:\n\t" \
    ""
#define ASM_CV \
    "v_mov_b32 v0, %0\n\t" \
    "v_mov_b32 v2, %2\n\t" \
    "v_subrev_u32 v1, 768, %1\n\t" \
    "v_mov_b32 v4, 0\n\t" \
    "v_mov_b32 v5, 0\n\t" \
    "v_mov_b32 v6, 0\n\t" \
    "v_mov_b32 v7, 0\n\t" \
    "v_mov_b32 v8, 0\n\t" \
    "v_mov_b32 v9, 0\n\t" \
    "v_mov_b32 v10, 0\n\t" \
    "v_mov_b32 v11, 0\n\t" \
    "v_mov_b32 v12, 0\n\t" \
    "v_mov_b32 v13, 0\n\t" \
    "v_mov_b32 v14, 0\n\t" \
    "v_mov_b32 v15, 0\n\t" \
    "v_mov_b32 v16, 0\n\t" \
    "v_mov_b32 v17, 0\n\t" \
    "v_mov_b32 v18, 0\n\t" \
    "v_mov_b32 v19, 0\n\t" \
    "v_mov_b32 v20, 0\n\t" \
    "v_mov_b32 v21, 0\n\t" \
    "v_mov_b32 v22, 0\n\t" \
    "v_mov_b32 v23, 0\n\t" \
    "v_mov_b32 v24, 0\n\t" \
    "v_mov_b32 v25, 0\n\t" \
    "v_mov_b32 v26, 0\n\t" \
    "v_mov_b32 v27, 0\n\t" \
    "v_mov_b32 v28, 0\n\t" \
    "v_mov_b32 v29, 0\n\t" \
    "v_mov_b32 v30, 0\n\t" \
    "v_mov_b32 v31, 0\n\t" \
    "v_mov_b32 v32, 0\n\t" \
    "v_mov_b32 v33, 0\n\t" \
    "v_mov_b32 v34, 0\n\t" \
    "v_mov_b32 v35, 0\n\t" \
    "s_waitcnt lgkmcnt(0)\n\t" \
    "ds_read_b64 v[36:37], v0 offset:0\n\t" \
    "ds_read_b64 v[38:39], v0 offset:8\n\t" \
    "ds_read_b128 v[44:47], v1 offset:768\n\t" \
    "ds_read_b64 v[40:41], v0 offset:32\n\t" \
    "ds_read_b64 v[42:43], v0 offset:40\n\t" \
    "ds_read_b128 v[48:51], v1 offset:2304\n\t" \
    "ds_read_b128 v[52:55], v1 offset:3840\n\t" \
    "ds_read_b128 v[56:59], v1 offset:5376\n\t" \
    "s_mov_b32 s69, 4\n\t" \
    "cv_c1_%=:\n\t" \
    "s_waitcnt lgkmcnt(5)\n\t" \
    "v_mfma_f32_16x16x32_f16 v[4:7], v[36:39], v[44:47], v[4:7]\n\t" \
    "ds_read_b64 v[60:61], v0 offset:64\n\t" \
    "s_waitcnt lgkmcnt(4)\n\t" \
    "v_mfma_f32_16x16x32_f16 v[20:23], v[40:43], v[44:47], v[20:23]\n\t" \
    "ds_read_b64 v[62:63], v0 offset:72\n\t" \
    "s_waitcnt lgkmcnt(4)\n\t" \
    "v_mfma_f32_16x16x32_f16 v[8:11], v[36:39], v[48:51], v[8:11]\n\t" \
    "ds_read_b128 v[68:71], v1 offset:672\n\t" \
    "v_mfma_f32_16x16x32_f16 v[24:27], v[40:43], v[48:51], v[24:27]\n\t" \
    "ds_read_b64 v[64:65], v0 offset:96\n\t" \
    "s_waitcnt lgkmcnt(5)\n\t" \
    "v_mfma_f32_16x16x32_f16 v[12:15], v[36:39], v[52:55], v[12:15]\n\t" \
    "ds_read_b64 v[66:67], v0 offset:104\n\t" \
    "v_mfma_f32_16x16x32_f16 v[28:31], v[40:43], v[52:55], v[28:31]\n\t" \
    "ds_read_b128 v[72:75], v1 offset:2208\n\t" \
    "s_waitcnt lgkmcnt(6)\n\t" \
    "v_mfma_f32_16x16x32_f16 v[16:19], v[36:39], v[56:59], v[16:19]\n\t" \
    "ds_read_b128 v[76:79], v1 offset:3744\n\t" \
    "v_mfma_f32_16x16x32_f16 v[32:35], v[40:43], v[56:59], v[32:35]\n\t" \
    "ds_read_b128 v[80:83], v1 offset:5280\n\t" \
    "s_waitcnt lgkmcnt(5)\n\t" \
    "v_mfma_f32_16x16x32_f16 v[4:7], v[60:63], v[68:71], v[4:7]\n\t" \
    "ds_read_b64 v[84:85], v0 offset:128\n\t" \
    "s_waitcnt lgkmcnt(4)\n\t" \
    "v_mfma_f32_16x16x32_f16 v[20:23], v[64:67], v[68:71], v[20:23]\n\t" \
    "ds_read_b64 v[86:87], v0 offset:136\n\t" \
    "s_waitcnt lgkmcnt(4)\n\t" \
    "v_mfma_f32_16x16x32_f16 v[8:11], v[60:63], v[72:75], v[8:11]\n\t" \
    "ds_read_b128 v[92:95], v1 offset:576\n\t" \
    "v_mfma_f32_16x16x32_f16 v[24:27], v[64:67], v[72:75], v[24:27]\n\t" \
    "ds_read_b64 v[88:89], v0 offset:160\n\t" \
    "s_waitcnt lgkmcnt(5)\n\t" \
    "v_mfma_f32_16x16x32_f16 v[12:15], v[60:63], v[76:79], v[12:15]\n\t" \
    "ds_read_b64 v[90:91], v0 offset:168\n\t" \
    "v_mfma_f32_16x16x32_f16 v[28:31], v[64:67], v[76:79], v[28:31]\n\t" \
    "ds_read_b128 v[96:99], v1 offset:2112\n\t" \
    "s_waitcnt lgkmcnt(6)\n\t" \
    "v_mfma_f32_16x16x32_f16 v[16:19], v[60:63], v[80:83], v[16:19]\n\t" \
    "ds_read_b128 v[100:103], v1 offset:3648\n\t" \
    "v_mfma_f32_16x16x32_f16 v[32:35], v[64:67], v[80:83], v[32:35]\n\t" \
    "ds_read_b128 v[104:107], v1 offset:5184\n\t" \
    "s_waitcnt lgkmcnt(5)\n\t" \
    "v_mfma_f32_16x16x32_f16 v[4:7], v[84:87], v[92:95], v[4:7]\n\t" \
    "ds_read_b64 v[108:109], v0 offset:192\n\t" \
    "s_waitcnt lgkmcnt(4)\n\t" \
    "v_mfma_f32_16x16x32_f16 v[20:23], v[88:91], v[92:95], v[20:23]\n\t" \
    "ds_read_b64 v[110:111], v0 offset:200\n\t" \
    "s_waitcnt lgkmcnt(4)\n\t" \
    "v_mfma_f32_16x16x32_f16 v[8:11], v[84:87], v[96:99], v[8:11]\n\t" \
    "ds_read_b128 v[116:119], v1 offset:480\n\t" \
    "v_mfma_f32_16x16x32_f16 v[24:27], v[88:91], v[96:99], v[24:27]\n\t" \
    "ds_read_b64 v[112:113], v0 offset:224\n\t" \
    "s_waitcnt lgkmcnt(5)\n\t" \
    "v_mfma_f32_16x16x32_f16 v[12:15], v[84:87], v[100:103], v[12:15]\n\t" \
    "ds_read_b64 v[114:115], v0 offset:232\n\t" \
    "v_mfma_f32_16x16x32_f16 v[28:31], v[88:91], v[100:103], v[28:31]\n\t" \
    "ds_read_b128 v[120:123], v1 offset:2016\n\t" \
    "s_waitcnt lgkmcnt(6)\n\t" \
    "v_mfma_f32_16x16x32_f16 v[16:19], v[84:87], v[104:107], v[16:19]\n\t" \
    "ds_read_b128 v[124:127], v1 offset:3552\n\t" \
    "v_mfma_f32_16x16x32_f16 v[32:35], v[88:91], v[104:107], v[32:35]\n\t" \
    "ds_read_b128 v[128:131], v1 offset:5088\n\t" \
    "s_waitcnt lgkmcnt(5)\n\t" \
    "v_mfma_f32_16x16x32_f16 v[4:7], v[108:111], v[116:119], v[4:7]\n\t" \
    "ds_read_b64 v[36:37], v0 offset:256\n\t" \
    "s_waitcnt lgkmcnt(4)\n\t" \
    "v_mfma_f32_16x16x32_f16 v[20:23], v[112:115], v[116:119], v[20:23]\n\t" \
    "ds_read_b64 v[38:39], v0 offset:264\n\t" \
    "s_waitcnt lgkmcnt(4)\n\t" \
    "v_mfma_f32_16x16x32_f16 v[8:11], v[108:111], v[120:123], v[8:11]\n\t" \
    "ds_read_b128 v[44:47], v1 offset:384\n\t" \
    "v_mfma_f32_16x16x32_f16 v[24:27], v[112:115], v[120:123], v[24:27]\n\t" \
    "ds_read_b64 v[40:41], v0 offset:288\n\t" \
    "s_waitcnt lgkmcnt(5)\n\t" \
    "v_mfma_f32_16x16x32_f16 v[12:15], v[108:111], v[124:127], v[12:15]\n\t" \
    "ds_read_b64 v[42:43], v0 offset:296\n\t" \
    "v_mfma_f32_16x16x32_f16 v[28:31], v[112:115], v[124:127], v[28:31]\n\t" \
    "ds_read_b128 v[48:51], v1 offset:1920\n\t" \
    "s_waitcnt lgkmcnt(6)\n\t" \
    "v_mfma_f32_16x16x32_f16 v[16:19], v[108:111], v[128:131], v[16:19]\n\t" \
    "ds_read_b128 v[52:55], v1 offset:3456\n\t" \
    "v_mfma_f32_16x16x32_f16 v[32:35], v[112:115], v[128:131], v[32:35]\n\t" \
    "ds_read_b128 v[56:59], v1 offset:4992\n\t" \
    "v_add_u32 v0, 256, v0\n\t" \
    "v_subrev_u32 v1, 384, v1\n\t" \
    "s_sub_u32 s69, s69, 1\n\t" \
    "s_cmp_lg_u32 s69, 0\n\t" \
    "s_cbranch_scc1 cv_c1_%=\n\t" \
    "s_waitcnt lgkmcnt(0)\n\t" \
    "ds_read_b64 v[36:37], v0 offset:0\n\t" \
    "ds_read_b64 v[38:39], v0 offset:8\n\t" \
    "ds_read_b128 v[48:51], v1 offset:2304\n\t" \
    "ds_read_b64 v[40:41], v0 offset:32\n\t" \
    "ds_read_b64 v[42:43], v0 offset:40\n\t" \
    "ds_read_b128 v[52:55], v1 offset:3840\n\t" \
    "ds_read_b128 v[56:59], v1 offset:5376\n\t" \
    "ds_read_b64 v[60:61], v0 offset:64\n\t" \
    "ds_read_b64 v[62:63], v0 offset:72\n\t" \
    "ds_read_b128 v[72:75], v1 offset:2208\n\t" \
    "ds_read_b64 v[64:65], v0 offset:96\n\t" \
    "ds_read_b64 v[66:67], v0 offset:104\n\t" \
    "ds_read_b128 v[76:79], v1 offset:3744\n\t" \
    "ds_read_b128 v[80:83], v1 offset:5280\n\t" \
    "s_mov_b32 s69, 4\n\t" \
    "cv_c2_%=:\n\t" \
    "s_waitcnt lgkmcnt(11)\n\t" \
    "v_mfma_f32_16x16x32_f16 v[8:11], v[36:39], v[48:51], v[8:11]\n\t" \
    "ds_read_b64 v[84:85], v0 offset:128\n\t" \
    "ds_read_b64 v[86:87], v0 offset:136\n\t" \
    "s_waitcnt lgkmcnt(11)\n\t" \
    "v_mfma_f32_16x16x32_f16 v[24:27], v[40:43], v[48:51], v[24:27]\n\t" \
    "ds_read_b128 v[96:99], v1 offset:2112\n\t" \
    "ds_read_b64 v[88:89], v0 offset:160\n\t" \
    "s_waitcnt lgkmcnt(12)\n\t" \
    "v_mfma_f32_16x16x32_f16 v[12:15], v[36:39], v[52:55], v[12:15]\n\t" \
    "ds_read_b64 v[90:91], v0 offset:168\n\t" \
    "ds_read_b128 v[100:103], v1 offset:3648\n\t" \
    "v_mfma_f32_16x16x32_f16 v[28:31], v[40:43], v[52:55], v[28:31]\n\t" \
    "ds_read_b128 v[104:107], v1 offset:5184\n\t" \
    "s_waitcnt lgkmcnt(14)\n\t" \
    "v_mfma_f32_16x16x32_f16 v[16:19], v[36:39], v[56:59], v[16:19]\n\t" \
    "v_mfma_f32_16x16x32_f16 v[32:35], v[40:43], v[56:59], v[32:35]\n\t" \
    "s_waitcnt lgkmcnt(11)\n\t" \
    "v_mfma_f32_16x16x32_f16 v[8:11], v[60:63], v[72:75], v[8:11]\n\t" \
    "ds_read_b64 v[108:109], v0 offset:192\n\t" \
    "ds_read_b64 v[110:111], v0 offset:200\n\t" \
    "s_waitcnt lgkmcnt(11)\n\t" \
    "v_mfma_f32_16x16x32_f16 v[24:27], v[64:67], v[72:75], v[24:27]\n\t" \
    "ds_read_b128 v[120:123], v1 offset:2016\n\t" \
    "ds_read_b64 v[112:113], v0 offset:224\n\t" \
    "s_waitcnt lgkmcnt(12)\n\t" \
    "v_mfma_f32_16x16x32_f16 v[12:15], v[60:63], v[76:79], v[12:15]\n\t" \
    "ds_read_b64 v[114:115], v0 offset:232\n\t" \
    "ds_read_b128 v[124:127], v1 offset:3552\n\t" \
    "v_mfma_f32_16x16x32_f16 v[28:31], v[64:67], v[76:79], v[28:31]\n\t" \
    "ds_read_b128 v[128:131], v1 offset:5088\n\t" \
    "s_waitcnt lgkmcnt(14)\n\t" \
    "v_mfma_f32_16x16x32_f16 v[16:19], v[60:63], v[80:83], v[16:19]\n\t" \
    "v_mfma_f32_16x16x32_f16 v[32:35], v[64:67], v[80:83], v[32:35]\n\t" \
    "s_waitcnt lgkmcnt(11)\n\t" \
    "v_mfma_f32_16x16x32_f16 v[8:11], v[84:87], v[96:99], v[8:11]\n\t" \
    "ds_read_b64 v[36:37], v0 offset:256\n\t" \
    "ds_read_b64 v[38:39], v0 offset:264\n\t" \
    "s_waitcnt lgkmcnt(11)\n\t" \
    "v_mfma_f32_16x16x32_f16 v[24:27], v[88:91], v[96:99], v[24:27]\n\t" \
    "ds_read_b128 v[48:51], v1 offset:1920\n\t" \
    "ds_read_b64 v[40:41], v0 offset:288\n\t" \
    "s_waitcnt lgkmcnt(12)\n\t" \
    "v_mfma_f32_16x16x32_f16 v[12:15], v[84:87], v[100:103], v[12:15]\n\t" \
    "ds_read_b64 v[42:43], v0 offset:296\n\t" \
    "ds_read_b128 v[52:55], v1 offset:3456\n\t" \
    "v_mfma_f32_16x16x32_f16 v[28:31], v[88:91], v[100:103], v[28:31]\n\t" \
    "ds_read_b128 v[56:59], v1 offset:4992\n\t" \
    "s_waitcnt lgkmcnt(14)\n\t" \
    "v_mfma_f32_16x16x32_f16 v[16:19], v[84:87], v[104:107], v[16:19]\n\t" \
    "v_mfma_f32_16x16x32_f16 v[32:35], v[88:91], v[104:107], v[32:35]\n\t" \
    "s_waitcnt lgkmcnt(11)\n\t" \
    "v_mfma_f32_16x16x32_f16 v[8:11], v[108:111], v[120:123], v[8:11]\n\t" \
    "ds_read_b64 v[60:61], v0 offset:320\n\t" \
    "ds_read_b64 v[62:63], v0 offset:328\n\t" \
    "s_waitcnt lgkmcnt(11)\n\t" \
    "v_mfma_f32_16x16x32_f16 v[24:27], v[112:115], v[120:123], v[24:27]\n\t" \
    "ds_read_b128 v[72:75], v1 offset:1824\n\t" \
    "ds_read_b64 v[64:65], v0 offset:352\n\t" \
    "s_waitcnt lgkmcnt(12)\n\t" \
    "v_mfma_f32_16x16x32_f16 v[12:15], v[108:111], v[124:127], v[12:15]\n\t" \
    "ds_read_b64 v[66:67], v0 offset:360\n\t" \
    "ds_read_b128 v[76:79], v1 offset:3360\n\t" \
    "v_mfma_f32_16x16x32_f16 v[28:31], v[112:115], v[124:127], v[28:31]\n\t" \
    "ds_read_b128 v[80:83], v1 offset:4896\n\t" \
    "s_waitcnt lgkmcnt(14)\n\t" \
    "v_mfma_f32_16x16x32_f16 v[16:19], v[108:111], v[128:131], v[16:19]\n\t" \
    "v_mfma_f32_16x16x32_f16 v[32:35], v[112:115], v[128:131], v[32:35]\n\t" \
    "v_add_u32 v0, 256, v0\n\t" \
    "v_subrev_u32 v1, 384, v1\n\t" \
    "s_sub_u32 s69, s69, 1\n\t" \
    "s_cmp_lg_u32 s69, 0\n\t" \
    "s_cbranch_scc1 cv_c2_%=\n\t" \
    "s_waitcnt lgkmcnt(0)\n\t" \
    "ds_read_b64 v[36:37], v0 offset:0\n\t" \
    "ds_read_b64 v[38:39], v0 offset:8\n\t" \
    "ds_read_b128 v[52:55], v1 offset:3840\n\t" \
    "ds_read_b64 v[40:41], v0 offset:32\n\t" \
    "ds_read_b64 v[42:43], v0 offset:40\n\t" \
    "ds_read_b128 v[56:59], v1 offset:5376\n\t" \
    "ds_read_b64 v[60:61], v0 offset:64\n\t" \
    "ds_read_b64 v[62:63], v0 offset:72\n\t" \
    "ds_read_b128 v[76:79], v1 offset:3744\n\t" \
    "ds_read_b64 v[64:65], v0 offset:96\n\t" \
    "ds_read_b64 v[66:67], v0 offset:104\n\t" \
    "ds_read_b128 v[80:83], v1 offset:5280\n\t" \
    "s_mov_b32 s69, 4\n\t" \
    "cv_c3_%=:\n\t" \
    "s_waitcnt lgkmcnt(9)\n\t" \
    "v_mfma_f32_16x16x32_f16 v[12:15], v[36:39], v[52:55], v[12:15]\n\t" \
    "ds_read_b64 v[84:85], v0 offset:128\n\t" \
    "ds_read_b64 v[86:87], v0 offset:136\n\t" \
    "s_waitcnt lgkmcnt(9)\n\t" \
    "v_mfma_f32_16x16x32_f16 v[28:31], v[40:43], v[52:55], v[28:31]\n\t" \
    "ds_read_b128 v[100:103], v1 offset:3648\n\t" \
    "ds_read_b64 v[88:89], v0 offset:160\n\t" \
    "s_waitcnt lgkmcnt(10)\n\t" \
    "v_mfma_f32_16x16x32_f16 v[16:19], v[36:39], v[56:59], v[16:19]\n\t" \
    "ds_read_b64 v[90:91], v0 offset:168\n\t" \
    "ds_read_b128 v[104:107], v1 offset:5184\n\t" \
    "v_mfma_f32_16x16x32_f16 v[32:35], v[40:43], v[56:59], v[32:35]\n\t" \
    "s_waitcnt lgkmcnt(9)\n\t" \
    "v_mfma_f32_16x16x32_f16 v[12:15], v[60:63], v[76:79], v[12:15]\n\t" \
    "ds_read_b64 v[108:109], v0 offset:192\n\t" \
    "ds_read_b64 v[110:111], v0 offset:200\n\t" \
    "s_waitcnt lgkmcnt(9)\n\t" \
    "v_mfma_f32_16x16x32_f16 v[28:31], v[64:67], v[76:79], v[28:31]\n\t" \
    "ds_read_b128 v[124:127], v1 offset:3552\n\t" \
    "ds_read_b64 v[112:113], v0 offset:224\n\t" \
    "s_waitcnt lgkmcnt(10)\n\t" \
    "v_mfma_f32_16x16x32_f16 v[16:19], v[60:63], v[80:83], v[16:19]\n\t" \
    "ds_read_b64 v[114:115], v0 offset:232\n\t" \
    "ds_read_b128 v[128:131], v1 offset:5088\n\t" \
    "v_mfma_f32_16x16x32_f16 v[32:35], v[64:67], v[80:83], v[32:35]\n\t" \
    "s_waitcnt lgkmcnt(9)\n\t" \
    "v_mfma_f32_16x16x32_f16 v[12:15], v[84:87], v[100:103], v[12:15]\n\t" \
    "ds_read_b64 v[36:37], v0 offset:256\n\t" \
    "ds_read_b64 v[38:39], v0 offset:264\n\t" \
    "s_waitcnt lgkmcnt(9)\n\t" \
    "v_mfma_f32_16x16x32_f16 v[28:31], v[88:91], v[100:103], v[28:31]\n\t" \
    "ds_read_b128 v[52:55], v1 offset:3456\n\t" \
    "ds_read_b64 v[40:41], v0 offset:288\n\t" \
    "s_waitcnt lgkmcnt(10)\n\t" \
    "v_mfma_f32_16x16x32_f16 v[16:19], v[84:87], v[104:107], v[16:19]\n\t" \
    "ds_read_b64 v[42:43], v0 offset:296\n\t" \
    "ds_read_b128 v[56:59], v1 offset:4992\n\t" \
    "v_mfma_f32_16x16x32_f16 v[32:35], v[88:91], v[104:107], v[32:35]\n\t" \
    "s_waitcnt lgkmcnt(9)\n\t" \
    "v_mfma_f32_16x16x32_f16 v[12:15], v[108:111], v[124:127], v[12:15]\n\t" \
    "ds_read_b64 v[60:61], v0 offset:320\n\t" \
    "ds_read_b64 v[62:63], v0 offset:328\n\t" \
    "s_waitcnt lgkmcnt(9)\n\t" \
    "v_mfma_f32_16x16x32_f16 v[28:31], v[112:115], v[124:127], v[28:31]\n\t" \
    "ds_read_b128 v[76:79], v1 offset:3360\n\t" \
    "ds_read_b64 v[64:65], v0 offset:352\n\t" \
    "s_waitcnt lgkmcnt(10)\n\t" \
    "v_mfma_f32_16x16x32_f16 v[16:19], v[108:111], v[128:131], v[16:19]\n\t" \
    "ds_read_b64 v[66:67], v0 offset:360\n\t" \
    "ds_read_b128 v[80:83], v1 offset:4896\n\t" \
    "v_mfma_f32_16x16x32_f16 v[32:35], v[112:115], v[128:131], v[32:35]\n\t" \
    "v_add_u32 v0, 256, v0\n\t" \
    "v_subrev_u32 v1, 384, v1\n\t" \
    "s_sub_u32 s69, s69, 1\n\t" \
    "s_cmp_lg_u32 s69, 0\n\t" \
    "s_cbranch_scc1 cv_c3_%=\n\t" \
    "s_waitcnt lgkmcnt(0)\n\t" \
    "ds_read_b64 v[36:37], v0 offset:0\n\t" \
    "ds_read_b64 v[38:39], v0 offset:8\n\t" \
    "ds_read_b128 v[56:59], v1 offset:5376\n\t" \
    "ds_read_b64 v[40:41], v0 offset:32\n\t" \
    "ds_read_b64 v[42:43], v0 offset:40\n\t" \
    "ds_read_b64 v[60:61], v0 offset:64\n\t" \
    "ds_read_b64 v[62:63], v0 offset:72\n\t" \
    "ds_read_b128 v[80:83], v1 offset:5280\n\t" \
    "ds_read_b64 v[64:65], v0 offset:96\n\t" \
    "ds_read_b64 v[66:67], v0 offset:104\n\t" \
    "ds_read_b64 v[84:85], v0 offset:128\n\t" \
    "ds_read_b64 v[86:87], v0 offset:136\n\t" \
    "ds_read_b128 v[104:107], v1 offset:5184\n\t" \
    "ds_read_b64 v[88:89], v0 offset:160\n\t" \
    "ds_read_b64 v[90:91], v0 offset:168\n\t" \
    "s_mov_b32 s69, 4\n\t" \
    "cv_c4_%=:\n\t" \
    "s_waitcnt lgkmcnt(12)\n\t" \
    "v_mfma_f32_16x16x32_f16 v[16:19], v[36:39], v[56:59], v[16:19]\n\t" \
    "ds_read_b64 v[108:109], v0 offset:192\n\t" \
    "ds_read_b64 v[110:111], v0 offset:200\n\t" \
    "ds_read_b128 v[128:131], v1 offset:5088\n\t" \
    "s_waitcnt lgkmcnt(13)\n\t" \
    "v_mfma_f32_16x16x32_f16 v[32:35], v[40:43], v[56:59], v[32:35]\n\t" \
    "ds_read_b64 v[112:113], v0 offset:224\n\t" \
    "ds_read_b64 v[114:115], v0 offset:232\n\t" \
    "s_waitcnt lgkmcnt(12)\n\t" \
    "v_mfma_f32_16x16x32_f16 v[16:19], v[60:63], v[80:83], v[16:19]\n\t" \
    "ds_read_b64 v[36:37], v0 offset:256\n\t" \
    "ds_read_b64 v[38:39], v0 offset:264\n\t" \
    "ds_read_b128 v[56:59], v1 offset:4992\n\t" \
    "s_waitcnt lgkmcnt(13)\n\t" \
    "v_mfma_f32_16x16x32_f16 v[32:35], v[64:67], v[80:83], v[32:35]\n\t" \
    "ds_read_b64 v[40:41], v0 offset:288\n\t" \
    "ds_read_b64 v[42:43], v0 offset:296\n\t" \
    "s_waitcnt lgkmcnt(12)\n\t" \
    "v_mfma_f32_16x16x32_f16 v[16:19], v[84:87], v[104:107], v[16:19]\n\t" \
    "ds_read_b64 v[60:61], v0 offset:320\n\t" \
    "ds_read_b64 v[62:63], v0 offset:328\n\t" \
    "ds_read_b128 v[80:83], v1 offset:4896\n\t" \
    "s_waitcnt lgkmcnt(13)\n\t" \
    "v_mfma_f32_16x16x32_f16 v[32:35], v[88:91], v[104:107], v[32:35]\n\t" \
    "ds_read_b64 v[64:65], v0 offset:352\n\t" \
    "ds_read_b64 v[66:67], v0 offset:360\n\t" \
    "s_waitcnt lgkmcnt(12)\n\t" \
    "v_mfma_f32_16x16x32_f16 v[16:19], v[108:111], v[128:131], v[16:19]\n\t" \
    "ds_read_b64 v[84:85], v0 offset:384\n\t" \
    "ds_read_b64 v[86:87], v0 offset:392\n\t" \
    "ds_read_b128 v[104:107], v1 offset:4800\n\t" \
    "s_waitcnt lgkmcnt(13)\n\t" \
    "v_mfma_f32_16x16x32_f16 v[32:35], v[112:115], v[128:131], v[32:35]\n\t" \
    "ds_read_b64 v[88:89], v0 offset:416\n\t" \
    "ds_read_b64 v[90:91], v0 offset:424\n\t" \
    "v_add_u32 v0, 256, v0\n\t" \
    "v_subrev_u32 v1, 384, v1\n\t" \
    "s_sub_u32 s69, s69, 1\n\t" \
    "s_cmp_lg_u32 s69, 0\n\t" \
    "s_cbranch_scc1 cv_c4_%=\n\t" \
    "s_waitcnt lgkmcnt(0)\n\t" \
    "s_nop 7\n\t" \
    "ds_write_b128 v2, v[4:7] offset:0\n\t" \
    "ds_write_b128 v2, v[8:11] offset:1024\n\t" \
    "ds_write_b128 v2, v[12:15] offset:2048\n\t" \
    "ds_write_b128 v2, v[16:19] offset:3072\n\t" \
    "ds_write_b128 v2, v[20:23] offset:4096\n\t" \
    "ds_write_b128 v2, v[24:27] offset:5120\n\t" \
    "ds_write_b128 v2, v[28:31] offset:6144\n\t" \
    "ds_write_b128 v2, v[32:35] offset:7168\n\t" \
    "s_waitcnt lgkmcnt(0)\n\t" \
    "cv_last_%=:\n\t" \
    ""
#define ASM_CLOB "v0", "v1", "v2", "v3", "v4", "v5", "v6", "v7", "v8", "v9", "v10", "v11", "v12", "v13", "v14", "v15", "v16", "v17", "v18", "v19", "v20", "v21", "v22", "v23", "v24", "v25", "v26", "v27", "v28", "v29", "v30", "v31", "v32", "v33", "v34", "v35", "v36", "v37", "v38", "v39", "v40", "v41", "v42", "v43", "v44", "v45", "v46", "v47", "v48", "v49", "v50", "v51", "v52", "v53", "v54", "v55", "v56", "v57", "v58", "v59", "v60", "v61", "v62", "v63", "v64", "v65", "v66", "v67", "v68", "v69", "v70", "v71", "v72", "v73", "v74", "v75", "v76", "v77", "v78", "v79", "v80", "v81", "v82", "v83", "v84", "v85", "v86", "v87", "v88", "v89", "v90", "v91", "v92", "v93", "v94", "v95", "v96", "v97", "v98", "v99", "v100", "v101", "v102", "v103", "v104", "v105", "v106", "v107", "v108", "v109", "v110", "v111", "v112", "v113", "v114", "v115", "v116", "v117", "v118", "v119", "v120", "v121", "v122", "v123", "v124", "v125", "v126", "v127", "v128", "v129", "v130", "v131", "v132", "v133", "v134", "v135", "v136", "v137", "v138", "v139", "v140", "v141", "v142", "v143", "v144", "v145", "v146", "v147", "v148", "v149", "v150", "v151", "v152", "v153", "v154", "v155", "v156", "v157", "v158", "v159", "v160", "v161", "v162", "v163", "v164", "v165", "v166", "v167", "v168", "v169", "v170", "v171", "v172", "v173", "v174", "v175", "v176", "v177", "v178", "v179", "v180", "v181", "v182", "v183", "v184", "v185", "v186", "v187", "v188", "v189", "v190", "v191", "v192", "v193", "v194", "v195", "v196", "v197", "v198", "v199", "v200", "v201", "v202", "v203", "v204", "v205", "v206", "v207", "v208", "v209", "v210", "v211", "v212", "v213", "v214", "v215", "v222", "v223", "v224", "v225", "v226", "v227", "v228", "v229", "v230", "v231", "v232", "v233", "v234", "v235", "v236", "v237", "v238", "v239", "v240", "v241", "v242", "v243", "v244", "v245", "v246", "v247", "v248", "v249", "v250", "v251", "v252", "v253", "v254", "v255", "a0", "a1", "a2", "a3", "a4", "a5", "a6", "a7", "a8", "a9", "a10", "a11", "a12", "a13", "a14", "a15", "a16", "a17", "a18", "a19", "a20", "a21", "a22", "a23", "a24", "a25", "a26", "a27", "a28", "a29", "a30", "a31", "a32", "a33", "a34", "a35", "a36", "a37", "a38", "a39", "a40", "a41", "a42", "a43", "a44", "a45", "a46", "a47", "a48", "a49", "a50", "a51", "a52", "a53", "a54", "a55", "a56", "a57", "a58", "a59", "a60", "a61", "a62", "a63", "a64", "a65", "a66", "a67", "a68", "a69", "a70", "a71", "a72", "a73", "a74", "a75", "a76", "a77", "a78", "a79", "a80", "a81", "a82", "a83", "a84", "a85", "a86", "a87", "a88", "a89", "a90", "a91", "a92", "a93", "a94", "a95", "a96", "a97", "a98", "a99", "a100", "a101", "a102", "a103", "a104", "a105", "a106", "a107", "a108", "a109", "a110", "a111", "a112", "a113", "a114", "a115", "a116", "a117", "a118", "a119", "a120", "a121", "a122", "a123", "a124", "a125", "a126", "a127", "a128", "a129", "a130", "a131", "a132", "a133", "a134", "a135", "a136", "a137", "a138", "a139", "a140", "a141", "a142", "a143", "a144", "a145", "a146", "a147", "a148", "a149", "a150", "a151", "a152", "a153", "a154", "a155", "a156", "a157", "a158", "a159", "a160", "a161", "a162", "a163", "a164", "a165", "a166", "a167", "a168", "a169", "a170", "a171", "a172", "a173", "a174", "a175", "a176", "a177", "a178", "a179", "a180", "a181", "a182", "a183", "a184", "a185", "a186", "a187", "a188", "a189", "a190", "a191", "a192", "a193", "a194", "a195", "a196", "a197", "a198", "a199", "a200", "a201", "a202", "a203", "a204", "a205", "a206", "a207", "a208", "a209", "a210", "a211", "a212", "a213", "a214", "a215", "a216", "a217", "a218", "a219", "a220", "a221", "a222", "a223", "a224", "a225", "a226", "a227", "a228", "a229", "a230", "a231", "a232", "a233", "a234", "a235", "a236", "a237", "a238", "a239", "a240", "a241", "a242", "a243", "a244", "a245", "a246", "a247", "a248", "a249", "a250", "a251", "a252", "a253", "a254", "a255", "s33", "s34", "s35", "s36", "s37", "s38", "s39", "s40", "s41", "s42", "s43", "s44", "s45", "s46", "s47", "s48", "s49", "s50", "s51", "s52", "s53", "s54", "s55", "s56", "s57", "s58", "s59", "s60", "s61", "s62", "s63", "s64", "s65", "s66", "s67", "s68", "s69", "s70", "s71", "s72", "s73", "s74", "s75", "s76", "s77", "s78", "s79", "s80", "s81", "s82", "s83", "s84", "s85", "s86", "s87", "s88", "s89", "s90", "s91", "s92", "s93", "s94", "s95", "s96", "s97", "s98", "s99", "vcc", "scc", "memory"
#define CV_CLOB "v0", "v1", "v2", "v3", "v4", "v5", "v6", "v7", "v8", "v9", "v10", "v11", "v12", "v13", "v14", "v15", "v16", "v17", "v18", "v19", "v20", "v21", "v22", "v23", "v24", "v25", "v26", "v27", "v28", "v29", "v30", "v31", "v32", "v33", "v34", "v35", "v36", "v37", "v38", "v39", "v40", "v41", "v42", "v43", "v44", "v45", "v46", "v47", "v48", "v49", "v50", "v51", "v52", "v53", "v54", "v55", "v56", "v57", "v58", "v59", "v60", "v61", "v62", "v63", "v64", "v65", "v66", "v67", "v68", "v69", "v70", "v71", "v72", "v73", "v74", "v75", "v76", "v77", "v78", "v79", "v80", "v81", "v82", "v83", "v84", "v85", "v86", "v87", "v88", "v89", "v90", "v91", "v92", "v93", "v94", "v95", "v96", "v97", "v98", "v99", "v100", "v101", "v102", "v103", "v104", "v105", "v106", "v107", "v108", "v109", "v110", "v111", "v112", "v113", "v114", "v115", "v116", "v117", "v118", "v119", "v120", "v121", "v122", "v123", "v124", "v125", "v126", "v127", "v128", "v129", "v130", "v131", "s69", "scc", "memory"
static constexpr unsigned G_STEP = 0x80000;
static constexpr unsigned G_HOFF = 0x40000;
static constexpr unsigned G_RM = 0x4;
static constexpr unsigned G_RP = 0x2;
static constexpr unsigned G_HP = 0x20;
static constexpr unsigned G_HKB = 0x8;
static constexpr unsigned G_RHO = 0x1400;
static constexpr unsigned G_RHS = 0x600;
static constexpr unsigned G_RARM = 0x1;
static constexpr unsigned G_NARM = 0x4;
static constexpr unsigned G_WS_UM = 0x400000;
static constexpr unsigned G_WS_CTL = 0x5d0000;
static constexpr unsigned G_WS_DONE = 0x5f8000;
static constexpr unsigned G_WS_EP = 0x5fa000;
static constexpr unsigned G_WS_STAMPS = 0x10000000;
static constexpr unsigned G_C_TASK = 0x200;
static constexpr unsigned G_C_GX = 0x1000;
static constexpr unsigned G_C_GV = 0x4000;
static constexpr unsigned G_C_KM = 0x8000;
static constexpr unsigned G_C_UM = 0x9000;
static constexpr unsigned G_C_PC = 0xc000;
static constexpr unsigned G_C_UT = 0xd000;
static constexpr unsigned G_C_GK = 0x10000;
static constexpr unsigned G_C_YC = 0x11000;
static constexpr unsigned G_C_ERR = 0x100;
static constexpr unsigned G_C_ARR = 0x14000;
static constexpr unsigned G_C_KG = 0x1c000;
static constexpr unsigned G_C_END = 0x22000;
static constexpr unsigned G_CTF = 0x2000;
static constexpr unsigned G_GVC = 0x2;
static constexpr unsigned G_GVH = 0x14000;
static constexpr unsigned G_HS8 = 0x80000;
static constexpr unsigned G_HS4 = 0x40000;
static constexpr unsigned G_UXS = 0x200000;
static constexpr unsigned G_UMH = 0xa0000;
static constexpr unsigned G_D_QX = 0x600000;
static constexpr unsigned G_D_KX = 0x1600000;
static constexpr unsigned G_D_KM = 0x2600000;
static constexpr unsigned G_D_UX = 0x2e00000;
static constexpr unsigned G_D_UMX = 0x7600000;
static constexpr unsigned G_D_PX = 0x8c00000;
static constexpr unsigned G_D_PM = 0x9000000;
static constexpr unsigned G_D_KT = 0x9100000;
static constexpr unsigned G_D_UT = 0x9600000;
static constexpr unsigned G_D_YB = 0x9900000;
static constexpr unsigned G_D_END = 0x9c00000;
static constexpr unsigned G_SKM = 0x7;
static constexpr unsigned G_GVN = 0x2;
static constexpr unsigned G_STG = 0x9000;
static constexpr unsigned G_QKK = 0x1;
static constexpr unsigned G_NA_ = 0x22;
static constexpr unsigned G_KROW = 0x30;
#define FLG(x_) (kDefault[5])
#define FLG2(x_) (kDefault[7])
static constexpr int kDefault[8] = {PRM_DEFAULT};
#define MFMA32(a_, b_, c_) __builtin_amdgcn_mfma_f32_16x16x32_bf16(__builtin_bit_cast(bf16x8, a_), __builtin_bit_cast(bf16x8, b_), c_, 0, 0, 0)
#define MFMA16(a_, b_, c_) __builtin_amdgcn_mfma_f32_16x16x16bf16_1k(__builtin_bit_cast(s16x4, a_), __builtin_bit_cast(s16x4, b_), c_, 0, 0, 0)
#define WLD8(d_, voff_, base_, off_) asm volatile("s_nop 4\n\tglobal_load_dwordx2 %0, %1, %2 offset:%3 sc1" : "=v"(d_) : "v"(voff_), "s"(base_), "i"(off_) : "memory")
#define WLD16(d_, voff_, base_, off_) asm volatile("s_nop 4\n\tglobal_load_dwordx4 %0, %1, %2 offset:%3 sc1" : "=v"(d_) : "v"(voff_), "s"(base_), "i"(off_) : "memory")
#define WST16(voff_, d_, base_, off_) asm volatile("s_nop 4\n\tglobal_store_dwordx4 %0, %1, %2 offset:%3 sc1\n\ts_nop 1" :: "v"(voff_), "v"(d_), "s"(base_), "i"(off_) : "memory")
#define WLD16X4(d0, d1, d2, d3, voff_, base_) asm volatile("s_nop 4\n\tglobal_load_dwordx4 %0, %4, %5 sc1\n\tglobal_load_dwordx4 %1, %4, %5 offset:1024 sc1\n\t" \
    "global_load_dwordx4 %2, %4, %5 offset:2048 sc1\n\tglobal_load_dwordx4 %3, %4, %5 offset:3072 sc1" \
    : "=&v"(d0), "=&v"(d1), "=&v"(d2), "=&v"(d3) : "v"(voff_), "s"(base_) : "memory")

__device__ __forceinline__ u32x4 pack8(const f32x4& a, const f32x4& b) {
    return u32x4{pk2bf(a[0], a[1]), pk2bf(a[2], a[3]), pk2bf(b[0], b[1]), pk2bf(b[2], b[3])};
}
template <class T> __device__ __forceinline__ T* uni(T* q) {
    const u64 v = (u64)q;
    unsigned lo = __builtin_amdgcn_readfirstlane((unsigned)v), hi = __builtin_amdgcn_readfirstlane((unsigned)(v >> 32));
    asm volatile("s_nop 4" : "+s"(lo), "+s"(hi));
    return (T*)(((u64)hi << 32) | lo);
}
#define CTLO (G_WS_CTL + (unsigned)p.prm[0])
template <bool FAST = false>
__device__ __forceinline__ void wait_word(const MainP& p, const char* f, int want, unsigned code) {
    unsigned tries = 0;
    while (__any((int)__hip_atomic_load((const gu32*)f, RLX_AGENT) < want)) {
        if (++tries > kWaitCap) { __hip_atomic_store((gu32*)(p.ws + G_WS_CTL + G_C_ERR), code, RLX_AGENT); break; }
        if ((tries & 1023u) == 0u && __hip_atomic_load((const gu32*)(p.ws + G_WS_CTL + G_C_ERR), RLX_AGENT) != 0xffffffffu) break;
        if (FAST) __builtin_amdgcn_s_sleep(1); else __builtin_amdgcn_s_sleep(10);
    }
}

__device__ __forceinline__ int skew(int s, int id) { return (s ^ (G_SKM * id)) & 63; }
__device__ __forceinline__ const char* qx_ptr(const MainP& p, int s, int h) {
    return p.ws + G_D_QX + (size_t)h * G_HS8 + skew(s, h) * 8192;
}
__device__ __forceinline__ unsigned umx_base(int h) { return G_D_UMX + (h / 3) * 0x200000 + (h % 3) * G_UMH; }
typedef GAS unsigned long long gu64;
__device__ __forceinline__ void init_done(char* ws, int slot, unsigned val) {
    const unsigned e = __hip_atomic_load((const gu32*)(ws + G_WS_EP), RLX_AGENT);
    __hip_atomic_store((gu64*)(ws + G_WS_DONE + slot * 64), ((u64)val << 32) | (e + 1u), RLX_AGENT);
}
__device__ __forceinline__ void init_seen(char* ws, int lane, unsigned e) {
    for (unsigned tries = 0; tries < kWaitCap; ++tries) {
        if (!__any(__hip_atomic_load((const gu32*)(ws + G_WS_DONE + min(lane, 59) * 64), RLX_AGENT) != e + 1u)) break;
        __builtin_amdgcn_s_sleep(8);
    }
}
__device__ __forceinline__ void init_count(char* ws, int lane) { if (lane == 0) __hip_atomic_fetch_add((gu32*)(ws + G_WS_CTL + G_C_ARR), 1u, RLX_AGENT); }
__device__ __forceinline__ void fill_block(char* ws, int b, int tid, const u32x4& v0, const u32x4& v1) {
    const unsigned o = b * 8192 + tid * 16, o2 = o + 4096;
    asm volatile("global_store_dwordx4 %0, %1, %2 sc1\n\ts_nop 1" :: "v"(o), "v"(v0), "s"(ws) : "memory");
    asm volatile("global_store_dwordx4 %0, %1, %2 sc1\n\ts_nop 1" :: "v"(o2), "v"(v1), "s"(ws) : "memory");
}
__device__ __forceinline__ void init_fill(char* ws, const u16* mem0, int f, int tid) {
    const u32x4 sent = {0xffffffffu, 0xffffffffu, 0xffffffffu, 0xffffffffu};
    u32x4 v0 = sent, v1 = sent;
    if (f < 16) {
#pragma unroll
        for (int k = 0; k < 2; ++k) {
            const int q = tid + 256 * k + ((f & 2) ? 256 : 0), m = q & 15;
            if (!(f & 2) || (q >= 512 && q < 576)) {
                const unsigned* src = (const unsigned*)(mem0 + (2 * (m >> 2) + (m & 1)) * DM + 16 * (q >> 4) + (m & 2));
                const u32x4 o = {min(src[0], 0xfffffffeu), min(src[2], 0xfffffffeu), min(src[4], 0xfffffffeu), min(src[6], 0xfffffffeu)};
                if (k) v1 = o; else v0 = o;
            }
        }
    }
    static_assert(G_HKB == 8 && G_RP == 2 && G_RM == 4, "layout");
    constexpr int NH = 48, NP = NH + 16, NU = 144;
#pragma unroll 1
    for (int q = f; q < NP + NU + (int)(G_C_END >> 13); q += 48) {
        const int u = q - 32, y = q - NH;
        fill_block(ws, q < 32 ? (q >> 4) * 256 + ((q >> 3) & 1) * 16 + ((q >> 2) & 1) * 2 + 8 + (q & 2) * 2 + (q & 1) : q < NH ? (u >> 3) * 256 + (u & 7) : q < NP ? (y >> 3) * 256 + 32 + (y & 7) : q < NP + NU ? q - NP + 512 : q - NP - NU + (int)(G_WS_CTL >> 13), tid, v0, v1);
        v0 = sent; v1 = sent;
    }
    const unsigned e = __hip_atomic_load((const gu32*)(ws + G_WS_EP), RLX_AGENT);
    asm volatile("s_waitcnt vmcnt(0)" :: "v"(e) : "memory");
    __syncthreads();
    if (tid == 0) __hip_atomic_store((gu64*)(ws + G_WS_DONE + (12 + f) * 64), (u64)(e + 1u), RLX_AGENT);
}
__device__ __forceinline__ void init_cal(char* ws, char* smem, int id, int tid, int n) {
    const int sl = id % 6, w = tid >> 6;
    char* a = ws + (size_t)(sl >> 1) * 0x200000 + 0x1B0000 + (id / 6) * 16384 + (sl & 1) * 8192 + w * 2048;
    volatile unsigned* lds = (volatile unsigned*)smem;
    const u32x4 z = {0u, 0u, 0u, 0u};
    asm volatile("global_store_dwordx4 %0, %1, off sc1\n\tglobal_store_dwordx4 %0, %1, off offset:1024 sc1\n\ts_waitcnt vmcnt(0)" :: "v"(a + (tid & 63) * 16), "v"(z) : "memory");
    __syncthreads();
    if ((tid & 63) == 0) {
        unsigned best = 0xfffffffeu;
#pragma unroll 1
        for (int it = 0; it < 3 * n; ++it) {
            const unsigned c0 = (unsigned)__builtin_amdgcn_s_memtime();
            const unsigned v = __hip_atomic_fetch_or((gu32*)(a + it * 128), 0u, RLX_AGENT);
            asm volatile("s_waitcnt vmcnt(0)" :: "v"(v) : "memory");
            best = min(best, (unsigned)__builtin_amdgcn_s_memtime() - c0);
            if (it + 1 >= 2 * n && best > 560u) break;
        }
        lds[w] = best;
    }
    __syncthreads();
    if (tid == 0) init_done(ws, id, min(min(lds[0], lds[1]), min(lds[2], lds[3])));
    __syncthreads();
}

#define TST(slot_, i_)
#define PIN8(a) asm volatile("" : "+v"(a[0]), "+v"(a[1]), "+v"(a[2]), "+v"(a[3]), "+v"(a[4]), "+v"(a[5]), "+v"(a[6]), "+v"(a[7]))
struct AOps { u32x4 q[4], k[8]; int fl; };
struct APtr { const char* q; const char* k; const char* m; unsigned voff, vkm, voffm, vfl; };
__device__ __forceinline__ void attn_batch(const MainP& p, AOps& o, const APtr& a) {
    const char* q2 = a.q + 4096; const char* k2 = a.k + 4096;
    WLD16(o.q[0], a.voff, a.q, 0); WLD16(o.q[1], a.voff, a.q, 2048); WLD16(o.q[2], a.voff, q2, 0); WLD16(o.q[3], a.voff, q2, 2048);
    WLD16X4(o.k[0], o.k[1], o.k[2], o.k[3], a.voff, a.k); WLD16X4(o.k[4], o.k[5], o.k[6], o.k[7], a.voff, k2);
    asm volatile("global_load_dword %0, %1, %2 sc1" : "=v"(o.fl) : "v"(a.vfl), "s"(p.ws) : "memory");
}
__device__ __forceinline__ void attn_pin(AOps& o) {
    asm volatile("" : "+v"(o.fl), "+v"(o.q[0]), "+v"(o.q[1]), "+v"(o.q[2]), "+v"(o.q[3])); PIN8(o.k);
}
static constexpr int NFINE = 6;
static constexpr int NG1 = 64 - NFINE;
__device__ __forceinline__ void score_round4(float* r, const f32x4& x) {
    asm volatile("v_cvt_pk_bf16_f32 %0, 0, %4\n\tv_cvt_pk_bf16_f32 %1, 0, %5\n\tv_cvt_pk_bf16_f32 %2, 0, %6\n\tv_cvt_pk_bf16_f32 %3, 0, %7\n\t"
                 "v_mul_f32 %0, 0x3db504f3, %0\n\tv_mul_f32 %1, 0x3db504f3, %1\n\tv_mul_f32 %2, 0x3db504f3, %2\n\tv_mul_f32 %3, 0x3db504f3, %3\n\t"
                 "v_cvt_pk_bf16_f32 %0, 0, %0\n\tv_cvt_pk_bf16_f32 %1, 0, %1\n\tv_cvt_pk_bf16_f32 %2, 0, %2\n\tv_cvt_pk_bf16_f32 %3, 0, %3"
                 : "=&v"(r[0]), "=&v"(r[1]), "=&v"(r[2]), "=&v"(r[3]) : "v"(x[0]), "v"(x[1]), "v"(x[2]), "v"(x[3]));
}
__device__ __forceinline__ float vmax3(float a, float b, float c) { float r; asm("v_max3_f32 %0, %1, %2, %3" : "=v"(r) : "v"(a), "v"(b), "v"(c)); return r; }
template <int N>
__device__ __forceinline__ void attn_soft(float (&q)[12], u32x2& Pm, u32x4& Px) {
    float m = vmax3(q[0], q[1], q[2]);
#pragma unroll
    for (int i = 3; i < N; i += 2) m = vmax3(m, q[i], q[i + 1 < N ? i + 1 : i]);
    { auto a = __builtin_amdgcn_permlane16_swap(__float_as_uint(m), __float_as_uint(m), false, false); m = vmax3(m, __uint_as_float(a[0]), __uint_as_float(a[1])); }
    { auto a = __builtin_amdgcn_permlane32_swap(__float_as_uint(m), __float_as_uint(m), false, false); m = vmax3(m, __uint_as_float(a[0]), __uint_as_float(a[1])); }
    const float ml = -m * 1.4426950408889634f;
#pragma unroll
    for (int i = 0; i < N; ++i) q[i] = __builtin_amdgcn_exp2f(fmaf(q[i], 1.4426950408889634f, ml));
    float sum = 0.5f * ((q[0] + q[1]) + (q[2] + q[3])) + ((q[4] + q[5]) + (q[6] + q[7]));
    if (N > 8) sum += (q[8] + q[9]) + (q[10] + q[11]);
    sum = rowsum4(sum);
    const float inv = __builtin_amdgcn_rcpf(sum);
    Pm = u32x2{pk2bf(q[0] * inv, q[1] * inv), pk2bf(q[2] * inv, q[3] * inv)};
    Px = u32x4{pk2bf(q[4] * inv, q[5] * inv), pk2bf(q[6] * inv, q[7] * inv), N > 8 ? pk2bf(q[8] * inv, q[9] * inv) : 0u, N > 8 ? pk2bf(q[10] * inv, q[11] * inv) : 0u};
}
static constexpr int NA = G_NA_;
#define NPRE_ 38
static constexpr int NPRE = NPRE_;
__device__ __forceinline__ int attn_next(int s) { return s < NA ? s + 1 : s + 2; }
__device__ __forceinline__ unsigned attn_flag(const MainP& p, int s, int hq) { return CTLO + G_C_PC + ((s > NA && (s & 1)) ? 1024 : 0) + hq * 128; }
struct BOps { u32x4 q[8], k[8], m[4]; int fl; };
#define WLD16X4A(d0, d1, d2, d3, voff_, base_) asm volatile("s_nop 4\n\tglobal_load_dwordx4 %0, %4, %5 sc1\n\tglobal_load_dwordx4 %1, %4, %5 offset:1024 sc1\n\t" \
    "global_load_dwordx4 %2, %4, %5 offset:2048 sc1\n\tglobal_load_dwordx4 %3, %4, %5 offset:3072 sc1" \
    : "=&a"(d0), "=&a"(d1), "=&a"(d2), "=&a"(d3) : "v"(voff_), "s"(base_) : "memory")
#define PIN8A(a) asm volatile("" : "+a"(a[0]), "+a"(a[1]), "+a"(a[2]), "+a"(a[3]), "+a"(a[4]), "+a"(a[5]), "+a"(a[6]), "+a"(a[7]))
#define MFMA_AV0(acc, a_, b_) asm volatile("v_mfma_f32_16x16x32_bf16 %0, %1, %2, 0" : "=&v"(acc) : "a"(a_), "v"(b_))
#define MFMA_AV(acc, a_, b_) asm volatile("v_mfma_f32_16x16x32_bf16 %0, %1, %2, %0" : "+v"(acc) : "a"(a_), "v"(b_))
__device__ __forceinline__ void attn_batch8(const MainP& p, BOps& o, const APtr& a) {
    const char* q2 = a.q + 4096; const char* k2 = a.k + 4096;
    WLD16X4(o.q[0], o.q[1], o.q[2], o.q[3], a.voff, a.q); WLD16X4(o.q[4], o.q[5], o.q[6], o.q[7], a.voff, q2);
    WLD16X4A(o.k[0], o.k[1], o.k[2], o.k[3], a.voff, a.k); WLD16X4A(o.k[4], o.k[5], o.k[6], o.k[7], a.voff, k2);
}
template <bool PRE>
__device__ __forceinline__ void attn_step(const MainP& p, BOps& o, BOps& n1, BOps& n2, APtr& a, int& kg, int& kk, int s, int prev, int h, int w, int lane) {
    const int g = lane >> 4, c = lane & 15;
    const float scale = 0.08838834764831845f;
    TST(s * 64 + h * 2, 0);
    const int s2 = attn_next(attn_next(s));
    const int sm = PRE ? attn_next(s) : s;
    if (kg <= s2 || kk < sm) {
        VMC0();
        wait_word<true>(p, p.ws + a.vfl, (lane & 1) ? sm : s2 + 1, 13u);
    }
    TST(s * 64 + h * 2, 1);
    a.q = qx_ptr(p, s2, h); a.k = a.q + (G_D_KX - G_D_QX); a.m = p.ws + G_D_KM + (size_t)h * G_HS4 + skew(sm, h) * 4096;
    if (PRE) {
        asm volatile("s_waitcnt vmcnt(20)\n\ts_barrier" : "+a"(o.m[0]), "+a"(o.m[1]), "+a"(o.m[2]), "+a"(o.m[3]), "+v"(o.fl) :: "memory");
        PIN8(o.q); PIN8A(o.k);
    }
    __builtin_amdgcn_sched_barrier(0);
    BOps& d = PRE ? n1 : o;
    u32x4 (&km)[4] = o.m;
    int& fl = o.fl;
    WLD16X4A(d.m[0], d.m[1], d.m[2], d.m[3], a.vkm, a.m);
    asm volatile("global_load_dword %0, %1, %2 sc1" : "=v"(d.fl) : "v"(a.vfl), "s"(p.ws) : "memory");
    const char* q2 = a.q + 4096; const char* k2 = a.k + 4096;
#define SPREAD(x_) do { if (PRE) { __builtin_amdgcn_sched_barrier(0); x_; __builtin_amdgcn_sched_barrier(0); } } while (0)
    if (!PRE) attn_batch8(p, n2, a);
    f32x4 y[5];
#pragma unroll
    for (int wc = 0; wc < 4; ++wc)
#pragma unroll
        for (int i = 0; i < 3; ++i) { if (wc == 0) MFMA_AV0(y[i], o.k[i >> 1], o.q[(i + 1) >> 1]); else MFMA_AV(y[i], o.k[wc * 2 + (i >> 1)], o.q[wc * 2 + ((i + 1) >> 1)]); }
    asm volatile("s_nop 7" : "+v"(y[0]), "+v"(y[1]), "+v"(y[2]));
    SPREAD(WLD16X4(n2.q[0], n2.q[1], n2.q[2], n2.q[3], a.voff, a.q));
    float q0[12], q1[12];
    score_round4(q0 + 4, y[0]); score_round4(q1 + 4, y[1]); score_round4(q1 + 8, y[2]);
#pragma unroll
    for (int i = 0; i < 4; ++i) {
        const bool vis = 4 * g + i <= c;
        q0[4 + i] = vis ? q0[4 + i] : -INFINITY; q1[8 + i] = vis ? q1[8 + i] : -INFINITY;
    }
    __builtin_amdgcn_sched_barrier(0);
    if (!PRE) {
        asm volatile("s_waitcnt vmcnt(16)\n\ts_barrier" : "+a"(km[0]), "+a"(km[1]), "+a"(km[2]), "+a"(km[3]), "+v"(fl) :: "memory");
        PIN8(n1.q); PIN8A(n1.k);
    }
    { const unsigned fo = attn_flag(p, s, h >> 2); const int fv = PRE ? prev - 1 : s - 1;
      asm volatile("s_cmp_lg_u32 %3, 0\n\ts_cbranch_scc1 1f\n\tglobal_store_dword %0, %1, %2 sc1\n1:" :: "v"(fo), "v"(fv), "s"(p.ws), "s"(w) : "scc", "memory"); }
#pragma unroll
    for (int wc = 0; wc < 4; ++wc)
#pragma unroll
        for (int i = 0; i < 2; ++i) { if (wc == 0) MFMA_AV0(y[3 + i], km[0], o.q[i]); else MFMA_AV(y[3 + i], km[wc], o.q[wc * 2 + i]); }
    asm volatile("s_nop 7" : "+v"(y[3]), "+v"(y[4]));
    kg = __builtin_amdgcn_readlane(fl, 0); kk = __builtin_amdgcn_readlane(fl, 1);
    SPREAD(WLD16X4(n2.q[4], n2.q[5], n2.q[6], n2.q[7], a.voff, q2));
    score_round4(q0, y[3]); score_round4(q1, y[4]);
    u32x2 Pm0, Pm1; u32x4 Px0, Px1;
    attn_soft<8>(q0, Pm0, Px0);
    SPREAD(WLD16X4A(n2.k[0], n2.k[1], n2.k[2], n2.k[3], a.voff, a.k));
    attn_soft<12>(q1, Pm1, Px1);
    SPREAD(WLD16X4A(n2.k[4], n2.k[5], n2.k[6], n2.k[7], a.voff, k2));
    char* pxb = p.ws + G_D_PX + (size_t)(s * 32 + h) * 2048;
    char* pmb = p.ws + G_D_PM + (size_t)s * 16384 + h * 256; char* pm2 = pmb + 8192;
    WST16(a.voff, Px0, pxb, 0); WST16(a.voff, Px1, pxb, 1024);
    asm volatile("global_store_dwordx2 %0, %1, %3 sc1\n\tglobal_store_dwordx2 %0, %2, %4 sc1" :: "v"(a.voffm), "v"(Pm0), "v"(Pm1), "s"(pmb), "s"(pm2) : "memory");
    if (!PRE) { const unsigned fo = attn_flag(p, s, h >> 2);
      asm volatile("s_cmp_lt_u32 %4, 46\n\ts_cbranch_scc1 1f\n\ts_waitcnt vmcnt(0)\n\ts_barrier\n\ts_cmp_lg_u32 %3, 0\n\ts_cbranch_scc1 1f\n\tglobal_store_dword %0, %1, %2 sc1\n1:"
                   :: "v"(fo), "v"(s), "s"(p.ws), "s"(w), "s"(s) : "scc", "memory"); }
    TST(s * 64 + h * 2, 2);
}
__device__ __forceinline__ void attn_role(const MainP& p, int s, int h, int w, int lane) {
    APtr a;
    a.q = qx_ptr(p, s, h); a.k = a.q + (G_D_KX - G_D_QX);
    a.voff = lane * 16; a.vkm = (lane & 55) * 16; a.voffm = (lane & 15) * 16 + ((lane >> 4) & 1) * 8;
    a.vfl = CTLO + ((lane & 1) ? G_C_KM : G_C_GX) + h * 128;
    BOps o0, o1, o2;
    wait_word(p, p.ws + a.vfl, (lane & 1) ? s : attn_next(s) + 1, 10u);
    attn_batch8(p, o0, a);
    a.q = qx_ptr(p, attn_next(s), h); a.k = a.q + (G_D_KX - G_D_QX); a.m = p.ws + G_D_KM + (size_t)h * G_HS4 + skew(s, h) * 4096;
    attn_batch8(p, o1, a);
    WLD16X4A(o0.m[0], o0.m[1], o0.m[2], o0.m[3], a.vkm, a.m);
    VMC0();
    o0.fl = -1;
    PIN8(o0.q); PIN8A(o0.k); PIN8(o1.q); PIN8A(o1.k);
    int kg = -1, kk = -1, pv = s;
    static_assert(NA % 2 == 0 && NG1 + 3 < NSEG && NPRE + 6 < 46, "ranges");
#pragma unroll 1
    while (s < NPRE) {
        attn_step<true>(p, o0, o1, o2, a, kg, kk, s, pv, h, w, lane); pv = s; s = attn_next(s);
        attn_step<true>(p, o1, o2, o0, a, kg, kk, s, pv, h, w, lane); pv = s; s = attn_next(s);
        attn_step<true>(p, o2, o0, o1, a, kg, kk, s, pv, h, w, lane); pv = s; s = attn_next(s);
    }
    asm volatile("s_waitcnt vmcnt(0)" : "+a"(o0.m[0]), "+a"(o0.m[1]), "+a"(o0.m[2]), "+a"(o0.m[3]), "+v"(o0.fl) :: "memory");
    PIN8(o0.q); PIN8A(o0.k); PIN8(o1.q); PIN8A(o1.k);
#pragma unroll 1
    for (;;) {
        attn_step<false>(p, o0, o1, o2, a, kg, kk, s, 0, h, w, lane);
        if ((s = attn_next(s)) >= NG1) break;
        attn_step<false>(p, o1, o2, o0, a, kg, kk, s, 0, h, w, lane);
        if ((s = attn_next(s)) >= NG1) break;
        attn_step<false>(p, o2, o0, o1, a, kg, kk, s, 0, h, w, lane);
        if ((s = attn_next(s)) >= NG1) break;
    }
    VMC0();
}
__device__ __forceinline__ unsigned umax4(const u32x4& v) { return max(max(v[0], v[1]), max(v[2], v[3])); }
__device__ __forceinline__ bool give_up(const MainP& p, unsigned& tries, unsigned code) {
    if (++tries <= kWaitCap / 4) return false;
    __hip_atomic_store((gu32*)(p.ws + G_WS_CTL + G_C_ERR), code, RLX_AGENT);
    return true;
}
__device__ __forceinline__ void task_attn_tail(const MainP& p, int s, int h, int w, int lane) {
    const int g = lane >> 4, c = lane & 15, qt = w & 1;
    const float scale = 0.08838834764831845f;
    APtr a;
    a.q = qx_ptr(p, s, h) + qt * 1024; a.k = qx_ptr(p, s, h) + (G_D_KX - G_D_QX); a.m = p.ws + G_D_KM + (size_t)h * G_HS4 + skew(s, h) * 4096;
    a.voff = lane * 16; a.vkm = (lane & 55) * 16; a.voffm = c * 16 + (g & 1) * 8; a.vfl = G_WS_CTL;
    AOps o;
    wait_word(p, p.ws + CTLO + G_C_GX + h * 128, s + 1, 14u);
    attn_batch(p, o, a);
    VMC0();
    attn_pin(o);
    float q[12];
    u32x4 km[4];
#pragma unroll
    for (int kt = 1; kt >= -1; --kt) {
        if (kt < 0) {
            TST(s * 64 + h * 2 + qt, 0);
            wait_word(p, p.ws + CTLO + G_C_KM + h * 128, s - 2, 15u);
            if (FLG(p.prm[5]) & 2) wait_word<true>(p, p.ws + CTLO + G_C_KM + h * 128, s - 1, 15u);
            if (s < 63) for (int i = 0; i < (int)((FLG2(p.prm[7]) >> 3) & 31); ++i) __builtin_amdgcn_s_sleep(2);
            for (unsigned tries = 0;;) {
                WLD16X4(km[0], km[1], km[2], km[3], a.vkm, a.m);
                asm volatile("s_waitcnt vmcnt(0)" : "+v"(km[0]), "+v"(km[1]), "+v"(km[2]), "+v"(km[3]) :: "memory");
                if (!__any(max(max(umax4(km[0]), umax4(km[1])), max(umax4(km[2]), umax4(km[3]))) == 0xffffffffu) || give_up(p, tries, 16u)) break;
            }
            TST(s * 64 + h * 2 + qt, 1);
        }
        f32x4 y = {0.f, 0.f, 0.f, 0.f};
#pragma unroll
        for (int wc = 0; wc < 4; ++wc) y = MFMA32(kt < 0 ? km[wc] : o.k[wc * 2 + (kt < 0 ? 0 : kt)], o.q[wc], y);
#pragma unroll
        for (int i = 0; i < 4; ++i) q[4 + 4 * kt + i] = (16 * kt + 4 * g + i <= 16 * qt + c) ? bfr(bfr(y[i]) * scale) : -INFINITY;
    }
    u32x2 Pm; u32x4 Px;
    attn_soft<12>(q, Pm, Px);
    char* pxb = p.ws + G_D_PX + (size_t)(s * 32 + h) * 2048 + qt * 1024;
    char* pmb = p.ws + G_D_PM + (size_t)s * 16384 + h * 256 + qt * 8192;
    WST16(a.voff, Px, pxb, 0);
    asm volatile("global_store_dwordx2 %0, %1, %2 sc1" :: "v"(a.voffm), "v"(Pm), "s"(pmb) : "memory");
    TST(s * 64 + h * 2 + qt, 2);
}

#define RLD(d_, voff_, base_, off_) asm volatile("global_load_dwordx4 %0, %1, %2 offset:%3 sc1" : "=v"(d_) : "v"(voff_), "s"(base_), "i"(off_) : "memory")
#define RLDA(d_, voff_, base_, off_) asm volatile("global_load_dwordx4 %0, %1, %2 offset:%3 sc1" : "=a"(d_) : "v"(voff_), "s"(base_), "i"(off_) : "memory")
#define MFMA_ACC_A(acc, a_, b_) asm volatile("v_mfma_f32_16x16x32_bf16 %0, %1, %2, %0" : "+v"(acc) : "a"(a_), "v"(b_))
#define VMW(n_) asm volatile("s_waitcnt vmcnt(%0)" :: "i"(n_) : "memory")
template <int NT, int DEPTH, bool TAIL>
__device__ __forceinline__ void task_out(const MainP& p, char* smem, int s, int ct0, int w, int lane) {
    const int g = lane >> 4, c = lane & 15;
    const unsigned voff = lane * 16;
    f32x4 acc[NT][2];
#pragma unroll
    for (int t = 0; t < NT; ++t) { acc[t][0] = f32x4{0.f, 0.f, 0.f, 0.f}; acc[t][1] = acc[t][0]; asm volatile("" : "+v"(acc[t][0]), "+v"(acc[t][1])); }
    const char* ub[NT];
#pragma unroll
    for (int t = 0; t < NT; ++t) ub[t] = uni(p.ws + G_D_UX + (size_t)(ct0 + t) * G_UXS + skew(s, ct0 + t) * 32768 + w * 8192);
    const char* pb = uni(p.ws + G_D_PX + (size_t)(s * 32 + w * 8) * 2048);
    const char* um = uni(p.ws + s * 9216 + ct0 * 256);
    const unsigned vu0 = umx_base(w * 8 + g) + c * 16, vu1 = umx_base(w * 8 + 4 + g) + c * 16;
    const char* pm = uni(p.ws + G_D_PM + (size_t)s * 16384 + w * 2048);
    constexpr int NR = DEPTH + 1 > 10 ? 10 : DEPTH + 1;
    u32x4 a[NR][NT], b[10][2];
    unsigned vb = voff, va = voff;
    TST(4096 + (s * 36 + ct0) * 4 + w, 0);
    const int f0 = (int)__hip_atomic_load((const gu32*)(p.ws + CTLO + (lane < 8 ? G_C_UM + (w * 8 + lane) * 128 : lane < 16 ? G_C_GV + ((s & (G_GVN - 1)) * 32 + w * 8 + lane - 8) * 128
                                                                                                                                : attn_flag(p, s, w * 2 + (lane & 1)) - CTLO)), RLX_AGENT);
    const bool early = !TAIL && !__any(f0 < s);
    if (!early) wait_word(p, p.ws + CTLO + (lane < 8 ? G_C_GV + ((s & (G_GVN - 1)) * 32 + w * 8 + lane) * 128 : G_C_UM + w * 8 * 128), lane < 8 ? s : s - 3, 21u);
#define ALOAD(q) do { if ((q) < 8) { _Pragma("unroll") for (int t = 0; t < NT; ++t) RLDA(a[(q) % NR][t], va, ub[t], 0);  \
                                     asm volatile("v_add_u32 %0, 0x400, %0" : "+v"(va)); }                                            \
                      else { _Pragma("unroll") for (int t = 0; t < NT; ++t) RLDA(a[(q) % NR][t], ((q) & 1) ? vu1 : vu0, um, t * 256); } } while (0)
    constexpr int D0 = DEPTH < 8 ? DEPTH : 8;
    asm volatile("s_nop 4");
#pragma unroll
    for (int q = 0; q < D0; ++q) ALOAD(q);
    asm volatile("s_cmp_lg_u32 %0, 0\n\ts_cbranch_scc1 1f\n\ts_waitcnt vmcnt(0)\n1:\n\ts_waitcnt vmcnt(30)" :: "s"(__builtin_amdgcn_readfirstlane((int)early)) : "scc", "memory");
    unsigned tries = 0;
    if (TAIL) {
        const char* sp = pm + (lane & 7) * 256 + ((lane >> 3) & 1) * 8192;
        const int dl = (FLG(p.prm[5]) >> 26) & 31;
        if (dl) {
            wait_word<true>(p, p.ws + CTLO + G_C_KM + ((ct0 >> 1) + w) * 128, s - 1, 25u);
            for (int i = 0; i < dl + (s < 63 ? (int)((FLG2(p.prm[7]) >> 8) & 31) : 0); ++i) __builtin_amdgcn_s_sleep(2);
        } else
        while (__any(__hip_atomic_load((const gu32*)sp, RLX_AGENT) == 0xffffffffu) && !give_up(p, tries, 23u)) __builtin_amdgcn_s_sleep(1);
    } else if (!early) wait_word(p, p.ws + CTLO + (lane < 8 ? G_C_UM + (w * 8 + lane) * 128 : attn_flag(p, s, w * 2 + (lane & 1)) - CTLO), s, 22u);
    if (!early) TST(4096 + (s * 36 + ct0) * 4 + w, 1);
    for (;;) {
    asm volatile("s_nop 4");
#pragma unroll
    for (int q = 0; q < 8; ++q) { RLD(b[q][0], vb, pb, 0); RLD(b[q][1], vb, pb, 1024); asm volatile("v_add_u32 %0, 0x800, %0" : "+v"(vb)); }
#pragma unroll
    for (int q = 8; q < 10; ++q) { RLD(b[q][0], voff, pm, (q & 1) * 1024); RLD(b[q][1], voff, pm + 8192, (q & 1) * 1024); }
#pragma unroll
    for (int q = D0; q < NR; ++q) ALOAD(q);
#pragma unroll
    for (int q = 0; q < 10; ++q) {
        const int issuedA = (NR + q < 10 ? NR + q : 10) - D0;
        if (q < D0) VMW(2 * (9 - q) + issuedA * NT); else VMW((issuedA - (q - D0 + 1)) * NT);
#pragma unroll
        for (int t = 0; t < NT; ++t) { MFMA_ACC_A(acc[t][0], a[q % NR][t], b[q][0]); MFMA_ACC_A(acc[t][1], a[q % NR][t], b[q][1]); }
        if (NR + q < 10) ALOAD(NR + q);
    }
    asm volatile("s_nop 7" ::: "memory");
#pragma unroll
    for (int t = 0; t < NT; ++t) asm volatile("" : "+v"(acc[t][0]), "+v"(acc[t][1]));
    if (!TAIL) break;
    f32x4 chk = acc[0][0];
#pragma unroll
    for (int t = 0; t < NT; ++t) chk += acc[t][1] + acc[t][0];
    const float ck = (chk[0] + chk[1]) + (chk[2] + chk[3]);
    if (!__any(ck != ck)) break;
    unsigned mx = 0;
#pragma unroll
    for (int q = 0; q < 10; ++q) { asm volatile("" : "+v"(b[q][0]), "+v"(b[q][1])); mx = max(mx, max(umax4(b[q][0]), umax4(b[q][1]))); }
#pragma unroll
    for (int t = 0; t < NT; ++t) { asm volatile("" : "+a"(a[8 % NR][t]), "+a"(a[9 % NR][t])); mx = max(mx, max(umax4(a[8 % NR][t]), umax4(a[9 % NR][t]))); }
    if (!__any(mx == 0xffffffffu) || give_up(p, tries, 24u)) break;
    vb = voff;
#pragma unroll
    for (int t = 0; t < NT; ++t) { acc[t][0] = f32x4{0.f, 0.f, 0.f, 0.f}; acc[t][1] = acc[t][0]; asm volatile("" : "+v"(acc[t][0]), "+v"(acc[t][1])); }
    }
    TST(4096 + (s * 36 + ct0) * 4 + w, 2);
    f32x4* red = (f32x4*)smem;
#pragma unroll
    for (int t = 0; t < NT; ++t) { red[((w * NT + t) * 2 + 0) * 64 + lane] = acc[t][0]; red[((w * NT + t) * 2 + 1) * 64 + lane] = acc[t][1]; }
    __syncthreads();
    u16* orow = p.out + (size_t)(32 * s + 16 * (w & 1) + c) * OUTW + 16 * ct0 + 4 * g;
#pragma unroll
    for (int t = 0; t < NT; ++t)
        if ((t & 1) == (w >> 1)) {
            const int m = w & 1;
            const f32x4 v = (red[((0 * NT + t) * 2 + m) * 64 + lane] + red[((1 * NT + t) * 2 + m) * 64 + lane]) +
                            (red[((2 * NT + t) * 2 + m) * 64 + lane] + red[((3 * NT + t) * 2 + m) * 64 + lane]);
            *(u32x2*)(orow + 16 * t) = u32x2{pk2bf(v[0], v[1]), pk2bf(v[2], v[3])};
        }
}

static constexpr int KROW = G_KROW;
static constexpr int KARR = 80 * KROW;
static constexpr int UCS = 2208;
struct BCh {
    u16 uc[4][UCS];
    f32x4 cst[NST];
    u16 Kh[KARR + 256];
    u16 ub[SL];
};
static_assert(4 * sizeof(BCh) <= LDS_TOTAL - 32, "LDS");
typedef __attribute__((ext_vector_type(8))) _Float16 f16x8;
typedef __attribute__((ext_vector_type(2))) _Float16 f16x2;
__device__ __forceinline__ unsigned wave_umax(unsigned m) {
#pragma unroll
    for (int o = 32; o; o >>= 1) m = max(m, (unsigned)__shfl_xor((int)m, o));
    return m;
}
__device__ __forceinline__ unsigned scale_exp(unsigned absbits) { return min(max(absbits >> 23, 15u), 254u); }
__device__ __forceinline__ unsigned pk2h(float a, float b) { unsigned r; asm("v_cvt_pk_f16_f32 %0, %1, %2" : "=v"(r) : "v"(a), "v"(b)); return r; }
__device__ __forceinline__ unsigned prm(unsigned hi, unsigned lo, unsigned sel) { return __builtin_amdgcn_perm(hi, lo, sel); }
#define BST(i)
#define BST2(i)
__device__ __forceinline__ void task_ut(const MainP& p, char* smem, int rb, int w, int lane) {
    const int tid = w * 64 + lane, g = lane >> 4, c = lane & 15;
    const u32x4* src = (const u32x4*)(p.U + (size_t)rb * 64 * DM);
    u32x4 v[18];
#pragma unroll
    for (int i = 0; i < 18; ++i) {
        v[i] = src[tid + 256 * i];
    }
    BST2(0);
#pragma unroll
    for (int i = 0; i < 18; ++i) { const int ch = tid + 256 * i, row = ch / 72; *(u32x4*)(smem + row * 1184 + (ch - 72 * row) * 16) = v[i]; }
    __syncthreads();
    const unsigned ad = (unsigned)(uintptr_t)smem + (16 * g + (c >> 2)) * 1184 + (w * 144 + 4 * (c & 3)) * 2;
#pragma unroll
    for (int i = 0; i < 9; ++i) {
        u32x2 x0, x1, x2, x3;
        asm volatile("ds_read_b64_tr_b16 %0, %4 offset:%5\n\tds_read_b64_tr_b16 %1, %4 offset:%6\n\tds_read_b64_tr_b16 %2, %4 offset:%7\n\tds_read_b64_tr_b16 %3, %4 offset:%8\n\ts_waitcnt lgkmcnt(0)"
                     : "=&v"(x0), "=&v"(x1), "=&v"(x2), "=&v"(x3) : "v"(ad), "i"(i * 32), "i"(4 * 1184 + i * 32), "i"(8 * 1184 + i * 32), "i"(12 * 1184 + i * 32) : "memory");
        v[2 * i] = u32x4{x0[0], x0[1], x1[0], x1[1]}; v[2 * i + 1] = u32x4{x2[0], x2[1], x3[0], x3[1]};
    }
    __syncthreads();
    BST2(1);
    char* tw = smem + w * (144 * 144);
#pragma unroll
    for (int i = 0; i < 9; ++i) { *(u32x4*)(tw + (16 * i + c) * 144 + g * 32) = v[2 * i]; *(u32x4*)(tw + (16 * i + c) * 144 + g * 32 + 16) = v[2 * i + 1]; }
    const char* dst = uni(p.ws + G_D_UT + (size_t)rb * 73728 + w * 18432);
#pragma unroll
    for (int i = 0; i < 18; ++i) {
        const u32x4 o = *(const u32x4*)(tw + (8 * i + (lane >> 3)) * 144 + (lane & 7) * 16);
        const unsigned vi = lane * 16 + i * 1024;
        WST16(vi, o, dst, 0);
    }
    BST2(2);
    const u32x4 sent = {0xffffffffu, 0xffffffffu, 0xffffffffu, 0xffffffffu};
#pragma unroll 1
    for (int sg = NG1; sg < 64; ++sg) {
        const unsigned ov = umx_base(rb) + sg * 9216 + tid * 16, ok = G_D_KM + rb * G_HS4 + skew(sg, rb) * 4096 + tid * 16;
        const unsigned op = tid < 128 ? G_D_PX + (sg * 32 + rb) * 2048 + tid * 16
                          : tid < 192 ? ov + 6144 : G_D_PM + sg * 16384 + ((tid >> 4) & 1) * 8192 + rb * 256 + (tid & 15) * 16;
        asm volatile("global_store_dwordx4 %0, %4, %5 sc1\n\tglobal_store_dwordx4 %1, %4, %5 sc1\n\tglobal_store_dwordx4 %2, %4, %5 sc1\n\t"
                     "global_store_dwordx4 %3, %4, %5 sc1\n\ts_nop 1" :: "v"(ov), "v"(ov + 4096), "v"(ok), "v"(op), "v"(sent), "s"(p.ws) : "memory");
    }
    BST2(3);
    VMC0();
    BST2(4);
    __syncthreads();
    if (tid == 0) __hip_atomic_store((gu32*)(p.ws + CTLO + G_C_UT + rb * 128), 1u, RLX_AGENT);
}
struct KPar { u16 ldt, pa[2][4]; };
__device__ __forceinline__ KPar kgen_par(const MainP& p, int d, int lane) {
    KPar k;
    k.ldt = p.logdt[d];
#pragma unroll
    for (int q = 0; q < 2; ++q) { const int i = d * NST + lane + 64 * q; k.pa[q][0] = p.logA[i]; k.pa[q][1] = p.Aim[i]; k.pa[q][2] = p.Cre[i]; k.pa[q][3] = p.Cim[i]; }
    return k;
}
struct Pull { const char* a; const char* b; const char* c; unsigned at; };
__device__ __forceinline__ void pull_inputs(Pull& q, int w, bool now) {
    if (q.a && (now || (int)((unsigned)__builtin_amdgcn_s_memrealtime() - q.at) >= 0)) {
#pragma unroll
        for (int i = 0; i < 3; ++i)
            if (i < 2 || w == 0) asm volatile("s_mov_b32 m0, %3\n\ts_nop 0\n\tglobal_load_lds_dword %0, %1\n\tglobal_load_lds_dword %0, %2" :: "v"(threadIdx.x * 128 + i * 32768), "s"(q.a), "s"(q.b), "s"(155648) : "memory");
        asm volatile("global_load_lds_dword %0, %1" :: "v"(min(threadIdx.x, 143u) * 128), "s"(q.c) : "memory");
        q.a = nullptr;
    }
}
__device__ __forceinline__ void task_kgen(const MainP& p, char* smem, int d, int w, int lane, const KPar& kp, bool first, unsigned ep, Pull& pl) {
    BCh& L = reinterpret_cast<BCh*>(smem)[w];
    const int g = lane >> 4, c = lane & 15, d0 = d;
    const int tid = (d & 3) * 64 + lane;
    unsigned* Kd = (unsigned*)L.uc;
    unsigned ek;
    BST(10);
    const auto& pa = kp.pa;
    const float dt = bfr(expf(bf2f(kp.ldt)));
    f32x4 cs[2];
#pragma unroll
    for (int q = 0; q < 2; ++q) {
        const float ar = -bfr(expf(bf2f(pa[q][0])));
        const float cr = bf2f(pa[q][2]) * dt, ci = bf2f(pa[q][3]) * dt;
        cs[q] = f32x4{ar * dt * 1.4426950408889634f, bf2f(pa[q][1]) * dt * 0.15915494309189535f, __log2f(hypotf(cr, ci)), atan2f(ci, cr) * 0.15915494309189535f};
    }
    const int sh = 134 - (int)wave_umax((unsigned)(int)ceilf(fminf(fmaxf(fmaxf(cs[0][2], cs[1][2]), -120.f), 120.f) + 120.f));
#pragma unroll
    for (int q = 0; q < 2; ++q) { cs[q][2] += (float)sh; L.cst[lane + 64 * q] = cs[q]; }
    BST(11);
    {
        f32x4 acc[3];
#pragma unroll
        for (int v = 0; v < 3; ++v) acc[v] = f32x4{0.f, 0.f, 0.f, 0.f};
        auto base_of = [](int v, int r) { return v == 0 ? (r & 7) + 128 * (r >> 3) : v == 1 ? (r < 8 ? 256 + 2 * (r & 3) + 128 * (r >> 2) : 512 + 4 * (r & 1) + 128 * ((r - 8) >> 1))
                                                                                           : 1024 + 128 * min(r, 8); };
        const float b0 = (float)base_of(0, c), b1 = (float)base_of(1, c), b2 = (float)base_of(2, c), off = (float)(8 * c);
#define KG_POLAR(lg_, f_, a_, b_) const float m_ = __builtin_amdgcn_exp2f(fmaf(cc[0], b_, lg_)), p_ = __builtin_amdgcn_fractf(fmaf(a_, b_, f_)); \
            const float re_ = m_ * __builtin_amdgcn_cosf(p_), im_ = m_ * __builtin_amdgcn_sinf(p_)
#define MFMA32H(a_, b_, c_) __builtin_amdgcn_mfma_f32_16x16x32_f16(__builtin_bit_cast(f16x8, a_), __builtin_bit_cast(f16x8, b_), c_, 0, 0, 0)
#pragma unroll 1
        for (int t = 0; t < NST / 16; ++t) {
            u32x4 fh, fl, ah[3];
            pull_inputs(pl, w, false);
#pragma unroll
            for (int j = 0; j < 4; ++j) {
                const f32x4 cc = L.cst[16 * t + 4 * g + j];
                { KG_POLAR(12.f, 0.f, -cc[1], off); const unsigned hh = pk2h(re_, im_); const f16x2 h2 = __builtin_bit_cast(f16x2, hh); fh[j] = hh; fl[j] = pk2h(re_ - (float)h2[0], im_ - (float)h2[1]); }
                { KG_POLAR(cc[2], cc[3], cc[1], b0); ah[0][j] = pk2h(re_, im_); }
                { KG_POLAR(cc[2], cc[3], cc[1], b1); ah[1][j] = pk2h(re_, im_); }
                { KG_POLAR(cc[2], cc[3], cc[1], b2); ah[2][j] = pk2h(re_, im_); }
            }
            asm volatile("s_nop 1" : "+v"(fh), "+v"(fl), "+v"(ah[0]), "+v"(ah[1]), "+v"(ah[2]));
#pragma unroll
            for (int v = 0; v < 3; ++v) { acc[v] = MFMA32H(ah[v], fl, acc[v]); acc[v] = MFMA32H(ah[v], fh, acc[v]); }
        }
        BST(12);
        unsigned mx = 0;
#pragma unroll
        for (int v = 0; v < 3; ++v)
#pragma unroll
            for (int i = 0; i < 4; ++i)
                if (base_of(v, 4 * g + i) + 8 * c <= 2048) mx = max(mx, __float_as_uint(acc[v][i]) & 0x7fffffffu);
        ek = scale_exp(wave_umax(mx));
        const float sk = __uint_as_float((268u - ek) << 23);
#pragma unroll
        for (int v = 0; v < 3; ++v)
#pragma unroll
            for (int i = 0; i < 4; ++i) {
                const int pos = base_of(v, 4 * g + i) + 8 * c;
                if (pos <= 2048) Kd[pos] = pk2h(acc[v][i] * sk, 0.f);
            }
    }
    char* kt = p.ws + G_D_KT + (size_t)d * 8192;
    char* kt2 = kt + 4096;
    const unsigned voff = lane * 16;
#pragma unroll
    for (int q = 0; q < 4; ++q) {
        const int e = lane + 64 * q;
        unsigned t[8], k[8];
        const unsigned k0 = Kd[8 * e], k8 = Kd[8 * e + 8];
        if (q == 0) {
            const u32x4 a = *(const u32x4*)&Kd[8 * e], b = *(const u32x4*)&Kd[8 * e + 4];
            const bool two = lane >= 32;
            t[0] = a[0]; t[1] = two ? a[0] : a[1]; t[2] = a[2]; t[3] = two ? b[0] : a[3]; t[4] = b[0]; t[5] = two ? b[0] : b[1]; t[6] = b[2]; t[7] = two ? k8 : b[3];
        } else if (q == 1) {
            const unsigned k4 = Kd[8 * e + 4];
            t[0] = t[1] = t[2] = k0; t[3] = t[4] = t[5] = k4; t[6] = t[7] = k8;
        } else {
            t[0] = t[1] = t[2] = t[3] = k0; t[4] = (lane & 1) ? k8 : k0; t[5] = t[6] = t[7] = k8;
        }
#pragma unroll
        for (int j = 0; j < 8; ++j) k[j] = t[7 - j];
        u32x4 kh;
#pragma unroll
        for (int j = 0; j < 4; ++j) kh[j] = prm(k[2 * j + 1], k[2 * j], 0x05040100u);
        WST16(voff, kh, kt, q * 1024);
    }
    ek = (unsigned)min(max((int)ek - sh - 12, 15), 253);
    if (lane < 8) { const u32x4 e4 = {ek, ek, ek, ek}; WST16(voff, e4, kt2, 0); }
    BST(13);
    VMC0();
    BST(14);
    if (first) init_seen(p.ws, lane, ep);
    BST(15);
    if (lane == 0) __hip_atomic_fetch_add((gu32*)(p.ws + G_WS_CTL + G_C_KG + (d >> 2) * 128), 1u, RLX_AGENT);
}
__device__ __forceinline__ void task_s4d(const MainP& p, char* smem, int d0, int w, int lane, int& nb, bool more) {
    BCh* LL = reinterpret_cast<BCh*>(smem);
    BCh& L = LL[w];
    const int tid = w * 64 + lane, g = lane >> 4, c = lane & 15, d = d0 + w;
    BST(0);
    wait_word(p, p.ws + (lane < 32 ? CTLO + G_C_UT + lane * 128 : G_WS_CTL + G_C_KG + (d0 >> 2) * 128), lane < 32 ? 1 : 3, 30u);
    const float dsk = bf2f(p.Dskip[d]);
    const char* kt = p.ws + G_D_KT + (size_t)d * 8192;
    const char* kt2 = kt + 4096;
    const char* ut = p.ws + G_D_UT + d * 128;
    const unsigned voff = lane * 16;
    u32x4 kh[4], uu[4];
    unsigned ek;
#pragma unroll
    for (int q = 0; q < 4; ++q) { const unsigned vu = (8 * q + (lane >> 3)) * 73728 + (lane & 7) * 16; WLD16(uu[q], vu, ut, 0); }
    WLD16X4(kh[0], kh[1], kh[2], kh[3], voff, kt);
    asm volatile("global_load_dword %0, %1, %2 sc1" : "=v"(ek) : "v"(0u), "s"(kt2) : "memory");
    const u32x4 z4 = {0u, 0u, 0u, 0u};
    *(u32x4*)&L.Kh[8 * lane] = z4;
    *(u32x4*)&L.Kh[512 + 8 * (lane & 31)] = z4;
    *(u32x4*)&L.uc[0][64 + SL] = z4;
    *(u32x4*)&L.uc[(lane >> 3) & 3][8 * (lane & 7)] = z4;
    asm volatile("s_waitcnt vmcnt(0)" : "+v"(kh[0]), "+v"(kh[1]), "+v"(kh[2]), "+v"(kh[3]), "+v"(ek),
                 "+v"(uu[0]), "+v"(uu[1]), "+v"(uu[2]), "+v"(uu[3]) :: "memory");
    BST(1);
    unsigned mu = 0;
#pragma unroll
    for (int q = 0; q < 4; ++q)
#pragma unroll
        for (int j = 0; j < 4; ++j) mu = max(mu, max(uu[q][j] & 0x7fff0000u, (uu[q][j] << 16) & 0x7fff0000u));
    const unsigned eu = scale_exp(wave_umax(mu));
    const float su = __uint_as_float((268u - eu) << 23), iu = __uint_as_float((eu - 14u) << 23), ik = __uint_as_float((ek - 13u) << 23);
#pragma unroll
    for (int q = 0; q < 4; ++q) {
        const int e = lane + 64 * q, at = (16 + (e >> 2)) * KROW + 24 - 8 * (e & 3);
        u32x4 h;
#pragma unroll
        for (int j = 0; j < 4; ++j) h[j] = pk2h(lo2f(uu[q][j]) * su, hi2f(uu[q][j]) * su);
        *(u32x4*)&L.uc[0][64 + 8 * e] = h;
        *(u32x4*)&L.ub[8 * e] = uu[q];
        *(u32x4*)&L.Kh[at] = kh[q];
    }
    BST(2);
    BST(3);
#pragma unroll
    for (int q = 0; q < 4; ++q) {
        const int e = lane + 64 * q;
        const int ue = 56 + 8 * e;
        const u32x4 a = *(const u32x4*)&L.uc[0][ue];
        const u32x2 b = *(const u32x2*)&L.uc[0][ue + 8];
        const unsigned s0 = __builtin_amdgcn_alignbit(a[1], a[0], 16), s1 = __builtin_amdgcn_alignbit(a[2], a[1], 16), s2 = __builtin_amdgcn_alignbit(a[3], a[2], 16),
                       s3 = __builtin_amdgcn_alignbit(b[0], a[3], 16), s4 = __builtin_amdgcn_alignbit(b[1], b[0], 16);
        *(u32x4*)&L.uc[1][ue] = u32x4{s0, s1, s2, s3};
        *(u32x4*)&L.uc[2][ue] = u32x4{a[1], a[2], a[3], b[0]};
        *(u32x4*)&L.uc[3][ue] = u32x4{s1, s2, s3, s4};
    }
    if (lane == 0) {
        const u32x4 a = *(const u32x4*)&L.uc[0][56 + SL];
        const unsigned s0 = __builtin_amdgcn_alignbit(a[1], a[0], 16), s1 = __builtin_amdgcn_alignbit(a[2], a[1], 16), s2 = __builtin_amdgcn_alignbit(a[3], a[2], 16), s3 = a[3] >> 16;
        *(u32x4*)&L.uc[1][56 + SL] = u32x4{s0, s1, s2, s3};
        *(u32x4*)&L.uc[2][56 + SL] = u32x4{a[1], a[2], a[3], 0u};
        *(u32x4*)&L.uc[3][56 + SL] = u32x4{s1, s2, s3, 0u};
    }
    BST(4);
    {
        const int o = 33 + c + 8 * g;
        const unsigned ua = (unsigned)(uintptr_t)&L.uc[o & 3][o & ~3], ka = (unsigned)(uintptr_t)&L.Kh[(16 + c) * KROW + 8 * g];
        const f32x4* res = (const f32x4*)L.Kh + lane;
        asm volatile(ASM_CV :: "v"(ua), "v"(ka), "v"((unsigned)(uintptr_t)res) : CV_CLOB);
        BST(5);
#pragma unroll
        for (int par = 0; par < 2; ++par)
#pragma unroll
            for (int t = 0; t < 4; ++t) {
                const int n = 16 * (par + 2 * (16 * t + c)) + 4 * g;
                const u32x2 uu = *(const u32x2*)&L.ub[n];
                const f32x4 y4 = res[(par * 4 + t) * 64] * fminf(ik, iu) * fmaxf(ik, iu);
                const float r0 = bfr(y4[0]) + bfr(lo2f(uu[0]) * dsk), r1 = bfr(y4[1]) + bfr(hi2f(uu[0]) * dsk);
                const float r2 = bfr(y4[2]) + bfr(lo2f(uu[1]) * dsk), r3 = bfr(y4[3]) + bfr(hi2f(uu[1]) * dsk);
                *(u32x2*)&L.uc[1][n] = u32x2{pk2bf(r0, r1), pk2bf(r2, r3)};
            }
    }
    __syncthreads();
    BST(6);
    u32x4 y[4];
#pragma unroll
    for (int ch = 0; ch < 4; ++ch) y[ch] = *(const u32x4*)&LL[ch].uc[1][8 * tid];
    const unsigned vt = tid * 16;
    char* yb = p.ws + G_D_YB + (size_t)(d0 >> 2) * 16384;
#pragma unroll
    for (int i = 0; i < 4; ++i) {
        const u32x4 o = {prm(y[1][i], y[0][i], 0x05040100u), prm(y[3][i], y[2][i], 0x05040100u), prm(y[1][i], y[0][i], 0x07060302u), prm(y[3][i], y[2][i], 0x07060302u)};
        char* yi = yb + i * 4096;
        WST16(vt, o, yi, 0);
    }
    BST(7);
    if (tid == 0 && more) nb = (int)(__hip_atomic_fetch_add((gu32*)(p.ws + CTLO + G_C_TASK + 128), 1u, RLX_AGENT) + 1u);
    VMC0();
    __syncthreads();
    if (tid == 0) __hip_atomic_fetch_add((gu32*)(p.ws + CTLO + G_C_YC + (d0 >> 6) * 128), 1u, RLX_AGENT);
    BST(8);
}
__device__ __forceinline__ void task_merge(const MainP& p, char* smem, int d0, int i, int w, int lane) {
    const unsigned vt = (w * 64 + lane) * 16;
    wait_word(p, p.ws + CTLO + G_C_YC + (d0 >> 6) * 128, 15, 31u);
    {
        const char* sb = p.ws + G_D_YB + (size_t)(d0 >> 6) * 262144 + i * 4096;
        u32x4 z[16];
#pragma unroll
        for (int j = 0; j < 16; ++j) { const char* sj = sb + j * 16384; WLD16(z[j], vt, sj, 0); }
        asm volatile("s_waitcnt vmcnt(0)" : "+v"(z[0]), "+v"(z[1]), "+v"(z[2]), "+v"(z[3]), "+v"(z[4]), "+v"(z[5]), "+v"(z[6]), "+v"(z[7]),
                     "+v"(z[8]), "+v"(z[9]), "+v"(z[10]), "+v"(z[11]), "+v"(z[12]), "+v"(z[13]), "+v"(z[14]), "+v"(z[15]) :: "memory");
        char* tw = smem + w * 18432;
#pragma unroll
        for (int j = 0; j < 8; ++j) {
            *(u32x4*)(tw + lane * 144 + j * 16) = u32x4{z[2 * j][0], z[2 * j][1], z[2 * j + 1][0], z[2 * j + 1][1]};
            *(u32x4*)(tw + (64 + lane) * 144 + j * 16) = u32x4{z[2 * j][2], z[2 * j][3], z[2 * j + 1][2], z[2 * j + 1][3]};
        }
#pragma unroll
        for (int k = 0; k < 16; ++k) {
            const int q = 8 * k + (lane >> 3);
            const int row = 8 * (64 * w + (q & 63)) + 2 * i + (q >> 6);
            *(u32x4*)(p.out + (size_t)row * OUTW + DM + (d0 & ~63) + 8 * (lane & 7)) = *(const u32x4*)(tw + q * 144 + (lane & 7) * 16);
        }
    }
}

static constexpr int NKG = 144, E_B = NKG + 36;
static constexpr int Q1 = 0, Q2 = Q1 + (NFINE - 2) * 25;
static constexpr int NTASK = E_B + Q2 + 68;

__device__ __forceinline__ void worker_body(const MainP& p, char* smem, const int w, const int lane, int quota) {
    const int tid = w * 64 + lane;
    int* slot = (int*)(smem + LDS_TOTAL - 16);
    int nb = -1;
    int nst = 0;
    gu32* qa = (gu32*)(p.ws + CTLO + G_C_TASK);
    for (;;) {
        if (tid == 0) {
            int t = E_B;
            if (quota > 0) {
                const int k = nb >= 0 ? nb : (int)(__hip_atomic_fetch_add(qa + 32, 1u, RLX_AGENT) + 1u);
                if (k < E_B) t = k;
                nb = -1;
            }
            if (t >= E_B) t = E_B + (int)(__hip_atomic_fetch_add(qa, 1u, RLX_AGENT) + 1u);
            *slot = t;
        }
        __syncthreads();
        const int t = __builtin_amdgcn_readfirstlane(*slot);
        if (t >= NTASK) break;
        quota = t < E_B ? quota - 1 : 0;
        if (t < E_B) {
            if (t < 128 || (t >= 144 && t < 160)) task_s4d(p, smem, 4 * (t < 128 ? t : t - 16), w, lane, nb, quota > 0);
            else task_merge(p, smem, 64 * ((t < 144 ? t - 128 : t - 144) >> 2), t & 3, w, lane);
        } else {
            const int u = t - E_B;
            const bool last2 = u >= Q2;
            const int sg = last2 ? 62 + (u - Q2) / 34 : NG1 + (u - Q1) / 25, r = last2 ? (u - Q2) % 34 : (u - Q1) % 25;
            if (r < 16) task_attn_tail(p, sg, 2 * r + (w >> 1), w, lane);
            else if (last2) task_out<2, 9, true>(p, smem, sg, 2 * (r - 16), w, lane);
            else task_out<4, 9, true>(p, smem, sg, 4 * (r - 16), w, lane);
            if (last2) break;
        }
        __syncthreads();
    }
}

__host__ __device__ constexpr unsigned phys(unsigned x) { return (x >> 13) * 32768 + (x & 8191); }
__host__ __device__ constexpr unsigned cbase(int c) { return (c >> 1) * 131072 + (c & 1) * 16384; }
__host__ __device__ constexpr unsigned hbase(int c) { return G_HOFF + (c >> 1) * 32768 + (c & 1) * 16384; }
__host__ __device__ constexpr unsigned aMB(int f, int b) { return phys(f < 8 ? 16384 + f * 1024 + b : 28672 + b); }
__host__ __device__ constexpr unsigned aPX(int pr, int b) { return phys(pr * 1024 + b); }
__host__ __device__ constexpr unsigned aPM(int u, int b) { return phys(24576 + u * 1024 + b); }
#define IST(k)

static constexpr int L_RW = 160000;
__device__ __forceinline__ int lane_id() { int l; asm volatile("v_mbcnt_lo_u32_b32 %0, -1, 0\n\tv_mbcnt_hi_u32_b32 %0, -1, %0" : "=v"(l)); return l; }
typedef const __attribute__((address_space(4))) MainP* KArg;

__global__ __launch_bounds__(256, 1) void main_kernel(MainP p_) {
    __shared__ __attribute__((aligned(16))) char smem[LDS_TOTAL];
    KArg ka = (KArg)__builtin_amdgcn_kernarg_segment_ptr();
    static_assert(offsetof(MainP, W) == 0 && offsetof(MainP, ws) == 0x20 && offsetof(MainP, X) == 0x28 && offsetof(MainP, prm) == 0x40, "argument layout");
    asm volatile(ASM_EA :: "s"(ka), "s"(blockIdx.x), "v"(threadIdx.x) : ASM_CLOB);
    asm volatile("" : "+s"(ka));
    const unsigned t00 = (unsigned)__builtin_amdgcn_s_memrealtime();
    const int b = blockIdx.x, x = b & 7, ix = b >> 3;
    const int w = __builtin_amdgcn_readfirstlane(threadIdx.x >> 6);
    const bool isr = x < 4 && ix >= 16;
    if (x < 4 && ix >= 29) init_cal(ka->ws, smem, x * 3 + ix - 29, threadIdx.x, min((FLG(ka->prm[5]) >> 16) & 7, 5));
    if (x >= 4 ? ix >= 24 : (ix >= 25 && ix < 29)) init_fill(ka->ws, ka->mem0, x >= 4 ? (ix - 24) * 4 + (x & 3) : 32 + (ix - 25) * 4 + x, threadIdx.x);
    IST(0);
    const int NS = (FLG(ka->prm[5]) >> 8) & 3, NF = (FLG(ka->prm[5]) >> 21) & 7, RK = (FLG(ka->prm[5]) >> 10) & 3;
    const int NSP = G_RARM ? 7 : FLG2(ka->prm[7]) & 7;
    const int NA1 = 18 + 4 * NSP, NB1 = NA1 - 3 * max(NSP - 4, 0), N1 = RK ? NA1 + (RK - 1) * NB1 : 0, N2 = N1 + (28 - 4 * NSP) * NS, NKE = min(N2 + max(24 * NF - 8, 0), NKG);
    if (ix >= (x < 4 ? 16 : 24)) {
        MainP p;
        p.ws = ka->ws; p.logA = ka->logA; p.Aim = ka->Aim; p.Cre = ka->Cre; p.Cim = ka->Cim; p.logdt = ka->logdt;
        Pull pl = {nullptr, nullptr, nullptr, t00 + 400 + 50 * ((FLG(ka->prm[5]) >> 4) & 15)};
        const bool own = x < 4 && ix < 25 + NSP;
        int d0, nr, dd;
        if (own) {
            const int jt = ix < 25 ? ix - 16 : ix - 25, solo = jt < NSP;
            const int k = ix >= 25 ? 18 + NSP + 3 * jt + w - 1 : solo ? 3 * jt + w - 1 : NSP + 2 * jt + w - 2;
            d0 = k < NB1 ? x * NB1 + k : 4 * NB1 * RK + x * (NA1 - NB1) + k - NB1; nr = w < 2 - solo ? 0 : k < NB1 ? RK : min(RK, 1); dd = 4 * NB1;
            if (threadIdx.x < 8) ((unsigned*)(smem + L_RW))[threadIdx.x] = 0xffffffffu;
        } else if (x < 4) { d0 = 4 * (N1 + (ix - 25 - NSP) * 4 + x) + w; nr = NS; dd = 4 * (28 - 4 * NSP); }
        else {
            d0 = 4 * (N2 + (ix - 24) * 4 + (x & 3)) + w; nr = ix < 28 ? NF : ix < 30 ? max(NF - 1, 0) : 0; dd = 96;
            const size_t o = (size_t)((ix - 24) * 4 + (x & 3)) * 73728; pl.a = (const char*)ka->X + o; pl.b = (const char*)ka->U + o;
            pl.c = (const char*)((x & 2) ? ((x & 1) ? ka->Cim : ka->Cre) : ((x & 1) ? ka->Aim : ka->logA)) + (ix - 24) * 18432;
        }
        while (nr > 0 && d0 + dd * (nr - 1) >= 4 * NKG) --nr;
        KPar kn;
        if (nr > 0) kn = kgen_par(p, d0, threadIdx.x & 63);
        __builtin_amdgcn_sched_barrier(0);
        const unsigned ep = __hip_atomic_load((const gu32*)(p.ws + G_WS_EP), RLX_AGENT);
        asm volatile("s_waitcnt vmcnt(0)" :: "v"(ep) : "memory");
        __syncthreads();
        if (x >= 4 && ix == 24) {
            const char* xs = (const char*)ka->X + threadIdx.x * 128;
#pragma unroll
            for (int i = 0; i < 7; ++i) asm volatile("s_mov_b32 m0, %1\n\ts_nop 0\n\tglobal_load_lds_dword %0, off" :: "v"(xs + i * 32768), "s"(155648) : "memory");
        }
        {
            const int q0 = x < 4 ? ix - 25 : ix - 26, q = q0 < 4 ? q0 : 2 * q0 - 8, lo = (int)((u64)ka->W[q & 2] >> 8);
            const int hs = q0 < 4 ? (x & 3) * 8 + 4 * (q & 1) + ((1 - lo) & 3) : (31 - lo) & 31;
            if (q0 >= 0 && q0 < (x < 4 ? 4 : 6) && (hs >> 3) == (x & 3) && (FLG(ka->prm[5]) & 1) && (x >= 4 || (FLG(ka->prm[5]) & 8))) {
                const char* src = (q & 2) ? (const char*)ka->W[3] + (size_t)hs * 147456 + threadIdx.x * 128 : (const char*)ka->W[x < 4 && !G_QKK] + hs * 256 + (threadIdx.x >> 1) * 8192 + (threadIdx.x & 1) * 128;
                const unsigned st = (q & 2) ? 32768u : 1048576u;
#pragma unroll
                for (int i = 0; i < 5; ++i)
                    if (i < 4 || w < 2) {
                        if (FLG(ka->prm[5]) & 4) asm volatile("s_mov_b32 m0, %1\n\ts_nop 0\n\tglobal_load_lds_dword %0, off nt" :: "v"(src + (size_t)i * st), "s"(155648) : "memory");
                        else asm volatile("s_mov_b32 m0, %1\n\ts_nop 0\n\tglobal_load_lds_dword %0, off" :: "v"(src + (size_t)i * st), "s"(155648) : "memory");
                    }
            }
        }
        if (nr == 0) init_seen(p.ws, threadIdx.x & 63, ep);
#pragma unroll 1
        for (int r = 0; r < nr; ++r) {
            const KPar kp = kn;
            kn = kgen_par(p, d0 + dd * min(r + 1, nr - 1), threadIdx.x & 63);
            __builtin_amdgcn_sched_barrier(0);
            task_kgen(p, smem, d0 + dd * r, w, threadIdx.x & 63, kp, r == 0, ep, pl);
        }
        if (w == 0) init_count(p.ws, threadIdx.x & 63);
        while (pl.a) { pull_inputs(pl, w, false); __builtin_amdgcn_s_sleep(8); }
        if (x < 4 && !own) return;
        asm volatile("" : "+s"(ka));
    }
    if (isr) {
        const int lane = threadIdx.x & 63, g = lane >> 4, c = lane & 15;
        const int jt = ix < 25 ? ix - 16 : ix - 25, solo = jt < NSP, par = solo ? ix >= 25 : w;
        const unsigned lb = w * G_STG;
        unsigned* ct = (unsigned*)(smem + lb) + lane;
        asm volatile("" :: "v"(ct) : "memory");
#define CTW(k, val) ct[(k) * 64] = (unsigned)(val)
        const u64 ws = (u64)ka->ws, prm = (u64)ka + 0x40;
        const u64 stb = 0;
        IST(1);
        CTW(0, lane * 16);
        const int j = x * 9 + jt;
        const int rot = (jt * 16) / 9;
        if (w >= 2 - solo) return;
        CTW(1, lane * 16 + 4096);
        CTW(2, aMB(j >> 2, (j & 3) * 256 + (4 * g + 2 * ((c >> 1) & 1) + (c & 1)) * 16 + (c >> 2) * 4));
        for (int k = 0; k < (int)G_NARM; ++k) {
            constexpr int N0 = G_HKB, N1 = N0 + (9 * G_RM + 3) / 4, N2 = N1 + 5 * G_RP;
            constexpr int NO = G_RARM ? 7 : 9;
            static_assert(N2 <= NO * G_NARM && G_NARM <= 5, "shares");
            const int q = min(jt + NO * k, N2 - 1), qm = min(x * (N1 - N0) + q - N0, (int)(9 * G_RM) - 1), qp = x * 5 * G_RP + q - N1;
            const int cp = q < N0 ? 4 + x : q < N1 ? qm / 9 : qp / 20, kb = q < N0 ? q : q < N1 ? (qm % 9 == 8 ? 28 : 16 + qm % 9) : (qp % 20 < 16 ? qp % 20 : 8 + qp % 20);
            CTW(47 + k, (cp >= 4 ? hbase(x) : cbase(cp)) + phys(kb * 1024 + lane * 16));
        }
        for (int k = 0; k < 3; ++k) { const int q = min(jt + 7 * k, 15); CTW(52 + k, (x * 9 + 7 + (q >> 3)) * 16384 + (q & 7) * 1024 + lane * 16); }
        for (int k = 0; k < 2; ++k) { const int L = min(jt * 74 + min(k * 64 + lane, 73), 511), tl = x * 9 + 7 + (L >> 8); CTW(55 + k, G_D_UX + tl * G_UXS + skew(0, tl) * 32768 + (L & 255) * 128); }
        const unsigned pc = 0;
        CTW(3, hbase(lane & 3) + G_RHO + ((lane >> 2) & 1) * G_RHS + j * 32); CTW(46, hbase(x & 2) + G_RHO + min(lane, 35) * 32);
        CTW(4, (c & 2) ? 0xEEEE : 0x4444); CTW(5, (c & 1) ? 0x03020706u : 0x05040100u);
        CTW(6, G_GVC ? G_WS_CTL + G_C_GV + G_GVH + x * 2048 + (par * 32 + (lane & 31)) * 32 : G_WS_CTL + G_C_GV + (par * 32 + (lane & 31)) * 128); CTW(7, hbase(x & 2) + (lane & 31) * G_HP); CTW(8, lane * 4); CTW(9, L_RW);
        for (int k = 0; k < 16; ++k) { const int i = (k + rot) & 15; CTW(10 + k, pc + aPX(i, lane * 16)); CTW(26 + k, i * 2048 + lane * 16); }
        for (int k = 0; k < 4; ++k) CTW(42 + k, pc + aPM(k, lane * 16));
        IST(2);
        asm volatile(ASM_R :: "s"(ws), "s"(ws), "s"((u64)(t00 << 4)), "s"(__builtin_amdgcn_readfirstlane(par)), "s"(__builtin_amdgcn_readfirstlane(j)), "s"((u64)__builtin_amdgcn_readfirstlane(2 * rot) + ((u64)__builtin_amdgcn_readfirstlane(solo) << 32)), "s"(prm), "s"(lb), "s"(stb) : ASM_CLOB);
    }
    asm volatile("" : "+s"(ka));
    MainP p;
    p.prm[5] = ka->prm[5]; p.prm[7] = ka->prm[7];
    p.U = ka->U; p.ws = ka->ws; p.logA = ka->logA; p.Aim = ka->Aim; p.Cre = ka->Cre; p.Cim = ka->Cim; p.logdt = ka->logdt; p.Dskip = ka->Dskip; p.out = ka->out;
    {
        auto cal = [&](int k) { return __hip_atomic_load((const gu32*)(p.ws + G_WS_DONE + k * 64 + 4), RLX_AGENT); };
        p.prm[0] = __builtin_amdgcn_readfirstlane((min(cal(5), cal(11)) < min(cal(4), cal(10)) ? 8192 : 0) ^ G_CTF);
    }
    const bool s4 = ix < 22 - ((FLG(p.prm[5]) >> 12) & 15);
    if (s4 && ix >= 8 && ix < 24) for (int i = 0; i < (int)(((ix & 7) * 4 + (x & 3)) * ((FLG(p.prm[5]) >> 24) & 3)); ++i) __builtin_amdgcn_s_sleep(3);
    if (x >= 4 && ix >= 8 && ix < 22) {
#pragma unroll 1
        for (int q = NKE + (ix - 8) * 4 + (x & 3); q < NKG; q += 56) {
            Pull pl = {nullptr, nullptr, nullptr, 0u};
            task_kgen(p, smem, 4 * q + w, w, lane_id(), kgen_par(p, 4 * q + w, lane_id()), false, 0u, pl);
        }
    }
    if (ix >= 24 && ix < 30) {
#pragma unroll 1
        for (int rb = (ix - 24) * 4 + (x & 3); rb < 32; rb += 8) {
            __syncthreads();
            task_ut(p, smem, rb, w, lane_id());
            if (ix < 28) break;
        }
        __syncthreads();
        const int lane = lane_id(), g = lane >> 4, c = lane & 15, ct0 = 6 * (ix - 24), s0 = x & 3;
        const unsigned lb = w * G_STG;
        unsigned* ct = (unsigned*)(smem + lb) + lane;
        asm volatile("" :: "v"(ct) : "memory");
        for (int t = 0; t < 6; ++t) CTW(t, (ct0 + t) * G_UXS + skew(0, ct0 + t) * 32768 + w * 8192 + 4096 + lane * 16);
        CTW(6, w * 16384 + 4096 + lane * 16); CTW(7, w * 16384 + 12288 + lane * 16); CTW(8, w * 2048 + lane * 16); CTW(9, w * 2048 + 8192 + lane * 16);
        CTW(10, umx_base(w * 8 + g) + ct0 * 256 + c * 16); CTW(11, umx_base(w * 8 + 4 + g) + ct0 * 256 + c * 16);
        const unsigned fa = lane < 8 ? CTLO + G_C_UM + (w * 8 + lane) * 128 : CTLO + G_C_GV + ((s0 & 1) * 32 + w * 8 + (lane & 7)) * 128;
        CTW(12, lane < 16 ? fa : attn_flag(p, s0, w * 2 + (lane & 1))); CTW(13, lane < 16 ? fa : attn_flag(p, s0 + 60, w * 2 + (lane & 1)));
        CTW(14, w * 12288 + lane * 16); CTW(15, w * 1024 + lane * 16);
        CTW(16, (16 * (w & 1) + c) * (OUTW * 2) + (16 * ct0 + 16 * (w >> 1) + 4 * g) * 2); CTW(17, lane * 4);
        static_assert(NG1 == 58, "as in the generated block");
        const u64 ws = (u64)p.ws;
        const u64 stb = 0;
        asm volatile(ASM_O :: "s"(ws), "s"((u64)p.out), "s"(ws), "s"(w), "s"(s0), "s"(ws), "s"(ws), "s"(lb), "s"(stb) : ASM_CLOB);
        asm volatile("" : "+s"(ka));
        p.prm[5] = ka->prm[5]; p.prm[7] = ka->prm[7];
        p.U = ka->U; p.ws = ka->ws; p.logA = ka->logA; p.Aim = ka->Aim; p.Cre = ka->Cre; p.Cim = ka->Cim; p.logdt = ka->logdt; p.Dskip = ka->Dskip; p.out = ka->out;
    }
    if ((ix & 22) == 22 && x >= 4) { attn_role(p, ix >= 30 ? 0 : NA + 1, (x & 3) * 8 + (ix & 1) * 4 + w, w, lane_id());
        if (b == 255) {
            wait_word(p, p.ws + G_WS_CTL + G_C_ARR, 255, 50u);
            if (w == 0 && lane_id() == 0) __hip_atomic_fetch_add((gu32*)(p.ws + G_WS_EP), 1u, RLX_AGENT);
        }
        __syncthreads();
    }
    worker_body(p, smem, w, lane_id(), s4 ? 1000 : 0);
}

extern "C" __global__ void code_end_marker() {}

static constexpr int g_prm[8] = {PRM_DEFAULT};

extern "C" void kernel_launch(void* const* d_in, const int* in_sizes, int n_in, void* d_out, int out_size,
                              void* d_ws, size_t ws_size, hipStream_t stream) {
    char* ws = (char*)(((uintptr_t)d_ws + 0x1FFFFF) & ~(uintptr_t)0x1FFFFF);
    MainP p;
    for (int i = 0; i < 8; ++i) p.prm[i] = g_prm[i];
    p.mem0 = (const u16*)d_in[1];
    for (int i = 0; i < 4; ++i) p.W[i] = (const u16*)d_in[2 + i];
    p.X = (const u16*)d_in[0]; p.U = (const u16*)d_in[7]; p.ws = ws;
    p.logA = (const u16*)d_in[8]; p.Aim = (const u16*)d_in[9]; p.Cre = (const u16*)d_in[10]; p.Cim = (const u16*)d_in[11];
    p.logdt = (const u16*)d_in[12]; p.Dskip = (const u16*)d_in[13]; p.out = (u16*)d_out;
    main_kernel<<<256, 256, 0, stream>>>(p);
}
```
